```python
import torch

def _softmax(x, axis=-1):
    return torch.softmax(x, dim=axis)

def _gelu(x):
    return torch.nn.functional.gelu(x, approximate='tanh')

def _silu(x):
    return torch.nn.functional.silu(x)

def _sigmoid(x):
    return torch.sigmoid(x)

def _layernorm(x, axis=-1, eps=1e-05):
    m = x.mean(dim=axis, keepdim=True)
    v = x.var(dim=axis, keepdim=True, unbiased=False)
    return (x - m) / torch.sqrt(v + eps)

def _scatter_add(tgt, idx, src, axis=0):
    return tgt.clone().index_add_(axis, idx, src)

def _segment_sum(src, seg, n):
    out = torch.zeros((n,) + src.shape[1:], dtype=src.dtype)
    return out.index_add_(0, seg, src)

def _pad(x, pw):
    flat = tuple((v for pair in reversed(pw) for v in pair))
    return torch.nn.functional.pad(x, flat)
kernel_name = 'interleave_layers__memory_aug_recurrent_mem_tr__glu_gated_glu__prompt'

def setup_inputs():

    _w = lambda a: (lambda t: t.to(torch.bfloat16) if t.is_floating_point() else t)(torch.as_tensor(a))

    def _A_setup():
        B, S, Dm = (1, 2048, 640)
        H, Dh = (32, 192)
        M, seg = (8, 32)
        seg = min(seg, S)
        S = S // seg * seg
        return {'x': _w((lambda *s: torch.randn(*s, dtype=torch.bfloat16) * 0.05)(B, S, Dm)), 'mem0': _w((lambda *s: torch.randn(*s, dtype=torch.bfloat16) * 0.8)(B, M, Dm)), 'Wq': _w((lambda *s: torch.randn(*s, dtype=torch.bfloat16) * 0.8)(Dm, H, Dh) * Dm ** (-0.5)), 'Wk': _w((lambda *s: torch.randn(*s, dtype=torch.bfloat16) * 0.8)(Dm, H, Dh) * Dm ** (-0.5)), 'Wv': _w((lambda *s: torch.randn(*s, dtype=torch.bfloat16) * 0.8)(Dm, H, Dh) * Dm ** (-0.5)), 'Wo': _w((lambda *s: torch.randn(*s, dtype=torch.bfloat16) * 0.8)(H, Dh, Dm) * (H * Dh) ** (-0.5)), 'seg_len': seg}
    _A_in = _A_setup()

    def _B_setup():
        B, S, D, F = (1, 2048, 640, 1536)
        return {'x': _w((lambda *s: torch.randn(*s, dtype=torch.bfloat16) * 0.05)(B, S, D)), 'w_gate': _w((lambda *s: torch.randn(*s, dtype=torch.bfloat16) * 0.05)(D, F)), 'w_up': _w((lambda *s: torch.randn(*s, dtype=torch.bfloat16) * 0.05)(D, F)), 'w_down': _w((lambda *s: torch.randn(*s, dtype=torch.bfloat16) * 0.05)(F, D))}
    _B_in = _B_setup()
    return {'A_x': _A_in['x'], 'A_mem0': _A_in['mem0'], 'A_Wq': _A_in['Wq'], 'A_Wk': _A_in['Wk'], 'A_Wv': _A_in['Wv'], 'A_Wo': _A_in['Wo'], 'A_seg_len': _A_in['seg_len'], 'B_x': _B_in['x'], 'B_w_gate': _B_in['w_gate'], 'B_w_up': _B_in['w_up'], 'B_w_down': _B_in['w_down']}

def reference(A_x, A_mem0, A_Wq, A_Wk, A_Wv, A_Wo, A_seg_len, B_x, B_w_gate, B_w_up, B_w_down):

    def _A_ref(x, mem0, Wq, Wk, Wv, Wo, seg_len):
        B, S, Dm = x.shape
        H, Dh = (Wq.shape[1], Wq.shape[2])
        M = mem0.shape[1]
        scale = Dh ** (-0.5)
        n_seg = S // seg_len
        L = M + seg_len
        idx = torch.arange(L)
        mask = (idx[:, None] < M) | (idx[:, None] >= idx[None, :])
        mem = mem0
        outs = []
        for s in range(n_seg):
            xs = x[:, s * seg_len:(s + 1) * seg_len, :].clone()
            h = torch.concatenate([mem, xs], axis=1)
            q = torch.einsum('btd,dhe->bhte', h, Wq)
            k = torch.einsum('btd,dhe->bhte', h, Wk)
            v = torch.einsum('btd,dhe->bhte', h, Wv)
            scores = torch.einsum('bhqe,bhke->bhqk', q, k) * scale
            scores = torch.where(mask[None, None], scores, -1000000000.0)
            p = _softmax(scores, axis=-1)
            o = torch.einsum('bhqk,bhke->bhqe', p, v)
            o = torch.einsum('bhte,hed->btd', o, Wo)
            mem = o[:, :M, :].clone()
            outs.append(o[:, M:, :])
        return torch.concatenate(outs, axis=1)

    def _B_ref(x, w_gate, w_up, w_down):
        g = torch.einsum('bsd,df->bsf', x, w_gate)
        u = torch.einsum('bsd,df->bsf', x, w_up)
        h = _sigmoid(g) * u
        return torch.einsum('bsf,fd->bsd', h, w_down)
    x = A_x
    x = x + _A_ref(_layernorm(x, axis=-1), A_mem0, A_Wq, A_Wk, A_Wv, A_Wo, A_seg_len)
    x = x + _B_ref(_layernorm(x, axis=-1), B_w_gate, B_w_up, B_w_down)
    x = x + _A_ref(_layernorm(x, axis=-1), A_mem0, A_Wq, A_Wk, A_Wv, A_Wo, A_seg_len)
    x = x + _B_ref(_layernorm(x, axis=-1), B_w_gate, B_w_up, B_w_down)
    return x
```

```cpp
#include <hip/hip_runtime.h>
#include <stdint.h>

typedef __attribute__((ext_vector_type(8))) short bf16x8;
typedef __attribute__((ext_vector_type(16))) short bf16x16;
typedef __attribute__((ext_vector_type(4))) float f32x4;
typedef __attribute__((ext_vector_type(2))) float f32x2;
typedef __attribute__((ext_vector_type(4))) unsigned u32x4;
typedef __attribute__((ext_vector_type(2))) unsigned u32x2;
typedef __attribute__((ext_vector_type(2))) __bf16 bf16v2;
typedef unsigned short u16;
typedef __attribute__((address_space(3))) void* lds_ptr_t;
typedef __attribute__((address_space(1))) const void* gptr_t;
typedef __amdgpu_buffer_rsrc_t rsrc_t;

#define NSEG 64
#define LAG 9
#define EUP (LAG + 2)
#define VAH 5
#define QAH 4
#define VHD 20
#define OBD (LAG + 1)
#define HM 0
#define HELPS 0
#define BRN 16
#define C1D 600
#define GLD 1000
#define DW 200
#define UPK 8
#define DRL 1
#define DX 120
#define UDW 0
#define USL 3
#define DXD 290
#define DXL 460
#define DCK 250
#define DQ 196
#define DV 196
#define DR 118
#define DRU 104
#define BE 2
#define NCAL 8
#define PFL 0
#define RMASK 79
#define RING(id) ((id) & RMASK)
static_assert(LAG <= 13 && QAH <= 13, "rings too small");
#define QS0 4
#define QS1 4
#define WFS 2
#define WAUX 0
#define DQ3 27
#define PL 4
#define GH 22
#define GHD 800
#define DLAST 230
#define GSW 1
#define DH 70
#define BNH 16
#define RFX (PL == 7 ? 0x2000 : 0)
#define QSX 0x2000
#define KSX 0
#define AUX_SC1 16
#define POISON 0xFFFFFFFFu
#define SPIN_CAP (1u << 17)

constexpr unsigned FL_PAR = 1024;
constexpr unsigned FL_T = 2048;
constexpr unsigned FL_B = 4096;
constexpr unsigned FL_N = FL_B + 128u * 16384;
constexpr unsigned FL_K = FL_N + 64u * 512;
constexpr unsigned FL_V = FL_K + 130u * 8192;
constexpr unsigned CTRL_BYTES = FL_V + 130u * 16384;
constexpr unsigned OFF_X1 = CTRL_BYTES;
constexpr unsigned OFF_X3 = OFF_X1 + 64u * 40960;
constexpr unsigned OFF_X0 = OFF_X3 + 64u * 40960;
constexpr unsigned OFF_X2 = OFF_X0 + 64u * 40960;
constexpr unsigned OFF_XN = OFF_X2 + 64u * 40960;
constexpr unsigned OFF_HG = OFF_XN + 128u * 40960;
constexpr unsigned OFF_HOT = (OFF_HG + 2u * 64 * 98304 + 0x1FFFFFu) & ~0x1FFFFFu;
constexpr unsigned OFF_BT = OFF_HOT + 3u * 2097152;
constexpr unsigned OFF_QK = OFF_BT + 2u * BRN * 2621440;
constexpr unsigned OFF_END = OFF_QK + 80u * 1572864;
__host__ __device__ constexpr unsigned blk(unsigned piece) { return (piece >> 3) * 16384u + (piece & 7) * 1024u; }
constexpr unsigned HB_MEM = 0;
constexpr unsigned HB_PM = 16u * 16384;
constexpr unsigned HB_PT = 48u * 16384;
constexpr unsigned HB_TS = 112u * 16384;
constexpr unsigned HB_TM = 116u * 16384;
constexpr unsigned HB_END = 120u * 16384;
static_assert(OFF_END + 2097152u <= 262000000u, "workspace too large");

__device__ __forceinline__ unsigned pack2(float a, float b) {
    f32x2 v = {a, b};
    bf16v2 r = __builtin_convertvector(v, bf16v2);
    return __builtin_bit_cast(unsigned, r);
}
__device__ __forceinline__ float bflo(unsigned u) { return __uint_as_float(u << 16); }
__device__ __forceinline__ float bfhi(unsigned u) { return __uint_as_float(u & 0xFFFF0000u); }
__device__ __forceinline__ float bfr(float f) { return bflo(pack2(f, 0.f)); }
__device__ __forceinline__ unsigned nop(unsigned d) { return min(d, 0xFFFFFFFEu); }
__device__ __forceinline__ u32x4 ld128(rsrc_t rs, unsigned voff, unsigned soff) { return __builtin_amdgcn_raw_buffer_load_b128(rs, (int)voff, (int)soff, AUX_SC1); }
__device__ __forceinline__ unsigned ld32(rsrc_t rs, unsigned voff, unsigned soff) { return __builtin_amdgcn_raw_buffer_load_b32(rs, (int)voff, (int)soff, AUX_SC1); }
__device__ __forceinline__ void st128(rsrc_t rs, unsigned voff, unsigned soff, u32x4 v) { __builtin_amdgcn_raw_buffer_store_b128(v, rs, (int)(voff + soff), 0, AUX_SC1); }
__device__ __forceinline__ void st32(rsrc_t rs, unsigned voff, unsigned soff, unsigned v) { __builtin_amdgcn_raw_buffer_store_b32(v, rs, (int)voff, (int)soff, AUX_SC1); }
__device__ __forceinline__ bf16x8 asbf(u32x4 v) { return __builtin_bit_cast(bf16x8, v); }
__device__ __forceinline__ unsigned mx4(u32x4 v) { return max(max(v[0], v[1]), max(v[2], v[3])); }
__device__ __forceinline__ unsigned mn4(u32x4 v) { return min(min(v[0], v[1]), min(v[2], v[3])); }
__device__ __forceinline__ u32x4 poison4() { unsigned z = POISON; asm volatile("" : "+v"(z)); return (u32x4){z, z, z, z}; }
__device__ __forceinline__ u32x4 ones4() { unsigned z = 1u; asm volatile("" : "+v"(z)); return (u32x4){z, z, z, z}; }
#define MFMA(a, b, c) __builtin_amdgcn_mfma_f32_16x16x32_bf16((a), (b), (c), 0, 0, 0)
#define MFMAP(d, a, b) { d = MFMA(a, b, d); asm volatile("" : "+v"(d)); }
#define Z4 ((f32x4){0.f, 0.f, 0.f, 0.f})
#define RAWBAR() { asm volatile("s_waitcnt lgkmcnt(0)" ::: "memory"); __builtin_amdgcn_s_barrier(); asm volatile("" ::: "memory"); }
#define WAITV0() { __builtin_amdgcn_s_waitcnt(0x0F70); asm volatile("" ::: "memory"); }
#define WAITVN(n) { __builtin_amdgcn_s_waitcnt(0x0F70 | (n)); asm volatile("" ::: "memory"); }
#define WAITL0() asm volatile("s_waitcnt lgkmcnt(0)" ::: "memory")
#define SB0() __builtin_amdgcn_sched_barrier(0)
#define MEMB() asm volatile("" ::: "memory")
#define ROR8U(x) ((unsigned)__builtin_amdgcn_update_dpp(0, (int)(x), 0x128, 0xf, 0xf, false))
#define ROR8(x) __uint_as_float(ROR8U(__float_as_uint(x)))
#define QX1U(x) ((unsigned)__builtin_amdgcn_update_dpp(0, (int)(x), 0xB1, 0xf, 0xf, false))
#define QEV(x) ((unsigned)__builtin_amdgcn_update_dpp(0, (int)(x), 0xA0, 0xf, 0xf, false))
#define QOD(x) ((unsigned)__builtin_amdgcn_update_dpp(0, (int)(x), 0xF5, 0xf, 0xf, false))
#define DMA(dst, voff, soff) __builtin_amdgcn_raw_ptr_buffer_load_lds(rs, (lds_ptr_t)(dst), 16, (int)(voff), (int)(soff), 0, AUX_SC1)
#define DMAI(dst, voff, soff, imm) __builtin_amdgcn_raw_ptr_buffer_load_lds(rs, (lds_ptr_t)(dst), 16, (int)(voff), (int)(soff), imm, AUX_SC1)
#define OPAQUE_LO() unsigned lo; asm volatile("v_mbcnt_lo_u32_b32 %0, -1, 0\n\tv_mbcnt_hi_u32_b32 %0, -1, %0\n\tv_lshlrev_b32 %0, 4, %0" : "=v"(lo));
__device__ __forceinline__ int lane_id() { int l; asm volatile("v_mbcnt_lo_u32_b32 %0, -1, 0\n\tv_mbcnt_hi_u32_b32 %0, -1, %0" : "=v"(l)); return l; }
__device__ __forceinline__ unsigned now32() { return (unsigned)__builtin_amdgcn_s_memrealtime(); }
#define FTS(slot)
#define FLV ones4()
__device__ __forceinline__ void wait_until(unsigned t) { while ((int)(t - now32()) > 0) __builtin_amdgcn_s_sleep(1); }
__device__ __forceinline__ float xmax(float v) {
    float t;
    asm("v_mov_b32 %1, %0\n\ts_nop 1\n\tv_permlane16_swap_b32 %0, %1\n\tv_max_f32 %0, %0, %1\n\t"
        "v_mov_b32 %1, %0\n\ts_nop 1\n\tv_permlane32_swap_b32 %0, %1\n\tv_max_f32 %0, %0, %1" : "+v"(v), "=&v"(t));
    return v;
}
__device__ __forceinline__ float max3(float a, float b, float c) { return fmaxf(fmaxf(a, b), c); }
__device__ __forceinline__ float xsum(float v) {
    unsigned a = __float_as_uint(v);
    auto r = __builtin_amdgcn_permlane16_swap(a, a, false, false);
    v = __uint_as_float(r[0]) + __uint_as_float(r[1]);
    a = __float_as_uint(v);
    auto s = __builtin_amdgcn_permlane32_swap(a, a, false, false);
    return __uint_as_float(s[0]) + __uint_as_float(s[1]);
}

struct Params {
    const u16* x; const u16* mem0; const u16* Wq; const u16* Wk; const u16* Wv; const u16* Wo;
    const u16* wg; const u16* wu; const u16* wd;
    char* ws; u16* out;
};

__device__ __forceinline__ bool spin_fail(const Params& p, unsigned& spins) {
    ++spins;
    if ((spins & 255u) == 0) {
        unsigned* a = (unsigned*)p.ws;
        if (spins > SPIN_CAP) { __hip_atomic_store(a, 1u, __ATOMIC_RELAXED, __HIP_MEMORY_SCOPE_AGENT); return true; }
        if (__hip_atomic_load(a, __ATOMIC_RELAXED, __HIP_MEMORY_SCOPE_AGENT)) return true;
    }
    return false;
}
#define POLL(BODY) { unsigned sp_ = 0; bool bad; \
    do { int z_ = 0; do { MEMB(); BODY } while (bad && ++z_ < 16); } while (__builtin_expect(bad, 0) && !spin_fail16(p, sp_)); }
__device__ __forceinline__ bool spin_fail16(const Params& p, unsigned& spins) { spins += 15; return spin_fail(p, spins); }
__device__ __forceinline__ void wait_flags(const Params& p, rsrc_t rs, unsigned voff, unsigned soff, bool use) {
    unsigned spins = 0;
    for (;;) {
        const unsigned f = use ? ld32(rs, voff, soff) : 1u;
        if (__all(f != 0u)) break;
        MEMB();
        __builtin_amdgcn_s_sleep(WFS);
        if (spin_fail(p, spins)) break;
    }
    MEMB();
}
template <int N> __device__ __forceinline__ bool bad_any(const u32x4 (&v)[N]) {
    unsigned m = mx4(v[0]);
#pragma unroll
    for (int i = 1; i < N; ++i) m = max(m, mx4(v[i]));
    return __any(m == POISON);
}
template <int N>
__device__ __forceinline__ void regather(const Params& p, rsrc_t rs, unsigned lo, unsigned soff, u32x4 (&v)[N], bool slp) {
    unsigned spins = 0;
    do {
        if (slp) __builtin_amdgcn_s_sleep(4);
#pragma unroll
        for (int i = 0; i < N; ++i) if (mx4(v[i]) == POISON) v[i] = ld128(rs, lo + i * 1024, soff);
        WAITV0();
    } while (bad_any(v) && !spin_fail(p, spins));
}
template <int N>
__device__ __forceinline__ void gather(const Params& p, rsrc_t rs, unsigned lo, unsigned soff, u32x4 (&v)[N], bool slp) {
#pragma unroll
    for (int i = 0; i < N; ++i) v[i] = ld128(rs, lo + i * 1024, soff);
    if (__builtin_expect(bad_any(v), 0)) regather(p, rs, lo, soff, v, slp);
}

template <int CPR, int NROW, typename SrcF>
__device__ __forceinline__ void stage_issue(char* buf, int w, int l, SrcF src  ) {
    constexpr int PPW = CPR * NROW / 512;
    static_assert(CPR * NROW % 512 == 0 && CPR % 8 == 0, "chunk size");
#pragma unroll
    for (int i = 0; i < PPW; ++i) {
        const unsigned idx = (unsigned)((w * PPW + i) * 64 + l), r = idx / CPR, c = (idx % CPR) ^ ((r & 3) << 1);
        __builtin_amdgcn_global_load_lds((gptr_t)src(r, c), (lds_ptr_t)(buf + (w * PPW + i) * 1024), 16, 0, 0);
    }
}
template <int CPR, bool NAT>
__device__ __forceinline__ bf16x8 tr_frag(const char* buf, int r0, unsigned cb, int l) {
    const int g = l >> 4, q = (l & 15) >> 2;
    const unsigned ra = r0 + (NAT ? 8 * g : 4 * g) + q;
    const unsigned co = (((cb >> 4) ^ (q << 1)) << 4) + (cb & 15);
    const unsigned ad = (unsigned)(size_t)(lds_ptr_t)buf + ra * (CPR * 16) + co;
    u32x2 a, b;
    asm volatile("ds_read_b64_tr_b16 %0, %2\n\tds_read_b64_tr_b16 %1, %2 offset:%3\n\ts_waitcnt lgkmcnt(0)"
                 : "=&v"(a), "=&v"(b) : "v"(ad), "i"((NAT ? 4 : 16) * CPR * 16) : "memory");
    return asbf((u32x4){a[0], a[1], b[0], b[1]});
}
template <int CPR>
__device__ __forceinline__ void tr_frag6(bf16x8 (&f)[6], const char* buf, unsigned cb, int l) {
    const int g = l >> 4, q = (l & 15) >> 2;
    const unsigned ad = (unsigned)(size_t)(lds_ptr_t)buf + (8 * g + q) * (CPR * 16) + (((cb >> 4) ^ (q << 1)) << 4) + (cb & 15);
    u32x2 a[12];
    static_assert(CPR == 16, "offsets below");
    asm volatile("ds_read_b64_tr_b16 %0, %12\n\tds_read_b64_tr_b16 %1, %12 offset:1024\n\t"
                 "ds_read_b64_tr_b16 %2, %12 offset:8192\n\tds_read_b64_tr_b16 %3, %12 offset:9216\n\t"
                 "ds_read_b64_tr_b16 %4, %12 offset:16384\n\tds_read_b64_tr_b16 %5, %12 offset:17408\n\t"
                 "ds_read_b64_tr_b16 %6, %12 offset:24576\n\tds_read_b64_tr_b16 %7, %12 offset:25600\n\t"
                 "ds_read_b64_tr_b16 %8, %12 offset:32768\n\tds_read_b64_tr_b16 %9, %12 offset:33792\n\t"
                 "ds_read_b64_tr_b16 %10, %12 offset:40960\n\tds_read_b64_tr_b16 %11, %12 offset:41984\n\ts_waitcnt lgkmcnt(0)"
                 : "=&v"(a[0]), "=&v"(a[1]), "=&v"(a[2]), "=&v"(a[3]), "=&v"(a[4]), "=&v"(a[5]), "=&v"(a[6]), "=&v"(a[7]), "=&v"(a[8]), "=&v"(a[9]), "=&v"(a[10]), "=&v"(a[11])
                 : "v"(ad) : "memory");
#pragma unroll
    for (int i = 0; i < 6; ++i) f[i] = asbf((u32x4){a[2 * i][0], a[2 * i][1], a[2 * i + 1][0], a[2 * i + 1][1]});
}
#define ONES8 ((bf16x8){0x3f80, 0x3f80, 0x3f80, 0x3f80, 0x3f80, 0x3f80, 0x3f80, 0x3f80})

#define SMFMA(a, b, c, ix) __builtin_amdgcn_smfmac_f32_16x16x64_bf16((a), (b), (c), (ix), 0, 0)
__device__ __forceinline__ bf16x16 cat16(bf16x8 a, bf16x8 b) { return __builtin_shufflevector(a, b, 0, 1, 2, 3, 4, 5, 6, 7, 8, 9, 10, 11, 12, 13, 14, 15); }
__device__ __forceinline__ bf16x8 half16(bf16x16 a, int hi) {
    return hi ? __builtin_shufflevector(a, a, 8, 9, 10, 11, 12, 13, 14, 15) : __builtin_shufflevector(a, a, 0, 1, 2, 3, 4, 5, 6, 7);
}
__device__ __forceinline__ u32x2 fold2(f32x4 a, f32x4 b) {
    float s[4];
#pragma unroll
    for (int i = 0; i < 4; ++i) {
        auto r = __builtin_amdgcn_permlane32_swap(__float_as_uint(a[i]), __float_as_uint(b[i]), false, false);
        s[i] = __uint_as_float(r[0]) + __uint_as_float(r[1]);
    }
    return (u32x2){pack2(s[0], s[1]), pack2(s[2], s[3])};
}
typedef __attribute__((ext_vector_type(4))) short s16x4;
__device__ __forceinline__ unsigned troff(unsigned lo) { return ((lo >> 6) << 4) + ((lo >> 1) & 8); }
__device__ __forceinline__ bf16x8 trf(const char* base, int i) {
    typedef __attribute__((address_space(3))) s16x4* tp_t;
    const s16x4 a = __builtin_amdgcn_ds_read_tr16_b64_v4i16((tp_t)(base + i * 512)), b = __builtin_amdgcn_ds_read_tr16_b64_v4i16((tp_t)(base + i * 512 + 256));
    return __builtin_shufflevector(a, b, 0, 1, 2, 3, 4, 5, 6, 7);
}
#define LDS(T, a) (*(__attribute__((address_space(3))) T*)(unsigned)(a))
__device__ __forceinline__ bf16x8 trfa(unsigned a) {
    typedef __attribute__((address_space(3))) s16x4* tp_t;
    const s16x4 x = __builtin_amdgcn_ds_read_tr16_b64_v4i16((tp_t)a), y = __builtin_amdgcn_ds_read_tr16_b64_v4i16((tp_t)(a + 256));
    return __builtin_shufflevector(x, y, 0, 1, 2, 3, 4, 5, 6, 7);
}
#define LSPIN(off, val) { for (;;) { MEMB(); if (__builtin_amdgcn_readfirstlane(mn4(*(const u32x4*)(smem + (off)))) >= (unsigned)(val)) break; } MEMB(); }
#define LADD(off) { unsigned o_; \
    asm volatile("v_mov_b32 %0, 1\n\ts_mov_b64 exec, 1\n\tds_add_u32 %1, %0 offset:%2\n\ts_mov_b64 exec, -1" : "=&v"(o_) : "v"(lo), "i"(off) : "memory"); }
#define LADDV(off, sv) { unsigned o_; \
    asm volatile("v_mov_b32 %0, %3\n\ts_mov_b64 exec, 1\n\tds_add_u32 %1, %0 offset:%2\n\ts_mov_b64 exec, -1" : "=&v"(o_) : "v"(lo), "i"(off), "s"(sv) : "memory"); }
#define LWORD(off) (*(unsigned*)(smem + (off) + (lo >> 10)))
template <int IMM>
__device__ __forceinline__ void dma_raw(u32x4 rd, unsigned dst, unsigned voff, unsigned soff) {
    unsigned keep;
    asm volatile("s_mov_b32 %0, m0\n\ts_mov_b32 m0, %1\n\ts_nop 0\n\tbuffer_load_dwordx4 %2, %3, %4 offen offset:%5 sc1 lds\n\ts_mov_b32 m0, %0"
                 : "=&s"(keep) : "s"(__builtin_amdgcn_readfirstlane(dst)), "v"(voff), "s"(rd), "s"(__builtin_amdgcn_readfirstlane(soff)), "i"(IMM) : "memory");
}
#define RAWRD() const u32x4 rd = (u32x4){(unsigned)(size_t)p.ws, (unsigned)((size_t)p.ws >> 32) & 0xffffu, 0x7fffffffu, 0x00020000u}; \
    const unsigned lb = (unsigned)(size_t)(lds_ptr_t)smem;
#define LWAITX(off, val, slp) { unsigned a_, d_, s_, n_; \
    asm volatile("v_mov_b32 %0, %4\n\ts_mov_b32 %3, 0x400000\n1:\n\tds_read_b32 %1, %0\n\ts_waitcnt lgkmcnt(0)\n\tv_readfirstlane_b32 %2, %1\n\t" \
                 "s_cmp_ge_u32 %2, %5\n\ts_cbranch_scc1 2f\n\t" slp "s_sub_u32 %3, %3, 1\n\ts_cbranch_scc0 1b\n2:" \
                 : "=&v"(a_), "=&v"(d_), "=&s"(s_), "=&s"(n_) : "s"(__builtin_amdgcn_readfirstlane((unsigned)(off))), "s"(__builtin_amdgcn_readfirstlane((unsigned)(val))) : "memory", "scc"); }
#define LWAIT(off, val) LWAITX(off, val, "")
#define LWAITS(off, val) LWAITX(off, val, "s_sleep 1\n\t")
#define FLRD(off) { MEMB(); fl = LWORD(off); }
#define FLOK(val) (__builtin_amdgcn_readfirstlane(fl) >= (unsigned)(val))
#define LSET(off, val) { MEMB(); LWORD(off) = (unsigned)(val); MEMB(); }
__device__ __forceinline__ f32x2 ln_stat(f32x2 a) {
    const float mean = a[0] * (1.f / 640.f);
    const float var = bfr(fmaxf(__builtin_fmaf(-mean, mean, a[1] * (1.f / 640.f)), 0.f));
    const float den = bfr(__builtin_amdgcn_sqrtf(bfr(var + 1e-5f)));
    return (f32x2){bfr(mean), __builtin_amdgcn_rcpf(den)};
}
#define KSO(i) ((i) % 5 == 4 ? 16 + (i) / 5 : (i) - (i) / 5)
#define XPC(dst, src, gq, i) { if ((i) < 4) DMAI((dst) + (gq) * 5120, lo, (src) + (unsigned)(gq) * 4096u, ((i) & 3) * 1024); else DMA((dst) + (gq) * 5120 + 4096, lo, (src) + (unsigned)(16 + (gq)) * 1024u); }
#define XGRP(dst, src, gq) { XPC(dst, src, gq, 0) XPC(dst, src, gq, 1) XPC(dst, src, gq, 2) XPC(dst, src, gq, 3) XPC(dst, src, gq, 4) }
template <int I> struct IC { static constexpr int v = I; };
template <int I, int N, typename F> __device__ __forceinline__ void static_for(F f) { if constexpr (I < N) { f(IC<I>{}); static_for<I + 1, N>(f); } }
template <typename F, typename E>
__device__ __forceinline__ void proj2(const bf16x16 (&wt)[3][8], const char* xf, const char* xg, const char* wl, F side, E epi) {
    bf16x8 B[40], W[8][3];
    f32x4 a[2][3] = {{Z4, Z4, Z4}, {Z4, Z4, Z4}};
#pragma unroll
    for (int i = 0; i < 3; ++i) B[i] = *(const bf16x8*)(xf + i * 1024);
    static_for<0, 40>([&](auto ic) __attribute__((always_inline)) {
        constexpr int i = decltype(ic)::v, k = i % 20, tt = i / 20;
        side(ic);
        SB0();
#pragma unroll
        for (int j = 0; j < 3; ++j) {
            a[tt][j] = MFMA(i % 5 == 4 ? W[i / 5][j] : half16(wt[j][(KSO(k) & 15) >> 1], KSO(k) & 1), B[i], a[tt][j]);
            asm volatile("" : "+v"(a[tt][j]));
            SB0();
            if (j == 0) { if constexpr (i + 3 < 40) B[i + 3] = *(const bf16x8*)((i + 3 < 20 ? xf : xg) + (i + 3) * 1024); }
            if (j == 1) { if constexpr (i % 5 < 3 && i != 20) W[i / 5][i % 5] = *(const bf16x8*)(wl + ((k / 5) * 3 + i % 5) * 1024); }
            if (j == 2) { if constexpr (i == 21) W[4][0] = *(const bf16x8*)wl; }
            SB0();
        }
        if constexpr (i == 20) { epi(0, 0, a[0][0]); epi(0, 1, a[0][1]); }
        if constexpr (i == 21) epi(0, 2, a[0][2]);
        SB0();
    });
#pragma unroll
    for (int j = 0; j < 3; ++j) epi(1, j, a[1][j]);
}

template <typename F, typename E>
__device__ __forceinline__ void proj1(const bf16x16 (&wt)[3][8], const unsigned xf, const unsigned wl, F side, E epi) {
    bf16x8 B[20], W[4][3];
    f32x4 a[3] = {Z4, Z4, Z4};
#pragma unroll
    for (int i = 0; i < 3; ++i) B[i] = LDS(bf16x8, xf + i * 1024);
    static_for<0, 20>([&](auto ic) __attribute__((always_inline)) {
        constexpr int i = decltype(ic)::v;
        side(ic);
        SB0();
#pragma unroll
        for (int j = 0; j < 3; ++j) {
            a[j] = MFMA(i % 5 == 4 ? W[i / 5][j] : half16(wt[j][(KSO(i) & 15) >> 1], KSO(i) & 1), B[i], a[j]);
            asm volatile("" : "+v"(a[j]));
            SB0();
            if (j == 0) { if constexpr (i + 3 < 20) B[i + 3] = LDS(bf16x8, xf + (i + 3) * 1024); }
            if (j == 1) { if constexpr (i % 5 < 3) W[i / 5][i % 5] = LDS(bf16x8, wl + ((i / 5) * 3 + i % 5) * 1024); }
            SB0();
        }
    });
    epi((u32x4){pack2(a[0][0], a[0][1]), pack2(a[0][2], a[0][3]), pack2(a[1][0], a[1][1]), pack2(a[1][2], a[1][3])}, (u32x2){pack2(a[2][0], a[2][1]), pack2(a[2][2], a[2][3])});
}

template <typename F, typename E>
__device__ __forceinline__ void proj1s(const bf16x16 (&wt)[3][8], bf16x8 (&hd)[3], const unsigned xf, const unsigned& xn, const unsigned wl, F side, E epi) {
    bf16x8 B[23], W[4][3];
    f32x4 a[3];
#pragma unroll
    for (int i = 0; i < 3; ++i) B[i] = hd[i];
    static_for<0, 20>([&](auto ic) __attribute__((always_inline)) {
        constexpr int i = decltype(ic)::v;
        side(ic);
        SB0();
#pragma unroll
        for (int j = 0; j < 3; ++j) {
            a[j] = MFMA(i % 5 == 4 ? W[i / 5][j] : half16(wt[j][(KSO(i) & 15) >> 1], KSO(i) & 1), B[i], i ? a[j] : Z4);
            asm volatile("" : "+v"(a[j]));
            SB0();
            if (j == 0) B[i + 3] = LDS(bf16x8, i + 3 < 20 ? xf + (i + 3) * 1024 : xn + (i - 17) * 1024);
            if (j == 1) { if constexpr (i % 5 < 3) W[i / 5][i % 5] = LDS(bf16x8, wl + ((i / 5) * 3 + i % 5) * 1024); }
            SB0();
        }
    });
#pragma unroll
    for (int i = 0; i < 3; ++i) hd[i] = B[20 + i];
    epi((u32x4){pack2(a[0][0], a[0][1]), pack2(a[0][2], a[0][3]), pack2(a[1][0], a[1][1]), pack2(a[1][2], a[1][3])}, (u32x2){pack2(a[2][0], a[2][1]), pack2(a[2][2], a[2][3])});
}

template <int I0, typename S>
__device__ __forceinline__ void projh(const bf16x16 (&wt)[3][8], const unsigned xf, const unsigned wl, f32x4 (&a)[3], S sync) {
    bf16x8 B[20], W[4][3];
#pragma unroll
    for (int i = I0; i < I0 + 3; ++i) B[i] = LDS(bf16x8, xf + i * 1024);
    sync();
    static_for<I0, I0 + 10>([&](auto ic) __attribute__((always_inline)) {
        constexpr int i = decltype(ic)::v;
        SB0();
#pragma unroll
        for (int j = 0; j < 3; ++j) {
            a[j] = MFMA(i % 5 == 4 ? W[i / 5][j] : half16(wt[j][(KSO(i) & 15) >> 1], KSO(i) & 1), B[i], i ? a[j] : Z4);
            asm volatile("" : "+v"(a[j]));
            SB0();
            if (j == 0) { if constexpr (i + 3 < I0 + 10) B[i + 3] = LDS(bf16x8, xf + (i + 3) * 1024); }
            if (j == 1) { if constexpr (i % 5 < 3) W[i / 5][i % 5] = LDS(bf16x8, wl + ((i / 5) * 3 + i % 5) * 1024); }
            SB0();
        }
    });
}

#define PDIST 4
__device__ __forceinline__ void fill16(char* dst, unsigned bytes, unsigned gt, unsigned val) {
    for (unsigned i = gt; i < bytes / 16; i += 256 * 512) *(u32x4*)(dst + i * 16) = (u32x4){val, val, val, val};
}
__device__ __forceinline__ unsigned hot_base(const Params&) { return OFF_HOT; }
__global__ __launch_bounds__(512, 2) void init_kernel(Params p) {
    extern __shared__ __attribute__((aligned(16))) char smem[];
    const int tid = threadIdx.x, l = tid & 63, w = __builtin_amdgcn_readfirstlane(tid >> 6), b = blockIdx.x;
    const unsigned gt = b * 512 + tid;
    const unsigned* xr = (const unsigned*)(p.x + (size_t)(b * 8 + w) * 640);
    unsigned xd[5];
#pragma unroll
    for (int i = 0; i < 5; ++i) xd[i] = xr[i * 64 + l];
    fill16(p.ws, CTRL_BYTES, gt, 0u);
    fill16(p.ws + OFF_X1, PDIST * 40960, (gt + 90000) & 131071, POISON);
    fill16(p.ws + OFF_X2, PDIST * 40960, (gt + 40000) & 131071, POISON);
    fill16(p.ws + OFF_X3, PDIST * 40960, (gt + 60000) & 131071, POISON);
    fill16(p.ws + OFF_HG, PDIST * 98304, (gt + 70000) & 131071, POISON);
    fill16(p.ws + OFF_HG + 64u * 98304, PDIST * 98304, (gt + 100000) & 131071, POISON);
    const unsigned hb = hot_base(p);
    fill16(p.ws + hb + 4194304 + HB_MEM, 16u * 16384, (gt + 20000) & 131071, POISON);
    fill16(p.ws + hb + 4194304 + HB_TS, 8u * 16384, (gt + 50000) & 131071, POISON);
    {
        float v[10], sm = 0.f;
#pragma unroll
        for (int i = 0; i < 5; ++i) { v[2 * i] = bflo(xd[i]); v[2 * i + 1] = bfhi(xd[i]); sm += v[2 * i] + v[2 * i + 1]; }
#pragma unroll
        for (int o = 1; o < 64; o <<= 1) sm += __shfl_xor(sm, o);
        const float mean = sm * (1.f / 640.f);
        float q = 0.f;
#pragma unroll
        for (int i = 0; i < 10; ++i) { const float d = v[i] - mean; q += d * d; }
#pragma unroll
        for (int o = 1; o < 64; o <<= 1) q += __shfl_xor(q, o);
        const float den = bfr(sqrtf(bfr(bfr(q * (1.f / 640.f)) + 1e-5f))), mb = bfr(mean);
        unsigned* xn = (unsigned*)(smem + w * 1280);
#pragma unroll
        for (int i = 0; i < 5; ++i) { xn[i * 64 + l] = pack2(bfr(v[2 * i] - mb) / den, bfr(v[2 * i + 1] - mb) / den); xn[2560 + i * 64 + l] = xd[i]; }
        RAWBAR()
        for (int i = tid; i < 1280; i += 512) {
            const int raw = i >= 640, k = i - raw * 640, r = k & 7, u = k >> 3;
            const char* src = smem + raw * 10240 + r * 1280 + ((u >> 2) * 32 + (u & 3) * 4) * 2;
            const u32x2 y0 = *(const u32x2*)src, y1 = *(const u32x2*)(src + 32);
            *(u32x4*)(p.ws + (raw ? OFF_X0 : OFF_XN) + (size_t)(b >> 1) * 20480 + u * 256 + ((b & 1) * 8 + r) * 16) = (u32x4){y0[0], y0[1], y1[0], y1[1]};
        }
    }
    WAITV0();
}

#define SU(k)
#define TDECL(c)
#define TDECLL(c, off)
#define STAMP(k)
#define STFLUSH(id, it)
#define DBGDECL(bit, c, off)

__device__ __forceinline__ unsigned hot_page(const Params& p, rsrc_t rs, const int pg, const int far = 0) {
    unsigned spins = 0, v;
    for (;;) {
        v = __builtin_amdgcn_readfirstlane(ld32(rs, 0, FL_PAR + pg * 4));
        MEMB();
        if (v || spin_fail(p, spins)) break;
        __builtin_amdgcn_s_sleep(8);
    }
    return __builtin_amdgcn_readfirstlane(hot_base(p) + pg * 2097152u + ((v ^ far) & 1u) * 8192u);
}
#define BSX 0x2000
__device__ __forceinline__ unsigned near_sel(unsigned hp) { return ((hp ^ (hp >> 8)) & 0x2000u) ^ BSX; }
__device__ __forceinline__ void cal_role(const Params& p, const int w, const int pg) {
    if (w) return;
    const int tid = lane_id();
    const rsrc_t rs = __builtin_amdgcn_make_buffer_rsrc((void*)p.ws, 0, 0x7fffffff, 0x00020000);
    const unsigned base = hot_base(p) + pg * 2097152u + HB_END;
#pragma unroll
    for (int i = 0; i < 16; ++i) st128(rs, tid * 16, base + i * 2048, (u32x4){0u, 0u, 0u, 0u});
    WAITV0();
    unsigned best[2] = {~0u, ~0u};
#pragma unroll 1
    for (int i = 0; i < NCAL; ++i) {
#pragma unroll
        for (int k = 0; k < 2; ++k) {
            const unsigned t0 = (unsigned)__builtin_amdgcn_s_memtime();
            unsigned v = ld32(rs, 0, base + k * 8192 + (i & 3) * 2048 + ((i >> 2) & 1) * 16384 + (i >> 3) * 128);
            WAITV0();
            asm volatile("" : "+v"(v));
            best[k] = min(best[k], (unsigned)__builtin_amdgcn_s_memtime() - t0);
        }
    }
    if (tid == 0) {
        unsigned xcc; asm volatile("s_getreg_b32 %0, hwreg(HW_REG_XCC_ID)" : "=s"(xcc));
        unsigned v = (best[1] < best[0] ? 1u : 0u) ^ ((xcc >> 2) & 1u);
        st32(rs, 0, FL_PAR + pg * 4, v | 2u);
    }
    WAITV0();
}

__device__ __forceinline__ void ck_wait(const Params& p, rsrc_t rs, const int s, const int q, const int d) {
    unsigned spins = 0;
    u32x2 tv;
    const int tag = min(s + 2, NSEG);
    for (;;) {
        MEMB();
        tv = __builtin_amdgcn_raw_buffer_load_b64(rs, 0, (int)(FL_T + ((tag & 1) ^ 1) * 512 + q * 128), AUX_SC1);
        if ((int)__builtin_amdgcn_readfirstlane(tv[1]) >= tag || spin_fail(p, spins)) break;
        __builtin_amdgcn_s_sleep(4);
    }
    if ((int)__builtin_amdgcn_readfirstlane(tv[1]) == tag) wait_until(__builtin_amdgcn_readfirstlane(tv[0]) + d + (s == NSEG - 1 ? DLAST : 0));
}

#define QKM_ASM \
    "s_mov_b32 s32, 0\n" \
    "s_mov_b32 s34, 0\n" \
    "s_mov_b32 s35, 1\n" \
    "s_mov_b32 s47, 0x40000\n" \
    "s_mov_b32 s45, 0\n" \
    "s_memrealtime s[50:51]\n" \
    "s_waitcnt lgkmcnt(0)\n" \
    "s_sub_u32 s48, s50, 1000000\n" \
    "s_mov_b32 s49, s48\n" \
    "s_cmp_gt_u32 s16, 3\n" \
    "s_cbranch_scc1 .Lqkm_w4_%=\n" \
    "s_cmp_eq_u32 s16, 3\n" \
    "s_cbranch_scc1 .Lqkm_w3_%=\n" \
    "s_cmp_eq_u32 s16, 2\n" \
    "s_cbranch_scc1 .Lqkm_w2_%=\n" \
    "s_cmp_eq_u32 s16, 1\n" \
    "s_cbranch_scc1 .Lqkm_w1_%=\n" \
    ".Lqkm_w0_%=:\n" \
    "s_mov_b32 s39, 139264\n" \
    "s_mov_b32 s52, 0xff00ff00\n" \
    "s_mov_b32 s53, 0xff00ff00\n" \
    ".Lqkm_l0_%=:\n" \
    "ds_read_b128 v[240:243], v192 offset:16384\n" \
    "ds_read_b128 v[244:247], v192 offset:19456\n" \
    "ds_read_b128 v[248:251], v192 offset:17408\n" \
    "ds_read_b128 v[252:255], v192 offset:20480\n" \
    "v_mov_b64 v[204:205], 0\n" \
    "v_mov_b64 v[206:207], 0\n" \
    "v_mov_b64 v[208:209], 0\n" \
    "v_mov_b64 v[210:211], 0\n" \
    "v_mov_b64 v[212:213], 0\n" \
    "v_mov_b64 v[214:215], 0\n" \
    "s_barrier\n" \
    "ds_read_b128 v[216:219], v192 offset:0\n" \
    "ds_read_b128 v[220:223], v192 offset:1024\n" \
    "ds_read_b128 v[232:235], v192 offset:8192\n" \
    "ds_read_b128 v[224:227], v192 offset:2048\n" \
    "s_xor_b32 s39, s39, 253952\n" \
    "v_add_u32 v200, s39, v192\n" \
    "s_mov_b32 s33, s34\n" \
    "s_add_u32 s37, s32, 1\n" \
    "s_sub_i32 s36, s37, %[lag]\n" \
    "s_bitcmp1_b32 s36, 0\n" \
    "s_cselect_b32 s34, 128, 2*%[lag]\n" \
    "s_add_i32 s34, s34, s36\n" \
    "s_lshr_b32 s34, s34, 1\n" \
    "s_cmp_gt_u32 s36, 127-2*%[lag]\n" \
    "s_cselect_b32 s34, s37, s34\n" \
    "s_lshr_b32 s36, s33, 6\n" \
    "s_lshl_b32 s36, s36, 2\n" \
    "s_and_b32 s37, s33, 3\n" \
    "s_or_b32 s36, s36, s37\n" \
    "s_waitcnt lgkmcnt(3)\n" \
    "v_smfmac_f32_16x16x64_bf16 v[204:207], v[216:219], v[0:7], v193\n" \
    "ds_read_b128 v[228:231], v192 offset:3072\n" \
    "v_smfmac_f32_16x16x64_bf16 v[208:211], v[216:219], v[64:71], v193\n" \
    "v_smfmac_f32_16x16x64_bf16 v[212:215], v[216:219], v[128:135], v193\n" \
    "s_waitcnt lgkmcnt(3)\n" \
    "v_smfmac_f32_16x16x64_bf16 v[204:207], v[220:223], v[8:15], v193\n" \
    "ds_read_b128 v[216:219], v192 offset:4096\n" \
    "v_smfmac_f32_16x16x64_bf16 v[208:211], v[220:223], v[72:79], v193\n" \
    "ds_read_b128 v[236:239], v192 offset:9216\n" \
    "v_smfmac_f32_16x16x64_bf16 v[212:215], v[220:223], v[136:143], v193\n" \
    "s_waitcnt lgkmcnt(4)\n" \
    "v_smfmac_f32_16x16x64_bf16 v[204:207], v[232:235], v[240:247], v193\n" \
    "s_waitcnt lgkmcnt(3)\n" \
    "v_smfmac_f32_16x16x64_bf16 v[204:207], v[224:227], v[16:23], v193\n" \
    "ds_read_b128 v[220:223], v192 offset:5120\n" \
    "v_smfmac_f32_16x16x64_bf16 v[208:211], v[224:227], v[80:87], v193\n" \
    "ds_read_b128 v[240:243], v192 offset:18432\n" \
    "v_smfmac_f32_16x16x64_bf16 v[212:215], v[224:227], v[144:151], v193\n" \
    "ds_read_b128 v[244:247], v192 offset:21504\n" \
    "v_smfmac_f32_16x16x64_bf16 v[208:211], v[232:235], v[248:255], v193\n" \
    "s_waitcnt lgkmcnt(5)\n" \
    "v_smfmac_f32_16x16x64_bf16 v[204:207], v[228:231], v[24:31], v193\n" \
    "ds_read_b128 v[224:227], v192 offset:6144\n" \
    "v_smfmac_f32_16x16x64_bf16 v[208:211], v[228:231], v[88:95], v193\n" \
    "ds_read_b128 v[248:251], v192 offset:22528\n" \
    "v_smfmac_f32_16x16x64_bf16 v[212:215], v[228:231], v[152:159], v193\n" \
    "ds_read_b128 v[252:255], v192 offset:25600\n" \
    "s_waitcnt lgkmcnt(7)\n" \
    "v_smfmac_f32_16x16x64_bf16 v[204:207], v[216:219], v[32:39], v193\n" \
    "ds_read_b128 v[228:231], v192 offset:7168\n" \
    "v_smfmac_f32_16x16x64_bf16 v[208:211], v[216:219], v[96:103], v193\n" \
    "v_smfmac_f32_16x16x64_bf16 v[212:215], v[216:219], v[160:167], v193\n" \
    "s_waitcnt lgkmcnt(4)\n" \
    "v_smfmac_f32_16x16x64_bf16 v[212:215], v[232:235], v[240:247], v193\n" \
    "s_waitcnt lgkmcnt(1)\n" \
    "v_smfmac_f32_16x16x64_bf16 v[204:207], v[236:239], v[248:255], v193\n" \
    "ds_read_b128 v[240:243], v192 offset:23552\n" \
    "v_smfmac_f32_16x16x64_bf16 v[204:207], v[220:223], v[40:47], v193\n" \
    "ds_read_b128 v[244:247], v192 offset:26624\n" \
    "v_smfmac_f32_16x16x64_bf16 v[208:211], v[220:223], v[104:111], v193\n" \
    "ds_read_b128 v[248:251], v192 offset:24576\n" \
    "v_smfmac_f32_16x16x64_bf16 v[212:215], v[220:223], v[168:175], v193\n" \
    "ds_read_b128 v[252:255], v192 offset:27648\n" \
    "v_smfmac_f32_16x16x64_bf16 v[204:207], v[224:227], v[48:55], v193\n" \
    "v_smfmac_f32_16x16x64_bf16 v[208:211], v[224:227], v[112:119], v193\n" \
    "v_smfmac_f32_16x16x64_bf16 v[212:215], v[224:227], v[176:183], v193\n" \
    "s_waitcnt lgkmcnt(4)\n" \
    "v_smfmac_f32_16x16x64_bf16 v[204:207], v[228:231], v[56:63], v193\n" \
    "s_waitcnt lgkmcnt(2)\n" \
    "v_smfmac_f32_16x16x64_bf16 v[208:211], v[236:239], v[240:247], v193\n" \
    "v_smfmac_f32_16x16x64_bf16 v[208:211], v[228:231], v[120:127], v193\n" \
    "s_waitcnt lgkmcnt(0)\n" \
    "v_smfmac_f32_16x16x64_bf16 v[212:215], v[236:239], v[248:255], v193\n" \
    "v_smfmac_f32_16x16x64_bf16 v[212:215], v[228:231], v[184:191], v193\n" \
    "ds_read_b128 v[216:219], v200 offset:13312\n" \
    "ds_read_b128 v[220:223], v200 offset:14336\n" \
    "ds_read_b128 v[224:227], v200 offset:15360\n" \
    "ds_read_b128 v[228:231], v200 offset:16384\n" \
    "s_lshl_b32 s36, s36, s18\n" \
    "s_add_u32 s38, s17, s36\n" \
    "s_add_u32 s32, s32, 1\n" \
    "v_pk_add_f32 v[204:205], v[204:205], v[206:207]\n" \
    "v_pk_add_f32 v[208:209], v[208:209], v[210:211]\n" \
    "v_pk_add_f32 v[212:213], v[212:213], v[214:215]\n" \
    "v_cvt_pk_bf16_f32 v204, v204, v205\n" \
    "v_cvt_pk_bf16_f32 v208, v208, v209\n" \
    "ds_write2st64_b32 v194, v204, v208 offset1:1\n" \
    "v_cvt_pk_bf16_f32 v212, v212, v213\n" \
    "ds_write_b32 v195, v212\n" \
    "ds_read_b128 v[212:215], v200 offset:12288\n" \
    "s_waitcnt lgkmcnt(1)\n" \
    "s_barrier\n" \
    "ds_read_b64_tr_b16 v[232:233], v196 offset:10240\n" \
    "ds_read_b64_tr_b16 v[234:235], v196 offset:10496\n" \
    "ds_read_b64_tr_b16 v[236:237], v196 offset:10752\n" \
    "ds_read_b64_tr_b16 v[238:239], v196 offset:11008\n" \
    "ds_read_b64_tr_b16 v[240:241], v196 offset:11264\n" \
    "ds_read_b64_tr_b16 v[242:243], v196 offset:11520\n" \
    "ds_read_b64_tr_b16 v[244:245], v196 offset:11776\n" \
    "ds_read_b64_tr_b16 v[246:247], v196 offset:12032\n" \
    "ds_read_b64_tr_b16 v[248:249], v196 offset:12288\n" \
    "ds_read_b64_tr_b16 v[250:251], v196 offset:12544\n" \
    "ds_read_b64_tr_b16 v[252:253], v196 offset:12800\n" \
    "ds_read_b64_tr_b16 v[254:255], v196 offset:13056\n" \
    "s_waitcnt lgkmcnt(10)\n" \
    "v_mfma_f32_16x16x32_bf16 v[204:207], v[212:215], v[232:235], 0\n" \
    "ds_read_b128 v[212:215], v200 offset:17408\n" \
    "s_waitcnt lgkmcnt(9)\n" \
    "v_mfma_f32_16x16x32_bf16 v[204:207], v[216:219], v[236:239], v[204:207]\n" \
    "ds_read_b128 v[216:219], v200 offset:18432\n" \
    "s_waitcnt lgkmcnt(8)\n" \
    "v_mfma_f32_16x16x32_bf16 v[204:207], v[220:223], v[240:243], v[204:207]\n" \
    "ds_read_b128 v[220:223], v200 offset:19456\n" \
    "s_waitcnt lgkmcnt(7)\n" \
    "v_mfma_f32_16x16x32_bf16 v[204:207], v[224:227], v[244:247], v[204:207]\n" \
    "ds_read_b128 v[224:227], v200 offset:20480\n" \
    "s_waitcnt lgkmcnt(6)\n" \
    "v_mfma_f32_16x16x32_bf16 v[204:207], v[228:231], v[248:251], v[204:207]\n" \
    "ds_read_b128 v[228:231], v200 offset:21504\n" \
    "s_waitcnt lgkmcnt(4)\n" \
    "v_mfma_f32_16x16x32_bf16 v[204:207], v[212:215], v[252:255], v[204:207]\n" \
    "ds_read_b128 v[212:215], v200 offset:22528\n" \
    "s_waitcnt lgkmcnt(4)\n" \
    "v_mfma_f32_16x16x32_bf16 v[208:211], v[216:219], v[232:235], 0\n" \
    "ds_read_b128 v[216:219], v200 offset:23552\n" \
    "s_waitcnt lgkmcnt(4)\n" \
    "v_mfma_f32_16x16x32_bf16 v[208:211], v[220:223], v[236:239], v[208:211]\n" \
    "ds_read_b64_tr_b16 v[220:221], v196 offset:13312\n" \
    "ds_read_b64_tr_b16 v[222:223], v196 offset:13568\n" \
    "s_waitcnt lgkmcnt(5)\n" \
    "v_mfma_f32_16x16x32_bf16 v[208:211], v[224:227], v[240:243], v[208:211]\n" \
    "ds_read_b64_tr_b16 v[224:225], v196 offset:13824\n" \
    "ds_read_b64_tr_b16 v[226:227], v196 offset:14080\n" \
    "s_waitcnt lgkmcnt(6)\n" \
    "v_mfma_f32_16x16x32_bf16 v[208:211], v[228:231], v[244:247], v[208:211]\n" \
    "ds_read_b64_tr_b16 v[228:229], v196 offset:14336\n" \
    "ds_read_b64_tr_b16 v[230:231], v196 offset:14592\n" \
    "s_waitcnt lgkmcnt(7)\n" \
    "v_mfma_f32_16x16x32_bf16 v[208:211], v[212:215], v[248:251], v[208:211]\n" \
    "ds_read_b64_tr_b16 v[212:213], v196 offset:14848\n" \
    "ds_read_b64_tr_b16 v[214:215], v196 offset:15104\n" \
    "s_waitcnt lgkmcnt(8)\n" \
    "v_mfma_f32_16x16x32_bf16 v[208:211], v[216:219], v[252:255], v[208:211]\n" \
    "ds_read_b64_tr_b16 v[216:217], v196 offset:15360\n" \
    "ds_read_b64_tr_b16 v[218:219], v196 offset:15616\n" \
    "s_waitcnt lgkmcnt(8)\n" \
    "v_mfma_f32_16x16x32_bf16 v[232:235], v[220:223], v[232:235], 0\n" \
    "ds_read_b64_tr_b16 v[220:221], v196 offset:15872\n" \
    "ds_read_b64_tr_b16 v[222:223], v196 offset:16128\n" \
    "s_nop 1\n" \
    "s_waitcnt lgkmcnt(8)\n" \
    "v_mfma_f32_16x16x32_bf16 v[232:235], v[224:227], v[236:239], v[232:235]\n" \
    "s_nop 1\n" \
    "s_waitcnt lgkmcnt(6)\n" \
    "v_mfma_f32_16x16x32_bf16 v[232:235], v[228:231], v[240:243], v[232:235]\n" \
    "v_cndmask_b32 v208, v204, v208, s[52:53]\n" \
    "v_cndmask_b32 v209, v205, v209, s[52:53]\n" \
    "v_cndmask_b32 v210, v206, v210, s[52:53]\n" \
    "s_waitcnt lgkmcnt(4)\n" \
    "v_mfma_f32_16x16x32_bf16 v[232:235], v[212:215], v[244:247], v[232:235]\n" \
    "v_cndmask_b32 v211, v207, v211, s[52:53]\n" \
    "v_max3_f32 v200, v208, v209, v210\n" \
    "v_max_f32 v200, v200, v211\n" \
    "s_waitcnt lgkmcnt(2)\n" \
    "v_mfma_f32_16x16x32_bf16 v[232:235], v[216:219], v[248:251], v[232:235]\n" \
    "v_max_f32_dpp v201, v200, v200 row_ror:8 row_mask:0xf bank_mask:0xf bound_ctrl:1\n" \
    "v_max_f32_dpp v203, v200, v200 row_ror:8 row_mask:0xf bank_mask:0xf bound_ctrl:1\n" \
    "s_nop 1\n" \
    "v_permlane16_swap_b32 v201, v203\n" \
    "s_waitcnt lgkmcnt(0)\n" \
    "v_mfma_f32_16x16x32_bf16 v[232:235], v[220:223], v[252:255], v[232:235]\n" \
    "v_max_f32 v200, v201, v203\n" \
    "v_max_f32 v201, v201, v203\n" \
    "s_nop 1\n" \
    "v_permlane32_swap_b32 v201, v200\n" \
    "v_max_f32 v201, v201, v200\n" \
    "s_nop 1\n" \
    "v_max3_f32 v236, v232, v233, v201\n" \
    "v_max3_f32 v237, v236, v234, v235\n" \
    "v_max3_f32 v236, v236, v234, v235\n" \
    "s_nop 1\n" \
    "v_permlane16_swap_b32 v236, v237\n" \
    "v_max_f32 v236, v236, v237\n" \
    "v_mul_f32 v236, 0xbdd53b94, v236\n" \
    "v_fmamk_f32 v208, v208, 0x3dd53b94, v236\n" \
    "v_fmamk_f32 v209, v209, 0x3dd53b94, v236\n" \
    "v_fmamk_f32 v210, v210, 0x3dd53b94, v236\n" \
    "v_fmamk_f32 v211, v211, 0x3dd53b94, v236\n" \
    "v_fmamk_f32 v232, v232, 0x3dd53b94, v236\n" \
    "v_fmamk_f32 v233, v233, 0x3dd53b94, v236\n" \
    "v_fmamk_f32 v234, v234, 0x3dd53b94, v236\n" \
    "v_fmamk_f32 v235, v235, 0x3dd53b94, v236\n" \
    "v_exp_f32 v208, v208\n" \
    "v_exp_f32 v209, v209\n" \
    "v_exp_f32 v210, v210\n" \
    "v_exp_f32 v211, v211\n" \
    "v_exp_f32 v232, v232\n" \
    "v_add_f32 v200, v208, v209\n" \
    "v_add_f32 v203, v210, v211\n" \
    "v_exp_f32 v233, v233\n" \
    "v_add_f32 v200, v200, v203\n" \
    "v_exp_f32 v234, v234\n" \
    "v_exp_f32 v235, v235\n" \
    "v_add_f32_dpp v202, v200, v200 row_ror:8 row_mask:0xf bank_mask:0xf bound_ctrl:1\n" \
    "v_add_f32_dpp v203, v200, v200 row_ror:8 row_mask:0xf bank_mask:0xf bound_ctrl:1\n" \
    "v_add_f32 v236, v232, v233\n" \
    "v_add_f32 v237, v234, v235\n" \
    "v_permlane32_swap_b32 v202, v203\n" \
    "v_add_f32 v236, v236, v202\n" \
    "v_add_f32 v237, v237, v203\n" \
    "v_add_f32 v238, v236, v237\n" \
    "v_add_f32 v236, v236, v237\n" \
    "s_nop 1\n" \
    "v_permlane16_swap_b32 v236, v238\n" \
    "v_add_f32 v236, v236, v238\n" \
    "v_rcp_f32 v236, v236\n" \
    "s_nop 0\n" \
    "v_pk_mul_f32 v[208:209], v[208:209], v[236:237] op_sel_hi:[1,0]\n" \
    "v_pk_mul_f32 v[210:211], v[210:211], v[236:237] op_sel_hi:[1,0]\n" \
    "v_pk_mul_f32 v[232:233], v[232:233], v[236:237] op_sel_hi:[1,0]\n" \
    "v_pk_mul_f32 v[234:235], v[234:235], v[236:237] op_sel_hi:[1,0]\n" \
    "v_cvt_pk_bf16_f32 v216, v208, v209\n" \
    "v_cvt_pk_bf16_f32 v217, v210, v211\n" \
    "buffer_store_dwordx2 v[216:217], v197, s[8:11], s38 offen sc1\n" \
    "v_cvt_pk_bf16_f32 v220, v232, v233\n" \
    "v_cvt_pk_bf16_f32 v221, v234, v235\n" \
    "buffer_store_dwordx2 v[220:221], v198, s[8:11], s38 offen sc1\n" \
    "s_cmp_lt_u32 s32, 128\n" \
    "s_cbranch_scc1 .Lqkm_l0_%=\n" \
    "s_branch .Lqkm_end_%=\n" \
    ".Lqkm_w1_%=:\n" \
    "s_mov_b32 s39, 139264\n" \
    "v_bfe_u32 v201, v192, 4, 4\n" \
    "v_lshrrev_b32 v202, 6, v192\n" \
    "v_and_b32 v202, 12, v202\n" \
    "v_sub_u32 v201, v201, v202\n" \
    "v_cmp_gt_i32 s[54:55], 0, v201\n" \
    "v_cmp_gt_i32 s[56:57], 1, v201\n" \
    "v_cmp_gt_i32 s[58:59], 2, v201\n" \
    "v_cmp_gt_i32 s[60:61], 3, v201\n" \
    ".Lqkm_l1_%=:\n" \
    "ds_read_b128 v[240:243], v192 offset:28672\n" \
    "ds_read_b128 v[244:247], v192 offset:31744\n" \
    "ds_read_b128 v[248:251], v192 offset:29696\n" \
    "ds_read_b128 v[252:255], v192 offset:32768\n" \
    "v_mov_b64 v[204:205], 0\n" \
    "v_mov_b64 v[206:207], 0\n" \
    "v_mov_b64 v[208:209], 0\n" \
    "v_mov_b64 v[210:211], 0\n" \
    "v_mov_b64 v[212:213], 0\n" \
    "v_mov_b64 v[214:215], 0\n" \
    "s_barrier\n" \
    "ds_read_b128 v[216:219], v192 offset:0\n" \
    "ds_read_b128 v[220:223], v192 offset:1024\n" \
    "ds_read_b128 v[232:235], v192 offset:8192\n" \
    "ds_read_b128 v[224:227], v192 offset:2048\n" \
    "s_xor_b32 s39, s39, 253952\n" \
    "v_add_u32 v200, s39, v192\n" \
    "v_mov_b32 v203, 0xf149f2ca\n" \
    "s_mov_b32 s33, s34\n" \
    "s_add_u32 s37, s32, 1\n" \
    "s_sub_i32 s36, s37, %[lag]\n" \
    "s_bitcmp1_b32 s36, 0\n" \
    "s_cselect_b32 s34, 128, 2*%[lag]\n" \
    "s_add_i32 s34, s34, s36\n" \
    "s_lshr_b32 s34, s34, 1\n" \
    "s_cmp_gt_u32 s36, 127-2*%[lag]\n" \
    "s_cselect_b32 s34, s37, s34\n" \
    "s_lshr_b32 s36, s33, 6\n" \
    "s_lshl_b32 s36, s36, 2\n" \
    "s_and_b32 s37, s33, 3\n" \
    "s_or_b32 s36, s36, s37\n" \
    "s_waitcnt lgkmcnt(3)\n" \
    "v_smfmac_f32_16x16x64_bf16 v[204:207], v[216:219], v[0:7], v193\n" \
    "ds_read_b128 v[228:231], v192 offset:3072\n" \
    "v_smfmac_f32_16x16x64_bf16 v[208:211], v[216:219], v[64:71], v193\n" \
    "v_smfmac_f32_16x16x64_bf16 v[212:215], v[216:219], v[128:135], v193\n" \
    "s_waitcnt lgkmcnt(3)\n" \
    "v_smfmac_f32_16x16x64_bf16 v[204:207], v[220:223], v[8:15], v193\n" \
    "ds_read_b128 v[216:219], v192 offset:4096\n" \
    "v_smfmac_f32_16x16x64_bf16 v[208:211], v[220:223], v[72:79], v193\n" \
    "ds_read_b128 v[236:239], v192 offset:9216\n" \
    "v_smfmac_f32_16x16x64_bf16 v[212:215], v[220:223], v[136:143], v193\n" \
    "s_waitcnt lgkmcnt(4)\n" \
    "v_smfmac_f32_16x16x64_bf16 v[204:207], v[232:235], v[240:247], v193\n" \
    "s_waitcnt lgkmcnt(3)\n" \
    "v_smfmac_f32_16x16x64_bf16 v[204:207], v[224:227], v[16:23], v193\n" \
    "ds_read_b128 v[220:223], v192 offset:5120\n" \
    "v_smfmac_f32_16x16x64_bf16 v[208:211], v[224:227], v[80:87], v193\n" \
    "ds_read_b128 v[240:243], v192 offset:30720\n" \
    "v_smfmac_f32_16x16x64_bf16 v[212:215], v[224:227], v[144:151], v193\n" \
    "ds_read_b128 v[244:247], v192 offset:33792\n" \
    "v_smfmac_f32_16x16x64_bf16 v[208:211], v[232:235], v[248:255], v193\n" \
    "s_waitcnt lgkmcnt(5)\n" \
    "v_smfmac_f32_16x16x64_bf16 v[204:207], v[228:231], v[24:31], v193\n" \
    "ds_read_b128 v[224:227], v192 offset:6144\n" \
    "v_smfmac_f32_16x16x64_bf16 v[208:211], v[228:231], v[88:95], v193\n" \
    "ds_read_b128 v[248:251], v192 offset:34816\n" \
    "v_smfmac_f32_16x16x64_bf16 v[212:215], v[228:231], v[152:159], v193\n" \
    "ds_read_b128 v[252:255], v192 offset:37888\n" \
    "s_waitcnt lgkmcnt(7)\n" \
    "v_smfmac_f32_16x16x64_bf16 v[204:207], v[216:219], v[32:39], v193\n" \
    "ds_read_b128 v[228:231], v192 offset:7168\n" \
    "v_smfmac_f32_16x16x64_bf16 v[208:211], v[216:219], v[96:103], v193\n" \
    "v_smfmac_f32_16x16x64_bf16 v[212:215], v[216:219], v[160:167], v193\n" \
    "s_waitcnt lgkmcnt(4)\n" \
    "v_smfmac_f32_16x16x64_bf16 v[212:215], v[232:235], v[240:247], v193\n" \
    "s_waitcnt lgkmcnt(1)\n" \
    "v_smfmac_f32_16x16x64_bf16 v[204:207], v[236:239], v[248:255], v193\n" \
    "ds_read_b128 v[240:243], v192 offset:35840\n" \
    "v_smfmac_f32_16x16x64_bf16 v[204:207], v[220:223], v[40:47], v193\n" \
    "ds_read_b128 v[244:247], v192 offset:38912\n" \
    "v_smfmac_f32_16x16x64_bf16 v[208:211], v[220:223], v[104:111], v193\n" \
    "ds_read_b128 v[248:251], v192 offset:36864\n" \
    "v_smfmac_f32_16x16x64_bf16 v[212:215], v[220:223], v[168:175], v193\n" \
    "ds_read_b128 v[252:255], v192 offset:39936\n" \
    "v_smfmac_f32_16x16x64_bf16 v[204:207], v[224:227], v[48:55], v193\n" \
    "v_smfmac_f32_16x16x64_bf16 v[208:211], v[224:227], v[112:119], v193\n" \
    "v_smfmac_f32_16x16x64_bf16 v[212:215], v[224:227], v[176:183], v193\n" \
    "s_waitcnt lgkmcnt(4)\n" \
    "v_smfmac_f32_16x16x64_bf16 v[204:207], v[228:231], v[56:63], v193\n" \
    "s_waitcnt lgkmcnt(2)\n" \
    "v_smfmac_f32_16x16x64_bf16 v[208:211], v[236:239], v[240:247], v193\n" \
    "v_smfmac_f32_16x16x64_bf16 v[208:211], v[228:231], v[120:127], v193\n" \
    "s_waitcnt lgkmcnt(0)\n" \
    "v_smfmac_f32_16x16x64_bf16 v[212:215], v[236:239], v[248:255], v193\n" \
    "v_smfmac_f32_16x16x64_bf16 v[212:215], v[228:231], v[184:191], v193\n" \
    "ds_read_b128 v[216:219], v200 offset:13312\n" \
    "ds_read_b128 v[220:223], v200 offset:14336\n" \
    "ds_read_b128 v[224:227], v200 offset:15360\n" \
    "ds_read_b128 v[228:231], v200 offset:16384\n" \
    "ds_read_b128 v[232:235], v200 offset:0\n" \
    "ds_read_b128 v[236:239], v200 offset:1024\n" \
    "ds_read_b128 v[240:243], v200 offset:2048\n" \
    "ds_read_b128 v[244:247], v200 offset:3072\n" \
    "ds_read_b128 v[248:251], v200 offset:4096\n" \
    "ds_read_b128 v[252:255], v200 offset:5120\n" \
    "s_lshl_b32 s36, s36, s18\n" \
    "s_add_u32 s38, s17, s36\n" \
    "s_add_u32 s32, s32, 1\n" \
    "v_pk_add_f32 v[204:205], v[204:205], v[206:207]\n" \
    "v_pk_add_f32 v[208:209], v[208:209], v[210:211]\n" \
    "v_pk_add_f32 v[212:213], v[212:213], v[214:215]\n" \
    "v_cvt_pk_bf16_f32 v204, v204, v205\n" \
    "v_cvt_pk_bf16_f32 v208, v208, v209\n" \
    "ds_write2st64_b32 v194, v204, v208 offset1:1\n" \
    "v_cvt_pk_bf16_f32 v212, v212, v213\n" \
    "ds_write_b32 v195, v212\n" \
    "ds_read_b128 v[212:215], v200 offset:12288\n" \
    "s_waitcnt lgkmcnt(1)\n" \
    "s_barrier\n" \
    "v_cndmask_b32 v208, 0, v203, s[54:55]\n" \
    "v_cndmask_b32 v209, 0, v203, s[56:57]\n" \
    "v_cndmask_b32 v210, 0, v203, s[58:59]\n" \
    "v_cndmask_b32 v211, 0, v203, s[60:61]\n" \
    "s_waitcnt lgkmcnt(0)\n" \
    "s_nop 0\n" \
    "v_mfma_f32_16x16x32_bf16 v[208:211], v[212:215], v[232:235], v[208:211]\n" \
    "ds_read_b128 v[212:215], v200 offset:17408\n" \
    "v_mfma_f32_16x16x32_bf16 v[208:211], v[216:219], v[236:239], v[208:211]\n" \
    "ds_read_b64_tr_b16 v[216:217], v196 offset:13312\n" \
    "ds_read_b64_tr_b16 v[218:219], v196 offset:13568\n" \
    "v_mfma_f32_16x16x32_bf16 v[208:211], v[220:223], v[240:243], v[208:211]\n" \
    "ds_read_b64_tr_b16 v[220:221], v196 offset:13824\n" \
    "ds_read_b64_tr_b16 v[222:223], v196 offset:14080\n" \
    "v_mfma_f32_16x16x32_bf16 v[208:211], v[224:227], v[244:247], v[208:211]\n" \
    "ds_read_b64_tr_b16 v[224:225], v196 offset:14336\n" \
    "ds_read_b64_tr_b16 v[226:227], v196 offset:14592\n" \
    "v_mfma_f32_16x16x32_bf16 v[208:211], v[228:231], v[248:251], v[208:211]\n" \
    "ds_read_b64_tr_b16 v[228:229], v196 offset:14848\n" \
    "ds_read_b64_tr_b16 v[230:231], v196 offset:15104\n" \
    "s_waitcnt lgkmcnt(8)\n" \
    "v_mfma_f32_16x16x32_bf16 v[208:211], v[212:215], v[252:255], v[208:211]\n" \
    "ds_read_b64_tr_b16 v[212:213], v196 offset:15360\n" \
    "ds_read_b64_tr_b16 v[214:215], v196 offset:15616\n" \
    "s_waitcnt lgkmcnt(8)\n" \
    "v_mfma_f32_16x16x32_bf16 v[204:207], v[216:219], v[232:235], 0\n" \
    "ds_read_b64_tr_b16 v[216:217], v196 offset:15872\n" \
    "ds_read_b64_tr_b16 v[218:219], v196 offset:16128\n" \
    "s_nop 1\n" \
    "s_waitcnt lgkmcnt(8)\n" \
    "v_mfma_f32_16x16x32_bf16 v[204:207], v[220:223], v[236:239], v[204:207]\n" \
    "s_nop 1\n" \
    "s_waitcnt lgkmcnt(6)\n" \
    "v_mfma_f32_16x16x32_bf16 v[204:207], v[224:227], v[240:243], v[204:207]\n" \
    "v_max3_f32 v200, v208, v209, v210\n" \
    "v_max_f32 v201, v200, v211\n" \
    "v_max_f32 v203, v200, v211\n" \
    "s_waitcnt lgkmcnt(4)\n" \
    "v_mfma_f32_16x16x32_bf16 v[204:207], v[228:231], v[244:247], v[204:207]\n" \
    "v_permlane16_swap_b32 v201, v203\n" \
    "v_max_f32 v200, v201, v203\n" \
    "v_max_f32 v201, v201, v203\n" \
    "s_waitcnt lgkmcnt(2)\n" \
    "v_mfma_f32_16x16x32_bf16 v[204:207], v[212:215], v[248:251], v[204:207]\n" \
    "v_permlane32_swap_b32 v201, v200\n" \
    "v_max_f32 v201, v201, v200\n" \
    "s_waitcnt lgkmcnt(0)\n" \
    "v_mfma_f32_16x16x32_bf16 v[204:207], v[216:219], v[252:255], v[204:207]\n" \
    "s_nop 7\n" \
    "v_max3_f32 v236, v204, v205, v201\n" \
    "v_max3_f32 v237, v236, v206, v207\n" \
    "v_max3_f32 v236, v236, v206, v207\n" \
    "s_nop 1\n" \
    "v_permlane16_swap_b32 v236, v237\n" \
    "v_max_f32 v236, v236, v237\n" \
    "v_mul_f32 v236, 0xbdd53b94, v236\n" \
    "v_fmamk_f32 v208, v208, 0x3dd53b94, v236\n" \
    "v_fmamk_f32 v209, v209, 0x3dd53b94, v236\n" \
    "v_fmamk_f32 v210, v210, 0x3dd53b94, v236\n" \
    "v_fmamk_f32 v211, v211, 0x3dd53b94, v236\n" \
    "v_fmamk_f32 v204, v204, 0x3dd53b94, v236\n" \
    "v_fmamk_f32 v205, v205, 0x3dd53b94, v236\n" \
    "v_fmamk_f32 v206, v206, 0x3dd53b94, v236\n" \
    "v_fmamk_f32 v207, v207, 0x3dd53b94, v236\n" \
    "v_exp_f32 v208, v208\n" \
    "v_exp_f32 v209, v209\n" \
    "v_exp_f32 v210, v210\n" \
    "v_exp_f32 v211, v211\n" \
    "v_exp_f32 v204, v204\n" \
    "v_add_f32 v200, v208, v209\n" \
    "v_add_f32 v203, v210, v211\n" \
    "v_exp_f32 v205, v205\n" \
    "v_add_f32 v202, v200, v203\n" \
    "v_add_f32 v203, v200, v203\n" \
    "v_exp_f32 v206, v206\n" \
    "v_exp_f32 v207, v207\n" \
    "v_add_f32 v236, v204, v205\n" \
    "v_add_f32 v237, v206, v207\n" \
    "v_permlane32_swap_b32 v202, v203\n" \
    "v_add_f32 v236, v236, v202\n" \
    "v_add_f32 v237, v237, v203\n" \
    "v_add_f32 v238, v236, v237\n" \
    "v_add_f32 v236, v236, v237\n" \
    "s_nop 1\n" \
    "v_permlane16_swap_b32 v236, v238\n" \
    "v_add_f32 v236, v236, v238\n" \
    "v_rcp_f32 v236, v236\n" \
    "s_nop 0\n" \
    "v_pk_mul_f32 v[208:209], v[208:209], v[236:237] op_sel_hi:[1,0]\n" \
    "v_pk_mul_f32 v[210:211], v[210:211], v[236:237] op_sel_hi:[1,0]\n" \
    "v_pk_mul_f32 v[204:205], v[204:205], v[236:237] op_sel_hi:[1,0]\n" \
    "v_pk_mul_f32 v[206:207], v[206:207], v[236:237] op_sel_hi:[1,0]\n" \
    "v_cvt_pk_bf16_f32 v216, v208, v209\n" \
    "v_cvt_pk_bf16_f32 v217, v210, v211\n" \
    "buffer_store_dwordx2 v[216:217], v197, s[8:11], s38 offen sc1\n" \
    "v_cvt_pk_bf16_f32 v220, v204, v205\n" \
    "v_cvt_pk_bf16_f32 v221, v206, v207\n" \
    "buffer_store_dwordx2 v[220:221], v198, s[8:11], s38 offen sc1\n" \
    "s_cmp_lt_u32 s32, 128\n" \
    "s_cbranch_scc1 .Lqkm_l1_%=\n" \
    "s_branch .Lqkm_end_%=\n" \
    ".Lqkm_w2_%=:\n" \
    "s_mov_b32 s39, 139264\n" \
    "v_bfe_u32 v201, v192, 4, 4\n" \
    "v_lshrrev_b32 v202, 6, v192\n" \
    "v_and_b32 v202, 12, v202\n" \
    "v_sub_u32 v201, v201, v202\n" \
    "v_cmp_gt_i32 s[54:55], 0, v201\n" \
    "v_cmp_gt_i32 s[56:57], 1, v201\n" \
    "v_cmp_gt_i32 s[58:59], 2, v201\n" \
    "v_cmp_gt_i32 s[60:61], 3, v201\n" \
    ".Lqkm_l2_%=:\n" \
    "ds_read_b128 v[240:243], v192 offset:40960\n" \
    "ds_read_b128 v[244:247], v192 offset:44032\n" \
    "ds_read_b128 v[248:251], v192 offset:41984\n" \
    "ds_read_b128 v[252:255], v192 offset:45056\n" \
    "v_mov_b64 v[204:205], 0\n" \
    "v_mov_b64 v[206:207], 0\n" \
    "v_mov_b64 v[208:209], 0\n" \
    "v_mov_b64 v[210:211], 0\n" \
    "v_mov_b64 v[212:213], 0\n" \
    "v_mov_b64 v[214:215], 0\n" \
    "s_barrier\n" \
    "ds_read_b128 v[216:219], v192 offset:0\n" \
    "ds_read_b128 v[220:223], v192 offset:1024\n" \
    "ds_read_b128 v[232:235], v192 offset:8192\n" \
    "ds_read_b128 v[224:227], v192 offset:2048\n" \
    "s_xor_b32 s39, s39, 253952\n" \
    "v_add_u32 v200, s39, v192\n" \
    "v_mov_b32 v203, 0xf149f2ca\n" \
    "s_mov_b32 s33, s34\n" \
    "s_add_u32 s37, s32, 1\n" \
    "s_sub_i32 s36, s37, %[lag]\n" \
    "s_bitcmp1_b32 s36, 0\n" \
    "s_cselect_b32 s34, 128, 2*%[lag]\n" \
    "s_add_i32 s34, s34, s36\n" \
    "s_lshr_b32 s34, s34, 1\n" \
    "s_cmp_gt_u32 s36, 127-2*%[lag]\n" \
    "s_cselect_b32 s34, s37, s34\n" \
    "s_lshr_b32 s36, s33, 6\n" \
    "s_lshl_b32 s36, s36, 2\n" \
    "s_and_b32 s37, s33, 3\n" \
    "s_or_b32 s36, s36, s37\n" \
    "s_waitcnt lgkmcnt(3)\n" \
    "v_smfmac_f32_16x16x64_bf16 v[204:207], v[216:219], v[0:7], v193\n" \
    "ds_read_b128 v[228:231], v192 offset:3072\n" \
    "v_smfmac_f32_16x16x64_bf16 v[208:211], v[216:219], v[64:71], v193\n" \
    "v_smfmac_f32_16x16x64_bf16 v[212:215], v[216:219], v[128:135], v193\n" \
    "s_waitcnt lgkmcnt(3)\n" \
    "v_smfmac_f32_16x16x64_bf16 v[204:207], v[220:223], v[8:15], v193\n" \
    "ds_read_b128 v[216:219], v192 offset:4096\n" \
    "v_smfmac_f32_16x16x64_bf16 v[208:211], v[220:223], v[72:79], v193\n" \
    "ds_read_b128 v[236:239], v192 offset:9216\n" \
    "v_smfmac_f32_16x16x64_bf16 v[212:215], v[220:223], v[136:143], v193\n" \
    "s_waitcnt lgkmcnt(4)\n" \
    "v_smfmac_f32_16x16x64_bf16 v[204:207], v[232:235], v[240:247], v193\n" \
    "s_waitcnt lgkmcnt(3)\n" \
    "v_smfmac_f32_16x16x64_bf16 v[204:207], v[224:227], v[16:23], v193\n" \
    "ds_read_b128 v[220:223], v192 offset:5120\n" \
    "v_smfmac_f32_16x16x64_bf16 v[208:211], v[224:227], v[80:87], v193\n" \
    "ds_read_b128 v[240:243], v192 offset:43008\n" \
    "v_smfmac_f32_16x16x64_bf16 v[212:215], v[224:227], v[144:151], v193\n" \
    "ds_read_b128 v[244:247], v192 offset:46080\n" \
    "v_smfmac_f32_16x16x64_bf16 v[208:211], v[232:235], v[248:255], v193\n" \
    "s_waitcnt lgkmcnt(5)\n" \
    "v_smfmac_f32_16x16x64_bf16 v[204:207], v[228:231], v[24:31], v193\n" \
    "ds_read_b128 v[224:227], v192 offset:6144\n" \
    "v_smfmac_f32_16x16x64_bf16 v[208:211], v[228:231], v[88:95], v193\n" \
    "ds_read_b128 v[248:251], v192 offset:47104\n" \
    "v_smfmac_f32_16x16x64_bf16 v[212:215], v[228:231], v[152:159], v193\n" \
    "ds_read_b128 v[252:255], v192 offset:50176\n" \
    "s_waitcnt lgkmcnt(7)\n" \
    "v_smfmac_f32_16x16x64_bf16 v[204:207], v[216:219], v[32:39], v193\n" \
    "ds_read_b128 v[228:231], v192 offset:7168\n" \
    "v_smfmac_f32_16x16x64_bf16 v[208:211], v[216:219], v[96:103], v193\n" \
    "v_smfmac_f32_16x16x64_bf16 v[212:215], v[216:219], v[160:167], v193\n" \
    "s_waitcnt lgkmcnt(4)\n" \
    "v_smfmac_f32_16x16x64_bf16 v[212:215], v[232:235], v[240:247], v193\n" \
    "s_waitcnt lgkmcnt(1)\n" \
    "v_smfmac_f32_16x16x64_bf16 v[204:207], v[236:239], v[248:255], v193\n" \
    "ds_read_b128 v[240:243], v192 offset:48128\n" \
    "v_smfmac_f32_16x16x64_bf16 v[204:207], v[220:223], v[40:47], v193\n" \
    "ds_read_b128 v[244:247], v192 offset:51200\n" \
    "v_smfmac_f32_16x16x64_bf16 v[208:211], v[220:223], v[104:111], v193\n" \
    "ds_read_b128 v[248:251], v192 offset:49152\n" \
    "v_smfmac_f32_16x16x64_bf16 v[212:215], v[220:223], v[168:175], v193\n" \
    "ds_read_b128 v[252:255], v192 offset:52224\n" \
    "v_smfmac_f32_16x16x64_bf16 v[204:207], v[224:227], v[48:55], v193\n" \
    "v_smfmac_f32_16x16x64_bf16 v[208:211], v[224:227], v[112:119], v193\n" \
    "v_smfmac_f32_16x16x64_bf16 v[212:215], v[224:227], v[176:183], v193\n" \
    "s_waitcnt lgkmcnt(4)\n" \
    "v_smfmac_f32_16x16x64_bf16 v[204:207], v[228:231], v[56:63], v193\n" \
    "s_waitcnt lgkmcnt(2)\n" \
    "v_smfmac_f32_16x16x64_bf16 v[208:211], v[236:239], v[240:247], v193\n" \
    "v_smfmac_f32_16x16x64_bf16 v[208:211], v[228:231], v[120:127], v193\n" \
    "s_waitcnt lgkmcnt(0)\n" \
    "v_smfmac_f32_16x16x64_bf16 v[212:215], v[236:239], v[248:255], v193\n" \
    "v_smfmac_f32_16x16x64_bf16 v[212:215], v[228:231], v[184:191], v193\n" \
    "ds_read_b128 v[216:219], v200 offset:13312\n" \
    "ds_read_b128 v[220:223], v200 offset:14336\n" \
    "ds_read_b128 v[224:227], v200 offset:15360\n" \
    "ds_read_b128 v[228:231], v200 offset:16384\n" \
    "ds_read_b128 v[232:235], v200 offset:6144\n" \
    "ds_read_b128 v[236:239], v200 offset:7168\n" \
    "ds_read_b128 v[240:243], v200 offset:8192\n" \
    "ds_read_b128 v[244:247], v200 offset:9216\n" \
    "ds_read_b128 v[248:251], v200 offset:10240\n" \
    "ds_read_b128 v[252:255], v200 offset:11264\n" \
    "s_lshl_b32 s36, s36, s18\n" \
    "s_add_u32 s38, s17, s36\n" \
    "s_add_u32 s32, s32, 1\n" \
    "v_pk_add_f32 v[204:205], v[204:205], v[206:207]\n" \
    "v_pk_add_f32 v[208:209], v[208:209], v[210:211]\n" \
    "v_pk_add_f32 v[212:213], v[212:213], v[214:215]\n" \
    "v_cvt_pk_bf16_f32 v204, v204, v205\n" \
    "v_cvt_pk_bf16_f32 v208, v208, v209\n" \
    "ds_write2st64_b32 v194, v204, v208 offset1:1\n" \
    "v_cvt_pk_bf16_f32 v212, v212, v213\n" \
    "ds_write_b32 v195, v212\n" \
    "ds_read_b128 v[212:215], v200 offset:12288\n" \
    "s_waitcnt lgkmcnt(1)\n" \
    "s_barrier\n" \
    "s_waitcnt lgkmcnt(0)\n" \
    "v_mfma_f32_16x16x32_bf16 v[204:207], v[212:215], v[232:235], 0\n" \
    "ds_read_b128 v[212:215], v200 offset:17408\n" \
    "v_cndmask_b32 v208, 0, v203, s[54:55]\n" \
    "v_mfma_f32_16x16x32_bf16 v[204:207], v[216:219], v[236:239], v[204:207]\n" \
    "ds_read_b128 v[216:219], v200 offset:18432\n" \
    "v_cndmask_b32 v209, 0, v203, s[56:57]\n" \
    "v_mfma_f32_16x16x32_bf16 v[204:207], v[220:223], v[240:243], v[204:207]\n" \
    "ds_read_b128 v[220:223], v200 offset:19456\n" \
    "v_cndmask_b32 v210, 0, v203, s[58:59]\n" \
    "v_mfma_f32_16x16x32_bf16 v[204:207], v[224:227], v[244:247], v[204:207]\n" \
    "ds_read_b128 v[224:227], v200 offset:20480\n" \
    "v_cndmask_b32 v211, 0, v203, s[60:61]\n" \
    "v_mfma_f32_16x16x32_bf16 v[204:207], v[228:231], v[248:251], v[204:207]\n" \
    "ds_read_b128 v[228:231], v200 offset:21504\n" \
    "s_waitcnt lgkmcnt(4)\n" \
    "v_mfma_f32_16x16x32_bf16 v[204:207], v[212:215], v[252:255], v[204:207]\n" \
    "ds_read_b128 v[212:215], v200 offset:22528\n" \
    "s_waitcnt lgkmcnt(4)\n" \
    "v_mfma_f32_16x16x32_bf16 v[208:211], v[216:219], v[232:235], v[208:211]\n" \
    "ds_read_b128 v[216:219], v200 offset:23552\n" \
    "s_waitcnt lgkmcnt(4)\n" \
    "v_mfma_f32_16x16x32_bf16 v[208:211], v[220:223], v[236:239], v[208:211]\n" \
    "ds_read_b64_tr_b16 v[220:221], v196 offset:13312\n" \
    "ds_read_b64_tr_b16 v[222:223], v196 offset:13568\n" \
    "s_waitcnt lgkmcnt(5)\n" \
    "v_mfma_f32_16x16x32_bf16 v[208:211], v[224:227], v[240:243], v[208:211]\n" \
    "ds_read_b64_tr_b16 v[224:225], v196 offset:13824\n" \
    "ds_read_b64_tr_b16 v[226:227], v196 offset:14080\n" \
    "s_waitcnt lgkmcnt(6)\n" \
    "v_mfma_f32_16x16x32_bf16 v[208:211], v[228:231], v[244:247], v[208:211]\n" \
    "ds_read_b64_tr_b16 v[228:229], v196 offset:14336\n" \
    "ds_read_b64_tr_b16 v[230:231], v196 offset:14592\n" \
    "v_max3_f32 v203, v204, v205, v206\n" \
    "s_waitcnt lgkmcnt(7)\n" \
    "v_mfma_f32_16x16x32_bf16 v[208:211], v[212:215], v[248:251], v[208:211]\n" \
    "ds_read_b64_tr_b16 v[212:213], v196 offset:14848\n" \
    "ds_read_b64_tr_b16 v[214:215], v196 offset:15104\n" \
    "v_max_f32 v203, v203, v207\n" \
    "s_waitcnt lgkmcnt(8)\n" \
    "v_mfma_f32_16x16x32_bf16 v[208:211], v[216:219], v[252:255], v[208:211]\n" \
    "ds_read_b64_tr_b16 v[216:217], v196 offset:15360\n" \
    "ds_read_b64_tr_b16 v[218:219], v196 offset:15616\n" \
    "s_waitcnt lgkmcnt(8)\n" \
    "v_mfma_f32_16x16x32_bf16 v[232:235], v[220:223], v[232:235], 0\n" \
    "ds_read_b64_tr_b16 v[220:221], v196 offset:15872\n" \
    "ds_read_b64_tr_b16 v[222:223], v196 offset:16128\n" \
    "s_nop 1\n" \
    "s_waitcnt lgkmcnt(8)\n" \
    "v_mfma_f32_16x16x32_bf16 v[232:235], v[224:227], v[236:239], v[232:235]\n" \
    "s_nop 1\n" \
    "s_waitcnt lgkmcnt(6)\n" \
    "v_mfma_f32_16x16x32_bf16 v[232:235], v[228:231], v[240:243], v[232:235]\n" \
    "v_max3_f32 v200, v203, v208, v209\n" \
    "v_max3_f32 v201, v200, v210, v211\n" \
    "v_max3_f32 v203, v200, v210, v211\n" \
    "s_waitcnt lgkmcnt(4)\n" \
    "v_mfma_f32_16x16x32_bf16 v[232:235], v[212:215], v[244:247], v[232:235]\n" \
    "v_permlane16_swap_b32 v201, v203\n" \
    "v_max_f32 v200, v201, v203\n" \
    "v_max_f32 v201, v201, v203\n" \
    "s_waitcnt lgkmcnt(2)\n" \
    "v_mfma_f32_16x16x32_bf16 v[232:235], v[216:219], v[248:251], v[232:235]\n" \
    "v_permlane32_swap_b32 v201, v200\n" \
    "v_max_f32 v201, v201, v200\n" \
    "s_waitcnt lgkmcnt(0)\n" \
    "v_mfma_f32_16x16x32_bf16 v[232:235], v[220:223], v[252:255], v[232:235]\n" \
    "s_nop 7\n" \
    "v_max3_f32 v236, v232, v233, v201\n" \
    "v_max3_f32 v237, v236, v234, v235\n" \
    "v_max3_f32 v236, v236, v234, v235\n" \
    "s_nop 1\n" \
    "v_permlane16_swap_b32 v236, v237\n" \
    "v_max_f32 v236, v236, v237\n" \
    "v_mul_f32 v236, 0xbdd53b94, v236\n" \
    "v_fmamk_f32 v208, v208, 0x3dd53b94, v236\n" \
    "v_fmamk_f32 v209, v209, 0x3dd53b94, v236\n" \
    "v_fmamk_f32 v210, v210, 0x3dd53b94, v236\n" \
    "v_fmamk_f32 v211, v211, 0x3dd53b94, v236\n" \
    "v_fmamk_f32 v204, v204, 0x3dd53b94, v236\n" \
    "v_fmamk_f32 v205, v205, 0x3dd53b94, v236\n" \
    "v_fmamk_f32 v206, v206, 0x3dd53b94, v236\n" \
    "v_fmamk_f32 v207, v207, 0x3dd53b94, v236\n" \
    "v_fmamk_f32 v232, v232, 0x3dd53b94, v236\n" \
    "v_fmamk_f32 v233, v233, 0x3dd53b94, v236\n" \
    "v_fmamk_f32 v234, v234, 0x3dd53b94, v236\n" \
    "v_fmamk_f32 v235, v235, 0x3dd53b94, v236\n" \
    "v_exp_f32 v208, v208\n" \
    "v_exp_f32 v209, v209\n" \
    "v_exp_f32 v210, v210\n" \
    "v_exp_f32 v211, v211\n" \
    "v_exp_f32 v204, v204\n" \
    "v_exp_f32 v205, v205\n" \
    "v_exp_f32 v206, v206\n" \
    "v_exp_f32 v207, v207\n" \
    "v_exp_f32 v232, v232\n" \
    "v_add_f32 v200, v208, v209\n" \
    "v_add_f32 v203, v210, v211\n" \
    "v_exp_f32 v233, v233\n" \
    "v_add_f32 v200, v200, v204\n" \
    "v_add_f32 v203, v203, v205\n" \
    "v_add_f32 v200, v200, v206\n" \
    "v_add_f32 v203, v203, v207\n" \
    "v_add_f32 v202, v200, v203\n" \
    "v_add_f32 v203, v200, v203\n" \
    "v_exp_f32 v234, v234\n" \
    "v_exp_f32 v235, v235\n" \
    "v_add_f32 v236, v232, v233\n" \
    "v_add_f32 v237, v234, v235\n" \
    "v_permlane32_swap_b32 v202, v203\n" \
    "v_add_f32 v236, v236, v202\n" \
    "v_add_f32 v237, v237, v203\n" \
    "v_add_f32 v238, v236, v237\n" \
    "v_add_f32 v236, v236, v237\n" \
    "s_nop 1\n" \
    "v_permlane16_swap_b32 v236, v238\n" \
    "v_add_f32 v236, v236, v238\n" \
    "v_rcp_f32 v236, v236\n" \
    "s_nop 0\n" \
    "v_pk_mul_f32 v[204:205], v[204:205], v[236:237] op_sel_hi:[1,0]\n" \
    "v_pk_mul_f32 v[208:209], v[208:209], v[236:237] op_sel_hi:[1,0]\n" \
    "v_pk_mul_f32 v[206:207], v[206:207], v[236:237] op_sel_hi:[1,0]\n" \
    "v_pk_mul_f32 v[210:211], v[210:211], v[236:237] op_sel_hi:[1,0]\n" \
    "v_pk_mul_f32 v[232:233], v[232:233], v[236:237] op_sel_hi:[1,0]\n" \
    "v_pk_mul_f32 v[234:235], v[234:235], v[236:237] op_sel_hi:[1,0]\n" \
    "v_cvt_pk_bf16_f32 v216, v204, v205\n" \
    "v_cvt_pk_bf16_f32 v217, v208, v209\n" \
    "v_cvt_pk_bf16_f32 v218, v206, v207\n" \
    "v_cvt_pk_bf16_f32 v219, v210, v211\n" \
    "buffer_store_dwordx4 v[216:219], v197, s[8:11], s38 offen sc1\n" \
    "v_cvt_pk_bf16_f32 v220, v232, v233\n" \
    "v_cvt_pk_bf16_f32 v221, v234, v235\n" \
    "buffer_store_dwordx2 v[220:221], v198, s[8:11], s38 offen sc1\n" \
    "s_cmp_lt_u32 s32, 128\n" \
    "s_cbranch_scc1 .Lqkm_l2_%=\n" \
    "s_branch .Lqkm_end_%=\n" \
    ".Lqkm_w3_%=:\n" \
    "s_sub_u32 s36, s16, 3\n" \
    "s_lshl_b32 s36, s36, 11\n" \
    "v_add_u32 v200, s36, v192\n" \
    "s_mov_b32 s33, s34\n" \
    "s_lshr_b32 s63, s33, 6\n" \
    "s_lshl_b32 s63, s63, 2\n" \
    "s_and_b32 s37, s33, 3\n" \
    "s_or_b32 s63, s63, s37\n" \
    "s_lshl_b32 s40, s63, 15\n" \
    "s_add_u32 s40, s40, s19\n" \
    "s_cmp_gt_u32 s33, 63\n" \
    "s_cselect_b32 s41, s49, s48\n" \
    "s_add_u32 s41, s41, %[dq]\n" \
    ".Lqkm_l3_%=:\n" \
    "s_branch .Lqkm_t3b_%=\n" \
    ".Lqkm_t3a_%=:\n" \
    "s_sleep 1\n" \
    ".Lqkm_t3b_%=:\n" \
    "s_memrealtime vcc\n" \
    "s_waitcnt lgkmcnt(0)\n" \
    "s_sub_i32 vcc_lo, s41, vcc_lo\n" \
    "s_cmp_gt_i32 vcc_lo, 8\n" \
    "s_cbranch_scc1 .Lqkm_t3a_%=\n" \
    "s_cmp_gt_i32 vcc_lo, 0\n" \
    "s_cbranch_scc1 .Lqkm_t3b_%=\n" \
    "buffer_load_dwordx4 v[216:219], v192, s[8:11], s40 offen sc1\n" \
    "buffer_load_dwordx4 v[220:223], v192, s[8:11], s40 offen offset:1024 sc1\n" \
    "ds_read_b128 v[240:243], v192 offset:53248\n" \
    "ds_read_b128 v[244:247], v192 offset:56320\n" \
    "ds_read_b128 v[248:251], v192 offset:54272\n" \
    "ds_read_b128 v[252:255], v192 offset:57344\n" \
    "v_mov_b64 v[204:205], 0\n" \
    "v_mov_b64 v[206:207], 0\n" \
    "v_mov_b64 v[208:209], 0\n" \
    "v_mov_b64 v[210:211], 0\n" \
    "v_mov_b64 v[212:213], 0\n" \
    "v_mov_b64 v[214:215], 0\n" \
    "s_add_u32 s37, s32, 1\n" \
    "s_sub_i32 s36, s37, %[lag]\n" \
    "s_bitcmp1_b32 s36, 0\n" \
    "s_cselect_b32 s34, 128, 2*%[lag]\n" \
    "s_add_i32 s34, s34, s36\n" \
    "s_lshr_b32 s34, s34, 1\n" \
    "s_cmp_gt_u32 s36, 127-2*%[lag]\n" \
    "s_cselect_b32 s34, s37, s34\n" \
    "s_waitcnt vmcnt(1)\n" \
    "ds_write_b128 v200, v[216:219]\n" \
    "v_max3_u32 v224, v216, v217, v218\n" \
    "s_waitcnt vmcnt(0)\n" \
    "ds_write_b128 v200, v[220:223] offset:1024\n" \
    "v_max3_u32 v224, v224, v219, v220\n" \
    "v_max3_u32 v225, v221, v222, v223\n" \
    "v_max_u32 v224, v224, v225\n" \
    "v_cmp_eq_u32 vcc, -1, v224\n" \
    "s_nop 0\n" \
    "s_cbranch_vccnz .Lqkm_cg3_%=\n" \
    ".Lqkm_cgr3_%=:\n" \
    "s_waitcnt lgkmcnt(0)\n" \
    "s_barrier\n" \
    "ds_read_b128 v[216:219], v192 offset:0\n" \
    "ds_read_b128 v[220:223], v192 offset:1024\n" \
    "ds_read_b128 v[232:235], v192 offset:8192\n" \
    "ds_read_b128 v[224:227], v192 offset:2048\n" \
    "s_waitcnt lgkmcnt(3)\n" \
    "v_smfmac_f32_16x16x64_bf16 v[204:207], v[216:219], v[0:7], v193\n" \
    "ds_read_b128 v[228:231], v192 offset:3072\n" \
    "v_smfmac_f32_16x16x64_bf16 v[208:211], v[216:219], v[64:71], v193\n" \
    "v_smfmac_f32_16x16x64_bf16 v[212:215], v[216:219], v[128:135], v193\n" \
    "s_waitcnt lgkmcnt(3)\n" \
    "v_smfmac_f32_16x16x64_bf16 v[204:207], v[220:223], v[8:15], v193\n" \
    "ds_read_b128 v[216:219], v192 offset:4096\n" \
    "v_smfmac_f32_16x16x64_bf16 v[208:211], v[220:223], v[72:79], v193\n" \
    "ds_read_b128 v[236:239], v192 offset:9216\n" \
    "v_smfmac_f32_16x16x64_bf16 v[212:215], v[220:223], v[136:143], v193\n" \
    "s_waitcnt lgkmcnt(4)\n" \
    "v_smfmac_f32_16x16x64_bf16 v[204:207], v[232:235], v[240:247], v193\n" \
    "s_waitcnt lgkmcnt(3)\n" \
    "v_smfmac_f32_16x16x64_bf16 v[204:207], v[224:227], v[16:23], v193\n" \
    "ds_read_b128 v[220:223], v192 offset:5120\n" \
    "v_smfmac_f32_16x16x64_bf16 v[208:211], v[224:227], v[80:87], v193\n" \
    "ds_read_b128 v[240:243], v192 offset:55296\n" \
    "v_smfmac_f32_16x16x64_bf16 v[212:215], v[224:227], v[144:151], v193\n" \
    "ds_read_b128 v[244:247], v192 offset:58368\n" \
    "v_smfmac_f32_16x16x64_bf16 v[208:211], v[232:235], v[248:255], v193\n" \
    "s_waitcnt lgkmcnt(5)\n" \
    "v_smfmac_f32_16x16x64_bf16 v[204:207], v[228:231], v[24:31], v193\n" \
    "ds_read_b128 v[224:227], v192 offset:6144\n" \
    "v_smfmac_f32_16x16x64_bf16 v[208:211], v[228:231], v[88:95], v193\n" \
    "ds_read_b128 v[248:251], v192 offset:59392\n" \
    "v_smfmac_f32_16x16x64_bf16 v[212:215], v[228:231], v[152:159], v193\n" \
    "ds_read_b128 v[252:255], v192 offset:62464\n" \
    "s_waitcnt lgkmcnt(7)\n" \
    "v_smfmac_f32_16x16x64_bf16 v[204:207], v[216:219], v[32:39], v193\n" \
    "ds_read_b128 v[228:231], v192 offset:7168\n" \
    "v_smfmac_f32_16x16x64_bf16 v[208:211], v[216:219], v[96:103], v193\n" \
    "v_smfmac_f32_16x16x64_bf16 v[212:215], v[216:219], v[160:167], v193\n" \
    "s_waitcnt lgkmcnt(4)\n" \
    "v_smfmac_f32_16x16x64_bf16 v[212:215], v[232:235], v[240:247], v193\n" \
    "s_waitcnt lgkmcnt(1)\n" \
    "v_smfmac_f32_16x16x64_bf16 v[204:207], v[236:239], v[248:255], v193\n" \
    "ds_read_b128 v[240:243], v192 offset:60416\n" \
    "v_smfmac_f32_16x16x64_bf16 v[204:207], v[220:223], v[40:47], v193\n" \
    "ds_read_b128 v[244:247], v192 offset:63488\n" \
    "v_smfmac_f32_16x16x64_bf16 v[208:211], v[220:223], v[104:111], v193\n" \
    "ds_read_b128 v[248:251], v192 offset:61440\n" \
    "v_smfmac_f32_16x16x64_bf16 v[212:215], v[220:223], v[168:175], v193\n" \
    "ds_read_b128 v[252:255], v192 offset:64512\n" \
    "v_smfmac_f32_16x16x64_bf16 v[204:207], v[224:227], v[48:55], v193\n" \
    "v_smfmac_f32_16x16x64_bf16 v[208:211], v[224:227], v[112:119], v193\n" \
    "v_smfmac_f32_16x16x64_bf16 v[212:215], v[224:227], v[176:183], v193\n" \
    "s_waitcnt lgkmcnt(4)\n" \
    "v_smfmac_f32_16x16x64_bf16 v[204:207], v[228:231], v[56:63], v193\n" \
    "s_waitcnt lgkmcnt(2)\n" \
    "v_smfmac_f32_16x16x64_bf16 v[208:211], v[236:239], v[240:247], v193\n" \
    "v_smfmac_f32_16x16x64_bf16 v[208:211], v[228:231], v[120:127], v193\n" \
    "s_waitcnt lgkmcnt(0)\n" \
    "v_smfmac_f32_16x16x64_bf16 v[212:215], v[236:239], v[248:255], v193\n" \
    "v_smfmac_f32_16x16x64_bf16 v[212:215], v[228:231], v[184:191], v193\n" \
    "s_memrealtime s[50:51]\n" \
    "s_waitcnt lgkmcnt(0)\n" \
    "s_sub_u32 s50, s50, %[dq3]\n" \
    "s_cmp_gt_u32 s33, 63\n" \
    "s_cselect_b32 s49, s50, s49\n" \
    "s_cselect_b32 s48, s48, s50\n" \
    "s_add_u32 s32, s32, 1\n" \
    "s_mov_b32 s65, s33\n" \
    "s_mov_b32 s33, s34\n" \
    "s_lshr_b32 s63, s33, 6\n" \
    "s_lshl_b32 s63, s63, 2\n" \
    "s_and_b32 s37, s33, 3\n" \
    "s_or_b32 s63, s63, s37\n" \
    "s_lshl_b32 s40, s63, 15\n" \
    "s_add_u32 s40, s40, s19\n" \
    "s_cmp_gt_u32 s33, 63\n" \
    "s_cselect_b32 s41, s49, s48\n" \
    "s_add_u32 s41, s41, %[dq]\n" \
    "v_pk_add_f32 v[204:205], v[204:205], v[206:207]\n" \
    "v_pk_add_f32 v[208:209], v[208:209], v[210:211]\n" \
    "v_pk_add_f32 v[212:213], v[212:213], v[214:215]\n" \
    "v_cvt_pk_bf16_f32 v204, v204, v205\n" \
    "v_cvt_pk_bf16_f32 v208, v208, v209\n" \
    "ds_write2st64_b32 v194, v204, v208 offset1:1\n" \
    "v_cvt_pk_bf16_f32 v212, v212, v213\n" \
    "ds_write_b32 v195, v212\n" \
    "s_waitcnt lgkmcnt(0)\n" \
    "s_barrier\n" \
    "s_cmp_lt_u32 s32, 128\n" \
    "s_cbranch_scc1 .Lqkm_l3_%=\n" \
    "s_branch .Lqkm_end_%=\n" \
    ".Lqkm_cg3_%=:\n" \
    "s_mov_b32 s46, 0\n" \
    ".Lqkm_cg23_%=:\n" \
    "buffer_load_dwordx4 v[216:219], v192, s[8:11], s40 offen sc1\n" \
    "buffer_load_dwordx4 v[220:223], v192, s[8:11], s40 offen offset:1024 sc1\n" \
    "s_waitcnt vmcnt(1)\n" \
    "ds_write_b128 v200, v[216:219]\n" \
    "v_max3_u32 v224, v216, v217, v218\n" \
    "s_waitcnt vmcnt(0)\n" \
    "ds_write_b128 v200, v[220:223] offset:1024\n" \
    "v_max3_u32 v224, v224, v219, v220\n" \
    "v_max3_u32 v225, v221, v222, v223\n" \
    "v_max_u32 v224, v224, v225\n" \
    "v_cmp_eq_u32 vcc, -1, v224\n" \
    "s_nop 0\n" \
    "s_cbranch_vccz .Lqkm_cgr3_%=\n" \
    "s_add_u32 s46, s46, 1\n" \
    "s_cmp_lt_u32 s46, s47\n" \
    "s_cbranch_scc1 .Lqkm_cg23_%=\n" \
    "s_mov_b32 s47, 0\n" \
    "s_branch .Lqkm_cgr3_%=\n" \
    ".Lqkm_w4_%=:\n" \
    "s_sub_u32 s36, s16, 3\n" \
    "s_lshl_b32 s36, s36, 11\n" \
    "v_add_u32 v200, s36, v192\n" \
    "v_mov_b32 v201, 0\n" \
    "v_min_u32 v202, 80, v192\n" \
    "s_mov_b32 s33, s34\n" \
    "s_lshr_b32 s63, s33, 6\n" \
    "s_lshl_b32 s63, s63, 2\n" \
    "s_and_b32 s37, s33, 3\n" \
    "s_or_b32 s63, s63, s37\n" \
    "s_lshl_b32 s40, s63, 15\n" \
    "s_add_u32 s40, s40, s19\n" \
    "s_cmp_gt_u32 s33, 63\n" \
    "s_cselect_b32 s41, s49, s48\n" \
    "s_add_u32 s41, s41, %[dq]\n" \
    ".Lqkm_l4_%=:\n" \
    "s_cmp_eq_u32 s45, 0\n" \
    "s_cbranch_scc1 .Lqkm_cq_%=\n" \
    ".Lqkm_cqr_%=:\n" \
    "s_branch .Lqkm_t4b_%=\n" \
    ".Lqkm_t4a_%=:\n" \
    "s_sleep 1\n" \
    ".Lqkm_t4b_%=:\n" \
    "s_memrealtime vcc\n" \
    "s_waitcnt lgkmcnt(0)\n" \
    "s_sub_i32 vcc_lo, s41, vcc_lo\n" \
    "s_cmp_gt_i32 vcc_lo, 8\n" \
    "s_cbranch_scc1 .Lqkm_t4a_%=\n" \
    "s_cmp_gt_i32 vcc_lo, 0\n" \
    "s_cbranch_scc1 .Lqkm_t4b_%=\n" \
    "buffer_load_dwordx4 v[216:219], v192, s[8:11], s40 offen sc1\n" \
    "buffer_load_dwordx4 v[220:223], v192, s[8:11], s40 offen offset:1024 sc1\n" \
    "ds_read_b128 v[240:243], v196 offset:0\n" \
    "ds_read_b128 v[244:247], v196 offset:3072\n" \
    "ds_read_b128 v[248:251], v196 offset:1024\n" \
    "ds_read_b128 v[252:255], v196 offset:4096\n" \
    "v_mov_b64 v[204:205], 0\n" \
    "v_mov_b64 v[206:207], 0\n" \
    "v_mov_b64 v[208:209], 0\n" \
    "v_mov_b64 v[210:211], 0\n" \
    "v_mov_b64 v[212:213], 0\n" \
    "v_mov_b64 v[214:215], 0\n" \
    "s_waitcnt vmcnt(2)\n" \
    "v_cmp_ne_u32 vcc, 0, v201\n" \
    "s_mov_b32 s34, s35\n" \
    "s_add_u32 s37, s32, 2\n" \
    "s_sub_i32 s36, s37, %[lag]\n" \
    "s_bitcmp1_b32 s36, 0\n" \
    "s_cselect_b32 s35, 128, 2*%[lag]\n" \
    "s_add_i32 s35, s35, s36\n" \
    "s_lshr_b32 s35, s35, 1\n" \
    "s_cmp_gt_u32 s36, 127-2*%[lag]\n" \
    "s_cselect_b32 s35, s37, s35\n" \
    "s_xor_b32 s36, s63, 2\n" \
    "s_lshl_b32 s36, s36, s18\n" \
    "s_add_u32 s38, s17, s36\n" \
    "s_lshr_b32 s36, s33, 6\n" \
    "s_lshl_b32 s36, s36, 1\n" \
    "s_and_b32 s37, s33, 1\n" \
    "s_or_b32 s36, s36, s37\n" \
    "s_lshl_b32 s36, s36, 14\n" \
    "s_add_u32 s36, s36, s23\n" \
    "s_and_b32 s64, s33, 63\n" \
    "s_add_u32 s64, s64, 1\n" \
    "s_cmp_eq_u32 s16, 7\n" \
    "s_cselect_b32 s38, s36, s38\n" \
    "s_cselect_b32 s64, s64, -1\n" \
    "v_mov_b32 v229, s64\n" \
    "v_mov_b32 v230, -1\n" \
    "v_mov_b32 v231, -1\n" \
    "s_cmp_gt_u32 s34, 63\n" \
    "s_cselect_b32 s36, %[rmask], 63\n" \
    "s_and_b32 s36, s34, s36\n" \
    "s_mul_i32 s42, s36, 1572864\n" \
    "s_add_u32 s68, s42, s24\n" \
    "s_add_u32 s42, s42, s20\n" \
    "s_lshr_b32 s36, s42, 8\n" \
    "s_xor_b32 s36, s36, s26\n" \
    "s_and_b32 s36, s36, 0x2000\n" \
    "s_or_b32 s42, s42, s36\n" \
    "s_lshr_b32 s36, s68, 8\n" \
    "s_xor_b32 s36, s36, s26\n" \
    "s_and_b32 s36, s36, 0x2000\n" \
    "s_or_b32 s68, s68, s36\n" \
    "s_bitcmp0_b32 s32, 0\n" \
    "s_cselect_b32 s43, 24576, 0\n" \
    "s_add_u32 s43, s43, s21\n" \
    "s_lshl_b32 s44, s35, 13\n" \
    "s_add_u32 s44, s44, s22\n" \
    "s_cmp_eq_u64 vcc, -1\n" \
    "s_cselect_b32 s45, 1, 0\n" \
    "s_cmp_lt_u32 s34, 128\n" \
    "s_cselect_b32 s45, s45, 0\n" \
    "s_cmp_eq_u32 s45, 0\n" \
    "s_cselect_b32 s42, 0x80000000, s42\n" \
    "s_cselect_b32 s68, 0x80000000, s68\n" \
    "s_waitcnt vmcnt(1)\n" \
    "ds_write_b128 v200, v[216:219]\n" \
    "v_max3_u32 v224, v216, v217, v218\n" \
    "s_waitcnt vmcnt(0)\n" \
    "ds_write_b128 v200, v[220:223] offset:1024\n" \
    "v_max3_u32 v224, v224, v219, v220\n" \
    "v_max3_u32 v225, v221, v222, v223\n" \
    "v_max_u32 v224, v224, v225\n" \
    "v_cmp_eq_u32 vcc, -1, v224\n" \
    "s_nop 0\n" \
    "s_cbranch_vccnz .Lqkm_cg4_%=\n" \
    ".Lqkm_cgr4_%=:\n" \
    "s_waitcnt lgkmcnt(0)\n" \
    "s_barrier\n" \
    "s_memrealtime s[50:51]\n" \
    "s_cmp_eq_u32 s16, 7\n" \
    "s_waitcnt lgkmcnt(0)\n" \
    "s_cselect_b32 s36, s50, -1\n" \
    "v_mov_b32 v228, s36\n" \
    "buffer_store_dwordx4 v[228:231], v197, s[8:11], s38 offen sc1\n" \
    "ds_read_b128 v[216:219], v192 offset:0\n" \
    "ds_read_b128 v[220:223], v192 offset:1024\n" \
    "ds_read_b128 v[232:235], v192 offset:8192\n" \
    "ds_read_b128 v[224:227], v192 offset:2048\n" \
    "buffer_store_dwordx4 v[228:231], v198, s[8:11], s38 offen sc1\n" \
    "s_mov_b32 m0, s43\n" \
    "s_add_u32 s43, s43, s25\n" \
    "buffer_load_dwordx4 v192, s[8:11], s42 offen offset:0 sc1 lds\n" \
    "buffer_load_dwordx4 v192, s[8:11], s42 offen offset:1024 sc1 lds\n" \
    "buffer_load_dwordx4 v192, s[8:11], s42 offen offset:2048 sc1 lds\n" \
    "buffer_load_dwordx4 v192, s[8:11], s42 offen offset:3072 sc1 lds\n" \
    "s_mov_b32 m0, s43\n" \
    "s_nop 0\n" \
    "buffer_load_dwordx4 v192, s[8:11], s68 offen offset:0 sc1 lds\n" \
    "buffer_load_dwordx4 v192, s[8:11], s68 offen offset:1024 sc1 lds\n" \
    "buffer_load_dword v201, v202, s[8:11], s44 offen sc1\n" \
    "s_cmp_gt_u32 s33, 63\n" \
    "s_cselect_b32 s49, s50, s49\n" \
    "s_cselect_b32 s48, s48, s50\n" \
    "s_add_u32 s32, s32, 1\n" \
    "s_mov_b32 s65, s33\n" \
    "s_mov_b32 s33, s34\n" \
    "s_lshr_b32 s63, s33, 6\n" \
    "s_lshl_b32 s63, s63, 2\n" \
    "s_and_b32 s37, s33, 3\n" \
    "s_or_b32 s63, s63, s37\n" \
    "s_lshl_b32 s40, s63, 15\n" \
    "s_add_u32 s40, s40, s19\n" \
    "s_cmp_gt_u32 s33, 63\n" \
    "s_cselect_b32 s41, s49, s48\n" \
    "s_add_u32 s41, s41, %[dq]\n" \
    "s_waitcnt lgkmcnt(3)\n" \
    "v_smfmac_f32_16x16x64_bf16 v[204:207], v[216:219], v[0:7], v193\n" \
    "ds_read_b128 v[228:231], v192 offset:3072\n" \
    "v_smfmac_f32_16x16x64_bf16 v[208:211], v[216:219], v[64:71], v193\n" \
    "v_smfmac_f32_16x16x64_bf16 v[212:215], v[216:219], v[128:135], v193\n" \
    "s_waitcnt lgkmcnt(3)\n" \
    "v_smfmac_f32_16x16x64_bf16 v[204:207], v[220:223], v[8:15], v193\n" \
    "ds_read_b128 v[216:219], v192 offset:4096\n" \
    "v_smfmac_f32_16x16x64_bf16 v[208:211], v[220:223], v[72:79], v193\n" \
    "ds_read_b128 v[236:239], v192 offset:9216\n" \
    "v_smfmac_f32_16x16x64_bf16 v[212:215], v[220:223], v[136:143], v193\n" \
    "s_waitcnt lgkmcnt(4)\n" \
    "v_smfmac_f32_16x16x64_bf16 v[204:207], v[232:235], v[240:247], v193\n" \
    "s_waitcnt lgkmcnt(3)\n" \
    "v_smfmac_f32_16x16x64_bf16 v[204:207], v[224:227], v[16:23], v193\n" \
    "ds_read_b128 v[220:223], v192 offset:5120\n" \
    "v_smfmac_f32_16x16x64_bf16 v[208:211], v[224:227], v[80:87], v193\n" \
    "ds_read_b128 v[240:243], v196 offset:2048\n" \
    "v_smfmac_f32_16x16x64_bf16 v[212:215], v[224:227], v[144:151], v193\n" \
    "ds_read_b128 v[244:247], v196 offset:5120\n" \
    "v_smfmac_f32_16x16x64_bf16 v[208:211], v[232:235], v[248:255], v193\n" \
    "s_waitcnt lgkmcnt(5)\n" \
    "v_smfmac_f32_16x16x64_bf16 v[204:207], v[228:231], v[24:31], v193\n" \
    "ds_read_b128 v[224:227], v192 offset:6144\n" \
    "v_smfmac_f32_16x16x64_bf16 v[208:211], v[228:231], v[88:95], v193\n" \
    "ds_read_b128 v[248:251], v196 offset:6144\n" \
    "v_smfmac_f32_16x16x64_bf16 v[212:215], v[228:231], v[152:159], v193\n" \
    "ds_read_b128 v[252:255], v196 offset:9216\n" \
    "s_waitcnt lgkmcnt(7)\n" \
    "v_smfmac_f32_16x16x64_bf16 v[204:207], v[216:219], v[32:39], v193\n" \
    "ds_read_b128 v[228:231], v192 offset:7168\n" \
    "v_smfmac_f32_16x16x64_bf16 v[208:211], v[216:219], v[96:103], v193\n" \
    "v_smfmac_f32_16x16x64_bf16 v[212:215], v[216:219], v[160:167], v193\n" \
    "s_waitcnt lgkmcnt(4)\n" \
    "v_smfmac_f32_16x16x64_bf16 v[212:215], v[232:235], v[240:247], v193\n" \
    "s_waitcnt lgkmcnt(1)\n" \
    "v_smfmac_f32_16x16x64_bf16 v[204:207], v[236:239], v[248:255], v193\n" \
    "ds_read_b128 v[240:243], v196 offset:7168\n" \
    "v_smfmac_f32_16x16x64_bf16 v[204:207], v[220:223], v[40:47], v193\n" \
    "ds_read_b128 v[244:247], v196 offset:10240\n" \
    "v_smfmac_f32_16x16x64_bf16 v[208:211], v[220:223], v[104:111], v193\n" \
    "ds_read_b128 v[248:251], v196 offset:8192\n" \
    "v_smfmac_f32_16x16x64_bf16 v[212:215], v[220:223], v[168:175], v193\n" \
    "ds_read_b128 v[252:255], v196 offset:11264\n" \
    "v_smfmac_f32_16x16x64_bf16 v[204:207], v[224:227], v[48:55], v193\n" \
    "v_smfmac_f32_16x16x64_bf16 v[208:211], v[224:227], v[112:119], v193\n" \
    "v_smfmac_f32_16x16x64_bf16 v[212:215], v[224:227], v[176:183], v193\n" \
    "s_waitcnt lgkmcnt(4)\n" \
    "v_smfmac_f32_16x16x64_bf16 v[204:207], v[228:231], v[56:63], v193\n" \
    "s_waitcnt lgkmcnt(2)\n" \
    "v_smfmac_f32_16x16x64_bf16 v[208:211], v[236:239], v[240:247], v193\n" \
    "v_smfmac_f32_16x16x64_bf16 v[208:211], v[228:231], v[120:127], v193\n" \
    "s_waitcnt lgkmcnt(0)\n" \
    "v_smfmac_f32_16x16x64_bf16 v[212:215], v[236:239], v[248:255], v193\n" \
    "v_smfmac_f32_16x16x64_bf16 v[212:215], v[228:231], v[184:191], v193\n" \
    "s_nop 1\n" \
    "v_pk_add_f32 v[204:205], v[204:205], v[206:207]\n" \
    "s_nop 1\n" \
    "v_pk_add_f32 v[208:209], v[208:209], v[210:211]\n" \
    "s_nop 1\n" \
    "v_pk_add_f32 v[212:213], v[212:213], v[214:215]\n" \
    "v_cvt_pk_bf16_f32 v204, v204, v205\n" \
    "v_cvt_pk_bf16_f32 v208, v208, v209\n" \
    "ds_write2st64_b32 v194, v204, v208 offset1:1\n" \
    "v_cvt_pk_bf16_f32 v212, v212, v213\n" \
    "ds_write_b32 v195, v212\n" \
    "s_waitcnt lgkmcnt(0)\n" \
    "s_barrier\n" \
    "s_cmp_lt_u32 s32, 128\n" \
    "s_cbranch_scc1 .Lqkm_l4_%=\n" \
    "s_branch .Lqkm_end_%=\n" \
    ".Lqkm_cg4_%=:\n" \
    "s_mov_b32 s46, 0\n" \
    ".Lqkm_cg24_%=:\n" \
    "buffer_load_dwordx4 v[216:219], v192, s[8:11], s40 offen sc1\n" \
    "buffer_load_dwordx4 v[220:223], v192, s[8:11], s40 offen offset:1024 sc1\n" \
    "s_waitcnt vmcnt(1)\n" \
    "ds_write_b128 v200, v[216:219]\n" \
    "v_max3_u32 v224, v216, v217, v218\n" \
    "s_waitcnt vmcnt(0)\n" \
    "ds_write_b128 v200, v[220:223] offset:1024\n" \
    "v_max3_u32 v224, v224, v219, v220\n" \
    "v_max3_u32 v225, v221, v222, v223\n" \
    "v_max_u32 v224, v224, v225\n" \
    "v_cmp_eq_u32 vcc, -1, v224\n" \
    "s_nop 0\n" \
    "s_cbranch_vccz .Lqkm_cgr4_%=\n" \
    "s_add_u32 s46, s46, 1\n" \
    "s_cmp_lt_u32 s46, s47\n" \
    "s_cbranch_scc1 .Lqkm_cg24_%=\n" \
    "s_mov_b32 s47, 0\n" \
    "s_branch .Lqkm_cgr4_%=\n" \
    ".Lqkm_cq_%=:\n" \
    "s_lshl_b32 s37, s33, 13\n" \
    "s_add_u32 s37, s37, s22\n" \
    "s_mov_b32 s46, 0\n" \
    ".Lqkm_cq2_%=:\n" \
    "buffer_load_dword v216, v202, s[8:11], s37 offen sc1\n" \
    "s_waitcnt vmcnt(0)\n" \
    "v_cmp_ne_u32 s[66:67], 0, v216\n" \
    "s_nop 0\n" \
    "s_cmp_eq_u64 s[66:67], -1\n" \
    "s_cbranch_scc1 .Lqkm_cq3_%=\n" \
    "s_sleep 2\n" \
    "s_add_u32 s46, s46, 1\n" \
    "s_cmp_lt_u32 s46, s47\n" \
    "s_cbranch_scc1 .Lqkm_cq2_%=\n" \
    "s_mov_b32 s47, 0\n" \
    "s_branch .Lqkm_cq3_%=\n" \
    ".Lqkm_cq3_%=:\n" \
    "s_cmp_gt_u32 s33, 63\n" \
    "s_cselect_b32 s37, %[rmask], 63\n" \
    "s_and_b32 s37, s33, s37\n" \
    "s_mul_i32 s36, s37, 1572864\n" \
    "s_add_u32 s69, s36, s24\n" \
    "s_add_u32 s36, s36, s20\n" \
    "s_lshr_b32 s37, s36, 8\n" \
    "s_xor_b32 s37, s37, s26\n" \
    "s_and_b32 s37, s37, 0x2000\n" \
    "s_or_b32 s36, s36, s37\n" \
    "s_lshr_b32 s37, s69, 8\n" \
    "s_xor_b32 s37, s37, s26\n" \
    "s_and_b32 s37, s37, 0x2000\n" \
    "s_or_b32 s69, s69, s37\n" \
    "s_bitcmp1_b32 s32, 0\n" \
    "s_cselect_b32 s37, 24576, 0\n" \
    "s_add_u32 s37, s37, s21\n" \
    "s_mov_b32 m0, s37\n" \
    "s_add_u32 s37, s37, s25\n" \
    "buffer_load_dwordx4 v192, s[8:11], s36 offen offset:0 sc1 lds\n" \
    "buffer_load_dwordx4 v192, s[8:11], s36 offen offset:1024 sc1 lds\n" \
    "buffer_load_dwordx4 v192, s[8:11], s36 offen offset:2048 sc1 lds\n" \
    "buffer_load_dwordx4 v192, s[8:11], s36 offen offset:3072 sc1 lds\n" \
    "s_mov_b32 m0, s37\n" \
    "s_nop 0\n" \
    "buffer_load_dwordx4 v192, s[8:11], s69 offen offset:0 sc1 lds\n" \
    "buffer_load_dwordx4 v192, s[8:11], s69 offen offset:1024 sc1 lds\n" \
    "s_branch .Lqkm_cqr_%=\n" \
    ".Lqkm_end_%=:\n" \
    "s_waitcnt vmcnt(0)\n" \
    "s_endpgm\n" \
    ""
#define UAV_ASM \
    "s_mov_b32 s32, 0\n" \
    "s_mov_b32 s42, 0x40000\n" \
    "s_memrealtime s[44:45]\n" \
    "s_waitcnt lgkmcnt(0)\n" \
    "s_sub_u32 s44, s44, 100000\n" \
    "s_mov_b32 s45, s44\n" \
    ".Luav_l_%=:\n" \
    "ds_read_b128 v[240:243], v196 offset:0\n" \
    "ds_read_b128 v[244:247], v196 offset:3072\n" \
    "ds_read_b128 v[248:251], v196 offset:1024\n" \
    "ds_read_b128 v[252:255], v196 offset:4096\n" \
    "ds_read_b128 v[204:207], v192 offset:13312\n" \
    "ds_read_b128 v[208:211], v192 offset:13312\n" \
    "ds_read_b128 v[212:215], v192 offset:13312\n" \
    "s_sub_i32 s34, s32, %[lag]\n" \
    "s_bitcmp1_b32 s34, 0\n" \
    "s_cselect_b32 s33, 128, 2*%[lag]\n" \
    "s_add_i32 s33, s33, s34\n" \
    "s_lshr_b32 s33, s33, 1\n" \
    "s_cmp_gt_u32 s34, 127-2*%[lag]\n" \
    "s_cselect_b32 s33, s32, s33\n" \
    "s_add_u32 s32, s32, 1\n" \
    "s_lshr_b32 s34, s33, 6\n" \
    "s_lshl_b32 s36, s34, 2\n" \
    "s_and_b32 s35, s33, 3\n" \
    "s_or_b32 s36, s36, s35\n" \
    "s_lshl_b32 s36, s36, 15\n" \
    "s_add_u32 s37, s36, s18\n" \
    "s_add_u32 s36, s36, s17\n" \
    "s_lshl_b32 s39, s34, 1\n" \
    "s_and_b32 s35, s33, 1\n" \
    "s_xor_b32 s35, s35, 1\n" \
    "s_or_b32 s39, s39, s35\n" \
    "s_lshl_b32 s39, s39, 14\n" \
    "s_add_u32 s39, s39, s19\n" \
    "s_mov_b32 s41, 0\n" \
    "s_and_b32 s40, s33, 63\n" \
    "s_cbranch_scc0 .Luav_go_%=\n" \
    ".Luav_an_%=:\n" \
    "buffer_load_dwordx2 v[216:217], v199, s[8:11], s39 offen sc1\n" \
    "s_memrealtime s[50:51]\n" \
    "s_waitcnt vmcnt(0) lgkmcnt(0)\n" \
    "s_waitcnt vmcnt(0)\n" \
    "v_cmp_ne_u32 vcc, s40, v217\n" \
    "s_waitcnt vmcnt(0) lgkmcnt(0)\n" \
    "v_subrev_u32 v216, s50, v216\n" \
    "s_cbranch_vccnz .Luav_can_%=\n" \
    "s_waitcnt vmcnt(0)\n" \
    "v_max_i32_dpp v216, v216, v216 row_ror:8 row_mask:0xf bank_mask:0xf bound_ctrl:1\n" \
    "s_waitcnt vmcnt(0)\n" \
    "s_nop 0\n" \
    "v_max_i32_dpp v216, v216, v216 row_ror:4 row_mask:0xf bank_mask:0xf bound_ctrl:1\n" \
    "s_waitcnt vmcnt(0)\n" \
    "s_nop 0\n" \
    "v_max_i32_dpp v216, v216, v216 row_ror:2 row_mask:0xf bank_mask:0xf bound_ctrl:1\n" \
    "s_waitcnt vmcnt(0)\n" \
    "s_nop 0\n" \
    "v_max_i32_dpp v216, v216, v216 row_ror:1 row_mask:0xf bank_mask:0xf bound_ctrl:1\n" \
    "s_waitcnt vmcnt(0)\n" \
    "v_mov_b32 v217, v216\n" \
    "s_waitcnt vmcnt(0)\n" \
    "s_nop 0\n" \
    "v_permlane16_swap_b32 v216, v217\n" \
    "s_waitcnt vmcnt(0)\n" \
    "v_max_i32 v216, v216, v217\n" \
    "s_waitcnt vmcnt(0)\n" \
    "v_readfirstlane_b32 s38, v216\n" \
    "s_waitcnt lgkmcnt(0)\n" \
    "s_add_u32 s38, s38, s50\n" \
    "s_add_u32 s38, s38, %[dv]\n" \
    "s_branch .Luav_tb_%=\n" \
    ".Luav_ta_%=:\n" \
    "s_sleep 1\n" \
    ".Luav_tb_%=:\n" \
    "s_memrealtime vcc\n" \
    "s_waitcnt lgkmcnt(0)\n" \
    "s_waitcnt lgkmcnt(0)\n" \
    "s_sub_i32 vcc_lo, s38, vcc_lo\n" \
    "s_waitcnt lgkmcnt(0)\n" \
    "s_cmp_gt_i32 vcc_lo, 8\n" \
    "s_cbranch_scc1 .Luav_ta_%=\n" \
    "s_waitcnt lgkmcnt(0)\n" \
    "s_cmp_gt_i32 vcc_lo, 0\n" \
    "s_cbranch_scc1 .Luav_tb_%=\n" \
    ".Luav_go_%=:\n" \
    "s_waitcnt vmcnt(0)\n" \
    "buffer_load_dwordx4 v[216:219], v192, s[8:11], s36 offen sc1\n" \
    "buffer_load_dwordx4 v[220:223], v192, s[8:11], s36 offen offset:1024 sc1\n" \
    "buffer_load_dwordx4 v[224:227], v192, s[8:11], s37 offen sc1\n" \
    "s_waitcnt vmcnt(2)\n" \
    "ds_write_b128 v197, v[216:219]\n" \
    "v_max3_u32 v228, v216, v217, v218\n" \
    "s_waitcnt vmcnt(1)\n" \
    "ds_write_b128 v197, v[220:223] offset:1024\n" \
    "v_max3_u32 v228, v228, v219, v220\n" \
    "v_max3_u32 v229, v221, v222, v223\n" \
    "s_waitcnt vmcnt(0)\n" \
    "ds_write_b128 v198, v[224:227]\n" \
    "v_max3_u32 v228, v228, v224, v225\n" \
    "v_max3_u32 v229, v229, v226, v227\n" \
    "v_max_u32 v228, v228, v229\n" \
    "s_waitcnt lgkmcnt(0)\n" \
    "v_cmp_eq_u32 vcc, -1, v228\n" \
    "s_nop 0\n" \
    "s_cbranch_vccnz .Luav_cg_%=\n" \
    ".Luav_cgr_%=:\n" \
    "s_waitcnt lgkmcnt(0)\n" \
    "s_barrier\n" \
    "ds_read_b128 v[216:219], v192 offset:0\n" \
    "ds_read_b128 v[220:223], v192 offset:1024\n" \
    "ds_read_b128 v[232:235], v192 offset:8192\n" \
    "ds_read_b128 v[224:227], v192 offset:2048\n" \
    "s_waitcnt lgkmcnt(3)\n" \
    "v_smfmac_f32_16x16x64_bf16 v[204:207], v[216:219], v[0:7], v193\n" \
    "ds_read_b128 v[228:231], v192 offset:3072\n" \
    "v_smfmac_f32_16x16x64_bf16 v[208:211], v[216:219], v[64:71], v193\n" \
    "v_smfmac_f32_16x16x64_bf16 v[212:215], v[216:219], v[128:135], v193\n" \
    "s_waitcnt lgkmcnt(3)\n" \
    "v_smfmac_f32_16x16x64_bf16 v[204:207], v[220:223], v[8:15], v193\n" \
    "ds_read_b128 v[216:219], v192 offset:4096\n" \
    "v_smfmac_f32_16x16x64_bf16 v[208:211], v[220:223], v[72:79], v193\n" \
    "ds_read_b128 v[236:239], v192 offset:9216\n" \
    "v_smfmac_f32_16x16x64_bf16 v[212:215], v[220:223], v[136:143], v193\n" \
    "s_waitcnt lgkmcnt(4)\n" \
    "v_smfmac_f32_16x16x64_bf16 v[204:207], v[232:235], v[240:247], v193\n" \
    "s_waitcnt lgkmcnt(3)\n" \
    "v_smfmac_f32_16x16x64_bf16 v[204:207], v[224:227], v[16:23], v193\n" \
    "ds_read_b128 v[220:223], v192 offset:5120\n" \
    "v_smfmac_f32_16x16x64_bf16 v[208:211], v[224:227], v[80:87], v193\n" \
    "ds_read_b128 v[240:243], v196 offset:2048\n" \
    "v_smfmac_f32_16x16x64_bf16 v[212:215], v[224:227], v[144:151], v193\n" \
    "ds_read_b128 v[244:247], v196 offset:5120\n" \
    "v_smfmac_f32_16x16x64_bf16 v[208:211], v[232:235], v[248:255], v193\n" \
    "s_waitcnt lgkmcnt(5)\n" \
    "v_smfmac_f32_16x16x64_bf16 v[204:207], v[228:231], v[24:31], v193\n" \
    "ds_read_b128 v[224:227], v192 offset:6144\n" \
    "v_smfmac_f32_16x16x64_bf16 v[208:211], v[228:231], v[88:95], v193\n" \
    "ds_read_b128 v[248:251], v196 offset:6144\n" \
    "v_smfmac_f32_16x16x64_bf16 v[212:215], v[228:231], v[152:159], v193\n" \
    "ds_read_b128 v[252:255], v196 offset:9216\n" \
    "s_waitcnt lgkmcnt(7)\n" \
    "v_smfmac_f32_16x16x64_bf16 v[204:207], v[216:219], v[32:39], v193\n" \
    "ds_read_b128 v[228:231], v192 offset:7168\n" \
    "v_smfmac_f32_16x16x64_bf16 v[208:211], v[216:219], v[96:103], v193\n" \
    "v_smfmac_f32_16x16x64_bf16 v[212:215], v[216:219], v[160:167], v193\n" \
    "s_waitcnt lgkmcnt(4)\n" \
    "v_smfmac_f32_16x16x64_bf16 v[212:215], v[232:235], v[240:247], v193\n" \
    "s_waitcnt lgkmcnt(1)\n" \
    "v_smfmac_f32_16x16x64_bf16 v[204:207], v[236:239], v[248:255], v193\n" \
    "ds_read_b128 v[240:243], v196 offset:7168\n" \
    "v_smfmac_f32_16x16x64_bf16 v[204:207], v[220:223], v[40:47], v193\n" \
    "ds_read_b128 v[244:247], v196 offset:10240\n" \
    "v_smfmac_f32_16x16x64_bf16 v[208:211], v[220:223], v[104:111], v193\n" \
    "ds_read_b128 v[248:251], v196 offset:8192\n" \
    "v_smfmac_f32_16x16x64_bf16 v[212:215], v[220:223], v[168:175], v193\n" \
    "ds_read_b128 v[252:255], v196 offset:11264\n" \
    "v_smfmac_f32_16x16x64_bf16 v[204:207], v[224:227], v[48:55], v193\n" \
    "v_smfmac_f32_16x16x64_bf16 v[208:211], v[224:227], v[112:119], v193\n" \
    "v_smfmac_f32_16x16x64_bf16 v[212:215], v[224:227], v[176:183], v193\n" \
    "s_waitcnt lgkmcnt(4)\n" \
    "v_smfmac_f32_16x16x64_bf16 v[204:207], v[228:231], v[56:63], v193\n" \
    "s_waitcnt lgkmcnt(2)\n" \
    "v_smfmac_f32_16x16x64_bf16 v[208:211], v[236:239], v[240:247], v193\n" \
    "v_smfmac_f32_16x16x64_bf16 v[208:211], v[228:231], v[120:127], v193\n" \
    "s_waitcnt lgkmcnt(0)\n" \
    "v_smfmac_f32_16x16x64_bf16 v[212:215], v[236:239], v[248:255], v193\n" \
    "v_smfmac_f32_16x16x64_bf16 v[212:215], v[228:231], v[184:191], v193\n" \
    "s_nop 1\n" \
    "v_pk_add_f32 v[204:205], v[204:205], v[206:207]\n" \
    "s_nop 1\n" \
    "v_pk_add_f32 v[208:209], v[208:209], v[210:211]\n" \
    "s_nop 1\n" \
    "v_pk_add_f32 v[212:213], v[212:213], v[214:215]\n" \
    "v_cvt_pk_bf16_f32 v204, v204, v205\n" \
    "v_cvt_pk_bf16_f32 v208, v208, v209\n" \
    "ds_write2st64_b32 v194, v204, v208 offset1:1\n" \
    "v_cvt_pk_bf16_f32 v212, v212, v213\n" \
    "ds_write_b32 v195, v212\n" \
    "s_waitcnt lgkmcnt(0)\n" \
    "s_barrier\n" \
    "s_cmp_lt_u32 s32, 128\n" \
    "s_cbranch_scc1 .Luav_l_%=\n" \
    "s_endpgm\n" \
    ".Luav_cg_%=:\n" \
    "buffer_load_dwordx4 v[216:219], v192, s[8:11], s36 offen sc1\n" \
    "buffer_load_dwordx4 v[220:223], v192, s[8:11], s36 offen offset:1024 sc1\n" \
    "buffer_load_dwordx4 v[224:227], v192, s[8:11], s37 offen sc1\n" \
    "s_waitcnt vmcnt(2)\n" \
    "ds_write_b128 v197, v[216:219]\n" \
    "v_max3_u32 v228, v216, v217, v218\n" \
    "s_waitcnt vmcnt(1)\n" \
    "ds_write_b128 v197, v[220:223] offset:1024\n" \
    "v_max3_u32 v228, v228, v219, v220\n" \
    "v_max3_u32 v229, v221, v222, v223\n" \
    "s_waitcnt vmcnt(0)\n" \
    "ds_write_b128 v198, v[224:227]\n" \
    "v_max3_u32 v228, v228, v224, v225\n" \
    "v_max3_u32 v229, v229, v226, v227\n" \
    "v_max_u32 v228, v228, v229\n" \
    "v_cmp_eq_u32 vcc, -1, v228\n" \
    "s_nop 0\n" \
    "s_cbranch_vccz .Luav_cgr_%=\n" \
    "s_add_u32 s41, s41, 1\n" \
    "s_cmp_lt_u32 s41, s42\n" \
    "s_cbranch_scc1 .Luav_cg_%=\n" \
    "s_mov_b32 s42, 0\n" \
    "s_branch .Luav_cgr_%=\n" \
    ".Luav_can_%=:\n" \
    "s_sleep 2\n" \
    "s_add_u32 s41, s41, 1\n" \
    "s_cmp_lt_u32 s41, s42\n" \
    "s_cbranch_scc1 .Luav_an_%=\n" \
    "s_mov_b32 s42, 0\n" \
    "s_branch .Luav_go_%=\n" \
    ""
#define UAO_ASM \
    "s_mov_b32 s32, 0\n" \
    "s_mov_b32 s39, 0\n" \
    "s_mov_b32 s40, 0\n" \
    "s_setprio 2\n" \
    ".Luao_l_%=:\n" \
    "s_sub_i32 s34, s32, %[lag]\n" \
    "s_bitcmp1_b32 s34, 0\n" \
    "s_cselect_b32 s33, 128, 2*%[lag]\n" \
    "s_add_i32 s33, s33, s34\n" \
    "s_lshr_b32 s33, s33, 1\n" \
    "s_cmp_gt_u32 s34, 127-2*%[lag]\n" \
    "s_cselect_b32 s33, s32, s33\n" \
    "ds_read_b128 v[236:239], v194 offset:0\n" \
    "ds_read_b128 v[240:243], v194 offset:1024\n" \
    "ds_read_b128 v[244:247], v194 offset:2048\n" \
    "ds_read_b128 v[248:251], v194 offset:3072\n" \
    "ds_read_b128 v[228:231], v192 offset:13312\n" \
    "ds_read_b128 v[212:215], v192 offset:13312\n" \
    "ds_read_b128 v[216:219], v192 offset:13312\n" \
    "ds_read_b128 v[232:235], v192 offset:13312\n" \
    "ds_read_b128 v[220:223], v192 offset:13312\n" \
    "ds_read_b128 v[224:227], v192 offset:13312\n" \
    "ds_read_b128 v[252:255], v192 offset:13312\n" \
    "s_add_u32 s32, s32, 1\n" \
    "s_cmp_gt_u32 s33, 63\n" \
    "s_cselect_b32 s34, s40, s39\n" \
    "s_cselect_b32 s35, s18, s17\n" \
    "s_add_u32 s36, s34, s35\n" \
    "s_sub_u32 s37, s34, 655360\n" \
    "s_cmp_eq_u32 s34, 0\n" \
    "s_cselect_b32 s37, 1310720, s37\n" \
    "s_add_u32 s37, s37, s35\n" \
    "s_add_u32 s34, s34, 655360\n" \
    "s_cmp_eq_u32 s34, 1966080\n" \
    "s_cselect_b32 s34, 0, s34\n" \
    "s_cmp_gt_u32 s33, 63\n" \
    "s_cselect_b32 s40, s34, s40\n" \
    "s_cselect_b32 s39, s39, s34\n" \
    "s_lshr_b32 s34, s33, 6\n" \
    "s_lshl_b32 s34, s34, 1\n" \
    "s_and_b32 s35, s33, 1\n" \
    "s_or_b32 s34, s34, s35\n" \
    "s_lshl_b32 s38, s34, s20\n" \
    "s_add_u32 s38, s38, s19\n" \
    "s_and_b32 s35, s33, 63\n" \
    "s_add_u32 s35, s35, 1\n" \
    "v_mov_b32 v201, s35\n" \
    "v_mov_b32 v204, -1\n" \
    "v_mov_b32 v205, -1\n" \
    "v_mov_b32 v206, -1\n" \
    "v_mov_b32 v207, -1\n" \
    "s_waitcnt vmcnt(5) lgkmcnt(0)\n" \
    "s_barrier\n" \
    "s_memrealtime s[50:51]\n" \
    "s_waitcnt lgkmcnt(0)\n" \
    "v_mov_b32 v200, s50\n" \
    "buffer_store_dwordx4 v[200:203], v198, s[8:11], s38 offen sc1\n" \
    "buffer_store_dwordx4 v[204:207], v197, s[8:11], s37 offen sc1\n" \
    "s_add_u32 s37, s37, 65536\n" \
    "buffer_store_dwordx4 v[204:207], v197, s[8:11], s37 offen sc1\n" \
    "s_add_u32 s37, s37, 32768\n" \
    "buffer_store_dwordx4 v[204:207], v197, s[8:11], s37 offen sc1\n" \
    "s_add_u32 s41, s36, 32768\n" \
    "s_add_u32 s42, s36, 65536\n" \
    "s_add_u32 s43, s36, 98304\n" \
    "s_add_u32 s44, s36, 131072\n" \
    "s_barrier\n" \
    "ds_read_b64_tr_b16 v[200:201], v195 offset:0\n" \
    "ds_read_b64_tr_b16 v[202:203], v195 offset:64\n" \
    "ds_read_b64_tr_b16 v[204:205], v195 offset:1024\n" \
    "ds_read_b64_tr_b16 v[206:207], v195 offset:1088\n" \
    "ds_read_b64_tr_b16 v[208:209], v195 offset:2048\n" \
    "ds_read_b64_tr_b16 v[210:211], v195 offset:2112\n" \
    "s_waitcnt lgkmcnt(4)\n" \
    "v_smfmac_f32_16x16x64_bf16 v[212:215], v[200:203], v[0:7], v193\n" \
    "s_waitcnt lgkmcnt(2)\n" \
    "v_smfmac_f32_16x16x64_bf16 v[212:215], v[204:207], v[8:15], v193\n" \
    "s_waitcnt lgkmcnt(0)\n" \
    "v_smfmac_f32_16x16x64_bf16 v[212:215], v[208:211], v[16:23], v193\n" \
    "v_smfmac_f32_16x16x64_bf16 v[216:219], v[200:203], v[24:31], v193\n" \
    "v_smfmac_f32_16x16x64_bf16 v[216:219], v[204:207], v[32:39], v193\n" \
    "v_smfmac_f32_16x16x64_bf16 v[216:219], v[208:211], v[40:47], v193\n" \
    "v_smfmac_f32_16x16x64_bf16 v[228:231], v[200:203], v[236:243], v193\n" \
    "v_smfmac_f32_16x16x64_bf16 v[228:231], v[204:207], v[244:251], v193\n" \
    "ds_read_b128 v[236:239], v194 offset:4096\n" \
    "ds_read_b128 v[240:243], v194 offset:5120\n" \
    "s_nop 0\n" \
    "v_add_f32 v212, v212, v214\n" \
    "v_add_f32 v213, v213, v215\n" \
    "v_smfmac_f32_16x16x64_bf16 v[220:223], v[200:203], v[48:55], v193\n" \
    "ds_read_b128 v[244:247], v194 offset:6144\n" \
    "ds_read_b128 v[248:251], v194 offset:7168\n" \
    "v_smfmac_f32_16x16x64_bf16 v[220:223], v[204:207], v[56:63], v193\n" \
    "v_cvt_pk_bf16_f32 v212, v212, v213\n" \
    "v_add_f32 v216, v216, v218\n" \
    "v_add_f32 v217, v217, v219\n" \
    "v_smfmac_f32_16x16x64_bf16 v[220:223], v[208:211], v[64:71], v193\n" \
    "v_cvt_pk_bf16_f32 v213, v216, v217\n" \
    "v_smfmac_f32_16x16x64_bf16 v[224:227], v[200:203], v[72:79], v193\n" \
    "ds_read_b128 v[216:219], v192 offset:13312\n" \
    "v_smfmac_f32_16x16x64_bf16 v[224:227], v[204:207], v[80:87], v193\n" \
    "v_permlane16_swap_b32 v212, v213\n" \
    "v_smfmac_f32_16x16x64_bf16 v[224:227], v[208:211], v[88:95], v193\n" \
    "buffer_store_dwordx2 v[212:213], v196, s[8:11], s36 offen sc1\n" \
    "s_waitcnt lgkmcnt(3)\n" \
    "v_smfmac_f32_16x16x64_bf16 v[228:231], v[208:211], v[236:243], v193\n" \
    "ds_read_b128 v[212:215], v192 offset:13312\n" \
    "s_waitcnt lgkmcnt(2)\n" \
    "v_smfmac_f32_16x16x64_bf16 v[232:235], v[200:203], v[244:251], v193\n" \
    "ds_read_b128 v[236:239], v194 offset:8192\n" \
    "ds_read_b128 v[240:243], v194 offset:9216\n" \
    "v_add_f32 v220, v220, v222\n" \
    "v_add_f32 v221, v221, v223\n" \
    "v_smfmac_f32_16x16x64_bf16 v[252:255], v[200:203], v[96:103], v193\n" \
    "ds_read_b128 v[244:247], v194 offset:10240\n" \
    "ds_read_b128 v[248:251], v194 offset:11264\n" \
    "v_smfmac_f32_16x16x64_bf16 v[252:255], v[204:207], v[104:111], v193\n" \
    "v_cvt_pk_bf16_f32 v220, v220, v221\n" \
    "v_add_f32 v224, v224, v226\n" \
    "v_add_f32 v225, v225, v227\n" \
    "v_smfmac_f32_16x16x64_bf16 v[252:255], v[208:211], v[112:119], v193\n" \
    "v_cvt_pk_bf16_f32 v221, v224, v225\n" \
    "s_waitcnt lgkmcnt(5)\n" \
    "v_smfmac_f32_16x16x64_bf16 v[216:219], v[200:203], v[120:127], v193\n" \
    "ds_read_b128 v[224:227], v192 offset:13312\n" \
    "v_permlane16_swap_b32 v220, v221\n" \
    "v_smfmac_f32_16x16x64_bf16 v[216:219], v[204:207], v[128:135], v193\n" \
    "buffer_store_dwordx2 v[220:221], v196, s[8:11], s41 offen sc1\n" \
    "v_add_f32 v228, v228, v230\n" \
    "v_add_f32 v229, v229, v231\n" \
    "v_smfmac_f32_16x16x64_bf16 v[216:219], v[208:211], v[136:143], v193\n" \
    "v_cvt_pk_bf16_f32 v228, v228, v229\n" \
    "s_waitcnt lgkmcnt(3)\n" \
    "v_smfmac_f32_16x16x64_bf16 v[232:235], v[204:207], v[236:243], v193\n" \
    "s_waitcnt lgkmcnt(1)\n" \
    "v_smfmac_f32_16x16x64_bf16 v[232:235], v[208:211], v[244:251], v193\n" \
    "v_add_f32 v252, v252, v254\n" \
    "v_add_f32 v253, v253, v255\n" \
    "v_smfmac_f32_16x16x64_bf16 v[212:215], v[200:203], v[144:151], v193\n" \
    "v_cvt_pk_bf16_f32 v252, v252, v253\n" \
    "v_smfmac_f32_16x16x64_bf16 v[212:215], v[204:207], v[152:159], v193\n" \
    "v_add_f32 v216, v216, v218\n" \
    "v_add_f32 v217, v217, v219\n" \
    "v_smfmac_f32_16x16x64_bf16 v[212:215], v[208:211], v[160:167], v193\n" \
    "v_cvt_pk_bf16_f32 v253, v216, v217\n" \
    "s_waitcnt lgkmcnt(0)\n" \
    "v_smfmac_f32_16x16x64_bf16 v[224:227], v[200:203], v[168:175], v193\n" \
    "v_add_f32 v232, v232, v234\n" \
    "v_add_f32 v233, v233, v235\n" \
    "v_permlane16_swap_b32 v252, v253\n" \
    "v_smfmac_f32_16x16x64_bf16 v[224:227], v[204:207], v[176:183], v193\n" \
    "buffer_store_dwordx2 v[252:253], v196, s[8:11], s42 offen sc1\n" \
    "v_smfmac_f32_16x16x64_bf16 v[224:227], v[208:211], v[184:191], v193\n" \
    "v_cvt_pk_bf16_f32 v229, v232, v233\n" \
    "v_add_f32 v212, v212, v214\n" \
    "v_add_f32 v213, v213, v215\n" \
    "v_permlane16_swap_b32 v228, v229\n" \
    "buffer_store_dwordx2 v[228:229], v196, s[8:11], s44 offen sc1\n" \
    "v_cvt_pk_bf16_f32 v212, v212, v213\n" \
    "s_nop 1\n" \
    "v_add_f32 v224, v224, v226\n" \
    "v_add_f32 v225, v225, v227\n" \
    "v_cvt_pk_bf16_f32 v213, v224, v225\n" \
    "s_nop 1\n" \
    "v_permlane16_swap_b32 v212, v213\n" \
    "buffer_store_dwordx2 v[212:213], v196, s[8:11], s43 offen sc1\n" \
    "s_cmp_lt_u32 s32, 128\n" \
    "s_cbranch_scc1 .Luao_l_%=\n" \
    "s_waitcnt vmcnt(0)\n" \
    "s_endpgm\n" \
    ""
#define R_ASM \
    "s_mov_b32 s32, 0\n" \
    "s_mov_b32 s46, 0\n" \
    "s_mov_b32 s53, 0x40000\n" \
    "s_mov_b64 s[56:57], 0xff\n" \
    "s_mov_b32 s50, 0\n" \
    "s_mov_b32 s51, 0\n" \
    "s_mov_b32 s45, 0x80000000\n" \
    "s_lshr_b32 s33, s19, 8\n" \
    "s_xor_b32 s33, s33, s27\n" \
    "s_and_b32 s33, s33, 0x2000\n" \
    "s_or_b32 s39, s19, s33\n" \
    "s_add_u32 s40, s39, 65536\n" \
    "v_mov_b32 v160, -1\n" \
    "v_mov_b32 v161, -1\n" \
    "v_mov_b32 v162, -1\n" \
    "v_mov_b32 v163, -1\n" \
    "s_mov_b32 s52, 0\n" \
    ".Lr_b0_%=:\n" \
    "buffer_load_dword v18, v2, s[8:11], s50 offen sc1\n" \
    "s_waitcnt vmcnt(0)\n" \
    "v_cmp_eq_u32 vcc, 0, v18\n" \
    "s_nop 0\n" \
    "s_and_b64 vcc, vcc, s[56:57]\n" \
    "s_cbranch_scc0 .Lr_b1_%=\n" \
    "s_sleep 2\n" \
    "s_add_u32 s52, s52, 1\n" \
    "s_cmp_lt_u32 s52, s53\n" \
    "s_cbranch_scc1 .Lr_b0_%=\n" \
    "s_mov_b32 s53, 0\n" \
    "s_branch .Lr_b1_%=\n" \
    ".Lr_b1_%=:\n" \
    "buffer_load_dwordx4 v[192:195], v0, s[8:11], s39 offen offset:0 sc1\n" \
    "buffer_load_dwordx4 v[196:199], v0, s[8:11], s39 offen offset:1024 sc1\n" \
    "buffer_load_dwordx4 v[200:203], v0, s[8:11], s39 offen offset:2048 sc1\n" \
    "buffer_load_dwordx4 v[204:207], v0, s[8:11], s39 offen offset:3072 sc1\n" \
    "buffer_load_dwordx4 v[208:211], v0, s[8:11], s40 offen offset:0 sc1\n" \
    "buffer_load_dwordx4 v[212:215], v0, s[8:11], s40 offen offset:1024 sc1\n" \
    "buffer_load_dwordx4 v[216:219], v0, s[8:11], s40 offen offset:2048 sc1\n" \
    "buffer_load_dwordx4 v[220:223], v0, s[8:11], s40 offen offset:3072 sc1\n" \
    "s_mov_b32 s50, 16384\n" \
    "buffer_load_dword v18, v2, s[8:11], s50 offen sc1\n" \
    "s_mov_b32 s52, 0\n" \
    "s_mov_b32 s68, s40\n" \
    "s_memrealtime s[64:65]\n" \
    "s_waitcnt vmcnt(0) lgkmcnt(0)\n" \
    "s_cmp_eq_u32 s16, 0\n" \
    "s_cbranch_scc1 .Lr_lA_%=\n" \
    "s_cmp_gt_u32 s16, 4\n" \
    "s_cbranch_scc1 .Lr_lC_%=\n" \
    "s_bitcmp1_b32 s16, 1\n" \
    "s_cbranch_scc1 .Lr_lB_%=\n" \
    ".Lr_lD_%=:\n" \
    "s_and_b32 s33, s32, 3\n" \
    "s_lshl_b32 s35, s33, 16\n" \
    "s_add_u32 s35, s35, s17\n" \
    "s_lshl_b32 s61, s33, 17\n" \
    "s_add_u32 s41, s61, s20\n" \
    "s_add_u32 s42, s41, 4096\n" \
    "s_add_u32 s36, s18, s46\n" \
    "s_add_u32 s37, s36, 16384\n" \
    "s_add_u32 s46, s46, 655360\n" \
    "s_cmp_eq_u32 s46, 1966080\n" \
    "s_cselect_b32 s46, 0, s46\n" \
    "s_add_u32 s49, s32, 1\n" \
    "s_and_b32 s33, s49, 3\n" \
    "s_lshl_b32 s38, s33, 15\n" \
    "s_and_b32 s33, s49, %[brn]-1\n" \
    "s_mul_i32 s39, s33, 2621440\n" \
    "s_add_u32 s39, s39, s19\n" \
    "s_lshr_b32 s33, s39, 8\n" \
    "s_xor_b32 s33, s33, s27\n" \
    "s_and_b32 s33, s33, 0x2000\n" \
    "s_or_b32 s39, s39, s33\n" \
    "s_mul_i32 s43, s32, 40960\n" \
    "s_add_u32 s43, s43, s21\n" \
    "s_memrealtime s[58:59]\n" \
    "s_cmp_eq_u32 s32, 63\n" \
    "s_cselect_b32 s38, 0x80000000, s38\n" \
    "s_waitcnt lgkmcnt(0)\n" \
    "s_mov_b32 s55, s58\n" \
    "s_branch .Lr_hD0b_%=\n" \
    ".Lr_hD0a_%=:\n" \
    "s_sleep 1\n" \
    ".Lr_hD0b_%=:\n" \
    "s_memrealtime vcc\n" \
    "s_waitcnt lgkmcnt(0)\n" \
    "s_sub_i32 vcc_lo, s64, vcc_lo\n" \
    "s_cmp_gt_i32 vcc_lo, 8\n" \
    "s_cbranch_scc1 .Lr_hD0a_%=\n" \
    "s_cmp_gt_i32 vcc_lo, 0\n" \
    "s_cbranch_scc1 .Lr_hD0b_%=\n" \
    "s_mov_b32 s52, 0\n" \
    ".Lr_tvD0_%=:\n" \
    "buffer_load_dwordx2 v[16:17], v3, s[8:11], s51 offen sc1\n" \
    "s_add_u32 s52, s52, 1\n" \
    "s_cmp_lt_u32 s52, s53\n" \
    "s_cselect_b32 s53, s53, 0\n" \
    "s_cselect_b64 s[66:67], s[56:57], 0\n" \
    "s_waitcnt vmcnt(0)\n" \
    "v_cmp_ne_u32 vcc, s49, v17\n" \
    "v_subrev_u32 v19, s55, v16\n" \
    "s_and_b64 vcc, vcc, s[66:67]\n" \
    "s_cbranch_scc1 .Lr_tvD0_%=\n" \
    "v_max_i32_dpp v19, v19, v19 quad_perm:[1,0,3,2] row_mask:0xf bank_mask:0xf bound_ctrl:1\n" \
    "s_nop 1\n" \
    "v_max_i32_dpp v19, v19, v19 quad_perm:[2,3,0,1] row_mask:0xf bank_mask:0xf bound_ctrl:1\n" \
    "s_nop 0\n" \
    "v_readlane_b32 s48, v19, 4\n" \
    "v_readlane_b32 s47, v19, 0\n" \
    "s_min_i32 s48, s48, 1000\n" \
    "s_min_i32 s47, s47, 1000\n" \
    "s_add_u32 s48, s48, s55\n" \
    "s_add_u32 s48, s48, s22\n" \
    ".Lr_goD0_%=:\n" \
    "s_branch .Lr_uD0b_%=\n" \
    ".Lr_uD0a_%=:\n" \
    "s_sleep 1\n" \
    ".Lr_uD0b_%=:\n" \
    "s_memrealtime vcc\n" \
    "s_waitcnt lgkmcnt(0)\n" \
    "s_sub_i32 vcc_lo, s48, vcc_lo\n" \
    "s_cmp_gt_i32 vcc_lo, 8\n" \
    "s_cbranch_scc1 .Lr_uD0a_%=\n" \
    "s_cmp_gt_i32 vcc_lo, 0\n" \
    "s_cbranch_scc1 .Lr_uD0b_%=\n" \
    "buffer_load_dwordx4 v[36:39], v0, s[8:11], s36 offen sc1\n" \
    "buffer_load_dwordx4 v[40:43], v0, s[8:11], s37 offen sc1\n" \
    "s_add_u32 s47, s47, s55\n" \
    "s_add_u32 s47, s47, %[dr]\n" \
    "v_cmp_eq_u32 vcc, 0, v18\n" \
    "s_nop 0\n" \
    "s_and_b64 vcc, vcc, s[56:57]\n" \
    "s_cselect_b32 s54, 0, 1\n" \
    "s_cmp_eq_u32 s32, 63\n" \
    "s_cselect_b32 s54, 2, s54\n" \
    "s_cmp_eq_u32 s54, 1\n" \
    "s_cselect_b32 s39, s39, 0x80000000\n" \
    "s_add_u32 s40, s39, 65536\n" \
    "s_branch .Lr_pD0b_%=\n" \
    ".Lr_pD0a_%=:\n" \
    "s_sleep 1\n" \
    ".Lr_pD0b_%=:\n" \
    "s_memrealtime vcc\n" \
    "s_waitcnt lgkmcnt(0)\n" \
    "s_sub_i32 vcc_lo, s47, vcc_lo\n" \
    "s_cmp_gt_i32 vcc_lo, 8\n" \
    "s_cbranch_scc1 .Lr_pD0a_%=\n" \
    "s_cmp_gt_i32 vcc_lo, 0\n" \
    "s_cbranch_scc1 .Lr_pD0b_%=\n" \
    "buffer_load_dwordx4 v[32:35], v4, s[8:11], s35 offen sc1\n" \
    "buffer_load_dwordx4 v[24:27], v0, s[8:11], s35 offen sc1\n" \
    "buffer_load_dwordx4 v[28:31], v0, s[8:11], s35 offen offset:1024 sc1\n" \
    "s_barrier\n" \
    "buffer_load_dwordx4 v[52:55], v0, s[8:11], s41 offen offset:2048 sc1\n" \
    "buffer_load_dwordx4 v[44:47], v0, s[8:11], s41 offen offset:0 sc1\n" \
    "buffer_load_dwordx4 v[48:51], v0, s[8:11], s41 offen offset:1024 sc1\n" \
    "buffer_load_dwordx4 v[56:59], v0, s[8:11], s41 offen offset:3072 sc1\n" \
    "buffer_load_dword v21, v13, s[8:11], s61 offen sc1\n" \
    "s_waitcnt vmcnt(9)\n" \
    "v_max3_u32 v19, v36, v37, v38\n" \
    "s_waitcnt vmcnt(8)\n" \
    "v_max3_u32 v19, v19, v39, v40\n" \
    "v_max3_u32 v19, v19, v41, v42\n" \
    "v_max_u32 v19, v19, v43\n" \
    "s_waitcnt vmcnt(7)\n" \
    "v_mfma_f32_16x16x32_bf16 v[80:83], v[32:35], v[36:39], 0\n" \
    "v_max3_u32 v20, v32, v33, v34\n" \
    "v_mfma_f32_16x16x32_bf16 v[84:87], v[32:35], v[40:43], 0\n" \
    "v_max_u32 v20, v20, v35\n" \
    "s_waitcnt vmcnt(6)\n" \
    "v_smfmac_f32_16x16x64_bf16 v[80:83], v[24:27], v[192:199], v10\n" \
    "v_smfmac_f32_16x16x64_bf16 v[84:87], v[24:27], v[208:215], v10\n" \
    "v_max3_u32 v19, v19, v24, v25\n" \
    "v_max3_u32 v19, v19, v26, v27\n" \
    "s_waitcnt vmcnt(5)\n" \
    "v_smfmac_f32_16x16x64_bf16 v[80:83], v[28:31], v[200:207], v10\n" \
    "v_smfmac_f32_16x16x64_bf16 v[84:87], v[28:31], v[216:223], v10\n" \
    "v_max3_u32 v20, v20, v28, v29\n" \
    "v_max3_u32 v20, v20, v30, v31\n" \
    "v_max_u32 v19, v19, v20\n" \
    "v_cmp_eq_u32 vcc, -1, v19\n" \
    "s_nop 0\n" \
    "s_cbranch_vccnz .Lr_cpD0_%=\n" \
    ".Lr_cprD0_%=:\n" \
    "s_nop 0\n" \
    "v_pk_add_f32 v[80:81], v[80:81], v[82:83]\n" \
    "v_pk_add_f32 v[84:85], v[84:85], v[86:87]\n" \
    "ds_write2_b32 v5, v80, v81 offset0:0 offset1:4\n" \
    "ds_write2_b32 v5, v84, v85 offset0:64 offset1:68\n" \
    "s_waitcnt lgkmcnt(0)\n" \
    "s_barrier\n" \
    "s_barrier\n" \
    "s_memrealtime s[62:63]\n" \
    "buffer_load_dwordx4 v[60:63], v0, s[8:11], s42 offen offset:0 sc1\n" \
    "buffer_load_dwordx4 v[64:67], v0, s[8:11], s42 offen offset:1024 sc1\n" \
    "buffer_load_dwordx4 v[68:71], v0, s[8:11], s42 offen offset:2048 sc1\n" \
    "buffer_load_dwordx4 v[72:75], v0, s[8:11], s42 offen offset:3072 sc1\n" \
    "s_barrier\n" \
    "buffer_load_dwordx4 v[224:227], v0, s[8:11], s39 offen offset:0 sc1\n" \
    "buffer_load_dwordx4 v[228:231], v0, s[8:11], s39 offen offset:1024 sc1\n" \
    "buffer_load_dwordx4 v[232:235], v0, s[8:11], s39 offen offset:2048 sc1\n" \
    "buffer_load_dwordx4 v[236:239], v0, s[8:11], s39 offen offset:3072 sc1\n" \
    "buffer_load_dwordx4 v[240:243], v0, s[8:11], s40 offen offset:0 sc1\n" \
    "buffer_load_dwordx4 v[244:247], v0, s[8:11], s40 offen offset:1024 sc1\n" \
    "buffer_load_dwordx4 v[248:251], v0, s[8:11], s40 offen offset:2048 sc1\n" \
    "buffer_load_dwordx4 v[252:255], v0, s[8:11], s40 offen offset:3072 sc1\n" \
    "s_and_b32 s33, s32, s23\n" \
    "s_mul_i32 s45, s33, s24\n" \
    "s_cmp_lt_u32 s33, 64-%[pdist]\n" \
    "s_cselect_b32 s45, s45, 0x80000000\n" \
    "s_add_u32 s44, s43, %[x1]\n" \
    "s_add_u32 s50, s50, 16384\n" \
    "s_xor_b32 s51, s51, 16384\n" \
    "s_waitcnt vmcnt(15)\n" \
    "v_max3_u32 v19, v44, v45, v46\n" \
    "s_waitcnt vmcnt(14)\n" \
    "v_max3_u32 v19, v19, v47, v48\n" \
    "v_max3_u32 v19, v19, v49, v50\n" \
    "v_max3_u32 v19, v19, v51, v52\n" \
    "v_max3_u32 v19, v19, v53, v54\n" \
    "s_waitcnt vmcnt(13)\n" \
    "v_max3_u32 v19, v19, v55, v56\n" \
    "v_max3_u32 v19, v19, v57, v58\n" \
    "s_waitcnt vmcnt(11)\n" \
    "v_max3_u32 v19, v19, v59, v60\n" \
    "v_max3_u32 v19, v19, v61, v62\n" \
    "s_waitcnt vmcnt(10)\n" \
    "v_max3_u32 v19, v19, v63, v64\n" \
    "v_max3_u32 v19, v19, v65, v66\n" \
    "s_waitcnt vmcnt(9)\n" \
    "v_max3_u32 v19, v19, v67, v68\n" \
    "v_max3_u32 v19, v19, v69, v70\n" \
    "s_waitcnt vmcnt(8)\n" \
    "v_max3_u32 v19, v19, v71, v72\n" \
    "v_max3_u32 v19, v19, v73, v74\n" \
    "v_max_u32 v19, v19, v75\n" \
    "v_cmp_eq_u32 vcc, -1, v19\n" \
    "s_nop 0\n" \
    "s_cbranch_vccnz .Lr_ctD0_%=\n" \
    ".Lr_ctrD0_%=:\n" \
    "v_mfma_f32_16x16x32_bf16 v[88:91], v[36:39], v[72:75], 0\n" \
    "v_mfma_f32_16x16x32_bf16 v[88:91], v[192:195], v[56:59], v[88:91]\n" \
    "v_mfma_f32_16x16x32_bf16 v[88:91], v[196:199], v[60:63], v[88:91]\n" \
    "v_mfma_f32_16x16x32_bf16 v[88:91], v[200:203], v[64:67], v[88:91]\n" \
    "v_mfma_f32_16x16x32_bf16 v[88:91], v[204:207], v[68:71], v[88:91]\n" \
    "v_mfma_f32_16x16x32_bf16 v[92:95], v[40:43], v[72:75], 0\n" \
    "v_mfma_f32_16x16x32_bf16 v[92:95], v[208:211], v[56:59], v[92:95]\n" \
    "v_mfma_f32_16x16x32_bf16 v[92:95], v[212:215], v[60:63], v[92:95]\n" \
    "v_mfma_f32_16x16x32_bf16 v[92:95], v[216:219], v[64:67], v[92:95]\n" \
    "v_mfma_f32_16x16x32_bf16 v[92:95], v[220:223], v[68:71], v[92:95]\n" \
    "v_mfma_f32_16x16x32_bf16 v[80:83], v[36:39], v[52:55], 0\n" \
    "v_mfma_f32_16x16x32_bf16 v[84:87], v[40:43], v[52:55], 0\n" \
    "v_mov_b32 v193, v194\n" \
    "v_mov_b32 v194, v196\n" \
    "v_mov_b32 v195, v198\n" \
    "v_mov_b32 v201, v202\n" \
    "v_mov_b32 v202, v204\n" \
    "v_mov_b32 v203, v206\n" \
    "v_mov_b32 v209, v210\n" \
    "v_mov_b32 v210, v212\n" \
    "v_mov_b32 v211, v214\n" \
    "v_mov_b32 v217, v218\n" \
    "v_mov_b32 v218, v220\n" \
    "v_mov_b32 v219, v222\n" \
    "ds_write_b128 v8, v[88:91] offset:2048\n" \
    "ds_write_b128 v8, v[92:95] offset:3072\n" \
    "v_permlane16_swap_b32 v192, v194\n" \
    "v_permlane16_swap_b32 v193, v195\n" \
    "v_permlane16_swap_b32 v200, v202\n" \
    "v_permlane16_swap_b32 v201, v203\n" \
    "v_permlane16_swap_b32 v208, v210\n" \
    "v_permlane16_swap_b32 v209, v211\n" \
    "v_permlane16_swap_b32 v216, v218\n" \
    "v_permlane16_swap_b32 v217, v219\n" \
    "v_mfma_f32_16x16x32_bf16 v[80:83], v[192:195], v[44:47], v[80:83]\n" \
    "v_mfma_f32_16x16x32_bf16 v[84:87], v[208:211], v[44:47], v[84:87]\n" \
    "v_mfma_f32_16x16x32_bf16 v[80:83], v[200:203], v[48:51], v[80:83]\n" \
    "v_mfma_f32_16x16x32_bf16 v[84:87], v[216:219], v[48:51], v[84:87]\n" \
    "buffer_load_dword v18, v2, s[8:11], s50 offen sc1\n" \
    "s_nop 5\n" \
    "ds_write_b128 v8, v[80:83] offset:0\n" \
    "ds_write_b128 v8, v[84:87] offset:1024\n" \
    "s_waitcnt lgkmcnt(0)\n" \
    "s_barrier\n" \
    "s_add_u32 s64, s62, %[dh]\n" \
    "s_cmp_eq_u32 s54, 0\n" \
    "s_cbranch_scc1 .Lr_cbD0_%=\n" \
    ".Lr_cbrD0_%=:\n" \
    "s_add_u32 s32, s32, 1\n" \
    "s_mov_b32 s68, s40\n" \
    "s_and_b32 s33, s32, 3\n" \
    "s_lshl_b32 s35, s33, 16\n" \
    "s_add_u32 s35, s35, s17\n" \
    "s_lshl_b32 s61, s33, 17\n" \
    "s_add_u32 s41, s61, s20\n" \
    "s_add_u32 s42, s41, 4096\n" \
    "s_add_u32 s36, s18, s46\n" \
    "s_add_u32 s37, s36, 16384\n" \
    "s_add_u32 s46, s46, 655360\n" \
    "s_cmp_eq_u32 s46, 1966080\n" \
    "s_cselect_b32 s46, 0, s46\n" \
    "s_add_u32 s49, s32, 1\n" \
    "s_and_b32 s33, s49, 3\n" \
    "s_lshl_b32 s38, s33, 15\n" \
    "s_and_b32 s33, s49, %[brn]-1\n" \
    "s_mul_i32 s39, s33, 2621440\n" \
    "s_add_u32 s39, s39, s19\n" \
    "s_lshr_b32 s33, s39, 8\n" \
    "s_xor_b32 s33, s33, s27\n" \
    "s_and_b32 s33, s33, 0x2000\n" \
    "s_or_b32 s39, s39, s33\n" \
    "s_mul_i32 s43, s32, 40960\n" \
    "s_add_u32 s43, s43, s21\n" \
    "s_memrealtime s[58:59]\n" \
    "s_cmp_eq_u32 s32, 63\n" \
    "s_cselect_b32 s38, 0x80000000, s38\n" \
    "s_waitcnt lgkmcnt(0)\n" \
    "s_mov_b32 s55, s58\n" \
    "s_branch .Lr_hD1b_%=\n" \
    ".Lr_hD1a_%=:\n" \
    "s_sleep 1\n" \
    ".Lr_hD1b_%=:\n" \
    "s_memrealtime vcc\n" \
    "s_waitcnt lgkmcnt(0)\n" \
    "s_sub_i32 vcc_lo, s64, vcc_lo\n" \
    "s_cmp_gt_i32 vcc_lo, 8\n" \
    "s_cbranch_scc1 .Lr_hD1a_%=\n" \
    "s_cmp_gt_i32 vcc_lo, 0\n" \
    "s_cbranch_scc1 .Lr_hD1b_%=\n" \
    "s_mov_b32 s52, 0\n" \
    ".Lr_tvD1_%=:\n" \
    "buffer_load_dwordx2 v[16:17], v3, s[8:11], s51 offen sc1\n" \
    "s_add_u32 s52, s52, 1\n" \
    "s_cmp_lt_u32 s52, s53\n" \
    "s_cselect_b32 s53, s53, 0\n" \
    "s_cselect_b64 s[66:67], s[56:57], 0\n" \
    "s_waitcnt vmcnt(0)\n" \
    "v_cmp_ne_u32 vcc, s49, v17\n" \
    "v_subrev_u32 v19, s55, v16\n" \
    "s_and_b64 vcc, vcc, s[66:67]\n" \
    "s_cbranch_scc1 .Lr_tvD1_%=\n" \
    "v_max_i32_dpp v19, v19, v19 quad_perm:[1,0,3,2] row_mask:0xf bank_mask:0xf bound_ctrl:1\n" \
    "s_nop 1\n" \
    "v_max_i32_dpp v19, v19, v19 quad_perm:[2,3,0,1] row_mask:0xf bank_mask:0xf bound_ctrl:1\n" \
    "s_nop 0\n" \
    "v_readlane_b32 s48, v19, 4\n" \
    "v_readlane_b32 s47, v19, 0\n" \
    "s_min_i32 s48, s48, 1000\n" \
    "s_min_i32 s47, s47, 1000\n" \
    "s_add_u32 s48, s48, s55\n" \
    "s_add_u32 s48, s48, s22\n" \
    ".Lr_goD1_%=:\n" \
    "s_branch .Lr_uD1b_%=\n" \
    ".Lr_uD1a_%=:\n" \
    "s_sleep 1\n" \
    ".Lr_uD1b_%=:\n" \
    "s_memrealtime vcc\n" \
    "s_waitcnt lgkmcnt(0)\n" \
    "s_sub_i32 vcc_lo, s48, vcc_lo\n" \
    "s_cmp_gt_i32 vcc_lo, 8\n" \
    "s_cbranch_scc1 .Lr_uD1a_%=\n" \
    "s_cmp_gt_i32 vcc_lo, 0\n" \
    "s_cbranch_scc1 .Lr_uD1b_%=\n" \
    "buffer_load_dwordx4 v[36:39], v0, s[8:11], s36 offen sc1\n" \
    "buffer_load_dwordx4 v[40:43], v0, s[8:11], s37 offen sc1\n" \
    "s_add_u32 s47, s47, s55\n" \
    "s_add_u32 s47, s47, %[dr]\n" \
    "v_cmp_eq_u32 vcc, 0, v18\n" \
    "s_nop 0\n" \
    "s_and_b64 vcc, vcc, s[56:57]\n" \
    "s_cselect_b32 s54, 0, 1\n" \
    "s_cmp_eq_u32 s32, 63\n" \
    "s_cselect_b32 s54, 2, s54\n" \
    "s_cmp_eq_u32 s54, 1\n" \
    "s_cselect_b32 s39, s39, 0x80000000\n" \
    "s_add_u32 s40, s39, 65536\n" \
    "s_branch .Lr_pD1b_%=\n" \
    ".Lr_pD1a_%=:\n" \
    "s_sleep 1\n" \
    ".Lr_pD1b_%=:\n" \
    "s_memrealtime vcc\n" \
    "s_waitcnt lgkmcnt(0)\n" \
    "s_sub_i32 vcc_lo, s47, vcc_lo\n" \
    "s_cmp_gt_i32 vcc_lo, 8\n" \
    "s_cbranch_scc1 .Lr_pD1a_%=\n" \
    "s_cmp_gt_i32 vcc_lo, 0\n" \
    "s_cbranch_scc1 .Lr_pD1b_%=\n" \
    "buffer_load_dwordx4 v[32:35], v4, s[8:11], s35 offen sc1\n" \
    "buffer_load_dwordx4 v[24:27], v0, s[8:11], s35 offen sc1\n" \
    "buffer_load_dwordx4 v[28:31], v0, s[8:11], s35 offen offset:1024 sc1\n" \
    "s_barrier\n" \
    "buffer_load_dwordx4 v[52:55], v0, s[8:11], s41 offen offset:2048 sc1\n" \
    "buffer_load_dwordx4 v[44:47], v0, s[8:11], s41 offen offset:0 sc1\n" \
    "buffer_load_dwordx4 v[48:51], v0, s[8:11], s41 offen offset:1024 sc1\n" \
    "buffer_load_dwordx4 v[56:59], v0, s[8:11], s41 offen offset:3072 sc1\n" \
    "buffer_load_dword v21, v13, s[8:11], s61 offen sc1\n" \
    "s_waitcnt vmcnt(9)\n" \
    "v_max3_u32 v19, v36, v37, v38\n" \
    "s_waitcnt vmcnt(8)\n" \
    "v_max3_u32 v19, v19, v39, v40\n" \
    "v_max3_u32 v19, v19, v41, v42\n" \
    "v_max_u32 v19, v19, v43\n" \
    "s_waitcnt vmcnt(7)\n" \
    "v_mfma_f32_16x16x32_bf16 v[80:83], v[32:35], v[36:39], 0\n" \
    "v_max3_u32 v20, v32, v33, v34\n" \
    "v_mfma_f32_16x16x32_bf16 v[84:87], v[32:35], v[40:43], 0\n" \
    "v_max_u32 v20, v20, v35\n" \
    "s_waitcnt vmcnt(6)\n" \
    "v_smfmac_f32_16x16x64_bf16 v[80:83], v[24:27], v[224:231], v10\n" \
    "v_smfmac_f32_16x16x64_bf16 v[84:87], v[24:27], v[240:247], v10\n" \
    "v_max3_u32 v19, v19, v24, v25\n" \
    "v_max3_u32 v19, v19, v26, v27\n" \
    "s_waitcnt vmcnt(5)\n" \
    "v_smfmac_f32_16x16x64_bf16 v[80:83], v[28:31], v[232:239], v10\n" \
    "v_smfmac_f32_16x16x64_bf16 v[84:87], v[28:31], v[248:255], v10\n" \
    "v_max3_u32 v20, v20, v28, v29\n" \
    "v_max3_u32 v20, v20, v30, v31\n" \
    "v_max_u32 v19, v19, v20\n" \
    "v_cmp_eq_u32 vcc, -1, v19\n" \
    "s_nop 0\n" \
    "s_cbranch_vccnz .Lr_cpD1_%=\n" \
    ".Lr_cprD1_%=:\n" \
    "s_nop 0\n" \
    "v_pk_add_f32 v[80:81], v[80:81], v[82:83]\n" \
    "v_pk_add_f32 v[84:85], v[84:85], v[86:87]\n" \
    "ds_write2_b32 v5, v80, v81 offset0:0 offset1:4\n" \
    "ds_write2_b32 v5, v84, v85 offset0:64 offset1:68\n" \
    "s_waitcnt lgkmcnt(0)\n" \
    "s_barrier\n" \
    "s_barrier\n" \
    "s_memrealtime s[62:63]\n" \
    "buffer_load_dwordx4 v[60:63], v0, s[8:11], s42 offen offset:0 sc1\n" \
    "buffer_load_dwordx4 v[64:67], v0, s[8:11], s42 offen offset:1024 sc1\n" \
    "buffer_load_dwordx4 v[68:71], v0, s[8:11], s42 offen offset:2048 sc1\n" \
    "buffer_load_dwordx4 v[72:75], v0, s[8:11], s42 offen offset:3072 sc1\n" \
    "s_barrier\n" \
    "buffer_load_dwordx4 v[192:195], v0, s[8:11], s39 offen offset:0 sc1\n" \
    "buffer_load_dwordx4 v[196:199], v0, s[8:11], s39 offen offset:1024 sc1\n" \
    "buffer_load_dwordx4 v[200:203], v0, s[8:11], s39 offen offset:2048 sc1\n" \
    "buffer_load_dwordx4 v[204:207], v0, s[8:11], s39 offen offset:3072 sc1\n" \
    "buffer_load_dwordx4 v[208:211], v0, s[8:11], s40 offen offset:0 sc1\n" \
    "buffer_load_dwordx4 v[212:215], v0, s[8:11], s40 offen offset:1024 sc1\n" \
    "buffer_load_dwordx4 v[216:219], v0, s[8:11], s40 offen offset:2048 sc1\n" \
    "buffer_load_dwordx4 v[220:223], v0, s[8:11], s40 offen offset:3072 sc1\n" \
    "s_and_b32 s33, s32, s23\n" \
    "s_mul_i32 s45, s33, s24\n" \
    "s_cmp_lt_u32 s33, 64-%[pdist]\n" \
    "s_cselect_b32 s45, s45, 0x80000000\n" \
    "s_add_u32 s44, s43, %[x1]\n" \
    "s_add_u32 s50, s50, 16384\n" \
    "s_xor_b32 s51, s51, 16384\n" \
    "s_waitcnt vmcnt(15)\n" \
    "v_max3_u32 v19, v44, v45, v46\n" \
    "s_waitcnt vmcnt(14)\n" \
    "v_max3_u32 v19, v19, v47, v48\n" \
    "v_max3_u32 v19, v19, v49, v50\n" \
    "v_max3_u32 v19, v19, v51, v52\n" \
    "v_max3_u32 v19, v19, v53, v54\n" \
    "s_waitcnt vmcnt(13)\n" \
    "v_max3_u32 v19, v19, v55, v56\n" \
    "v_max3_u32 v19, v19, v57, v58\n" \
    "s_waitcnt vmcnt(11)\n" \
    "v_max3_u32 v19, v19, v59, v60\n" \
    "v_max3_u32 v19, v19, v61, v62\n" \
    "s_waitcnt vmcnt(10)\n" \
    "v_max3_u32 v19, v19, v63, v64\n" \
    "v_max3_u32 v19, v19, v65, v66\n" \
    "s_waitcnt vmcnt(9)\n" \
    "v_max3_u32 v19, v19, v67, v68\n" \
    "v_max3_u32 v19, v19, v69, v70\n" \
    "s_waitcnt vmcnt(8)\n" \
    "v_max3_u32 v19, v19, v71, v72\n" \
    "v_max3_u32 v19, v19, v73, v74\n" \
    "v_max_u32 v19, v19, v75\n" \
    "v_cmp_eq_u32 vcc, -1, v19\n" \
    "s_nop 0\n" \
    "s_cbranch_vccnz .Lr_ctD1_%=\n" \
    ".Lr_ctrD1_%=:\n" \
    "v_mfma_f32_16x16x32_bf16 v[88:91], v[36:39], v[72:75], 0\n" \
    "v_mfma_f32_16x16x32_bf16 v[88:91], v[224:227], v[56:59], v[88:91]\n" \
    "v_mfma_f32_16x16x32_bf16 v[88:91], v[228:231], v[60:63], v[88:91]\n" \
    "v_mfma_f32_16x16x32_bf16 v[88:91], v[232:235], v[64:67], v[88:91]\n" \
    "v_mfma_f32_16x16x32_bf16 v[88:91], v[236:239], v[68:71], v[88:91]\n" \
    "v_mfma_f32_16x16x32_bf16 v[92:95], v[40:43], v[72:75], 0\n" \
    "v_mfma_f32_16x16x32_bf16 v[92:95], v[240:243], v[56:59], v[92:95]\n" \
    "v_mfma_f32_16x16x32_bf16 v[92:95], v[244:247], v[60:63], v[92:95]\n" \
    "v_mfma_f32_16x16x32_bf16 v[92:95], v[248:251], v[64:67], v[92:95]\n" \
    "v_mfma_f32_16x16x32_bf16 v[92:95], v[252:255], v[68:71], v[92:95]\n" \
    "v_mfma_f32_16x16x32_bf16 v[80:83], v[36:39], v[52:55], 0\n" \
    "v_mfma_f32_16x16x32_bf16 v[84:87], v[40:43], v[52:55], 0\n" \
    "v_mov_b32 v225, v226\n" \
    "v_mov_b32 v226, v228\n" \
    "v_mov_b32 v227, v230\n" \
    "v_mov_b32 v233, v234\n" \
    "v_mov_b32 v234, v236\n" \
    "v_mov_b32 v235, v238\n" \
    "v_mov_b32 v241, v242\n" \
    "v_mov_b32 v242, v244\n" \
    "v_mov_b32 v243, v246\n" \
    "v_mov_b32 v249, v250\n" \
    "v_mov_b32 v250, v252\n" \
    "v_mov_b32 v251, v254\n" \
    "ds_write_b128 v8, v[88:91] offset:2048\n" \
    "ds_write_b128 v8, v[92:95] offset:3072\n" \
    "v_permlane16_swap_b32 v224, v226\n" \
    "v_permlane16_swap_b32 v225, v227\n" \
    "v_permlane16_swap_b32 v232, v234\n" \
    "v_permlane16_swap_b32 v233, v235\n" \
    "v_permlane16_swap_b32 v240, v242\n" \
    "v_permlane16_swap_b32 v241, v243\n" \
    "v_permlane16_swap_b32 v248, v250\n" \
    "v_permlane16_swap_b32 v249, v251\n" \
    "v_mfma_f32_16x16x32_bf16 v[80:83], v[224:227], v[44:47], v[80:83]\n" \
    "v_mfma_f32_16x16x32_bf16 v[84:87], v[240:243], v[44:47], v[84:87]\n" \
    "v_mfma_f32_16x16x32_bf16 v[80:83], v[232:235], v[48:51], v[80:83]\n" \
    "v_mfma_f32_16x16x32_bf16 v[84:87], v[248:251], v[48:51], v[84:87]\n" \
    "buffer_load_dword v18, v2, s[8:11], s50 offen sc1\n" \
    "s_nop 5\n" \
    "ds_write_b128 v8, v[80:83] offset:0\n" \
    "ds_write_b128 v8, v[84:87] offset:1024\n" \
    "s_waitcnt lgkmcnt(0)\n" \
    "s_barrier\n" \
    "s_add_u32 s64, s62, %[dh]\n" \
    "s_cmp_eq_u32 s54, 0\n" \
    "s_cbranch_scc1 .Lr_cbD1_%=\n" \
    ".Lr_cbrD1_%=:\n" \
    "s_add_u32 s32, s32, 1\n" \
    "s_mov_b32 s68, s40\n" \
    "s_cmp_lt_u32 s32, 64\n" \
    "s_cbranch_scc1 .Lr_lD_%=\n" \
    "s_waitcnt vmcnt(0)\n" \
    "s_endpgm\n" \
    ".Lr_lA_%=:\n" \
    "s_and_b32 s33, s32, 3\n" \
    "s_lshl_b32 s35, s33, 16\n" \
    "s_add_u32 s35, s35, s17\n" \
    "s_lshl_b32 s61, s33, 17\n" \
    "s_add_u32 s41, s61, s20\n" \
    "s_add_u32 s42, s41, 4096\n" \
    "s_add_u32 s36, s18, s46\n" \
    "s_add_u32 s37, s36, 16384\n" \
    "s_add_u32 s46, s46, 655360\n" \
    "s_cmp_eq_u32 s46, 1966080\n" \
    "s_cselect_b32 s46, 0, s46\n" \
    "s_add_u32 s49, s32, 1\n" \
    "s_and_b32 s33, s49, 3\n" \
    "s_lshl_b32 s38, s33, 15\n" \
    "s_and_b32 s33, s49, %[brn]-1\n" \
    "s_mul_i32 s39, s33, 2621440\n" \
    "s_add_u32 s39, s39, s19\n" \
    "s_lshr_b32 s33, s39, 8\n" \
    "s_xor_b32 s33, s33, s27\n" \
    "s_and_b32 s33, s33, 0x2000\n" \
    "s_or_b32 s39, s39, s33\n" \
    "s_mul_i32 s43, s32, 40960\n" \
    "s_add_u32 s43, s43, s21\n" \
    "s_memrealtime s[58:59]\n" \
    "s_cmp_eq_u32 s32, 63\n" \
    "s_cselect_b32 s38, 0x80000000, s38\n" \
    "s_waitcnt lgkmcnt(0)\n" \
    "s_mov_b32 s55, s58\n" \
    "s_branch .Lr_hA0b_%=\n" \
    ".Lr_hA0a_%=:\n" \
    "s_sleep 1\n" \
    ".Lr_hA0b_%=:\n" \
    "s_memrealtime vcc\n" \
    "s_waitcnt lgkmcnt(0)\n" \
    "s_sub_i32 vcc_lo, s64, vcc_lo\n" \
    "s_cmp_gt_i32 vcc_lo, 8\n" \
    "s_cbranch_scc1 .Lr_hA0a_%=\n" \
    "s_cmp_gt_i32 vcc_lo, 0\n" \
    "s_cbranch_scc1 .Lr_hA0b_%=\n" \
    "s_mov_b32 s52, 0\n" \
    ".Lr_tvA0_%=:\n" \
    "buffer_load_dwordx2 v[16:17], v3, s[8:11], s51 offen sc1\n" \
    "s_add_u32 s52, s52, 1\n" \
    "s_cmp_lt_u32 s52, s53\n" \
    "s_cselect_b32 s53, s53, 0\n" \
    "s_cselect_b64 s[66:67], s[56:57], 0\n" \
    "s_waitcnt vmcnt(0)\n" \
    "v_cmp_ne_u32 vcc, s49, v17\n" \
    "v_subrev_u32 v19, s55, v16\n" \
    "s_and_b64 vcc, vcc, s[66:67]\n" \
    "s_cbranch_scc1 .Lr_tvA0_%=\n" \
    "v_max_i32_dpp v19, v19, v19 quad_perm:[1,0,3,2] row_mask:0xf bank_mask:0xf bound_ctrl:1\n" \
    "s_nop 1\n" \
    "v_max_i32_dpp v19, v19, v19 quad_perm:[2,3,0,1] row_mask:0xf bank_mask:0xf bound_ctrl:1\n" \
    "s_nop 0\n" \
    "v_readlane_b32 s48, v19, 4\n" \
    "v_readlane_b32 s47, v19, 0\n" \
    "s_min_i32 s48, s48, 1000\n" \
    "s_min_i32 s47, s47, 1000\n" \
    "s_add_u32 s48, s48, s55\n" \
    "s_add_u32 s48, s48, s22\n" \
    ".Lr_goA0_%=:\n" \
    "s_branch .Lr_uA0b_%=\n" \
    ".Lr_uA0a_%=:\n" \
    "s_sleep 1\n" \
    ".Lr_uA0b_%=:\n" \
    "s_memrealtime vcc\n" \
    "s_waitcnt lgkmcnt(0)\n" \
    "s_sub_i32 vcc_lo, s48, vcc_lo\n" \
    "s_cmp_gt_i32 vcc_lo, 8\n" \
    "s_cbranch_scc1 .Lr_uA0a_%=\n" \
    "s_cmp_gt_i32 vcc_lo, 0\n" \
    "s_cbranch_scc1 .Lr_uA0b_%=\n" \
    "buffer_load_dwordx4 v[36:39], v0, s[8:11], s36 offen sc1\n" \
    "buffer_load_dwordx4 v[40:43], v0, s[8:11], s37 offen sc1\n" \
    "s_add_u32 s47, s47, s55\n" \
    "s_add_u32 s47, s47, %[dr]\n" \
    "v_cmp_eq_u32 vcc, 0, v18\n" \
    "s_nop 0\n" \
    "s_and_b64 vcc, vcc, s[56:57]\n" \
    "s_cselect_b32 s54, 0, 1\n" \
    "s_cmp_eq_u32 s32, 63\n" \
    "s_cselect_b32 s54, 2, s54\n" \
    "s_cmp_eq_u32 s54, 1\n" \
    "s_cselect_b32 s39, s39, 0x80000000\n" \
    "s_add_u32 s40, s39, 65536\n" \
    "s_branch .Lr_pA0b_%=\n" \
    ".Lr_pA0a_%=:\n" \
    "s_sleep 1\n" \
    ".Lr_pA0b_%=:\n" \
    "s_memrealtime vcc\n" \
    "s_waitcnt lgkmcnt(0)\n" \
    "s_sub_i32 vcc_lo, s47, vcc_lo\n" \
    "s_cmp_gt_i32 vcc_lo, 8\n" \
    "s_cbranch_scc1 .Lr_pA0a_%=\n" \
    "s_cmp_gt_i32 vcc_lo, 0\n" \
    "s_cbranch_scc1 .Lr_pA0b_%=\n" \
    "buffer_load_dwordx4 v[32:35], v4, s[8:11], s35 offen sc1\n" \
    "buffer_load_dwordx4 v[24:27], v0, s[8:11], s35 offen sc1\n" \
    "buffer_load_dwordx4 v[28:31], v0, s[8:11], s35 offen offset:1024 sc1\n" \
    "s_barrier\n" \
    "buffer_load_dwordx4 v[52:55], v0, s[8:11], s41 offen offset:2048 sc1\n" \
    "buffer_load_dwordx4 v[44:47], v0, s[8:11], s41 offen offset:0 sc1\n" \
    "buffer_load_dwordx4 v[48:51], v0, s[8:11], s41 offen offset:1024 sc1\n" \
    "buffer_load_dwordx4 v[56:59], v0, s[8:11], s41 offen offset:3072 sc1\n" \
    "s_waitcnt vmcnt(8)\n" \
    "v_max3_u32 v19, v36, v37, v38\n" \
    "s_waitcnt vmcnt(7)\n" \
    "v_max3_u32 v19, v19, v39, v40\n" \
    "v_max3_u32 v19, v19, v41, v42\n" \
    "v_max_u32 v19, v19, v43\n" \
    "s_waitcnt vmcnt(6)\n" \
    "v_mfma_f32_16x16x32_bf16 v[80:83], v[32:35], v[36:39], 0\n" \
    "v_max3_u32 v20, v32, v33, v34\n" \
    "v_mfma_f32_16x16x32_bf16 v[84:87], v[32:35], v[40:43], 0\n" \
    "v_max_u32 v20, v20, v35\n" \
    "s_waitcnt vmcnt(5)\n" \
    "v_smfmac_f32_16x16x64_bf16 v[80:83], v[24:27], v[192:199], v10\n" \
    "v_smfmac_f32_16x16x64_bf16 v[84:87], v[24:27], v[208:215], v10\n" \
    "v_max3_u32 v19, v19, v24, v25\n" \
    "v_max3_u32 v19, v19, v26, v27\n" \
    "s_waitcnt vmcnt(4)\n" \
    "v_smfmac_f32_16x16x64_bf16 v[80:83], v[28:31], v[200:207], v10\n" \
    "v_smfmac_f32_16x16x64_bf16 v[84:87], v[28:31], v[216:223], v10\n" \
    "v_max3_u32 v20, v20, v28, v29\n" \
    "v_max3_u32 v20, v20, v30, v31\n" \
    "v_max_u32 v19, v19, v20\n" \
    "v_cmp_eq_u32 vcc, -1, v19\n" \
    "s_nop 0\n" \
    "s_cbranch_vccnz .Lr_cpA0_%=\n" \
    ".Lr_cprA0_%=:\n" \
    "s_nop 0\n" \
    "v_pk_add_f32 v[80:81], v[80:81], v[82:83]\n" \
    "v_pk_add_f32 v[84:85], v[84:85], v[86:87]\n" \
    "ds_write2_b32 v5, v80, v81 offset0:0 offset1:4\n" \
    "ds_write2_b32 v5, v84, v85 offset0:64 offset1:68\n" \
    "s_waitcnt lgkmcnt(0)\n" \
    "s_barrier\n" \
    "ds_read_b128 v[96:99], v0 offset:0\n" \
    "ds_read_b128 v[100:103], v0 offset:1024\n" \
    "ds_read_b128 v[104:107], v0 offset:2048\n" \
    "ds_read_b128 v[108:111], v0 offset:3072\n" \
    "ds_read_b128 v[112:115], v0 offset:4096\n" \
    "ds_read_b128 v[116:119], v0 offset:5120\n" \
    "ds_read_b128 v[120:123], v0 offset:6144\n" \
    "ds_read_b128 v[124:127], v0 offset:7168\n" \
    "s_waitcnt lgkmcnt(6)\n" \
    "v_pk_add_f32 v[96:97], v[96:97], v[100:101]\n" \
    "v_pk_add_f32 v[98:99], v[98:99], v[102:103]\n" \
    "s_waitcnt lgkmcnt(4)\n" \
    "v_pk_add_f32 v[104:105], v[104:105], v[108:109]\n" \
    "v_pk_add_f32 v[106:107], v[106:107], v[110:111]\n" \
    "s_waitcnt lgkmcnt(2)\n" \
    "v_pk_add_f32 v[112:113], v[112:113], v[116:117]\n" \
    "v_pk_add_f32 v[114:115], v[114:115], v[118:119]\n" \
    "v_pk_add_f32 v[96:97], v[96:97], v[104:105]\n" \
    "v_pk_add_f32 v[98:99], v[98:99], v[106:107]\n" \
    "s_waitcnt lgkmcnt(0)\n" \
    "v_pk_add_f32 v[120:121], v[120:121], v[124:125]\n" \
    "v_pk_add_f32 v[122:123], v[122:123], v[126:127]\n" \
    "v_pk_add_f32 v[96:97], v[96:97], v[112:113]\n" \
    "v_pk_add_f32 v[98:99], v[98:99], v[114:115]\n" \
    "v_pk_add_f32 v[96:97], v[96:97], v[120:121]\n" \
    "v_pk_add_f32 v[98:99], v[98:99], v[122:123]\n" \
    "v_cvt_pk_bf16_f32 v96, v96, v97\n" \
    "v_cvt_pk_bf16_f32 v97, v98, v99\n" \
    "buffer_store_dwordx2 v[96:97], v1, s[8:11], s38 offen sc1\n" \
    "s_barrier\n" \
    "s_memrealtime s[62:63]\n" \
    "buffer_load_dwordx4 v[60:63], v0, s[8:11], s42 offen offset:0 sc1\n" \
    "buffer_load_dwordx4 v[64:67], v0, s[8:11], s42 offen offset:1024 sc1\n" \
    "buffer_load_dwordx4 v[68:71], v0, s[8:11], s42 offen offset:2048 sc1\n" \
    "buffer_load_dwordx4 v[72:75], v0, s[8:11], s42 offen offset:3072 sc1\n" \
    "s_barrier\n" \
    "buffer_load_dwordx4 v[224:227], v0, s[8:11], s39 offen offset:0 sc1\n" \
    "buffer_load_dwordx4 v[228:231], v0, s[8:11], s39 offen offset:1024 sc1\n" \
    "buffer_load_dwordx4 v[232:235], v0, s[8:11], s39 offen offset:2048 sc1\n" \
    "buffer_load_dwordx4 v[236:239], v0, s[8:11], s39 offen offset:3072 sc1\n" \
    "buffer_load_dwordx4 v[240:243], v0, s[8:11], s40 offen offset:0 sc1\n" \
    "buffer_load_dwordx4 v[244:247], v0, s[8:11], s40 offen offset:1024 sc1\n" \
    "buffer_load_dwordx4 v[248:251], v0, s[8:11], s40 offen offset:2048 sc1\n" \
    "buffer_load_dwordx4 v[252:255], v0, s[8:11], s40 offen offset:3072 sc1\n" \
    "s_and_b32 s33, s32, s23\n" \
    "s_mul_i32 s45, s33, s24\n" \
    "s_cmp_lt_u32 s33, 64-%[pdist]\n" \
    "s_cselect_b32 s45, s45, 0x80000000\n" \
    "s_add_u32 s44, s43, %[x1]\n" \
    "s_add_u32 s50, s50, 16384\n" \
    "s_xor_b32 s51, s51, 16384\n" \
    "s_waitcnt vmcnt(15)\n" \
    "v_max3_u32 v19, v44, v45, v46\n" \
    "s_waitcnt vmcnt(14)\n" \
    "v_max3_u32 v19, v19, v47, v48\n" \
    "v_max3_u32 v19, v19, v49, v50\n" \
    "v_max3_u32 v19, v19, v51, v52\n" \
    "v_max3_u32 v19, v19, v53, v54\n" \
    "s_waitcnt vmcnt(13)\n" \
    "v_max3_u32 v19, v19, v55, v56\n" \
    "v_max3_u32 v19, v19, v57, v58\n" \
    "s_waitcnt vmcnt(11)\n" \
    "v_max3_u32 v19, v19, v59, v60\n" \
    "v_max3_u32 v19, v19, v61, v62\n" \
    "s_waitcnt vmcnt(10)\n" \
    "v_max3_u32 v19, v19, v63, v64\n" \
    "v_max3_u32 v19, v19, v65, v66\n" \
    "s_waitcnt vmcnt(9)\n" \
    "v_max3_u32 v19, v19, v67, v68\n" \
    "v_max3_u32 v19, v19, v69, v70\n" \
    "s_waitcnt vmcnt(8)\n" \
    "v_max3_u32 v19, v19, v71, v72\n" \
    "v_max3_u32 v19, v19, v73, v74\n" \
    "v_max_u32 v19, v19, v75\n" \
    "v_cmp_eq_u32 vcc, -1, v19\n" \
    "s_nop 0\n" \
    "s_cbranch_vccnz .Lr_ctA0_%=\n" \
    ".Lr_ctrA0_%=:\n" \
    "v_mfma_f32_16x16x32_bf16 v[88:91], v[36:39], v[72:75], 0\n" \
    "v_mfma_f32_16x16x32_bf16 v[88:91], v[192:195], v[56:59], v[88:91]\n" \
    "v_mfma_f32_16x16x32_bf16 v[88:91], v[196:199], v[60:63], v[88:91]\n" \
    "v_mfma_f32_16x16x32_bf16 v[88:91], v[200:203], v[64:67], v[88:91]\n" \
    "v_mfma_f32_16x16x32_bf16 v[88:91], v[204:207], v[68:71], v[88:91]\n" \
    "v_mfma_f32_16x16x32_bf16 v[92:95], v[40:43], v[72:75], 0\n" \
    "v_mfma_f32_16x16x32_bf16 v[92:95], v[208:211], v[56:59], v[92:95]\n" \
    "v_mfma_f32_16x16x32_bf16 v[92:95], v[212:215], v[60:63], v[92:95]\n" \
    "v_mfma_f32_16x16x32_bf16 v[92:95], v[216:219], v[64:67], v[92:95]\n" \
    "v_mfma_f32_16x16x32_bf16 v[92:95], v[220:223], v[68:71], v[92:95]\n" \
    "v_mfma_f32_16x16x32_bf16 v[80:83], v[36:39], v[52:55], 0\n" \
    "v_mfma_f32_16x16x32_bf16 v[84:87], v[40:43], v[52:55], 0\n" \
    "v_mov_b32 v193, v194\n" \
    "v_mov_b32 v194, v196\n" \
    "v_mov_b32 v195, v198\n" \
    "v_mov_b32 v201, v202\n" \
    "v_mov_b32 v202, v204\n" \
    "v_mov_b32 v203, v206\n" \
    "v_mov_b32 v209, v210\n" \
    "v_mov_b32 v210, v212\n" \
    "v_mov_b32 v211, v214\n" \
    "v_mov_b32 v217, v218\n" \
    "v_mov_b32 v218, v220\n" \
    "v_mov_b32 v219, v222\n" \
    "ds_write_b128 v8, v[88:91] offset:2048\n" \
    "ds_write_b128 v8, v[92:95] offset:3072\n" \
    "v_permlane16_swap_b32 v192, v194\n" \
    "v_permlane16_swap_b32 v193, v195\n" \
    "v_permlane16_swap_b32 v200, v202\n" \
    "v_permlane16_swap_b32 v201, v203\n" \
    "v_permlane16_swap_b32 v208, v210\n" \
    "v_permlane16_swap_b32 v209, v211\n" \
    "v_permlane16_swap_b32 v216, v218\n" \
    "v_permlane16_swap_b32 v217, v219\n" \
    "v_mfma_f32_16x16x32_bf16 v[80:83], v[192:195], v[44:47], v[80:83]\n" \
    "v_mfma_f32_16x16x32_bf16 v[84:87], v[208:211], v[44:47], v[84:87]\n" \
    "v_mfma_f32_16x16x32_bf16 v[80:83], v[200:203], v[48:51], v[80:83]\n" \
    "v_mfma_f32_16x16x32_bf16 v[84:87], v[216:219], v[48:51], v[84:87]\n" \
    "buffer_load_dword v18, v2, s[8:11], s50 offen sc1\n" \
    "s_nop 5\n" \
    "ds_write_b128 v8, v[80:83] offset:0\n" \
    "ds_write_b128 v8, v[84:87] offset:1024\n" \
    "s_waitcnt lgkmcnt(0)\n" \
    "s_barrier\n" \
    "s_add_u32 s64, s62, %[dh]\n" \
    "s_cmp_eq_u32 s54, 0\n" \
    "s_cbranch_scc1 .Lr_cbA0_%=\n" \
    ".Lr_cbrA0_%=:\n" \
    "s_add_u32 s32, s32, 1\n" \
    "s_mov_b32 s68, s40\n" \
    "s_and_b32 s33, s32, 3\n" \
    "s_lshl_b32 s35, s33, 16\n" \
    "s_add_u32 s35, s35, s17\n" \
    "s_lshl_b32 s61, s33, 17\n" \
    "s_add_u32 s41, s61, s20\n" \
    "s_add_u32 s42, s41, 4096\n" \
    "s_add_u32 s36, s18, s46\n" \
    "s_add_u32 s37, s36, 16384\n" \
    "s_add_u32 s46, s46, 655360\n" \
    "s_cmp_eq_u32 s46, 1966080\n" \
    "s_cselect_b32 s46, 0, s46\n" \
    "s_add_u32 s49, s32, 1\n" \
    "s_and_b32 s33, s49, 3\n" \
    "s_lshl_b32 s38, s33, 15\n" \
    "s_and_b32 s33, s49, %[brn]-1\n" \
    "s_mul_i32 s39, s33, 2621440\n" \
    "s_add_u32 s39, s39, s19\n" \
    "s_lshr_b32 s33, s39, 8\n" \
    "s_xor_b32 s33, s33, s27\n" \
    "s_and_b32 s33, s33, 0x2000\n" \
    "s_or_b32 s39, s39, s33\n" \
    "s_mul_i32 s43, s32, 40960\n" \
    "s_add_u32 s43, s43, s21\n" \
    "s_memrealtime s[58:59]\n" \
    "s_cmp_eq_u32 s32, 63\n" \
    "s_cselect_b32 s38, 0x80000000, s38\n" \
    "s_waitcnt lgkmcnt(0)\n" \
    "s_mov_b32 s55, s58\n" \
    "s_branch .Lr_hA1b_%=\n" \
    ".Lr_hA1a_%=:\n" \
    "s_sleep 1\n" \
    ".Lr_hA1b_%=:\n" \
    "s_memrealtime vcc\n" \
    "s_waitcnt lgkmcnt(0)\n" \
    "s_sub_i32 vcc_lo, s64, vcc_lo\n" \
    "s_cmp_gt_i32 vcc_lo, 8\n" \
    "s_cbranch_scc1 .Lr_hA1a_%=\n" \
    "s_cmp_gt_i32 vcc_lo, 0\n" \
    "s_cbranch_scc1 .Lr_hA1b_%=\n" \
    "s_mov_b32 s52, 0\n" \
    ".Lr_tvA1_%=:\n" \
    "buffer_load_dwordx2 v[16:17], v3, s[8:11], s51 offen sc1\n" \
    "s_add_u32 s52, s52, 1\n" \
    "s_cmp_lt_u32 s52, s53\n" \
    "s_cselect_b32 s53, s53, 0\n" \
    "s_cselect_b64 s[66:67], s[56:57], 0\n" \
    "s_waitcnt vmcnt(0)\n" \
    "v_cmp_ne_u32 vcc, s49, v17\n" \
    "v_subrev_u32 v19, s55, v16\n" \
    "s_and_b64 vcc, vcc, s[66:67]\n" \
    "s_cbranch_scc1 .Lr_tvA1_%=\n" \
    "v_max_i32_dpp v19, v19, v19 quad_perm:[1,0,3,2] row_mask:0xf bank_mask:0xf bound_ctrl:1\n" \
    "s_nop 1\n" \
    "v_max_i32_dpp v19, v19, v19 quad_perm:[2,3,0,1] row_mask:0xf bank_mask:0xf bound_ctrl:1\n" \
    "s_nop 0\n" \
    "v_readlane_b32 s48, v19, 4\n" \
    "v_readlane_b32 s47, v19, 0\n" \
    "s_min_i32 s48, s48, 1000\n" \
    "s_min_i32 s47, s47, 1000\n" \
    "s_add_u32 s48, s48, s55\n" \
    "s_add_u32 s48, s48, s22\n" \
    ".Lr_goA1_%=:\n" \
    "s_branch .Lr_uA1b_%=\n" \
    ".Lr_uA1a_%=:\n" \
    "s_sleep 1\n" \
    ".Lr_uA1b_%=:\n" \
    "s_memrealtime vcc\n" \
    "s_waitcnt lgkmcnt(0)\n" \
    "s_sub_i32 vcc_lo, s48, vcc_lo\n" \
    "s_cmp_gt_i32 vcc_lo, 8\n" \
    "s_cbranch_scc1 .Lr_uA1a_%=\n" \
    "s_cmp_gt_i32 vcc_lo, 0\n" \
    "s_cbranch_scc1 .Lr_uA1b_%=\n" \
    "buffer_load_dwordx4 v[36:39], v0, s[8:11], s36 offen sc1\n" \
    "buffer_load_dwordx4 v[40:43], v0, s[8:11], s37 offen sc1\n" \
    "s_add_u32 s47, s47, s55\n" \
    "s_add_u32 s47, s47, %[dr]\n" \
    "v_cmp_eq_u32 vcc, 0, v18\n" \
    "s_nop 0\n" \
    "s_and_b64 vcc, vcc, s[56:57]\n" \
    "s_cselect_b32 s54, 0, 1\n" \
    "s_cmp_eq_u32 s32, 63\n" \
    "s_cselect_b32 s54, 2, s54\n" \
    "s_cmp_eq_u32 s54, 1\n" \
    "s_cselect_b32 s39, s39, 0x80000000\n" \
    "s_add_u32 s40, s39, 65536\n" \
    "s_branch .Lr_pA1b_%=\n" \
    ".Lr_pA1a_%=:\n" \
    "s_sleep 1\n" \
    ".Lr_pA1b_%=:\n" \
    "s_memrealtime vcc\n" \
    "s_waitcnt lgkmcnt(0)\n" \
    "s_sub_i32 vcc_lo, s47, vcc_lo\n" \
    "s_cmp_gt_i32 vcc_lo, 8\n" \
    "s_cbranch_scc1 .Lr_pA1a_%=\n" \
    "s_cmp_gt_i32 vcc_lo, 0\n" \
    "s_cbranch_scc1 .Lr_pA1b_%=\n" \
    "buffer_load_dwordx4 v[32:35], v4, s[8:11], s35 offen sc1\n" \
    "buffer_load_dwordx4 v[24:27], v0, s[8:11], s35 offen sc1\n" \
    "buffer_load_dwordx4 v[28:31], v0, s[8:11], s35 offen offset:1024 sc1\n" \
    "s_barrier\n" \
    "buffer_load_dwordx4 v[52:55], v0, s[8:11], s41 offen offset:2048 sc1\n" \
    "buffer_load_dwordx4 v[44:47], v0, s[8:11], s41 offen offset:0 sc1\n" \
    "buffer_load_dwordx4 v[48:51], v0, s[8:11], s41 offen offset:1024 sc1\n" \
    "buffer_load_dwordx4 v[56:59], v0, s[8:11], s41 offen offset:3072 sc1\n" \
    "s_waitcnt vmcnt(8)\n" \
    "v_max3_u32 v19, v36, v37, v38\n" \
    "s_waitcnt vmcnt(7)\n" \
    "v_max3_u32 v19, v19, v39, v40\n" \
    "v_max3_u32 v19, v19, v41, v42\n" \
    "v_max_u32 v19, v19, v43\n" \
    "s_waitcnt vmcnt(6)\n" \
    "v_mfma_f32_16x16x32_bf16 v[80:83], v[32:35], v[36:39], 0\n" \
    "v_max3_u32 v20, v32, v33, v34\n" \
    "v_mfma_f32_16x16x32_bf16 v[84:87], v[32:35], v[40:43], 0\n" \
    "v_max_u32 v20, v20, v35\n" \
    "s_waitcnt vmcnt(5)\n" \
    "v_smfmac_f32_16x16x64_bf16 v[80:83], v[24:27], v[224:231], v10\n" \
    "v_smfmac_f32_16x16x64_bf16 v[84:87], v[24:27], v[240:247], v10\n" \
    "v_max3_u32 v19, v19, v24, v25\n" \
    "v_max3_u32 v19, v19, v26, v27\n" \
    "s_waitcnt vmcnt(4)\n" \
    "v_smfmac_f32_16x16x64_bf16 v[80:83], v[28:31], v[232:239], v10\n" \
    "v_smfmac_f32_16x16x64_bf16 v[84:87], v[28:31], v[248:255], v10\n" \
    "v_max3_u32 v20, v20, v28, v29\n" \
    "v_max3_u32 v20, v20, v30, v31\n" \
    "v_max_u32 v19, v19, v20\n" \
    "v_cmp_eq_u32 vcc, -1, v19\n" \
    "s_nop 0\n" \
    "s_cbranch_vccnz .Lr_cpA1_%=\n" \
    ".Lr_cprA1_%=:\n" \
    "s_nop 0\n" \
    "v_pk_add_f32 v[80:81], v[80:81], v[82:83]\n" \
    "v_pk_add_f32 v[84:85], v[84:85], v[86:87]\n" \
    "ds_write2_b32 v5, v80, v81 offset0:0 offset1:4\n" \
    "ds_write2_b32 v5, v84, v85 offset0:64 offset1:68\n" \
    "s_waitcnt lgkmcnt(0)\n" \
    "s_barrier\n" \
    "ds_read_b128 v[96:99], v0 offset:0\n" \
    "ds_read_b128 v[100:103], v0 offset:1024\n" \
    "ds_read_b128 v[104:107], v0 offset:2048\n" \
    "ds_read_b128 v[108:111], v0 offset:3072\n" \
    "ds_read_b128 v[112:115], v0 offset:4096\n" \
    "ds_read_b128 v[116:119], v0 offset:5120\n" \
    "ds_read_b128 v[120:123], v0 offset:6144\n" \
    "ds_read_b128 v[124:127], v0 offset:7168\n" \
    "s_waitcnt lgkmcnt(6)\n" \
    "v_pk_add_f32 v[96:97], v[96:97], v[100:101]\n" \
    "v_pk_add_f32 v[98:99], v[98:99], v[102:103]\n" \
    "s_waitcnt lgkmcnt(4)\n" \
    "v_pk_add_f32 v[104:105], v[104:105], v[108:109]\n" \
    "v_pk_add_f32 v[106:107], v[106:107], v[110:111]\n" \
    "s_waitcnt lgkmcnt(2)\n" \
    "v_pk_add_f32 v[112:113], v[112:113], v[116:117]\n" \
    "v_pk_add_f32 v[114:115], v[114:115], v[118:119]\n" \
    "v_pk_add_f32 v[96:97], v[96:97], v[104:105]\n" \
    "v_pk_add_f32 v[98:99], v[98:99], v[106:107]\n" \
    "s_waitcnt lgkmcnt(0)\n" \
    "v_pk_add_f32 v[120:121], v[120:121], v[124:125]\n" \
    "v_pk_add_f32 v[122:123], v[122:123], v[126:127]\n" \
    "v_pk_add_f32 v[96:97], v[96:97], v[112:113]\n" \
    "v_pk_add_f32 v[98:99], v[98:99], v[114:115]\n" \
    "v_pk_add_f32 v[96:97], v[96:97], v[120:121]\n" \
    "v_pk_add_f32 v[98:99], v[98:99], v[122:123]\n" \
    "v_cvt_pk_bf16_f32 v96, v96, v97\n" \
    "v_cvt_pk_bf16_f32 v97, v98, v99\n" \
    "buffer_store_dwordx2 v[96:97], v1, s[8:11], s38 offen sc1\n" \
    "s_barrier\n" \
    "s_memrealtime s[62:63]\n" \
    "buffer_load_dwordx4 v[60:63], v0, s[8:11], s42 offen offset:0 sc1\n" \
    "buffer_load_dwordx4 v[64:67], v0, s[8:11], s42 offen offset:1024 sc1\n" \
    "buffer_load_dwordx4 v[68:71], v0, s[8:11], s42 offen offset:2048 sc1\n" \
    "buffer_load_dwordx4 v[72:75], v0, s[8:11], s42 offen offset:3072 sc1\n" \
    "s_barrier\n" \
    "buffer_load_dwordx4 v[192:195], v0, s[8:11], s39 offen offset:0 sc1\n" \
    "buffer_load_dwordx4 v[196:199], v0, s[8:11], s39 offen offset:1024 sc1\n" \
    "buffer_load_dwordx4 v[200:203], v0, s[8:11], s39 offen offset:2048 sc1\n" \
    "buffer_load_dwordx4 v[204:207], v0, s[8:11], s39 offen offset:3072 sc1\n" \
    "buffer_load_dwordx4 v[208:211], v0, s[8:11], s40 offen offset:0 sc1\n" \
    "buffer_load_dwordx4 v[212:215], v0, s[8:11], s40 offen offset:1024 sc1\n" \
    "buffer_load_dwordx4 v[216:219], v0, s[8:11], s40 offen offset:2048 sc1\n" \
    "buffer_load_dwordx4 v[220:223], v0, s[8:11], s40 offen offset:3072 sc1\n" \
    "s_and_b32 s33, s32, s23\n" \
    "s_mul_i32 s45, s33, s24\n" \
    "s_cmp_lt_u32 s33, 64-%[pdist]\n" \
    "s_cselect_b32 s45, s45, 0x80000000\n" \
    "s_add_u32 s44, s43, %[x1]\n" \
    "s_add_u32 s50, s50, 16384\n" \
    "s_xor_b32 s51, s51, 16384\n" \
    "s_waitcnt vmcnt(15)\n" \
    "v_max3_u32 v19, v44, v45, v46\n" \
    "s_waitcnt vmcnt(14)\n" \
    "v_max3_u32 v19, v19, v47, v48\n" \
    "v_max3_u32 v19, v19, v49, v50\n" \
    "v_max3_u32 v19, v19, v51, v52\n" \
    "v_max3_u32 v19, v19, v53, v54\n" \
    "s_waitcnt vmcnt(13)\n" \
    "v_max3_u32 v19, v19, v55, v56\n" \
    "v_max3_u32 v19, v19, v57, v58\n" \
    "s_waitcnt vmcnt(11)\n" \
    "v_max3_u32 v19, v19, v59, v60\n" \
    "v_max3_u32 v19, v19, v61, v62\n" \
    "s_waitcnt vmcnt(10)\n" \
    "v_max3_u32 v19, v19, v63, v64\n" \
    "v_max3_u32 v19, v19, v65, v66\n" \
    "s_waitcnt vmcnt(9)\n" \
    "v_max3_u32 v19, v19, v67, v68\n" \
    "v_max3_u32 v19, v19, v69, v70\n" \
    "s_waitcnt vmcnt(8)\n" \
    "v_max3_u32 v19, v19, v71, v72\n" \
    "v_max3_u32 v19, v19, v73, v74\n" \
    "v_max_u32 v19, v19, v75\n" \
    "v_cmp_eq_u32 vcc, -1, v19\n" \
    "s_nop 0\n" \
    "s_cbranch_vccnz .Lr_ctA1_%=\n" \
    ".Lr_ctrA1_%=:\n" \
    "v_mfma_f32_16x16x32_bf16 v[88:91], v[36:39], v[72:75], 0\n" \
    "v_mfma_f32_16x16x32_bf16 v[88:91], v[224:227], v[56:59], v[88:91]\n" \
    "v_mfma_f32_16x16x32_bf16 v[88:91], v[228:231], v[60:63], v[88:91]\n" \
    "v_mfma_f32_16x16x32_bf16 v[88:91], v[232:235], v[64:67], v[88:91]\n" \
    "v_mfma_f32_16x16x32_bf16 v[88:91], v[236:239], v[68:71], v[88:91]\n" \
    "v_mfma_f32_16x16x32_bf16 v[92:95], v[40:43], v[72:75], 0\n" \
    "v_mfma_f32_16x16x32_bf16 v[92:95], v[240:243], v[56:59], v[92:95]\n" \
    "v_mfma_f32_16x16x32_bf16 v[92:95], v[244:247], v[60:63], v[92:95]\n" \
    "v_mfma_f32_16x16x32_bf16 v[92:95], v[248:251], v[64:67], v[92:95]\n" \
    "v_mfma_f32_16x16x32_bf16 v[92:95], v[252:255], v[68:71], v[92:95]\n" \
    "v_mfma_f32_16x16x32_bf16 v[80:83], v[36:39], v[52:55], 0\n" \
    "v_mfma_f32_16x16x32_bf16 v[84:87], v[40:43], v[52:55], 0\n" \
    "v_mov_b32 v225, v226\n" \
    "v_mov_b32 v226, v228\n" \
    "v_mov_b32 v227, v230\n" \
    "v_mov_b32 v233, v234\n" \
    "v_mov_b32 v234, v236\n" \
    "v_mov_b32 v235, v238\n" \
    "v_mov_b32 v241, v242\n" \
    "v_mov_b32 v242, v244\n" \
    "v_mov_b32 v243, v246\n" \
    "v_mov_b32 v249, v250\n" \
    "v_mov_b32 v250, v252\n" \
    "v_mov_b32 v251, v254\n" \
    "ds_write_b128 v8, v[88:91] offset:2048\n" \
    "ds_write_b128 v8, v[92:95] offset:3072\n" \
    "v_permlane16_swap_b32 v224, v226\n" \
    "v_permlane16_swap_b32 v225, v227\n" \
    "v_permlane16_swap_b32 v232, v234\n" \
    "v_permlane16_swap_b32 v233, v235\n" \
    "v_permlane16_swap_b32 v240, v242\n" \
    "v_permlane16_swap_b32 v241, v243\n" \
    "v_permlane16_swap_b32 v248, v250\n" \
    "v_permlane16_swap_b32 v249, v251\n" \
    "v_mfma_f32_16x16x32_bf16 v[80:83], v[224:227], v[44:47], v[80:83]\n" \
    "v_mfma_f32_16x16x32_bf16 v[84:87], v[240:243], v[44:47], v[84:87]\n" \
    "v_mfma_f32_16x16x32_bf16 v[80:83], v[232:235], v[48:51], v[80:83]\n" \
    "v_mfma_f32_16x16x32_bf16 v[84:87], v[248:251], v[48:51], v[84:87]\n" \
    "buffer_load_dword v18, v2, s[8:11], s50 offen sc1\n" \
    "s_nop 5\n" \
    "ds_write_b128 v8, v[80:83] offset:0\n" \
    "ds_write_b128 v8, v[84:87] offset:1024\n" \
    "s_waitcnt lgkmcnt(0)\n" \
    "s_barrier\n" \
    "s_add_u32 s64, s62, %[dh]\n" \
    "s_cmp_eq_u32 s54, 0\n" \
    "s_cbranch_scc1 .Lr_cbA1_%=\n" \
    ".Lr_cbrA1_%=:\n" \
    "s_add_u32 s32, s32, 1\n" \
    "s_mov_b32 s68, s40\n" \
    "s_cmp_lt_u32 s32, 64\n" \
    "s_cbranch_scc1 .Lr_lA_%=\n" \
    "s_waitcnt vmcnt(0)\n" \
    "s_endpgm\n" \
    ".Lr_lB_%=:\n" \
    "s_and_b32 s33, s32, 3\n" \
    "s_lshl_b32 s35, s33, 16\n" \
    "s_add_u32 s35, s35, s17\n" \
    "s_lshl_b32 s61, s33, 17\n" \
    "s_add_u32 s41, s61, s20\n" \
    "s_add_u32 s42, s41, 4096\n" \
    "s_add_u32 s36, s18, s46\n" \
    "s_add_u32 s37, s36, 16384\n" \
    "s_add_u32 s46, s46, 655360\n" \
    "s_cmp_eq_u32 s46, 1966080\n" \
    "s_cselect_b32 s46, 0, s46\n" \
    "s_add_u32 s49, s32, 1\n" \
    "s_and_b32 s33, s49, 3\n" \
    "s_lshl_b32 s38, s33, 15\n" \
    "s_and_b32 s33, s49, %[brn]-1\n" \
    "s_mul_i32 s39, s33, 2621440\n" \
    "s_add_u32 s39, s39, s19\n" \
    "s_lshr_b32 s33, s39, 8\n" \
    "s_xor_b32 s33, s33, s27\n" \
    "s_and_b32 s33, s33, 0x2000\n" \
    "s_or_b32 s39, s39, s33\n" \
    "s_mul_i32 s43, s32, 40960\n" \
    "s_add_u32 s43, s43, s21\n" \
    "s_memrealtime s[58:59]\n" \
    "s_cmp_eq_u32 s32, 63\n" \
    "s_cselect_b32 s38, 0x80000000, s38\n" \
    "s_waitcnt lgkmcnt(0)\n" \
    "s_mov_b32 s55, s58\n" \
    "s_branch .Lr_hB0b_%=\n" \
    ".Lr_hB0a_%=:\n" \
    "s_sleep 1\n" \
    ".Lr_hB0b_%=:\n" \
    "s_memrealtime vcc\n" \
    "s_waitcnt lgkmcnt(0)\n" \
    "s_sub_i32 vcc_lo, s64, vcc_lo\n" \
    "s_cmp_gt_i32 vcc_lo, 8\n" \
    "s_cbranch_scc1 .Lr_hB0a_%=\n" \
    "s_cmp_gt_i32 vcc_lo, 0\n" \
    "s_cbranch_scc1 .Lr_hB0b_%=\n" \
    "s_mov_b32 s52, 0\n" \
    ".Lr_tvB0_%=:\n" \
    "buffer_load_dwordx2 v[16:17], v3, s[8:11], s51 offen sc1\n" \
    "s_add_u32 s52, s52, 1\n" \
    "s_cmp_lt_u32 s52, s53\n" \
    "s_cselect_b32 s53, s53, 0\n" \
    "s_cselect_b64 s[66:67], s[56:57], 0\n" \
    "s_waitcnt vmcnt(0)\n" \
    "v_cmp_ne_u32 vcc, s49, v17\n" \
    "v_subrev_u32 v19, s55, v16\n" \
    "s_and_b64 vcc, vcc, s[66:67]\n" \
    "s_cbranch_scc1 .Lr_tvB0_%=\n" \
    "v_max_i32_dpp v19, v19, v19 quad_perm:[1,0,3,2] row_mask:0xf bank_mask:0xf bound_ctrl:1\n" \
    "s_nop 1\n" \
    "v_max_i32_dpp v19, v19, v19 quad_perm:[2,3,0,1] row_mask:0xf bank_mask:0xf bound_ctrl:1\n" \
    "s_nop 0\n" \
    "v_readlane_b32 s48, v19, 4\n" \
    "v_readlane_b32 s47, v19, 0\n" \
    "s_min_i32 s48, s48, 1000\n" \
    "s_min_i32 s47, s47, 1000\n" \
    "s_add_u32 s48, s48, s55\n" \
    "s_add_u32 s48, s48, s22\n" \
    ".Lr_goB0_%=:\n" \
    "s_branch .Lr_uB0b_%=\n" \
    ".Lr_uB0a_%=:\n" \
    "s_sleep 1\n" \
    ".Lr_uB0b_%=:\n" \
    "s_memrealtime vcc\n" \
    "s_waitcnt lgkmcnt(0)\n" \
    "s_sub_i32 vcc_lo, s48, vcc_lo\n" \
    "s_cmp_gt_i32 vcc_lo, 8\n" \
    "s_cbranch_scc1 .Lr_uB0a_%=\n" \
    "s_cmp_gt_i32 vcc_lo, 0\n" \
    "s_cbranch_scc1 .Lr_uB0b_%=\n" \
    "buffer_load_dwordx4 v[36:39], v0, s[8:11], s36 offen sc1\n" \
    "buffer_load_dwordx4 v[40:43], v0, s[8:11], s37 offen sc1\n" \
    "s_add_u32 s47, s47, s55\n" \
    "s_add_u32 s47, s47, %[dr]\n" \
    "v_cmp_eq_u32 vcc, 0, v18\n" \
    "s_nop 0\n" \
    "s_and_b64 vcc, vcc, s[56:57]\n" \
    "s_cselect_b32 s54, 0, 1\n" \
    "s_cmp_eq_u32 s32, 63\n" \
    "s_cselect_b32 s54, 2, s54\n" \
    "s_cmp_eq_u32 s54, 1\n" \
    "s_cselect_b32 s39, s39, 0x80000000\n" \
    "s_add_u32 s40, s39, 65536\n" \
    "s_branch .Lr_pB0b_%=\n" \
    ".Lr_pB0a_%=:\n" \
    "s_sleep 1\n" \
    ".Lr_pB0b_%=:\n" \
    "s_memrealtime vcc\n" \
    "s_waitcnt lgkmcnt(0)\n" \
    "s_sub_i32 vcc_lo, s47, vcc_lo\n" \
    "s_cmp_gt_i32 vcc_lo, 8\n" \
    "s_cbranch_scc1 .Lr_pB0a_%=\n" \
    "s_cmp_gt_i32 vcc_lo, 0\n" \
    "s_cbranch_scc1 .Lr_pB0b_%=\n" \
    "buffer_load_dwordx4 v[32:35], v4, s[8:11], s35 offen sc1\n" \
    "buffer_load_dwordx4 v[24:27], v0, s[8:11], s35 offen sc1\n" \
    "buffer_load_dwordx4 v[28:31], v0, s[8:11], s35 offen offset:1024 sc1\n" \
    "s_barrier\n" \
    "buffer_load_dwordx4 v[52:55], v0, s[8:11], s41 offen offset:2048 sc1\n" \
    "buffer_load_dwordx4 v[44:47], v0, s[8:11], s41 offen offset:0 sc1\n" \
    "buffer_load_dwordx4 v[48:51], v0, s[8:11], s41 offen offset:1024 sc1\n" \
    "buffer_load_dwordx4 v[56:59], v0, s[8:11], s41 offen offset:3072 sc1\n" \
    "s_waitcnt vmcnt(8)\n" \
    "v_max3_u32 v19, v36, v37, v38\n" \
    "s_waitcnt vmcnt(7)\n" \
    "v_max3_u32 v19, v19, v39, v40\n" \
    "v_max3_u32 v19, v19, v41, v42\n" \
    "v_max_u32 v19, v19, v43\n" \
    "s_waitcnt vmcnt(6)\n" \
    "v_mfma_f32_16x16x32_bf16 v[80:83], v[32:35], v[36:39], 0\n" \
    "v_max3_u32 v20, v32, v33, v34\n" \
    "v_mfma_f32_16x16x32_bf16 v[84:87], v[32:35], v[40:43], 0\n" \
    "v_max_u32 v20, v20, v35\n" \
    "s_waitcnt vmcnt(5)\n" \
    "v_smfmac_f32_16x16x64_bf16 v[80:83], v[24:27], v[192:199], v10\n" \
    "v_smfmac_f32_16x16x64_bf16 v[84:87], v[24:27], v[208:215], v10\n" \
    "v_max3_u32 v19, v19, v24, v25\n" \
    "v_max3_u32 v19, v19, v26, v27\n" \
    "s_waitcnt vmcnt(4)\n" \
    "v_smfmac_f32_16x16x64_bf16 v[80:83], v[28:31], v[200:207], v10\n" \
    "v_smfmac_f32_16x16x64_bf16 v[84:87], v[28:31], v[216:223], v10\n" \
    "v_max3_u32 v20, v20, v28, v29\n" \
    "v_max3_u32 v20, v20, v30, v31\n" \
    "v_max_u32 v19, v19, v20\n" \
    "v_cmp_eq_u32 vcc, -1, v19\n" \
    "s_nop 0\n" \
    "s_cbranch_vccnz .Lr_cpB0_%=\n" \
    ".Lr_cprB0_%=:\n" \
    "s_nop 0\n" \
    "v_pk_add_f32 v[80:81], v[80:81], v[82:83]\n" \
    "v_pk_add_f32 v[84:85], v[84:85], v[86:87]\n" \
    "ds_write2_b32 v5, v80, v81 offset0:0 offset1:4\n" \
    "ds_write2_b32 v5, v84, v85 offset0:64 offset1:68\n" \
    "s_waitcnt lgkmcnt(0)\n" \
    "s_barrier\n" \
    "s_barrier\n" \
    "s_memrealtime s[62:63]\n" \
    "buffer_load_dwordx4 v[60:63], v0, s[8:11], s42 offen offset:0 sc1\n" \
    "buffer_load_dwordx4 v[64:67], v0, s[8:11], s42 offen offset:1024 sc1\n" \
    "buffer_load_dwordx4 v[68:71], v0, s[8:11], s42 offen offset:2048 sc1\n" \
    "buffer_load_dwordx4 v[72:75], v0, s[8:11], s42 offen offset:3072 sc1\n" \
    "s_barrier\n" \
    "s_add_u32 s33, s43, %[x0]\n" \
    "buffer_load_dwordx4 v[76:79], v6, s[8:11], s33 offen sc1\n" \
    "buffer_load_dwordx4 v[224:227], v0, s[8:11], s39 offen offset:0 sc1\n" \
    "buffer_load_dwordx4 v[228:231], v0, s[8:11], s39 offen offset:1024 sc1\n" \
    "buffer_load_dwordx4 v[232:235], v0, s[8:11], s39 offen offset:2048 sc1\n" \
    "buffer_load_dwordx4 v[236:239], v0, s[8:11], s39 offen offset:3072 sc1\n" \
    "buffer_load_dwordx4 v[240:243], v0, s[8:11], s40 offen offset:0 sc1\n" \
    "buffer_load_dwordx4 v[244:247], v0, s[8:11], s40 offen offset:1024 sc1\n" \
    "buffer_load_dwordx4 v[248:251], v0, s[8:11], s40 offen offset:2048 sc1\n" \
    "buffer_load_dwordx4 v[252:255], v0, s[8:11], s40 offen offset:3072 sc1\n" \
    "s_and_b32 s33, s32, s23\n" \
    "s_mul_i32 s45, s33, s24\n" \
    "s_cmp_lt_u32 s33, 64-%[pdist]\n" \
    "s_cselect_b32 s45, s45, 0x80000000\n" \
    "s_add_u32 s44, s43, %[x1]\n" \
    "s_add_u32 s50, s50, 16384\n" \
    "s_xor_b32 s51, s51, 16384\n" \
    "s_waitcnt vmcnt(15)\n" \
    "v_max3_u32 v19, v44, v45, v46\n" \
    "s_waitcnt vmcnt(14)\n" \
    "v_max3_u32 v19, v19, v47, v48\n" \
    "v_max3_u32 v19, v19, v49, v50\n" \
    "v_max3_u32 v19, v19, v51, v52\n" \
    "v_max3_u32 v19, v19, v53, v54\n" \
    "s_waitcnt vmcnt(13)\n" \
    "v_max3_u32 v19, v19, v55, v56\n" \
    "v_max3_u32 v19, v19, v57, v58\n" \
    "s_waitcnt vmcnt(12)\n" \
    "v_max3_u32 v19, v19, v59, v60\n" \
    "v_max3_u32 v19, v19, v61, v62\n" \
    "s_waitcnt vmcnt(11)\n" \
    "v_max3_u32 v19, v19, v63, v64\n" \
    "v_max3_u32 v19, v19, v65, v66\n" \
    "s_waitcnt vmcnt(10)\n" \
    "v_max3_u32 v19, v19, v67, v68\n" \
    "v_max3_u32 v19, v19, v69, v70\n" \
    "s_waitcnt vmcnt(9)\n" \
    "v_max3_u32 v19, v19, v71, v72\n" \
    "v_max3_u32 v19, v19, v73, v74\n" \
    "v_max_u32 v19, v19, v75\n" \
    "v_cmp_eq_u32 vcc, -1, v19\n" \
    "s_nop 0\n" \
    "s_cbranch_vccnz .Lr_ctB0_%=\n" \
    ".Lr_ctrB0_%=:\n" \
    "v_mfma_f32_16x16x32_bf16 v[88:91], v[36:39], v[72:75], 0\n" \
    "v_mfma_f32_16x16x32_bf16 v[88:91], v[192:195], v[56:59], v[88:91]\n" \
    "v_mfma_f32_16x16x32_bf16 v[88:91], v[196:199], v[60:63], v[88:91]\n" \
    "v_mfma_f32_16x16x32_bf16 v[88:91], v[200:203], v[64:67], v[88:91]\n" \
    "v_mfma_f32_16x16x32_bf16 v[88:91], v[204:207], v[68:71], v[88:91]\n" \
    "v_mfma_f32_16x16x32_bf16 v[92:95], v[40:43], v[72:75], 0\n" \
    "v_mfma_f32_16x16x32_bf16 v[92:95], v[208:211], v[56:59], v[92:95]\n" \
    "v_mfma_f32_16x16x32_bf16 v[92:95], v[212:215], v[60:63], v[92:95]\n" \
    "v_mfma_f32_16x16x32_bf16 v[92:95], v[216:219], v[64:67], v[92:95]\n" \
    "v_mfma_f32_16x16x32_bf16 v[92:95], v[220:223], v[68:71], v[92:95]\n" \
    "v_mfma_f32_16x16x32_bf16 v[80:83], v[36:39], v[52:55], 0\n" \
    "v_mfma_f32_16x16x32_bf16 v[84:87], v[40:43], v[52:55], 0\n" \
    "v_mov_b32 v193, v194\n" \
    "v_mov_b32 v194, v196\n" \
    "v_mov_b32 v195, v198\n" \
    "v_mov_b32 v201, v202\n" \
    "v_mov_b32 v202, v204\n" \
    "v_mov_b32 v203, v206\n" \
    "v_mov_b32 v209, v210\n" \
    "v_mov_b32 v210, v212\n" \
    "v_mov_b32 v211, v214\n" \
    "v_mov_b32 v217, v218\n" \
    "v_mov_b32 v218, v220\n" \
    "v_mov_b32 v219, v222\n" \
    "ds_write_b128 v8, v[88:91] offset:2048\n" \
    "ds_write_b128 v8, v[92:95] offset:3072\n" \
    "v_permlane16_swap_b32 v192, v194\n" \
    "v_permlane16_swap_b32 v193, v195\n" \
    "v_permlane16_swap_b32 v200, v202\n" \
    "v_permlane16_swap_b32 v201, v203\n" \
    "v_permlane16_swap_b32 v208, v210\n" \
    "v_permlane16_swap_b32 v209, v211\n" \
    "v_permlane16_swap_b32 v216, v218\n" \
    "v_permlane16_swap_b32 v217, v219\n" \
    "v_mfma_f32_16x16x32_bf16 v[80:83], v[192:195], v[44:47], v[80:83]\n" \
    "v_mfma_f32_16x16x32_bf16 v[84:87], v[208:211], v[44:47], v[84:87]\n" \
    "v_mfma_f32_16x16x32_bf16 v[80:83], v[200:203], v[48:51], v[80:83]\n" \
    "v_mfma_f32_16x16x32_bf16 v[84:87], v[216:219], v[48:51], v[84:87]\n" \
    "buffer_load_dword v18, v2, s[8:11], s50 offen sc1\n" \
    "s_nop 5\n" \
    "ds_write_b128 v8, v[80:83] offset:0\n" \
    "ds_write_b128 v8, v[84:87] offset:1024\n" \
    "s_waitcnt lgkmcnt(0)\n" \
    "s_barrier\n" \
    "s_add_u32 s64, s62, %[dh]\n" \
    "ds_read_b128 v[96:99], v7 offset:0\n" \
    "ds_read_b128 v[100:103], v7 offset:256\n" \
    "ds_read_b128 v[104:107], v7 offset:4096\n" \
    "ds_read_b128 v[108:111], v7 offset:4352\n" \
    "ds_read_b128 v[112:115], v7 offset:8192\n" \
    "ds_read_b128 v[116:119], v7 offset:8448\n" \
    "ds_read_b128 v[120:123], v7 offset:12288\n" \
    "ds_read_b128 v[124:127], v7 offset:12544\n" \
    "ds_read_b128 v[128:131], v7 offset:16384\n" \
    "ds_read_b128 v[132:135], v7 offset:16640\n" \
    "ds_read_b128 v[136:139], v7 offset:20480\n" \
    "ds_read_b128 v[140:143], v7 offset:20736\n" \
    "ds_read_b128 v[144:147], v7 offset:24576\n" \
    "ds_read_b128 v[148:151], v7 offset:24832\n" \
    "ds_read_b128 v[152:155], v7 offset:28672\n" \
    "ds_read_b128 v[156:159], v7 offset:28928\n" \
    "s_waitcnt lgkmcnt(13)\n" \
    "v_pk_add_f32 v[96:97], v[96:97], v[104:105]\n" \
    "v_pk_add_f32 v[98:99], v[98:99], v[106:107]\n" \
    "s_waitcnt lgkmcnt(12)\n" \
    "v_pk_add_f32 v[100:101], v[100:101], v[108:109]\n" \
    "v_pk_add_f32 v[102:103], v[102:103], v[110:111]\n" \
    "s_waitcnt lgkmcnt(9)\n" \
    "v_pk_add_f32 v[112:113], v[112:113], v[120:121]\n" \
    "v_pk_add_f32 v[114:115], v[114:115], v[122:123]\n" \
    "s_waitcnt lgkmcnt(8)\n" \
    "v_pk_add_f32 v[116:117], v[116:117], v[124:125]\n" \
    "v_pk_add_f32 v[118:119], v[118:119], v[126:127]\n" \
    "v_pk_add_f32 v[96:97], v[96:97], v[112:113]\n" \
    "v_pk_add_f32 v[98:99], v[98:99], v[114:115]\n" \
    "v_pk_add_f32 v[100:101], v[100:101], v[116:117]\n" \
    "v_pk_add_f32 v[102:103], v[102:103], v[118:119]\n" \
    "s_waitcnt lgkmcnt(5)\n" \
    "v_pk_add_f32 v[128:129], v[128:129], v[136:137]\n" \
    "v_pk_add_f32 v[130:131], v[130:131], v[138:139]\n" \
    "s_waitcnt lgkmcnt(4)\n" \
    "v_pk_add_f32 v[132:133], v[132:133], v[140:141]\n" \
    "v_pk_add_f32 v[134:135], v[134:135], v[142:143]\n" \
    "s_waitcnt lgkmcnt(1)\n" \
    "v_pk_add_f32 v[144:145], v[144:145], v[152:153]\n" \
    "v_pk_add_f32 v[146:147], v[146:147], v[154:155]\n" \
    "s_waitcnt lgkmcnt(0)\n" \
    "v_pk_add_f32 v[148:149], v[148:149], v[156:157]\n" \
    "v_pk_add_f32 v[150:151], v[150:151], v[158:159]\n" \
    "v_pk_add_f32 v[128:129], v[128:129], v[144:145]\n" \
    "v_pk_add_f32 v[130:131], v[130:131], v[146:147]\n" \
    "v_pk_add_f32 v[132:133], v[132:133], v[148:149]\n" \
    "v_pk_add_f32 v[134:135], v[134:135], v[150:151]\n" \
    "v_pk_add_f32 v[96:97], v[96:97], v[128:129]\n" \
    "v_pk_add_f32 v[98:99], v[98:99], v[130:131]\n" \
    "v_pk_add_f32 v[100:101], v[100:101], v[132:133]\n" \
    "v_pk_add_f32 v[102:103], v[102:103], v[134:135]\n" \
    "v_cvt_pk_bf16_f32 v104, v96, v97\n" \
    "v_cvt_pk_bf16_f32 v105, v98, v99\n" \
    "v_cvt_pk_bf16_f32 v106, v100, v101\n" \
    "v_cvt_pk_bf16_f32 v107, v102, v103\n" \
    "v_lshlrev_b32 v96, 16, v104\n" \
    "v_and_b32 v97, 0xffff0000, v104\n" \
    "s_waitcnt vmcnt(9)\n" \
    "v_lshlrev_b32 v112, 16, v76\n" \
    "v_and_b32 v113, 0xffff0000, v76\n" \
    "v_lshlrev_b32 v98, 16, v105\n" \
    "v_and_b32 v99, 0xffff0000, v105\n" \
    "v_lshlrev_b32 v114, 16, v77\n" \
    "v_and_b32 v115, 0xffff0000, v77\n" \
    "v_lshlrev_b32 v100, 16, v106\n" \
    "v_and_b32 v101, 0xffff0000, v106\n" \
    "v_lshlrev_b32 v116, 16, v78\n" \
    "v_and_b32 v117, 0xffff0000, v78\n" \
    "v_lshlrev_b32 v102, 16, v107\n" \
    "v_and_b32 v103, 0xffff0000, v107\n" \
    "v_lshlrev_b32 v118, 16, v79\n" \
    "v_and_b32 v119, 0xffff0000, v79\n" \
    "v_pk_add_f32 v[96:97], v[96:97], v[112:113]\n" \
    "v_pk_add_f32 v[98:99], v[98:99], v[114:115]\n" \
    "v_pk_add_f32 v[100:101], v[100:101], v[116:117]\n" \
    "v_pk_add_f32 v[102:103], v[102:103], v[118:119]\n" \
    "v_cvt_pk_bf16_f32 v76, v96, v97\n" \
    "v_cvt_pk_bf16_f32 v77, v98, v99\n" \
    "v_cvt_pk_bf16_f32 v78, v100, v101\n" \
    "v_cvt_pk_bf16_f32 v79, v102, v103\n" \
    "v_min_u32 v76, -2, v76\n" \
    "v_min_u32 v77, -2, v77\n" \
    "v_min_u32 v78, -2, v78\n" \
    "v_min_u32 v79, -2, v79\n" \
    "buffer_store_dwordx4 v[76:79], v6, s[8:11], s44 offen sc1\n" \
    "s_cmp_eq_u32 s54, 0\n" \
    "s_cbranch_scc1 .Lr_cbB0_%=\n" \
    ".Lr_cbrB0_%=:\n" \
    "s_add_u32 s32, s32, 1\n" \
    "s_mov_b32 s68, s40\n" \
    "s_and_b32 s33, s32, 3\n" \
    "s_lshl_b32 s35, s33, 16\n" \
    "s_add_u32 s35, s35, s17\n" \
    "s_lshl_b32 s61, s33, 17\n" \
    "s_add_u32 s41, s61, s20\n" \
    "s_add_u32 s42, s41, 4096\n" \
    "s_add_u32 s36, s18, s46\n" \
    "s_add_u32 s37, s36, 16384\n" \
    "s_add_u32 s46, s46, 655360\n" \
    "s_cmp_eq_u32 s46, 1966080\n" \
    "s_cselect_b32 s46, 0, s46\n" \
    "s_add_u32 s49, s32, 1\n" \
    "s_and_b32 s33, s49, 3\n" \
    "s_lshl_b32 s38, s33, 15\n" \
    "s_and_b32 s33, s49, %[brn]-1\n" \
    "s_mul_i32 s39, s33, 2621440\n" \
    "s_add_u32 s39, s39, s19\n" \
    "s_lshr_b32 s33, s39, 8\n" \
    "s_xor_b32 s33, s33, s27\n" \
    "s_and_b32 s33, s33, 0x2000\n" \
    "s_or_b32 s39, s39, s33\n" \
    "s_mul_i32 s43, s32, 40960\n" \
    "s_add_u32 s43, s43, s21\n" \
    "s_memrealtime s[58:59]\n" \
    "s_cmp_eq_u32 s32, 63\n" \
    "s_cselect_b32 s38, 0x80000000, s38\n" \
    "s_waitcnt lgkmcnt(0)\n" \
    "s_mov_b32 s55, s58\n" \
    "s_branch .Lr_hB1b_%=\n" \
    ".Lr_hB1a_%=:\n" \
    "s_sleep 1\n" \
    ".Lr_hB1b_%=:\n" \
    "s_memrealtime vcc\n" \
    "s_waitcnt lgkmcnt(0)\n" \
    "s_sub_i32 vcc_lo, s64, vcc_lo\n" \
    "s_cmp_gt_i32 vcc_lo, 8\n" \
    "s_cbranch_scc1 .Lr_hB1a_%=\n" \
    "s_cmp_gt_i32 vcc_lo, 0\n" \
    "s_cbranch_scc1 .Lr_hB1b_%=\n" \
    "s_mov_b32 s52, 0\n" \
    ".Lr_tvB1_%=:\n" \
    "buffer_load_dwordx2 v[16:17], v3, s[8:11], s51 offen sc1\n" \
    "s_add_u32 s52, s52, 1\n" \
    "s_cmp_lt_u32 s52, s53\n" \
    "s_cselect_b32 s53, s53, 0\n" \
    "s_cselect_b64 s[66:67], s[56:57], 0\n" \
    "s_waitcnt vmcnt(0)\n" \
    "v_cmp_ne_u32 vcc, s49, v17\n" \
    "v_subrev_u32 v19, s55, v16\n" \
    "s_and_b64 vcc, vcc, s[66:67]\n" \
    "s_cbranch_scc1 .Lr_tvB1_%=\n" \
    "v_max_i32_dpp v19, v19, v19 quad_perm:[1,0,3,2] row_mask:0xf bank_mask:0xf bound_ctrl:1\n" \
    "s_nop 1\n" \
    "v_max_i32_dpp v19, v19, v19 quad_perm:[2,3,0,1] row_mask:0xf bank_mask:0xf bound_ctrl:1\n" \
    "s_nop 0\n" \
    "v_readlane_b32 s48, v19, 4\n" \
    "v_readlane_b32 s47, v19, 0\n" \
    "s_min_i32 s48, s48, 1000\n" \
    "s_min_i32 s47, s47, 1000\n" \
    "s_add_u32 s48, s48, s55\n" \
    "s_add_u32 s48, s48, s22\n" \
    ".Lr_goB1_%=:\n" \
    "s_branch .Lr_uB1b_%=\n" \
    ".Lr_uB1a_%=:\n" \
    "s_sleep 1\n" \
    ".Lr_uB1b_%=:\n" \
    "s_memrealtime vcc\n" \
    "s_waitcnt lgkmcnt(0)\n" \
    "s_sub_i32 vcc_lo, s48, vcc_lo\n" \
    "s_cmp_gt_i32 vcc_lo, 8\n" \
    "s_cbranch_scc1 .Lr_uB1a_%=\n" \
    "s_cmp_gt_i32 vcc_lo, 0\n" \
    "s_cbranch_scc1 .Lr_uB1b_%=\n" \
    "buffer_load_dwordx4 v[36:39], v0, s[8:11], s36 offen sc1\n" \
    "buffer_load_dwordx4 v[40:43], v0, s[8:11], s37 offen sc1\n" \
    "s_add_u32 s47, s47, s55\n" \
    "s_add_u32 s47, s47, %[dr]\n" \
    "v_cmp_eq_u32 vcc, 0, v18\n" \
    "s_nop 0\n" \
    "s_and_b64 vcc, vcc, s[56:57]\n" \
    "s_cselect_b32 s54, 0, 1\n" \
    "s_cmp_eq_u32 s32, 63\n" \
    "s_cselect_b32 s54, 2, s54\n" \
    "s_cmp_eq_u32 s54, 1\n" \
    "s_cselect_b32 s39, s39, 0x80000000\n" \
    "s_add_u32 s40, s39, 65536\n" \
    "s_branch .Lr_pB1b_%=\n" \
    ".Lr_pB1a_%=:\n" \
    "s_sleep 1\n" \
    ".Lr_pB1b_%=:\n" \
    "s_memrealtime vcc\n" \
    "s_waitcnt lgkmcnt(0)\n" \
    "s_sub_i32 vcc_lo, s47, vcc_lo\n" \
    "s_cmp_gt_i32 vcc_lo, 8\n" \
    "s_cbranch_scc1 .Lr_pB1a_%=\n" \
    "s_cmp_gt_i32 vcc_lo, 0\n" \
    "s_cbranch_scc1 .Lr_pB1b_%=\n" \
    "buffer_load_dwordx4 v[32:35], v4, s[8:11], s35 offen sc1\n" \
    "buffer_load_dwordx4 v[24:27], v0, s[8:11], s35 offen sc1\n" \
    "buffer_load_dwordx4 v[28:31], v0, s[8:11], s35 offen offset:1024 sc1\n" \
    "s_barrier\n" \
    "buffer_load_dwordx4 v[52:55], v0, s[8:11], s41 offen offset:2048 sc1\n" \
    "buffer_load_dwordx4 v[44:47], v0, s[8:11], s41 offen offset:0 sc1\n" \
    "buffer_load_dwordx4 v[48:51], v0, s[8:11], s41 offen offset:1024 sc1\n" \
    "buffer_load_dwordx4 v[56:59], v0, s[8:11], s41 offen offset:3072 sc1\n" \
    "s_waitcnt vmcnt(8)\n" \
    "v_max3_u32 v19, v36, v37, v38\n" \
    "s_waitcnt vmcnt(7)\n" \
    "v_max3_u32 v19, v19, v39, v40\n" \
    "v_max3_u32 v19, v19, v41, v42\n" \
    "v_max_u32 v19, v19, v43\n" \
    "s_waitcnt vmcnt(6)\n" \
    "v_mfma_f32_16x16x32_bf16 v[80:83], v[32:35], v[36:39], 0\n" \
    "v_max3_u32 v20, v32, v33, v34\n" \
    "v_mfma_f32_16x16x32_bf16 v[84:87], v[32:35], v[40:43], 0\n" \
    "v_max_u32 v20, v20, v35\n" \
    "s_waitcnt vmcnt(5)\n" \
    "v_smfmac_f32_16x16x64_bf16 v[80:83], v[24:27], v[224:231], v10\n" \
    "v_smfmac_f32_16x16x64_bf16 v[84:87], v[24:27], v[240:247], v10\n" \
    "v_max3_u32 v19, v19, v24, v25\n" \
    "v_max3_u32 v19, v19, v26, v27\n" \
    "s_waitcnt vmcnt(4)\n" \
    "v_smfmac_f32_16x16x64_bf16 v[80:83], v[28:31], v[232:239], v10\n" \
    "v_smfmac_f32_16x16x64_bf16 v[84:87], v[28:31], v[248:255], v10\n" \
    "v_max3_u32 v20, v20, v28, v29\n" \
    "v_max3_u32 v20, v20, v30, v31\n" \
    "v_max_u32 v19, v19, v20\n" \
    "v_cmp_eq_u32 vcc, -1, v19\n" \
    "s_nop 0\n" \
    "s_cbranch_vccnz .Lr_cpB1_%=\n" \
    ".Lr_cprB1_%=:\n" \
    "s_nop 0\n" \
    "v_pk_add_f32 v[80:81], v[80:81], v[82:83]\n" \
    "v_pk_add_f32 v[84:85], v[84:85], v[86:87]\n" \
    "ds_write2_b32 v5, v80, v81 offset0:0 offset1:4\n" \
    "ds_write2_b32 v5, v84, v85 offset0:64 offset1:68\n" \
    "s_waitcnt lgkmcnt(0)\n" \
    "s_barrier\n" \
    "s_barrier\n" \
    "s_memrealtime s[62:63]\n" \
    "buffer_load_dwordx4 v[60:63], v0, s[8:11], s42 offen offset:0 sc1\n" \
    "buffer_load_dwordx4 v[64:67], v0, s[8:11], s42 offen offset:1024 sc1\n" \
    "buffer_load_dwordx4 v[68:71], v0, s[8:11], s42 offen offset:2048 sc1\n" \
    "buffer_load_dwordx4 v[72:75], v0, s[8:11], s42 offen offset:3072 sc1\n" \
    "s_barrier\n" \
    "s_add_u32 s33, s43, %[x0]\n" \
    "buffer_load_dwordx4 v[76:79], v6, s[8:11], s33 offen sc1\n" \
    "buffer_load_dwordx4 v[192:195], v0, s[8:11], s39 offen offset:0 sc1\n" \
    "buffer_load_dwordx4 v[196:199], v0, s[8:11], s39 offen offset:1024 sc1\n" \
    "buffer_load_dwordx4 v[200:203], v0, s[8:11], s39 offen offset:2048 sc1\n" \
    "buffer_load_dwordx4 v[204:207], v0, s[8:11], s39 offen offset:3072 sc1\n" \
    "buffer_load_dwordx4 v[208:211], v0, s[8:11], s40 offen offset:0 sc1\n" \
    "buffer_load_dwordx4 v[212:215], v0, s[8:11], s40 offen offset:1024 sc1\n" \
    "buffer_load_dwordx4 v[216:219], v0, s[8:11], s40 offen offset:2048 sc1\n" \
    "buffer_load_dwordx4 v[220:223], v0, s[8:11], s40 offen offset:3072 sc1\n" \
    "s_and_b32 s33, s32, s23\n" \
    "s_mul_i32 s45, s33, s24\n" \
    "s_cmp_lt_u32 s33, 64-%[pdist]\n" \
    "s_cselect_b32 s45, s45, 0x80000000\n" \
    "s_add_u32 s44, s43, %[x1]\n" \
    "s_add_u32 s50, s50, 16384\n" \
    "s_xor_b32 s51, s51, 16384\n" \
    "s_waitcnt vmcnt(15)\n" \
    "v_max3_u32 v19, v44, v45, v46\n" \
    "s_waitcnt vmcnt(14)\n" \
    "v_max3_u32 v19, v19, v47, v48\n" \
    "v_max3_u32 v19, v19, v49, v50\n" \
    "v_max3_u32 v19, v19, v51, v52\n" \
    "v_max3_u32 v19, v19, v53, v54\n" \
    "s_waitcnt vmcnt(13)\n" \
    "v_max3_u32 v19, v19, v55, v56\n" \
    "v_max3_u32 v19, v19, v57, v58\n" \
    "s_waitcnt vmcnt(12)\n" \
    "v_max3_u32 v19, v19, v59, v60\n" \
    "v_max3_u32 v19, v19, v61, v62\n" \
    "s_waitcnt vmcnt(11)\n" \
    "v_max3_u32 v19, v19, v63, v64\n" \
    "v_max3_u32 v19, v19, v65, v66\n" \
    "s_waitcnt vmcnt(10)\n" \
    "v_max3_u32 v19, v19, v67, v68\n" \
    "v_max3_u32 v19, v19, v69, v70\n" \
    "s_waitcnt vmcnt(9)\n" \
    "v_max3_u32 v19, v19, v71, v72\n" \
    "v_max3_u32 v19, v19, v73, v74\n" \
    "v_max_u32 v19, v19, v75\n" \
    "v_cmp_eq_u32 vcc, -1, v19\n" \
    "s_nop 0\n" \
    "s_cbranch_vccnz .Lr_ctB1_%=\n" \
    ".Lr_ctrB1_%=:\n" \
    "v_mfma_f32_16x16x32_bf16 v[88:91], v[36:39], v[72:75], 0\n" \
    "v_mfma_f32_16x16x32_bf16 v[88:91], v[224:227], v[56:59], v[88:91]\n" \
    "v_mfma_f32_16x16x32_bf16 v[88:91], v[228:231], v[60:63], v[88:91]\n" \
    "v_mfma_f32_16x16x32_bf16 v[88:91], v[232:235], v[64:67], v[88:91]\n" \
    "v_mfma_f32_16x16x32_bf16 v[88:91], v[236:239], v[68:71], v[88:91]\n" \
    "v_mfma_f32_16x16x32_bf16 v[92:95], v[40:43], v[72:75], 0\n" \
    "v_mfma_f32_16x16x32_bf16 v[92:95], v[240:243], v[56:59], v[92:95]\n" \
    "v_mfma_f32_16x16x32_bf16 v[92:95], v[244:247], v[60:63], v[92:95]\n" \
    "v_mfma_f32_16x16x32_bf16 v[92:95], v[248:251], v[64:67], v[92:95]\n" \
    "v_mfma_f32_16x16x32_bf16 v[92:95], v[252:255], v[68:71], v[92:95]\n" \
    "v_mfma_f32_16x16x32_bf16 v[80:83], v[36:39], v[52:55], 0\n" \
    "v_mfma_f32_16x16x32_bf16 v[84:87], v[40:43], v[52:55], 0\n" \
    "v_mov_b32 v225, v226\n" \
    "v_mov_b32 v226, v228\n" \
    "v_mov_b32 v227, v230\n" \
    "v_mov_b32 v233, v234\n" \
    "v_mov_b32 v234, v236\n" \
    "v_mov_b32 v235, v238\n" \
    "v_mov_b32 v241, v242\n" \
    "v_mov_b32 v242, v244\n" \
    "v_mov_b32 v243, v246\n" \
    "v_mov_b32 v249, v250\n" \
    "v_mov_b32 v250, v252\n" \
    "v_mov_b32 v251, v254\n" \
    "ds_write_b128 v8, v[88:91] offset:2048\n" \
    "ds_write_b128 v8, v[92:95] offset:3072\n" \
    "v_permlane16_swap_b32 v224, v226\n" \
    "v_permlane16_swap_b32 v225, v227\n" \
    "v_permlane16_swap_b32 v232, v234\n" \
    "v_permlane16_swap_b32 v233, v235\n" \
    "v_permlane16_swap_b32 v240, v242\n" \
    "v_permlane16_swap_b32 v241, v243\n" \
    "v_permlane16_swap_b32 v248, v250\n" \
    "v_permlane16_swap_b32 v249, v251\n" \
    "v_mfma_f32_16x16x32_bf16 v[80:83], v[224:227], v[44:47], v[80:83]\n" \
    "v_mfma_f32_16x16x32_bf16 v[84:87], v[240:243], v[44:47], v[84:87]\n" \
    "v_mfma_f32_16x16x32_bf16 v[80:83], v[232:235], v[48:51], v[80:83]\n" \
    "v_mfma_f32_16x16x32_bf16 v[84:87], v[248:251], v[48:51], v[84:87]\n" \
    "buffer_load_dword v18, v2, s[8:11], s50 offen sc1\n" \
    "s_nop 5\n" \
    "ds_write_b128 v8, v[80:83] offset:0\n" \
    "ds_write_b128 v8, v[84:87] offset:1024\n" \
    "s_waitcnt lgkmcnt(0)\n" \
    "s_barrier\n" \
    "s_add_u32 s64, s62, %[dh]\n" \
    "ds_read_b128 v[96:99], v7 offset:0\n" \
    "ds_read_b128 v[100:103], v7 offset:256\n" \
    "ds_read_b128 v[104:107], v7 offset:4096\n" \
    "ds_read_b128 v[108:111], v7 offset:4352\n" \
    "ds_read_b128 v[112:115], v7 offset:8192\n" \
    "ds_read_b128 v[116:119], v7 offset:8448\n" \
    "ds_read_b128 v[120:123], v7 offset:12288\n" \
    "ds_read_b128 v[124:127], v7 offset:12544\n" \
    "ds_read_b128 v[128:131], v7 offset:16384\n" \
    "ds_read_b128 v[132:135], v7 offset:16640\n" \
    "ds_read_b128 v[136:139], v7 offset:20480\n" \
    "ds_read_b128 v[140:143], v7 offset:20736\n" \
    "ds_read_b128 v[144:147], v7 offset:24576\n" \
    "ds_read_b128 v[148:151], v7 offset:24832\n" \
    "ds_read_b128 v[152:155], v7 offset:28672\n" \
    "ds_read_b128 v[156:159], v7 offset:28928\n" \
    "s_waitcnt lgkmcnt(13)\n" \
    "v_pk_add_f32 v[96:97], v[96:97], v[104:105]\n" \
    "v_pk_add_f32 v[98:99], v[98:99], v[106:107]\n" \
    "s_waitcnt lgkmcnt(12)\n" \
    "v_pk_add_f32 v[100:101], v[100:101], v[108:109]\n" \
    "v_pk_add_f32 v[102:103], v[102:103], v[110:111]\n" \
    "s_waitcnt lgkmcnt(9)\n" \
    "v_pk_add_f32 v[112:113], v[112:113], v[120:121]\n" \
    "v_pk_add_f32 v[114:115], v[114:115], v[122:123]\n" \
    "s_waitcnt lgkmcnt(8)\n" \
    "v_pk_add_f32 v[116:117], v[116:117], v[124:125]\n" \
    "v_pk_add_f32 v[118:119], v[118:119], v[126:127]\n" \
    "v_pk_add_f32 v[96:97], v[96:97], v[112:113]\n" \
    "v_pk_add_f32 v[98:99], v[98:99], v[114:115]\n" \
    "v_pk_add_f32 v[100:101], v[100:101], v[116:117]\n" \
    "v_pk_add_f32 v[102:103], v[102:103], v[118:119]\n" \
    "s_waitcnt lgkmcnt(5)\n" \
    "v_pk_add_f32 v[128:129], v[128:129], v[136:137]\n" \
    "v_pk_add_f32 v[130:131], v[130:131], v[138:139]\n" \
    "s_waitcnt lgkmcnt(4)\n" \
    "v_pk_add_f32 v[132:133], v[132:133], v[140:141]\n" \
    "v_pk_add_f32 v[134:135], v[134:135], v[142:143]\n" \
    "s_waitcnt lgkmcnt(1)\n" \
    "v_pk_add_f32 v[144:145], v[144:145], v[152:153]\n" \
    "v_pk_add_f32 v[146:147], v[146:147], v[154:155]\n" \
    "s_waitcnt lgkmcnt(0)\n" \
    "v_pk_add_f32 v[148:149], v[148:149], v[156:157]\n" \
    "v_pk_add_f32 v[150:151], v[150:151], v[158:159]\n" \
    "v_pk_add_f32 v[128:129], v[128:129], v[144:145]\n" \
    "v_pk_add_f32 v[130:131], v[130:131], v[146:147]\n" \
    "v_pk_add_f32 v[132:133], v[132:133], v[148:149]\n" \
    "v_pk_add_f32 v[134:135], v[134:135], v[150:151]\n" \
    "v_pk_add_f32 v[96:97], v[96:97], v[128:129]\n" \
    "v_pk_add_f32 v[98:99], v[98:99], v[130:131]\n" \
    "v_pk_add_f32 v[100:101], v[100:101], v[132:133]\n" \
    "v_pk_add_f32 v[102:103], v[102:103], v[134:135]\n" \
    "v_cvt_pk_bf16_f32 v104, v96, v97\n" \
    "v_cvt_pk_bf16_f32 v105, v98, v99\n" \
    "v_cvt_pk_bf16_f32 v106, v100, v101\n" \
    "v_cvt_pk_bf16_f32 v107, v102, v103\n" \
    "v_lshlrev_b32 v96, 16, v104\n" \
    "v_and_b32 v97, 0xffff0000, v104\n" \
    "s_waitcnt vmcnt(9)\n" \
    "v_lshlrev_b32 v112, 16, v76\n" \
    "v_and_b32 v113, 0xffff0000, v76\n" \
    "v_lshlrev_b32 v98, 16, v105\n" \
    "v_and_b32 v99, 0xffff0000, v105\n" \
    "v_lshlrev_b32 v114, 16, v77\n" \
    "v_and_b32 v115, 0xffff0000, v77\n" \
    "v_lshlrev_b32 v100, 16, v106\n" \
    "v_and_b32 v101, 0xffff0000, v106\n" \
    "v_lshlrev_b32 v116, 16, v78\n" \
    "v_and_b32 v117, 0xffff0000, v78\n" \
    "v_lshlrev_b32 v102, 16, v107\n" \
    "v_and_b32 v103, 0xffff0000, v107\n" \
    "v_lshlrev_b32 v118, 16, v79\n" \
    "v_and_b32 v119, 0xffff0000, v79\n" \
    "v_pk_add_f32 v[96:97], v[96:97], v[112:113]\n" \
    "v_pk_add_f32 v[98:99], v[98:99], v[114:115]\n" \
    "v_pk_add_f32 v[100:101], v[100:101], v[116:117]\n" \
    "v_pk_add_f32 v[102:103], v[102:103], v[118:119]\n" \
    "v_cvt_pk_bf16_f32 v76, v96, v97\n" \
    "v_cvt_pk_bf16_f32 v77, v98, v99\n" \
    "v_cvt_pk_bf16_f32 v78, v100, v101\n" \
    "v_cvt_pk_bf16_f32 v79, v102, v103\n" \
    "v_min_u32 v76, -2, v76\n" \
    "v_min_u32 v77, -2, v77\n" \
    "v_min_u32 v78, -2, v78\n" \
    "v_min_u32 v79, -2, v79\n" \
    "buffer_store_dwordx4 v[76:79], v6, s[8:11], s44 offen sc1\n" \
    "s_cmp_eq_u32 s54, 0\n" \
    "s_cbranch_scc1 .Lr_cbB1_%=\n" \
    ".Lr_cbrB1_%=:\n" \
    "s_add_u32 s32, s32, 1\n" \
    "s_mov_b32 s68, s40\n" \
    "s_cmp_lt_u32 s32, 64\n" \
    "s_cbranch_scc1 .Lr_lB_%=\n" \
    "s_waitcnt vmcnt(0)\n" \
    "s_endpgm\n" \
    ".Lr_lC_%=:\n" \
    "s_and_b32 s33, s32, 3\n" \
    "s_lshl_b32 s35, s33, 16\n" \
    "s_add_u32 s35, s35, s17\n" \
    "s_lshl_b32 s61, s33, 17\n" \
    "s_add_u32 s41, s61, s20\n" \
    "s_add_u32 s42, s41, 4096\n" \
    "s_add_u32 s36, s18, s46\n" \
    "s_add_u32 s37, s36, 16384\n" \
    "s_add_u32 s46, s46, 655360\n" \
    "s_cmp_eq_u32 s46, 1966080\n" \
    "s_cselect_b32 s46, 0, s46\n" \
    "s_add_u32 s49, s32, 1\n" \
    "s_and_b32 s33, s49, 3\n" \
    "s_lshl_b32 s38, s33, 15\n" \
    "s_and_b32 s33, s49, %[brn]-1\n" \
    "s_mul_i32 s39, s33, 2621440\n" \
    "s_add_u32 s39, s39, s19\n" \
    "s_lshr_b32 s33, s39, 8\n" \
    "s_xor_b32 s33, s33, s27\n" \
    "s_and_b32 s33, s33, 0x2000\n" \
    "s_or_b32 s39, s39, s33\n" \
    "s_mul_i32 s43, s32, 40960\n" \
    "s_add_u32 s43, s43, s21\n" \
    "s_memrealtime s[58:59]\n" \
    "s_cmp_eq_u32 s32, 63\n" \
    "s_cselect_b32 s38, 0x80000000, s38\n" \
    "s_waitcnt lgkmcnt(0)\n" \
    "s_mov_b32 s55, s58\n" \
    "s_branch .Lr_hC0b_%=\n" \
    ".Lr_hC0a_%=:\n" \
    "s_sleep 1\n" \
    ".Lr_hC0b_%=:\n" \
    "s_memrealtime vcc\n" \
    "s_waitcnt lgkmcnt(0)\n" \
    "s_sub_i32 vcc_lo, s64, vcc_lo\n" \
    "s_cmp_gt_i32 vcc_lo, 8\n" \
    "s_cbranch_scc1 .Lr_hC0a_%=\n" \
    "s_cmp_gt_i32 vcc_lo, 0\n" \
    "s_cbranch_scc1 .Lr_hC0b_%=\n" \
    "s_mov_b32 s52, 0\n" \
    ".Lr_tvC0_%=:\n" \
    "buffer_load_dwordx2 v[16:17], v3, s[8:11], s51 offen sc1\n" \
    "s_add_u32 s52, s52, 1\n" \
    "s_cmp_lt_u32 s52, s53\n" \
    "s_cselect_b32 s53, s53, 0\n" \
    "s_cselect_b64 s[66:67], s[56:57], 0\n" \
    "s_waitcnt vmcnt(0)\n" \
    "v_cmp_ne_u32 vcc, s49, v17\n" \
    "v_subrev_u32 v19, s55, v16\n" \
    "s_and_b64 vcc, vcc, s[66:67]\n" \
    "s_cbranch_scc1 .Lr_tvC0_%=\n" \
    "v_max_i32_dpp v19, v19, v19 quad_perm:[1,0,3,2] row_mask:0xf bank_mask:0xf bound_ctrl:1\n" \
    "s_nop 1\n" \
    "v_max_i32_dpp v19, v19, v19 quad_perm:[2,3,0,1] row_mask:0xf bank_mask:0xf bound_ctrl:1\n" \
    "s_nop 0\n" \
    "v_readlane_b32 s48, v19, 4\n" \
    "v_readlane_b32 s47, v19, 0\n" \
    "s_min_i32 s48, s48, 1000\n" \
    "s_min_i32 s47, s47, 1000\n" \
    "s_add_u32 s48, s48, s55\n" \
    "s_add_u32 s48, s48, s22\n" \
    ".Lr_goC0_%=:\n" \
    "s_branch .Lr_uC0b_%=\n" \
    ".Lr_uC0a_%=:\n" \
    "s_sleep 1\n" \
    ".Lr_uC0b_%=:\n" \
    "s_memrealtime vcc\n" \
    "s_waitcnt lgkmcnt(0)\n" \
    "s_sub_i32 vcc_lo, s48, vcc_lo\n" \
    "s_cmp_gt_i32 vcc_lo, 8\n" \
    "s_cbranch_scc1 .Lr_uC0a_%=\n" \
    "s_cmp_gt_i32 vcc_lo, 0\n" \
    "s_cbranch_scc1 .Lr_uC0b_%=\n" \
    "buffer_load_dwordx4 v[36:39], v0, s[8:11], s36 offen sc1\n" \
    "buffer_load_dwordx4 v[40:43], v0, s[8:11], s37 offen sc1\n" \
    "s_add_u32 s47, s47, s55\n" \
    "s_add_u32 s47, s47, %[dr]\n" \
    "v_cmp_eq_u32 vcc, 0, v18\n" \
    "s_nop 0\n" \
    "s_and_b64 vcc, vcc, s[56:57]\n" \
    "s_cselect_b32 s54, 0, 1\n" \
    "s_cmp_eq_u32 s32, 63\n" \
    "s_cselect_b32 s54, 2, s54\n" \
    "s_cmp_eq_u32 s54, 1\n" \
    "s_cselect_b32 s39, s39, 0x80000000\n" \
    "s_add_u32 s40, s39, 65536\n" \
    "s_branch .Lr_pC0b_%=\n" \
    ".Lr_pC0a_%=:\n" \
    "s_sleep 1\n" \
    ".Lr_pC0b_%=:\n" \
    "s_memrealtime vcc\n" \
    "s_waitcnt lgkmcnt(0)\n" \
    "s_sub_i32 vcc_lo, s47, vcc_lo\n" \
    "s_cmp_gt_i32 vcc_lo, 8\n" \
    "s_cbranch_scc1 .Lr_pC0a_%=\n" \
    "s_cmp_gt_i32 vcc_lo, 0\n" \
    "s_cbranch_scc1 .Lr_pC0b_%=\n" \
    "buffer_load_dwordx4 v[32:35], v4, s[8:11], s35 offen sc1\n" \
    "buffer_load_dwordx4 v[24:27], v0, s[8:11], s35 offen sc1\n" \
    "buffer_load_dwordx4 v[28:31], v0, s[8:11], s35 offen offset:1024 sc1\n" \
    "s_barrier\n" \
    "buffer_load_dwordx4 v[52:55], v0, s[8:11], s41 offen offset:2048 sc1\n" \
    "buffer_load_dwordx4 v[44:47], v0, s[8:11], s41 offen offset:0 sc1\n" \
    "buffer_load_dwordx4 v[48:51], v0, s[8:11], s41 offen offset:1024 sc1\n" \
    "buffer_load_dwordx4 v[56:59], v0, s[8:11], s41 offen offset:3072 sc1\n" \
    "s_waitcnt vmcnt(8)\n" \
    "v_max3_u32 v19, v36, v37, v38\n" \
    "s_waitcnt vmcnt(7)\n" \
    "v_max3_u32 v19, v19, v39, v40\n" \
    "v_max3_u32 v19, v19, v41, v42\n" \
    "v_max_u32 v19, v19, v43\n" \
    "s_waitcnt vmcnt(6)\n" \
    "v_mfma_f32_16x16x32_bf16 v[80:83], v[32:35], v[36:39], 0\n" \
    "v_max3_u32 v20, v32, v33, v34\n" \
    "v_mfma_f32_16x16x32_bf16 v[84:87], v[32:35], v[40:43], 0\n" \
    "v_max_u32 v20, v20, v35\n" \
    "s_waitcnt vmcnt(5)\n" \
    "v_smfmac_f32_16x16x64_bf16 v[80:83], v[24:27], v[192:199], v10\n" \
    "v_smfmac_f32_16x16x64_bf16 v[84:87], v[24:27], v[208:215], v10\n" \
    "v_max3_u32 v19, v19, v24, v25\n" \
    "v_max3_u32 v19, v19, v26, v27\n" \
    "s_waitcnt vmcnt(4)\n" \
    "v_smfmac_f32_16x16x64_bf16 v[80:83], v[28:31], v[200:207], v10\n" \
    "v_smfmac_f32_16x16x64_bf16 v[84:87], v[28:31], v[216:223], v10\n" \
    "v_max3_u32 v20, v20, v28, v29\n" \
    "v_max3_u32 v20, v20, v30, v31\n" \
    "v_max_u32 v19, v19, v20\n" \
    "v_cmp_eq_u32 vcc, -1, v19\n" \
    "s_nop 0\n" \
    "s_cbranch_vccnz .Lr_cpC0_%=\n" \
    ".Lr_cprC0_%=:\n" \
    "s_nop 0\n" \
    "v_pk_add_f32 v[80:81], v[80:81], v[82:83]\n" \
    "v_pk_add_f32 v[84:85], v[84:85], v[86:87]\n" \
    "ds_write2_b32 v5, v80, v81 offset0:0 offset1:4\n" \
    "ds_write2_b32 v5, v84, v85 offset0:64 offset1:68\n" \
    "s_waitcnt lgkmcnt(0)\n" \
    "s_barrier\n" \
    "s_barrier\n" \
    "s_memrealtime s[62:63]\n" \
    "buffer_load_dwordx4 v[60:63], v0, s[8:11], s42 offen offset:0 sc1\n" \
    "buffer_load_dwordx4 v[64:67], v0, s[8:11], s42 offen offset:1024 sc1\n" \
    "buffer_load_dwordx4 v[68:71], v0, s[8:11], s42 offen offset:2048 sc1\n" \
    "buffer_load_dwordx4 v[72:75], v0, s[8:11], s42 offen offset:3072 sc1\n" \
    "s_barrier\n" \
    "buffer_store_dwordx4 v[160:163], v9, s[8:11], s45 offen sc1\n" \
    "buffer_load_dwordx4 v[224:227], v0, s[8:11], s39 offen offset:0 sc1\n" \
    "buffer_load_dwordx4 v[228:231], v0, s[8:11], s39 offen offset:1024 sc1\n" \
    "buffer_load_dwordx4 v[232:235], v0, s[8:11], s39 offen offset:2048 sc1\n" \
    "buffer_load_dwordx4 v[236:239], v0, s[8:11], s39 offen offset:3072 sc1\n" \
    "buffer_load_dwordx4 v[240:243], v0, s[8:11], s40 offen offset:0 sc1\n" \
    "buffer_load_dwordx4 v[244:247], v0, s[8:11], s40 offen offset:1024 sc1\n" \
    "buffer_load_dwordx4 v[248:251], v0, s[8:11], s40 offen offset:2048 sc1\n" \
    "buffer_load_dwordx4 v[252:255], v0, s[8:11], s40 offen offset:3072 sc1\n" \
    "s_and_b32 s33, s32, s23\n" \
    "s_mul_i32 s45, s33, s24\n" \
    "s_cmp_lt_u32 s33, 64-%[pdist]\n" \
    "s_cselect_b32 s45, s45, 0x80000000\n" \
    "s_add_u32 s44, s43, %[x1]\n" \
    "s_add_u32 s50, s50, 16384\n" \
    "s_xor_b32 s51, s51, 16384\n" \
    "s_waitcnt vmcnt(15)\n" \
    "v_max3_u32 v19, v44, v45, v46\n" \
    "s_waitcnt vmcnt(14)\n" \
    "v_max3_u32 v19, v19, v47, v48\n" \
    "v_max3_u32 v19, v19, v49, v50\n" \
    "v_max3_u32 v19, v19, v51, v52\n" \
    "v_max3_u32 v19, v19, v53, v54\n" \
    "s_waitcnt vmcnt(13)\n" \
    "v_max3_u32 v19, v19, v55, v56\n" \
    "v_max3_u32 v19, v19, v57, v58\n" \
    "s_waitcnt vmcnt(12)\n" \
    "v_max3_u32 v19, v19, v59, v60\n" \
    "v_max3_u32 v19, v19, v61, v62\n" \
    "s_waitcnt vmcnt(11)\n" \
    "v_max3_u32 v19, v19, v63, v64\n" \
    "v_max3_u32 v19, v19, v65, v66\n" \
    "s_waitcnt vmcnt(10)\n" \
    "v_max3_u32 v19, v19, v67, v68\n" \
    "v_max3_u32 v19, v19, v69, v70\n" \
    "s_waitcnt vmcnt(9)\n" \
    "v_max3_u32 v19, v19, v71, v72\n" \
    "v_max3_u32 v19, v19, v73, v74\n" \
    "v_max_u32 v19, v19, v75\n" \
    "v_cmp_eq_u32 vcc, -1, v19\n" \
    "s_nop 0\n" \
    "s_cbranch_vccnz .Lr_ctC0_%=\n" \
    ".Lr_ctrC0_%=:\n" \
    "v_mfma_f32_16x16x32_bf16 v[88:91], v[36:39], v[72:75], 0\n" \
    "v_mfma_f32_16x16x32_bf16 v[88:91], v[192:195], v[56:59], v[88:91]\n" \
    "v_mfma_f32_16x16x32_bf16 v[88:91], v[196:199], v[60:63], v[88:91]\n" \
    "v_mfma_f32_16x16x32_bf16 v[88:91], v[200:203], v[64:67], v[88:91]\n" \
    "v_mfma_f32_16x16x32_bf16 v[88:91], v[204:207], v[68:71], v[88:91]\n" \
    "v_mfma_f32_16x16x32_bf16 v[92:95], v[40:43], v[72:75], 0\n" \
    "v_mfma_f32_16x16x32_bf16 v[92:95], v[208:211], v[56:59], v[92:95]\n" \
    "v_mfma_f32_16x16x32_bf16 v[92:95], v[212:215], v[60:63], v[92:95]\n" \
    "v_mfma_f32_16x16x32_bf16 v[92:95], v[216:219], v[64:67], v[92:95]\n" \
    "v_mfma_f32_16x16x32_bf16 v[92:95], v[220:223], v[68:71], v[92:95]\n" \
    "v_mfma_f32_16x16x32_bf16 v[80:83], v[36:39], v[52:55], 0\n" \
    "v_mfma_f32_16x16x32_bf16 v[84:87], v[40:43], v[52:55], 0\n" \
    "v_mov_b32 v193, v194\n" \
    "v_mov_b32 v194, v196\n" \
    "v_mov_b32 v195, v198\n" \
    "v_mov_b32 v201, v202\n" \
    "v_mov_b32 v202, v204\n" \
    "v_mov_b32 v203, v206\n" \
    "v_mov_b32 v209, v210\n" \
    "v_mov_b32 v210, v212\n" \
    "v_mov_b32 v211, v214\n" \
    "v_mov_b32 v217, v218\n" \
    "v_mov_b32 v218, v220\n" \
    "v_mov_b32 v219, v222\n" \
    "ds_write_b128 v8, v[88:91] offset:2048\n" \
    "ds_write_b128 v8, v[92:95] offset:3072\n" \
    "v_permlane16_swap_b32 v192, v194\n" \
    "v_permlane16_swap_b32 v193, v195\n" \
    "v_permlane16_swap_b32 v200, v202\n" \
    "v_permlane16_swap_b32 v201, v203\n" \
    "v_permlane16_swap_b32 v208, v210\n" \
    "v_permlane16_swap_b32 v209, v211\n" \
    "v_permlane16_swap_b32 v216, v218\n" \
    "v_permlane16_swap_b32 v217, v219\n" \
    "v_mfma_f32_16x16x32_bf16 v[80:83], v[192:195], v[44:47], v[80:83]\n" \
    "v_mfma_f32_16x16x32_bf16 v[84:87], v[208:211], v[44:47], v[84:87]\n" \
    "v_mfma_f32_16x16x32_bf16 v[80:83], v[200:203], v[48:51], v[80:83]\n" \
    "v_mfma_f32_16x16x32_bf16 v[84:87], v[216:219], v[48:51], v[84:87]\n" \
    "buffer_load_dword v18, v2, s[8:11], s50 offen sc1\n" \
    "s_nop 5\n" \
    "ds_write_b128 v8, v[80:83] offset:0\n" \
    "ds_write_b128 v8, v[84:87] offset:1024\n" \
    "s_waitcnt lgkmcnt(0)\n" \
    "s_barrier\n" \
    "s_add_u32 s64, s62, %[dh]\n" \
    "s_cmp_eq_u32 s54, 0\n" \
    "s_cbranch_scc1 .Lr_cbC0_%=\n" \
    ".Lr_cbrC0_%=:\n" \
    "s_add_u32 s32, s32, 1\n" \
    "s_mov_b32 s68, s40\n" \
    "s_and_b32 s33, s32, 3\n" \
    "s_lshl_b32 s35, s33, 16\n" \
    "s_add_u32 s35, s35, s17\n" \
    "s_lshl_b32 s61, s33, 17\n" \
    "s_add_u32 s41, s61, s20\n" \
    "s_add_u32 s42, s41, 4096\n" \
    "s_add_u32 s36, s18, s46\n" \
    "s_add_u32 s37, s36, 16384\n" \
    "s_add_u32 s46, s46, 655360\n" \
    "s_cmp_eq_u32 s46, 1966080\n" \
    "s_cselect_b32 s46, 0, s46\n" \
    "s_add_u32 s49, s32, 1\n" \
    "s_and_b32 s33, s49, 3\n" \
    "s_lshl_b32 s38, s33, 15\n" \
    "s_and_b32 s33, s49, %[brn]-1\n" \
    "s_mul_i32 s39, s33, 2621440\n" \
    "s_add_u32 s39, s39, s19\n" \
    "s_lshr_b32 s33, s39, 8\n" \
    "s_xor_b32 s33, s33, s27\n" \
    "s_and_b32 s33, s33, 0x2000\n" \
    "s_or_b32 s39, s39, s33\n" \
    "s_mul_i32 s43, s32, 40960\n" \
    "s_add_u32 s43, s43, s21\n" \
    "s_memrealtime s[58:59]\n" \
    "s_cmp_eq_u32 s32, 63\n" \
    "s_cselect_b32 s38, 0x80000000, s38\n" \
    "s_waitcnt lgkmcnt(0)\n" \
    "s_mov_b32 s55, s58\n" \
    "s_branch .Lr_hC1b_%=\n" \
    ".Lr_hC1a_%=:\n" \
    "s_sleep 1\n" \
    ".Lr_hC1b_%=:\n" \
    "s_memrealtime vcc\n" \
    "s_waitcnt lgkmcnt(0)\n" \
    "s_sub_i32 vcc_lo, s64, vcc_lo\n" \
    "s_cmp_gt_i32 vcc_lo, 8\n" \
    "s_cbranch_scc1 .Lr_hC1a_%=\n" \
    "s_cmp_gt_i32 vcc_lo, 0\n" \
    "s_cbranch_scc1 .Lr_hC1b_%=\n" \
    "s_mov_b32 s52, 0\n" \
    ".Lr_tvC1_%=:\n" \
    "buffer_load_dwordx2 v[16:17], v3, s[8:11], s51 offen sc1\n" \
    "s_add_u32 s52, s52, 1\n" \
    "s_cmp_lt_u32 s52, s53\n" \
    "s_cselect_b32 s53, s53, 0\n" \
    "s_cselect_b64 s[66:67], s[56:57], 0\n" \
    "s_waitcnt vmcnt(0)\n" \
    "v_cmp_ne_u32 vcc, s49, v17\n" \
    "v_subrev_u32 v19, s55, v16\n" \
    "s_and_b64 vcc, vcc, s[66:67]\n" \
    "s_cbranch_scc1 .Lr_tvC1_%=\n" \
    "v_max_i32_dpp v19, v19, v19 quad_perm:[1,0,3,2] row_mask:0xf bank_mask:0xf bound_ctrl:1\n" \
    "s_nop 1\n" \
    "v_max_i32_dpp v19, v19, v19 quad_perm:[2,3,0,1] row_mask:0xf bank_mask:0xf bound_ctrl:1\n" \
    "s_nop 0\n" \
    "v_readlane_b32 s48, v19, 4\n" \
    "v_readlane_b32 s47, v19, 0\n" \
    "s_min_i32 s48, s48, 1000\n" \
    "s_min_i32 s47, s47, 1000\n" \
    "s_add_u32 s48, s48, s55\n" \
    "s_add_u32 s48, s48, s22\n" \
    ".Lr_goC1_%=:\n" \
    "s_branch .Lr_uC1b_%=\n" \
    ".Lr_uC1a_%=:\n" \
    "s_sleep 1\n" \
    ".Lr_uC1b_%=:\n" \
    "s_memrealtime vcc\n" \
    "s_waitcnt lgkmcnt(0)\n" \
    "s_sub_i32 vcc_lo, s48, vcc_lo\n" \
    "s_cmp_gt_i32 vcc_lo, 8\n" \
    "s_cbranch_scc1 .Lr_uC1a_%=\n" \
    "s_cmp_gt_i32 vcc_lo, 0\n" \
    "s_cbranch_scc1 .Lr_uC1b_%=\n" \
    "buffer_load_dwordx4 v[36:39], v0, s[8:11], s36 offen sc1\n" \
    "buffer_load_dwordx4 v[40:43], v0, s[8:11], s37 offen sc1\n" \
    "s_add_u32 s47, s47, s55\n" \
    "s_add_u32 s47, s47, %[dr]\n" \
    "v_cmp_eq_u32 vcc, 0, v18\n" \
    "s_nop 0\n" \
    "s_and_b64 vcc, vcc, s[56:57]\n" \
    "s_cselect_b32 s54, 0, 1\n" \
    "s_cmp_eq_u32 s32, 63\n" \
    "s_cselect_b32 s54, 2, s54\n" \
    "s_cmp_eq_u32 s54, 1\n" \
    "s_cselect_b32 s39, s39, 0x80000000\n" \
    "s_add_u32 s40, s39, 65536\n" \
    "s_branch .Lr_pC1b_%=\n" \
    ".Lr_pC1a_%=:\n" \
    "s_sleep 1\n" \
    ".Lr_pC1b_%=:\n" \
    "s_memrealtime vcc\n" \
    "s_waitcnt lgkmcnt(0)\n" \
    "s_sub_i32 vcc_lo, s47, vcc_lo\n" \
    "s_cmp_gt_i32 vcc_lo, 8\n" \
    "s_cbranch_scc1 .Lr_pC1a_%=\n" \
    "s_cmp_gt_i32 vcc_lo, 0\n" \
    "s_cbranch_scc1 .Lr_pC1b_%=\n" \
    "buffer_load_dwordx4 v[32:35], v4, s[8:11], s35 offen sc1\n" \
    "buffer_load_dwordx4 v[24:27], v0, s[8:11], s35 offen sc1\n" \
    "buffer_load_dwordx4 v[28:31], v0, s[8:11], s35 offen offset:1024 sc1\n" \
    "s_barrier\n" \
    "buffer_load_dwordx4 v[52:55], v0, s[8:11], s41 offen offset:2048 sc1\n" \
    "buffer_load_dwordx4 v[44:47], v0, s[8:11], s41 offen offset:0 sc1\n" \
    "buffer_load_dwordx4 v[48:51], v0, s[8:11], s41 offen offset:1024 sc1\n" \
    "buffer_load_dwordx4 v[56:59], v0, s[8:11], s41 offen offset:3072 sc1\n" \
    "s_waitcnt vmcnt(8)\n" \
    "v_max3_u32 v19, v36, v37, v38\n" \
    "s_waitcnt vmcnt(7)\n" \
    "v_max3_u32 v19, v19, v39, v40\n" \
    "v_max3_u32 v19, v19, v41, v42\n" \
    "v_max_u32 v19, v19, v43\n" \
    "s_waitcnt vmcnt(6)\n" \
    "v_mfma_f32_16x16x32_bf16 v[80:83], v[32:35], v[36:39], 0\n" \
    "v_max3_u32 v20, v32, v33, v34\n" \
    "v_mfma_f32_16x16x32_bf16 v[84:87], v[32:35], v[40:43], 0\n" \
    "v_max_u32 v20, v20, v35\n" \
    "s_waitcnt vmcnt(5)\n" \
    "v_smfmac_f32_16x16x64_bf16 v[80:83], v[24:27], v[224:231], v10\n" \
    "v_smfmac_f32_16x16x64_bf16 v[84:87], v[24:27], v[240:247], v10\n" \
    "v_max3_u32 v19, v19, v24, v25\n" \
    "v_max3_u32 v19, v19, v26, v27\n" \
    "s_waitcnt vmcnt(4)\n" \
    "v_smfmac_f32_16x16x64_bf16 v[80:83], v[28:31], v[232:239], v10\n" \
    "v_smfmac_f32_16x16x64_bf16 v[84:87], v[28:31], v[248:255], v10\n" \
    "v_max3_u32 v20, v20, v28, v29\n" \
    "v_max3_u32 v20, v20, v30, v31\n" \
    "v_max_u32 v19, v19, v20\n" \
    "v_cmp_eq_u32 vcc, -1, v19\n" \
    "s_nop 0\n" \
    "s_cbranch_vccnz .Lr_cpC1_%=\n" \
    ".Lr_cprC1_%=:\n" \
    "s_nop 0\n" \
    "v_pk_add_f32 v[80:81], v[80:81], v[82:83]\n" \
    "v_pk_add_f32 v[84:85], v[84:85], v[86:87]\n" \
    "ds_write2_b32 v5, v80, v81 offset0:0 offset1:4\n" \
    "ds_write2_b32 v5, v84, v85 offset0:64 offset1:68\n" \
    "s_waitcnt lgkmcnt(0)\n" \
    "s_barrier\n" \
    "s_barrier\n" \
    "s_memrealtime s[62:63]\n" \
    "buffer_load_dwordx4 v[60:63], v0, s[8:11], s42 offen offset:0 sc1\n" \
    "buffer_load_dwordx4 v[64:67], v0, s[8:11], s42 offen offset:1024 sc1\n" \
    "buffer_load_dwordx4 v[68:71], v0, s[8:11], s42 offen offset:2048 sc1\n" \
    "buffer_load_dwordx4 v[72:75], v0, s[8:11], s42 offen offset:3072 sc1\n" \
    "s_barrier\n" \
    "buffer_store_dwordx4 v[160:163], v9, s[8:11], s45 offen sc1\n" \
    "buffer_load_dwordx4 v[192:195], v0, s[8:11], s39 offen offset:0 sc1\n" \
    "buffer_load_dwordx4 v[196:199], v0, s[8:11], s39 offen offset:1024 sc1\n" \
    "buffer_load_dwordx4 v[200:203], v0, s[8:11], s39 offen offset:2048 sc1\n" \
    "buffer_load_dwordx4 v[204:207], v0, s[8:11], s39 offen offset:3072 sc1\n" \
    "buffer_load_dwordx4 v[208:211], v0, s[8:11], s40 offen offset:0 sc1\n" \
    "buffer_load_dwordx4 v[212:215], v0, s[8:11], s40 offen offset:1024 sc1\n" \
    "buffer_load_dwordx4 v[216:219], v0, s[8:11], s40 offen offset:2048 sc1\n" \
    "buffer_load_dwordx4 v[220:223], v0, s[8:11], s40 offen offset:3072 sc1\n" \
    "s_and_b32 s33, s32, s23\n" \
    "s_mul_i32 s45, s33, s24\n" \
    "s_cmp_lt_u32 s33, 64-%[pdist]\n" \
    "s_cselect_b32 s45, s45, 0x80000000\n" \
    "s_add_u32 s44, s43, %[x1]\n" \
    "s_add_u32 s50, s50, 16384\n" \
    "s_xor_b32 s51, s51, 16384\n" \
    "s_waitcnt vmcnt(15)\n" \
    "v_max3_u32 v19, v44, v45, v46\n" \
    "s_waitcnt vmcnt(14)\n" \
    "v_max3_u32 v19, v19, v47, v48\n" \
    "v_max3_u32 v19, v19, v49, v50\n" \
    "v_max3_u32 v19, v19, v51, v52\n" \
    "v_max3_u32 v19, v19, v53, v54\n" \
    "s_waitcnt vmcnt(13)\n" \
    "v_max3_u32 v19, v19, v55, v56\n" \
    "v_max3_u32 v19, v19, v57, v58\n" \
    "s_waitcnt vmcnt(12)\n" \
    "v_max3_u32 v19, v19, v59, v60\n" \
    "v_max3_u32 v19, v19, v61, v62\n" \
    "s_waitcnt vmcnt(11)\n" \
    "v_max3_u32 v19, v19, v63, v64\n" \
    "v_max3_u32 v19, v19, v65, v66\n" \
    "s_waitcnt vmcnt(10)\n" \
    "v_max3_u32 v19, v19, v67, v68\n" \
    "v_max3_u32 v19, v19, v69, v70\n" \
    "s_waitcnt vmcnt(9)\n" \
    "v_max3_u32 v19, v19, v71, v72\n" \
    "v_max3_u32 v19, v19, v73, v74\n" \
    "v_max_u32 v19, v19, v75\n" \
    "v_cmp_eq_u32 vcc, -1, v19\n" \
    "s_nop 0\n" \
    "s_cbranch_vccnz .Lr_ctC1_%=\n" \
    ".Lr_ctrC1_%=:\n" \
    "v_mfma_f32_16x16x32_bf16 v[88:91], v[36:39], v[72:75], 0\n" \
    "v_mfma_f32_16x16x32_bf16 v[88:91], v[224:227], v[56:59], v[88:91]\n" \
    "v_mfma_f32_16x16x32_bf16 v[88:91], v[228:231], v[60:63], v[88:91]\n" \
    "v_mfma_f32_16x16x32_bf16 v[88:91], v[232:235], v[64:67], v[88:91]\n" \
    "v_mfma_f32_16x16x32_bf16 v[88:91], v[236:239], v[68:71], v[88:91]\n" \
    "v_mfma_f32_16x16x32_bf16 v[92:95], v[40:43], v[72:75], 0\n" \
    "v_mfma_f32_16x16x32_bf16 v[92:95], v[240:243], v[56:59], v[92:95]\n" \
    "v_mfma_f32_16x16x32_bf16 v[92:95], v[244:247], v[60:63], v[92:95]\n" \
    "v_mfma_f32_16x16x32_bf16 v[92:95], v[248:251], v[64:67], v[92:95]\n" \
    "v_mfma_f32_16x16x32_bf16 v[92:95], v[252:255], v[68:71], v[92:95]\n" \
    "v_mfma_f32_16x16x32_bf16 v[80:83], v[36:39], v[52:55], 0\n" \
    "v_mfma_f32_16x16x32_bf16 v[84:87], v[40:43], v[52:55], 0\n" \
    "v_mov_b32 v225, v226\n" \
    "v_mov_b32 v226, v228\n" \
    "v_mov_b32 v227, v230\n" \
    "v_mov_b32 v233, v234\n" \
    "v_mov_b32 v234, v236\n" \
    "v_mov_b32 v235, v238\n" \
    "v_mov_b32 v241, v242\n" \
    "v_mov_b32 v242, v244\n" \
    "v_mov_b32 v243, v246\n" \
    "v_mov_b32 v249, v250\n" \
    "v_mov_b32 v250, v252\n" \
    "v_mov_b32 v251, v254\n" \
    "ds_write_b128 v8, v[88:91] offset:2048\n" \
    "ds_write_b128 v8, v[92:95] offset:3072\n" \
    "v_permlane16_swap_b32 v224, v226\n" \
    "v_permlane16_swap_b32 v225, v227\n" \
    "v_permlane16_swap_b32 v232, v234\n" \
    "v_permlane16_swap_b32 v233, v235\n" \
    "v_permlane16_swap_b32 v240, v242\n" \
    "v_permlane16_swap_b32 v241, v243\n" \
    "v_permlane16_swap_b32 v248, v250\n" \
    "v_permlane16_swap_b32 v249, v251\n" \
    "v_mfma_f32_16x16x32_bf16 v[80:83], v[224:227], v[44:47], v[80:83]\n" \
    "v_mfma_f32_16x16x32_bf16 v[84:87], v[240:243], v[44:47], v[84:87]\n" \
    "v_mfma_f32_16x16x32_bf16 v[80:83], v[232:235], v[48:51], v[80:83]\n" \
    "v_mfma_f32_16x16x32_bf16 v[84:87], v[248:251], v[48:51], v[84:87]\n" \
    "buffer_load_dword v18, v2, s[8:11], s50 offen sc1\n" \
    "s_nop 5\n" \
    "ds_write_b128 v8, v[80:83] offset:0\n" \
    "ds_write_b128 v8, v[84:87] offset:1024\n" \
    "s_waitcnt lgkmcnt(0)\n" \
    "s_barrier\n" \
    "s_add_u32 s64, s62, %[dh]\n" \
    "s_cmp_eq_u32 s54, 0\n" \
    "s_cbranch_scc1 .Lr_cbC1_%=\n" \
    ".Lr_cbrC1_%=:\n" \
    "s_add_u32 s32, s32, 1\n" \
    "s_mov_b32 s68, s40\n" \
    "s_cmp_lt_u32 s32, 64\n" \
    "s_cbranch_scc1 .Lr_lC_%=\n" \
    "s_waitcnt vmcnt(0)\n" \
    "s_endpgm\n" \
    ".Lr_cpD0_%=:\n" \
    "s_mov_b32 s52, 0\n" \
    "s_nop 7\n" \
    ".Lr_cqD0_%=:\n" \
    "buffer_load_dwordx4 v[36:39], v0, s[8:11], s36 offen sc1\n" \
    "buffer_load_dwordx4 v[40:43], v0, s[8:11], s37 offen sc1\n" \
    "buffer_load_dwordx4 v[32:35], v4, s[8:11], s35 offen sc1\n" \
    "buffer_load_dwordx4 v[24:27], v0, s[8:11], s35 offen sc1\n" \
    "buffer_load_dwordx4 v[28:31], v0, s[8:11], s35 offen offset:1024 sc1\n" \
    "s_waitcnt vmcnt(4)\n" \
    "v_max3_u32 v19, v36, v37, v38\n" \
    "s_waitcnt vmcnt(3)\n" \
    "v_max3_u32 v19, v19, v39, v40\n" \
    "v_max3_u32 v19, v19, v41, v42\n" \
    "v_max_u32 v19, v19, v43\n" \
    "s_waitcnt vmcnt(2)\n" \
    "v_mfma_f32_16x16x32_bf16 v[80:83], v[32:35], v[36:39], 0\n" \
    "v_max3_u32 v20, v32, v33, v34\n" \
    "v_mfma_f32_16x16x32_bf16 v[84:87], v[32:35], v[40:43], 0\n" \
    "v_max_u32 v20, v20, v35\n" \
    "s_waitcnt vmcnt(1)\n" \
    "v_smfmac_f32_16x16x64_bf16 v[80:83], v[24:27], v[192:199], v10\n" \
    "v_smfmac_f32_16x16x64_bf16 v[84:87], v[24:27], v[208:215], v10\n" \
    "v_max3_u32 v19, v19, v24, v25\n" \
    "v_max3_u32 v19, v19, v26, v27\n" \
    "s_waitcnt vmcnt(0)\n" \
    "v_smfmac_f32_16x16x64_bf16 v[80:83], v[28:31], v[200:207], v10\n" \
    "v_smfmac_f32_16x16x64_bf16 v[84:87], v[28:31], v[216:223], v10\n" \
    "v_max3_u32 v20, v20, v28, v29\n" \
    "v_max3_u32 v20, v20, v30, v31\n" \
    "v_max_u32 v19, v19, v20\n" \
    "v_cmp_eq_u32 vcc, -1, v19\n" \
    "s_nop 7\n" \
    "s_cbranch_vccz .Lr_cprD0_%=\n" \
    "s_add_u32 s52, s52, 1\n" \
    "s_cmp_lt_u32 s52, s53\n" \
    "s_cbranch_scc1 .Lr_cqD0_%=\n" \
    "s_mov_b32 s53, 0\n" \
    "s_branch .Lr_cprD0_%=\n" \
    ".Lr_ctD0_%=:\n" \
    "s_mov_b32 s52, 0\n" \
    "s_add_u32 s73, s32, s25\n" \
    ".Lr_cuD0_%=:\n" \
    "s_sleep 4\n" \
    "buffer_load_dwordx4 v[44:47], v0, s[8:11], s41 offen offset:0 sc1\n" \
    "buffer_load_dwordx4 v[48:51], v0, s[8:11], s41 offen offset:1024 sc1\n" \
    "buffer_load_dwordx4 v[52:55], v0, s[8:11], s41 offen offset:2048 sc1\n" \
    "buffer_load_dwordx4 v[56:59], v0, s[8:11], s41 offen offset:3072 sc1\n" \
    "buffer_load_dwordx4 v[60:63], v0, s[8:11], s42 offen offset:0 sc1\n" \
    "buffer_load_dwordx4 v[64:67], v0, s[8:11], s42 offen offset:1024 sc1\n" \
    "buffer_load_dwordx4 v[68:71], v0, s[8:11], s42 offen offset:2048 sc1\n" \
    "buffer_load_dwordx4 v[72:75], v0, s[8:11], s42 offen offset:3072 sc1\n" \
    "s_waitcnt vmcnt(7)\n" \
    "v_max3_u32 v19, v44, v45, v46\n" \
    "s_waitcnt vmcnt(6)\n" \
    "v_max3_u32 v19, v19, v47, v48\n" \
    "v_max3_u32 v19, v19, v49, v50\n" \
    "s_waitcnt vmcnt(5)\n" \
    "v_max3_u32 v19, v19, v51, v52\n" \
    "v_max3_u32 v19, v19, v53, v54\n" \
    "s_waitcnt vmcnt(4)\n" \
    "v_max3_u32 v19, v19, v55, v56\n" \
    "v_max3_u32 v19, v19, v57, v58\n" \
    "s_waitcnt vmcnt(3)\n" \
    "v_max3_u32 v19, v19, v59, v60\n" \
    "v_max3_u32 v19, v19, v61, v62\n" \
    "s_waitcnt vmcnt(2)\n" \
    "v_max3_u32 v19, v19, v63, v64\n" \
    "v_max3_u32 v19, v19, v65, v66\n" \
    "s_waitcnt vmcnt(1)\n" \
    "v_max3_u32 v19, v19, v67, v68\n" \
    "v_max3_u32 v19, v19, v69, v70\n" \
    "s_waitcnt vmcnt(0)\n" \
    "v_max3_u32 v19, v19, v71, v72\n" \
    "v_max3_u32 v19, v19, v73, v74\n" \
    "v_max_u32 v19, v19, v75\n" \
    "v_cmp_eq_u32 vcc, -1, v19\n" \
    "s_nop 0\n" \
    "s_cbranch_vccz .Lr_ctrD0_%=\n" \
    "s_add_u32 s52, s52, 1\n" \
    "s_cmp_lt_u32 s52, s53\n" \
    "s_cbranch_scc1 .Lr_cuD0_%=\n" \
    "s_mov_b32 s53, 0\n" \
    "s_branch .Lr_ctrD0_%=\n" \
    ".Lr_cbD0_%=:\n" \
    "s_add_u32 s73, s32, s25\n" \
    "s_sub_u32 s50, s50, 16384\n" \
    "s_mov_b32 s52, 0\n" \
    ".Lr_ccD0_%=:\n" \
    "buffer_load_dword v18, v2, s[8:11], s50 offen sc1\n" \
    "s_waitcnt vmcnt(0)\n" \
    "v_cmp_eq_u32 vcc, 0, v18\n" \
    "s_nop 0\n" \
    "s_and_b64 vcc, vcc, s[56:57]\n" \
    "s_cbranch_scc0 .Lr_cdD0_%=\n" \
    "s_sleep 2\n" \
    "s_add_u32 s52, s52, 1\n" \
    "s_cmp_lt_u32 s52, s53\n" \
    "s_cbranch_scc1 .Lr_ccD0_%=\n" \
    "s_mov_b32 s53, 0\n" \
    "s_branch .Lr_cdD0_%=\n" \
    ".Lr_cdD0_%=:\n" \
    "s_add_u32 s50, s50, 16384\n" \
    "s_and_b32 s33, s49, %[brn]-1\n" \
    "s_mul_i32 s39, s33, 2621440\n" \
    "s_add_u32 s39, s39, s19\n" \
    "s_lshr_b32 s33, s39, 8\n" \
    "s_xor_b32 s33, s33, s27\n" \
    "s_and_b32 s33, s33, 0x2000\n" \
    "s_or_b32 s39, s39, s33\n" \
    "s_add_u32 s40, s39, 65536\n" \
    "s_waitcnt vmcnt(0)\n" \
    "buffer_load_dwordx4 v[224:227], v0, s[8:11], s39 offen offset:0 sc1\n" \
    "buffer_load_dwordx4 v[228:231], v0, s[8:11], s39 offen offset:1024 sc1\n" \
    "buffer_load_dwordx4 v[232:235], v0, s[8:11], s39 offen offset:2048 sc1\n" \
    "buffer_load_dwordx4 v[236:239], v0, s[8:11], s39 offen offset:3072 sc1\n" \
    "buffer_load_dwordx4 v[240:243], v0, s[8:11], s40 offen offset:0 sc1\n" \
    "buffer_load_dwordx4 v[244:247], v0, s[8:11], s40 offen offset:1024 sc1\n" \
    "buffer_load_dwordx4 v[248:251], v0, s[8:11], s40 offen offset:2048 sc1\n" \
    "buffer_load_dwordx4 v[252:255], v0, s[8:11], s40 offen offset:3072 sc1\n" \
    "buffer_load_dword v18, v2, s[8:11], s50 offen sc1\n" \
    "s_waitcnt vmcnt(0)\n" \
    "s_branch .Lr_cbrD0_%=\n" \
    ".Lr_cpD1_%=:\n" \
    "s_mov_b32 s52, 0\n" \
    "s_nop 7\n" \
    ".Lr_cqD1_%=:\n" \
    "buffer_load_dwordx4 v[36:39], v0, s[8:11], s36 offen sc1\n" \
    "buffer_load_dwordx4 v[40:43], v0, s[8:11], s37 offen sc1\n" \
    "buffer_load_dwordx4 v[32:35], v4, s[8:11], s35 offen sc1\n" \
    "buffer_load_dwordx4 v[24:27], v0, s[8:11], s35 offen sc1\n" \
    "buffer_load_dwordx4 v[28:31], v0, s[8:11], s35 offen offset:1024 sc1\n" \
    "s_waitcnt vmcnt(4)\n" \
    "v_max3_u32 v19, v36, v37, v38\n" \
    "s_waitcnt vmcnt(3)\n" \
    "v_max3_u32 v19, v19, v39, v40\n" \
    "v_max3_u32 v19, v19, v41, v42\n" \
    "v_max_u32 v19, v19, v43\n" \
    "s_waitcnt vmcnt(2)\n" \
    "v_mfma_f32_16x16x32_bf16 v[80:83], v[32:35], v[36:39], 0\n" \
    "v_max3_u32 v20, v32, v33, v34\n" \
    "v_mfma_f32_16x16x32_bf16 v[84:87], v[32:35], v[40:43], 0\n" \
    "v_max_u32 v20, v20, v35\n" \
    "s_waitcnt vmcnt(1)\n" \
    "v_smfmac_f32_16x16x64_bf16 v[80:83], v[24:27], v[224:231], v10\n" \
    "v_smfmac_f32_16x16x64_bf16 v[84:87], v[24:27], v[240:247], v10\n" \
    "v_max3_u32 v19, v19, v24, v25\n" \
    "v_max3_u32 v19, v19, v26, v27\n" \
    "s_waitcnt vmcnt(0)\n" \
    "v_smfmac_f32_16x16x64_bf16 v[80:83], v[28:31], v[232:239], v10\n" \
    "v_smfmac_f32_16x16x64_bf16 v[84:87], v[28:31], v[248:255], v10\n" \
    "v_max3_u32 v20, v20, v28, v29\n" \
    "v_max3_u32 v20, v20, v30, v31\n" \
    "v_max_u32 v19, v19, v20\n" \
    "v_cmp_eq_u32 vcc, -1, v19\n" \
    "s_nop 7\n" \
    "s_cbranch_vccz .Lr_cprD1_%=\n" \
    "s_add_u32 s52, s52, 1\n" \
    "s_cmp_lt_u32 s52, s53\n" \
    "s_cbranch_scc1 .Lr_cqD1_%=\n" \
    "s_mov_b32 s53, 0\n" \
    "s_branch .Lr_cprD1_%=\n" \
    ".Lr_ctD1_%=:\n" \
    "s_mov_b32 s52, 0\n" \
    "s_add_u32 s73, s32, s25\n" \
    ".Lr_cuD1_%=:\n" \
    "s_sleep 4\n" \
    "buffer_load_dwordx4 v[44:47], v0, s[8:11], s41 offen offset:0 sc1\n" \
    "buffer_load_dwordx4 v[48:51], v0, s[8:11], s41 offen offset:1024 sc1\n" \
    "buffer_load_dwordx4 v[52:55], v0, s[8:11], s41 offen offset:2048 sc1\n" \
    "buffer_load_dwordx4 v[56:59], v0, s[8:11], s41 offen offset:3072 sc1\n" \
    "buffer_load_dwordx4 v[60:63], v0, s[8:11], s42 offen offset:0 sc1\n" \
    "buffer_load_dwordx4 v[64:67], v0, s[8:11], s42 offen offset:1024 sc1\n" \
    "buffer_load_dwordx4 v[68:71], v0, s[8:11], s42 offen offset:2048 sc1\n" \
    "buffer_load_dwordx4 v[72:75], v0, s[8:11], s42 offen offset:3072 sc1\n" \
    "s_waitcnt vmcnt(7)\n" \
    "v_max3_u32 v19, v44, v45, v46\n" \
    "s_waitcnt vmcnt(6)\n" \
    "v_max3_u32 v19, v19, v47, v48\n" \
    "v_max3_u32 v19, v19, v49, v50\n" \
    "s_waitcnt vmcnt(5)\n" \
    "v_max3_u32 v19, v19, v51, v52\n" \
    "v_max3_u32 v19, v19, v53, v54\n" \
    "s_waitcnt vmcnt(4)\n" \
    "v_max3_u32 v19, v19, v55, v56\n" \
    "v_max3_u32 v19, v19, v57, v58\n" \
    "s_waitcnt vmcnt(3)\n" \
    "v_max3_u32 v19, v19, v59, v60\n" \
    "v_max3_u32 v19, v19, v61, v62\n" \
    "s_waitcnt vmcnt(2)\n" \
    "v_max3_u32 v19, v19, v63, v64\n" \
    "v_max3_u32 v19, v19, v65, v66\n" \
    "s_waitcnt vmcnt(1)\n" \
    "v_max3_u32 v19, v19, v67, v68\n" \
    "v_max3_u32 v19, v19, v69, v70\n" \
    "s_waitcnt vmcnt(0)\n" \
    "v_max3_u32 v19, v19, v71, v72\n" \
    "v_max3_u32 v19, v19, v73, v74\n" \
    "v_max_u32 v19, v19, v75\n" \
    "v_cmp_eq_u32 vcc, -1, v19\n" \
    "s_nop 0\n" \
    "s_cbranch_vccz .Lr_ctrD1_%=\n" \
    "s_add_u32 s52, s52, 1\n" \
    "s_cmp_lt_u32 s52, s53\n" \
    "s_cbranch_scc1 .Lr_cuD1_%=\n" \
    "s_mov_b32 s53, 0\n" \
    "s_branch .Lr_ctrD1_%=\n" \
    ".Lr_cbD1_%=:\n" \
    "s_add_u32 s73, s32, s25\n" \
    "s_sub_u32 s50, s50, 16384\n" \
    "s_mov_b32 s52, 0\n" \
    ".Lr_ccD1_%=:\n" \
    "buffer_load_dword v18, v2, s[8:11], s50 offen sc1\n" \
    "s_waitcnt vmcnt(0)\n" \
    "v_cmp_eq_u32 vcc, 0, v18\n" \
    "s_nop 0\n" \
    "s_and_b64 vcc, vcc, s[56:57]\n" \
    "s_cbranch_scc0 .Lr_cdD1_%=\n" \
    "s_sleep 2\n" \
    "s_add_u32 s52, s52, 1\n" \
    "s_cmp_lt_u32 s52, s53\n" \
    "s_cbranch_scc1 .Lr_ccD1_%=\n" \
    "s_mov_b32 s53, 0\n" \
    "s_branch .Lr_cdD1_%=\n" \
    ".Lr_cdD1_%=:\n" \
    "s_add_u32 s50, s50, 16384\n" \
    "s_and_b32 s33, s49, %[brn]-1\n" \
    "s_mul_i32 s39, s33, 2621440\n" \
    "s_add_u32 s39, s39, s19\n" \
    "s_lshr_b32 s33, s39, 8\n" \
    "s_xor_b32 s33, s33, s27\n" \
    "s_and_b32 s33, s33, 0x2000\n" \
    "s_or_b32 s39, s39, s33\n" \
    "s_add_u32 s40, s39, 65536\n" \
    "s_waitcnt vmcnt(0)\n" \
    "buffer_load_dwordx4 v[192:195], v0, s[8:11], s39 offen offset:0 sc1\n" \
    "buffer_load_dwordx4 v[196:199], v0, s[8:11], s39 offen offset:1024 sc1\n" \
    "buffer_load_dwordx4 v[200:203], v0, s[8:11], s39 offen offset:2048 sc1\n" \
    "buffer_load_dwordx4 v[204:207], v0, s[8:11], s39 offen offset:3072 sc1\n" \
    "buffer_load_dwordx4 v[208:211], v0, s[8:11], s40 offen offset:0 sc1\n" \
    "buffer_load_dwordx4 v[212:215], v0, s[8:11], s40 offen offset:1024 sc1\n" \
    "buffer_load_dwordx4 v[216:219], v0, s[8:11], s40 offen offset:2048 sc1\n" \
    "buffer_load_dwordx4 v[220:223], v0, s[8:11], s40 offen offset:3072 sc1\n" \
    "buffer_load_dword v18, v2, s[8:11], s50 offen sc1\n" \
    "s_waitcnt vmcnt(0)\n" \
    "s_branch .Lr_cbrD1_%=\n" \
    ".Lr_cpA0_%=:\n" \
    "s_mov_b32 s52, 0\n" \
    "s_nop 7\n" \
    ".Lr_cqA0_%=:\n" \
    "buffer_load_dwordx4 v[36:39], v0, s[8:11], s36 offen sc1\n" \
    "buffer_load_dwordx4 v[40:43], v0, s[8:11], s37 offen sc1\n" \
    "buffer_load_dwordx4 v[32:35], v4, s[8:11], s35 offen sc1\n" \
    "buffer_load_dwordx4 v[24:27], v0, s[8:11], s35 offen sc1\n" \
    "buffer_load_dwordx4 v[28:31], v0, s[8:11], s35 offen offset:1024 sc1\n" \
    "s_waitcnt vmcnt(4)\n" \
    "v_max3_u32 v19, v36, v37, v38\n" \
    "s_waitcnt vmcnt(3)\n" \
    "v_max3_u32 v19, v19, v39, v40\n" \
    "v_max3_u32 v19, v19, v41, v42\n" \
    "v_max_u32 v19, v19, v43\n" \
    "s_waitcnt vmcnt(2)\n" \
    "v_mfma_f32_16x16x32_bf16 v[80:83], v[32:35], v[36:39], 0\n" \
    "v_max3_u32 v20, v32, v33, v34\n" \
    "v_mfma_f32_16x16x32_bf16 v[84:87], v[32:35], v[40:43], 0\n" \
    "v_max_u32 v20, v20, v35\n" \
    "s_waitcnt vmcnt(1)\n" \
    "v_smfmac_f32_16x16x64_bf16 v[80:83], v[24:27], v[192:199], v10\n" \
    "v_smfmac_f32_16x16x64_bf16 v[84:87], v[24:27], v[208:215], v10\n" \
    "v_max3_u32 v19, v19, v24, v25\n" \
    "v_max3_u32 v19, v19, v26, v27\n" \
    "s_waitcnt vmcnt(0)\n" \
    "v_smfmac_f32_16x16x64_bf16 v[80:83], v[28:31], v[200:207], v10\n" \
    "v_smfmac_f32_16x16x64_bf16 v[84:87], v[28:31], v[216:223], v10\n" \
    "v_max3_u32 v20, v20, v28, v29\n" \
    "v_max3_u32 v20, v20, v30, v31\n" \
    "v_max_u32 v19, v19, v20\n" \
    "v_cmp_eq_u32 vcc, -1, v19\n" \
    "s_nop 7\n" \
    "s_cbranch_vccz .Lr_cprA0_%=\n" \
    "s_add_u32 s52, s52, 1\n" \
    "s_cmp_lt_u32 s52, s53\n" \
    "s_cbranch_scc1 .Lr_cqA0_%=\n" \
    "s_mov_b32 s53, 0\n" \
    "s_branch .Lr_cprA0_%=\n" \
    ".Lr_ctA0_%=:\n" \
    "s_mov_b32 s52, 0\n" \
    "s_add_u32 s73, s32, s25\n" \
    ".Lr_cuA0_%=:\n" \
    "s_sleep 4\n" \
    "buffer_load_dwordx4 v[44:47], v0, s[8:11], s41 offen offset:0 sc1\n" \
    "buffer_load_dwordx4 v[48:51], v0, s[8:11], s41 offen offset:1024 sc1\n" \
    "buffer_load_dwordx4 v[52:55], v0, s[8:11], s41 offen offset:2048 sc1\n" \
    "buffer_load_dwordx4 v[56:59], v0, s[8:11], s41 offen offset:3072 sc1\n" \
    "buffer_load_dwordx4 v[60:63], v0, s[8:11], s42 offen offset:0 sc1\n" \
    "buffer_load_dwordx4 v[64:67], v0, s[8:11], s42 offen offset:1024 sc1\n" \
    "buffer_load_dwordx4 v[68:71], v0, s[8:11], s42 offen offset:2048 sc1\n" \
    "buffer_load_dwordx4 v[72:75], v0, s[8:11], s42 offen offset:3072 sc1\n" \
    "s_waitcnt vmcnt(7)\n" \
    "v_max3_u32 v19, v44, v45, v46\n" \
    "s_waitcnt vmcnt(6)\n" \
    "v_max3_u32 v19, v19, v47, v48\n" \
    "v_max3_u32 v19, v19, v49, v50\n" \
    "s_waitcnt vmcnt(5)\n" \
    "v_max3_u32 v19, v19, v51, v52\n" \
    "v_max3_u32 v19, v19, v53, v54\n" \
    "s_waitcnt vmcnt(4)\n" \
    "v_max3_u32 v19, v19, v55, v56\n" \
    "v_max3_u32 v19, v19, v57, v58\n" \
    "s_waitcnt vmcnt(3)\n" \
    "v_max3_u32 v19, v19, v59, v60\n" \
    "v_max3_u32 v19, v19, v61, v62\n" \
    "s_waitcnt vmcnt(2)\n" \
    "v_max3_u32 v19, v19, v63, v64\n" \
    "v_max3_u32 v19, v19, v65, v66\n" \
    "s_waitcnt vmcnt(1)\n" \
    "v_max3_u32 v19, v19, v67, v68\n" \
    "v_max3_u32 v19, v19, v69, v70\n" \
    "s_waitcnt vmcnt(0)\n" \
    "v_max3_u32 v19, v19, v71, v72\n" \
    "v_max3_u32 v19, v19, v73, v74\n" \
    "v_max_u32 v19, v19, v75\n" \
    "v_cmp_eq_u32 vcc, -1, v19\n" \
    "s_nop 0\n" \
    "s_cbranch_vccz .Lr_ctrA0_%=\n" \
    "s_add_u32 s52, s52, 1\n" \
    "s_cmp_lt_u32 s52, s53\n" \
    "s_cbranch_scc1 .Lr_cuA0_%=\n" \
    "s_mov_b32 s53, 0\n" \
    "s_branch .Lr_ctrA0_%=\n" \
    ".Lr_cbA0_%=:\n" \
    "s_add_u32 s73, s32, s25\n" \
    "s_sub_u32 s50, s50, 16384\n" \
    "s_mov_b32 s52, 0\n" \
    ".Lr_ccA0_%=:\n" \
    "buffer_load_dword v18, v2, s[8:11], s50 offen sc1\n" \
    "s_waitcnt vmcnt(0)\n" \
    "v_cmp_eq_u32 vcc, 0, v18\n" \
    "s_nop 0\n" \
    "s_and_b64 vcc, vcc, s[56:57]\n" \
    "s_cbranch_scc0 .Lr_cdA0_%=\n" \
    "s_sleep 2\n" \
    "s_add_u32 s52, s52, 1\n" \
    "s_cmp_lt_u32 s52, s53\n" \
    "s_cbranch_scc1 .Lr_ccA0_%=\n" \
    "s_mov_b32 s53, 0\n" \
    "s_branch .Lr_cdA0_%=\n" \
    ".Lr_cdA0_%=:\n" \
    "s_add_u32 s50, s50, 16384\n" \
    "s_and_b32 s33, s49, %[brn]-1\n" \
    "s_mul_i32 s39, s33, 2621440\n" \
    "s_add_u32 s39, s39, s19\n" \
    "s_lshr_b32 s33, s39, 8\n" \
    "s_xor_b32 s33, s33, s27\n" \
    "s_and_b32 s33, s33, 0x2000\n" \
    "s_or_b32 s39, s39, s33\n" \
    "s_add_u32 s40, s39, 65536\n" \
    "s_waitcnt vmcnt(0)\n" \
    "buffer_load_dwordx4 v[224:227], v0, s[8:11], s39 offen offset:0 sc1\n" \
    "buffer_load_dwordx4 v[228:231], v0, s[8:11], s39 offen offset:1024 sc1\n" \
    "buffer_load_dwordx4 v[232:235], v0, s[8:11], s39 offen offset:2048 sc1\n" \
    "buffer_load_dwordx4 v[236:239], v0, s[8:11], s39 offen offset:3072 sc1\n" \
    "buffer_load_dwordx4 v[240:243], v0, s[8:11], s40 offen offset:0 sc1\n" \
    "buffer_load_dwordx4 v[244:247], v0, s[8:11], s40 offen offset:1024 sc1\n" \
    "buffer_load_dwordx4 v[248:251], v0, s[8:11], s40 offen offset:2048 sc1\n" \
    "buffer_load_dwordx4 v[252:255], v0, s[8:11], s40 offen offset:3072 sc1\n" \
    "buffer_load_dword v18, v2, s[8:11], s50 offen sc1\n" \
    "s_waitcnt vmcnt(0)\n" \
    "s_branch .Lr_cbrA0_%=\n" \
    ".Lr_cpA1_%=:\n" \
    "s_mov_b32 s52, 0\n" \
    "s_nop 7\n" \
    ".Lr_cqA1_%=:\n" \
    "buffer_load_dwordx4 v[36:39], v0, s[8:11], s36 offen sc1\n" \
    "buffer_load_dwordx4 v[40:43], v0, s[8:11], s37 offen sc1\n" \
    "buffer_load_dwordx4 v[32:35], v4, s[8:11], s35 offen sc1\n" \
    "buffer_load_dwordx4 v[24:27], v0, s[8:11], s35 offen sc1\n" \
    "buffer_load_dwordx4 v[28:31], v0, s[8:11], s35 offen offset:1024 sc1\n" \
    "s_waitcnt vmcnt(4)\n" \
    "v_max3_u32 v19, v36, v37, v38\n" \
    "s_waitcnt vmcnt(3)\n" \
    "v_max3_u32 v19, v19, v39, v40\n" \
    "v_max3_u32 v19, v19, v41, v42\n" \
    "v_max_u32 v19, v19, v43\n" \
    "s_waitcnt vmcnt(2)\n" \
    "v_mfma_f32_16x16x32_bf16 v[80:83], v[32:35], v[36:39], 0\n" \
    "v_max3_u32 v20, v32, v33, v34\n" \
    "v_mfma_f32_16x16x32_bf16 v[84:87], v[32:35], v[40:43], 0\n" \
    "v_max_u32 v20, v20, v35\n" \
    "s_waitcnt vmcnt(1)\n" \
    "v_smfmac_f32_16x16x64_bf16 v[80:83], v[24:27], v[224:231], v10\n" \
    "v_smfmac_f32_16x16x64_bf16 v[84:87], v[24:27], v[240:247], v10\n" \
    "v_max3_u32 v19, v19, v24, v25\n" \
    "v_max3_u32 v19, v19, v26, v27\n" \
    "s_waitcnt vmcnt(0)\n" \
    "v_smfmac_f32_16x16x64_bf16 v[80:83], v[28:31], v[232:239], v10\n" \
    "v_smfmac_f32_16x16x64_bf16 v[84:87], v[28:31], v[248:255], v10\n" \
    "v_max3_u32 v20, v20, v28, v29\n" \
    "v_max3_u32 v20, v20, v30, v31\n" \
    "v_max_u32 v19, v19, v20\n" \
    "v_cmp_eq_u32 vcc, -1, v19\n" \
    "s_nop 7\n" \
    "s_cbranch_vccz .Lr_cprA1_%=\n" \
    "s_add_u32 s52, s52, 1\n" \
    "s_cmp_lt_u32 s52, s53\n" \
    "s_cbranch_scc1 .Lr_cqA1_%=\n" \
    "s_mov_b32 s53, 0\n" \
    "s_branch .Lr_cprA1_%=\n" \
    ".Lr_ctA1_%=:\n" \
    "s_mov_b32 s52, 0\n" \
    "s_add_u32 s73, s32, s25\n" \
    ".Lr_cuA1_%=:\n" \
    "s_sleep 4\n" \
    "buffer_load_dwordx4 v[44:47], v0, s[8:11], s41 offen offset:0 sc1\n" \
    "buffer_load_dwordx4 v[48:51], v0, s[8:11], s41 offen offset:1024 sc1\n" \
    "buffer_load_dwordx4 v[52:55], v0, s[8:11], s41 offen offset:2048 sc1\n" \
    "buffer_load_dwordx4 v[56:59], v0, s[8:11], s41 offen offset:3072 sc1\n" \
    "buffer_load_dwordx4 v[60:63], v0, s[8:11], s42 offen offset:0 sc1\n" \
    "buffer_load_dwordx4 v[64:67], v0, s[8:11], s42 offen offset:1024 sc1\n" \
    "buffer_load_dwordx4 v[68:71], v0, s[8:11], s42 offen offset:2048 sc1\n" \
    "buffer_load_dwordx4 v[72:75], v0, s[8:11], s42 offen offset:3072 sc1\n" \
    "s_waitcnt vmcnt(7)\n" \
    "v_max3_u32 v19, v44, v45, v46\n" \
    "s_waitcnt vmcnt(6)\n" \
    "v_max3_u32 v19, v19, v47, v48\n" \
    "v_max3_u32 v19, v19, v49, v50\n" \
    "s_waitcnt vmcnt(5)\n" \
    "v_max3_u32 v19, v19, v51, v52\n" \
    "v_max3_u32 v19, v19, v53, v54\n" \
    "s_waitcnt vmcnt(4)\n" \
    "v_max3_u32 v19, v19, v55, v56\n" \
    "v_max3_u32 v19, v19, v57, v58\n" \
    "s_waitcnt vmcnt(3)\n" \
    "v_max3_u32 v19, v19, v59, v60\n" \
    "v_max3_u32 v19, v19, v61, v62\n" \
    "s_waitcnt vmcnt(2)\n" \
    "v_max3_u32 v19, v19, v63, v64\n" \
    "v_max3_u32 v19, v19, v65, v66\n" \
    "s_waitcnt vmcnt(1)\n" \
    "v_max3_u32 v19, v19, v67, v68\n" \
    "v_max3_u32 v19, v19, v69, v70\n" \
    "s_waitcnt vmcnt(0)\n" \
    "v_max3_u32 v19, v19, v71, v72\n" \
    "v_max3_u32 v19, v19, v73, v74\n" \
    "v_max_u32 v19, v19, v75\n" \
    "v_cmp_eq_u32 vcc, -1, v19\n" \
    "s_nop 0\n" \
    "s_cbranch_vccz .Lr_ctrA1_%=\n" \
    "s_add_u32 s52, s52, 1\n" \
    "s_cmp_lt_u32 s52, s53\n" \
    "s_cbranch_scc1 .Lr_cuA1_%=\n" \
    "s_mov_b32 s53, 0\n" \
    "s_branch .Lr_ctrA1_%=\n" \
    ".Lr_cbA1_%=:\n" \
    "s_add_u32 s73, s32, s25\n" \
    "s_sub_u32 s50, s50, 16384\n" \
    "s_mov_b32 s52, 0\n" \
    ".Lr_ccA1_%=:\n" \
    "buffer_load_dword v18, v2, s[8:11], s50 offen sc1\n" \
    "s_waitcnt vmcnt(0)\n" \
    "v_cmp_eq_u32 vcc, 0, v18\n" \
    "s_nop 0\n" \
    "s_and_b64 vcc, vcc, s[56:57]\n" \
    "s_cbranch_scc0 .Lr_cdA1_%=\n" \
    "s_sleep 2\n" \
    "s_add_u32 s52, s52, 1\n" \
    "s_cmp_lt_u32 s52, s53\n" \
    "s_cbranch_scc1 .Lr_ccA1_%=\n" \
    "s_mov_b32 s53, 0\n" \
    "s_branch .Lr_cdA1_%=\n" \
    ".Lr_cdA1_%=:\n" \
    "s_add_u32 s50, s50, 16384\n" \
    "s_and_b32 s33, s49, %[brn]-1\n" \
    "s_mul_i32 s39, s33, 2621440\n" \
    "s_add_u32 s39, s39, s19\n" \
    "s_lshr_b32 s33, s39, 8\n" \
    "s_xor_b32 s33, s33, s27\n" \
    "s_and_b32 s33, s33, 0x2000\n" \
    "s_or_b32 s39, s39, s33\n" \
    "s_add_u32 s40, s39, 65536\n" \
    "s_waitcnt vmcnt(0)\n" \
    "buffer_load_dwordx4 v[192:195], v0, s[8:11], s39 offen offset:0 sc1\n" \
    "buffer_load_dwordx4 v[196:199], v0, s[8:11], s39 offen offset:1024 sc1\n" \
    "buffer_load_dwordx4 v[200:203], v0, s[8:11], s39 offen offset:2048 sc1\n" \
    "buffer_load_dwordx4 v[204:207], v0, s[8:11], s39 offen offset:3072 sc1\n" \
    "buffer_load_dwordx4 v[208:211], v0, s[8:11], s40 offen offset:0 sc1\n" \
    "buffer_load_dwordx4 v[212:215], v0, s[8:11], s40 offen offset:1024 sc1\n" \
    "buffer_load_dwordx4 v[216:219], v0, s[8:11], s40 offen offset:2048 sc1\n" \
    "buffer_load_dwordx4 v[220:223], v0, s[8:11], s40 offen offset:3072 sc1\n" \
    "buffer_load_dword v18, v2, s[8:11], s50 offen sc1\n" \
    "s_waitcnt vmcnt(0)\n" \
    "s_branch .Lr_cbrA1_%=\n" \
    ".Lr_cpB0_%=:\n" \
    "s_mov_b32 s52, 0\n" \
    "s_nop 7\n" \
    ".Lr_cqB0_%=:\n" \
    "buffer_load_dwordx4 v[36:39], v0, s[8:11], s36 offen sc1\n" \
    "buffer_load_dwordx4 v[40:43], v0, s[8:11], s37 offen sc1\n" \
    "buffer_load_dwordx4 v[32:35], v4, s[8:11], s35 offen sc1\n" \
    "buffer_load_dwordx4 v[24:27], v0, s[8:11], s35 offen sc1\n" \
    "buffer_load_dwordx4 v[28:31], v0, s[8:11], s35 offen offset:1024 sc1\n" \
    "s_waitcnt vmcnt(4)\n" \
    "v_max3_u32 v19, v36, v37, v38\n" \
    "s_waitcnt vmcnt(3)\n" \
    "v_max3_u32 v19, v19, v39, v40\n" \
    "v_max3_u32 v19, v19, v41, v42\n" \
    "v_max_u32 v19, v19, v43\n" \
    "s_waitcnt vmcnt(2)\n" \
    "v_mfma_f32_16x16x32_bf16 v[80:83], v[32:35], v[36:39], 0\n" \
    "v_max3_u32 v20, v32, v33, v34\n" \
    "v_mfma_f32_16x16x32_bf16 v[84:87], v[32:35], v[40:43], 0\n" \
    "v_max_u32 v20, v20, v35\n" \
    "s_waitcnt vmcnt(1)\n" \
    "v_smfmac_f32_16x16x64_bf16 v[80:83], v[24:27], v[192:199], v10\n" \
    "v_smfmac_f32_16x16x64_bf16 v[84:87], v[24:27], v[208:215], v10\n" \
    "v_max3_u32 v19, v19, v24, v25\n" \
    "v_max3_u32 v19, v19, v26, v27\n" \
    "s_waitcnt vmcnt(0)\n" \
    "v_smfmac_f32_16x16x64_bf16 v[80:83], v[28:31], v[200:207], v10\n" \
    "v_smfmac_f32_16x16x64_bf16 v[84:87], v[28:31], v[216:223], v10\n" \
    "v_max3_u32 v20, v20, v28, v29\n" \
    "v_max3_u32 v20, v20, v30, v31\n" \
    "v_max_u32 v19, v19, v20\n" \
    "v_cmp_eq_u32 vcc, -1, v19\n" \
    "s_nop 7\n" \
    "s_cbranch_vccz .Lr_cprB0_%=\n" \
    "s_add_u32 s52, s52, 1\n" \
    "s_cmp_lt_u32 s52, s53\n" \
    "s_cbranch_scc1 .Lr_cqB0_%=\n" \
    "s_mov_b32 s53, 0\n" \
    "s_branch .Lr_cprB0_%=\n" \
    ".Lr_ctB0_%=:\n" \
    "s_mov_b32 s52, 0\n" \
    "s_add_u32 s73, s32, s25\n" \
    ".Lr_cuB0_%=:\n" \
    "s_sleep 4\n" \
    "buffer_load_dwordx4 v[44:47], v0, s[8:11], s41 offen offset:0 sc1\n" \
    "buffer_load_dwordx4 v[48:51], v0, s[8:11], s41 offen offset:1024 sc1\n" \
    "buffer_load_dwordx4 v[52:55], v0, s[8:11], s41 offen offset:2048 sc1\n" \
    "buffer_load_dwordx4 v[56:59], v0, s[8:11], s41 offen offset:3072 sc1\n" \
    "buffer_load_dwordx4 v[60:63], v0, s[8:11], s42 offen offset:0 sc1\n" \
    "buffer_load_dwordx4 v[64:67], v0, s[8:11], s42 offen offset:1024 sc1\n" \
    "buffer_load_dwordx4 v[68:71], v0, s[8:11], s42 offen offset:2048 sc1\n" \
    "buffer_load_dwordx4 v[72:75], v0, s[8:11], s42 offen offset:3072 sc1\n" \
    "s_waitcnt vmcnt(7)\n" \
    "v_max3_u32 v19, v44, v45, v46\n" \
    "s_waitcnt vmcnt(6)\n" \
    "v_max3_u32 v19, v19, v47, v48\n" \
    "v_max3_u32 v19, v19, v49, v50\n" \
    "s_waitcnt vmcnt(5)\n" \
    "v_max3_u32 v19, v19, v51, v52\n" \
    "v_max3_u32 v19, v19, v53, v54\n" \
    "s_waitcnt vmcnt(4)\n" \
    "v_max3_u32 v19, v19, v55, v56\n" \
    "v_max3_u32 v19, v19, v57, v58\n" \
    "s_waitcnt vmcnt(3)\n" \
    "v_max3_u32 v19, v19, v59, v60\n" \
    "v_max3_u32 v19, v19, v61, v62\n" \
    "s_waitcnt vmcnt(2)\n" \
    "v_max3_u32 v19, v19, v63, v64\n" \
    "v_max3_u32 v19, v19, v65, v66\n" \
    "s_waitcnt vmcnt(1)\n" \
    "v_max3_u32 v19, v19, v67, v68\n" \
    "v_max3_u32 v19, v19, v69, v70\n" \
    "s_waitcnt vmcnt(0)\n" \
    "v_max3_u32 v19, v19, v71, v72\n" \
    "v_max3_u32 v19, v19, v73, v74\n" \
    "v_max_u32 v19, v19, v75\n" \
    "v_cmp_eq_u32 vcc, -1, v19\n" \
    "s_nop 0\n" \
    "s_cbranch_vccz .Lr_ctrB0_%=\n" \
    "s_add_u32 s52, s52, 1\n" \
    "s_cmp_lt_u32 s52, s53\n" \
    "s_cbranch_scc1 .Lr_cuB0_%=\n" \
    "s_mov_b32 s53, 0\n" \
    "s_branch .Lr_ctrB0_%=\n" \
    ".Lr_cbB0_%=:\n" \
    "s_add_u32 s73, s32, s25\n" \
    "s_sub_u32 s50, s50, 16384\n" \
    "s_mov_b32 s52, 0\n" \
    ".Lr_ccB0_%=:\n" \
    "buffer_load_dword v18, v2, s[8:11], s50 offen sc1\n" \
    "s_waitcnt vmcnt(0)\n" \
    "v_cmp_eq_u32 vcc, 0, v18\n" \
    "s_nop 0\n" \
    "s_and_b64 vcc, vcc, s[56:57]\n" \
    "s_cbranch_scc0 .Lr_cdB0_%=\n" \
    "s_sleep 2\n" \
    "s_add_u32 s52, s52, 1\n" \
    "s_cmp_lt_u32 s52, s53\n" \
    "s_cbranch_scc1 .Lr_ccB0_%=\n" \
    "s_mov_b32 s53, 0\n" \
    "s_branch .Lr_cdB0_%=\n" \
    ".Lr_cdB0_%=:\n" \
    "s_add_u32 s50, s50, 16384\n" \
    "s_and_b32 s33, s49, %[brn]-1\n" \
    "s_mul_i32 s39, s33, 2621440\n" \
    "s_add_u32 s39, s39, s19\n" \
    "s_lshr_b32 s33, s39, 8\n" \
    "s_xor_b32 s33, s33, s27\n" \
    "s_and_b32 s33, s33, 0x2000\n" \
    "s_or_b32 s39, s39, s33\n" \
    "s_add_u32 s40, s39, 65536\n" \
    "s_waitcnt vmcnt(0)\n" \
    "buffer_load_dwordx4 v[224:227], v0, s[8:11], s39 offen offset:0 sc1\n" \
    "buffer_load_dwordx4 v[228:231], v0, s[8:11], s39 offen offset:1024 sc1\n" \
    "buffer_load_dwordx4 v[232:235], v0, s[8:11], s39 offen offset:2048 sc1\n" \
    "buffer_load_dwordx4 v[236:239], v0, s[8:11], s39 offen offset:3072 sc1\n" \
    "buffer_load_dwordx4 v[240:243], v0, s[8:11], s40 offen offset:0 sc1\n" \
    "buffer_load_dwordx4 v[244:247], v0, s[8:11], s40 offen offset:1024 sc1\n" \
    "buffer_load_dwordx4 v[248:251], v0, s[8:11], s40 offen offset:2048 sc1\n" \
    "buffer_load_dwordx4 v[252:255], v0, s[8:11], s40 offen offset:3072 sc1\n" \
    "buffer_load_dword v18, v2, s[8:11], s50 offen sc1\n" \
    "s_waitcnt vmcnt(0)\n" \
    "s_branch .Lr_cbrB0_%=\n" \
    ".Lr_cpB1_%=:\n" \
    "s_mov_b32 s52, 0\n" \
    "s_nop 7\n" \
    ".Lr_cqB1_%=:\n" \
    "buffer_load_dwordx4 v[36:39], v0, s[8:11], s36 offen sc1\n" \
    "buffer_load_dwordx4 v[40:43], v0, s[8:11], s37 offen sc1\n" \
    "buffer_load_dwordx4 v[32:35], v4, s[8:11], s35 offen sc1\n" \
    "buffer_load_dwordx4 v[24:27], v0, s[8:11], s35 offen sc1\n" \
    "buffer_load_dwordx4 v[28:31], v0, s[8:11], s35 offen offset:1024 sc1\n" \
    "s_waitcnt vmcnt(4)\n" \
    "v_max3_u32 v19, v36, v37, v38\n" \
    "s_waitcnt vmcnt(3)\n" \
    "v_max3_u32 v19, v19, v39, v40\n" \
    "v_max3_u32 v19, v19, v41, v42\n" \
    "v_max_u32 v19, v19, v43\n" \
    "s_waitcnt vmcnt(2)\n" \
    "v_mfma_f32_16x16x32_bf16 v[80:83], v[32:35], v[36:39], 0\n" \
    "v_max3_u32 v20, v32, v33, v34\n" \
    "v_mfma_f32_16x16x32_bf16 v[84:87], v[32:35], v[40:43], 0\n" \
    "v_max_u32 v20, v20, v35\n" \
    "s_waitcnt vmcnt(1)\n" \
    "v_smfmac_f32_16x16x64_bf16 v[80:83], v[24:27], v[224:231], v10\n" \
    "v_smfmac_f32_16x16x64_bf16 v[84:87], v[24:27], v[240:247], v10\n" \
    "v_max3_u32 v19, v19, v24, v25\n" \
    "v_max3_u32 v19, v19, v26, v27\n" \
    "s_waitcnt vmcnt(0)\n" \
    "v_smfmac_f32_16x16x64_bf16 v[80:83], v[28:31], v[232:239], v10\n" \
    "v_smfmac_f32_16x16x64_bf16 v[84:87], v[28:31], v[248:255], v10\n" \
    "v_max3_u32 v20, v20, v28, v29\n" \
    "v_max3_u32 v20, v20, v30, v31\n" \
    "v_max_u32 v19, v19, v20\n" \
    "v_cmp_eq_u32 vcc, -1, v19\n" \
    "s_nop 7\n" \
    "s_cbranch_vccz .Lr_cprB1_%=\n" \
    "s_add_u32 s52, s52, 1\n" \
    "s_cmp_lt_u32 s52, s53\n" \
    "s_cbranch_scc1 .Lr_cqB1_%=\n" \
    "s_mov_b32 s53, 0\n" \
    "s_branch .Lr_cprB1_%=\n" \
    ".Lr_ctB1_%=:\n" \
    "s_mov_b32 s52, 0\n" \
    "s_add_u32 s73, s32, s25\n" \
    ".Lr_cuB1_%=:\n" \
    "s_sleep 4\n" \
    "buffer_load_dwordx4 v[44:47], v0, s[8:11], s41 offen offset:0 sc1\n" \
    "buffer_load_dwordx4 v[48:51], v0, s[8:11], s41 offen offset:1024 sc1\n" \
    "buffer_load_dwordx4 v[52:55], v0, s[8:11], s41 offen offset:2048 sc1\n" \
    "buffer_load_dwordx4 v[56:59], v0, s[8:11], s41 offen offset:3072 sc1\n" \
    "buffer_load_dwordx4 v[60:63], v0, s[8:11], s42 offen offset:0 sc1\n" \
    "buffer_load_dwordx4 v[64:67], v0, s[8:11], s42 offen offset:1024 sc1\n" \
    "buffer_load_dwordx4 v[68:71], v0, s[8:11], s42 offen offset:2048 sc1\n" \
    "buffer_load_dwordx4 v[72:75], v0, s[8:11], s42 offen offset:3072 sc1\n" \
    "s_waitcnt vmcnt(7)\n" \
    "v_max3_u32 v19, v44, v45, v46\n" \
    "s_waitcnt vmcnt(6)\n" \
    "v_max3_u32 v19, v19, v47, v48\n" \
    "v_max3_u32 v19, v19, v49, v50\n" \
    "s_waitcnt vmcnt(5)\n" \
    "v_max3_u32 v19, v19, v51, v52\n" \
    "v_max3_u32 v19, v19, v53, v54\n" \
    "s_waitcnt vmcnt(4)\n" \
    "v_max3_u32 v19, v19, v55, v56\n" \
    "v_max3_u32 v19, v19, v57, v58\n" \
    "s_waitcnt vmcnt(3)\n" \
    "v_max3_u32 v19, v19, v59, v60\n" \
    "v_max3_u32 v19, v19, v61, v62\n" \
    "s_waitcnt vmcnt(2)\n" \
    "v_max3_u32 v19, v19, v63, v64\n" \
    "v_max3_u32 v19, v19, v65, v66\n" \
    "s_waitcnt vmcnt(1)\n" \
    "v_max3_u32 v19, v19, v67, v68\n" \
    "v_max3_u32 v19, v19, v69, v70\n" \
    "s_waitcnt vmcnt(0)\n" \
    "v_max3_u32 v19, v19, v71, v72\n" \
    "v_max3_u32 v19, v19, v73, v74\n" \
    "v_max_u32 v19, v19, v75\n" \
    "v_cmp_eq_u32 vcc, -1, v19\n" \
    "s_nop 0\n" \
    "s_cbranch_vccz .Lr_ctrB1_%=\n" \
    "s_add_u32 s52, s52, 1\n" \
    "s_cmp_lt_u32 s52, s53\n" \
    "s_cbranch_scc1 .Lr_cuB1_%=\n" \
    "s_mov_b32 s53, 0\n" \
    "s_branch .Lr_ctrB1_%=\n" \
    ".Lr_cbB1_%=:\n" \
    "s_add_u32 s73, s32, s25\n" \
    "s_sub_u32 s50, s50, 16384\n" \
    "s_mov_b32 s52, 0\n" \
    ".Lr_ccB1_%=:\n" \
    "buffer_load_dword v18, v2, s[8:11], s50 offen sc1\n" \
    "s_waitcnt vmcnt(0)\n" \
    "v_cmp_eq_u32 vcc, 0, v18\n" \
    "s_nop 0\n" \
    "s_and_b64 vcc, vcc, s[56:57]\n" \
    "s_cbranch_scc0 .Lr_cdB1_%=\n" \
    "s_sleep 2\n" \
    "s_add_u32 s52, s52, 1\n" \
    "s_cmp_lt_u32 s52, s53\n" \
    "s_cbranch_scc1 .Lr_ccB1_%=\n" \
    "s_mov_b32 s53, 0\n" \
    "s_branch .Lr_cdB1_%=\n" \
    ".Lr_cdB1_%=:\n" \
    "s_add_u32 s50, s50, 16384\n" \
    "s_and_b32 s33, s49, %[brn]-1\n" \
    "s_mul_i32 s39, s33, 2621440\n" \
    "s_add_u32 s39, s39, s19\n" \
    "s_lshr_b32 s33, s39, 8\n" \
    "s_xor_b32 s33, s33, s27\n" \
    "s_and_b32 s33, s33, 0x2000\n" \
    "s_or_b32 s39, s39, s33\n" \
    "s_add_u32 s40, s39, 65536\n" \
    "s_waitcnt vmcnt(0)\n" \
    "buffer_load_dwordx4 v[192:195], v0, s[8:11], s39 offen offset:0 sc1\n" \
    "buffer_load_dwordx4 v[196:199], v0, s[8:11], s39 offen offset:1024 sc1\n" \
    "buffer_load_dwordx4 v[200:203], v0, s[8:11], s39 offen offset:2048 sc1\n" \
    "buffer_load_dwordx4 v[204:207], v0, s[8:11], s39 offen offset:3072 sc1\n" \
    "buffer_load_dwordx4 v[208:211], v0, s[8:11], s40 offen offset:0 sc1\n" \
    "buffer_load_dwordx4 v[212:215], v0, s[8:11], s40 offen offset:1024 sc1\n" \
    "buffer_load_dwordx4 v[216:219], v0, s[8:11], s40 offen offset:2048 sc1\n" \
    "buffer_load_dwordx4 v[220:223], v0, s[8:11], s40 offen offset:3072 sc1\n" \
    "buffer_load_dword v18, v2, s[8:11], s50 offen sc1\n" \
    "s_waitcnt vmcnt(0)\n" \
    "s_branch .Lr_cbrB1_%=\n" \
    ".Lr_cpC0_%=:\n" \
    "s_mov_b32 s52, 0\n" \
    "s_nop 7\n" \
    ".Lr_cqC0_%=:\n" \
    "buffer_load_dwordx4 v[36:39], v0, s[8:11], s36 offen sc1\n" \
    "buffer_load_dwordx4 v[40:43], v0, s[8:11], s37 offen sc1\n" \
    "buffer_load_dwordx4 v[32:35], v4, s[8:11], s35 offen sc1\n" \
    "buffer_load_dwordx4 v[24:27], v0, s[8:11], s35 offen sc1\n" \
    "buffer_load_dwordx4 v[28:31], v0, s[8:11], s35 offen offset:1024 sc1\n" \
    "s_waitcnt vmcnt(4)\n" \
    "v_max3_u32 v19, v36, v37, v38\n" \
    "s_waitcnt vmcnt(3)\n" \
    "v_max3_u32 v19, v19, v39, v40\n" \
    "v_max3_u32 v19, v19, v41, v42\n" \
    "v_max_u32 v19, v19, v43\n" \
    "s_waitcnt vmcnt(2)\n" \
    "v_mfma_f32_16x16x32_bf16 v[80:83], v[32:35], v[36:39], 0\n" \
    "v_max3_u32 v20, v32, v33, v34\n" \
    "v_mfma_f32_16x16x32_bf16 v[84:87], v[32:35], v[40:43], 0\n" \
    "v_max_u32 v20, v20, v35\n" \
    "s_waitcnt vmcnt(1)\n" \
    "v_smfmac_f32_16x16x64_bf16 v[80:83], v[24:27], v[192:199], v10\n" \
    "v_smfmac_f32_16x16x64_bf16 v[84:87], v[24:27], v[208:215], v10\n" \
    "v_max3_u32 v19, v19, v24, v25\n" \
    "v_max3_u32 v19, v19, v26, v27\n" \
    "s_waitcnt vmcnt(0)\n" \
    "v_smfmac_f32_16x16x64_bf16 v[80:83], v[28:31], v[200:207], v10\n" \
    "v_smfmac_f32_16x16x64_bf16 v[84:87], v[28:31], v[216:223], v10\n" \
    "v_max3_u32 v20, v20, v28, v29\n" \
    "v_max3_u32 v20, v20, v30, v31\n" \
    "v_max_u32 v19, v19, v20\n" \
    "v_cmp_eq_u32 vcc, -1, v19\n" \
    "s_nop 7\n" \
    "s_cbranch_vccz .Lr_cprC0_%=\n" \
    "s_add_u32 s52, s52, 1\n" \
    "s_cmp_lt_u32 s52, s53\n" \
    "s_cbranch_scc1 .Lr_cqC0_%=\n" \
    "s_mov_b32 s53, 0\n" \
    "s_branch .Lr_cprC0_%=\n" \
    ".Lr_ctC0_%=:\n" \
    "s_mov_b32 s52, 0\n" \
    "s_add_u32 s73, s32, s25\n" \
    ".Lr_cuC0_%=:\n" \
    "s_sleep 4\n" \
    "buffer_load_dwordx4 v[44:47], v0, s[8:11], s41 offen offset:0 sc1\n" \
    "buffer_load_dwordx4 v[48:51], v0, s[8:11], s41 offen offset:1024 sc1\n" \
    "buffer_load_dwordx4 v[52:55], v0, s[8:11], s41 offen offset:2048 sc1\n" \
    "buffer_load_dwordx4 v[56:59], v0, s[8:11], s41 offen offset:3072 sc1\n" \
    "buffer_load_dwordx4 v[60:63], v0, s[8:11], s42 offen offset:0 sc1\n" \
    "buffer_load_dwordx4 v[64:67], v0, s[8:11], s42 offen offset:1024 sc1\n" \
    "buffer_load_dwordx4 v[68:71], v0, s[8:11], s42 offen offset:2048 sc1\n" \
    "buffer_load_dwordx4 v[72:75], v0, s[8:11], s42 offen offset:3072 sc1\n" \
    "s_waitcnt vmcnt(7)\n" \
    "v_max3_u32 v19, v44, v45, v46\n" \
    "s_waitcnt vmcnt(6)\n" \
    "v_max3_u32 v19, v19, v47, v48\n" \
    "v_max3_u32 v19, v19, v49, v50\n" \
    "s_waitcnt vmcnt(5)\n" \
    "v_max3_u32 v19, v19, v51, v52\n" \
    "v_max3_u32 v19, v19, v53, v54\n" \
    "s_waitcnt vmcnt(4)\n" \
    "v_max3_u32 v19, v19, v55, v56\n" \
    "v_max3_u32 v19, v19, v57, v58\n" \
    "s_waitcnt vmcnt(3)\n" \
    "v_max3_u32 v19, v19, v59, v60\n" \
    "v_max3_u32 v19, v19, v61, v62\n" \
    "s_waitcnt vmcnt(2)\n" \
    "v_max3_u32 v19, v19, v63, v64\n" \
    "v_max3_u32 v19, v19, v65, v66\n" \
    "s_waitcnt vmcnt(1)\n" \
    "v_max3_u32 v19, v19, v67, v68\n" \
    "v_max3_u32 v19, v19, v69, v70\n" \
    "s_waitcnt vmcnt(0)\n" \
    "v_max3_u32 v19, v19, v71, v72\n" \
    "v_max3_u32 v19, v19, v73, v74\n" \
    "v_max_u32 v19, v19, v75\n" \
    "v_cmp_eq_u32 vcc, -1, v19\n" \
    "s_nop 0\n" \
    "s_cbranch_vccz .Lr_ctrC0_%=\n" \
    "s_add_u32 s52, s52, 1\n" \
    "s_cmp_lt_u32 s52, s53\n" \
    "s_cbranch_scc1 .Lr_cuC0_%=\n" \
    "s_mov_b32 s53, 0\n" \
    "s_branch .Lr_ctrC0_%=\n" \
    ".Lr_cbC0_%=:\n" \
    "s_add_u32 s73, s32, s25\n" \
    "s_sub_u32 s50, s50, 16384\n" \
    "s_mov_b32 s52, 0\n" \
    ".Lr_ccC0_%=:\n" \
    "buffer_load_dword v18, v2, s[8:11], s50 offen sc1\n" \
    "s_waitcnt vmcnt(0)\n" \
    "v_cmp_eq_u32 vcc, 0, v18\n" \
    "s_nop 0\n" \
    "s_and_b64 vcc, vcc, s[56:57]\n" \
    "s_cbranch_scc0 .Lr_cdC0_%=\n" \
    "s_sleep 2\n" \
    "s_add_u32 s52, s52, 1\n" \
    "s_cmp_lt_u32 s52, s53\n" \
    "s_cbranch_scc1 .Lr_ccC0_%=\n" \
    "s_mov_b32 s53, 0\n" \
    "s_branch .Lr_cdC0_%=\n" \
    ".Lr_cdC0_%=:\n" \
    "s_add_u32 s50, s50, 16384\n" \
    "s_and_b32 s33, s49, %[brn]-1\n" \
    "s_mul_i32 s39, s33, 2621440\n" \
    "s_add_u32 s39, s39, s19\n" \
    "s_lshr_b32 s33, s39, 8\n" \
    "s_xor_b32 s33, s33, s27\n" \
    "s_and_b32 s33, s33, 0x2000\n" \
    "s_or_b32 s39, s39, s33\n" \
    "s_add_u32 s40, s39, 65536\n" \
    "s_waitcnt vmcnt(0)\n" \
    "buffer_load_dwordx4 v[224:227], v0, s[8:11], s39 offen offset:0 sc1\n" \
    "buffer_load_dwordx4 v[228:231], v0, s[8:11], s39 offen offset:1024 sc1\n" \
    "buffer_load_dwordx4 v[232:235], v0, s[8:11], s39 offen offset:2048 sc1\n" \
    "buffer_load_dwordx4 v[236:239], v0, s[8:11], s39 offen offset:3072 sc1\n" \
    "buffer_load_dwordx4 v[240:243], v0, s[8:11], s40 offen offset:0 sc1\n" \
    "buffer_load_dwordx4 v[244:247], v0, s[8:11], s40 offen offset:1024 sc1\n" \
    "buffer_load_dwordx4 v[248:251], v0, s[8:11], s40 offen offset:2048 sc1\n" \
    "buffer_load_dwordx4 v[252:255], v0, s[8:11], s40 offen offset:3072 sc1\n" \
    "buffer_load_dword v18, v2, s[8:11], s50 offen sc1\n" \
    "s_waitcnt vmcnt(0)\n" \
    "s_branch .Lr_cbrC0_%=\n" \
    ".Lr_cpC1_%=:\n" \
    "s_mov_b32 s52, 0\n" \
    "s_nop 7\n" \
    ".Lr_cqC1_%=:\n" \
    "buffer_load_dwordx4 v[36:39], v0, s[8:11], s36 offen sc1\n" \
    "buffer_load_dwordx4 v[40:43], v0, s[8:11], s37 offen sc1\n" \
    "buffer_load_dwordx4 v[32:35], v4, s[8:11], s35 offen sc1\n" \
    "buffer_load_dwordx4 v[24:27], v0, s[8:11], s35 offen sc1\n" \
    "buffer_load_dwordx4 v[28:31], v0, s[8:11], s35 offen offset:1024 sc1\n" \
    "s_waitcnt vmcnt(4)\n" \
    "v_max3_u32 v19, v36, v37, v38\n" \
    "s_waitcnt vmcnt(3)\n" \
    "v_max3_u32 v19, v19, v39, v40\n" \
    "v_max3_u32 v19, v19, v41, v42\n" \
    "v_max_u32 v19, v19, v43\n" \
    "s_waitcnt vmcnt(2)\n" \
    "v_mfma_f32_16x16x32_bf16 v[80:83], v[32:35], v[36:39], 0\n" \
    "v_max3_u32 v20, v32, v33, v34\n" \
    "v_mfma_f32_16x16x32_bf16 v[84:87], v[32:35], v[40:43], 0\n" \
    "v_max_u32 v20, v20, v35\n" \
    "s_waitcnt vmcnt(1)\n" \
    "v_smfmac_f32_16x16x64_bf16 v[80:83], v[24:27], v[224:231], v10\n" \
    "v_smfmac_f32_16x16x64_bf16 v[84:87], v[24:27], v[240:247], v10\n" \
    "v_max3_u32 v19, v19, v24, v25\n" \
    "v_max3_u32 v19, v19, v26, v27\n" \
    "s_waitcnt vmcnt(0)\n" \
    "v_smfmac_f32_16x16x64_bf16 v[80:83], v[28:31], v[232:239], v10\n" \
    "v_smfmac_f32_16x16x64_bf16 v[84:87], v[28:31], v[248:255], v10\n" \
    "v_max3_u32 v20, v20, v28, v29\n" \
    "v_max3_u32 v20, v20, v30, v31\n" \
    "v_max_u32 v19, v19, v20\n" \
    "v_cmp_eq_u32 vcc, -1, v19\n" \
    "s_nop 7\n" \
    "s_cbranch_vccz .Lr_cprC1_%=\n" \
    "s_add_u32 s52, s52, 1\n" \
    "s_cmp_lt_u32 s52, s53\n" \
    "s_cbranch_scc1 .Lr_cqC1_%=\n" \
    "s_mov_b32 s53, 0\n" \
    "s_branch .Lr_cprC1_%=\n" \
    ".Lr_ctC1_%=:\n" \
    "s_mov_b32 s52, 0\n" \
    "s_add_u32 s73, s32, s25\n" \
    ".Lr_cuC1_%=:\n" \
    "s_sleep 4\n" \
    "buffer_load_dwordx4 v[44:47], v0, s[8:11], s41 offen offset:0 sc1\n" \
    "buffer_load_dwordx4 v[48:51], v0, s[8:11], s41 offen offset:1024 sc1\n" \
    "buffer_load_dwordx4 v[52:55], v0, s[8:11], s41 offen offset:2048 sc1\n" \
    "buffer_load_dwordx4 v[56:59], v0, s[8:11], s41 offen offset:3072 sc1\n" \
    "buffer_load_dwordx4 v[60:63], v0, s[8:11], s42 offen offset:0 sc1\n" \
    "buffer_load_dwordx4 v[64:67], v0, s[8:11], s42 offen offset:1024 sc1\n" \
    "buffer_load_dwordx4 v[68:71], v0, s[8:11], s42 offen offset:2048 sc1\n" \
    "buffer_load_dwordx4 v[72:75], v0, s[8:11], s42 offen offset:3072 sc1\n" \
    "s_waitcnt vmcnt(7)\n" \
    "v_max3_u32 v19, v44, v45, v46\n" \
    "s_waitcnt vmcnt(6)\n" \
    "v_max3_u32 v19, v19, v47, v48\n" \
    "v_max3_u32 v19, v19, v49, v50\n" \
    "s_waitcnt vmcnt(5)\n" \
    "v_max3_u32 v19, v19, v51, v52\n" \
    "v_max3_u32 v19, v19, v53, v54\n" \
    "s_waitcnt vmcnt(4)\n" \
    "v_max3_u32 v19, v19, v55, v56\n" \
    "v_max3_u32 v19, v19, v57, v58\n" \
    "s_waitcnt vmcnt(3)\n" \
    "v_max3_u32 v19, v19, v59, v60\n" \
    "v_max3_u32 v19, v19, v61, v62\n" \
    "s_waitcnt vmcnt(2)\n" \
    "v_max3_u32 v19, v19, v63, v64\n" \
    "v_max3_u32 v19, v19, v65, v66\n" \
    "s_waitcnt vmcnt(1)\n" \
    "v_max3_u32 v19, v19, v67, v68\n" \
    "v_max3_u32 v19, v19, v69, v70\n" \
    "s_waitcnt vmcnt(0)\n" \
    "v_max3_u32 v19, v19, v71, v72\n" \
    "v_max3_u32 v19, v19, v73, v74\n" \
    "v_max_u32 v19, v19, v75\n" \
    "v_cmp_eq_u32 vcc, -1, v19\n" \
    "s_nop 0\n" \
    "s_cbranch_vccz .Lr_ctrC1_%=\n" \
    "s_add_u32 s52, s52, 1\n" \
    "s_cmp_lt_u32 s52, s53\n" \
    "s_cbranch_scc1 .Lr_cuC1_%=\n" \
    "s_mov_b32 s53, 0\n" \
    "s_branch .Lr_ctrC1_%=\n" \
    ".Lr_cbC1_%=:\n" \
    "s_add_u32 s73, s32, s25\n" \
    "s_sub_u32 s50, s50, 16384\n" \
    "s_mov_b32 s52, 0\n" \
    ".Lr_ccC1_%=:\n" \
    "buffer_load_dword v18, v2, s[8:11], s50 offen sc1\n" \
    "s_waitcnt vmcnt(0)\n" \
    "v_cmp_eq_u32 vcc, 0, v18\n" \
    "s_nop 0\n" \
    "s_and_b64 vcc, vcc, s[56:57]\n" \
    "s_cbranch_scc0 .Lr_cdC1_%=\n" \
    "s_sleep 2\n" \
    "s_add_u32 s52, s52, 1\n" \
    "s_cmp_lt_u32 s52, s53\n" \
    "s_cbranch_scc1 .Lr_ccC1_%=\n" \
    "s_mov_b32 s53, 0\n" \
    "s_branch .Lr_cdC1_%=\n" \
    ".Lr_cdC1_%=:\n" \
    "s_add_u32 s50, s50, 16384\n" \
    "s_and_b32 s33, s49, %[brn]-1\n" \
    "s_mul_i32 s39, s33, 2621440\n" \
    "s_add_u32 s39, s39, s19\n" \
    "s_lshr_b32 s33, s39, 8\n" \
    "s_xor_b32 s33, s33, s27\n" \
    "s_and_b32 s33, s33, 0x2000\n" \
    "s_or_b32 s39, s39, s33\n" \
    "s_add_u32 s40, s39, 65536\n" \
    "s_waitcnt vmcnt(0)\n" \
    "buffer_load_dwordx4 v[192:195], v0, s[8:11], s39 offen offset:0 sc1\n" \
    "buffer_load_dwordx4 v[196:199], v0, s[8:11], s39 offen offset:1024 sc1\n" \
    "buffer_load_dwordx4 v[200:203], v0, s[8:11], s39 offen offset:2048 sc1\n" \
    "buffer_load_dwordx4 v[204:207], v0, s[8:11], s39 offen offset:3072 sc1\n" \
    "buffer_load_dwordx4 v[208:211], v0, s[8:11], s40 offen offset:0 sc1\n" \
    "buffer_load_dwordx4 v[212:215], v0, s[8:11], s40 offen offset:1024 sc1\n" \
    "buffer_load_dwordx4 v[216:219], v0, s[8:11], s40 offen offset:2048 sc1\n" \
    "buffer_load_dwordx4 v[220:223], v0, s[8:11], s40 offen offset:3072 sc1\n" \
    "buffer_load_dword v18, v2, s[8:11], s50 offen sc1\n" \
    "s_waitcnt vmcnt(0)\n" \
    "s_branch .Lr_cbrC1_%=\n" \
    ""
#define G_ASM \
    "s_mov_b32 s32, s24\n" \
    "s_mov_b32 s33, s27\n" \
    "s_mov_b32 s34, -1\n" \
    "s_mov_b32 s35, -1\n" \
    "s_mov_b32 s37, 0\n" \
    "s_mov_b32 s45, 0x80000000\n" \
    "s_mov_b32 s38, 0x80000000\n" \
    "s_mov_b32 s41, 0\n" \
    "s_mov_b32 s42, 0\n" \
    "s_mov_b32 s43, 0\n" \
    "s_mov_b32 s46, 0\n" \
    "s_mov_b32 s48, 0\n" \
    "v_mov_b32 v165, 81920\n" \
    "v_mov_b32 v168, 1\n" \
    "v_mov_b32 v169, 1\n" \
    "v_mov_b32 v170, 1\n" \
    "v_mov_b32 v171, 1\n" \
    "s_cmp_eq_u32 s16, 0\n" \
    "s_cbranch_scc1 .Lg_lS_%=\n" \
    "s_cmp_lt_u32 s16, 4\n" \
    "s_cbranch_scc1 .Lg_lO_%=\n" \
    ".Lg_lY_%=:\n" \
    "s_barrier\n" \
    "ds_read_b32 v167, v165\n" \
    "s_branch .Lg_nt_%=\n" \
    ".Lg_lZ_%=:\n" \
    "s_barrier\n" \
    "s_waitcnt lgkmcnt(3)\n" \
    "v_mfma_f32_16x16x32_bf16 v[208:211], v[64:67], v[176:179], v[208:211]\n" \
    "v_mfma_f32_16x16x32_bf16 v[212:215], v[144:147], v[176:179], v[212:215]\n" \
    "s_waitcnt lgkmcnt(0)\n" \
    "ds_read_b32 v167, v165\n" \
    "s_waitcnt lgkmcnt(3)\n" \
    "v_mfma_f32_16x16x32_bf16 v[208:211], v[68:71], v[180:183], v[208:211]\n" \
    "v_mfma_f32_16x16x32_bf16 v[212:215], v[148:151], v[180:183], v[212:215]\n" \
    "s_waitcnt lgkmcnt(2)\n" \
    "v_mfma_f32_16x16x32_bf16 v[208:211], v[72:75], v[184:187], v[208:211]\n" \
    "v_mfma_f32_16x16x32_bf16 v[212:215], v[152:155], v[184:187], v[212:215]\n" \
    "s_waitcnt lgkmcnt(1)\n" \
    "v_mfma_f32_16x16x32_bf16 v[208:211], v[76:79], v[188:191], v[208:211]\n" \
    "v_mfma_f32_16x16x32_bf16 v[212:215], v[156:159], v[188:191], v[212:215]\n" \
    ".Lg_nt_%=:\n" \
    "v_xor_b32 v165, 4, v165\n" \
    "s_waitcnt lgkmcnt(0)\n" \
    "v_readfirstlane_b32 s44, v167\n" \
    "s_cmp_lt_u32 s32, s25\n" \
    "s_cselect_b32 s36, s32, -1\n" \
    "s_add_u32 s39, s33, 128\n" \
    "s_cmp_lt_u32 s33, s44\n" \
    "s_cselect_b32 s36, s39, s36\n" \
    "s_addc_u32 s33, s33, 0\n" \
    "s_cmp_lt_u32 s36, 128\n" \
    "s_addc_u32 s32, s32, 0\n" \
    "v_cvt_pk_bf16_f32 v172, v208, v209\n" \
    "v_cvt_pk_bf16_f32 v173, v210, v211\n" \
    "v_cvt_pk_bf16_f32 v174, v212, v213\n" \
    "v_cvt_pk_bf16_f32 v175, v214, v215\n" \
    "buffer_store_dwordx4 v[172:175], v160, s[8:11], s45 offen sc1\n" \
    "s_lshr_b32 s39, s37, 1\n" \
    "s_mul_i32 s38, s39, s21\n" \
    "s_add_u32 s38, s38, s20\n" \
    "s_bitcmp1_b32 s37, 0\n" \
    "s_cselect_b32 s38, s38, 0x80000000\n" \
    "s_add_u32 s39, s41, 40960\n" \
    "s_cmp_ge_u32 s39, 81920\n" \
    "s_cselect_b32 s40, 81920, 0\n" \
    "s_sub_u32 s39, s39, s40\n" \
    "s_add_u32 s39, s39, s23\n" \
    "s_mov_b32 m0, s39\n" \
    "s_mul_i32 s40, s36, 20480\n" \
    "s_add_u32 s40, s40, s22\n" \
    "s_cmp_eq_u32 s36, -1\n" \
    "s_cselect_b32 s40, 0x80000000, s40\n" \
    "buffer_load_dwordx4 v160, s[8:11], s40 offen offset:0 sc1 lds\n" \
    "buffer_load_dwordx4 v160, s[8:11], s40 offen offset:1024 sc1 lds\n" \
    "buffer_load_dwordx4 v160, s[8:11], s40 offen offset:2048 sc1 lds\n" \
    "buffer_load_dwordx4 v160, s[8:11], s40 offen offset:3072 sc1 lds\n" \
    "s_add_u32 m0, s39, 4096\n" \
    "s_add_u32 s40, s40, 4096\n" \
    "buffer_load_dwordx4 v160, s[8:11], s40 offen sc1 lds\n" \
    "v_add_u32 v164, s41, v160\n" \
    "s_setprio 0\n" \
    "s_mov_b32 s37, s34\n" \
    "s_cmp_eq_u32 s34, -1\n" \
    "s_cbranch_scc1 .Lg_eY_%=\n" \
    "ds_read_b128 v[176:179], v164 offset:0\n" \
    "ds_read_b128 v[180:183], v164 offset:1024\n" \
    "ds_read_b128 v[184:187], v164 offset:2048\n" \
    "ds_read_b128 v[188:191], v164 offset:3072\n" \
    "ds_read_b128 v[192:195], v164 offset:4096\n" \
    "s_lshr_b32 s39, s37, 1\n" \
    "s_cmp_gt_u32 s39, 63\n" \
    "s_cselect_b32 s40, %[rmask], 63\n" \
    "s_and_b32 s40, s40, s39\n" \
    "s_mul_i32 s45, s40, 1572864\n" \
    "s_bitcmp1_b32 s37, 0\n" \
    "s_cselect_b32 s40, s18, s17\n" \
    "s_add_u32 s45, s45, s40\n" \
    "s_lshr_b32 s40, s45, 8\n" \
    "s_xor_b32 s40, s40, s19\n" \
    "s_and_b32 s40, s40, 0x2000\n" \
    "s_or_b32 s45, s45, s40\n" \
    "s_mov_b32 s34, s35\n" \
    "s_mov_b32 s35, s36\n" \
    "s_add_u32 s41, s41, 20480\n" \
    "s_cmp_eq_u32 s41, 81920\n" \
    "s_cselect_b32 s41, 0, s41\n" \
    "s_mov_b32 s46, 0\n" \
    "s_waitcnt lgkmcnt(4)\n" \
    "v_mfma_f32_16x16x32_bf16 v[208:211], v[0:3], v[176:179], 0\n" \
    "ds_read_b128 v[196:199], v164 offset:5120\n" \
    "v_mfma_f32_16x16x32_bf16 v[212:215], v[80:83], v[176:179], 0\n" \
    "s_waitcnt lgkmcnt(4)\n" \
    "v_mfma_f32_16x16x32_bf16 v[208:211], v[4:7], v[180:183], v[208:211]\n" \
    "ds_read_b128 v[200:203], v164 offset:6144\n" \
    "v_mfma_f32_16x16x32_bf16 v[212:215], v[84:87], v[180:183], v[212:215]\n" \
    "s_waitcnt lgkmcnt(4)\n" \
    "v_mfma_f32_16x16x32_bf16 v[208:211], v[8:11], v[184:187], v[208:211]\n" \
    "ds_read_b128 v[204:207], v164 offset:7168\n" \
    "v_mfma_f32_16x16x32_bf16 v[212:215], v[88:91], v[184:187], v[212:215]\n" \
    "s_waitcnt lgkmcnt(4)\n" \
    "v_mfma_f32_16x16x32_bf16 v[208:211], v[12:15], v[188:191], v[208:211]\n" \
    "ds_read_b128 v[176:179], v164 offset:8192\n" \
    "v_mfma_f32_16x16x32_bf16 v[212:215], v[92:95], v[188:191], v[212:215]\n" \
    "s_waitcnt lgkmcnt(4)\n" \
    "v_mfma_f32_16x16x32_bf16 v[208:211], v[16:19], v[192:195], v[208:211]\n" \
    "ds_read_b128 v[180:183], v164 offset:9216\n" \
    "v_mfma_f32_16x16x32_bf16 v[212:215], v[96:99], v[192:195], v[212:215]\n" \
    "s_waitcnt lgkmcnt(4)\n" \
    "v_mfma_f32_16x16x32_bf16 v[208:211], v[20:23], v[196:199], v[208:211]\n" \
    "ds_read_b128 v[184:187], v164 offset:10240\n" \
    "v_mfma_f32_16x16x32_bf16 v[212:215], v[100:103], v[196:199], v[212:215]\n" \
    "s_waitcnt lgkmcnt(4)\n" \
    "v_mfma_f32_16x16x32_bf16 v[208:211], v[24:27], v[200:203], v[208:211]\n" \
    "ds_read_b128 v[188:191], v164 offset:11264\n" \
    "v_mfma_f32_16x16x32_bf16 v[212:215], v[104:107], v[200:203], v[212:215]\n" \
    "s_waitcnt lgkmcnt(4)\n" \
    "v_mfma_f32_16x16x32_bf16 v[208:211], v[28:31], v[204:207], v[208:211]\n" \
    "ds_read_b128 v[192:195], v164 offset:12288\n" \
    "v_mfma_f32_16x16x32_bf16 v[212:215], v[108:111], v[204:207], v[212:215]\n" \
    "s_waitcnt lgkmcnt(4)\n" \
    "v_mfma_f32_16x16x32_bf16 v[208:211], v[32:35], v[176:179], v[208:211]\n" \
    "ds_read_b128 v[196:199], v164 offset:13312\n" \
    "v_mfma_f32_16x16x32_bf16 v[212:215], v[112:115], v[176:179], v[212:215]\n" \
    "s_waitcnt lgkmcnt(4)\n" \
    "v_mfma_f32_16x16x32_bf16 v[208:211], v[36:39], v[180:183], v[208:211]\n" \
    "ds_read_b128 v[200:203], v164 offset:14336\n" \
    "v_mfma_f32_16x16x32_bf16 v[212:215], v[116:119], v[180:183], v[212:215]\n" \
    "s_waitcnt lgkmcnt(4)\n" \
    "v_mfma_f32_16x16x32_bf16 v[208:211], v[40:43], v[184:187], v[208:211]\n" \
    "ds_read_b128 v[204:207], v164 offset:15360\n" \
    "v_mfma_f32_16x16x32_bf16 v[212:215], v[120:123], v[184:187], v[212:215]\n" \
    "s_waitcnt lgkmcnt(4)\n" \
    "v_mfma_f32_16x16x32_bf16 v[208:211], v[44:47], v[188:191], v[208:211]\n" \
    "ds_read_b128 v[176:179], v164 offset:16384\n" \
    "v_mfma_f32_16x16x32_bf16 v[212:215], v[124:127], v[188:191], v[212:215]\n" \
    "s_waitcnt lgkmcnt(4)\n" \
    "v_mfma_f32_16x16x32_bf16 v[208:211], v[48:51], v[192:195], v[208:211]\n" \
    "ds_read_b128 v[180:183], v164 offset:17408\n" \
    "v_mfma_f32_16x16x32_bf16 v[212:215], v[128:131], v[192:195], v[212:215]\n" \
    "s_waitcnt lgkmcnt(4)\n" \
    "v_mfma_f32_16x16x32_bf16 v[208:211], v[52:55], v[196:199], v[208:211]\n" \
    "ds_read_b128 v[184:187], v164 offset:18432\n" \
    "v_mfma_f32_16x16x32_bf16 v[212:215], v[132:135], v[196:199], v[212:215]\n" \
    "s_waitcnt vmcnt(5)\n" \
    "buffer_store_dwordx4 v[168:171], v161, s[8:11], s38 offen sc1\n" \
    "s_mov_b32 s38, 0x80000000\n" \
    "s_waitcnt lgkmcnt(4)\n" \
    "v_mfma_f32_16x16x32_bf16 v[208:211], v[56:59], v[200:203], v[208:211]\n" \
    "ds_read_b128 v[188:191], v164 offset:19456\n" \
    "v_mfma_f32_16x16x32_bf16 v[212:215], v[136:139], v[200:203], v[212:215]\n" \
    "s_setprio 1\n" \
    "s_waitcnt lgkmcnt(4)\n" \
    "v_mfma_f32_16x16x32_bf16 v[208:211], v[60:63], v[204:207], v[208:211]\n" \
    "v_mfma_f32_16x16x32_bf16 v[212:215], v[140:143], v[204:207], v[212:215]\n" \
    "s_branch .Lg_lZ_%=\n" \
    ".Lg_eY_%=:\n" \
    "s_waitcnt vmcnt(0)\n" \
    "buffer_store_dwordx4 v[168:171], v161, s[8:11], s38 offen sc1\n" \
    "s_mov_b32 s38, 0x80000000\n" \
    "s_mov_b32 s34, s35\n" \
    "s_mov_b32 s35, s36\n" \
    "s_add_u32 s41, s41, 20480\n" \
    "s_cmp_eq_u32 s41, 81920\n" \
    "s_cselect_b32 s41, 0, s41\n" \
    "s_mov_b32 s45, 0x80000000\n" \
    "s_mov_b32 s37, 0\n" \
    "s_and_b32 s39, s34, s35\n" \
    "s_add_u32 s40, s32, s33\n" \
    "s_cmp_eq_u32 s39, -1\n" \
    "s_cselect_b32 s40, s40, 0\n" \
    "s_cmp_eq_u32 s40, s26\n" \
    "s_cbranch_scc1 .Lg_end_%=\n" \
    "s_add_u32 s46, s46, 1\n" \
    "s_cmp_lt_u32 s46, 0x20000\n" \
    "s_cbranch_scc1 .Lg_lY_%=\n" \
    "s_branch .Lg_end_%=\n" \
    ".Lg_lO_%=:\n" \
    "v_add_u32 v164, s41, v160\n" \
    "s_cmp_eq_u32 s34, -1\n" \
    "s_barrier\n" \
    "s_cbranch_scc1 .Lg_eO_%=\n" \
    "ds_read_b128 v[176:179], v164 offset:0\n" \
    "ds_read_b128 v[180:183], v164 offset:1024\n" \
    "ds_read_b128 v[184:187], v164 offset:2048\n" \
    "ds_read_b128 v[188:191], v164 offset:3072\n" \
    "ds_read_b128 v[192:195], v164 offset:4096\n" \
    "ds_read_b32 v167, v165\n" \
    "s_lshr_b32 s39, s34, 1\n" \
    "s_cmp_gt_u32 s39, 63\n" \
    "s_cselect_b32 s40, %[rmask], 63\n" \
    "s_and_b32 s40, s40, s39\n" \
    "s_mul_i32 s45, s40, 1572864\n" \
    "s_bitcmp1_b32 s34, 0\n" \
    "s_cselect_b32 s40, s18, s17\n" \
    "s_add_u32 s45, s45, s40\n" \
    "s_lshr_b32 s40, s45, 8\n" \
    "s_xor_b32 s40, s40, s19\n" \
    "s_and_b32 s40, s40, 0x2000\n" \
    "s_or_b32 s45, s45, s40\n" \
    "s_waitcnt lgkmcnt(5)\n" \
    "v_mfma_f32_16x16x32_bf16 v[208:211], v[0:3], v[176:179], 0\n" \
    "ds_read_b128 v[196:199], v164 offset:5120\n" \
    "v_mfma_f32_16x16x32_bf16 v[212:215], v[80:83], v[176:179], 0\n" \
    "s_waitcnt lgkmcnt(5)\n" \
    "v_mfma_f32_16x16x32_bf16 v[208:211], v[4:7], v[180:183], v[208:211]\n" \
    "ds_read_b128 v[200:203], v164 offset:6144\n" \
    "v_mfma_f32_16x16x32_bf16 v[212:215], v[84:87], v[180:183], v[212:215]\n" \
    "s_waitcnt lgkmcnt(5)\n" \
    "v_mfma_f32_16x16x32_bf16 v[208:211], v[8:11], v[184:187], v[208:211]\n" \
    "ds_read_b128 v[204:207], v164 offset:7168\n" \
    "v_mfma_f32_16x16x32_bf16 v[212:215], v[88:91], v[184:187], v[212:215]\n" \
    "s_waitcnt lgkmcnt(5)\n" \
    "v_mfma_f32_16x16x32_bf16 v[208:211], v[12:15], v[188:191], v[208:211]\n" \
    "ds_read_b128 v[176:179], v164 offset:8192\n" \
    "v_mfma_f32_16x16x32_bf16 v[212:215], v[92:95], v[188:191], v[212:215]\n" \
    "s_waitcnt lgkmcnt(5)\n" \
    "v_mfma_f32_16x16x32_bf16 v[208:211], v[16:19], v[192:195], v[208:211]\n" \
    "ds_read_b128 v[180:183], v164 offset:9216\n" \
    "v_mfma_f32_16x16x32_bf16 v[212:215], v[96:99], v[192:195], v[212:215]\n" \
    "s_waitcnt lgkmcnt(4)\n" \
    "v_mfma_f32_16x16x32_bf16 v[208:211], v[20:23], v[196:199], v[208:211]\n" \
    "ds_read_b128 v[184:187], v164 offset:10240\n" \
    "v_mfma_f32_16x16x32_bf16 v[212:215], v[100:103], v[196:199], v[212:215]\n" \
    "s_waitcnt lgkmcnt(4)\n" \
    "v_mfma_f32_16x16x32_bf16 v[208:211], v[24:27], v[200:203], v[208:211]\n" \
    "ds_read_b128 v[188:191], v164 offset:11264\n" \
    "v_mfma_f32_16x16x32_bf16 v[212:215], v[104:107], v[200:203], v[212:215]\n" \
    "s_waitcnt lgkmcnt(4)\n" \
    "v_mfma_f32_16x16x32_bf16 v[208:211], v[28:31], v[204:207], v[208:211]\n" \
    "ds_read_b128 v[192:195], v164 offset:12288\n" \
    "v_mfma_f32_16x16x32_bf16 v[212:215], v[108:111], v[204:207], v[212:215]\n" \
    "s_waitcnt lgkmcnt(4)\n" \
    "v_mfma_f32_16x16x32_bf16 v[208:211], v[32:35], v[176:179], v[208:211]\n" \
    "ds_read_b128 v[196:199], v164 offset:13312\n" \
    "v_mfma_f32_16x16x32_bf16 v[212:215], v[112:115], v[176:179], v[212:215]\n" \
    "s_waitcnt lgkmcnt(4)\n" \
    "v_mfma_f32_16x16x32_bf16 v[208:211], v[36:39], v[180:183], v[208:211]\n" \
    "ds_read_b128 v[200:203], v164 offset:14336\n" \
    "v_mfma_f32_16x16x32_bf16 v[212:215], v[116:119], v[180:183], v[212:215]\n" \
    "s_waitcnt lgkmcnt(4)\n" \
    "v_mfma_f32_16x16x32_bf16 v[208:211], v[40:43], v[184:187], v[208:211]\n" \
    "ds_read_b128 v[204:207], v164 offset:15360\n" \
    "v_mfma_f32_16x16x32_bf16 v[212:215], v[120:123], v[184:187], v[212:215]\n" \
    "s_waitcnt lgkmcnt(4)\n" \
    "v_mfma_f32_16x16x32_bf16 v[208:211], v[44:47], v[188:191], v[208:211]\n" \
    "ds_read_b128 v[176:179], v164 offset:16384\n" \
    "v_mfma_f32_16x16x32_bf16 v[212:215], v[124:127], v[188:191], v[212:215]\n" \
    "s_waitcnt lgkmcnt(4)\n" \
    "v_mfma_f32_16x16x32_bf16 v[208:211], v[48:51], v[192:195], v[208:211]\n" \
    "ds_read_b128 v[180:183], v164 offset:17408\n" \
    "v_mfma_f32_16x16x32_bf16 v[212:215], v[128:131], v[192:195], v[212:215]\n" \
    "s_waitcnt lgkmcnt(4)\n" \
    "v_mfma_f32_16x16x32_bf16 v[208:211], v[52:55], v[196:199], v[208:211]\n" \
    "ds_read_b128 v[184:187], v164 offset:18432\n" \
    "v_mfma_f32_16x16x32_bf16 v[212:215], v[132:135], v[196:199], v[212:215]\n" \
    "s_waitcnt lgkmcnt(4)\n" \
    "v_mfma_f32_16x16x32_bf16 v[208:211], v[56:59], v[200:203], v[208:211]\n" \
    "ds_read_b128 v[188:191], v164 offset:19456\n" \
    "v_mfma_f32_16x16x32_bf16 v[212:215], v[136:139], v[200:203], v[212:215]\n" \
    "s_waitcnt lgkmcnt(4)\n" \
    "v_mfma_f32_16x16x32_bf16 v[208:211], v[60:63], v[204:207], v[208:211]\n" \
    "v_mfma_f32_16x16x32_bf16 v[212:215], v[140:143], v[204:207], v[212:215]\n" \
    "s_waitcnt lgkmcnt(3)\n" \
    "v_mfma_f32_16x16x32_bf16 v[208:211], v[64:67], v[176:179], v[208:211]\n" \
    "v_mfma_f32_16x16x32_bf16 v[212:215], v[144:147], v[176:179], v[212:215]\n" \
    "s_waitcnt lgkmcnt(2)\n" \
    "v_mfma_f32_16x16x32_bf16 v[208:211], v[68:71], v[180:183], v[208:211]\n" \
    "v_mfma_f32_16x16x32_bf16 v[212:215], v[148:151], v[180:183], v[212:215]\n" \
    "s_waitcnt lgkmcnt(1)\n" \
    "v_mfma_f32_16x16x32_bf16 v[208:211], v[72:75], v[184:187], v[208:211]\n" \
    "v_mfma_f32_16x16x32_bf16 v[212:215], v[152:155], v[184:187], v[212:215]\n" \
    "s_waitcnt vmcnt(0)\n" \
    "buffer_store_dwordx4 v[168:171], v161, s[8:11], s38 offen sc1\n" \
    "s_mov_b32 s38, 0x80000000\n" \
    "s_waitcnt lgkmcnt(0)\n" \
    "v_mfma_f32_16x16x32_bf16 v[208:211], v[76:79], v[188:191], v[208:211]\n" \
    "v_mfma_f32_16x16x32_bf16 v[212:215], v[156:159], v[188:191], v[212:215]\n" \
    "v_readfirstlane_b32 s44, v167\n" \
    "s_cmp_lt_u32 s32, s25\n" \
    "s_cselect_b32 s36, s32, -1\n" \
    "s_add_u32 s39, s33, 128\n" \
    "s_cmp_lt_u32 s33, s44\n" \
    "s_cselect_b32 s36, s39, s36\n" \
    "s_addc_u32 s33, s33, 0\n" \
    "s_cmp_lt_u32 s36, 128\n" \
    "s_addc_u32 s32, s32, 0\n" \
    "v_cvt_pk_bf16_f32 v172, v208, v209\n" \
    "v_cvt_pk_bf16_f32 v173, v210, v211\n" \
    "v_cvt_pk_bf16_f32 v174, v212, v213\n" \
    "v_cvt_pk_bf16_f32 v175, v214, v215\n" \
    "buffer_store_dwordx4 v[172:175], v160, s[8:11], s45 offen sc1\n" \
    "s_lshr_b32 s39, s34, 1\n" \
    "s_mul_i32 s38, s39, s21\n" \
    "s_add_u32 s38, s38, s20\n" \
    "s_bitcmp1_b32 s34, 0\n" \
    "s_cselect_b32 s38, s38, 0x80000000\n" \
    "v_xor_b32 v165, 4, v165\n" \
    "s_mov_b32 s46, 0\n" \
    "s_mov_b32 s34, s35\n" \
    "s_mov_b32 s35, s36\n" \
    "s_add_u32 s41, s41, 20480\n" \
    "s_cmp_eq_u32 s41, 81920\n" \
    "s_cselect_b32 s41, 0, s41\n" \
    "s_waitcnt lgkmcnt(0)\n" \
    "s_branch .Lg_lO_%=\n" \
    ".Lg_eO_%=:\n" \
    "ds_read_b32 v167, v165\n" \
    "s_waitcnt vmcnt(0)\n" \
    "buffer_store_dwordx4 v[168:171], v161, s[8:11], s38 offen sc1\n" \
    "s_mov_b32 s38, 0x80000000\n" \
    "s_waitcnt lgkmcnt(0)\n" \
    "v_readfirstlane_b32 s44, v167\n" \
    "s_cmp_lt_u32 s32, s25\n" \
    "s_cselect_b32 s36, s32, -1\n" \
    "s_add_u32 s39, s33, 128\n" \
    "s_cmp_lt_u32 s33, s44\n" \
    "s_cselect_b32 s36, s39, s36\n" \
    "s_addc_u32 s33, s33, 0\n" \
    "s_cmp_lt_u32 s36, 128\n" \
    "s_addc_u32 s32, s32, 0\n" \
    "v_xor_b32 v165, 4, v165\n" \
    "s_mov_b32 s34, s35\n" \
    "s_mov_b32 s35, s36\n" \
    "s_add_u32 s41, s41, 20480\n" \
    "s_cmp_eq_u32 s41, 81920\n" \
    "s_cselect_b32 s41, 0, s41\n" \
    "s_waitcnt lgkmcnt(0)\n" \
    "s_and_b32 s39, s34, s35\n" \
    "s_add_u32 s40, s32, s33\n" \
    "s_cmp_eq_u32 s39, -1\n" \
    "s_cselect_b32 s40, s40, 0\n" \
    "s_cmp_eq_u32 s40, s26\n" \
    "s_cbranch_scc1 .Lg_end_%=\n" \
    "s_add_u32 s46, s46, 1\n" \
    "s_cmp_lt_u32 s46, 0x20000\n" \
    "s_cbranch_scc1 .Lg_lO_%=\n" \
    "s_branch .Lg_end_%=\n" \
    ".Lg_lS_%=:\n" \
    "v_add_u32 v164, s41, v160\n" \
    "s_cmp_eq_u32 s34, -1\n" \
    "s_barrier\n" \
    "s_cbranch_scc1 .Lg_eS_%=\n" \
    "ds_read_b128 v[176:179], v164 offset:0\n" \
    "ds_read_b128 v[180:183], v164 offset:1024\n" \
    "ds_read_b128 v[184:187], v164 offset:2048\n" \
    "ds_read_b128 v[188:191], v164 offset:3072\n" \
    "ds_read_b128 v[192:195], v164 offset:4096\n" \
    "ds_read_b32 v167, v165\n" \
    "s_lshr_b32 s39, s34, 1\n" \
    "s_cmp_gt_u32 s39, 63\n" \
    "s_cselect_b32 s40, %[rmask], 63\n" \
    "s_and_b32 s40, s40, s39\n" \
    "s_mul_i32 s45, s40, 1572864\n" \
    "s_bitcmp1_b32 s34, 0\n" \
    "s_cselect_b32 s40, s18, s17\n" \
    "s_add_u32 s45, s45, s40\n" \
    "s_lshr_b32 s40, s45, 8\n" \
    "s_xor_b32 s40, s40, s19\n" \
    "s_and_b32 s40, s40, 0x2000\n" \
    "s_or_b32 s45, s45, s40\n" \
    "s_waitcnt lgkmcnt(5)\n" \
    "v_mfma_f32_16x16x32_bf16 v[208:211], v[0:3], v[176:179], 0\n" \
    "ds_read_b128 v[196:199], v164 offset:5120\n" \
    "v_mfma_f32_16x16x32_bf16 v[212:215], v[80:83], v[176:179], 0\n" \
    "s_lshl_b32 s39, s43, 8\n" \
    "buffer_load_dword v166, v162, s[8:11], s39 offen sc1\n" \
    "s_waitcnt lgkmcnt(5)\n" \
    "v_mfma_f32_16x16x32_bf16 v[208:211], v[4:7], v[180:183], v[208:211]\n" \
    "ds_read_b128 v[200:203], v164 offset:6144\n" \
    "v_mfma_f32_16x16x32_bf16 v[212:215], v[84:87], v[180:183], v[212:215]\n" \
    "s_waitcnt lgkmcnt(5)\n" \
    "v_mfma_f32_16x16x32_bf16 v[208:211], v[8:11], v[184:187], v[208:211]\n" \
    "ds_read_b128 v[204:207], v164 offset:7168\n" \
    "v_mfma_f32_16x16x32_bf16 v[212:215], v[88:91], v[184:187], v[212:215]\n" \
    "s_waitcnt lgkmcnt(5)\n" \
    "v_mfma_f32_16x16x32_bf16 v[208:211], v[12:15], v[188:191], v[208:211]\n" \
    "ds_read_b128 v[176:179], v164 offset:8192\n" \
    "v_mfma_f32_16x16x32_bf16 v[212:215], v[92:95], v[188:191], v[212:215]\n" \
    "s_waitcnt lgkmcnt(5)\n" \
    "v_mfma_f32_16x16x32_bf16 v[208:211], v[16:19], v[192:195], v[208:211]\n" \
    "ds_read_b128 v[180:183], v164 offset:9216\n" \
    "v_mfma_f32_16x16x32_bf16 v[212:215], v[96:99], v[192:195], v[212:215]\n" \
    "s_waitcnt lgkmcnt(4)\n" \
    "v_mfma_f32_16x16x32_bf16 v[208:211], v[20:23], v[196:199], v[208:211]\n" \
    "ds_read_b128 v[184:187], v164 offset:10240\n" \
    "v_mfma_f32_16x16x32_bf16 v[212:215], v[100:103], v[196:199], v[212:215]\n" \
    "s_waitcnt lgkmcnt(4)\n" \
    "v_mfma_f32_16x16x32_bf16 v[208:211], v[24:27], v[200:203], v[208:211]\n" \
    "ds_read_b128 v[188:191], v164 offset:11264\n" \
    "v_mfma_f32_16x16x32_bf16 v[212:215], v[104:107], v[200:203], v[212:215]\n" \
    "s_waitcnt lgkmcnt(4)\n" \
    "v_mfma_f32_16x16x32_bf16 v[208:211], v[28:31], v[204:207], v[208:211]\n" \
    "ds_read_b128 v[192:195], v164 offset:12288\n" \
    "v_mfma_f32_16x16x32_bf16 v[212:215], v[108:111], v[204:207], v[212:215]\n" \
    "s_waitcnt lgkmcnt(4)\n" \
    "v_mfma_f32_16x16x32_bf16 v[208:211], v[32:35], v[176:179], v[208:211]\n" \
    "ds_read_b128 v[196:199], v164 offset:13312\n" \
    "v_mfma_f32_16x16x32_bf16 v[212:215], v[112:115], v[176:179], v[212:215]\n" \
    "s_waitcnt lgkmcnt(4)\n" \
    "v_mfma_f32_16x16x32_bf16 v[208:211], v[36:39], v[180:183], v[208:211]\n" \
    "ds_read_b128 v[200:203], v164 offset:14336\n" \
    "v_mfma_f32_16x16x32_bf16 v[212:215], v[116:119], v[180:183], v[212:215]\n" \
    "s_waitcnt lgkmcnt(4)\n" \
    "v_mfma_f32_16x16x32_bf16 v[208:211], v[40:43], v[184:187], v[208:211]\n" \
    "ds_read_b128 v[204:207], v164 offset:15360\n" \
    "v_mfma_f32_16x16x32_bf16 v[212:215], v[120:123], v[184:187], v[212:215]\n" \
    "s_waitcnt lgkmcnt(4)\n" \
    "v_mfma_f32_16x16x32_bf16 v[208:211], v[44:47], v[188:191], v[208:211]\n" \
    "ds_read_b128 v[176:179], v164 offset:16384\n" \
    "v_mfma_f32_16x16x32_bf16 v[212:215], v[124:127], v[188:191], v[212:215]\n" \
    "s_waitcnt lgkmcnt(4)\n" \
    "v_mfma_f32_16x16x32_bf16 v[208:211], v[48:51], v[192:195], v[208:211]\n" \
    "ds_read_b128 v[180:183], v164 offset:17408\n" \
    "v_mfma_f32_16x16x32_bf16 v[212:215], v[128:131], v[192:195], v[212:215]\n" \
    "s_waitcnt lgkmcnt(4)\n" \
    "v_mfma_f32_16x16x32_bf16 v[208:211], v[52:55], v[196:199], v[208:211]\n" \
    "ds_read_b128 v[184:187], v164 offset:18432\n" \
    "v_mfma_f32_16x16x32_bf16 v[212:215], v[132:135], v[196:199], v[212:215]\n" \
    "s_waitcnt lgkmcnt(4)\n" \
    "v_mfma_f32_16x16x32_bf16 v[208:211], v[56:59], v[200:203], v[208:211]\n" \
    "ds_read_b128 v[188:191], v164 offset:19456\n" \
    "v_mfma_f32_16x16x32_bf16 v[212:215], v[136:139], v[200:203], v[212:215]\n" \
    "s_waitcnt lgkmcnt(4)\n" \
    "v_mfma_f32_16x16x32_bf16 v[208:211], v[60:63], v[204:207], v[208:211]\n" \
    "v_mfma_f32_16x16x32_bf16 v[212:215], v[140:143], v[204:207], v[212:215]\n" \
    "s_waitcnt lgkmcnt(3)\n" \
    "v_mfma_f32_16x16x32_bf16 v[208:211], v[64:67], v[176:179], v[208:211]\n" \
    "v_mfma_f32_16x16x32_bf16 v[212:215], v[144:147], v[176:179], v[212:215]\n" \
    "s_waitcnt lgkmcnt(2)\n" \
    "v_mfma_f32_16x16x32_bf16 v[208:211], v[68:71], v[180:183], v[208:211]\n" \
    "v_mfma_f32_16x16x32_bf16 v[212:215], v[148:151], v[180:183], v[212:215]\n" \
    "s_waitcnt lgkmcnt(1)\n" \
    "v_mfma_f32_16x16x32_bf16 v[208:211], v[72:75], v[184:187], v[208:211]\n" \
    "v_mfma_f32_16x16x32_bf16 v[212:215], v[152:155], v[184:187], v[212:215]\n" \
    "s_waitcnt vmcnt(1)\n" \
    "buffer_store_dwordx4 v[168:171], v161, s[8:11], s38 offen sc1\n" \
    "s_mov_b32 s38, 0x80000000\n" \
    "s_waitcnt lgkmcnt(0)\n" \
    "v_mfma_f32_16x16x32_bf16 v[208:211], v[76:79], v[188:191], v[208:211]\n" \
    "v_mfma_f32_16x16x32_bf16 v[212:215], v[156:159], v[188:191], v[212:215]\n" \
    "v_readfirstlane_b32 s44, v167\n" \
    "s_cmp_lt_u32 s32, s25\n" \
    "s_cselect_b32 s36, s32, -1\n" \
    "s_add_u32 s39, s33, 128\n" \
    "s_cmp_lt_u32 s33, s44\n" \
    "s_cselect_b32 s36, s39, s36\n" \
    "s_addc_u32 s33, s33, 0\n" \
    "s_cmp_lt_u32 s36, 128\n" \
    "s_addc_u32 s32, s32, 0\n" \
    "v_cvt_pk_bf16_f32 v172, v208, v209\n" \
    "v_cvt_pk_bf16_f32 v173, v210, v211\n" \
    "v_cvt_pk_bf16_f32 v174, v212, v213\n" \
    "v_cvt_pk_bf16_f32 v175, v214, v215\n" \
    "buffer_store_dwordx4 v[172:175], v160, s[8:11], s45 offen sc1\n" \
    "s_lshr_b32 s39, s34, 1\n" \
    "s_mul_i32 s38, s39, s21\n" \
    "s_add_u32 s38, s38, s20\n" \
    "s_bitcmp1_b32 s34, 0\n" \
    "s_cselect_b32 s38, s38, 0x80000000\n" \
    "s_waitcnt vmcnt(2)\n" \
    "v_cmp_ne_u32 vcc, 0, v166\n" \
    "s_nop 0\n" \
    "s_not_b32 s39, vcc_lo\n" \
    "s_bitset1_b32 s39, 16\n" \
    "s_ff1_i32_b32 s39, s39\n" \
    "s_lshr_b32 s39, s39, 2\n" \
    "s_add_u32 s43, s43, s39\n" \
    "s_min_u32 s43, s43, 128\n" \
    "v_mov_b32 v167, s43\n" \
    "v_xor_b32 v165, 4, v165\n" \
    "ds_write_b32 v165, v167\n" \
    "s_mov_b32 s46, 0\n" \
    "s_mov_b32 s34, s35\n" \
    "s_mov_b32 s35, s36\n" \
    "s_add_u32 s41, s41, 20480\n" \
    "s_cmp_eq_u32 s41, 81920\n" \
    "s_cselect_b32 s41, 0, s41\n" \
    "s_waitcnt lgkmcnt(0)\n" \
    "s_branch .Lg_lS_%=\n" \
    ".Lg_eS_%=:\n" \
    "ds_read_b32 v167, v165\n" \
    "s_sleep 2\n" \
    "s_lshl_b32 s39, s43, 8\n" \
    "buffer_load_dword v166, v162, s[8:11], s39 offen sc1\n" \
    "s_waitcnt vmcnt(0)\n" \
    "buffer_store_dwordx4 v[168:171], v161, s[8:11], s38 offen sc1\n" \
    "s_mov_b32 s38, 0x80000000\n" \
    "s_waitcnt lgkmcnt(0)\n" \
    "v_readfirstlane_b32 s44, v167\n" \
    "s_cmp_lt_u32 s32, s25\n" \
    "s_cselect_b32 s36, s32, -1\n" \
    "s_add_u32 s39, s33, 128\n" \
    "s_cmp_lt_u32 s33, s44\n" \
    "s_cselect_b32 s36, s39, s36\n" \
    "s_addc_u32 s33, s33, 0\n" \
    "s_cmp_lt_u32 s36, 128\n" \
    "s_addc_u32 s32, s32, 0\n" \
    "v_cmp_ne_u32 vcc, 0, v166\n" \
    "s_nop 0\n" \
    "s_not_b32 s39, vcc_lo\n" \
    "s_bitset1_b32 s39, 16\n" \
    "s_ff1_i32_b32 s39, s39\n" \
    "s_lshr_b32 s39, s39, 2\n" \
    "s_add_u32 s43, s43, s39\n" \
    "s_min_u32 s43, s43, 128\n" \
    "v_mov_b32 v167, s43\n" \
    "v_xor_b32 v165, 4, v165\n" \
    "ds_write_b32 v165, v167\n" \
    "s_mov_b32 s34, s35\n" \
    "s_mov_b32 s35, s36\n" \
    "s_add_u32 s41, s41, 20480\n" \
    "s_cmp_eq_u32 s41, 81920\n" \
    "s_cselect_b32 s41, 0, s41\n" \
    "s_waitcnt lgkmcnt(0)\n" \
    "s_and_b32 s39, s34, s35\n" \
    "s_add_u32 s40, s32, s33\n" \
    "s_cmp_eq_u32 s39, -1\n" \
    "s_cselect_b32 s40, s40, 0\n" \
    "s_cmp_eq_u32 s40, s26\n" \
    "s_cbranch_scc1 .Lg_end_%=\n" \
    "s_add_u32 s46, s46, 1\n" \
    "s_cmp_lt_u32 s46, 0x20000\n" \
    "s_cbranch_scc1 .Lg_lS_%=\n" \
    "s_branch .Lg_end_%=\n" \
    ".Lg_end_%=:\n" \
    "s_setprio 0\n" \
    "s_waitcnt vmcnt(0)\n" \
    ""
#define OB_ASM \
    "s_mov_b32 s32, 0\n" \
    "s_mov_b32 s33, 0\n" \
    "s_mov_b32 s34, -1\n" \
    "s_mov_b32 s36, 0x80000000\n" \
    "s_mov_b32 s39, 0\n" \
    "s_mov_b32 s40, 0\n" \
    "s_mov_b32 s41, 0\n" \
    "s_mov_b32 s45, 0\n" \
    "v_mov_b32 v125, 24576\n" \
    "v_mov_b32 v132, 1\n" \
    "v_mov_b32 v133, 1\n" \
    "v_mov_b32 v134, 1\n" \
    "v_mov_b32 v135, 1\n" \
    "s_cmp_eq_u32 s16, 0\n" \
    "s_cbranch_scc1 .Lo_lS_%=\n" \
    "s_cmp_lt_u32 s16, 4\n" \
    "s_cbranch_scc1 .Lo_lO_%=\n" \
    ".Lo_lY_%=:\n" \
    "v_add_u32 v124, s39, v120\n" \
    "s_cmp_eq_u32 s34, -1\n" \
    "s_barrier\n" \
    "ds_read_b64 v[128:129], v125\n" \
    "v_xor_b32 v125, 8, v125\n" \
    "s_cbranch_scc1 .Lo_eY_%=\n" \
    "ds_read_b128 v[160:163], v124 offset:0\n" \
    "ds_read_b128 v[184:187], v124 offset:6144\n" \
    "ds_read_b128 v[164:167], v124 offset:1024\n" \
    "ds_read_b128 v[188:191], v124 offset:7168\n" \
    "ds_read_b128 v[168:171], v124 offset:2048\n" \
    "ds_read_b128 v[192:195], v124 offset:8192\n" \
    "ds_read_b128 v[172:175], v124 offset:3072\n" \
    "ds_read_b128 v[196:199], v124 offset:9216\n" \
    "ds_read_b128 v[176:179], v124 offset:4096\n" \
    "ds_read_b128 v[200:203], v124 offset:10240\n" \
    "ds_read_b128 v[180:183], v124 offset:5120\n" \
    "ds_read_b128 v[204:207], v124 offset:11264\n" \
    "s_waitcnt lgkmcnt(12)\n" \
    "v_readfirstlane_b32 s42, v128\n" \
    "v_readfirstlane_b32 s43, v129\n" \
    "s_add_u32 s37, s33, %[brn]\n" \
    "s_min_u32 s42, s42, s37\n" \
    "s_cmp_lt_u32 s32, s42\n" \
    "s_cselect_b32 s35, s32, -1\n" \
    "s_add_u32 s37, s33, 64\n" \
    "s_cmp_lt_u32 s33, s43\n" \
    "s_cselect_b32 s35, s37, s35\n" \
    "s_addc_u32 s33, s33, 0\n" \
    "s_cmp_lt_u32 s35, 64\n" \
    "s_addc_u32 s32, s32, 0\n" \
    "s_xor_b32 s37, s39, 12288\n" \
    "s_add_u32 m0, s37, s20\n" \
    "s_cmp_gt_u32 s35, 63\n" \
    "s_cselect_b32 s38, %[rmask], 63\n" \
    "s_and_b32 s38, s38, s35\n" \
    "s_mul_i32 s38, s38, 1572864\n" \
    "s_add_u32 s38, s38, s19\n" \
    "s_lshr_b32 s37, s38, 8\n" \
    "s_xor_b32 s37, s37, s22\n" \
    "s_and_b32 s37, s37, 0x2000\n" \
    "s_or_b32 s38, s38, s37\n" \
    "s_cmp_eq_u32 s35, -1\n" \
    "s_cselect_b32 s38, 0x80000000, s38\n" \
    "buffer_load_dwordx4 v120, s[8:11], s38 offen offset:0 sc1 lds\n" \
    "buffer_load_dwordx4 v120, s[8:11], s38 offen offset:1024 sc1 lds\n" \
    "buffer_load_dwordx4 v120, s[8:11], s38 offen offset:2048 sc1 lds\n" \
    "buffer_load_dwordx4 v120, s[8:11], s38 offen offset:3072 sc1 lds\n" \
    "s_lshr_b32 s37, s34, 6\n" \
    "s_mul_i32 s37, s37, %[brn]\n" \
    "s_and_b32 s38, s34, %[brn]-1\n" \
    "s_add_u32 s37, s37, s38\n" \
    "s_mul_i32 s44, s37, 2621440\n" \
    "s_add_u32 s44, s44, s17\n" \
    "s_waitcnt vmcnt(4)\n" \
    "buffer_store_dwordx4 v[132:135], v121, s[8:11], s36 offen sc1\n" \
    "s_mov_b32 s36, 0x80000000\n" \
    "s_waitcnt lgkmcnt(11)\n" \
    "v_mfma_f32_16x16x32_bf16 v[144:147], v[160:163], v[0:3], 0\n" \
    "s_waitcnt lgkmcnt(10)\n" \
    "v_mfma_f32_16x16x32_bf16 v[148:151], v[184:187], v[0:3], 0\n" \
    "s_waitcnt lgkmcnt(9)\n" \
    "v_mfma_f32_16x16x32_bf16 v[144:147], v[164:167], v[4:7], v[144:147]\n" \
    "s_waitcnt lgkmcnt(8)\n" \
    "v_mfma_f32_16x16x32_bf16 v[148:151], v[188:191], v[4:7], v[148:151]\n" \
    "s_waitcnt lgkmcnt(7)\n" \
    "v_mfma_f32_16x16x32_bf16 v[144:147], v[168:171], v[8:11], v[144:147]\n" \
    "s_waitcnt lgkmcnt(6)\n" \
    "v_mfma_f32_16x16x32_bf16 v[148:151], v[192:195], v[8:11], v[148:151]\n" \
    "s_waitcnt lgkmcnt(5)\n" \
    "v_mfma_f32_16x16x32_bf16 v[144:147], v[172:175], v[12:15], v[144:147]\n" \
    "s_waitcnt lgkmcnt(4)\n" \
    "v_mfma_f32_16x16x32_bf16 v[148:151], v[196:199], v[12:15], v[148:151]\n" \
    "s_waitcnt lgkmcnt(3)\n" \
    "v_mfma_f32_16x16x32_bf16 v[144:147], v[176:179], v[16:19], v[144:147]\n" \
    "s_waitcnt lgkmcnt(2)\n" \
    "v_mfma_f32_16x16x32_bf16 v[148:151], v[200:203], v[16:19], v[148:151]\n" \
    "s_waitcnt lgkmcnt(1)\n" \
    "v_mfma_f32_16x16x32_bf16 v[144:147], v[180:183], v[20:23], v[144:147]\n" \
    "s_waitcnt lgkmcnt(0)\n" \
    "v_mfma_f32_16x16x32_bf16 v[148:151], v[204:207], v[20:23], v[148:151]\n" \
    "v_mfma_f32_16x16x32_bf16 v[152:155], v[160:163], v[24:27], 0\n" \
    "v_mfma_f32_16x16x32_bf16 v[156:159], v[184:187], v[24:27], 0\n" \
    "v_mfma_f32_16x16x32_bf16 v[152:155], v[164:167], v[28:31], v[152:155]\n" \
    "v_mfma_f32_16x16x32_bf16 v[156:159], v[188:191], v[28:31], v[156:159]\n" \
    "v_mfma_f32_16x16x32_bf16 v[152:155], v[168:171], v[32:35], v[152:155]\n" \
    "v_mfma_f32_16x16x32_bf16 v[156:159], v[192:195], v[32:35], v[156:159]\n" \
    "s_lshr_b32 s46, s44, 8\n" \
    "s_xor_b32 s46, s46, s21\n" \
    "s_and_b32 s46, s46, 0x2000\n" \
    "s_or_b32 s46, s44, s46\n" \
    "v_mfma_f32_16x16x32_bf16 v[152:155], v[172:175], v[36:39], v[152:155]\n" \
    "v_mfma_f32_16x16x32_bf16 v[156:159], v[196:199], v[36:39], v[156:159]\n" \
    "v_cvt_pk_bf16_f32 v136, v144, v145\n" \
    "v_cvt_pk_bf16_f32 v137, v148, v149\n" \
    "v_mfma_f32_16x16x32_bf16 v[152:155], v[176:179], v[40:43], v[152:155]\n" \
    "v_mfma_f32_16x16x32_bf16 v[156:159], v[200:203], v[40:43], v[156:159]\n" \
    "v_cvt_pk_bf16_f32 v138, v146, v147\n" \
    "v_cvt_pk_bf16_f32 v139, v150, v151\n" \
    "v_mfma_f32_16x16x32_bf16 v[152:155], v[180:183], v[44:47], v[152:155]\n" \
    "v_mfma_f32_16x16x32_bf16 v[156:159], v[204:207], v[44:47], v[156:159]\n" \
    "buffer_store_dwordx4 v[136:139], v120, s[8:11], s46 offen sc1\n" \
    "s_add_u32 s44, s44, 65536\n" \
    "v_mfma_f32_16x16x32_bf16 v[144:147], v[160:163], v[48:51], 0\n" \
    "v_mfma_f32_16x16x32_bf16 v[148:151], v[184:187], v[48:51], 0\n" \
    "v_mfma_f32_16x16x32_bf16 v[144:147], v[164:167], v[52:55], v[144:147]\n" \
    "v_mfma_f32_16x16x32_bf16 v[148:151], v[188:191], v[52:55], v[148:151]\n" \
    "v_mfma_f32_16x16x32_bf16 v[144:147], v[168:171], v[56:59], v[144:147]\n" \
    "v_mfma_f32_16x16x32_bf16 v[148:151], v[192:195], v[56:59], v[148:151]\n" \
    "s_lshr_b32 s46, s44, 8\n" \
    "s_xor_b32 s46, s46, s21\n" \
    "s_and_b32 s46, s46, 0x2000\n" \
    "s_or_b32 s46, s44, s46\n" \
    "v_mfma_f32_16x16x32_bf16 v[144:147], v[172:175], v[60:63], v[144:147]\n" \
    "v_mfma_f32_16x16x32_bf16 v[148:151], v[196:199], v[60:63], v[148:151]\n" \
    "v_cvt_pk_bf16_f32 v140, v152, v153\n" \
    "v_cvt_pk_bf16_f32 v141, v156, v157\n" \
    "v_mfma_f32_16x16x32_bf16 v[144:147], v[176:179], v[64:67], v[144:147]\n" \
    "v_mfma_f32_16x16x32_bf16 v[148:151], v[200:203], v[64:67], v[148:151]\n" \
    "v_cvt_pk_bf16_f32 v142, v154, v155\n" \
    "v_cvt_pk_bf16_f32 v143, v158, v159\n" \
    "v_mfma_f32_16x16x32_bf16 v[144:147], v[180:183], v[68:71], v[144:147]\n" \
    "v_mfma_f32_16x16x32_bf16 v[148:151], v[204:207], v[68:71], v[148:151]\n" \
    "buffer_store_dwordx4 v[140:143], v120, s[8:11], s46 offen sc1\n" \
    "s_add_u32 s44, s44, 65536\n" \
    "v_mfma_f32_16x16x32_bf16 v[152:155], v[160:163], v[72:75], 0\n" \
    "v_mfma_f32_16x16x32_bf16 v[156:159], v[184:187], v[72:75], 0\n" \
    "v_mfma_f32_16x16x32_bf16 v[152:155], v[164:167], v[76:79], v[152:155]\n" \
    "v_mfma_f32_16x16x32_bf16 v[156:159], v[188:191], v[76:79], v[156:159]\n" \
    "v_mfma_f32_16x16x32_bf16 v[152:155], v[168:171], v[80:83], v[152:155]\n" \
    "v_mfma_f32_16x16x32_bf16 v[156:159], v[192:195], v[80:83], v[156:159]\n" \
    "s_lshr_b32 s46, s44, 8\n" \
    "s_xor_b32 s46, s46, s21\n" \
    "s_and_b32 s46, s46, 0x2000\n" \
    "s_or_b32 s46, s44, s46\n" \
    "v_mfma_f32_16x16x32_bf16 v[152:155], v[172:175], v[84:87], v[152:155]\n" \
    "v_mfma_f32_16x16x32_bf16 v[156:159], v[196:199], v[84:87], v[156:159]\n" \
    "v_cvt_pk_bf16_f32 v136, v144, v145\n" \
    "v_cvt_pk_bf16_f32 v137, v148, v149\n" \
    "v_mfma_f32_16x16x32_bf16 v[152:155], v[176:179], v[88:91], v[152:155]\n" \
    "v_mfma_f32_16x16x32_bf16 v[156:159], v[200:203], v[88:91], v[156:159]\n" \
    "v_cvt_pk_bf16_f32 v138, v146, v147\n" \
    "v_cvt_pk_bf16_f32 v139, v150, v151\n" \
    "v_mfma_f32_16x16x32_bf16 v[152:155], v[180:183], v[92:95], v[152:155]\n" \
    "v_mfma_f32_16x16x32_bf16 v[156:159], v[204:207], v[92:95], v[156:159]\n" \
    "buffer_store_dwordx4 v[136:139], v120, s[8:11], s46 offen sc1\n" \
    "s_add_u32 s44, s44, 65536\n" \
    "v_mfma_f32_16x16x32_bf16 v[144:147], v[160:163], v[96:99], 0\n" \
    "v_mfma_f32_16x16x32_bf16 v[148:151], v[184:187], v[96:99], 0\n" \
    "v_mfma_f32_16x16x32_bf16 v[144:147], v[164:167], v[100:103], v[144:147]\n" \
    "v_mfma_f32_16x16x32_bf16 v[148:151], v[188:191], v[100:103], v[148:151]\n" \
    "v_mfma_f32_16x16x32_bf16 v[144:147], v[168:171], v[104:107], v[144:147]\n" \
    "v_mfma_f32_16x16x32_bf16 v[148:151], v[192:195], v[104:107], v[148:151]\n" \
    "s_lshr_b32 s46, s44, 8\n" \
    "s_xor_b32 s46, s46, s21\n" \
    "s_and_b32 s46, s46, 0x2000\n" \
    "s_or_b32 s46, s44, s46\n" \
    "v_mfma_f32_16x16x32_bf16 v[144:147], v[172:175], v[108:111], v[144:147]\n" \
    "v_mfma_f32_16x16x32_bf16 v[148:151], v[196:199], v[108:111], v[148:151]\n" \
    "v_cvt_pk_bf16_f32 v140, v152, v153\n" \
    "v_cvt_pk_bf16_f32 v141, v156, v157\n" \
    "v_mfma_f32_16x16x32_bf16 v[144:147], v[176:179], v[112:115], v[144:147]\n" \
    "v_mfma_f32_16x16x32_bf16 v[148:151], v[200:203], v[112:115], v[148:151]\n" \
    "v_cvt_pk_bf16_f32 v142, v154, v155\n" \
    "v_cvt_pk_bf16_f32 v143, v158, v159\n" \
    "v_mfma_f32_16x16x32_bf16 v[144:147], v[180:183], v[116:119], v[144:147]\n" \
    "v_mfma_f32_16x16x32_bf16 v[148:151], v[204:207], v[116:119], v[148:151]\n" \
    "buffer_store_dwordx4 v[140:143], v120, s[8:11], s46 offen sc1\n" \
    "s_add_u32 s44, s44, 65536\n" \
    "s_lshr_b32 s46, s44, 8\n" \
    "s_xor_b32 s46, s46, s21\n" \
    "s_and_b32 s46, s46, 0x2000\n" \
    "s_or_b32 s46, s44, s46\n" \
    "s_nop 0\n" \
    "v_cvt_pk_bf16_f32 v136, v144, v145\n" \
    "v_cvt_pk_bf16_f32 v137, v148, v149\n" \
    "v_cvt_pk_bf16_f32 v138, v146, v147\n" \
    "v_cvt_pk_bf16_f32 v139, v150, v151\n" \
    "buffer_store_dwordx4 v[136:139], v120, s[8:11], s46 offen sc1\n" \
    "s_add_u32 s44, s44, 65536\n" \
    "s_lshl_b32 s36, s34, 14\n" \
    "s_add_u32 s36, s36, s18\n" \
    "s_mov_b32 s45, 0\n" \
    "s_waitcnt vmcnt(6)\n" \
    "s_mov_b32 s34, s35\n" \
    "s_xor_b32 s39, s39, 12288\n" \
    "s_waitcnt lgkmcnt(0)\n" \
    "s_branch .Lo_lY_%=\n" \
    ".Lo_eY_%=:\n" \
    "s_waitcnt lgkmcnt(0)\n" \
    "v_readfirstlane_b32 s42, v128\n" \
    "v_readfirstlane_b32 s43, v129\n" \
    "s_add_u32 s37, s33, %[brn]\n" \
    "s_min_u32 s42, s42, s37\n" \
    "s_cmp_lt_u32 s32, s42\n" \
    "s_cselect_b32 s35, s32, -1\n" \
    "s_add_u32 s37, s33, 64\n" \
    "s_cmp_lt_u32 s33, s43\n" \
    "s_cselect_b32 s35, s37, s35\n" \
    "s_addc_u32 s33, s33, 0\n" \
    "s_cmp_lt_u32 s35, 64\n" \
    "s_addc_u32 s32, s32, 0\n" \
    "s_xor_b32 s37, s39, 12288\n" \
    "s_add_u32 m0, s37, s20\n" \
    "s_cmp_gt_u32 s35, 63\n" \
    "s_cselect_b32 s38, %[rmask], 63\n" \
    "s_and_b32 s38, s38, s35\n" \
    "s_mul_i32 s38, s38, 1572864\n" \
    "s_add_u32 s38, s38, s19\n" \
    "s_lshr_b32 s37, s38, 8\n" \
    "s_xor_b32 s37, s37, s22\n" \
    "s_and_b32 s37, s37, 0x2000\n" \
    "s_or_b32 s38, s38, s37\n" \
    "s_cmp_eq_u32 s35, -1\n" \
    "s_cselect_b32 s38, 0x80000000, s38\n" \
    "buffer_load_dwordx4 v120, s[8:11], s38 offen offset:0 sc1 lds\n" \
    "buffer_load_dwordx4 v120, s[8:11], s38 offen offset:1024 sc1 lds\n" \
    "buffer_load_dwordx4 v120, s[8:11], s38 offen offset:2048 sc1 lds\n" \
    "buffer_load_dwordx4 v120, s[8:11], s38 offen offset:3072 sc1 lds\n" \
    "s_waitcnt vmcnt(4)\n" \
    "buffer_store_dwordx4 v[132:135], v121, s[8:11], s36 offen sc1\n" \
    "s_mov_b32 s36, 0x80000000\n" \
    "s_waitcnt vmcnt(1)\n" \
    "s_mov_b32 s34, s35\n" \
    "s_xor_b32 s39, s39, 12288\n" \
    "s_waitcnt lgkmcnt(0)\n" \
    "s_add_u32 s38, s32, s33\n" \
    "s_cmp_eq_u32 s34, -1\n" \
    "s_cselect_b32 s38, s38, 0\n" \
    "s_cmp_eq_u32 s38, 128\n" \
    "s_cbranch_scc1 .Lo_end_%=\n" \
    "s_add_u32 s45, s45, 1\n" \
    "s_cmp_lt_u32 s45, 0x20000\n" \
    "s_cbranch_scc1 .Lo_lY_%=\n" \
    "s_branch .Lo_end_%=\n" \
    ".Lo_lO_%=:\n" \
    "v_add_u32 v124, s39, v120\n" \
    "s_cmp_eq_u32 s34, -1\n" \
    "s_barrier\n" \
    "ds_read_b64 v[128:129], v125\n" \
    "v_xor_b32 v125, 8, v125\n" \
    "s_cbranch_scc1 .Lo_eO_%=\n" \
    "ds_read_b128 v[160:163], v124 offset:0\n" \
    "ds_read_b128 v[184:187], v124 offset:6144\n" \
    "ds_read_b128 v[164:167], v124 offset:1024\n" \
    "ds_read_b128 v[188:191], v124 offset:7168\n" \
    "ds_read_b128 v[168:171], v124 offset:2048\n" \
    "ds_read_b128 v[192:195], v124 offset:8192\n" \
    "ds_read_b128 v[172:175], v124 offset:3072\n" \
    "ds_read_b128 v[196:199], v124 offset:9216\n" \
    "ds_read_b128 v[176:179], v124 offset:4096\n" \
    "ds_read_b128 v[200:203], v124 offset:10240\n" \
    "ds_read_b128 v[180:183], v124 offset:5120\n" \
    "ds_read_b128 v[204:207], v124 offset:11264\n" \
    "s_lshr_b32 s37, s34, 6\n" \
    "s_mul_i32 s37, s37, %[brn]\n" \
    "s_and_b32 s38, s34, %[brn]-1\n" \
    "s_add_u32 s37, s37, s38\n" \
    "s_mul_i32 s44, s37, 2621440\n" \
    "s_add_u32 s44, s44, s17\n" \
    "s_waitcnt lgkmcnt(11)\n" \
    "v_mfma_f32_16x16x32_bf16 v[144:147], v[160:163], v[0:3], 0\n" \
    "s_waitcnt lgkmcnt(10)\n" \
    "v_mfma_f32_16x16x32_bf16 v[148:151], v[184:187], v[0:3], 0\n" \
    "s_waitcnt lgkmcnt(9)\n" \
    "v_mfma_f32_16x16x32_bf16 v[144:147], v[164:167], v[4:7], v[144:147]\n" \
    "s_waitcnt lgkmcnt(8)\n" \
    "v_mfma_f32_16x16x32_bf16 v[148:151], v[188:191], v[4:7], v[148:151]\n" \
    "s_waitcnt lgkmcnt(7)\n" \
    "v_mfma_f32_16x16x32_bf16 v[144:147], v[168:171], v[8:11], v[144:147]\n" \
    "s_waitcnt lgkmcnt(6)\n" \
    "v_mfma_f32_16x16x32_bf16 v[148:151], v[192:195], v[8:11], v[148:151]\n" \
    "s_waitcnt lgkmcnt(5)\n" \
    "v_mfma_f32_16x16x32_bf16 v[144:147], v[172:175], v[12:15], v[144:147]\n" \
    "s_waitcnt lgkmcnt(4)\n" \
    "v_mfma_f32_16x16x32_bf16 v[148:151], v[196:199], v[12:15], v[148:151]\n" \
    "s_waitcnt lgkmcnt(3)\n" \
    "v_mfma_f32_16x16x32_bf16 v[144:147], v[176:179], v[16:19], v[144:147]\n" \
    "s_waitcnt lgkmcnt(2)\n" \
    "v_mfma_f32_16x16x32_bf16 v[148:151], v[200:203], v[16:19], v[148:151]\n" \
    "s_waitcnt lgkmcnt(1)\n" \
    "v_mfma_f32_16x16x32_bf16 v[144:147], v[180:183], v[20:23], v[144:147]\n" \
    "s_waitcnt lgkmcnt(0)\n" \
    "v_mfma_f32_16x16x32_bf16 v[148:151], v[204:207], v[20:23], v[148:151]\n" \
    "v_mfma_f32_16x16x32_bf16 v[152:155], v[160:163], v[24:27], 0\n" \
    "v_mfma_f32_16x16x32_bf16 v[156:159], v[184:187], v[24:27], 0\n" \
    "v_mfma_f32_16x16x32_bf16 v[152:155], v[164:167], v[28:31], v[152:155]\n" \
    "v_mfma_f32_16x16x32_bf16 v[156:159], v[188:191], v[28:31], v[156:159]\n" \
    "v_mfma_f32_16x16x32_bf16 v[152:155], v[168:171], v[32:35], v[152:155]\n" \
    "v_mfma_f32_16x16x32_bf16 v[156:159], v[192:195], v[32:35], v[156:159]\n" \
    "s_lshr_b32 s46, s44, 8\n" \
    "s_xor_b32 s46, s46, s21\n" \
    "s_and_b32 s46, s46, 0x2000\n" \
    "s_or_b32 s46, s44, s46\n" \
    "v_mfma_f32_16x16x32_bf16 v[152:155], v[172:175], v[36:39], v[152:155]\n" \
    "v_mfma_f32_16x16x32_bf16 v[156:159], v[196:199], v[36:39], v[156:159]\n" \
    "v_cvt_pk_bf16_f32 v136, v144, v145\n" \
    "v_cvt_pk_bf16_f32 v137, v148, v149\n" \
    "v_mfma_f32_16x16x32_bf16 v[152:155], v[176:179], v[40:43], v[152:155]\n" \
    "v_mfma_f32_16x16x32_bf16 v[156:159], v[200:203], v[40:43], v[156:159]\n" \
    "v_cvt_pk_bf16_f32 v138, v146, v147\n" \
    "v_cvt_pk_bf16_f32 v139, v150, v151\n" \
    "v_mfma_f32_16x16x32_bf16 v[152:155], v[180:183], v[44:47], v[152:155]\n" \
    "v_mfma_f32_16x16x32_bf16 v[156:159], v[204:207], v[44:47], v[156:159]\n" \
    "buffer_store_dwordx4 v[136:139], v120, s[8:11], s46 offen sc1\n" \
    "s_add_u32 s44, s44, 65536\n" \
    "v_mfma_f32_16x16x32_bf16 v[144:147], v[160:163], v[48:51], 0\n" \
    "v_mfma_f32_16x16x32_bf16 v[148:151], v[184:187], v[48:51], 0\n" \
    "v_mfma_f32_16x16x32_bf16 v[144:147], v[164:167], v[52:55], v[144:147]\n" \
    "v_mfma_f32_16x16x32_bf16 v[148:151], v[188:191], v[52:55], v[148:151]\n" \
    "v_mfma_f32_16x16x32_bf16 v[144:147], v[168:171], v[56:59], v[144:147]\n" \
    "v_mfma_f32_16x16x32_bf16 v[148:151], v[192:195], v[56:59], v[148:151]\n" \
    "s_lshr_b32 s46, s44, 8\n" \
    "s_xor_b32 s46, s46, s21\n" \
    "s_and_b32 s46, s46, 0x2000\n" \
    "s_or_b32 s46, s44, s46\n" \
    "v_mfma_f32_16x16x32_bf16 v[144:147], v[172:175], v[60:63], v[144:147]\n" \
    "v_mfma_f32_16x16x32_bf16 v[148:151], v[196:199], v[60:63], v[148:151]\n" \
    "v_cvt_pk_bf16_f32 v140, v152, v153\n" \
    "v_cvt_pk_bf16_f32 v141, v156, v157\n" \
    "v_mfma_f32_16x16x32_bf16 v[144:147], v[176:179], v[64:67], v[144:147]\n" \
    "v_mfma_f32_16x16x32_bf16 v[148:151], v[200:203], v[64:67], v[148:151]\n" \
    "v_cvt_pk_bf16_f32 v142, v154, v155\n" \
    "v_cvt_pk_bf16_f32 v143, v158, v159\n" \
    "v_mfma_f32_16x16x32_bf16 v[144:147], v[180:183], v[68:71], v[144:147]\n" \
    "v_mfma_f32_16x16x32_bf16 v[148:151], v[204:207], v[68:71], v[148:151]\n" \
    "buffer_store_dwordx4 v[140:143], v120, s[8:11], s46 offen sc1\n" \
    "s_add_u32 s44, s44, 65536\n" \
    "v_mfma_f32_16x16x32_bf16 v[152:155], v[160:163], v[72:75], 0\n" \
    "v_mfma_f32_16x16x32_bf16 v[156:159], v[184:187], v[72:75], 0\n" \
    "v_mfma_f32_16x16x32_bf16 v[152:155], v[164:167], v[76:79], v[152:155]\n" \
    "v_mfma_f32_16x16x32_bf16 v[156:159], v[188:191], v[76:79], v[156:159]\n" \
    "v_mfma_f32_16x16x32_bf16 v[152:155], v[168:171], v[80:83], v[152:155]\n" \
    "v_mfma_f32_16x16x32_bf16 v[156:159], v[192:195], v[80:83], v[156:159]\n" \
    "s_lshr_b32 s46, s44, 8\n" \
    "s_xor_b32 s46, s46, s21\n" \
    "s_and_b32 s46, s46, 0x2000\n" \
    "s_or_b32 s46, s44, s46\n" \
    "v_mfma_f32_16x16x32_bf16 v[152:155], v[172:175], v[84:87], v[152:155]\n" \
    "v_mfma_f32_16x16x32_bf16 v[156:159], v[196:199], v[84:87], v[156:159]\n" \
    "v_cvt_pk_bf16_f32 v136, v144, v145\n" \
    "v_cvt_pk_bf16_f32 v137, v148, v149\n" \
    "v_mfma_f32_16x16x32_bf16 v[152:155], v[176:179], v[88:91], v[152:155]\n" \
    "v_mfma_f32_16x16x32_bf16 v[156:159], v[200:203], v[88:91], v[156:159]\n" \
    "v_cvt_pk_bf16_f32 v138, v146, v147\n" \
    "v_cvt_pk_bf16_f32 v139, v150, v151\n" \
    "v_mfma_f32_16x16x32_bf16 v[152:155], v[180:183], v[92:95], v[152:155]\n" \
    "v_mfma_f32_16x16x32_bf16 v[156:159], v[204:207], v[92:95], v[156:159]\n" \
    "buffer_store_dwordx4 v[136:139], v120, s[8:11], s46 offen sc1\n" \
    "s_add_u32 s44, s44, 65536\n" \
    "v_mfma_f32_16x16x32_bf16 v[144:147], v[160:163], v[96:99], 0\n" \
    "v_mfma_f32_16x16x32_bf16 v[148:151], v[184:187], v[96:99], 0\n" \
    "v_mfma_f32_16x16x32_bf16 v[144:147], v[164:167], v[100:103], v[144:147]\n" \
    "v_mfma_f32_16x16x32_bf16 v[148:151], v[188:191], v[100:103], v[148:151]\n" \
    "v_mfma_f32_16x16x32_bf16 v[144:147], v[168:171], v[104:107], v[144:147]\n" \
    "v_mfma_f32_16x16x32_bf16 v[148:151], v[192:195], v[104:107], v[148:151]\n" \
    "s_lshr_b32 s46, s44, 8\n" \
    "s_xor_b32 s46, s46, s21\n" \
    "s_and_b32 s46, s46, 0x2000\n" \
    "s_or_b32 s46, s44, s46\n" \
    "v_mfma_f32_16x16x32_bf16 v[144:147], v[172:175], v[108:111], v[144:147]\n" \
    "v_mfma_f32_16x16x32_bf16 v[148:151], v[196:199], v[108:111], v[148:151]\n" \
    "v_cvt_pk_bf16_f32 v140, v152, v153\n" \
    "v_cvt_pk_bf16_f32 v141, v156, v157\n" \
    "v_mfma_f32_16x16x32_bf16 v[144:147], v[176:179], v[112:115], v[144:147]\n" \
    "v_mfma_f32_16x16x32_bf16 v[148:151], v[200:203], v[112:115], v[148:151]\n" \
    "v_cvt_pk_bf16_f32 v142, v154, v155\n" \
    "v_cvt_pk_bf16_f32 v143, v158, v159\n" \
    "v_mfma_f32_16x16x32_bf16 v[144:147], v[180:183], v[116:119], v[144:147]\n" \
    "v_mfma_f32_16x16x32_bf16 v[148:151], v[204:207], v[116:119], v[148:151]\n" \
    "buffer_store_dwordx4 v[140:143], v120, s[8:11], s46 offen sc1\n" \
    "s_add_u32 s44, s44, 65536\n" \
    "v_readfirstlane_b32 s42, v128\n" \
    "v_readfirstlane_b32 s43, v129\n" \
    "s_add_u32 s37, s33, %[brn]\n" \
    "s_min_u32 s42, s42, s37\n" \
    "s_cmp_lt_u32 s32, s42\n" \
    "s_cselect_b32 s35, s32, -1\n" \
    "s_add_u32 s37, s33, 64\n" \
    "s_cmp_lt_u32 s33, s43\n" \
    "s_cselect_b32 s35, s37, s35\n" \
    "s_addc_u32 s33, s33, 0\n" \
    "s_cmp_lt_u32 s35, 64\n" \
    "s_addc_u32 s32, s32, 0\n" \
    "s_lshr_b32 s46, s44, 8\n" \
    "s_xor_b32 s46, s46, s21\n" \
    "s_and_b32 s46, s46, 0x2000\n" \
    "s_or_b32 s46, s44, s46\n" \
    "v_cvt_pk_bf16_f32 v136, v144, v145\n" \
    "v_cvt_pk_bf16_f32 v137, v148, v149\n" \
    "v_cvt_pk_bf16_f32 v138, v146, v147\n" \
    "v_cvt_pk_bf16_f32 v139, v150, v151\n" \
    "buffer_store_dwordx4 v[136:139], v120, s[8:11], s46 offen sc1\n" \
    "s_add_u32 s44, s44, 65536\n" \
    "s_lshl_b32 s36, s34, 14\n" \
    "s_add_u32 s36, s36, s18\n" \
    "s_mov_b32 s45, 0\n" \
    "s_waitcnt vmcnt(0)\n" \
    "buffer_store_dwordx4 v[132:135], v121, s[8:11], s36 offen sc1\n" \
    "s_mov_b32 s36, 0x80000000\n" \
    "s_mov_b32 s34, s35\n" \
    "s_xor_b32 s39, s39, 12288\n" \
    "s_waitcnt lgkmcnt(0)\n" \
    "s_branch .Lo_lO_%=\n" \
    ".Lo_eO_%=:\n" \
    "s_waitcnt lgkmcnt(0)\n" \
    "v_readfirstlane_b32 s42, v128\n" \
    "v_readfirstlane_b32 s43, v129\n" \
    "s_add_u32 s37, s33, %[brn]\n" \
    "s_min_u32 s42, s42, s37\n" \
    "s_cmp_lt_u32 s32, s42\n" \
    "s_cselect_b32 s35, s32, -1\n" \
    "s_add_u32 s37, s33, 64\n" \
    "s_cmp_lt_u32 s33, s43\n" \
    "s_cselect_b32 s35, s37, s35\n" \
    "s_addc_u32 s33, s33, 0\n" \
    "s_cmp_lt_u32 s35, 64\n" \
    "s_addc_u32 s32, s32, 0\n" \
    "s_mov_b32 s34, s35\n" \
    "s_xor_b32 s39, s39, 12288\n" \
    "s_waitcnt lgkmcnt(0)\n" \
    "s_add_u32 s38, s32, s33\n" \
    "s_cmp_eq_u32 s34, -1\n" \
    "s_cselect_b32 s38, s38, 0\n" \
    "s_cmp_eq_u32 s38, 128\n" \
    "s_cbranch_scc1 .Lo_end_%=\n" \
    "s_add_u32 s45, s45, 1\n" \
    "s_cmp_lt_u32 s45, 0x20000\n" \
    "s_cbranch_scc1 .Lo_lO_%=\n" \
    "s_branch .Lo_end_%=\n" \
    ".Lo_lS_%=:\n" \
    "v_add_u32 v124, s39, v120\n" \
    "s_cmp_eq_u32 s34, -1\n" \
    "s_barrier\n" \
    "ds_read_b64 v[128:129], v125\n" \
    "v_xor_b32 v125, 8, v125\n" \
    "s_cbranch_scc1 .Lo_eS_%=\n" \
    "ds_read_b128 v[160:163], v124 offset:0\n" \
    "ds_read_b128 v[184:187], v124 offset:6144\n" \
    "ds_read_b128 v[164:167], v124 offset:1024\n" \
    "ds_read_b128 v[188:191], v124 offset:7168\n" \
    "ds_read_b128 v[168:171], v124 offset:2048\n" \
    "ds_read_b128 v[192:195], v124 offset:8192\n" \
    "ds_read_b128 v[172:175], v124 offset:3072\n" \
    "ds_read_b128 v[196:199], v124 offset:9216\n" \
    "ds_read_b128 v[176:179], v124 offset:4096\n" \
    "ds_read_b128 v[200:203], v124 offset:10240\n" \
    "ds_read_b128 v[180:183], v124 offset:5120\n" \
    "ds_read_b128 v[204:207], v124 offset:11264\n" \
    "s_lshr_b32 s37, s34, 6\n" \
    "s_mul_i32 s37, s37, %[brn]\n" \
    "s_and_b32 s38, s34, %[brn]-1\n" \
    "s_add_u32 s37, s37, s38\n" \
    "s_mul_i32 s44, s37, 2621440\n" \
    "s_add_u32 s44, s44, s17\n" \
    "s_waitcnt lgkmcnt(11)\n" \
    "v_mfma_f32_16x16x32_bf16 v[144:147], v[160:163], v[0:3], 0\n" \
    "s_waitcnt lgkmcnt(10)\n" \
    "v_mfma_f32_16x16x32_bf16 v[148:151], v[184:187], v[0:3], 0\n" \
    "s_waitcnt lgkmcnt(9)\n" \
    "v_mfma_f32_16x16x32_bf16 v[144:147], v[164:167], v[4:7], v[144:147]\n" \
    "s_waitcnt lgkmcnt(8)\n" \
    "v_mfma_f32_16x16x32_bf16 v[148:151], v[188:191], v[4:7], v[148:151]\n" \
    "s_waitcnt lgkmcnt(7)\n" \
    "v_mfma_f32_16x16x32_bf16 v[144:147], v[168:171], v[8:11], v[144:147]\n" \
    "s_waitcnt lgkmcnt(6)\n" \
    "v_mfma_f32_16x16x32_bf16 v[148:151], v[192:195], v[8:11], v[148:151]\n" \
    "s_waitcnt lgkmcnt(5)\n" \
    "v_mfma_f32_16x16x32_bf16 v[144:147], v[172:175], v[12:15], v[144:147]\n" \
    "s_waitcnt lgkmcnt(4)\n" \
    "v_mfma_f32_16x16x32_bf16 v[148:151], v[196:199], v[12:15], v[148:151]\n" \
    "s_waitcnt lgkmcnt(3)\n" \
    "v_mfma_f32_16x16x32_bf16 v[144:147], v[176:179], v[16:19], v[144:147]\n" \
    "s_waitcnt lgkmcnt(2)\n" \
    "v_mfma_f32_16x16x32_bf16 v[148:151], v[200:203], v[16:19], v[148:151]\n" \
    "s_waitcnt lgkmcnt(1)\n" \
    "v_mfma_f32_16x16x32_bf16 v[144:147], v[180:183], v[20:23], v[144:147]\n" \
    "s_waitcnt lgkmcnt(0)\n" \
    "v_mfma_f32_16x16x32_bf16 v[148:151], v[204:207], v[20:23], v[148:151]\n" \
    "v_mfma_f32_16x16x32_bf16 v[152:155], v[160:163], v[24:27], 0\n" \
    "v_mfma_f32_16x16x32_bf16 v[156:159], v[184:187], v[24:27], 0\n" \
    "v_mfma_f32_16x16x32_bf16 v[152:155], v[164:167], v[28:31], v[152:155]\n" \
    "v_mfma_f32_16x16x32_bf16 v[156:159], v[188:191], v[28:31], v[156:159]\n" \
    "v_mfma_f32_16x16x32_bf16 v[152:155], v[168:171], v[32:35], v[152:155]\n" \
    "v_mfma_f32_16x16x32_bf16 v[156:159], v[192:195], v[32:35], v[156:159]\n" \
    "s_lshr_b32 s46, s44, 8\n" \
    "s_xor_b32 s46, s46, s21\n" \
    "s_and_b32 s46, s46, 0x2000\n" \
    "s_or_b32 s46, s44, s46\n" \
    "v_mfma_f32_16x16x32_bf16 v[152:155], v[172:175], v[36:39], v[152:155]\n" \
    "v_mfma_f32_16x16x32_bf16 v[156:159], v[196:199], v[36:39], v[156:159]\n" \
    "v_cvt_pk_bf16_f32 v136, v144, v145\n" \
    "v_cvt_pk_bf16_f32 v137, v148, v149\n" \
    "v_mfma_f32_16x16x32_bf16 v[152:155], v[176:179], v[40:43], v[152:155]\n" \
    "v_mfma_f32_16x16x32_bf16 v[156:159], v[200:203], v[40:43], v[156:159]\n" \
    "v_cvt_pk_bf16_f32 v138, v146, v147\n" \
    "v_cvt_pk_bf16_f32 v139, v150, v151\n" \
    "v_mfma_f32_16x16x32_bf16 v[152:155], v[180:183], v[44:47], v[152:155]\n" \
    "v_mfma_f32_16x16x32_bf16 v[156:159], v[204:207], v[44:47], v[156:159]\n" \
    "buffer_store_dwordx4 v[136:139], v120, s[8:11], s46 offen sc1\n" \
    "s_add_u32 s44, s44, 65536\n" \
    "v_mfma_f32_16x16x32_bf16 v[144:147], v[160:163], v[48:51], 0\n" \
    "v_mfma_f32_16x16x32_bf16 v[148:151], v[184:187], v[48:51], 0\n" \
    "s_lshl_b32 s37, s40, 14\n" \
    "s_lshl_b32 s38, s41, 14\n" \
    "s_add_u32 s38, s38, 1048576\n" \
    "buffer_load_dword v126, v122, s[8:11], s37 offen sc1\n" \
    "buffer_load_dword v127, v122, s[8:11], s38 offen sc1\n" \
    "v_mfma_f32_16x16x32_bf16 v[144:147], v[164:167], v[52:55], v[144:147]\n" \
    "v_mfma_f32_16x16x32_bf16 v[148:151], v[188:191], v[52:55], v[148:151]\n" \
    "v_mfma_f32_16x16x32_bf16 v[144:147], v[168:171], v[56:59], v[144:147]\n" \
    "v_mfma_f32_16x16x32_bf16 v[148:151], v[192:195], v[56:59], v[148:151]\n" \
    "s_lshr_b32 s46, s44, 8\n" \
    "s_xor_b32 s46, s46, s21\n" \
    "s_and_b32 s46, s46, 0x2000\n" \
    "s_or_b32 s46, s44, s46\n" \
    "v_mfma_f32_16x16x32_bf16 v[144:147], v[172:175], v[60:63], v[144:147]\n" \
    "v_mfma_f32_16x16x32_bf16 v[148:151], v[196:199], v[60:63], v[148:151]\n" \
    "v_cvt_pk_bf16_f32 v140, v152, v153\n" \
    "v_cvt_pk_bf16_f32 v141, v156, v157\n" \
    "v_mfma_f32_16x16x32_bf16 v[144:147], v[176:179], v[64:67], v[144:147]\n" \
    "v_mfma_f32_16x16x32_bf16 v[148:151], v[200:203], v[64:67], v[148:151]\n" \
    "v_cvt_pk_bf16_f32 v142, v154, v155\n" \
    "v_cvt_pk_bf16_f32 v143, v158, v159\n" \
    "v_mfma_f32_16x16x32_bf16 v[144:147], v[180:183], v[68:71], v[144:147]\n" \
    "v_mfma_f32_16x16x32_bf16 v[148:151], v[204:207], v[68:71], v[148:151]\n" \
    "buffer_store_dwordx4 v[140:143], v120, s[8:11], s46 offen sc1\n" \
    "s_add_u32 s44, s44, 65536\n" \
    "v_mfma_f32_16x16x32_bf16 v[152:155], v[160:163], v[72:75], 0\n" \
    "v_mfma_f32_16x16x32_bf16 v[156:159], v[184:187], v[72:75], 0\n" \
    "v_mfma_f32_16x16x32_bf16 v[152:155], v[164:167], v[76:79], v[152:155]\n" \
    "v_mfma_f32_16x16x32_bf16 v[156:159], v[188:191], v[76:79], v[156:159]\n" \
    "v_mfma_f32_16x16x32_bf16 v[152:155], v[168:171], v[80:83], v[152:155]\n" \
    "v_mfma_f32_16x16x32_bf16 v[156:159], v[192:195], v[80:83], v[156:159]\n" \
    "s_lshr_b32 s46, s44, 8\n" \
    "s_xor_b32 s46, s46, s21\n" \
    "s_and_b32 s46, s46, 0x2000\n" \
    "s_or_b32 s46, s44, s46\n" \
    "v_mfma_f32_16x16x32_bf16 v[152:155], v[172:175], v[84:87], v[152:155]\n" \
    "v_mfma_f32_16x16x32_bf16 v[156:159], v[196:199], v[84:87], v[156:159]\n" \
    "v_cvt_pk_bf16_f32 v136, v144, v145\n" \
    "v_cvt_pk_bf16_f32 v137, v148, v149\n" \
    "v_mfma_f32_16x16x32_bf16 v[152:155], v[176:179], v[88:91], v[152:155]\n" \
    "v_mfma_f32_16x16x32_bf16 v[156:159], v[200:203], v[88:91], v[156:159]\n" \
    "v_cvt_pk_bf16_f32 v138, v146, v147\n" \
    "v_cvt_pk_bf16_f32 v139, v150, v151\n" \
    "v_mfma_f32_16x16x32_bf16 v[152:155], v[180:183], v[92:95], v[152:155]\n" \
    "v_mfma_f32_16x16x32_bf16 v[156:159], v[204:207], v[92:95], v[156:159]\n" \
    "buffer_store_dwordx4 v[136:139], v120, s[8:11], s46 offen sc1\n" \
    "s_add_u32 s44, s44, 65536\n" \
    "v_mfma_f32_16x16x32_bf16 v[144:147], v[160:163], v[96:99], 0\n" \
    "v_mfma_f32_16x16x32_bf16 v[148:151], v[184:187], v[96:99], 0\n" \
    "v_mfma_f32_16x16x32_bf16 v[144:147], v[164:167], v[100:103], v[144:147]\n" \
    "v_mfma_f32_16x16x32_bf16 v[148:151], v[188:191], v[100:103], v[148:151]\n" \
    "v_mfma_f32_16x16x32_bf16 v[144:147], v[168:171], v[104:107], v[144:147]\n" \
    "v_mfma_f32_16x16x32_bf16 v[148:151], v[192:195], v[104:107], v[148:151]\n" \
    "s_lshr_b32 s46, s44, 8\n" \
    "s_xor_b32 s46, s46, s21\n" \
    "s_and_b32 s46, s46, 0x2000\n" \
    "s_or_b32 s46, s44, s46\n" \
    "v_mfma_f32_16x16x32_bf16 v[144:147], v[172:175], v[108:111], v[144:147]\n" \
    "v_mfma_f32_16x16x32_bf16 v[148:151], v[196:199], v[108:111], v[148:151]\n" \
    "v_cvt_pk_bf16_f32 v140, v152, v153\n" \
    "v_cvt_pk_bf16_f32 v141, v156, v157\n" \
    "v_mfma_f32_16x16x32_bf16 v[144:147], v[176:179], v[112:115], v[144:147]\n" \
    "v_mfma_f32_16x16x32_bf16 v[148:151], v[200:203], v[112:115], v[148:151]\n" \
    "v_cvt_pk_bf16_f32 v142, v154, v155\n" \
    "v_cvt_pk_bf16_f32 v143, v158, v159\n" \
    "v_mfma_f32_16x16x32_bf16 v[144:147], v[180:183], v[116:119], v[144:147]\n" \
    "v_mfma_f32_16x16x32_bf16 v[148:151], v[204:207], v[116:119], v[148:151]\n" \
    "buffer_store_dwordx4 v[140:143], v120, s[8:11], s46 offen sc1\n" \
    "s_add_u32 s44, s44, 65536\n" \
    "v_readfirstlane_b32 s42, v128\n" \
    "v_readfirstlane_b32 s43, v129\n" \
    "s_add_u32 s37, s33, %[brn]\n" \
    "s_min_u32 s42, s42, s37\n" \
    "s_cmp_lt_u32 s32, s42\n" \
    "s_cselect_b32 s35, s32, -1\n" \
    "s_add_u32 s37, s33, 64\n" \
    "s_cmp_lt_u32 s33, s43\n" \
    "s_cselect_b32 s35, s37, s35\n" \
    "s_addc_u32 s33, s33, 0\n" \
    "s_cmp_lt_u32 s35, 64\n" \
    "s_addc_u32 s32, s32, 0\n" \
    "s_lshr_b32 s46, s44, 8\n" \
    "s_xor_b32 s46, s46, s21\n" \
    "s_and_b32 s46, s46, 0x2000\n" \
    "s_or_b32 s46, s44, s46\n" \
    "v_cvt_pk_bf16_f32 v136, v144, v145\n" \
    "v_cvt_pk_bf16_f32 v137, v148, v149\n" \
    "v_cvt_pk_bf16_f32 v138, v146, v147\n" \
    "v_cvt_pk_bf16_f32 v139, v150, v151\n" \
    "buffer_store_dwordx4 v[136:139], v120, s[8:11], s46 offen sc1\n" \
    "s_add_u32 s44, s44, 65536\n" \
    "s_lshl_b32 s36, s34, 14\n" \
    "s_add_u32 s36, s36, s18\n" \
    "s_mov_b32 s45, 0\n" \
    "s_waitcnt vmcnt(5)\n" \
    "v_cmp_ne_u32 vcc, 0, v126\n" \
    "s_nop 0\n" \
    "s_not_b32 s37, vcc_lo\n" \
    "s_bitset1_b32 s37, 16\n" \
    "s_ff1_i32_b32 s37, s37\n" \
    "s_lshr_b32 s37, s37, 3\n" \
    "s_add_u32 s40, s40, s37\n" \
    "s_min_u32 s40, s40, 64\n" \
    "v_mov_b32 v128, s40\n" \
    "s_waitcnt vmcnt(4)\n" \
    "v_cmp_ne_u32 vcc, 0, v127\n" \
    "s_nop 0\n" \
    "s_not_b32 s37, vcc_lo\n" \
    "s_bitset1_b32 s37, 16\n" \
    "s_ff1_i32_b32 s37, s37\n" \
    "s_lshr_b32 s37, s37, 3\n" \
    "s_add_u32 s41, s41, s37\n" \
    "s_min_u32 s41, s41, 64\n" \
    "v_mov_b32 v129, s41\n" \
    "ds_write_b64 v125, v[128:129]\n" \
    "s_waitcnt vmcnt(0)\n" \
    "buffer_store_dwordx4 v[132:135], v121, s[8:11], s36 offen sc1\n" \
    "s_mov_b32 s36, 0x80000000\n" \
    "s_mov_b32 s34, s35\n" \
    "s_xor_b32 s39, s39, 12288\n" \
    "s_waitcnt lgkmcnt(0)\n" \
    "s_branch .Lo_lS_%=\n" \
    ".Lo_eS_%=:\n" \
    "s_waitcnt lgkmcnt(0)\n" \
    "v_readfirstlane_b32 s42, v128\n" \
    "v_readfirstlane_b32 s43, v129\n" \
    "s_add_u32 s37, s33, %[brn]\n" \
    "s_min_u32 s42, s42, s37\n" \
    "s_cmp_lt_u32 s32, s42\n" \
    "s_cselect_b32 s35, s32, -1\n" \
    "s_add_u32 s37, s33, 64\n" \
    "s_cmp_lt_u32 s33, s43\n" \
    "s_cselect_b32 s35, s37, s35\n" \
    "s_addc_u32 s33, s33, 0\n" \
    "s_cmp_lt_u32 s35, 64\n" \
    "s_addc_u32 s32, s32, 0\n" \
    "s_cmp_lg_u32 s35, -1\n" \
    "s_cbranch_scc1 .Lo_ns_%=\n" \
    "s_sleep 2\n" \
    ".Lo_ns_%=:\n" \
    "s_lshl_b32 s37, s40, 14\n" \
    "s_lshl_b32 s38, s41, 14\n" \
    "s_add_u32 s38, s38, 1048576\n" \
    "buffer_load_dword v126, v122, s[8:11], s37 offen sc1\n" \
    "buffer_load_dword v127, v122, s[8:11], s38 offen sc1\n" \
    "s_waitcnt vmcnt(1)\n" \
    "v_cmp_ne_u32 vcc, 0, v126\n" \
    "s_nop 0\n" \
    "s_not_b32 s37, vcc_lo\n" \
    "s_bitset1_b32 s37, 16\n" \
    "s_ff1_i32_b32 s37, s37\n" \
    "s_lshr_b32 s37, s37, 3\n" \
    "s_add_u32 s40, s40, s37\n" \
    "s_min_u32 s40, s40, 64\n" \
    "v_mov_b32 v128, s40\n" \
    "s_waitcnt vmcnt(0)\n" \
    "v_cmp_ne_u32 vcc, 0, v127\n" \
    "s_nop 0\n" \
    "s_not_b32 s37, vcc_lo\n" \
    "s_bitset1_b32 s37, 16\n" \
    "s_ff1_i32_b32 s37, s37\n" \
    "s_lshr_b32 s37, s37, 3\n" \
    "s_add_u32 s41, s41, s37\n" \
    "s_min_u32 s41, s41, 64\n" \
    "v_mov_b32 v129, s41\n" \
    "ds_write_b64 v125, v[128:129]\n" \
    "s_mov_b32 s34, s35\n" \
    "s_xor_b32 s39, s39, 12288\n" \
    "s_waitcnt lgkmcnt(0)\n" \
    "s_add_u32 s38, s32, s33\n" \
    "s_cmp_eq_u32 s34, -1\n" \
    "s_cselect_b32 s38, s38, 0\n" \
    "s_cmp_eq_u32 s38, 128\n" \
    "s_cbranch_scc1 .Lo_end_%=\n" \
    "s_add_u32 s45, s45, 1\n" \
    "s_cmp_lt_u32 s45, 0x20000\n" \
    "s_cbranch_scc1 .Lo_lS_%=\n" \
    "s_branch .Lo_end_%=\n" \
    ".Lo_end_%=:\n" \
    "s_waitcnt vmcnt(0)\n" \
    "s_endpgm\n" \
    ""
#define UP_ASM \
    "s_mov_b32 s32, 0\n" \
    "s_mov_b32 s38, 0x40000\n" \
    "s_mov_b32 s44, 0xaaaaaaaa\n" \
    "s_mov_b32 s45, 0xaaaaaaaa\n" \
    "s_mov_b32 s46, 0xcccccccc\n" \
    "s_mov_b32 s47, 0xcccccccc\n" \
    "v_mov_b32 v182, -1\n" \
    "v_mov_b32 v183, -1\n" \
    "v_mov_b32 v178, 0x3f803f80\n" \
    "v_mov_b32 v179, 0x3f803f80\n" \
    "v_mov_b32 v180, 0x3f803f80\n" \
    "v_mov_b32 v181, 0x3f803f80\n" \
    "s_cmp_lt_u32 s16, 4\n" \
    "s_cbranch_scc1 .Lup_lO_%=\n" \
    ".Lup_lY_%=:\n" \
    "s_add_u32 s48, s32, 2\n" \
    "s_min_u32 s48, s48, 64\n" \
    "s_and_b32 s49, s48, 1\n" \
    "s_xor_b32 s49, s49, 1\n" \
    "s_lshl_b32 s49, s49, 9\n" \
    "s_add_u32 s49, s49, s18\n" \
    "buffer_load_dwordx2 v[176:177], off, s[8:11], s49 sc1\n" \
    "s_mov_b32 s33, 0\n" \
    "s_waitcnt vmcnt(0)\n" \
    ".Lup_kY_%=:\n" \
    "s_mov_b32 s37, 0\n" \
    "s_waitcnt vmcnt(2)\n" \
    ".Lup_Yap_%=:\n" \
    "v_readfirstlane_b32 s36, v177\n" \
    "s_nop 0\n" \
    "s_cmp_gt_i32 s36, s48\n" \
    "s_cbranch_scc1 .Lup_Yax_%=\n" \
    "s_cmp_eq_u32 s36, s48\n" \
    "s_cbranch_scc1 .Lup_Yaw_%=\n" \
    "s_sleep 3\n" \
    "buffer_load_dwordx2 v[176:177], off, s[8:11], s49 sc1\n" \
    "s_waitcnt vmcnt(0)\n" \
    "s_add_u32 s37, s37, 1\n" \
    "s_cmp_lt_u32 s37, s38\n" \
    "s_cbranch_scc1 .Lup_Yap_%=\n" \
    "s_mov_b32 s38, 0\n" \
    "s_branch .Lup_Yax_%=\n" \
    ".Lup_Yaw_%=:\n" \
    "v_readfirstlane_b32 s36, v176\n" \
    "s_cmp_eq_u32 s32, 63\n" \
    "s_cselect_b32 s34, %[dlast], 0\n" \
    "s_add_u32 s36, s36, s34\n" \
    "s_add_u32 s36, s36, %[dx]\n" \
    "s_branch .Lup_Yatb_%=\n" \
    ".Lup_Yata_%=:\n" \
    "s_sleep 1\n" \
    ".Lup_Yatb_%=:\n" \
    "s_memrealtime vcc\n" \
    "s_waitcnt lgkmcnt(0)\n" \
    "s_sub_i32 vcc_lo, s36, vcc_lo\n" \
    "s_cmp_gt_i32 vcc_lo, 8\n" \
    "s_cbranch_scc1 .Lup_Yata_%=\n" \
    "s_cmp_gt_i32 vcc_lo, 0\n" \
    "s_cbranch_scc1 .Lup_Yatb_%=\n" \
    ".Lup_Yax_%=:\n" \
    "s_mul_i32 s41, s32, 40960\n" \
    "s_add_u32 s41, s41, s17\n" \
    "buffer_load_dwordx4 v[216:219], v168, s[8:11], s41 offen sc1\n" \
    "s_add_u32 s41, s41, 8192\n" \
    "buffer_load_dwordx4 v[220:223], v168, s[8:11], s41 offen sc1\n" \
    "s_mov_b32 s37, 0\n" \
    ".Lup_srYa_%=:\n" \
    "s_waitcnt vmcnt(1)\n" \
    "v_max3_u32 v254, v216, v217, v218\n" \
    "s_waitcnt vmcnt(0)\n" \
    "v_max3_u32 v254, v254, v219, v220\n" \
    "v_max3_u32 v254, v254, v221, v222\n" \
    "v_max_u32 v254, v254, v223\n" \
    "v_cmp_eq_u32 vcc, -1, v254\n" \
    "s_nop 0\n" \
    "s_cbranch_vccnz .Lup_scYa_%=\n" \
    ".Lup_sxYa_%=:\n" \
    "v_mfma_f32_16x16x32_bf16 v[244:247], v[216:219], v[216:219], 0\n" \
    "ds_write_b128 v169, v[216:219] offset:0\n" \
    "v_mfma_f32_16x16x32_bf16 v[244:247], v[220:223], v[220:223], v[244:247]\n" \
    "ds_write_b128 v169, v[220:223] offset:8192\n" \
    "v_mfma_f32_16x16x32_bf16 v[240:243], v[178:181], v[216:219], 0\n" \
    "v_mfma_f32_16x16x32_bf16 v[240:243], v[178:181], v[220:223], v[240:243]\n" \
    "s_nop 4\n" \
    "v_cndmask_b32 v254, v244, v245, s[44:45]\n" \
    "v_cndmask_b32 v255, v246, v247, s[44:45]\n" \
    "s_nop 0\n" \
    "v_cndmask_b32 v241, v254, v255, s[46:47]\n" \
    "ds_write_b64 v171, v[240:241] offset:0\n" \
    "s_waitcnt lgkmcnt(0)\n" \
    "s_barrier\n" \
    "ds_read_b128 v[184:187], v168 offset:0\n" \
    "ds_read_b128 v[188:191], v168 offset:1024\n" \
    "ds_read_b128 v[192:195], v168 offset:2048\n" \
    "ds_read_b128 v[196:199], v168 offset:3072\n" \
    "ds_read_b128 v[200:203], v168 offset:4096\n" \
    "ds_read2_b64 v[248:251], v172 offset0:0 offset1:16\n" \
    "s_waitcnt lgkmcnt(5)\n" \
    "v_mfma_f32_16x16x32_bf16 v[208:211], v[0:3], v[184:187], 0\n" \
    "ds_read_b128 v[204:207], v168 offset:5120\n" \
    "v_mfma_f32_16x16x32_bf16 v[212:215], v[80:83], v[184:187], 0\n" \
    "s_mul_i32 s41, s33, 40960\n" \
    "s_add_u32 s41, s41, s17\n" \
    "s_add_u32 s41, s41, 2621440\n" \
    "buffer_load_dwordx4 v[228:231], v168, s[8:11], s41 offen sc1\n" \
    "s_add_u32 s41, s41, 8192\n" \
    "buffer_load_dwordx4 v[232:235], v168, s[8:11], s41 offen sc1\n" \
    "s_waitcnt lgkmcnt(5)\n" \
    "v_mfma_f32_16x16x32_bf16 v[208:211], v[4:7], v[188:191], v[208:211]\n" \
    "ds_read_b128 v[184:187], v168 offset:6144\n" \
    "v_mfma_f32_16x16x32_bf16 v[212:215], v[84:87], v[188:191], v[212:215]\n" \
    "s_mul_i32 s39, s32, 98304\n" \
    "s_add_u32 s39, s39, 0\n" \
    "s_waitcnt lgkmcnt(5)\n" \
    "v_mfma_f32_16x16x32_bf16 v[208:211], v[8:11], v[192:195], v[208:211]\n" \
    "ds_read_b128 v[188:191], v168 offset:7168\n" \
    "v_mfma_f32_16x16x32_bf16 v[212:215], v[88:91], v[192:195], v[212:215]\n" \
    "s_add_u32 s40, s39, %[pdist]*98304\n" \
    "s_cmp_lt_u32 s32, 64-%[pdist]\n" \
    "s_cselect_b32 s40, s40, 0x80000000\n" \
    "s_waitcnt lgkmcnt(5)\n" \
    "v_mfma_f32_16x16x32_bf16 v[208:211], v[12:15], v[196:199], v[208:211]\n" \
    "ds_read_b128 v[192:195], v168 offset:8192\n" \
    "v_mfma_f32_16x16x32_bf16 v[212:215], v[92:95], v[196:199], v[212:215]\n" \
    "s_waitcnt lgkmcnt(4)\n" \
    "v_add_f32 v248, v248, v250\n" \
    "v_mfma_f32_16x16x32_bf16 v[208:211], v[16:19], v[200:203], v[208:211]\n" \
    "ds_read_b128 v[196:199], v168 offset:9216\n" \
    "v_mfma_f32_16x16x32_bf16 v[212:215], v[96:99], v[200:203], v[212:215]\n" \
    "v_add_f32 v249, v249, v251\n" \
    "s_waitcnt lgkmcnt(4)\n" \
    "v_mfma_f32_16x16x32_bf16 v[208:211], v[20:23], v[204:207], v[208:211]\n" \
    "ds_read_b128 v[200:203], v168 offset:10240\n" \
    "v_mfma_f32_16x16x32_bf16 v[212:215], v[100:103], v[204:207], v[212:215]\n" \
    "v_permlane16_swap_b32 v248, v249\n" \
    "s_waitcnt lgkmcnt(4)\n" \
    "v_mfma_f32_16x16x32_bf16 v[208:211], v[24:27], v[184:187], v[208:211]\n" \
    "ds_read_b128 v[204:207], v168 offset:11264\n" \
    "v_mfma_f32_16x16x32_bf16 v[212:215], v[104:107], v[184:187], v[212:215]\n" \
    "v_add_f32 v250, v248, v249\n" \
    "s_waitcnt lgkmcnt(4)\n" \
    "v_mfma_f32_16x16x32_bf16 v[208:211], v[28:31], v[188:191], v[208:211]\n" \
    "ds_read_b128 v[184:187], v168 offset:12288\n" \
    "v_mfma_f32_16x16x32_bf16 v[212:215], v[108:111], v[188:191], v[212:215]\n" \
    "v_add_f32 v248, v248, v249\n" \
    "s_waitcnt lgkmcnt(4)\n" \
    "v_mfma_f32_16x16x32_bf16 v[208:211], v[32:35], v[192:195], v[208:211]\n" \
    "ds_read_b128 v[188:191], v168 offset:13312\n" \
    "v_mfma_f32_16x16x32_bf16 v[212:215], v[112:115], v[192:195], v[212:215]\n" \
    "v_permlane32_swap_b32 v248, v250\n" \
    "s_waitcnt lgkmcnt(4)\n" \
    "v_mfma_f32_16x16x32_bf16 v[208:211], v[36:39], v[196:199], v[208:211]\n" \
    "ds_read_b128 v[192:195], v168 offset:14336\n" \
    "v_mfma_f32_16x16x32_bf16 v[212:215], v[116:119], v[196:199], v[212:215]\n" \
    "v_add_f32 v249, v248, v250\n" \
    "s_waitcnt lgkmcnt(4)\n" \
    "v_mfma_f32_16x16x32_bf16 v[208:211], v[40:43], v[200:203], v[208:211]\n" \
    "ds_read_b128 v[196:199], v168 offset:15360\n" \
    "v_mfma_f32_16x16x32_bf16 v[212:215], v[120:123], v[200:203], v[212:215]\n" \
    "v_add_f32 v248, v248, v250\n" \
    "s_add_u32 s48, s32, 3\n" \
    "s_min_u32 s48, s48, 64\n" \
    "s_and_b32 s49, s48, 1\n" \
    "s_xor_b32 s49, s49, 1\n" \
    "s_lshl_b32 s49, s49, 9\n" \
    "s_add_u32 s49, s49, s18\n" \
    "buffer_load_dwordx2 v[176:177], off, s[8:11], s49 sc1\n" \
    "s_waitcnt lgkmcnt(4)\n" \
    "v_mfma_f32_16x16x32_bf16 v[208:211], v[44:47], v[204:207], v[208:211]\n" \
    "ds_read_b128 v[200:203], v168 offset:16384\n" \
    "v_mfma_f32_16x16x32_bf16 v[212:215], v[124:127], v[204:207], v[212:215]\n" \
    "v_permlane16_swap_b32 v248, v249\n" \
    "s_waitcnt lgkmcnt(4)\n" \
    "v_mfma_f32_16x16x32_bf16 v[208:211], v[48:51], v[184:187], v[208:211]\n" \
    "ds_read_b128 v[204:207], v168 offset:17408\n" \
    "v_mfma_f32_16x16x32_bf16 v[212:215], v[128:131], v[184:187], v[212:215]\n" \
    "v_mul_f32 v252, 0x3acccccd, v248\n" \
    "s_waitcnt lgkmcnt(4)\n" \
    "v_mfma_f32_16x16x32_bf16 v[208:211], v[52:55], v[188:191], v[208:211]\n" \
    "ds_read_b128 v[184:187], v168 offset:18432\n" \
    "v_mfma_f32_16x16x32_bf16 v[212:215], v[132:135], v[188:191], v[212:215]\n" \
    "v_mul_f32 v249, 0x3acccccd, v249\n" \
    "s_waitcnt lgkmcnt(4)\n" \
    "v_mfma_f32_16x16x32_bf16 v[208:211], v[56:59], v[192:195], v[208:211]\n" \
    "ds_read_b128 v[188:191], v168 offset:19456\n" \
    "v_mfma_f32_16x16x32_bf16 v[212:215], v[136:139], v[192:195], v[212:215]\n" \
    "v_fma_f32 v249, -v252, v252, v249\n" \
    "s_waitcnt lgkmcnt(4)\n" \
    "v_mfma_f32_16x16x32_bf16 v[208:211], v[60:63], v[196:199], v[208:211]\n" \
    "v_mfma_f32_16x16x32_bf16 v[212:215], v[140:143], v[196:199], v[212:215]\n" \
    "v_max_f32 v249, 0, v249\n" \
    "s_waitcnt lgkmcnt(3)\n" \
    "v_mfma_f32_16x16x32_bf16 v[208:211], v[64:67], v[200:203], v[208:211]\n" \
    "v_mfma_f32_16x16x32_bf16 v[212:215], v[144:147], v[200:203], v[212:215]\n" \
    "v_add_f32 v249, 0x3727c5ac, v249\n" \
    "s_waitcnt lgkmcnt(2)\n" \
    "v_mfma_f32_16x16x32_bf16 v[208:211], v[68:71], v[204:207], v[208:211]\n" \
    "v_mfma_f32_16x16x32_bf16 v[212:215], v[148:151], v[204:207], v[212:215]\n" \
    "v_rsq_f32 v253, v249\n" \
    "s_waitcnt lgkmcnt(1)\n" \
    "v_mfma_f32_16x16x32_bf16 v[208:211], v[72:75], v[184:187], v[208:211]\n" \
    "v_mfma_f32_16x16x32_bf16 v[212:215], v[152:155], v[184:187], v[212:215]\n" \
    "v_mul_f32 v251, 0xbfb8aa3b, v253\n" \
    "s_waitcnt lgkmcnt(0)\n" \
    "v_mfma_f32_16x16x32_bf16 v[208:211], v[76:79], v[188:191], v[208:211]\n" \
    "v_mfma_f32_16x16x32_bf16 v[212:215], v[156:159], v[188:191], v[212:215]\n" \
    "s_nop 6\n" \
    "v_fma_f32 v208, -v252, v160, v208\n" \
    "v_fma_f32 v209, -v252, v161, v209\n" \
    "v_fma_f32 v210, -v252, v162, v210\n" \
    "v_fma_f32 v211, -v252, v163, v211\n" \
    "v_mul_f32 v208, v251, v208\n" \
    "v_mul_f32 v209, v251, v209\n" \
    "v_mul_f32 v210, v251, v210\n" \
    "v_mul_f32 v211, v251, v211\n" \
    "v_exp_f32 v208, v208\n" \
    "v_fma_f32 v212, -v252, v164, v212\n" \
    "v_exp_f32 v209, v209\n" \
    "v_fma_f32 v213, -v252, v165, v213\n" \
    "v_exp_f32 v210, v210\n" \
    "v_fma_f32 v214, -v252, v166, v214\n" \
    "v_exp_f32 v211, v211\n" \
    "v_fma_f32 v215, -v252, v167, v215\n" \
    "v_add_f32 v208, 1.0, v208\n" \
    "v_add_f32 v209, 1.0, v209\n" \
    "v_add_f32 v210, 1.0, v210\n" \
    "v_add_f32 v211, 1.0, v211\n" \
    "v_rcp_f32 v208, v208\n" \
    "v_mul_f32 v212, v253, v212\n" \
    "v_rcp_f32 v209, v209\n" \
    "v_mul_f32 v213, v253, v213\n" \
    "v_rcp_f32 v210, v210\n" \
    "v_mul_f32 v214, v253, v214\n" \
    "v_rcp_f32 v211, v211\n" \
    "v_mul_f32 v215, v253, v215\n" \
    "v_mul_f32 v208, v208, v212\n" \
    "v_mul_f32 v209, v209, v213\n" \
    "v_mul_f32 v210, v210, v214\n" \
    "v_mul_f32 v211, v211, v215\n" \
    "v_cvt_pk_bf16_f32 v240, v208, v209\n" \
    "v_cvt_pk_bf16_f32 v241, v210, v211\n" \
    "v_min_u32 v240, -2, v240\n" \
    "v_min_u32 v241, -2, v241\n" \
    "buffer_store_dwordx2 v[240:241], v173, s[8:11], s39 offen sc1\n" \
    "buffer_store_dwordx2 v[182:183], v173, s[8:11], s40 offen sc1\n" \
    "s_add_u32 s32, s32, 1\n" \
    "s_cmp_lt_u32 s32, %[eup]\n" \
    "s_barrier\n" \
    "s_cbranch_scc1 .Lup_kY_%=\n" \
    ".Lup_mY_%=:\n" \
    "s_mov_b32 s37, 0\n" \
    "s_waitcnt vmcnt(2)\n" \
    ".Lup_Ybp_%=:\n" \
    "v_readfirstlane_b32 s36, v177\n" \
    "s_nop 0\n" \
    "s_cmp_gt_i32 s36, s48\n" \
    "s_cbranch_scc1 .Lup_Ybx_%=\n" \
    "s_cmp_eq_u32 s36, s48\n" \
    "s_cbranch_scc1 .Lup_Ybw_%=\n" \
    "s_sleep 3\n" \
    "buffer_load_dwordx2 v[176:177], off, s[8:11], s49 sc1\n" \
    "s_waitcnt vmcnt(0)\n" \
    "s_add_u32 s37, s37, 1\n" \
    "s_cmp_lt_u32 s37, s38\n" \
    "s_cbranch_scc1 .Lup_Ybp_%=\n" \
    "s_mov_b32 s38, 0\n" \
    "s_branch .Lup_Ybx_%=\n" \
    ".Lup_Ybw_%=:\n" \
    "v_readfirstlane_b32 s36, v176\n" \
    "s_cmp_eq_u32 s32, 63\n" \
    "s_cselect_b32 s34, %[dlast], 0\n" \
    "s_add_u32 s36, s36, s34\n" \
    "s_add_u32 s36, s36, %[dx]\n" \
    "s_branch .Lup_Ybtb_%=\n" \
    ".Lup_Ybta_%=:\n" \
    "s_sleep 1\n" \
    ".Lup_Ybtb_%=:\n" \
    "s_memrealtime vcc\n" \
    "s_waitcnt lgkmcnt(0)\n" \
    "s_sub_i32 vcc_lo, s36, vcc_lo\n" \
    "s_cmp_gt_i32 vcc_lo, 8\n" \
    "s_cbranch_scc1 .Lup_Ybta_%=\n" \
    "s_cmp_gt_i32 vcc_lo, 0\n" \
    "s_cbranch_scc1 .Lup_Ybtb_%=\n" \
    ".Lup_Ybx_%=:\n" \
    "s_mul_i32 s41, s32, 40960\n" \
    "s_add_u32 s41, s41, s17\n" \
    "buffer_load_dwordx4 v[216:219], v168, s[8:11], s41 offen sc1\n" \
    "s_add_u32 s41, s41, 8192\n" \
    "buffer_load_dwordx4 v[220:223], v168, s[8:11], s41 offen sc1\n" \
    "s_mov_b32 s37, 0\n" \
    ".Lup_srYb_%=:\n" \
    "s_waitcnt vmcnt(1)\n" \
    "v_max3_u32 v254, v216, v217, v218\n" \
    "s_waitcnt vmcnt(0)\n" \
    "v_max3_u32 v254, v254, v219, v220\n" \
    "v_max3_u32 v254, v254, v221, v222\n" \
    "v_max_u32 v254, v254, v223\n" \
    "v_cmp_eq_u32 vcc, -1, v254\n" \
    "s_nop 0\n" \
    "s_cbranch_vccnz .Lup_scYb_%=\n" \
    ".Lup_sxYb_%=:\n" \
    "v_mfma_f32_16x16x32_bf16 v[244:247], v[216:219], v[216:219], 0\n" \
    "ds_write_b128 v169, v[216:219] offset:0\n" \
    "v_mfma_f32_16x16x32_bf16 v[244:247], v[220:223], v[220:223], v[244:247]\n" \
    "ds_write_b128 v169, v[220:223] offset:8192\n" \
    "v_mfma_f32_16x16x32_bf16 v[240:243], v[178:181], v[216:219], 0\n" \
    "v_mfma_f32_16x16x32_bf16 v[240:243], v[178:181], v[220:223], v[240:243]\n" \
    "s_nop 4\n" \
    "v_cndmask_b32 v254, v244, v245, s[44:45]\n" \
    "v_cndmask_b32 v255, v246, v247, s[44:45]\n" \
    "s_nop 0\n" \
    "v_cndmask_b32 v241, v254, v255, s[46:47]\n" \
    "ds_write_b64 v171, v[240:241] offset:0\n" \
    "s_waitcnt lgkmcnt(0)\n" \
    "s_barrier\n" \
    ".Lup_srYc_%=:\n" \
    "v_max3_u32 v254, v228, v229, v230\n" \
    "v_max3_u32 v254, v254, v231, v232\n" \
    "v_max3_u32 v254, v254, v233, v234\n" \
    "v_max_u32 v254, v254, v235\n" \
    "v_cmp_eq_u32 vcc, -1, v254\n" \
    "s_nop 0\n" \
    "s_cbranch_vccnz .Lup_scYc_%=\n" \
    ".Lup_sxYc_%=:\n" \
    "v_mfma_f32_16x16x32_bf16 v[244:247], v[228:231], v[228:231], 0\n" \
    "ds_write_b128 v169, v[228:231] offset:20480\n" \
    "v_mfma_f32_16x16x32_bf16 v[244:247], v[232:235], v[232:235], v[244:247]\n" \
    "ds_write_b128 v169, v[232:235] offset:28672\n" \
    "v_mfma_f32_16x16x32_bf16 v[240:243], v[178:181], v[228:231], 0\n" \
    "v_mfma_f32_16x16x32_bf16 v[240:243], v[178:181], v[232:235], v[240:243]\n" \
    "s_nop 4\n" \
    "v_cndmask_b32 v254, v244, v245, s[44:45]\n" \
    "v_cndmask_b32 v255, v246, v247, s[44:45]\n" \
    "s_nop 0\n" \
    "v_cndmask_b32 v241, v254, v255, s[46:47]\n" \
    "ds_write_b64 v171, v[240:241] offset:1024\n" \
    "ds_read_b128 v[184:187], v168 offset:0\n" \
    "ds_read_b128 v[188:191], v168 offset:1024\n" \
    "ds_read_b128 v[192:195], v168 offset:2048\n" \
    "ds_read_b128 v[196:199], v168 offset:3072\n" \
    "ds_read_b128 v[200:203], v168 offset:4096\n" \
    "ds_read2_b64 v[248:251], v172 offset0:0 offset1:16\n" \
    "s_waitcnt lgkmcnt(5)\n" \
    "v_mfma_f32_16x16x32_bf16 v[208:211], v[0:3], v[184:187], 0\n" \
    "ds_read_b128 v[204:207], v168 offset:5120\n" \
    "v_mfma_f32_16x16x32_bf16 v[212:215], v[80:83], v[184:187], 0\n" \
    "s_waitcnt lgkmcnt(5)\n" \
    "v_mfma_f32_16x16x32_bf16 v[208:211], v[4:7], v[188:191], v[208:211]\n" \
    "ds_read_b128 v[184:187], v168 offset:6144\n" \
    "v_mfma_f32_16x16x32_bf16 v[212:215], v[84:87], v[188:191], v[212:215]\n" \
    "s_mul_i32 s39, s32, 98304\n" \
    "s_add_u32 s39, s39, 0\n" \
    "s_waitcnt lgkmcnt(5)\n" \
    "v_mfma_f32_16x16x32_bf16 v[208:211], v[8:11], v[192:195], v[208:211]\n" \
    "ds_read_b128 v[188:191], v168 offset:7168\n" \
    "v_mfma_f32_16x16x32_bf16 v[212:215], v[88:91], v[192:195], v[212:215]\n" \
    "s_add_u32 s40, s39, %[pdist]*98304\n" \
    "s_cmp_lt_u32 s32, 64-%[pdist]\n" \
    "s_cselect_b32 s40, s40, 0x80000000\n" \
    "s_waitcnt lgkmcnt(5)\n" \
    "v_mfma_f32_16x16x32_bf16 v[208:211], v[12:15], v[196:199], v[208:211]\n" \
    "ds_read_b128 v[192:195], v168 offset:8192\n" \
    "v_mfma_f32_16x16x32_bf16 v[212:215], v[92:95], v[196:199], v[212:215]\n" \
    "s_waitcnt lgkmcnt(4)\n" \
    "v_add_f32 v248, v248, v250\n" \
    "v_mfma_f32_16x16x32_bf16 v[208:211], v[16:19], v[200:203], v[208:211]\n" \
    "ds_read_b128 v[196:199], v168 offset:9216\n" \
    "v_mfma_f32_16x16x32_bf16 v[212:215], v[96:99], v[200:203], v[212:215]\n" \
    "v_add_f32 v249, v249, v251\n" \
    "s_waitcnt lgkmcnt(4)\n" \
    "v_mfma_f32_16x16x32_bf16 v[208:211], v[20:23], v[204:207], v[208:211]\n" \
    "ds_read_b128 v[200:203], v168 offset:10240\n" \
    "v_mfma_f32_16x16x32_bf16 v[212:215], v[100:103], v[204:207], v[212:215]\n" \
    "v_permlane16_swap_b32 v248, v249\n" \
    "s_waitcnt lgkmcnt(4)\n" \
    "v_mfma_f32_16x16x32_bf16 v[208:211], v[24:27], v[184:187], v[208:211]\n" \
    "ds_read_b128 v[204:207], v168 offset:11264\n" \
    "v_mfma_f32_16x16x32_bf16 v[212:215], v[104:107], v[184:187], v[212:215]\n" \
    "v_add_f32 v250, v248, v249\n" \
    "s_waitcnt lgkmcnt(4)\n" \
    "v_mfma_f32_16x16x32_bf16 v[208:211], v[28:31], v[188:191], v[208:211]\n" \
    "ds_read_b128 v[184:187], v168 offset:12288\n" \
    "v_mfma_f32_16x16x32_bf16 v[212:215], v[108:111], v[188:191], v[212:215]\n" \
    "v_add_f32 v248, v248, v249\n" \
    "s_waitcnt lgkmcnt(4)\n" \
    "v_mfma_f32_16x16x32_bf16 v[208:211], v[32:35], v[192:195], v[208:211]\n" \
    "ds_read_b128 v[188:191], v168 offset:13312\n" \
    "v_mfma_f32_16x16x32_bf16 v[212:215], v[112:115], v[192:195], v[212:215]\n" \
    "v_permlane32_swap_b32 v248, v250\n" \
    "s_waitcnt lgkmcnt(4)\n" \
    "v_mfma_f32_16x16x32_bf16 v[208:211], v[36:39], v[196:199], v[208:211]\n" \
    "ds_read_b128 v[192:195], v168 offset:14336\n" \
    "v_mfma_f32_16x16x32_bf16 v[212:215], v[116:119], v[196:199], v[212:215]\n" \
    "v_add_f32 v249, v248, v250\n" \
    "s_waitcnt lgkmcnt(4)\n" \
    "v_mfma_f32_16x16x32_bf16 v[208:211], v[40:43], v[200:203], v[208:211]\n" \
    "ds_read_b128 v[196:199], v168 offset:15360\n" \
    "v_mfma_f32_16x16x32_bf16 v[212:215], v[120:123], v[200:203], v[212:215]\n" \
    "v_add_f32 v248, v248, v250\n" \
    "s_waitcnt lgkmcnt(4)\n" \
    "v_mfma_f32_16x16x32_bf16 v[208:211], v[44:47], v[204:207], v[208:211]\n" \
    "ds_read_b128 v[200:203], v168 offset:16384\n" \
    "v_mfma_f32_16x16x32_bf16 v[212:215], v[124:127], v[204:207], v[212:215]\n" \
    "v_permlane16_swap_b32 v248, v249\n" \
    "s_waitcnt lgkmcnt(4)\n" \
    "v_mfma_f32_16x16x32_bf16 v[208:211], v[48:51], v[184:187], v[208:211]\n" \
    "ds_read_b128 v[204:207], v168 offset:17408\n" \
    "v_mfma_f32_16x16x32_bf16 v[212:215], v[128:131], v[184:187], v[212:215]\n" \
    "v_mul_f32 v252, 0x3acccccd, v248\n" \
    "s_waitcnt lgkmcnt(4)\n" \
    "v_mfma_f32_16x16x32_bf16 v[208:211], v[52:55], v[188:191], v[208:211]\n" \
    "ds_read_b128 v[184:187], v168 offset:18432\n" \
    "v_mfma_f32_16x16x32_bf16 v[212:215], v[132:135], v[188:191], v[212:215]\n" \
    "v_mul_f32 v249, 0x3acccccd, v249\n" \
    "s_waitcnt lgkmcnt(4)\n" \
    "v_mfma_f32_16x16x32_bf16 v[208:211], v[56:59], v[192:195], v[208:211]\n" \
    "ds_read_b128 v[188:191], v168 offset:19456\n" \
    "v_mfma_f32_16x16x32_bf16 v[212:215], v[136:139], v[192:195], v[212:215]\n" \
    "v_fma_f32 v249, -v252, v252, v249\n" \
    "s_waitcnt lgkmcnt(4)\n" \
    "v_mfma_f32_16x16x32_bf16 v[208:211], v[60:63], v[196:199], v[208:211]\n" \
    "v_mfma_f32_16x16x32_bf16 v[212:215], v[140:143], v[196:199], v[212:215]\n" \
    "v_max_f32 v249, 0, v249\n" \
    "s_waitcnt lgkmcnt(3)\n" \
    "v_mfma_f32_16x16x32_bf16 v[208:211], v[64:67], v[200:203], v[208:211]\n" \
    "v_mfma_f32_16x16x32_bf16 v[212:215], v[144:147], v[200:203], v[212:215]\n" \
    "v_add_f32 v249, 0x3727c5ac, v249\n" \
    "s_waitcnt lgkmcnt(2)\n" \
    "v_mfma_f32_16x16x32_bf16 v[208:211], v[68:71], v[204:207], v[208:211]\n" \
    "v_mfma_f32_16x16x32_bf16 v[212:215], v[148:151], v[204:207], v[212:215]\n" \
    "v_rsq_f32 v253, v249\n" \
    "s_waitcnt lgkmcnt(1)\n" \
    "v_mfma_f32_16x16x32_bf16 v[208:211], v[72:75], v[184:187], v[208:211]\n" \
    "v_mfma_f32_16x16x32_bf16 v[212:215], v[152:155], v[184:187], v[212:215]\n" \
    "v_mul_f32 v251, 0xbfb8aa3b, v253\n" \
    "s_waitcnt lgkmcnt(0)\n" \
    "v_mfma_f32_16x16x32_bf16 v[208:211], v[76:79], v[188:191], v[208:211]\n" \
    "v_mfma_f32_16x16x32_bf16 v[212:215], v[156:159], v[188:191], v[212:215]\n" \
    "s_waitcnt lgkmcnt(0)\n" \
    "s_barrier\n" \
    "s_nop 4\n" \
    "v_fma_f32 v208, -v252, v160, v208\n" \
    "v_fma_f32 v209, -v252, v161, v209\n" \
    "v_fma_f32 v210, -v252, v162, v210\n" \
    "v_fma_f32 v211, -v252, v163, v211\n" \
    "v_mul_f32 v208, v251, v208\n" \
    "v_mul_f32 v209, v251, v209\n" \
    "v_mul_f32 v210, v251, v210\n" \
    "v_mul_f32 v211, v251, v211\n" \
    "v_exp_f32 v208, v208\n" \
    "v_fma_f32 v212, -v252, v164, v212\n" \
    "v_exp_f32 v209, v209\n" \
    "v_fma_f32 v213, -v252, v165, v213\n" \
    "v_exp_f32 v210, v210\n" \
    "v_fma_f32 v214, -v252, v166, v214\n" \
    "v_exp_f32 v211, v211\n" \
    "v_fma_f32 v215, -v252, v167, v215\n" \
    "v_add_f32 v208, 1.0, v208\n" \
    "v_add_f32 v209, 1.0, v209\n" \
    "v_add_f32 v210, 1.0, v210\n" \
    "v_add_f32 v211, 1.0, v211\n" \
    "v_rcp_f32 v208, v208\n" \
    "v_mul_f32 v212, v253, v212\n" \
    "v_rcp_f32 v209, v209\n" \
    "v_mul_f32 v213, v253, v213\n" \
    "v_rcp_f32 v210, v210\n" \
    "v_mul_f32 v214, v253, v214\n" \
    "v_rcp_f32 v211, v211\n" \
    "v_mul_f32 v215, v253, v215\n" \
    "v_mul_f32 v208, v208, v212\n" \
    "v_mul_f32 v209, v209, v213\n" \
    "v_mul_f32 v210, v210, v214\n" \
    "v_mul_f32 v211, v211, v215\n" \
    "v_cvt_pk_bf16_f32 v240, v208, v209\n" \
    "v_cvt_pk_bf16_f32 v241, v210, v211\n" \
    "v_min_u32 v240, -2, v240\n" \
    "v_min_u32 v241, -2, v241\n" \
    "buffer_store_dwordx2 v[240:241], v173, s[8:11], s39 offen sc1\n" \
    "buffer_store_dwordx2 v[182:183], v173, s[8:11], s40 offen sc1\n" \
    "s_add_u32 s36, s33, 1\n" \
    "ds_read_b128 v[184:187], v168 offset:20480\n" \
    "ds_read_b128 v[188:191], v168 offset:21504\n" \
    "ds_read_b128 v[192:195], v168 offset:22528\n" \
    "ds_read_b128 v[196:199], v168 offset:23552\n" \
    "ds_read_b128 v[200:203], v168 offset:24576\n" \
    "ds_read2_b64 v[248:251], v172 offset0:128 offset1:144\n" \
    "s_waitcnt lgkmcnt(5)\n" \
    "v_mfma_f32_16x16x32_bf16 v[208:211], v[0:3], v[184:187], 0\n" \
    "ds_read_b128 v[204:207], v168 offset:25600\n" \
    "v_mfma_f32_16x16x32_bf16 v[212:215], v[80:83], v[184:187], 0\n" \
    "s_mul_i32 s41, s36, 40960\n" \
    "s_add_u32 s41, s41, s17\n" \
    "s_add_u32 s41, s41, 2621440\n" \
    "buffer_load_dwordx4 v[228:231], v168, s[8:11], s41 offen sc1\n" \
    "s_add_u32 s41, s41, 8192\n" \
    "buffer_load_dwordx4 v[232:235], v168, s[8:11], s41 offen sc1\n" \
    "s_waitcnt lgkmcnt(5)\n" \
    "v_mfma_f32_16x16x32_bf16 v[208:211], v[4:7], v[188:191], v[208:211]\n" \
    "ds_read_b128 v[184:187], v168 offset:26624\n" \
    "v_mfma_f32_16x16x32_bf16 v[212:215], v[84:87], v[188:191], v[212:215]\n" \
    "s_mul_i32 s39, s33, 98304\n" \
    "s_add_u32 s39, s39, 6291456\n" \
    "s_waitcnt lgkmcnt(5)\n" \
    "v_mfma_f32_16x16x32_bf16 v[208:211], v[8:11], v[192:195], v[208:211]\n" \
    "ds_read_b128 v[188:191], v168 offset:27648\n" \
    "v_mfma_f32_16x16x32_bf16 v[212:215], v[88:91], v[192:195], v[212:215]\n" \
    "s_add_u32 s40, s39, %[pdist]*98304\n" \
    "s_cmp_lt_u32 s33, 64-%[pdist]\n" \
    "s_cselect_b32 s40, s40, 0x80000000\n" \
    "s_waitcnt lgkmcnt(5)\n" \
    "v_mfma_f32_16x16x32_bf16 v[208:211], v[12:15], v[196:199], v[208:211]\n" \
    "ds_read_b128 v[192:195], v168 offset:28672\n" \
    "v_mfma_f32_16x16x32_bf16 v[212:215], v[92:95], v[196:199], v[212:215]\n" \
    "s_waitcnt lgkmcnt(4)\n" \
    "v_add_f32 v248, v248, v250\n" \
    "v_mfma_f32_16x16x32_bf16 v[208:211], v[16:19], v[200:203], v[208:211]\n" \
    "ds_read_b128 v[196:199], v168 offset:29696\n" \
    "v_mfma_f32_16x16x32_bf16 v[212:215], v[96:99], v[200:203], v[212:215]\n" \
    "v_add_f32 v249, v249, v251\n" \
    "s_waitcnt lgkmcnt(4)\n" \
    "v_mfma_f32_16x16x32_bf16 v[208:211], v[20:23], v[204:207], v[208:211]\n" \
    "ds_read_b128 v[200:203], v168 offset:30720\n" \
    "v_mfma_f32_16x16x32_bf16 v[212:215], v[100:103], v[204:207], v[212:215]\n" \
    "v_permlane16_swap_b32 v248, v249\n" \
    "s_waitcnt lgkmcnt(4)\n" \
    "v_mfma_f32_16x16x32_bf16 v[208:211], v[24:27], v[184:187], v[208:211]\n" \
    "ds_read_b128 v[204:207], v168 offset:31744\n" \
    "v_mfma_f32_16x16x32_bf16 v[212:215], v[104:107], v[184:187], v[212:215]\n" \
    "v_add_f32 v250, v248, v249\n" \
    "s_waitcnt lgkmcnt(4)\n" \
    "v_mfma_f32_16x16x32_bf16 v[208:211], v[28:31], v[188:191], v[208:211]\n" \
    "ds_read_b128 v[184:187], v168 offset:32768\n" \
    "v_mfma_f32_16x16x32_bf16 v[212:215], v[108:111], v[188:191], v[212:215]\n" \
    "v_add_f32 v248, v248, v249\n" \
    "s_waitcnt lgkmcnt(4)\n" \
    "v_mfma_f32_16x16x32_bf16 v[208:211], v[32:35], v[192:195], v[208:211]\n" \
    "ds_read_b128 v[188:191], v168 offset:33792\n" \
    "v_mfma_f32_16x16x32_bf16 v[212:215], v[112:115], v[192:195], v[212:215]\n" \
    "v_permlane32_swap_b32 v248, v250\n" \
    "s_waitcnt lgkmcnt(4)\n" \
    "v_mfma_f32_16x16x32_bf16 v[208:211], v[36:39], v[196:199], v[208:211]\n" \
    "ds_read_b128 v[192:195], v168 offset:34816\n" \
    "v_mfma_f32_16x16x32_bf16 v[212:215], v[116:119], v[196:199], v[212:215]\n" \
    "v_add_f32 v249, v248, v250\n" \
    "s_waitcnt lgkmcnt(4)\n" \
    "v_mfma_f32_16x16x32_bf16 v[208:211], v[40:43], v[200:203], v[208:211]\n" \
    "ds_read_b128 v[196:199], v168 offset:35840\n" \
    "v_mfma_f32_16x16x32_bf16 v[212:215], v[120:123], v[200:203], v[212:215]\n" \
    "v_add_f32 v248, v248, v250\n" \
    "s_add_u32 s48, s32, 3\n" \
    "s_min_u32 s48, s48, 64\n" \
    "s_and_b32 s49, s48, 1\n" \
    "s_xor_b32 s49, s49, 1\n" \
    "s_lshl_b32 s49, s49, 9\n" \
    "s_add_u32 s49, s49, s18\n" \
    "buffer_load_dwordx2 v[176:177], off, s[8:11], s49 sc1\n" \
    "s_waitcnt lgkmcnt(4)\n" \
    "v_mfma_f32_16x16x32_bf16 v[208:211], v[44:47], v[204:207], v[208:211]\n" \
    "ds_read_b128 v[200:203], v168 offset:36864\n" \
    "v_mfma_f32_16x16x32_bf16 v[212:215], v[124:127], v[204:207], v[212:215]\n" \
    "v_permlane16_swap_b32 v248, v249\n" \
    "s_waitcnt lgkmcnt(4)\n" \
    "v_mfma_f32_16x16x32_bf16 v[208:211], v[48:51], v[184:187], v[208:211]\n" \
    "ds_read_b128 v[204:207], v168 offset:37888\n" \
    "v_mfma_f32_16x16x32_bf16 v[212:215], v[128:131], v[184:187], v[212:215]\n" \
    "v_mul_f32 v252, 0x3acccccd, v248\n" \
    "s_waitcnt lgkmcnt(4)\n" \
    "v_mfma_f32_16x16x32_bf16 v[208:211], v[52:55], v[188:191], v[208:211]\n" \
    "ds_read_b128 v[184:187], v168 offset:38912\n" \
    "v_mfma_f32_16x16x32_bf16 v[212:215], v[132:135], v[188:191], v[212:215]\n" \
    "v_mul_f32 v249, 0x3acccccd, v249\n" \
    "s_waitcnt lgkmcnt(4)\n" \
    "v_mfma_f32_16x16x32_bf16 v[208:211], v[56:59], v[192:195], v[208:211]\n" \
    "ds_read_b128 v[188:191], v168 offset:39936\n" \
    "v_mfma_f32_16x16x32_bf16 v[212:215], v[136:139], v[192:195], v[212:215]\n" \
    "v_fma_f32 v249, -v252, v252, v249\n" \
    "s_waitcnt lgkmcnt(4)\n" \
    "v_mfma_f32_16x16x32_bf16 v[208:211], v[60:63], v[196:199], v[208:211]\n" \
    "v_mfma_f32_16x16x32_bf16 v[212:215], v[140:143], v[196:199], v[212:215]\n" \
    "v_max_f32 v249, 0, v249\n" \
    "s_waitcnt lgkmcnt(3)\n" \
    "v_mfma_f32_16x16x32_bf16 v[208:211], v[64:67], v[200:203], v[208:211]\n" \
    "v_mfma_f32_16x16x32_bf16 v[212:215], v[144:147], v[200:203], v[212:215]\n" \
    "v_add_f32 v249, 0x3727c5ac, v249\n" \
    "s_waitcnt lgkmcnt(2)\n" \
    "v_mfma_f32_16x16x32_bf16 v[208:211], v[68:71], v[204:207], v[208:211]\n" \
    "v_mfma_f32_16x16x32_bf16 v[212:215], v[148:151], v[204:207], v[212:215]\n" \
    "v_rsq_f32 v253, v249\n" \
    "s_waitcnt lgkmcnt(1)\n" \
    "v_mfma_f32_16x16x32_bf16 v[208:211], v[72:75], v[184:187], v[208:211]\n" \
    "v_mfma_f32_16x16x32_bf16 v[212:215], v[152:155], v[184:187], v[212:215]\n" \
    "v_mul_f32 v251, 0xbfb8aa3b, v253\n" \
    "s_waitcnt lgkmcnt(0)\n" \
    "v_mfma_f32_16x16x32_bf16 v[208:211], v[76:79], v[188:191], v[208:211]\n" \
    "v_mfma_f32_16x16x32_bf16 v[212:215], v[156:159], v[188:191], v[212:215]\n" \
    "s_nop 6\n" \
    "v_fma_f32 v208, -v252, v160, v208\n" \
    "v_fma_f32 v209, -v252, v161, v209\n" \
    "v_fma_f32 v210, -v252, v162, v210\n" \
    "v_fma_f32 v211, -v252, v163, v211\n" \
    "v_mul_f32 v208, v251, v208\n" \
    "v_mul_f32 v209, v251, v209\n" \
    "v_mul_f32 v210, v251, v210\n" \
    "v_mul_f32 v211, v251, v211\n" \
    "v_exp_f32 v208, v208\n" \
    "v_fma_f32 v212, -v252, v164, v212\n" \
    "v_exp_f32 v209, v209\n" \
    "v_fma_f32 v213, -v252, v165, v213\n" \
    "v_exp_f32 v210, v210\n" \
    "v_fma_f32 v214, -v252, v166, v214\n" \
    "v_exp_f32 v211, v211\n" \
    "v_fma_f32 v215, -v252, v167, v215\n" \
    "v_add_f32 v208, 1.0, v208\n" \
    "v_add_f32 v209, 1.0, v209\n" \
    "v_add_f32 v210, 1.0, v210\n" \
    "v_add_f32 v211, 1.0, v211\n" \
    "v_rcp_f32 v208, v208\n" \
    "v_mul_f32 v212, v253, v212\n" \
    "v_rcp_f32 v209, v209\n" \
    "v_mul_f32 v213, v253, v213\n" \
    "v_rcp_f32 v210, v210\n" \
    "v_mul_f32 v214, v253, v214\n" \
    "v_rcp_f32 v211, v211\n" \
    "v_mul_f32 v215, v253, v215\n" \
    "v_mul_f32 v208, v208, v212\n" \
    "v_mul_f32 v209, v209, v213\n" \
    "v_mul_f32 v210, v210, v214\n" \
    "v_mul_f32 v211, v211, v215\n" \
    "v_cvt_pk_bf16_f32 v240, v208, v209\n" \
    "v_cvt_pk_bf16_f32 v241, v210, v211\n" \
    "v_min_u32 v240, -2, v240\n" \
    "v_min_u32 v241, -2, v241\n" \
    "buffer_store_dwordx2 v[240:241], v173, s[8:11], s39 offen sc1\n" \
    "buffer_store_dwordx2 v[182:183], v173, s[8:11], s40 offen sc1\n" \
    "s_add_u32 s33, s33, 1\n" \
    "s_add_u32 s32, s32, 1\n" \
    "s_cmp_lt_u32 s32, 64\n" \
    "s_cbranch_scc1 .Lup_mY_%=\n" \
    "s_add_u32 s18, s18, 1024\n" \
    "s_waitcnt vmcnt(0)\n" \
    "s_add_u32 s48, s33, 2\n" \
    "s_min_u32 s48, s48, 64\n" \
    "s_and_b32 s49, s48, 1\n" \
    "s_xor_b32 s49, s49, 1\n" \
    "s_lshl_b32 s49, s49, 9\n" \
    "s_add_u32 s49, s49, s18\n" \
    "buffer_load_dwordx2 v[176:177], off, s[8:11], s49 sc1\n" \
    "s_waitcnt vmcnt(0)\n" \
    ".Lup_nY_%=:\n" \
    "s_barrier\n" \
    "s_mov_b32 s37, 0\n" \
    "s_waitcnt vmcnt(2)\n" \
    ".Lup_Ydp_%=:\n" \
    "v_readfirstlane_b32 s36, v177\n" \
    "s_nop 0\n" \
    "s_cmp_gt_i32 s36, s48\n" \
    "s_cbranch_scc1 .Lup_Ydx_%=\n" \
    "s_cmp_eq_u32 s36, s48\n" \
    "s_cbranch_scc1 .Lup_Ydw_%=\n" \
    "s_sleep 3\n" \
    "buffer_load_dwordx2 v[176:177], off, s[8:11], s49 sc1\n" \
    "s_waitcnt vmcnt(0)\n" \
    "s_add_u32 s37, s37, 1\n" \
    "s_cmp_lt_u32 s37, s38\n" \
    "s_cbranch_scc1 .Lup_Ydp_%=\n" \
    "s_mov_b32 s38, 0\n" \
    "s_branch .Lup_Ydx_%=\n" \
    ".Lup_Ydw_%=:\n" \
    "v_readfirstlane_b32 s36, v176\n" \
    "s_cmp_eq_u32 s33, 63\n" \
    "s_cselect_b32 s34, %[dlast], 0\n" \
    "s_add_u32 s36, s36, s34\n" \
    "s_add_u32 s36, s36, %[dx]\n" \
    "s_branch .Lup_Ydtb_%=\n" \
    ".Lup_Ydta_%=:\n" \
    "s_sleep 1\n" \
    ".Lup_Ydtb_%=:\n" \
    "s_memrealtime vcc\n" \
    "s_waitcnt lgkmcnt(0)\n" \
    "s_sub_i32 vcc_lo, s36, vcc_lo\n" \
    "s_cmp_gt_i32 vcc_lo, 8\n" \
    "s_cbranch_scc1 .Lup_Ydta_%=\n" \
    "s_cmp_gt_i32 vcc_lo, 0\n" \
    "s_cbranch_scc1 .Lup_Ydtb_%=\n" \
    ".Lup_Ydx_%=:\n" \
    "s_mul_i32 s41, s33, 40960\n" \
    "s_add_u32 s41, s41, s17\n" \
    "s_add_u32 s41, s41, 2621440\n" \
    "buffer_load_dwordx4 v[228:231], v168, s[8:11], s41 offen sc1\n" \
    "s_add_u32 s41, s41, 8192\n" \
    "buffer_load_dwordx4 v[232:235], v168, s[8:11], s41 offen sc1\n" \
    "s_mov_b32 s37, 0\n" \
    ".Lup_srYd_%=:\n" \
    "s_waitcnt vmcnt(1)\n" \
    "v_max3_u32 v254, v228, v229, v230\n" \
    "s_waitcnt vmcnt(0)\n" \
    "v_max3_u32 v254, v254, v231, v232\n" \
    "v_max3_u32 v254, v254, v233, v234\n" \
    "v_max_u32 v254, v254, v235\n" \
    "v_cmp_eq_u32 vcc, -1, v254\n" \
    "s_nop 0\n" \
    "s_cbranch_vccnz .Lup_scYd_%=\n" \
    ".Lup_sxYd_%=:\n" \
    "v_mfma_f32_16x16x32_bf16 v[244:247], v[228:231], v[228:231], 0\n" \
    "ds_write_b128 v169, v[228:231] offset:20480\n" \
    "v_mfma_f32_16x16x32_bf16 v[244:247], v[232:235], v[232:235], v[244:247]\n" \
    "ds_write_b128 v169, v[232:235] offset:28672\n" \
    "v_mfma_f32_16x16x32_bf16 v[240:243], v[178:181], v[228:231], 0\n" \
    "v_mfma_f32_16x16x32_bf16 v[240:243], v[178:181], v[232:235], v[240:243]\n" \
    "s_nop 4\n" \
    "v_cndmask_b32 v254, v244, v245, s[44:45]\n" \
    "v_cndmask_b32 v255, v246, v247, s[44:45]\n" \
    "s_nop 0\n" \
    "v_cndmask_b32 v241, v254, v255, s[46:47]\n" \
    "ds_write_b64 v171, v[240:241] offset:1024\n" \
    "s_waitcnt lgkmcnt(0)\n" \
    "s_barrier\n" \
    "ds_read_b128 v[184:187], v168 offset:20480\n" \
    "ds_read_b128 v[188:191], v168 offset:21504\n" \
    "ds_read_b128 v[192:195], v168 offset:22528\n" \
    "ds_read_b128 v[196:199], v168 offset:23552\n" \
    "ds_read_b128 v[200:203], v168 offset:24576\n" \
    "ds_read2_b64 v[248:251], v172 offset0:128 offset1:144\n" \
    "s_waitcnt lgkmcnt(5)\n" \
    "v_mfma_f32_16x16x32_bf16 v[208:211], v[0:3], v[184:187], 0\n" \
    "ds_read_b128 v[204:207], v168 offset:25600\n" \
    "v_mfma_f32_16x16x32_bf16 v[212:215], v[80:83], v[184:187], 0\n" \
    "s_waitcnt lgkmcnt(5)\n" \
    "v_mfma_f32_16x16x32_bf16 v[208:211], v[4:7], v[188:191], v[208:211]\n" \
    "ds_read_b128 v[184:187], v168 offset:26624\n" \
    "v_mfma_f32_16x16x32_bf16 v[212:215], v[84:87], v[188:191], v[212:215]\n" \
    "s_mul_i32 s39, s33, 98304\n" \
    "s_add_u32 s39, s39, 6291456\n" \
    "s_waitcnt lgkmcnt(5)\n" \
    "v_mfma_f32_16x16x32_bf16 v[208:211], v[8:11], v[192:195], v[208:211]\n" \
    "ds_read_b128 v[188:191], v168 offset:27648\n" \
    "v_mfma_f32_16x16x32_bf16 v[212:215], v[88:91], v[192:195], v[212:215]\n" \
    "s_add_u32 s40, s39, %[pdist]*98304\n" \
    "s_cmp_lt_u32 s33, 64-%[pdist]\n" \
    "s_cselect_b32 s40, s40, 0x80000000\n" \
    "s_waitcnt lgkmcnt(5)\n" \
    "v_mfma_f32_16x16x32_bf16 v[208:211], v[12:15], v[196:199], v[208:211]\n" \
    "ds_read_b128 v[192:195], v168 offset:28672\n" \
    "v_mfma_f32_16x16x32_bf16 v[212:215], v[92:95], v[196:199], v[212:215]\n" \
    "s_waitcnt lgkmcnt(4)\n" \
    "v_add_f32 v248, v248, v250\n" \
    "v_mfma_f32_16x16x32_bf16 v[208:211], v[16:19], v[200:203], v[208:211]\n" \
    "ds_read_b128 v[196:199], v168 offset:29696\n" \
    "v_mfma_f32_16x16x32_bf16 v[212:215], v[96:99], v[200:203], v[212:215]\n" \
    "v_add_f32 v249, v249, v251\n" \
    "s_waitcnt lgkmcnt(4)\n" \
    "v_mfma_f32_16x16x32_bf16 v[208:211], v[20:23], v[204:207], v[208:211]\n" \
    "ds_read_b128 v[200:203], v168 offset:30720\n" \
    "v_mfma_f32_16x16x32_bf16 v[212:215], v[100:103], v[204:207], v[212:215]\n" \
    "v_permlane16_swap_b32 v248, v249\n" \
    "s_waitcnt lgkmcnt(4)\n" \
    "v_mfma_f32_16x16x32_bf16 v[208:211], v[24:27], v[184:187], v[208:211]\n" \
    "ds_read_b128 v[204:207], v168 offset:31744\n" \
    "v_mfma_f32_16x16x32_bf16 v[212:215], v[104:107], v[184:187], v[212:215]\n" \
    "v_add_f32 v250, v248, v249\n" \
    "s_waitcnt lgkmcnt(4)\n" \
    "v_mfma_f32_16x16x32_bf16 v[208:211], v[28:31], v[188:191], v[208:211]\n" \
    "ds_read_b128 v[184:187], v168 offset:32768\n" \
    "v_mfma_f32_16x16x32_bf16 v[212:215], v[108:111], v[188:191], v[212:215]\n" \
    "v_add_f32 v248, v248, v249\n" \
    "s_waitcnt lgkmcnt(4)\n" \
    "v_mfma_f32_16x16x32_bf16 v[208:211], v[32:35], v[192:195], v[208:211]\n" \
    "ds_read_b128 v[188:191], v168 offset:33792\n" \
    "v_mfma_f32_16x16x32_bf16 v[212:215], v[112:115], v[192:195], v[212:215]\n" \
    "v_permlane32_swap_b32 v248, v250\n" \
    "s_waitcnt lgkmcnt(4)\n" \
    "v_mfma_f32_16x16x32_bf16 v[208:211], v[36:39], v[196:199], v[208:211]\n" \
    "ds_read_b128 v[192:195], v168 offset:34816\n" \
    "v_mfma_f32_16x16x32_bf16 v[212:215], v[116:119], v[196:199], v[212:215]\n" \
    "v_add_f32 v249, v248, v250\n" \
    "s_waitcnt lgkmcnt(4)\n" \
    "v_mfma_f32_16x16x32_bf16 v[208:211], v[40:43], v[200:203], v[208:211]\n" \
    "ds_read_b128 v[196:199], v168 offset:35840\n" \
    "v_mfma_f32_16x16x32_bf16 v[212:215], v[120:123], v[200:203], v[212:215]\n" \
    "v_add_f32 v248, v248, v250\n" \
    "s_add_u32 s48, s33, 3\n" \
    "s_min_u32 s48, s48, 64\n" \
    "s_and_b32 s49, s48, 1\n" \
    "s_xor_b32 s49, s49, 1\n" \
    "s_lshl_b32 s49, s49, 9\n" \
    "s_add_u32 s49, s49, s18\n" \
    "buffer_load_dwordx2 v[176:177], off, s[8:11], s49 sc1\n" \
    "s_waitcnt lgkmcnt(4)\n" \
    "v_mfma_f32_16x16x32_bf16 v[208:211], v[44:47], v[204:207], v[208:211]\n" \
    "ds_read_b128 v[200:203], v168 offset:36864\n" \
    "v_mfma_f32_16x16x32_bf16 v[212:215], v[124:127], v[204:207], v[212:215]\n" \
    "v_permlane16_swap_b32 v248, v249\n" \
    "s_waitcnt lgkmcnt(4)\n" \
    "v_mfma_f32_16x16x32_bf16 v[208:211], v[48:51], v[184:187], v[208:211]\n" \
    "ds_read_b128 v[204:207], v168 offset:37888\n" \
    "v_mfma_f32_16x16x32_bf16 v[212:215], v[128:131], v[184:187], v[212:215]\n" \
    "v_mul_f32 v252, 0x3acccccd, v248\n" \
    "s_waitcnt lgkmcnt(4)\n" \
    "v_mfma_f32_16x16x32_bf16 v[208:211], v[52:55], v[188:191], v[208:211]\n" \
    "ds_read_b128 v[184:187], v168 offset:38912\n" \
    "v_mfma_f32_16x16x32_bf16 v[212:215], v[132:135], v[188:191], v[212:215]\n" \
    "v_mul_f32 v249, 0x3acccccd, v249\n" \
    "s_waitcnt lgkmcnt(4)\n" \
    "v_mfma_f32_16x16x32_bf16 v[208:211], v[56:59], v[192:195], v[208:211]\n" \
    "ds_read_b128 v[188:191], v168 offset:39936\n" \
    "v_mfma_f32_16x16x32_bf16 v[212:215], v[136:139], v[192:195], v[212:215]\n" \
    "v_fma_f32 v249, -v252, v252, v249\n" \
    "s_waitcnt lgkmcnt(4)\n" \
    "v_mfma_f32_16x16x32_bf16 v[208:211], v[60:63], v[196:199], v[208:211]\n" \
    "v_mfma_f32_16x16x32_bf16 v[212:215], v[140:143], v[196:199], v[212:215]\n" \
    "v_max_f32 v249, 0, v249\n" \
    "s_waitcnt lgkmcnt(3)\n" \
    "v_mfma_f32_16x16x32_bf16 v[208:211], v[64:67], v[200:203], v[208:211]\n" \
    "v_mfma_f32_16x16x32_bf16 v[212:215], v[144:147], v[200:203], v[212:215]\n" \
    "v_add_f32 v249, 0x3727c5ac, v249\n" \
    "s_waitcnt lgkmcnt(2)\n" \
    "v_mfma_f32_16x16x32_bf16 v[208:211], v[68:71], v[204:207], v[208:211]\n" \
    "v_mfma_f32_16x16x32_bf16 v[212:215], v[148:151], v[204:207], v[212:215]\n" \
    "v_rsq_f32 v253, v249\n" \
    "s_waitcnt lgkmcnt(1)\n" \
    "v_mfma_f32_16x16x32_bf16 v[208:211], v[72:75], v[184:187], v[208:211]\n" \
    "v_mfma_f32_16x16x32_bf16 v[212:215], v[152:155], v[184:187], v[212:215]\n" \
    "v_mul_f32 v251, 0xbfb8aa3b, v253\n" \
    "s_waitcnt lgkmcnt(0)\n" \
    "v_mfma_f32_16x16x32_bf16 v[208:211], v[76:79], v[188:191], v[208:211]\n" \
    "v_mfma_f32_16x16x32_bf16 v[212:215], v[156:159], v[188:191], v[212:215]\n" \
    "s_nop 6\n" \
    "v_fma_f32 v208, -v252, v160, v208\n" \
    "v_fma_f32 v209, -v252, v161, v209\n" \
    "v_fma_f32 v210, -v252, v162, v210\n" \
    "v_fma_f32 v211, -v252, v163, v211\n" \
    "v_mul_f32 v208, v251, v208\n" \
    "v_mul_f32 v209, v251, v209\n" \
    "v_mul_f32 v210, v251, v210\n" \
    "v_mul_f32 v211, v251, v211\n" \
    "v_exp_f32 v208, v208\n" \
    "v_fma_f32 v212, -v252, v164, v212\n" \
    "v_exp_f32 v209, v209\n" \
    "v_fma_f32 v213, -v252, v165, v213\n" \
    "v_exp_f32 v210, v210\n" \
    "v_fma_f32 v214, -v252, v166, v214\n" \
    "v_exp_f32 v211, v211\n" \
    "v_fma_f32 v215, -v252, v167, v215\n" \
    "v_add_f32 v208, 1.0, v208\n" \
    "v_add_f32 v209, 1.0, v209\n" \
    "v_add_f32 v210, 1.0, v210\n" \
    "v_add_f32 v211, 1.0, v211\n" \
    "v_rcp_f32 v208, v208\n" \
    "v_mul_f32 v212, v253, v212\n" \
    "v_rcp_f32 v209, v209\n" \
    "v_mul_f32 v213, v253, v213\n" \
    "v_rcp_f32 v210, v210\n" \
    "v_mul_f32 v214, v253, v214\n" \
    "v_rcp_f32 v211, v211\n" \
    "v_mul_f32 v215, v253, v215\n" \
    "v_mul_f32 v208, v208, v212\n" \
    "v_mul_f32 v209, v209, v213\n" \
    "v_mul_f32 v210, v210, v214\n" \
    "v_mul_f32 v211, v211, v215\n" \
    "v_cvt_pk_bf16_f32 v240, v208, v209\n" \
    "v_cvt_pk_bf16_f32 v241, v210, v211\n" \
    "v_min_u32 v240, -2, v240\n" \
    "v_min_u32 v241, -2, v241\n" \
    "buffer_store_dwordx2 v[240:241], v173, s[8:11], s39 offen sc1\n" \
    "buffer_store_dwordx2 v[182:183], v173, s[8:11], s40 offen sc1\n" \
    "s_add_u32 s33, s33, 1\n" \
    "s_cmp_lt_u32 s33, 64\n" \
    "s_cbranch_scc1 .Lup_nY_%=\n" \
    "s_waitcnt vmcnt(0)\n" \
    "s_endpgm\n" \
    ".Lup_lO_%=:\n" \
    "s_add_u32 s48, s32, 2\n" \
    "s_min_u32 s48, s48, 64\n" \
    "s_and_b32 s49, s48, 1\n" \
    "s_xor_b32 s49, s49, 1\n" \
    "s_lshl_b32 s49, s49, 9\n" \
    "s_add_u32 s49, s49, s18\n" \
    "buffer_load_dwordx2 v[176:177], off, s[8:11], s49 sc1\n" \
    "s_mov_b32 s33, 0\n" \
    "s_waitcnt vmcnt(0)\n" \
    ".Lup_kO_%=:\n" \
    "s_mov_b32 s37, 0\n" \
    "s_waitcnt vmcnt(2)\n" \
    ".Lup_Oap_%=:\n" \
    "v_readfirstlane_b32 s36, v177\n" \
    "s_nop 0\n" \
    "s_cmp_gt_i32 s36, s48\n" \
    "s_cbranch_scc1 .Lup_Oax_%=\n" \
    "s_cmp_eq_u32 s36, s48\n" \
    "s_cbranch_scc1 .Lup_Oaw_%=\n" \
    "s_sleep 3\n" \
    "buffer_load_dwordx2 v[176:177], off, s[8:11], s49 sc1\n" \
    "s_waitcnt vmcnt(0)\n" \
    "s_add_u32 s37, s37, 1\n" \
    "s_cmp_lt_u32 s37, s38\n" \
    "s_cbranch_scc1 .Lup_Oap_%=\n" \
    "s_mov_b32 s38, 0\n" \
    "s_branch .Lup_Oax_%=\n" \
    ".Lup_Oaw_%=:\n" \
    "v_readfirstlane_b32 s36, v176\n" \
    "s_cmp_eq_u32 s32, 63\n" \
    "s_cselect_b32 s34, %[dlast], 0\n" \
    "s_add_u32 s36, s36, s34\n" \
    "s_add_u32 s36, s36, %[dx]\n" \
    "s_branch .Lup_Oatb_%=\n" \
    ".Lup_Oata_%=:\n" \
    "s_sleep 1\n" \
    ".Lup_Oatb_%=:\n" \
    "s_memrealtime vcc\n" \
    "s_waitcnt lgkmcnt(0)\n" \
    "s_sub_i32 vcc_lo, s36, vcc_lo\n" \
    "s_cmp_gt_i32 vcc_lo, 8\n" \
    "s_cbranch_scc1 .Lup_Oata_%=\n" \
    "s_cmp_gt_i32 vcc_lo, 0\n" \
    "s_cbranch_scc1 .Lup_Oatb_%=\n" \
    ".Lup_Oax_%=:\n" \
    "s_mul_i32 s41, s32, 40960\n" \
    "s_add_u32 s41, s41, s17\n" \
    "buffer_load_dwordx4 v[216:219], v168, s[8:11], s41 offen sc1\n" \
    "s_add_u32 s41, s41, 8192\n" \
    "buffer_load_dwordx4 v[220:223], v168, s[8:11], s41 offen sc1\n" \
    "s_add_u32 s41, s41, 8192\n" \
    "buffer_load_dwordx4 v[224:227], v168, s[8:11], s41 offen sc1\n" \
    "s_mov_b32 s37, 0\n" \
    ".Lup_srOa_%=:\n" \
    "s_waitcnt vmcnt(2)\n" \
    "v_max3_u32 v254, v216, v217, v218\n" \
    "s_waitcnt vmcnt(1)\n" \
    "v_max3_u32 v254, v254, v219, v220\n" \
    "v_max3_u32 v254, v254, v221, v222\n" \
    "s_waitcnt vmcnt(0)\n" \
    "v_max3_u32 v254, v254, v223, v224\n" \
    "v_max3_u32 v254, v254, v225, v226\n" \
    "v_max_u32 v254, v254, v227\n" \
    "v_cmp_eq_u32 vcc, -1, v254\n" \
    "s_nop 0\n" \
    "s_cbranch_vccnz .Lup_scOa_%=\n" \
    ".Lup_sxOa_%=:\n" \
    "v_mfma_f32_16x16x32_bf16 v[244:247], v[216:219], v[216:219], 0\n" \
    "ds_write_b128 v169, v[216:219] offset:0\n" \
    "v_mfma_f32_16x16x32_bf16 v[244:247], v[220:223], v[220:223], v[244:247]\n" \
    "ds_write_b128 v169, v[220:223] offset:8192\n" \
    "v_mfma_f32_16x16x32_bf16 v[244:247], v[224:227], v[224:227], v[244:247]\n" \
    "ds_write_b128 v169, v[224:227] offset:16384\n" \
    "v_mfma_f32_16x16x32_bf16 v[240:243], v[178:181], v[216:219], 0\n" \
    "v_mfma_f32_16x16x32_bf16 v[240:243], v[178:181], v[220:223], v[240:243]\n" \
    "v_mfma_f32_16x16x32_bf16 v[240:243], v[178:181], v[224:227], v[240:243]\n" \
    "s_nop 3\n" \
    "v_cndmask_b32 v254, v244, v245, s[44:45]\n" \
    "v_cndmask_b32 v255, v246, v247, s[44:45]\n" \
    "s_nop 1\n" \
    "v_cndmask_b32 v241, v254, v255, s[46:47]\n" \
    "ds_write_b64 v171, v[240:241] offset:0\n" \
    "s_waitcnt lgkmcnt(0)\n" \
    "s_barrier\n" \
    "ds_read_b128 v[184:187], v168 offset:0\n" \
    "ds_read_b128 v[188:191], v168 offset:1024\n" \
    "ds_read_b128 v[192:195], v168 offset:2048\n" \
    "ds_read_b128 v[196:199], v168 offset:3072\n" \
    "ds_read_b128 v[200:203], v168 offset:4096\n" \
    "ds_read2_b64 v[248:251], v172 offset0:0 offset1:16\n" \
    "s_waitcnt lgkmcnt(5)\n" \
    "v_mfma_f32_16x16x32_bf16 v[208:211], v[0:3], v[184:187], 0\n" \
    "ds_read_b128 v[204:207], v168 offset:5120\n" \
    "v_mfma_f32_16x16x32_bf16 v[212:215], v[80:83], v[184:187], 0\n" \
    "s_mul_i32 s41, s33, 40960\n" \
    "s_add_u32 s41, s41, s17\n" \
    "s_add_u32 s41, s41, 2621440\n" \
    "buffer_load_dwordx4 v[228:231], v168, s[8:11], s41 offen sc1\n" \
    "s_add_u32 s41, s41, 8192\n" \
    "buffer_load_dwordx4 v[232:235], v168, s[8:11], s41 offen sc1\n" \
    "s_add_u32 s41, s41, 8192\n" \
    "buffer_load_dwordx4 v[236:239], v168, s[8:11], s41 offen sc1\n" \
    "s_waitcnt lgkmcnt(5)\n" \
    "v_mfma_f32_16x16x32_bf16 v[208:211], v[4:7], v[188:191], v[208:211]\n" \
    "ds_read_b128 v[184:187], v168 offset:6144\n" \
    "v_mfma_f32_16x16x32_bf16 v[212:215], v[84:87], v[188:191], v[212:215]\n" \
    "s_mul_i32 s39, s32, 98304\n" \
    "s_add_u32 s39, s39, 0\n" \
    "s_waitcnt lgkmcnt(5)\n" \
    "v_mfma_f32_16x16x32_bf16 v[208:211], v[8:11], v[192:195], v[208:211]\n" \
    "ds_read_b128 v[188:191], v168 offset:7168\n" \
    "v_mfma_f32_16x16x32_bf16 v[212:215], v[88:91], v[192:195], v[212:215]\n" \
    "s_add_u32 s40, s39, %[pdist]*98304\n" \
    "s_cmp_lt_u32 s32, 64-%[pdist]\n" \
    "s_cselect_b32 s40, s40, 0x80000000\n" \
    "s_waitcnt lgkmcnt(5)\n" \
    "v_mfma_f32_16x16x32_bf16 v[208:211], v[12:15], v[196:199], v[208:211]\n" \
    "ds_read_b128 v[192:195], v168 offset:8192\n" \
    "v_mfma_f32_16x16x32_bf16 v[212:215], v[92:95], v[196:199], v[212:215]\n" \
    "s_waitcnt lgkmcnt(4)\n" \
    "v_add_f32 v248, v248, v250\n" \
    "v_mfma_f32_16x16x32_bf16 v[208:211], v[16:19], v[200:203], v[208:211]\n" \
    "ds_read_b128 v[196:199], v168 offset:9216\n" \
    "v_mfma_f32_16x16x32_bf16 v[212:215], v[96:99], v[200:203], v[212:215]\n" \
    "v_add_f32 v249, v249, v251\n" \
    "s_waitcnt lgkmcnt(4)\n" \
    "v_mfma_f32_16x16x32_bf16 v[208:211], v[20:23], v[204:207], v[208:211]\n" \
    "ds_read_b128 v[200:203], v168 offset:10240\n" \
    "v_mfma_f32_16x16x32_bf16 v[212:215], v[100:103], v[204:207], v[212:215]\n" \
    "v_permlane16_swap_b32 v248, v249\n" \
    "s_waitcnt lgkmcnt(4)\n" \
    "v_mfma_f32_16x16x32_bf16 v[208:211], v[24:27], v[184:187], v[208:211]\n" \
    "ds_read_b128 v[204:207], v168 offset:11264\n" \
    "v_mfma_f32_16x16x32_bf16 v[212:215], v[104:107], v[184:187], v[212:215]\n" \
    "v_add_f32 v250, v248, v249\n" \
    "s_waitcnt lgkmcnt(4)\n" \
    "v_mfma_f32_16x16x32_bf16 v[208:211], v[28:31], v[188:191], v[208:211]\n" \
    "ds_read_b128 v[184:187], v168 offset:12288\n" \
    "v_mfma_f32_16x16x32_bf16 v[212:215], v[108:111], v[188:191], v[212:215]\n" \
    "v_add_f32 v248, v248, v249\n" \
    "s_waitcnt lgkmcnt(4)\n" \
    "v_mfma_f32_16x16x32_bf16 v[208:211], v[32:35], v[192:195], v[208:211]\n" \
    "ds_read_b128 v[188:191], v168 offset:13312\n" \
    "v_mfma_f32_16x16x32_bf16 v[212:215], v[112:115], v[192:195], v[212:215]\n" \
    "v_permlane32_swap_b32 v248, v250\n" \
    "s_waitcnt lgkmcnt(4)\n" \
    "v_mfma_f32_16x16x32_bf16 v[208:211], v[36:39], v[196:199], v[208:211]\n" \
    "ds_read_b128 v[192:195], v168 offset:14336\n" \
    "v_mfma_f32_16x16x32_bf16 v[212:215], v[116:119], v[196:199], v[212:215]\n" \
    "v_add_f32 v249, v248, v250\n" \
    "s_waitcnt lgkmcnt(4)\n" \
    "v_mfma_f32_16x16x32_bf16 v[208:211], v[40:43], v[200:203], v[208:211]\n" \
    "ds_read_b128 v[196:199], v168 offset:15360\n" \
    "v_mfma_f32_16x16x32_bf16 v[212:215], v[120:123], v[200:203], v[212:215]\n" \
    "v_add_f32 v248, v248, v250\n" \
    "s_add_u32 s48, s32, 3\n" \
    "s_min_u32 s48, s48, 64\n" \
    "s_and_b32 s49, s48, 1\n" \
    "s_xor_b32 s49, s49, 1\n" \
    "s_lshl_b32 s49, s49, 9\n" \
    "s_add_u32 s49, s49, s18\n" \
    "buffer_load_dwordx2 v[176:177], off, s[8:11], s49 sc1\n" \
    "s_waitcnt lgkmcnt(4)\n" \
    "v_mfma_f32_16x16x32_bf16 v[208:211], v[44:47], v[204:207], v[208:211]\n" \
    "ds_read_b128 v[200:203], v168 offset:16384\n" \
    "v_mfma_f32_16x16x32_bf16 v[212:215], v[124:127], v[204:207], v[212:215]\n" \
    "v_permlane16_swap_b32 v248, v249\n" \
    "s_waitcnt lgkmcnt(4)\n" \
    "v_mfma_f32_16x16x32_bf16 v[208:211], v[48:51], v[184:187], v[208:211]\n" \
    "ds_read_b128 v[204:207], v168 offset:17408\n" \
    "v_mfma_f32_16x16x32_bf16 v[212:215], v[128:131], v[184:187], v[212:215]\n" \
    "v_mul_f32 v252, 0x3acccccd, v248\n" \
    "s_waitcnt lgkmcnt(4)\n" \
    "v_mfma_f32_16x16x32_bf16 v[208:211], v[52:55], v[188:191], v[208:211]\n" \
    "ds_read_b128 v[184:187], v168 offset:18432\n" \
    "v_mfma_f32_16x16x32_bf16 v[212:215], v[132:135], v[188:191], v[212:215]\n" \
    "v_mul_f32 v249, 0x3acccccd, v249\n" \
    "s_waitcnt lgkmcnt(4)\n" \
    "v_mfma_f32_16x16x32_bf16 v[208:211], v[56:59], v[192:195], v[208:211]\n" \
    "ds_read_b128 v[188:191], v168 offset:19456\n" \
    "v_mfma_f32_16x16x32_bf16 v[212:215], v[136:139], v[192:195], v[212:215]\n" \
    "v_fma_f32 v249, -v252, v252, v249\n" \
    "s_waitcnt lgkmcnt(4)\n" \
    "v_mfma_f32_16x16x32_bf16 v[208:211], v[60:63], v[196:199], v[208:211]\n" \
    "v_mfma_f32_16x16x32_bf16 v[212:215], v[140:143], v[196:199], v[212:215]\n" \
    "v_max_f32 v249, 0, v249\n" \
    "s_waitcnt lgkmcnt(3)\n" \
    "v_mfma_f32_16x16x32_bf16 v[208:211], v[64:67], v[200:203], v[208:211]\n" \
    "v_mfma_f32_16x16x32_bf16 v[212:215], v[144:147], v[200:203], v[212:215]\n" \
    "v_add_f32 v249, 0x3727c5ac, v249\n" \
    "s_waitcnt lgkmcnt(2)\n" \
    "v_mfma_f32_16x16x32_bf16 v[208:211], v[68:71], v[204:207], v[208:211]\n" \
    "v_mfma_f32_16x16x32_bf16 v[212:215], v[148:151], v[204:207], v[212:215]\n" \
    "v_rsq_f32 v253, v249\n" \
    "s_waitcnt lgkmcnt(1)\n" \
    "v_mfma_f32_16x16x32_bf16 v[208:211], v[72:75], v[184:187], v[208:211]\n" \
    "v_mfma_f32_16x16x32_bf16 v[212:215], v[152:155], v[184:187], v[212:215]\n" \
    "v_mul_f32 v251, 0xbfb8aa3b, v253\n" \
    "s_waitcnt lgkmcnt(0)\n" \
    "v_mfma_f32_16x16x32_bf16 v[208:211], v[76:79], v[188:191], v[208:211]\n" \
    "v_mfma_f32_16x16x32_bf16 v[212:215], v[156:159], v[188:191], v[212:215]\n" \
    "s_nop 6\n" \
    "v_fma_f32 v208, -v252, v160, v208\n" \
    "v_fma_f32 v209, -v252, v161, v209\n" \
    "v_fma_f32 v210, -v252, v162, v210\n" \
    "v_fma_f32 v211, -v252, v163, v211\n" \
    "v_mul_f32 v208, v251, v208\n" \
    "v_mul_f32 v209, v251, v209\n" \
    "v_mul_f32 v210, v251, v210\n" \
    "v_mul_f32 v211, v251, v211\n" \
    "v_exp_f32 v208, v208\n" \
    "v_fma_f32 v212, -v252, v164, v212\n" \
    "v_exp_f32 v209, v209\n" \
    "v_fma_f32 v213, -v252, v165, v213\n" \
    "v_exp_f32 v210, v210\n" \
    "v_fma_f32 v214, -v252, v166, v214\n" \
    "v_exp_f32 v211, v211\n" \
    "v_fma_f32 v215, -v252, v167, v215\n" \
    "v_add_f32 v208, 1.0, v208\n" \
    "v_add_f32 v209, 1.0, v209\n" \
    "v_add_f32 v210, 1.0, v210\n" \
    "v_add_f32 v211, 1.0, v211\n" \
    "v_rcp_f32 v208, v208\n" \
    "v_mul_f32 v212, v253, v212\n" \
    "v_rcp_f32 v209, v209\n" \
    "v_mul_f32 v213, v253, v213\n" \
    "v_rcp_f32 v210, v210\n" \
    "v_mul_f32 v214, v253, v214\n" \
    "v_rcp_f32 v211, v211\n" \
    "v_mul_f32 v215, v253, v215\n" \
    "v_mul_f32 v208, v208, v212\n" \
    "v_mul_f32 v209, v209, v213\n" \
    "v_mul_f32 v210, v210, v214\n" \
    "v_mul_f32 v211, v211, v215\n" \
    "v_cvt_pk_bf16_f32 v240, v208, v209\n" \
    "v_cvt_pk_bf16_f32 v241, v210, v211\n" \
    "v_min_u32 v240, -2, v240\n" \
    "v_min_u32 v241, -2, v241\n" \
    "buffer_store_dwordx2 v[240:241], v173, s[8:11], s39 offen sc1\n" \
    "buffer_store_dwordx2 v[182:183], v173, s[8:11], s40 offen sc1\n" \
    "s_add_u32 s32, s32, 1\n" \
    "s_cmp_lt_u32 s32, %[eup]\n" \
    "s_barrier\n" \
    "s_cbranch_scc1 .Lup_kO_%=\n" \
    ".Lup_mO_%=:\n" \
    "s_mov_b32 s37, 0\n" \
    "s_waitcnt vmcnt(2)\n" \
    ".Lup_Obp_%=:\n" \
    "v_readfirstlane_b32 s36, v177\n" \
    "s_nop 0\n" \
    "s_cmp_gt_i32 s36, s48\n" \
    "s_cbranch_scc1 .Lup_Obx_%=\n" \
    "s_cmp_eq_u32 s36, s48\n" \
    "s_cbranch_scc1 .Lup_Obw_%=\n" \
    "s_sleep 3\n" \
    "buffer_load_dwordx2 v[176:177], off, s[8:11], s49 sc1\n" \
    "s_waitcnt vmcnt(0)\n" \
    "s_add_u32 s37, s37, 1\n" \
    "s_cmp_lt_u32 s37, s38\n" \
    "s_cbranch_scc1 .Lup_Obp_%=\n" \
    "s_mov_b32 s38, 0\n" \
    "s_branch .Lup_Obx_%=\n" \
    ".Lup_Obw_%=:\n" \
    "v_readfirstlane_b32 s36, v176\n" \
    "s_cmp_eq_u32 s32, 63\n" \
    "s_cselect_b32 s34, %[dlast], 0\n" \
    "s_add_u32 s36, s36, s34\n" \
    "s_add_u32 s36, s36, %[dx]\n" \
    "s_branch .Lup_Obtb_%=\n" \
    ".Lup_Obta_%=:\n" \
    "s_sleep 1\n" \
    ".Lup_Obtb_%=:\n" \
    "s_memrealtime vcc\n" \
    "s_waitcnt lgkmcnt(0)\n" \
    "s_sub_i32 vcc_lo, s36, vcc_lo\n" \
    "s_cmp_gt_i32 vcc_lo, 8\n" \
    "s_cbranch_scc1 .Lup_Obta_%=\n" \
    "s_cmp_gt_i32 vcc_lo, 0\n" \
    "s_cbranch_scc1 .Lup_Obtb_%=\n" \
    ".Lup_Obx_%=:\n" \
    "s_mul_i32 s41, s32, 40960\n" \
    "s_add_u32 s41, s41, s17\n" \
    "buffer_load_dwordx4 v[216:219], v168, s[8:11], s41 offen sc1\n" \
    "s_add_u32 s41, s41, 8192\n" \
    "buffer_load_dwordx4 v[220:223], v168, s[8:11], s41 offen sc1\n" \
    "s_add_u32 s41, s41, 8192\n" \
    "buffer_load_dwordx4 v[224:227], v168, s[8:11], s41 offen sc1\n" \
    "s_mov_b32 s37, 0\n" \
    ".Lup_srOb_%=:\n" \
    "s_waitcnt vmcnt(2)\n" \
    "v_max3_u32 v254, v216, v217, v218\n" \
    "s_waitcnt vmcnt(1)\n" \
    "v_max3_u32 v254, v254, v219, v220\n" \
    "v_max3_u32 v254, v254, v221, v222\n" \
    "s_waitcnt vmcnt(0)\n" \
    "v_max3_u32 v254, v254, v223, v224\n" \
    "v_max3_u32 v254, v254, v225, v226\n" \
    "v_max_u32 v254, v254, v227\n" \
    "v_cmp_eq_u32 vcc, -1, v254\n" \
    "s_nop 0\n" \
    "s_cbranch_vccnz .Lup_scOb_%=\n" \
    ".Lup_sxOb_%=:\n" \
    "v_mfma_f32_16x16x32_bf16 v[244:247], v[216:219], v[216:219], 0\n" \
    "ds_write_b128 v169, v[216:219] offset:0\n" \
    "v_mfma_f32_16x16x32_bf16 v[244:247], v[220:223], v[220:223], v[244:247]\n" \
    "ds_write_b128 v169, v[220:223] offset:8192\n" \
    "v_mfma_f32_16x16x32_bf16 v[244:247], v[224:227], v[224:227], v[244:247]\n" \
    "ds_write_b128 v169, v[224:227] offset:16384\n" \
    "v_mfma_f32_16x16x32_bf16 v[240:243], v[178:181], v[216:219], 0\n" \
    "v_mfma_f32_16x16x32_bf16 v[240:243], v[178:181], v[220:223], v[240:243]\n" \
    "v_mfma_f32_16x16x32_bf16 v[240:243], v[178:181], v[224:227], v[240:243]\n" \
    "s_nop 3\n" \
    "v_cndmask_b32 v254, v244, v245, s[44:45]\n" \
    "v_cndmask_b32 v255, v246, v247, s[44:45]\n" \
    "s_nop 1\n" \
    "v_cndmask_b32 v241, v254, v255, s[46:47]\n" \
    "ds_write_b64 v171, v[240:241] offset:0\n" \
    "s_waitcnt lgkmcnt(0)\n" \
    "s_barrier\n" \
    "ds_read_b128 v[184:187], v168 offset:0\n" \
    "ds_read_b128 v[188:191], v168 offset:1024\n" \
    "ds_read_b128 v[192:195], v168 offset:2048\n" \
    "ds_read_b128 v[196:199], v168 offset:3072\n" \
    "ds_read_b128 v[200:203], v168 offset:4096\n" \
    "ds_read2_b64 v[248:251], v172 offset0:0 offset1:16\n" \
    "s_waitcnt lgkmcnt(5)\n" \
    "v_mfma_f32_16x16x32_bf16 v[208:211], v[0:3], v[184:187], 0\n" \
    "ds_read_b128 v[204:207], v168 offset:5120\n" \
    "v_mfma_f32_16x16x32_bf16 v[212:215], v[80:83], v[184:187], 0\n" \
    "s_waitcnt lgkmcnt(5)\n" \
    "v_mfma_f32_16x16x32_bf16 v[208:211], v[4:7], v[188:191], v[208:211]\n" \
    "ds_read_b128 v[184:187], v168 offset:6144\n" \
    "v_mfma_f32_16x16x32_bf16 v[212:215], v[84:87], v[188:191], v[212:215]\n" \
    "s_mul_i32 s39, s32, 98304\n" \
    "s_add_u32 s39, s39, 0\n" \
    "s_waitcnt lgkmcnt(5)\n" \
    "v_mfma_f32_16x16x32_bf16 v[208:211], v[8:11], v[192:195], v[208:211]\n" \
    "ds_read_b128 v[188:191], v168 offset:7168\n" \
    "v_mfma_f32_16x16x32_bf16 v[212:215], v[88:91], v[192:195], v[212:215]\n" \
    "s_add_u32 s40, s39, %[pdist]*98304\n" \
    "s_cmp_lt_u32 s32, 64-%[pdist]\n" \
    "s_cselect_b32 s40, s40, 0x80000000\n" \
    "s_waitcnt lgkmcnt(5)\n" \
    "v_mfma_f32_16x16x32_bf16 v[208:211], v[12:15], v[196:199], v[208:211]\n" \
    "ds_read_b128 v[192:195], v168 offset:8192\n" \
    "v_mfma_f32_16x16x32_bf16 v[212:215], v[92:95], v[196:199], v[212:215]\n" \
    "s_waitcnt lgkmcnt(4)\n" \
    "v_add_f32 v248, v248, v250\n" \
    "v_mfma_f32_16x16x32_bf16 v[208:211], v[16:19], v[200:203], v[208:211]\n" \
    "ds_read_b128 v[196:199], v168 offset:9216\n" \
    "v_mfma_f32_16x16x32_bf16 v[212:215], v[96:99], v[200:203], v[212:215]\n" \
    "v_add_f32 v249, v249, v251\n" \
    "s_waitcnt lgkmcnt(4)\n" \
    "v_mfma_f32_16x16x32_bf16 v[208:211], v[20:23], v[204:207], v[208:211]\n" \
    "ds_read_b128 v[200:203], v168 offset:10240\n" \
    "v_mfma_f32_16x16x32_bf16 v[212:215], v[100:103], v[204:207], v[212:215]\n" \
    "v_permlane16_swap_b32 v248, v249\n" \
    "s_waitcnt lgkmcnt(4)\n" \
    "v_mfma_f32_16x16x32_bf16 v[208:211], v[24:27], v[184:187], v[208:211]\n" \
    "ds_read_b128 v[204:207], v168 offset:11264\n" \
    "v_mfma_f32_16x16x32_bf16 v[212:215], v[104:107], v[184:187], v[212:215]\n" \
    "v_add_f32 v250, v248, v249\n" \
    "s_waitcnt lgkmcnt(4)\n" \
    "v_mfma_f32_16x16x32_bf16 v[208:211], v[28:31], v[188:191], v[208:211]\n" \
    "ds_read_b128 v[184:187], v168 offset:12288\n" \
    "v_mfma_f32_16x16x32_bf16 v[212:215], v[108:111], v[188:191], v[212:215]\n" \
    "v_add_f32 v248, v248, v249\n" \
    "s_waitcnt lgkmcnt(4)\n" \
    "v_mfma_f32_16x16x32_bf16 v[208:211], v[32:35], v[192:195], v[208:211]\n" \
    "ds_read_b128 v[188:191], v168 offset:13312\n" \
    "v_mfma_f32_16x16x32_bf16 v[212:215], v[112:115], v[192:195], v[212:215]\n" \
    "v_permlane32_swap_b32 v248, v250\n" \
    "s_waitcnt lgkmcnt(4)\n" \
    "v_mfma_f32_16x16x32_bf16 v[208:211], v[36:39], v[196:199], v[208:211]\n" \
    "ds_read_b128 v[192:195], v168 offset:14336\n" \
    "v_mfma_f32_16x16x32_bf16 v[212:215], v[116:119], v[196:199], v[212:215]\n" \
    "v_add_f32 v249, v248, v250\n" \
    "s_waitcnt lgkmcnt(4)\n" \
    "v_mfma_f32_16x16x32_bf16 v[208:211], v[40:43], v[200:203], v[208:211]\n" \
    "ds_read_b128 v[196:199], v168 offset:15360\n" \
    "v_mfma_f32_16x16x32_bf16 v[212:215], v[120:123], v[200:203], v[212:215]\n" \
    "v_add_f32 v248, v248, v250\n" \
    "s_waitcnt lgkmcnt(4)\n" \
    "v_mfma_f32_16x16x32_bf16 v[208:211], v[44:47], v[204:207], v[208:211]\n" \
    "ds_read_b128 v[200:203], v168 offset:16384\n" \
    "v_mfma_f32_16x16x32_bf16 v[212:215], v[124:127], v[204:207], v[212:215]\n" \
    "v_permlane16_swap_b32 v248, v249\n" \
    "s_waitcnt lgkmcnt(4)\n" \
    "v_mfma_f32_16x16x32_bf16 v[208:211], v[48:51], v[184:187], v[208:211]\n" \
    "ds_read_b128 v[204:207], v168 offset:17408\n" \
    "v_mfma_f32_16x16x32_bf16 v[212:215], v[128:131], v[184:187], v[212:215]\n" \
    "v_mul_f32 v252, 0x3acccccd, v248\n" \
    "s_waitcnt lgkmcnt(4)\n" \
    "v_mfma_f32_16x16x32_bf16 v[208:211], v[52:55], v[188:191], v[208:211]\n" \
    "ds_read_b128 v[184:187], v168 offset:18432\n" \
    "v_mfma_f32_16x16x32_bf16 v[212:215], v[132:135], v[188:191], v[212:215]\n" \
    "v_mul_f32 v249, 0x3acccccd, v249\n" \
    "s_waitcnt lgkmcnt(4)\n" \
    "v_mfma_f32_16x16x32_bf16 v[208:211], v[56:59], v[192:195], v[208:211]\n" \
    "ds_read_b128 v[188:191], v168 offset:19456\n" \
    "v_mfma_f32_16x16x32_bf16 v[212:215], v[136:139], v[192:195], v[212:215]\n" \
    "v_fma_f32 v249, -v252, v252, v249\n" \
    "s_waitcnt lgkmcnt(4)\n" \
    "v_mfma_f32_16x16x32_bf16 v[208:211], v[60:63], v[196:199], v[208:211]\n" \
    "v_mfma_f32_16x16x32_bf16 v[212:215], v[140:143], v[196:199], v[212:215]\n" \
    "v_max_f32 v249, 0, v249\n" \
    "s_waitcnt lgkmcnt(3)\n" \
    "v_mfma_f32_16x16x32_bf16 v[208:211], v[64:67], v[200:203], v[208:211]\n" \
    "v_mfma_f32_16x16x32_bf16 v[212:215], v[144:147], v[200:203], v[212:215]\n" \
    "v_add_f32 v249, 0x3727c5ac, v249\n" \
    "s_waitcnt lgkmcnt(2)\n" \
    "v_mfma_f32_16x16x32_bf16 v[208:211], v[68:71], v[204:207], v[208:211]\n" \
    "v_mfma_f32_16x16x32_bf16 v[212:215], v[148:151], v[204:207], v[212:215]\n" \
    "v_rsq_f32 v253, v249\n" \
    "s_waitcnt lgkmcnt(1)\n" \
    "v_mfma_f32_16x16x32_bf16 v[208:211], v[72:75], v[184:187], v[208:211]\n" \
    "v_mfma_f32_16x16x32_bf16 v[212:215], v[152:155], v[184:187], v[212:215]\n" \
    "v_mul_f32 v251, 0xbfb8aa3b, v253\n" \
    "s_waitcnt lgkmcnt(0)\n" \
    "v_mfma_f32_16x16x32_bf16 v[208:211], v[76:79], v[188:191], v[208:211]\n" \
    "v_mfma_f32_16x16x32_bf16 v[212:215], v[156:159], v[188:191], v[212:215]\n" \
    "s_nop 6\n" \
    "v_fma_f32 v208, -v252, v160, v208\n" \
    "v_fma_f32 v209, -v252, v161, v209\n" \
    "v_fma_f32 v210, -v252, v162, v210\n" \
    "v_fma_f32 v211, -v252, v163, v211\n" \
    "v_mul_f32 v208, v251, v208\n" \
    "v_mul_f32 v209, v251, v209\n" \
    "v_mul_f32 v210, v251, v210\n" \
    "v_mul_f32 v211, v251, v211\n" \
    "v_exp_f32 v208, v208\n" \
    "v_fma_f32 v212, -v252, v164, v212\n" \
    "v_exp_f32 v209, v209\n" \
    "v_fma_f32 v213, -v252, v165, v213\n" \
    "v_exp_f32 v210, v210\n" \
    "v_fma_f32 v214, -v252, v166, v214\n" \
    "v_exp_f32 v211, v211\n" \
    "v_fma_f32 v215, -v252, v167, v215\n" \
    "v_add_f32 v208, 1.0, v208\n" \
    "v_add_f32 v209, 1.0, v209\n" \
    "v_add_f32 v210, 1.0, v210\n" \
    "v_add_f32 v211, 1.0, v211\n" \
    "v_rcp_f32 v208, v208\n" \
    "v_mul_f32 v212, v253, v212\n" \
    "v_rcp_f32 v209, v209\n" \
    "v_mul_f32 v213, v253, v213\n" \
    "v_rcp_f32 v210, v210\n" \
    "v_mul_f32 v214, v253, v214\n" \
    "v_rcp_f32 v211, v211\n" \
    "v_mul_f32 v215, v253, v215\n" \
    "v_mul_f32 v208, v208, v212\n" \
    "v_mul_f32 v209, v209, v213\n" \
    "v_mul_f32 v210, v210, v214\n" \
    "v_mul_f32 v211, v211, v215\n" \
    "v_cvt_pk_bf16_f32 v240, v208, v209\n" \
    "v_cvt_pk_bf16_f32 v241, v210, v211\n" \
    "v_min_u32 v240, -2, v240\n" \
    "v_min_u32 v241, -2, v241\n" \
    "buffer_store_dwordx2 v[240:241], v173, s[8:11], s39 offen sc1\n" \
    "buffer_store_dwordx2 v[182:183], v173, s[8:11], s40 offen sc1\n" \
    ".Lup_srOc_%=:\n" \
    "v_max3_u32 v254, v228, v229, v230\n" \
    "v_max3_u32 v254, v254, v231, v232\n" \
    "v_max3_u32 v254, v254, v233, v234\n" \
    "v_max3_u32 v254, v254, v235, v236\n" \
    "v_max3_u32 v254, v254, v237, v238\n" \
    "v_max_u32 v254, v254, v239\n" \
    "v_cmp_eq_u32 vcc, -1, v254\n" \
    "s_nop 0\n" \
    "s_cbranch_vccnz .Lup_scOc_%=\n" \
    ".Lup_sxOc_%=:\n" \
    "v_mfma_f32_16x16x32_bf16 v[244:247], v[228:231], v[228:231], 0\n" \
    "ds_write_b128 v169, v[228:231] offset:20480\n" \
    "v_mfma_f32_16x16x32_bf16 v[244:247], v[232:235], v[232:235], v[244:247]\n" \
    "ds_write_b128 v169, v[232:235] offset:28672\n" \
    "v_mfma_f32_16x16x32_bf16 v[244:247], v[236:239], v[236:239], v[244:247]\n" \
    "ds_write_b128 v169, v[236:239] offset:36864\n" \
    "v_mfma_f32_16x16x32_bf16 v[240:243], v[178:181], v[228:231], 0\n" \
    "v_mfma_f32_16x16x32_bf16 v[240:243], v[178:181], v[232:235], v[240:243]\n" \
    "v_mfma_f32_16x16x32_bf16 v[240:243], v[178:181], v[236:239], v[240:243]\n" \
    "s_nop 3\n" \
    "v_cndmask_b32 v254, v244, v245, s[44:45]\n" \
    "v_cndmask_b32 v255, v246, v247, s[44:45]\n" \
    "s_nop 1\n" \
    "v_cndmask_b32 v241, v254, v255, s[46:47]\n" \
    "ds_write_b64 v171, v[240:241] offset:1024\n" \
    "s_waitcnt lgkmcnt(0)\n" \
    "s_barrier\n" \
    "s_add_u32 s36, s33, 1\n" \
    "ds_read_b128 v[184:187], v168 offset:20480\n" \
    "ds_read_b128 v[188:191], v168 offset:21504\n" \
    "ds_read_b128 v[192:195], v168 offset:22528\n" \
    "ds_read_b128 v[196:199], v168 offset:23552\n" \
    "ds_read_b128 v[200:203], v168 offset:24576\n" \
    "ds_read2_b64 v[248:251], v172 offset0:128 offset1:144\n" \
    "s_waitcnt lgkmcnt(5)\n" \
    "v_mfma_f32_16x16x32_bf16 v[208:211], v[0:3], v[184:187], 0\n" \
    "ds_read_b128 v[204:207], v168 offset:25600\n" \
    "v_mfma_f32_16x16x32_bf16 v[212:215], v[80:83], v[184:187], 0\n" \
    "s_mul_i32 s41, s36, 40960\n" \
    "s_add_u32 s41, s41, s17\n" \
    "s_add_u32 s41, s41, 2621440\n" \
    "buffer_load_dwordx4 v[228:231], v168, s[8:11], s41 offen sc1\n" \
    "s_add_u32 s41, s41, 8192\n" \
    "buffer_load_dwordx4 v[232:235], v168, s[8:11], s41 offen sc1\n" \
    "s_add_u32 s41, s41, 8192\n" \
    "buffer_load_dwordx4 v[236:239], v168, s[8:11], s41 offen sc1\n" \
    "s_waitcnt lgkmcnt(5)\n" \
    "v_mfma_f32_16x16x32_bf16 v[208:211], v[4:7], v[188:191], v[208:211]\n" \
    "ds_read_b128 v[184:187], v168 offset:26624\n" \
    "v_mfma_f32_16x16x32_bf16 v[212:215], v[84:87], v[188:191], v[212:215]\n" \
    "s_mul_i32 s39, s33, 98304\n" \
    "s_add_u32 s39, s39, 6291456\n" \
    "s_waitcnt lgkmcnt(5)\n" \
    "v_mfma_f32_16x16x32_bf16 v[208:211], v[8:11], v[192:195], v[208:211]\n" \
    "ds_read_b128 v[188:191], v168 offset:27648\n" \
    "v_mfma_f32_16x16x32_bf16 v[212:215], v[88:91], v[192:195], v[212:215]\n" \
    "s_add_u32 s40, s39, %[pdist]*98304\n" \
    "s_cmp_lt_u32 s33, 64-%[pdist]\n" \
    "s_cselect_b32 s40, s40, 0x80000000\n" \
    "s_waitcnt lgkmcnt(5)\n" \
    "v_mfma_f32_16x16x32_bf16 v[208:211], v[12:15], v[196:199], v[208:211]\n" \
    "ds_read_b128 v[192:195], v168 offset:28672\n" \
    "v_mfma_f32_16x16x32_bf16 v[212:215], v[92:95], v[196:199], v[212:215]\n" \
    "s_waitcnt lgkmcnt(4)\n" \
    "v_add_f32 v248, v248, v250\n" \
    "v_mfma_f32_16x16x32_bf16 v[208:211], v[16:19], v[200:203], v[208:211]\n" \
    "ds_read_b128 v[196:199], v168 offset:29696\n" \
    "v_mfma_f32_16x16x32_bf16 v[212:215], v[96:99], v[200:203], v[212:215]\n" \
    "v_add_f32 v249, v249, v251\n" \
    "s_waitcnt lgkmcnt(4)\n" \
    "v_mfma_f32_16x16x32_bf16 v[208:211], v[20:23], v[204:207], v[208:211]\n" \
    "ds_read_b128 v[200:203], v168 offset:30720\n" \
    "v_mfma_f32_16x16x32_bf16 v[212:215], v[100:103], v[204:207], v[212:215]\n" \
    "v_permlane16_swap_b32 v248, v249\n" \
    "s_waitcnt lgkmcnt(4)\n" \
    "v_mfma_f32_16x16x32_bf16 v[208:211], v[24:27], v[184:187], v[208:211]\n" \
    "ds_read_b128 v[204:207], v168 offset:31744\n" \
    "v_mfma_f32_16x16x32_bf16 v[212:215], v[104:107], v[184:187], v[212:215]\n" \
    "v_add_f32 v250, v248, v249\n" \
    "s_waitcnt lgkmcnt(4)\n" \
    "v_mfma_f32_16x16x32_bf16 v[208:211], v[28:31], v[188:191], v[208:211]\n" \
    "ds_read_b128 v[184:187], v168 offset:32768\n" \
    "v_mfma_f32_16x16x32_bf16 v[212:215], v[108:111], v[188:191], v[212:215]\n" \
    "v_add_f32 v248, v248, v249\n" \
    "s_waitcnt lgkmcnt(4)\n" \
    "v_mfma_f32_16x16x32_bf16 v[208:211], v[32:35], v[192:195], v[208:211]\n" \
    "ds_read_b128 v[188:191], v168 offset:33792\n" \
    "v_mfma_f32_16x16x32_bf16 v[212:215], v[112:115], v[192:195], v[212:215]\n" \
    "v_permlane32_swap_b32 v248, v250\n" \
    "s_waitcnt lgkmcnt(4)\n" \
    "v_mfma_f32_16x16x32_bf16 v[208:211], v[36:39], v[196:199], v[208:211]\n" \
    "ds_read_b128 v[192:195], v168 offset:34816\n" \
    "v_mfma_f32_16x16x32_bf16 v[212:215], v[116:119], v[196:199], v[212:215]\n" \
    "v_add_f32 v249, v248, v250\n" \
    "s_waitcnt lgkmcnt(4)\n" \
    "v_mfma_f32_16x16x32_bf16 v[208:211], v[40:43], v[200:203], v[208:211]\n" \
    "ds_read_b128 v[196:199], v168 offset:35840\n" \
    "v_mfma_f32_16x16x32_bf16 v[212:215], v[120:123], v[200:203], v[212:215]\n" \
    "v_add_f32 v248, v248, v250\n" \
    "s_add_u32 s48, s32, 3\n" \
    "s_min_u32 s48, s48, 64\n" \
    "s_and_b32 s49, s48, 1\n" \
    "s_xor_b32 s49, s49, 1\n" \
    "s_lshl_b32 s49, s49, 9\n" \
    "s_add_u32 s49, s49, s18\n" \
    "buffer_load_dwordx2 v[176:177], off, s[8:11], s49 sc1\n" \
    "s_waitcnt lgkmcnt(4)\n" \
    "v_mfma_f32_16x16x32_bf16 v[208:211], v[44:47], v[204:207], v[208:211]\n" \
    "ds_read_b128 v[200:203], v168 offset:36864\n" \
    "v_mfma_f32_16x16x32_bf16 v[212:215], v[124:127], v[204:207], v[212:215]\n" \
    "v_permlane16_swap_b32 v248, v249\n" \
    "s_waitcnt lgkmcnt(4)\n" \
    "v_mfma_f32_16x16x32_bf16 v[208:211], v[48:51], v[184:187], v[208:211]\n" \
    "ds_read_b128 v[204:207], v168 offset:37888\n" \
    "v_mfma_f32_16x16x32_bf16 v[212:215], v[128:131], v[184:187], v[212:215]\n" \
    "v_mul_f32 v252, 0x3acccccd, v248\n" \
    "s_waitcnt lgkmcnt(4)\n" \
    "v_mfma_f32_16x16x32_bf16 v[208:211], v[52:55], v[188:191], v[208:211]\n" \
    "ds_read_b128 v[184:187], v168 offset:38912\n" \
    "v_mfma_f32_16x16x32_bf16 v[212:215], v[132:135], v[188:191], v[212:215]\n" \
    "v_mul_f32 v249, 0x3acccccd, v249\n" \
    "s_waitcnt lgkmcnt(4)\n" \
    "v_mfma_f32_16x16x32_bf16 v[208:211], v[56:59], v[192:195], v[208:211]\n" \
    "ds_read_b128 v[188:191], v168 offset:39936\n" \
    "v_mfma_f32_16x16x32_bf16 v[212:215], v[136:139], v[192:195], v[212:215]\n" \
    "v_fma_f32 v249, -v252, v252, v249\n" \
    "s_waitcnt lgkmcnt(4)\n" \
    "v_mfma_f32_16x16x32_bf16 v[208:211], v[60:63], v[196:199], v[208:211]\n" \
    "v_mfma_f32_16x16x32_bf16 v[212:215], v[140:143], v[196:199], v[212:215]\n" \
    "v_max_f32 v249, 0, v249\n" \
    "s_waitcnt lgkmcnt(3)\n" \
    "v_mfma_f32_16x16x32_bf16 v[208:211], v[64:67], v[200:203], v[208:211]\n" \
    "v_mfma_f32_16x16x32_bf16 v[212:215], v[144:147], v[200:203], v[212:215]\n" \
    "v_add_f32 v249, 0x3727c5ac, v249\n" \
    "s_waitcnt lgkmcnt(2)\n" \
    "v_mfma_f32_16x16x32_bf16 v[208:211], v[68:71], v[204:207], v[208:211]\n" \
    "v_mfma_f32_16x16x32_bf16 v[212:215], v[148:151], v[204:207], v[212:215]\n" \
    "v_rsq_f32 v253, v249\n" \
    "s_waitcnt lgkmcnt(1)\n" \
    "v_mfma_f32_16x16x32_bf16 v[208:211], v[72:75], v[184:187], v[208:211]\n" \
    "v_mfma_f32_16x16x32_bf16 v[212:215], v[152:155], v[184:187], v[212:215]\n" \
    "v_mul_f32 v251, 0xbfb8aa3b, v253\n" \
    "s_waitcnt lgkmcnt(0)\n" \
    "v_mfma_f32_16x16x32_bf16 v[208:211], v[76:79], v[188:191], v[208:211]\n" \
    "v_mfma_f32_16x16x32_bf16 v[212:215], v[156:159], v[188:191], v[212:215]\n" \
    "s_nop 6\n" \
    "v_fma_f32 v208, -v252, v160, v208\n" \
    "v_fma_f32 v209, -v252, v161, v209\n" \
    "v_fma_f32 v210, -v252, v162, v210\n" \
    "v_fma_f32 v211, -v252, v163, v211\n" \
    "v_mul_f32 v208, v251, v208\n" \
    "v_mul_f32 v209, v251, v209\n" \
    "v_mul_f32 v210, v251, v210\n" \
    "v_mul_f32 v211, v251, v211\n" \
    "v_exp_f32 v208, v208\n" \
    "v_fma_f32 v212, -v252, v164, v212\n" \
    "v_exp_f32 v209, v209\n" \
    "v_fma_f32 v213, -v252, v165, v213\n" \
    "v_exp_f32 v210, v210\n" \
    "v_fma_f32 v214, -v252, v166, v214\n" \
    "v_exp_f32 v211, v211\n" \
    "v_fma_f32 v215, -v252, v167, v215\n" \
    "v_add_f32 v208, 1.0, v208\n" \
    "v_add_f32 v209, 1.0, v209\n" \
    "v_add_f32 v210, 1.0, v210\n" \
    "v_add_f32 v211, 1.0, v211\n" \
    "v_rcp_f32 v208, v208\n" \
    "v_mul_f32 v212, v253, v212\n" \
    "v_rcp_f32 v209, v209\n" \
    "v_mul_f32 v213, v253, v213\n" \
    "v_rcp_f32 v210, v210\n" \
    "v_mul_f32 v214, v253, v214\n" \
    "v_rcp_f32 v211, v211\n" \
    "v_mul_f32 v215, v253, v215\n" \
    "v_mul_f32 v208, v208, v212\n" \
    "v_mul_f32 v209, v209, v213\n" \
    "v_mul_f32 v210, v210, v214\n" \
    "v_mul_f32 v211, v211, v215\n" \
    "v_cvt_pk_bf16_f32 v240, v208, v209\n" \
    "v_cvt_pk_bf16_f32 v241, v210, v211\n" \
    "v_min_u32 v240, -2, v240\n" \
    "v_min_u32 v241, -2, v241\n" \
    "buffer_store_dwordx2 v[240:241], v173, s[8:11], s39 offen sc1\n" \
    "buffer_store_dwordx2 v[182:183], v173, s[8:11], s40 offen sc1\n" \
    "s_add_u32 s33, s33, 1\n" \
    "s_add_u32 s32, s32, 1\n" \
    "s_cmp_lt_u32 s32, 64\n" \
    "s_cbranch_scc1 .Lup_mO_%=\n" \
    "s_add_u32 s18, s18, 1024\n" \
    "s_waitcnt vmcnt(0)\n" \
    "s_add_u32 s48, s33, 2\n" \
    "s_min_u32 s48, s48, 64\n" \
    "s_and_b32 s49, s48, 1\n" \
    "s_xor_b32 s49, s49, 1\n" \
    "s_lshl_b32 s49, s49, 9\n" \
    "s_add_u32 s49, s49, s18\n" \
    "buffer_load_dwordx2 v[176:177], off, s[8:11], s49 sc1\n" \
    "s_waitcnt vmcnt(0)\n" \
    ".Lup_nO_%=:\n" \
    "s_barrier\n" \
    "s_mov_b32 s37, 0\n" \
    "s_waitcnt vmcnt(2)\n" \
    ".Lup_Odp_%=:\n" \
    "v_readfirstlane_b32 s36, v177\n" \
    "s_nop 0\n" \
    "s_cmp_gt_i32 s36, s48\n" \
    "s_cbranch_scc1 .Lup_Odx_%=\n" \
    "s_cmp_eq_u32 s36, s48\n" \
    "s_cbranch_scc1 .Lup_Odw_%=\n" \
    "s_sleep 3\n" \
    "buffer_load_dwordx2 v[176:177], off, s[8:11], s49 sc1\n" \
    "s_waitcnt vmcnt(0)\n" \
    "s_add_u32 s37, s37, 1\n" \
    "s_cmp_lt_u32 s37, s38\n" \
    "s_cbranch_scc1 .Lup_Odp_%=\n" \
    "s_mov_b32 s38, 0\n" \
    "s_branch .Lup_Odx_%=\n" \
    ".Lup_Odw_%=:\n" \
    "v_readfirstlane_b32 s36, v176\n" \
    "s_cmp_eq_u32 s33, 63\n" \
    "s_cselect_b32 s34, %[dlast], 0\n" \
    "s_add_u32 s36, s36, s34\n" \
    "s_add_u32 s36, s36, %[dx]\n" \
    "s_branch .Lup_Odtb_%=\n" \
    ".Lup_Odta_%=:\n" \
    "s_sleep 1\n" \
    ".Lup_Odtb_%=:\n" \
    "s_memrealtime vcc\n" \
    "s_waitcnt lgkmcnt(0)\n" \
    "s_sub_i32 vcc_lo, s36, vcc_lo\n" \
    "s_cmp_gt_i32 vcc_lo, 8\n" \
    "s_cbranch_scc1 .Lup_Odta_%=\n" \
    "s_cmp_gt_i32 vcc_lo, 0\n" \
    "s_cbranch_scc1 .Lup_Odtb_%=\n" \
    ".Lup_Odx_%=:\n" \
    "s_mul_i32 s41, s33, 40960\n" \
    "s_add_u32 s41, s41, s17\n" \
    "s_add_u32 s41, s41, 2621440\n" \
    "buffer_load_dwordx4 v[228:231], v168, s[8:11], s41 offen sc1\n" \
    "s_add_u32 s41, s41, 8192\n" \
    "buffer_load_dwordx4 v[232:235], v168, s[8:11], s41 offen sc1\n" \
    "s_add_u32 s41, s41, 8192\n" \
    "buffer_load_dwordx4 v[236:239], v168, s[8:11], s41 offen sc1\n" \
    "s_mov_b32 s37, 0\n" \
    ".Lup_srOd_%=:\n" \
    "s_waitcnt vmcnt(2)\n" \
    "v_max3_u32 v254, v228, v229, v230\n" \
    "s_waitcnt vmcnt(1)\n" \
    "v_max3_u32 v254, v254, v231, v232\n" \
    "v_max3_u32 v254, v254, v233, v234\n" \
    "s_waitcnt vmcnt(0)\n" \
    "v_max3_u32 v254, v254, v235, v236\n" \
    "v_max3_u32 v254, v254, v237, v238\n" \
    "v_max_u32 v254, v254, v239\n" \
    "v_cmp_eq_u32 vcc, -1, v254\n" \
    "s_nop 0\n" \
    "s_cbranch_vccnz .Lup_scOd_%=\n" \
    ".Lup_sxOd_%=:\n" \
    "v_mfma_f32_16x16x32_bf16 v[244:247], v[228:231], v[228:231], 0\n" \
    "ds_write_b128 v169, v[228:231] offset:20480\n" \
    "v_mfma_f32_16x16x32_bf16 v[244:247], v[232:235], v[232:235], v[244:247]\n" \
    "ds_write_b128 v169, v[232:235] offset:28672\n" \
    "v_mfma_f32_16x16x32_bf16 v[244:247], v[236:239], v[236:239], v[244:247]\n" \
    "ds_write_b128 v169, v[236:239] offset:36864\n" \
    "v_mfma_f32_16x16x32_bf16 v[240:243], v[178:181], v[228:231], 0\n" \
    "v_mfma_f32_16x16x32_bf16 v[240:243], v[178:181], v[232:235], v[240:243]\n" \
    "v_mfma_f32_16x16x32_bf16 v[240:243], v[178:181], v[236:239], v[240:243]\n" \
    "s_nop 3\n" \
    "v_cndmask_b32 v254, v244, v245, s[44:45]\n" \
    "v_cndmask_b32 v255, v246, v247, s[44:45]\n" \
    "s_nop 1\n" \
    "v_cndmask_b32 v241, v254, v255, s[46:47]\n" \
    "ds_write_b64 v171, v[240:241] offset:1024\n" \
    "s_waitcnt lgkmcnt(0)\n" \
    "s_barrier\n" \
    "ds_read_b128 v[184:187], v168 offset:20480\n" \
    "ds_read_b128 v[188:191], v168 offset:21504\n" \
    "ds_read_b128 v[192:195], v168 offset:22528\n" \
    "ds_read_b128 v[196:199], v168 offset:23552\n" \
    "ds_read_b128 v[200:203], v168 offset:24576\n" \
    "ds_read2_b64 v[248:251], v172 offset0:128 offset1:144\n" \
    "s_waitcnt lgkmcnt(5)\n" \
    "v_mfma_f32_16x16x32_bf16 v[208:211], v[0:3], v[184:187], 0\n" \
    "ds_read_b128 v[204:207], v168 offset:25600\n" \
    "v_mfma_f32_16x16x32_bf16 v[212:215], v[80:83], v[184:187], 0\n" \
    "s_waitcnt lgkmcnt(5)\n" \
    "v_mfma_f32_16x16x32_bf16 v[208:211], v[4:7], v[188:191], v[208:211]\n" \
    "ds_read_b128 v[184:187], v168 offset:26624\n" \
    "v_mfma_f32_16x16x32_bf16 v[212:215], v[84:87], v[188:191], v[212:215]\n" \
    "s_mul_i32 s39, s33, 98304\n" \
    "s_add_u32 s39, s39, 6291456\n" \
    "s_waitcnt lgkmcnt(5)\n" \
    "v_mfma_f32_16x16x32_bf16 v[208:211], v[8:11], v[192:195], v[208:211]\n" \
    "ds_read_b128 v[188:191], v168 offset:27648\n" \
    "v_mfma_f32_16x16x32_bf16 v[212:215], v[88:91], v[192:195], v[212:215]\n" \
    "s_add_u32 s40, s39, %[pdist]*98304\n" \
    "s_cmp_lt_u32 s33, 64-%[pdist]\n" \
    "s_cselect_b32 s40, s40, 0x80000000\n" \
    "s_waitcnt lgkmcnt(5)\n" \
    "v_mfma_f32_16x16x32_bf16 v[208:211], v[12:15], v[196:199], v[208:211]\n" \
    "ds_read_b128 v[192:195], v168 offset:28672\n" \
    "v_mfma_f32_16x16x32_bf16 v[212:215], v[92:95], v[196:199], v[212:215]\n" \
    "s_waitcnt lgkmcnt(4)\n" \
    "v_add_f32 v248, v248, v250\n" \
    "v_mfma_f32_16x16x32_bf16 v[208:211], v[16:19], v[200:203], v[208:211]\n" \
    "ds_read_b128 v[196:199], v168 offset:29696\n" \
    "v_mfma_f32_16x16x32_bf16 v[212:215], v[96:99], v[200:203], v[212:215]\n" \
    "v_add_f32 v249, v249, v251\n" \
    "s_waitcnt lgkmcnt(4)\n" \
    "v_mfma_f32_16x16x32_bf16 v[208:211], v[20:23], v[204:207], v[208:211]\n" \
    "ds_read_b128 v[200:203], v168 offset:30720\n" \
    "v_mfma_f32_16x16x32_bf16 v[212:215], v[100:103], v[204:207], v[212:215]\n" \
    "v_permlane16_swap_b32 v248, v249\n" \
    "s_waitcnt lgkmcnt(4)\n" \
    "v_mfma_f32_16x16x32_bf16 v[208:211], v[24:27], v[184:187], v[208:211]\n" \
    "ds_read_b128 v[204:207], v168 offset:31744\n" \
    "v_mfma_f32_16x16x32_bf16 v[212:215], v[104:107], v[184:187], v[212:215]\n" \
    "v_add_f32 v250, v248, v249\n" \
    "s_waitcnt lgkmcnt(4)\n" \
    "v_mfma_f32_16x16x32_bf16 v[208:211], v[28:31], v[188:191], v[208:211]\n" \
    "ds_read_b128 v[184:187], v168 offset:32768\n" \
    "v_mfma_f32_16x16x32_bf16 v[212:215], v[108:111], v[188:191], v[212:215]\n" \
    "v_add_f32 v248, v248, v249\n" \
    "s_waitcnt lgkmcnt(4)\n" \
    "v_mfma_f32_16x16x32_bf16 v[208:211], v[32:35], v[192:195], v[208:211]\n" \
    "ds_read_b128 v[188:191], v168 offset:33792\n" \
    "v_mfma_f32_16x16x32_bf16 v[212:215], v[112:115], v[192:195], v[212:215]\n" \
    "v_permlane32_swap_b32 v248, v250\n" \
    "s_waitcnt lgkmcnt(4)\n" \
    "v_mfma_f32_16x16x32_bf16 v[208:211], v[36:39], v[196:199], v[208:211]\n" \
    "ds_read_b128 v[192:195], v168 offset:34816\n" \
    "v_mfma_f32_16x16x32_bf16 v[212:215], v[116:119], v[196:199], v[212:215]\n" \
    "v_add_f32 v249, v248, v250\n" \
    "s_waitcnt lgkmcnt(4)\n" \
    "v_mfma_f32_16x16x32_bf16 v[208:211], v[40:43], v[200:203], v[208:211]\n" \
    "ds_read_b128 v[196:199], v168 offset:35840\n" \
    "v_mfma_f32_16x16x32_bf16 v[212:215], v[120:123], v[200:203], v[212:215]\n" \
    "v_add_f32 v248, v248, v250\n" \
    "s_add_u32 s48, s33, 3\n" \
    "s_min_u32 s48, s48, 64\n" \
    "s_and_b32 s49, s48, 1\n" \
    "s_xor_b32 s49, s49, 1\n" \
    "s_lshl_b32 s49, s49, 9\n" \
    "s_add_u32 s49, s49, s18\n" \
    "buffer_load_dwordx2 v[176:177], off, s[8:11], s49 sc1\n" \
    "s_waitcnt lgkmcnt(4)\n" \
    "v_mfma_f32_16x16x32_bf16 v[208:211], v[44:47], v[204:207], v[208:211]\n" \
    "ds_read_b128 v[200:203], v168 offset:36864\n" \
    "v_mfma_f32_16x16x32_bf16 v[212:215], v[124:127], v[204:207], v[212:215]\n" \
    "v_permlane16_swap_b32 v248, v249\n" \
    "s_waitcnt lgkmcnt(4)\n" \
    "v_mfma_f32_16x16x32_bf16 v[208:211], v[48:51], v[184:187], v[208:211]\n" \
    "ds_read_b128 v[204:207], v168 offset:37888\n" \
    "v_mfma_f32_16x16x32_bf16 v[212:215], v[128:131], v[184:187], v[212:215]\n" \
    "v_mul_f32 v252, 0x3acccccd, v248\n" \
    "s_waitcnt lgkmcnt(4)\n" \
    "v_mfma_f32_16x16x32_bf16 v[208:211], v[52:55], v[188:191], v[208:211]\n" \
    "ds_read_b128 v[184:187], v168 offset:38912\n" \
    "v_mfma_f32_16x16x32_bf16 v[212:215], v[132:135], v[188:191], v[212:215]\n" \
    "v_mul_f32 v249, 0x3acccccd, v249\n" \
    "s_waitcnt lgkmcnt(4)\n" \
    "v_mfma_f32_16x16x32_bf16 v[208:211], v[56:59], v[192:195], v[208:211]\n" \
    "ds_read_b128 v[188:191], v168 offset:39936\n" \
    "v_mfma_f32_16x16x32_bf16 v[212:215], v[136:139], v[192:195], v[212:215]\n" \
    "v_fma_f32 v249, -v252, v252, v249\n" \
    "s_waitcnt lgkmcnt(4)\n" \
    "v_mfma_f32_16x16x32_bf16 v[208:211], v[60:63], v[196:199], v[208:211]\n" \
    "v_mfma_f32_16x16x32_bf16 v[212:215], v[140:143], v[196:199], v[212:215]\n" \
    "v_max_f32 v249, 0, v249\n" \
    "s_waitcnt lgkmcnt(3)\n" \
    "v_mfma_f32_16x16x32_bf16 v[208:211], v[64:67], v[200:203], v[208:211]\n" \
    "v_mfma_f32_16x16x32_bf16 v[212:215], v[144:147], v[200:203], v[212:215]\n" \
    "v_add_f32 v249, 0x3727c5ac, v249\n" \
    "s_waitcnt lgkmcnt(2)\n" \
    "v_mfma_f32_16x16x32_bf16 v[208:211], v[68:71], v[204:207], v[208:211]\n" \
    "v_mfma_f32_16x16x32_bf16 v[212:215], v[148:151], v[204:207], v[212:215]\n" \
    "v_rsq_f32 v253, v249\n" \
    "s_waitcnt lgkmcnt(1)\n" \
    "v_mfma_f32_16x16x32_bf16 v[208:211], v[72:75], v[184:187], v[208:211]\n" \
    "v_mfma_f32_16x16x32_bf16 v[212:215], v[152:155], v[184:187], v[212:215]\n" \
    "v_mul_f32 v251, 0xbfb8aa3b, v253\n" \
    "s_waitcnt lgkmcnt(0)\n" \
    "v_mfma_f32_16x16x32_bf16 v[208:211], v[76:79], v[188:191], v[208:211]\n" \
    "v_mfma_f32_16x16x32_bf16 v[212:215], v[156:159], v[188:191], v[212:215]\n" \
    "s_nop 6\n" \
    "v_fma_f32 v208, -v252, v160, v208\n" \
    "v_fma_f32 v209, -v252, v161, v209\n" \
    "v_fma_f32 v210, -v252, v162, v210\n" \
    "v_fma_f32 v211, -v252, v163, v211\n" \
    "v_mul_f32 v208, v251, v208\n" \
    "v_mul_f32 v209, v251, v209\n" \
    "v_mul_f32 v210, v251, v210\n" \
    "v_mul_f32 v211, v251, v211\n" \
    "v_exp_f32 v208, v208\n" \
    "v_fma_f32 v212, -v252, v164, v212\n" \
    "v_exp_f32 v209, v209\n" \
    "v_fma_f32 v213, -v252, v165, v213\n" \
    "v_exp_f32 v210, v210\n" \
    "v_fma_f32 v214, -v252, v166, v214\n" \
    "v_exp_f32 v211, v211\n" \
    "v_fma_f32 v215, -v252, v167, v215\n" \
    "v_add_f32 v208, 1.0, v208\n" \
    "v_add_f32 v209, 1.0, v209\n" \
    "v_add_f32 v210, 1.0, v210\n" \
    "v_add_f32 v211, 1.0, v211\n" \
    "v_rcp_f32 v208, v208\n" \
    "v_mul_f32 v212, v253, v212\n" \
    "v_rcp_f32 v209, v209\n" \
    "v_mul_f32 v213, v253, v213\n" \
    "v_rcp_f32 v210, v210\n" \
    "v_mul_f32 v214, v253, v214\n" \
    "v_rcp_f32 v211, v211\n" \
    "v_mul_f32 v215, v253, v215\n" \
    "v_mul_f32 v208, v208, v212\n" \
    "v_mul_f32 v209, v209, v213\n" \
    "v_mul_f32 v210, v210, v214\n" \
    "v_mul_f32 v211, v211, v215\n" \
    "v_cvt_pk_bf16_f32 v240, v208, v209\n" \
    "v_cvt_pk_bf16_f32 v241, v210, v211\n" \
    "v_min_u32 v240, -2, v240\n" \
    "v_min_u32 v241, -2, v241\n" \
    "buffer_store_dwordx2 v[240:241], v173, s[8:11], s39 offen sc1\n" \
    "buffer_store_dwordx2 v[182:183], v173, s[8:11], s40 offen sc1\n" \
    "s_add_u32 s33, s33, 1\n" \
    "s_cmp_lt_u32 s33, 64\n" \
    "s_cbranch_scc1 .Lup_nO_%=\n" \
    "s_waitcnt vmcnt(0)\n" \
    "s_endpgm\n" \
    ".Lup_scYa_%=:\n" \
    "s_add_u32 s37, s37, 1\n" \
    "s_cmp_lt_u32 s37, s38\n" \
    "s_cselect_b32 s38, s38, 0\n" \
    "s_cbranch_scc0 .Lup_sxYa_%=\n" \
    "s_sleep 3\n" \
    "s_mul_i32 s41, s32, 40960\n" \
    "s_add_u32 s41, s41, s17\n" \
    "buffer_load_dwordx4 v[216:219], v168, s[8:11], s41 offen sc1\n" \
    "s_add_u32 s41, s41, 8192\n" \
    "buffer_load_dwordx4 v[220:223], v168, s[8:11], s41 offen sc1\n" \
    "s_add_u32 s41, s41, 8192\n" \
    "buffer_load_dwordx4 v[224:227], v168, s[8:11], s41 offen sc1\n" \
    "s_waitcnt vmcnt(0)\n" \
    "s_branch .Lup_srYa_%=\n" \
    ".Lup_scYb_%=:\n" \
    "s_add_u32 s37, s37, 1\n" \
    "s_cmp_lt_u32 s37, s38\n" \
    "s_cselect_b32 s38, s38, 0\n" \
    "s_cbranch_scc0 .Lup_sxYb_%=\n" \
    "s_sleep 3\n" \
    "s_mul_i32 s41, s32, 40960\n" \
    "s_add_u32 s41, s41, s17\n" \
    "buffer_load_dwordx4 v[216:219], v168, s[8:11], s41 offen sc1\n" \
    "s_add_u32 s41, s41, 8192\n" \
    "buffer_load_dwordx4 v[220:223], v168, s[8:11], s41 offen sc1\n" \
    "s_add_u32 s41, s41, 8192\n" \
    "buffer_load_dwordx4 v[224:227], v168, s[8:11], s41 offen sc1\n" \
    "s_waitcnt vmcnt(0)\n" \
    "s_branch .Lup_srYb_%=\n" \
    ".Lup_scYc_%=:\n" \
    "s_add_u32 s37, s37, 1\n" \
    "s_cmp_lt_u32 s37, s38\n" \
    "s_cselect_b32 s38, s38, 0\n" \
    "s_cbranch_scc0 .Lup_sxYc_%=\n" \
    "s_sleep 3\n" \
    "s_mul_i32 s41, s33, 40960\n" \
    "s_add_u32 s41, s41, s17\n" \
    "s_add_u32 s41, s41, 2621440\n" \
    "buffer_load_dwordx4 v[228:231], v168, s[8:11], s41 offen sc1\n" \
    "s_add_u32 s41, s41, 8192\n" \
    "buffer_load_dwordx4 v[232:235], v168, s[8:11], s41 offen sc1\n" \
    "s_add_u32 s41, s41, 8192\n" \
    "buffer_load_dwordx4 v[236:239], v168, s[8:11], s41 offen sc1\n" \
    "s_waitcnt vmcnt(0)\n" \
    "s_branch .Lup_srYc_%=\n" \
    ".Lup_scYd_%=:\n" \
    "s_add_u32 s37, s37, 1\n" \
    "s_cmp_lt_u32 s37, s38\n" \
    "s_cselect_b32 s38, s38, 0\n" \
    "s_cbranch_scc0 .Lup_sxYd_%=\n" \
    "s_sleep 3\n" \
    "s_mul_i32 s41, s33, 40960\n" \
    "s_add_u32 s41, s41, s17\n" \
    "s_add_u32 s41, s41, 2621440\n" \
    "buffer_load_dwordx4 v[228:231], v168, s[8:11], s41 offen sc1\n" \
    "s_add_u32 s41, s41, 8192\n" \
    "buffer_load_dwordx4 v[232:235], v168, s[8:11], s41 offen sc1\n" \
    "s_add_u32 s41, s41, 8192\n" \
    "buffer_load_dwordx4 v[236:239], v168, s[8:11], s41 offen sc1\n" \
    "s_waitcnt vmcnt(0)\n" \
    "s_branch .Lup_srYd_%=\n" \
    ".Lup_scOa_%=:\n" \
    "s_add_u32 s37, s37, 1\n" \
    "s_cmp_lt_u32 s37, s38\n" \
    "s_cselect_b32 s38, s38, 0\n" \
    "s_cbranch_scc0 .Lup_sxOa_%=\n" \
    "s_sleep 3\n" \
    "s_mul_i32 s41, s32, 40960\n" \
    "s_add_u32 s41, s41, s17\n" \
    "buffer_load_dwordx4 v[216:219], v168, s[8:11], s41 offen sc1\n" \
    "s_add_u32 s41, s41, 8192\n" \
    "buffer_load_dwordx4 v[220:223], v168, s[8:11], s41 offen sc1\n" \
    "s_add_u32 s41, s41, 8192\n" \
    "buffer_load_dwordx4 v[224:227], v168, s[8:11], s41 offen sc1\n" \
    "s_waitcnt vmcnt(0)\n" \
    "s_branch .Lup_srOa_%=\n" \
    ".Lup_scOb_%=:\n" \
    "s_add_u32 s37, s37, 1\n" \
    "s_cmp_lt_u32 s37, s38\n" \
    "s_cselect_b32 s38, s38, 0\n" \
    "s_cbranch_scc0 .Lup_sxOb_%=\n" \
    "s_sleep 3\n" \
    "s_mul_i32 s41, s32, 40960\n" \
    "s_add_u32 s41, s41, s17\n" \
    "buffer_load_dwordx4 v[216:219], v168, s[8:11], s41 offen sc1\n" \
    "s_add_u32 s41, s41, 8192\n" \
    "buffer_load_dwordx4 v[220:223], v168, s[8:11], s41 offen sc1\n" \
    "s_add_u32 s41, s41, 8192\n" \
    "buffer_load_dwordx4 v[224:227], v168, s[8:11], s41 offen sc1\n" \
    "s_waitcnt vmcnt(0)\n" \
    "s_branch .Lup_srOb_%=\n" \
    ".Lup_scOc_%=:\n" \
    "s_add_u32 s37, s37, 1\n" \
    "s_cmp_lt_u32 s37, s38\n" \
    "s_cselect_b32 s38, s38, 0\n" \
    "s_cbranch_scc0 .Lup_sxOc_%=\n" \
    "s_sleep 3\n" \
    "s_mul_i32 s41, s33, 40960\n" \
    "s_add_u32 s41, s41, s17\n" \
    "s_add_u32 s41, s41, 2621440\n" \
    "buffer_load_dwordx4 v[228:231], v168, s[8:11], s41 offen sc1\n" \
    "s_add_u32 s41, s41, 8192\n" \
    "buffer_load_dwordx4 v[232:235], v168, s[8:11], s41 offen sc1\n" \
    "s_add_u32 s41, s41, 8192\n" \
    "buffer_load_dwordx4 v[236:239], v168, s[8:11], s41 offen sc1\n" \
    "s_waitcnt vmcnt(0)\n" \
    "s_branch .Lup_srOc_%=\n" \
    ".Lup_scOd_%=:\n" \
    "s_add_u32 s37, s37, 1\n" \
    "s_cmp_lt_u32 s37, s38\n" \
    "s_cselect_b32 s38, s38, 0\n" \
    "s_cbranch_scc0 .Lup_sxOd_%=\n" \
    "s_sleep 3\n" \
    "s_mul_i32 s41, s33, 40960\n" \
    "s_add_u32 s41, s41, s17\n" \
    "s_add_u32 s41, s41, 2621440\n" \
    "buffer_load_dwordx4 v[228:231], v168, s[8:11], s41 offen sc1\n" \
    "s_add_u32 s41, s41, 8192\n" \
    "buffer_load_dwordx4 v[232:235], v168, s[8:11], s41 offen sc1\n" \
    "s_add_u32 s41, s41, 8192\n" \
    "buffer_load_dwordx4 v[236:239], v168, s[8:11], s41 offen sc1\n" \
    "s_waitcnt vmcnt(0)\n" \
    "s_branch .Lup_srOd_%=\n" \
    ""
#define DN_ASM \
    "s_mov_b32 s32, 0\n" \
    "s_mov_b32 s33, 0\n" \
    "s_mov_b32 s38, 0x40000\n" \
    "v_mov_b32 v236, -1\n" \
    "v_mov_b32 v237, -1\n" \
    "v_mov_b32 v246, v150\n" \
    "s_add_u32 s48, s32, 2\n" \
    "s_min_u32 s48, s48, 64\n" \
    "s_and_b32 s49, s48, 1\n" \
    "s_xor_b32 s49, s49, 1\n" \
    "s_lshl_b32 s49, s49, 9\n" \
    "s_add_u32 s49, s49, s18\n" \
    "buffer_load_dwordx2 v[150:151], off, s[8:11], s49 sc1\n" \
    "s_waitcnt vmcnt(0)\n" \
    ".Ldn_l_%=:\n" \
    "s_mov_b32 s37, 0\n" \
    "s_waitcnt vmcnt(63)\n" \
    ".Ldn_ap_%=:\n" \
    "v_readfirstlane_b32 s36, v151\n" \
    "s_nop 0\n" \
    "s_cmp_gt_i32 s36, s48\n" \
    "s_cbranch_scc1 .Ldn_ax_%=\n" \
    "s_cmp_eq_u32 s36, s48\n" \
    "s_cbranch_scc1 .Ldn_aw_%=\n" \
    "s_sleep 3\n" \
    "buffer_load_dwordx2 v[150:151], off, s[8:11], s49 sc1\n" \
    "s_waitcnt vmcnt(0)\n" \
    "s_add_u32 s37, s37, 1\n" \
    "s_cmp_lt_u32 s37, s38\n" \
    "s_cbranch_scc1 .Ldn_ap_%=\n" \
    "s_mov_b32 s38, 0\n" \
    "s_branch .Ldn_ax_%=\n" \
    ".Ldn_aw_%=:\n" \
    "v_readfirstlane_b32 s36, v150\n" \
    "s_cmp_eq_u32 s32, 63\n" \
    "s_cselect_b32 s34, %[dlast], 0\n" \
    "s_add_u32 s36, s36, s34\n" \
    "s_add_u32 s36, s36, %[dxd]\n" \
    "s_branch .Ldn_atb_%=\n" \
    ".Ldn_ata_%=:\n" \
    "s_sleep 1\n" \
    ".Ldn_atb_%=:\n" \
    "s_memrealtime vcc\n" \
    "s_waitcnt lgkmcnt(0)\n" \
    "s_sub_i32 vcc_lo, s36, vcc_lo\n" \
    "s_cmp_gt_i32 vcc_lo, 8\n" \
    "s_cbranch_scc1 .Ldn_ata_%=\n" \
    "s_cmp_gt_i32 vcc_lo, 0\n" \
    "s_cbranch_scc1 .Ldn_atb_%=\n" \
    ".Ldn_ax_%=:\n" \
    "s_mul_i32 s39, s32, 98304\n" \
    "s_add_u32 s39, s39, s17\n" \
    "buffer_load_dwordx4 v[152:155], v144, s[8:11], s39 offen offset:0 sc1\n" \
    "buffer_load_dwordx4 v[156:159], v144, s[8:11], s39 offen offset:1024 sc1\n" \
    "buffer_load_dwordx4 v[160:163], v144, s[8:11], s39 offen offset:2048 sc1\n" \
    "buffer_load_dwordx4 v[164:167], v144, s[8:11], s39 offen offset:3072 sc1\n" \
    "s_add_u32 s39, s39, 4096\n" \
    "buffer_load_dwordx4 v[168:171], v144, s[8:11], s39 offen offset:0 sc1\n" \
    "buffer_load_dwordx4 v[172:175], v144, s[8:11], s39 offen offset:1024 sc1\n" \
    "s_mul_i32 s39, s32, 40960\n" \
    "s_add_u32 s39, s39, %[x1]+0\n" \
    "buffer_load_dwordx2 v[176:177], v147, s[8:11], s39 offen sc1\n" \
    "s_add_u32 s48, s32, 3\n" \
    "s_min_u32 s48, s48, 64\n" \
    "s_and_b32 s49, s48, 1\n" \
    "s_xor_b32 s49, s49, 1\n" \
    "s_lshl_b32 s49, s49, 9\n" \
    "s_add_u32 s49, s49, s18\n" \
    "buffer_load_dwordx2 v[150:151], off, s[8:11], s49 sc1\n" \
    "s_mov_b32 s37, 0\n" \
    ".Ldn_ra_%=:\n" \
    "s_waitcnt vmcnt(7)\n" \
    "v_mfma_f32_16x16x32_bf16 v[204:207], v[0:3], v[152:155], 0\n" \
    "v_mfma_f32_16x16x32_bf16 v[208:211], v[24:27], v[152:155], 0\n" \
    "v_mfma_f32_16x16x32_bf16 v[212:215], v[48:51], v[152:155], 0\n" \
    "v_mfma_f32_16x16x32_bf16 v[216:219], v[72:75], v[152:155], 0\n" \
    "v_max3_u32 v238, v152, v153, v154\n" \
    "v_mfma_f32_16x16x32_bf16 v[220:223], v[96:99], v[152:155], 0\n" \
    "v_mfma_f32_16x16x32_bf16 v[224:227], v[120:123], v[152:155], 0\n" \
    "s_waitcnt vmcnt(6)\n" \
    "v_mfma_f32_16x16x32_bf16 v[204:207], v[4:7], v[156:159], v[204:207]\n" \
    "v_mfma_f32_16x16x32_bf16 v[208:211], v[28:31], v[156:159], v[208:211]\n" \
    "v_max3_u32 v238, v238, v155, v156\n" \
    "v_mfma_f32_16x16x32_bf16 v[212:215], v[52:55], v[156:159], v[212:215]\n" \
    "v_mfma_f32_16x16x32_bf16 v[216:219], v[76:79], v[156:159], v[216:219]\n" \
    "v_max3_u32 v238, v238, v157, v158\n" \
    "v_mfma_f32_16x16x32_bf16 v[220:223], v[100:103], v[156:159], v[220:223]\n" \
    "v_mfma_f32_16x16x32_bf16 v[224:227], v[124:127], v[156:159], v[224:227]\n" \
    "s_waitcnt vmcnt(5)\n" \
    "v_mfma_f32_16x16x32_bf16 v[204:207], v[8:11], v[160:163], v[204:207]\n" \
    "v_mfma_f32_16x16x32_bf16 v[208:211], v[32:35], v[160:163], v[208:211]\n" \
    "v_max3_u32 v238, v238, v159, v160\n" \
    "v_mfma_f32_16x16x32_bf16 v[212:215], v[56:59], v[160:163], v[212:215]\n" \
    "v_mfma_f32_16x16x32_bf16 v[216:219], v[80:83], v[160:163], v[216:219]\n" \
    "v_max3_u32 v238, v238, v161, v162\n" \
    "v_mfma_f32_16x16x32_bf16 v[220:223], v[104:107], v[160:163], v[220:223]\n" \
    "v_mfma_f32_16x16x32_bf16 v[224:227], v[128:131], v[160:163], v[224:227]\n" \
    "s_waitcnt vmcnt(4)\n" \
    "v_mfma_f32_16x16x32_bf16 v[204:207], v[12:15], v[164:167], v[204:207]\n" \
    "v_mfma_f32_16x16x32_bf16 v[208:211], v[36:39], v[164:167], v[208:211]\n" \
    "v_max3_u32 v238, v238, v163, v164\n" \
    "v_mfma_f32_16x16x32_bf16 v[212:215], v[60:63], v[164:167], v[212:215]\n" \
    "v_mfma_f32_16x16x32_bf16 v[216:219], v[84:87], v[164:167], v[216:219]\n" \
    "v_max3_u32 v238, v238, v165, v166\n" \
    "v_mfma_f32_16x16x32_bf16 v[220:223], v[108:111], v[164:167], v[220:223]\n" \
    "v_mfma_f32_16x16x32_bf16 v[224:227], v[132:135], v[164:167], v[224:227]\n" \
    "s_waitcnt vmcnt(3)\n" \
    "v_mfma_f32_16x16x32_bf16 v[204:207], v[16:19], v[168:171], v[204:207]\n" \
    "v_mfma_f32_16x16x32_bf16 v[208:211], v[40:43], v[168:171], v[208:211]\n" \
    "v_max3_u32 v238, v238, v167, v168\n" \
    "v_mfma_f32_16x16x32_bf16 v[212:215], v[64:67], v[168:171], v[212:215]\n" \
    "v_mfma_f32_16x16x32_bf16 v[216:219], v[88:91], v[168:171], v[216:219]\n" \
    "v_max3_u32 v238, v238, v169, v170\n" \
    "v_mfma_f32_16x16x32_bf16 v[220:223], v[112:115], v[168:171], v[220:223]\n" \
    "v_mfma_f32_16x16x32_bf16 v[224:227], v[136:139], v[168:171], v[224:227]\n" \
    "s_waitcnt vmcnt(2)\n" \
    "v_mfma_f32_16x16x32_bf16 v[204:207], v[20:23], v[172:175], v[204:207]\n" \
    "v_mfma_f32_16x16x32_bf16 v[208:211], v[44:47], v[172:175], v[208:211]\n" \
    "v_max3_u32 v238, v238, v171, v172\n" \
    "v_mfma_f32_16x16x32_bf16 v[212:215], v[68:71], v[172:175], v[212:215]\n" \
    "v_mfma_f32_16x16x32_bf16 v[216:219], v[92:95], v[172:175], v[216:219]\n" \
    "v_max3_u32 v238, v238, v173, v174\n" \
    "v_mfma_f32_16x16x32_bf16 v[220:223], v[116:119], v[172:175], v[220:223]\n" \
    "v_mfma_f32_16x16x32_bf16 v[224:227], v[140:143], v[172:175], v[224:227]\n" \
    "s_waitcnt vmcnt(1)\n" \
    "v_max3_u32 v238, v238, v175, v176\n" \
    "v_max_u32 v238, v238, v177\n" \
    "v_cmp_eq_u32 vcc, -1, v238\n" \
    "s_nop 0\n" \
    "s_cbranch_vccnz .Ldn_ca_%=\n" \
    ".Ldn_xa_%=:\n" \
    "ds_write_b128 v145, v[204:207] offset:0\n" \
    "ds_write_b128 v145, v[208:211] offset:1024\n" \
    "ds_write_b128 v145, v[212:215] offset:2048\n" \
    "ds_write_b128 v145, v[216:219] offset:3072\n" \
    "ds_write_b128 v145, v[220:223] offset:4096\n" \
    "ds_write_b128 v145, v[224:227] offset:5120\n" \
    "s_mul_i32 s34, s32, 40960\n" \
    "s_cmp_gt_u32 s16, 5\n" \
    "s_waitcnt vmcnt(0) lgkmcnt(0)\n" \
    "s_barrier\n" \
    "s_cbranch_scc1 .Ldn_sa_%=\n" \
    "ds_read_b128 v[204:207], v146 offset:0\n" \
    "ds_read_b128 v[208:211], v146 offset:6144\n" \
    "ds_read_b128 v[212:215], v146 offset:12288\n" \
    "ds_read_b128 v[216:219], v146 offset:18432\n" \
    "ds_read_b128 v[220:223], v146 offset:24576\n" \
    "ds_read_b128 v[224:227], v146 offset:30720\n" \
    "ds_read_b128 v[228:231], v146 offset:36864\n" \
    "ds_read_b128 v[232:235], v146 offset:43008\n" \
    "v_lshlrev_b32 v240, 16, v176\n" \
    "v_and_b32 v241, 0xffff0000, v176\n" \
    "v_lshlrev_b32 v242, 16, v177\n" \
    "v_and_b32 v243, 0xffff0000, v177\n" \
    "s_waitcnt lgkmcnt(6)\n" \
    "v_pk_add_f32 v[204:205], v[204:205], v[208:209]\n" \
    "v_pk_add_f32 v[206:207], v[206:207], v[210:211]\n" \
    "s_waitcnt lgkmcnt(4)\n" \
    "v_pk_add_f32 v[212:213], v[212:213], v[216:217]\n" \
    "v_pk_add_f32 v[214:215], v[214:215], v[218:219]\n" \
    "v_pk_add_f32 v[204:205], v[204:205], v[212:213]\n" \
    "v_pk_add_f32 v[206:207], v[206:207], v[214:215]\n" \
    "s_waitcnt lgkmcnt(2)\n" \
    "v_pk_add_f32 v[220:221], v[220:221], v[224:225]\n" \
    "v_pk_add_f32 v[222:223], v[222:223], v[226:227]\n" \
    "s_waitcnt lgkmcnt(0)\n" \
    "v_pk_add_f32 v[228:229], v[228:229], v[232:233]\n" \
    "v_pk_add_f32 v[230:231], v[230:231], v[234:235]\n" \
    "v_pk_add_f32 v[220:221], v[220:221], v[228:229]\n" \
    "v_pk_add_f32 v[222:223], v[222:223], v[230:231]\n" \
    "v_pk_add_f32 v[204:205], v[204:205], v[220:221]\n" \
    "v_pk_add_f32 v[206:207], v[206:207], v[222:223]\n" \
    "v_cvt_pk_bf16_f32 v208, v204, v205\n" \
    "v_cvt_pk_bf16_f32 v209, v206, v207\n" \
    "v_lshlrev_b32 v204, 16, v208\n" \
    "v_and_b32 v205, 0xffff0000, v208\n" \
    "v_lshlrev_b32 v206, 16, v209\n" \
    "v_and_b32 v207, 0xffff0000, v209\n" \
    "v_pk_add_f32 v[204:205], v[204:205], v[240:241]\n" \
    "v_pk_add_f32 v[206:207], v[206:207], v[242:243]\n" \
    "v_cvt_pk_bf16_f32 v244, v204, v205\n" \
    "v_cvt_pk_bf16_f32 v245, v206, v207\n" \
    "v_min_u32 v244, -2, v244\n" \
    "v_min_u32 v245, -2, v245\n" \
    "s_add_u32 s34, s34, %[x2]\n" \
    "buffer_store_dwordx2 v[244:245], v147, s[8:11], s34 offen sc1\n" \
    "s_add_u32 s34, s34, %[pdist]*40960\n" \
    "s_cmp_lt_u32 s32, 64-%[pdist]\n" \
    "s_cselect_b32 s34, s34, 0x80000000\n" \
    "buffer_store_dwordx2 v[236:237], v147, s[8:11], s34 offen sc1\n" \
    ".Ldn_sa_%=:\n" \
    "s_add_u32 s32, s32, 1\n" \
    "s_cmp_lt_u32 s32, %[eup]+1\n" \
    "s_barrier\n" \
    "s_cbranch_scc1 .Ldn_l_%=\n" \
    ".Ldn_m_%=:\n" \
    "s_mov_b32 s37, 0\n" \
    "s_waitcnt vmcnt(63)\n" \
    ".Ldn_bp_%=:\n" \
    "v_readfirstlane_b32 s36, v151\n" \
    "s_nop 0\n" \
    "s_cmp_gt_i32 s36, s48\n" \
    "s_cbranch_scc1 .Ldn_bx_%=\n" \
    "s_cmp_eq_u32 s36, s48\n" \
    "s_cbranch_scc1 .Ldn_bw_%=\n" \
    "s_sleep 3\n" \
    "buffer_load_dwordx2 v[150:151], off, s[8:11], s49 sc1\n" \
    "s_waitcnt vmcnt(0)\n" \
    "s_add_u32 s37, s37, 1\n" \
    "s_cmp_lt_u32 s37, s38\n" \
    "s_cbranch_scc1 .Ldn_bp_%=\n" \
    "s_mov_b32 s38, 0\n" \
    "s_branch .Ldn_bx_%=\n" \
    ".Ldn_bw_%=:\n" \
    "v_readfirstlane_b32 s36, v150\n" \
    "s_cmp_eq_u32 s32, 63\n" \
    "s_cselect_b32 s34, %[dlast], 0\n" \
    "s_add_u32 s36, s36, s34\n" \
    "s_add_u32 s36, s36, %[dxd]\n" \
    "s_branch .Ldn_btb_%=\n" \
    ".Ldn_bta_%=:\n" \
    "s_sleep 1\n" \
    ".Ldn_btb_%=:\n" \
    "s_memrealtime vcc\n" \
    "s_waitcnt lgkmcnt(0)\n" \
    "s_sub_i32 vcc_lo, s36, vcc_lo\n" \
    "s_cmp_gt_i32 vcc_lo, 8\n" \
    "s_cbranch_scc1 .Ldn_bta_%=\n" \
    "s_cmp_gt_i32 vcc_lo, 0\n" \
    "s_cbranch_scc1 .Ldn_btb_%=\n" \
    ".Ldn_bx_%=:\n" \
    "s_mul_i32 s39, s32, 98304\n" \
    "s_add_u32 s39, s39, s17\n" \
    "buffer_load_dwordx4 v[152:155], v144, s[8:11], s39 offen offset:0 sc1\n" \
    "buffer_load_dwordx4 v[156:159], v144, s[8:11], s39 offen offset:1024 sc1\n" \
    "buffer_load_dwordx4 v[160:163], v144, s[8:11], s39 offen offset:2048 sc1\n" \
    "buffer_load_dwordx4 v[164:167], v144, s[8:11], s39 offen offset:3072 sc1\n" \
    "s_add_u32 s39, s39, 4096\n" \
    "buffer_load_dwordx4 v[168:171], v144, s[8:11], s39 offen offset:0 sc1\n" \
    "buffer_load_dwordx4 v[172:175], v144, s[8:11], s39 offen offset:1024 sc1\n" \
    "s_mul_i32 s39, s32, 40960\n" \
    "s_add_u32 s39, s39, %[x1]+0\n" \
    "buffer_load_dwordx2 v[176:177], v147, s[8:11], s39 offen sc1\n" \
    "s_mov_b32 s37, 0\n" \
    ".Ldn_rb_%=:\n" \
    "s_waitcnt vmcnt(6)\n" \
    "v_mfma_f32_16x16x32_bf16 v[204:207], v[0:3], v[152:155], 0\n" \
    "v_mfma_f32_16x16x32_bf16 v[208:211], v[24:27], v[152:155], 0\n" \
    "v_mfma_f32_16x16x32_bf16 v[212:215], v[48:51], v[152:155], 0\n" \
    "v_mfma_f32_16x16x32_bf16 v[216:219], v[72:75], v[152:155], 0\n" \
    "v_max3_u32 v238, v152, v153, v154\n" \
    "v_mfma_f32_16x16x32_bf16 v[220:223], v[96:99], v[152:155], 0\n" \
    "v_mfma_f32_16x16x32_bf16 v[224:227], v[120:123], v[152:155], 0\n" \
    "s_waitcnt vmcnt(5)\n" \
    "v_mfma_f32_16x16x32_bf16 v[204:207], v[4:7], v[156:159], v[204:207]\n" \
    "v_mfma_f32_16x16x32_bf16 v[208:211], v[28:31], v[156:159], v[208:211]\n" \
    "v_max3_u32 v238, v238, v155, v156\n" \
    "v_mfma_f32_16x16x32_bf16 v[212:215], v[52:55], v[156:159], v[212:215]\n" \
    "v_mfma_f32_16x16x32_bf16 v[216:219], v[76:79], v[156:159], v[216:219]\n" \
    "v_max3_u32 v238, v238, v157, v158\n" \
    "v_mfma_f32_16x16x32_bf16 v[220:223], v[100:103], v[156:159], v[220:223]\n" \
    "v_mfma_f32_16x16x32_bf16 v[224:227], v[124:127], v[156:159], v[224:227]\n" \
    "s_waitcnt vmcnt(4)\n" \
    "v_mfma_f32_16x16x32_bf16 v[204:207], v[8:11], v[160:163], v[204:207]\n" \
    "v_mfma_f32_16x16x32_bf16 v[208:211], v[32:35], v[160:163], v[208:211]\n" \
    "v_max3_u32 v238, v238, v159, v160\n" \
    "v_mfma_f32_16x16x32_bf16 v[212:215], v[56:59], v[160:163], v[212:215]\n" \
    "v_mfma_f32_16x16x32_bf16 v[216:219], v[80:83], v[160:163], v[216:219]\n" \
    "v_max3_u32 v238, v238, v161, v162\n" \
    "v_mfma_f32_16x16x32_bf16 v[220:223], v[104:107], v[160:163], v[220:223]\n" \
    "v_mfma_f32_16x16x32_bf16 v[224:227], v[128:131], v[160:163], v[224:227]\n" \
    "s_waitcnt vmcnt(3)\n" \
    "v_mfma_f32_16x16x32_bf16 v[204:207], v[12:15], v[164:167], v[204:207]\n" \
    "v_mfma_f32_16x16x32_bf16 v[208:211], v[36:39], v[164:167], v[208:211]\n" \
    "v_max3_u32 v238, v238, v163, v164\n" \
    "v_mfma_f32_16x16x32_bf16 v[212:215], v[60:63], v[164:167], v[212:215]\n" \
    "v_mfma_f32_16x16x32_bf16 v[216:219], v[84:87], v[164:167], v[216:219]\n" \
    "v_max3_u32 v238, v238, v165, v166\n" \
    "v_mfma_f32_16x16x32_bf16 v[220:223], v[108:111], v[164:167], v[220:223]\n" \
    "v_mfma_f32_16x16x32_bf16 v[224:227], v[132:135], v[164:167], v[224:227]\n" \
    "s_waitcnt vmcnt(2)\n" \
    "v_mfma_f32_16x16x32_bf16 v[204:207], v[16:19], v[168:171], v[204:207]\n" \
    "v_mfma_f32_16x16x32_bf16 v[208:211], v[40:43], v[168:171], v[208:211]\n" \
    "v_max3_u32 v238, v238, v167, v168\n" \
    "v_mfma_f32_16x16x32_bf16 v[212:215], v[64:67], v[168:171], v[212:215]\n" \
    "v_mfma_f32_16x16x32_bf16 v[216:219], v[88:91], v[168:171], v[216:219]\n" \
    "v_max3_u32 v238, v238, v169, v170\n" \
    "v_mfma_f32_16x16x32_bf16 v[220:223], v[112:115], v[168:171], v[220:223]\n" \
    "v_mfma_f32_16x16x32_bf16 v[224:227], v[136:139], v[168:171], v[224:227]\n" \
    "s_waitcnt vmcnt(1)\n" \
    "v_mfma_f32_16x16x32_bf16 v[204:207], v[20:23], v[172:175], v[204:207]\n" \
    "v_mfma_f32_16x16x32_bf16 v[208:211], v[44:47], v[172:175], v[208:211]\n" \
    "v_max3_u32 v238, v238, v171, v172\n" \
    "v_mfma_f32_16x16x32_bf16 v[212:215], v[68:71], v[172:175], v[212:215]\n" \
    "v_mfma_f32_16x16x32_bf16 v[216:219], v[92:95], v[172:175], v[216:219]\n" \
    "v_max3_u32 v238, v238, v173, v174\n" \
    "v_mfma_f32_16x16x32_bf16 v[220:223], v[116:119], v[172:175], v[220:223]\n" \
    "v_mfma_f32_16x16x32_bf16 v[224:227], v[140:143], v[172:175], v[224:227]\n" \
    "s_waitcnt vmcnt(0)\n" \
    "v_max3_u32 v238, v238, v175, v176\n" \
    "v_max_u32 v238, v238, v177\n" \
    "v_cmp_eq_u32 vcc, -1, v238\n" \
    "s_nop 0\n" \
    "s_cbranch_vccnz .Ldn_cb_%=\n" \
    ".Ldn_xb_%=:\n" \
    "s_mul_i32 s39, s33, 98304\n" \
    "s_add_u32 s39, s39, s17\n" \
    "s_add_u32 s39, s39, 6291456\n" \
    "buffer_load_dwordx4 v[178:181], v144, s[8:11], s39 offen offset:0 sc1\n" \
    "buffer_load_dwordx4 v[182:185], v144, s[8:11], s39 offen offset:1024 sc1\n" \
    "buffer_load_dwordx4 v[186:189], v144, s[8:11], s39 offen offset:2048 sc1\n" \
    "buffer_load_dwordx4 v[190:193], v144, s[8:11], s39 offen offset:3072 sc1\n" \
    "s_add_u32 s39, s39, 4096\n" \
    "buffer_load_dwordx4 v[194:197], v144, s[8:11], s39 offen offset:0 sc1\n" \
    "buffer_load_dwordx4 v[198:201], v144, s[8:11], s39 offen offset:1024 sc1\n" \
    "s_mul_i32 s39, s33, 40960\n" \
    "s_add_u32 s39, s39, %[x1]+2621440\n" \
    "buffer_load_dwordx2 v[202:203], v147, s[8:11], s39 offen sc1\n" \
    "ds_write_b128 v145, v[204:207] offset:0\n" \
    "ds_write_b128 v145, v[208:211] offset:1024\n" \
    "ds_write_b128 v145, v[212:215] offset:2048\n" \
    "ds_write_b128 v145, v[216:219] offset:3072\n" \
    "ds_write_b128 v145, v[220:223] offset:4096\n" \
    "ds_write_b128 v145, v[224:227] offset:5120\n" \
    "s_mul_i32 s34, s32, 40960\n" \
    "s_cmp_gt_u32 s16, 5\n" \
    "s_waitcnt lgkmcnt(0)\n" \
    "s_barrier\n" \
    "s_cbranch_scc1 .Ldn_sb_%=\n" \
    "ds_read_b128 v[204:207], v146 offset:0\n" \
    "ds_read_b128 v[208:211], v146 offset:6144\n" \
    "ds_read_b128 v[212:215], v146 offset:12288\n" \
    "ds_read_b128 v[216:219], v146 offset:18432\n" \
    "ds_read_b128 v[220:223], v146 offset:24576\n" \
    "ds_read_b128 v[224:227], v146 offset:30720\n" \
    "ds_read_b128 v[228:231], v146 offset:36864\n" \
    "ds_read_b128 v[232:235], v146 offset:43008\n" \
    "v_lshlrev_b32 v240, 16, v176\n" \
    "v_and_b32 v241, 0xffff0000, v176\n" \
    "v_lshlrev_b32 v242, 16, v177\n" \
    "v_and_b32 v243, 0xffff0000, v177\n" \
    "s_waitcnt lgkmcnt(6)\n" \
    "v_pk_add_f32 v[204:205], v[204:205], v[208:209]\n" \
    "v_pk_add_f32 v[206:207], v[206:207], v[210:211]\n" \
    "s_waitcnt lgkmcnt(4)\n" \
    "v_pk_add_f32 v[212:213], v[212:213], v[216:217]\n" \
    "v_pk_add_f32 v[214:215], v[214:215], v[218:219]\n" \
    "v_pk_add_f32 v[204:205], v[204:205], v[212:213]\n" \
    "v_pk_add_f32 v[206:207], v[206:207], v[214:215]\n" \
    "s_waitcnt lgkmcnt(2)\n" \
    "v_pk_add_f32 v[220:221], v[220:221], v[224:225]\n" \
    "v_pk_add_f32 v[222:223], v[222:223], v[226:227]\n" \
    "s_waitcnt lgkmcnt(0)\n" \
    "v_pk_add_f32 v[228:229], v[228:229], v[232:233]\n" \
    "v_pk_add_f32 v[230:231], v[230:231], v[234:235]\n" \
    "v_pk_add_f32 v[220:221], v[220:221], v[228:229]\n" \
    "v_pk_add_f32 v[222:223], v[222:223], v[230:231]\n" \
    "v_pk_add_f32 v[204:205], v[204:205], v[220:221]\n" \
    "v_pk_add_f32 v[206:207], v[206:207], v[222:223]\n" \
    "v_cvt_pk_bf16_f32 v208, v204, v205\n" \
    "v_cvt_pk_bf16_f32 v209, v206, v207\n" \
    "v_lshlrev_b32 v204, 16, v208\n" \
    "v_and_b32 v205, 0xffff0000, v208\n" \
    "v_lshlrev_b32 v206, 16, v209\n" \
    "v_and_b32 v207, 0xffff0000, v209\n" \
    "v_pk_add_f32 v[204:205], v[204:205], v[240:241]\n" \
    "v_pk_add_f32 v[206:207], v[206:207], v[242:243]\n" \
    "v_cvt_pk_bf16_f32 v244, v204, v205\n" \
    "v_cvt_pk_bf16_f32 v245, v206, v207\n" \
    "v_min_u32 v244, -2, v244\n" \
    "v_min_u32 v245, -2, v245\n" \
    "s_add_u32 s34, s34, %[x2]\n" \
    "buffer_store_dwordx2 v[244:245], v147, s[8:11], s34 offen sc1\n" \
    "s_add_u32 s34, s34, %[pdist]*40960\n" \
    "s_cmp_lt_u32 s32, 64-%[pdist]\n" \
    "s_cselect_b32 s34, s34, 0x80000000\n" \
    "buffer_store_dwordx2 v[236:237], v147, s[8:11], s34 offen sc1\n" \
    ".Ldn_sb_%=:\n" \
    "s_barrier\n" \
    "s_add_u32 s48, s32, 3\n" \
    "s_min_u32 s48, s48, 64\n" \
    "s_and_b32 s49, s48, 1\n" \
    "s_xor_b32 s49, s49, 1\n" \
    "s_lshl_b32 s49, s49, 9\n" \
    "s_add_u32 s49, s49, s18\n" \
    "buffer_load_dwordx2 v[150:151], off, s[8:11], s49 sc1\n" \
    "s_mov_b32 s37, 0\n" \
    ".Ldn_rc_%=:\n" \
    "s_waitcnt vmcnt(7)\n" \
    "v_mfma_f32_16x16x32_bf16 v[204:207], v[0:3], v[178:181], 0\n" \
    "v_mfma_f32_16x16x32_bf16 v[208:211], v[24:27], v[178:181], 0\n" \
    "v_mfma_f32_16x16x32_bf16 v[212:215], v[48:51], v[178:181], 0\n" \
    "v_mfma_f32_16x16x32_bf16 v[216:219], v[72:75], v[178:181], 0\n" \
    "v_max3_u32 v238, v178, v179, v180\n" \
    "v_mfma_f32_16x16x32_bf16 v[220:223], v[96:99], v[178:181], 0\n" \
    "v_mfma_f32_16x16x32_bf16 v[224:227], v[120:123], v[178:181], 0\n" \
    "s_waitcnt vmcnt(6)\n" \
    "v_mfma_f32_16x16x32_bf16 v[204:207], v[4:7], v[182:185], v[204:207]\n" \
    "v_mfma_f32_16x16x32_bf16 v[208:211], v[28:31], v[182:185], v[208:211]\n" \
    "v_max3_u32 v238, v238, v181, v182\n" \
    "v_mfma_f32_16x16x32_bf16 v[212:215], v[52:55], v[182:185], v[212:215]\n" \
    "v_mfma_f32_16x16x32_bf16 v[216:219], v[76:79], v[182:185], v[216:219]\n" \
    "v_max3_u32 v238, v238, v183, v184\n" \
    "v_mfma_f32_16x16x32_bf16 v[220:223], v[100:103], v[182:185], v[220:223]\n" \
    "v_mfma_f32_16x16x32_bf16 v[224:227], v[124:127], v[182:185], v[224:227]\n" \
    "s_waitcnt vmcnt(5)\n" \
    "v_mfma_f32_16x16x32_bf16 v[204:207], v[8:11], v[186:189], v[204:207]\n" \
    "v_mfma_f32_16x16x32_bf16 v[208:211], v[32:35], v[186:189], v[208:211]\n" \
    "v_max3_u32 v238, v238, v185, v186\n" \
    "v_mfma_f32_16x16x32_bf16 v[212:215], v[56:59], v[186:189], v[212:215]\n" \
    "v_mfma_f32_16x16x32_bf16 v[216:219], v[80:83], v[186:189], v[216:219]\n" \
    "v_max3_u32 v238, v238, v187, v188\n" \
    "v_mfma_f32_16x16x32_bf16 v[220:223], v[104:107], v[186:189], v[220:223]\n" \
    "v_mfma_f32_16x16x32_bf16 v[224:227], v[128:131], v[186:189], v[224:227]\n" \
    "s_waitcnt vmcnt(4)\n" \
    "v_mfma_f32_16x16x32_bf16 v[204:207], v[12:15], v[190:193], v[204:207]\n" \
    "v_mfma_f32_16x16x32_bf16 v[208:211], v[36:39], v[190:193], v[208:211]\n" \
    "v_max3_u32 v238, v238, v189, v190\n" \
    "v_mfma_f32_16x16x32_bf16 v[212:215], v[60:63], v[190:193], v[212:215]\n" \
    "v_mfma_f32_16x16x32_bf16 v[216:219], v[84:87], v[190:193], v[216:219]\n" \
    "v_max3_u32 v238, v238, v191, v192\n" \
    "v_mfma_f32_16x16x32_bf16 v[220:223], v[108:111], v[190:193], v[220:223]\n" \
    "v_mfma_f32_16x16x32_bf16 v[224:227], v[132:135], v[190:193], v[224:227]\n" \
    "s_waitcnt vmcnt(3)\n" \
    "v_mfma_f32_16x16x32_bf16 v[204:207], v[16:19], v[194:197], v[204:207]\n" \
    "v_mfma_f32_16x16x32_bf16 v[208:211], v[40:43], v[194:197], v[208:211]\n" \
    "v_max3_u32 v238, v238, v193, v194\n" \
    "v_mfma_f32_16x16x32_bf16 v[212:215], v[64:67], v[194:197], v[212:215]\n" \
    "v_mfma_f32_16x16x32_bf16 v[216:219], v[88:91], v[194:197], v[216:219]\n" \
    "v_max3_u32 v238, v238, v195, v196\n" \
    "v_mfma_f32_16x16x32_bf16 v[220:223], v[112:115], v[194:197], v[220:223]\n" \
    "v_mfma_f32_16x16x32_bf16 v[224:227], v[136:139], v[194:197], v[224:227]\n" \
    "s_waitcnt vmcnt(2)\n" \
    "v_mfma_f32_16x16x32_bf16 v[204:207], v[20:23], v[198:201], v[204:207]\n" \
    "v_mfma_f32_16x16x32_bf16 v[208:211], v[44:47], v[198:201], v[208:211]\n" \
    "v_max3_u32 v238, v238, v197, v198\n" \
    "v_mfma_f32_16x16x32_bf16 v[212:215], v[68:71], v[198:201], v[212:215]\n" \
    "v_mfma_f32_16x16x32_bf16 v[216:219], v[92:95], v[198:201], v[216:219]\n" \
    "v_max3_u32 v238, v238, v199, v200\n" \
    "v_mfma_f32_16x16x32_bf16 v[220:223], v[116:119], v[198:201], v[220:223]\n" \
    "v_mfma_f32_16x16x32_bf16 v[224:227], v[140:143], v[198:201], v[224:227]\n" \
    "s_waitcnt vmcnt(1)\n" \
    "v_max3_u32 v238, v238, v201, v202\n" \
    "v_max_u32 v238, v238, v203\n" \
    "v_cmp_eq_u32 vcc, -1, v238\n" \
    "s_nop 0\n" \
    "s_cbranch_vccnz .Ldn_cc_%=\n" \
    ".Ldn_xc_%=:\n" \
    "ds_write_b128 v145, v[204:207] offset:49152\n" \
    "ds_write_b128 v145, v[208:211] offset:50176\n" \
    "ds_write_b128 v145, v[212:215] offset:51200\n" \
    "ds_write_b128 v145, v[216:219] offset:52224\n" \
    "ds_write_b128 v145, v[220:223] offset:53248\n" \
    "ds_write_b128 v145, v[224:227] offset:54272\n" \
    "s_mul_i32 s34, s33, 40960\n" \
    "s_cmp_gt_u32 s16, 5\n" \
    "s_waitcnt vmcnt(0) lgkmcnt(0)\n" \
    "s_barrier\n" \
    "s_cbranch_scc1 .Ldn_sc_%=\n" \
    "v_add_u32 v239, 49152, v146\n" \
    "ds_read_b128 v[204:207], v239 offset:0\n" \
    "ds_read_b128 v[208:211], v239 offset:6144\n" \
    "ds_read_b128 v[212:215], v239 offset:12288\n" \
    "ds_read_b128 v[216:219], v239 offset:18432\n" \
    "ds_read_b128 v[220:223], v239 offset:24576\n" \
    "ds_read_b128 v[224:227], v239 offset:30720\n" \
    "ds_read_b128 v[228:231], v239 offset:36864\n" \
    "ds_read_b128 v[232:235], v239 offset:43008\n" \
    "v_lshlrev_b32 v240, 16, v202\n" \
    "v_and_b32 v241, 0xffff0000, v202\n" \
    "v_lshlrev_b32 v242, 16, v203\n" \
    "v_and_b32 v243, 0xffff0000, v203\n" \
    "s_waitcnt lgkmcnt(6)\n" \
    "v_pk_add_f32 v[204:205], v[204:205], v[208:209]\n" \
    "v_pk_add_f32 v[206:207], v[206:207], v[210:211]\n" \
    "s_waitcnt lgkmcnt(4)\n" \
    "v_pk_add_f32 v[212:213], v[212:213], v[216:217]\n" \
    "v_pk_add_f32 v[214:215], v[214:215], v[218:219]\n" \
    "v_pk_add_f32 v[204:205], v[204:205], v[212:213]\n" \
    "v_pk_add_f32 v[206:207], v[206:207], v[214:215]\n" \
    "s_waitcnt lgkmcnt(2)\n" \
    "v_pk_add_f32 v[220:221], v[220:221], v[224:225]\n" \
    "v_pk_add_f32 v[222:223], v[222:223], v[226:227]\n" \
    "s_waitcnt lgkmcnt(0)\n" \
    "v_pk_add_f32 v[228:229], v[228:229], v[232:233]\n" \
    "v_pk_add_f32 v[230:231], v[230:231], v[234:235]\n" \
    "v_pk_add_f32 v[220:221], v[220:221], v[228:229]\n" \
    "v_pk_add_f32 v[222:223], v[222:223], v[230:231]\n" \
    "v_pk_add_f32 v[204:205], v[204:205], v[220:221]\n" \
    "v_pk_add_f32 v[206:207], v[206:207], v[222:223]\n" \
    "v_cvt_pk_bf16_f32 v208, v204, v205\n" \
    "v_cvt_pk_bf16_f32 v209, v206, v207\n" \
    "v_lshlrev_b32 v204, 16, v208\n" \
    "v_and_b32 v205, 0xffff0000, v208\n" \
    "v_lshlrev_b32 v206, 16, v209\n" \
    "v_and_b32 v207, 0xffff0000, v209\n" \
    "v_pk_add_f32 v[204:205], v[204:205], v[240:241]\n" \
    "v_pk_add_f32 v[206:207], v[206:207], v[242:243]\n" \
    "v_cvt_pk_bf16_f32 v244, v204, v205\n" \
    "v_cvt_pk_bf16_f32 v245, v206, v207\n" \
    "buffer_store_dwordx2 v[244:245], v148, s[12:15], s34 offen\n" \
    ".Ldn_sc_%=:\n" \
    "s_add_u32 s33, s33, 1\n" \
    "s_add_u32 s32, s32, 1\n" \
    "s_cmp_lt_u32 s32, 64\n" \
    "s_cbranch_scc1 .Ldn_m_%=\n" \
    "s_add_u32 s18, s18, 1024\n" \
    "s_add_u32 s48, s33, 2\n" \
    "s_min_u32 s48, s48, 64\n" \
    "s_and_b32 s49, s48, 1\n" \
    "s_xor_b32 s49, s49, 1\n" \
    "s_lshl_b32 s49, s49, 9\n" \
    "s_add_u32 s49, s49, s18\n" \
    "buffer_load_dwordx2 v[150:151], off, s[8:11], s49 sc1\n" \
    "s_waitcnt vmcnt(0)\n" \
    ".Ldn_n_%=:\n" \
    "s_barrier\n" \
    "s_mov_b32 s37, 0\n" \
    "s_waitcnt vmcnt(63)\n" \
    ".Ldn_dp_%=:\n" \
    "v_readfirstlane_b32 s36, v151\n" \
    "s_nop 0\n" \
    "s_cmp_gt_i32 s36, s48\n" \
    "s_cbranch_scc1 .Ldn_dx_%=\n" \
    "s_cmp_eq_u32 s36, s48\n" \
    "s_cbranch_scc1 .Ldn_dw_%=\n" \
    "s_sleep 3\n" \
    "buffer_load_dwordx2 v[150:151], off, s[8:11], s49 sc1\n" \
    "s_waitcnt vmcnt(0)\n" \
    "s_add_u32 s37, s37, 1\n" \
    "s_cmp_lt_u32 s37, s38\n" \
    "s_cbranch_scc1 .Ldn_dp_%=\n" \
    "s_mov_b32 s38, 0\n" \
    "s_branch .Ldn_dx_%=\n" \
    ".Ldn_dw_%=:\n" \
    "v_readfirstlane_b32 s36, v150\n" \
    "s_cmp_eq_u32 s33, 63\n" \
    "s_cselect_b32 s34, %[dlast], 0\n" \
    "s_add_u32 s36, s36, s34\n" \
    "s_add_u32 s36, s36, %[dxd]\n" \
    "s_branch .Ldn_dtb_%=\n" \
    ".Ldn_dta_%=:\n" \
    "s_sleep 1\n" \
    ".Ldn_dtb_%=:\n" \
    "s_memrealtime vcc\n" \
    "s_waitcnt lgkmcnt(0)\n" \
    "s_sub_i32 vcc_lo, s36, vcc_lo\n" \
    "s_cmp_gt_i32 vcc_lo, 8\n" \
    "s_cbranch_scc1 .Ldn_dta_%=\n" \
    "s_cmp_gt_i32 vcc_lo, 0\n" \
    "s_cbranch_scc1 .Ldn_dtb_%=\n" \
    ".Ldn_dx_%=:\n" \
    "s_mul_i32 s39, s33, 98304\n" \
    "s_add_u32 s39, s39, s17\n" \
    "s_add_u32 s39, s39, 6291456\n" \
    "buffer_load_dwordx4 v[178:181], v144, s[8:11], s39 offen offset:0 sc1\n" \
    "buffer_load_dwordx4 v[182:185], v144, s[8:11], s39 offen offset:1024 sc1\n" \
    "buffer_load_dwordx4 v[186:189], v144, s[8:11], s39 offen offset:2048 sc1\n" \
    "buffer_load_dwordx4 v[190:193], v144, s[8:11], s39 offen offset:3072 sc1\n" \
    "s_add_u32 s39, s39, 4096\n" \
    "buffer_load_dwordx4 v[194:197], v144, s[8:11], s39 offen offset:0 sc1\n" \
    "buffer_load_dwordx4 v[198:201], v144, s[8:11], s39 offen offset:1024 sc1\n" \
    "s_mul_i32 s39, s33, 40960\n" \
    "s_add_u32 s39, s39, %[x1]+2621440\n" \
    "buffer_load_dwordx2 v[202:203], v147, s[8:11], s39 offen sc1\n" \
    "s_add_u32 s48, s33, 3\n" \
    "s_min_u32 s48, s48, 64\n" \
    "s_and_b32 s49, s48, 1\n" \
    "s_xor_b32 s49, s49, 1\n" \
    "s_lshl_b32 s49, s49, 9\n" \
    "s_add_u32 s49, s49, s18\n" \
    "buffer_load_dwordx2 v[150:151], off, s[8:11], s49 sc1\n" \
    "s_mov_b32 s37, 0\n" \
    ".Ldn_rd_%=:\n" \
    "s_waitcnt vmcnt(7)\n" \
    "v_mfma_f32_16x16x32_bf16 v[204:207], v[0:3], v[178:181], 0\n" \
    "v_mfma_f32_16x16x32_bf16 v[208:211], v[24:27], v[178:181], 0\n" \
    "v_mfma_f32_16x16x32_bf16 v[212:215], v[48:51], v[178:181], 0\n" \
    "v_mfma_f32_16x16x32_bf16 v[216:219], v[72:75], v[178:181], 0\n" \
    "v_max3_u32 v238, v178, v179, v180\n" \
    "v_mfma_f32_16x16x32_bf16 v[220:223], v[96:99], v[178:181], 0\n" \
    "v_mfma_f32_16x16x32_bf16 v[224:227], v[120:123], v[178:181], 0\n" \
    "s_waitcnt vmcnt(6)\n" \
    "v_mfma_f32_16x16x32_bf16 v[204:207], v[4:7], v[182:185], v[204:207]\n" \
    "v_mfma_f32_16x16x32_bf16 v[208:211], v[28:31], v[182:185], v[208:211]\n" \
    "v_max3_u32 v238, v238, v181, v182\n" \
    "v_mfma_f32_16x16x32_bf16 v[212:215], v[52:55], v[182:185], v[212:215]\n" \
    "v_mfma_f32_16x16x32_bf16 v[216:219], v[76:79], v[182:185], v[216:219]\n" \
    "v_max3_u32 v238, v238, v183, v184\n" \
    "v_mfma_f32_16x16x32_bf16 v[220:223], v[100:103], v[182:185], v[220:223]\n" \
    "v_mfma_f32_16x16x32_bf16 v[224:227], v[124:127], v[182:185], v[224:227]\n" \
    "s_waitcnt vmcnt(5)\n" \
    "v_mfma_f32_16x16x32_bf16 v[204:207], v[8:11], v[186:189], v[204:207]\n" \
    "v_mfma_f32_16x16x32_bf16 v[208:211], v[32:35], v[186:189], v[208:211]\n" \
    "v_max3_u32 v238, v238, v185, v186\n" \
    "v_mfma_f32_16x16x32_bf16 v[212:215], v[56:59], v[186:189], v[212:215]\n" \
    "v_mfma_f32_16x16x32_bf16 v[216:219], v[80:83], v[186:189], v[216:219]\n" \
    "v_max3_u32 v238, v238, v187, v188\n" \
    "v_mfma_f32_16x16x32_bf16 v[220:223], v[104:107], v[186:189], v[220:223]\n" \
    "v_mfma_f32_16x16x32_bf16 v[224:227], v[128:131], v[186:189], v[224:227]\n" \
    "s_waitcnt vmcnt(4)\n" \
    "v_mfma_f32_16x16x32_bf16 v[204:207], v[12:15], v[190:193], v[204:207]\n" \
    "v_mfma_f32_16x16x32_bf16 v[208:211], v[36:39], v[190:193], v[208:211]\n" \
    "v_max3_u32 v238, v238, v189, v190\n" \
    "v_mfma_f32_16x16x32_bf16 v[212:215], v[60:63], v[190:193], v[212:215]\n" \
    "v_mfma_f32_16x16x32_bf16 v[216:219], v[84:87], v[190:193], v[216:219]\n" \
    "v_max3_u32 v238, v238, v191, v192\n" \
    "v_mfma_f32_16x16x32_bf16 v[220:223], v[108:111], v[190:193], v[220:223]\n" \
    "v_mfma_f32_16x16x32_bf16 v[224:227], v[132:135], v[190:193], v[224:227]\n" \
    "s_waitcnt vmcnt(3)\n" \
    "v_mfma_f32_16x16x32_bf16 v[204:207], v[16:19], v[194:197], v[204:207]\n" \
    "v_mfma_f32_16x16x32_bf16 v[208:211], v[40:43], v[194:197], v[208:211]\n" \
    "v_max3_u32 v238, v238, v193, v194\n" \
    "v_mfma_f32_16x16x32_bf16 v[212:215], v[64:67], v[194:197], v[212:215]\n" \
    "v_mfma_f32_16x16x32_bf16 v[216:219], v[88:91], v[194:197], v[216:219]\n" \
    "v_max3_u32 v238, v238, v195, v196\n" \
    "v_mfma_f32_16x16x32_bf16 v[220:223], v[112:115], v[194:197], v[220:223]\n" \
    "v_mfma_f32_16x16x32_bf16 v[224:227], v[136:139], v[194:197], v[224:227]\n" \
    "s_waitcnt vmcnt(2)\n" \
    "v_mfma_f32_16x16x32_bf16 v[204:207], v[20:23], v[198:201], v[204:207]\n" \
    "v_mfma_f32_16x16x32_bf16 v[208:211], v[44:47], v[198:201], v[208:211]\n" \
    "v_max3_u32 v238, v238, v197, v198\n" \
    "v_mfma_f32_16x16x32_bf16 v[212:215], v[68:71], v[198:201], v[212:215]\n" \
    "v_mfma_f32_16x16x32_bf16 v[216:219], v[92:95], v[198:201], v[216:219]\n" \
    "v_max3_u32 v238, v238, v199, v200\n" \
    "v_mfma_f32_16x16x32_bf16 v[220:223], v[116:119], v[198:201], v[220:223]\n" \
    "v_mfma_f32_16x16x32_bf16 v[224:227], v[140:143], v[198:201], v[224:227]\n" \
    "s_waitcnt vmcnt(1)\n" \
    "v_max3_u32 v238, v238, v201, v202\n" \
    "v_max_u32 v238, v238, v203\n" \
    "v_cmp_eq_u32 vcc, -1, v238\n" \
    "s_nop 0\n" \
    "s_cbranch_vccnz .Ldn_cd_%=\n" \
    ".Ldn_xd_%=:\n" \
    "ds_write_b128 v145, v[204:207] offset:49152\n" \
    "ds_write_b128 v145, v[208:211] offset:50176\n" \
    "ds_write_b128 v145, v[212:215] offset:51200\n" \
    "ds_write_b128 v145, v[216:219] offset:52224\n" \
    "ds_write_b128 v145, v[220:223] offset:53248\n" \
    "ds_write_b128 v145, v[224:227] offset:54272\n" \
    "s_mul_i32 s34, s33, 40960\n" \
    "s_cmp_gt_u32 s16, 5\n" \
    "s_waitcnt vmcnt(0) lgkmcnt(0)\n" \
    "s_barrier\n" \
    "s_cbranch_scc1 .Ldn_sd_%=\n" \
    "v_add_u32 v239, 49152, v146\n" \
    "ds_read_b128 v[204:207], v239 offset:0\n" \
    "ds_read_b128 v[208:211], v239 offset:6144\n" \
    "ds_read_b128 v[212:215], v239 offset:12288\n" \
    "ds_read_b128 v[216:219], v239 offset:18432\n" \
    "ds_read_b128 v[220:223], v239 offset:24576\n" \
    "ds_read_b128 v[224:227], v239 offset:30720\n" \
    "ds_read_b128 v[228:231], v239 offset:36864\n" \
    "ds_read_b128 v[232:235], v239 offset:43008\n" \
    "v_lshlrev_b32 v240, 16, v202\n" \
    "v_and_b32 v241, 0xffff0000, v202\n" \
    "v_lshlrev_b32 v242, 16, v203\n" \
    "v_and_b32 v243, 0xffff0000, v203\n" \
    "s_waitcnt lgkmcnt(6)\n" \
    "v_pk_add_f32 v[204:205], v[204:205], v[208:209]\n" \
    "v_pk_add_f32 v[206:207], v[206:207], v[210:211]\n" \
    "s_waitcnt lgkmcnt(4)\n" \
    "v_pk_add_f32 v[212:213], v[212:213], v[216:217]\n" \
    "v_pk_add_f32 v[214:215], v[214:215], v[218:219]\n" \
    "v_pk_add_f32 v[204:205], v[204:205], v[212:213]\n" \
    "v_pk_add_f32 v[206:207], v[206:207], v[214:215]\n" \
    "s_waitcnt lgkmcnt(2)\n" \
    "v_pk_add_f32 v[220:221], v[220:221], v[224:225]\n" \
    "v_pk_add_f32 v[222:223], v[222:223], v[226:227]\n" \
    "s_waitcnt lgkmcnt(0)\n" \
    "v_pk_add_f32 v[228:229], v[228:229], v[232:233]\n" \
    "v_pk_add_f32 v[230:231], v[230:231], v[234:235]\n" \
    "v_pk_add_f32 v[220:221], v[220:221], v[228:229]\n" \
    "v_pk_add_f32 v[222:223], v[222:223], v[230:231]\n" \
    "v_pk_add_f32 v[204:205], v[204:205], v[220:221]\n" \
    "v_pk_add_f32 v[206:207], v[206:207], v[222:223]\n" \
    "v_cvt_pk_bf16_f32 v208, v204, v205\n" \
    "v_cvt_pk_bf16_f32 v209, v206, v207\n" \
    "v_lshlrev_b32 v204, 16, v208\n" \
    "v_and_b32 v205, 0xffff0000, v208\n" \
    "v_lshlrev_b32 v206, 16, v209\n" \
    "v_and_b32 v207, 0xffff0000, v209\n" \
    "v_pk_add_f32 v[204:205], v[204:205], v[240:241]\n" \
    "v_pk_add_f32 v[206:207], v[206:207], v[242:243]\n" \
    "v_cvt_pk_bf16_f32 v244, v204, v205\n" \
    "v_cvt_pk_bf16_f32 v245, v206, v207\n" \
    "buffer_store_dwordx2 v[244:245], v148, s[12:15], s34 offen\n" \
    ".Ldn_sd_%=:\n" \
    "s_add_u32 s33, s33, 1\n" \
    "s_cmp_lt_u32 s33, 64\n" \
    "s_cbranch_scc1 .Ldn_n_%=\n" \
    "s_waitcnt vmcnt(0)\n" \
    "s_endpgm\n" \
    ".Ldn_ca_%=:\n" \
    "s_add_u32 s37, s37, 1\n" \
    "s_cmp_lt_u32 s37, s38\n" \
    "s_cselect_b32 s38, s38, 0\n" \
    "s_cbranch_scc0 .Ldn_xa_%=\n" \
    "s_sleep 4\n" \
    "s_mul_i32 s39, s32, 98304\n" \
    "s_add_u32 s39, s39, s17\n" \
    "buffer_load_dwordx4 v[152:155], v144, s[8:11], s39 offen offset:0 sc1\n" \
    "buffer_load_dwordx4 v[156:159], v144, s[8:11], s39 offen offset:1024 sc1\n" \
    "buffer_load_dwordx4 v[160:163], v144, s[8:11], s39 offen offset:2048 sc1\n" \
    "buffer_load_dwordx4 v[164:167], v144, s[8:11], s39 offen offset:3072 sc1\n" \
    "s_add_u32 s39, s39, 4096\n" \
    "buffer_load_dwordx4 v[168:171], v144, s[8:11], s39 offen offset:0 sc1\n" \
    "buffer_load_dwordx4 v[172:175], v144, s[8:11], s39 offen offset:1024 sc1\n" \
    "s_mul_i32 s39, s32, 40960\n" \
    "s_add_u32 s39, s39, %[x1]+0\n" \
    "buffer_load_dwordx2 v[176:177], v147, s[8:11], s39 offen sc1\n" \
    "s_waitcnt vmcnt(0)\n" \
    "s_branch .Ldn_ra_%=\n" \
    ".Ldn_cb_%=:\n" \
    "s_add_u32 s37, s37, 1\n" \
    "s_cmp_lt_u32 s37, s38\n" \
    "s_cselect_b32 s38, s38, 0\n" \
    "s_cbranch_scc0 .Ldn_xb_%=\n" \
    "s_sleep 4\n" \
    "s_mul_i32 s39, s32, 98304\n" \
    "s_add_u32 s39, s39, s17\n" \
    "buffer_load_dwordx4 v[152:155], v144, s[8:11], s39 offen offset:0 sc1\n" \
    "buffer_load_dwordx4 v[156:159], v144, s[8:11], s39 offen offset:1024 sc1\n" \
    "buffer_load_dwordx4 v[160:163], v144, s[8:11], s39 offen offset:2048 sc1\n" \
    "buffer_load_dwordx4 v[164:167], v144, s[8:11], s39 offen offset:3072 sc1\n" \
    "s_add_u32 s39, s39, 4096\n" \
    "buffer_load_dwordx4 v[168:171], v144, s[8:11], s39 offen offset:0 sc1\n" \
    "buffer_load_dwordx4 v[172:175], v144, s[8:11], s39 offen offset:1024 sc1\n" \
    "s_mul_i32 s39, s32, 40960\n" \
    "s_add_u32 s39, s39, %[x1]+0\n" \
    "buffer_load_dwordx2 v[176:177], v147, s[8:11], s39 offen sc1\n" \
    "s_waitcnt vmcnt(0)\n" \
    "s_branch .Ldn_rb_%=\n" \
    ".Ldn_cc_%=:\n" \
    "s_add_u32 s37, s37, 1\n" \
    "s_cmp_lt_u32 s37, s38\n" \
    "s_cselect_b32 s38, s38, 0\n" \
    "s_cbranch_scc0 .Ldn_xc_%=\n" \
    "s_sleep 4\n" \
    "s_mul_i32 s39, s33, 98304\n" \
    "s_add_u32 s39, s39, s17\n" \
    "s_add_u32 s39, s39, 6291456\n" \
    "buffer_load_dwordx4 v[178:181], v144, s[8:11], s39 offen offset:0 sc1\n" \
    "buffer_load_dwordx4 v[182:185], v144, s[8:11], s39 offen offset:1024 sc1\n" \
    "buffer_load_dwordx4 v[186:189], v144, s[8:11], s39 offen offset:2048 sc1\n" \
    "buffer_load_dwordx4 v[190:193], v144, s[8:11], s39 offen offset:3072 sc1\n" \
    "s_add_u32 s39, s39, 4096\n" \
    "buffer_load_dwordx4 v[194:197], v144, s[8:11], s39 offen offset:0 sc1\n" \
    "buffer_load_dwordx4 v[198:201], v144, s[8:11], s39 offen offset:1024 sc1\n" \
    "s_mul_i32 s39, s33, 40960\n" \
    "s_add_u32 s39, s39, %[x1]+2621440\n" \
    "buffer_load_dwordx2 v[202:203], v147, s[8:11], s39 offen sc1\n" \
    "s_waitcnt vmcnt(0)\n" \
    "s_branch .Ldn_rc_%=\n" \
    ".Ldn_cd_%=:\n" \
    "s_add_u32 s37, s37, 1\n" \
    "s_cmp_lt_u32 s37, s38\n" \
    "s_cselect_b32 s38, s38, 0\n" \
    "s_cbranch_scc0 .Ldn_xd_%=\n" \
    "s_sleep 4\n" \
    "s_mul_i32 s39, s33, 98304\n" \
    "s_add_u32 s39, s39, s17\n" \
    "s_add_u32 s39, s39, 6291456\n" \
    "buffer_load_dwordx4 v[178:181], v144, s[8:11], s39 offen offset:0 sc1\n" \
    "buffer_load_dwordx4 v[182:185], v144, s[8:11], s39 offen offset:1024 sc1\n" \
    "buffer_load_dwordx4 v[186:189], v144, s[8:11], s39 offen offset:2048 sc1\n" \
    "buffer_load_dwordx4 v[190:193], v144, s[8:11], s39 offen offset:3072 sc1\n" \
    "s_add_u32 s39, s39, 4096\n" \
    "buffer_load_dwordx4 v[194:197], v144, s[8:11], s39 offen offset:0 sc1\n" \
    "buffer_load_dwordx4 v[198:201], v144, s[8:11], s39 offen offset:1024 sc1\n" \
    "s_mul_i32 s39, s33, 40960\n" \
    "s_add_u32 s39, s39, %[x1]+2621440\n" \
    "buffer_load_dwordx2 v[202:203], v147, s[8:11], s39 offen sc1\n" \
    "s_waitcnt vmcnt(0)\n" \
    "s_branch .Ldn_rd_%=\n" \
    ""
#define QK_WL 65536
template <int WL, int SL2>
__device__ __forceinline__ void qk_weights(const u16* wa, const u16* wb, char* smem, const int w, const int l, bf16x16 (&wt)[3][8]) {
    const rsrc_t rq_ = __builtin_amdgcn_make_buffer_rsrc((void*)(w < 4 ? wa : wb), 0, 639 * 12288 + 384, 0x00020000);
#define QK_SLOT(r_) ((r_) % 3 == 0 ? SL2 : SL2 ? ((r_) % 3 - 1) * 49152 : ((r_) % 3) * 49152)
#define QK_ISSUE(r_) { if ((r_) < 10) { _Pragma("unroll") for (int i_ = 0; i_ < 6; ++i_) { \
        const unsigned ix_ = (unsigned)(((w & 3) * 6 + i_) * 64 + l), rw_ = ix_ / 24, cc_ = (ix_ % 24) ^ ((rw_ & 3) << 1); \
        __builtin_amdgcn_raw_ptr_buffer_load_lds(rq_, (lds_ptr_t)(smem + QK_SLOT(r_) + (w * 6 + i_) * 1024), 16, (int)(rw_ * 12288 + cc_ * 16), (r_) * 64 * 12288, 0, WAUX); } } }
    const unsigned c01 = (unsigned)((w & 3) * 96 + (l & 3) * 8);
    bf16x8 hold[6];
    char* wl = smem + WL + w * 12288 + l * 16;
    QK_ISSUE(0) QK_ISSUE(1)
#pragma unroll
    for (int r = 0; r < 10; ++r) {
        if (r + 1 < 10) WAITVN(6) else WAITV0();
        RAWBAR()
        QK_ISSUE(r + 2)
        const char* sb = smem + QK_SLOT(r) + (w >> 2) * 24576;
        if (r == 9) {
#pragma unroll
            for (int i = 0; i < 6; ++i) *(bf16x8*)(wl + i * 1024) = hold[i];
        }
#pragma unroll
        for (int i = 0; i < 3; ++i) {
            const bf16x8 f0 = tr_frag<24, false>(sb, 0, c01 + i * 32, l), f1 = tr_frag<24, false>(sb, 32, c01 + i * 32, l);
            if (r < 8) wt[i][r & 7] = cat16(f0, f1);
            else if (r == 8) { hold[i] = f0; hold[3 + i] = f1; }
            else { *(bf16x8*)(wl + (6 + i) * 1024) = f0; *(bf16x8*)(wl + (9 + i) * 1024) = f1; }
        }
    }
    RAWBAR()
}
#define WLT(i) cat16(LDS(bf16x8, wl + (((i) / 3) * 6 + (i) % 3) * 1024), LDS(bf16x8, wl + (((i) / 3) * 6 + 3 + (i) % 3) * 1024))
template <typename S, typename F, typename M>
__device__ __forceinline__ void projm(const bf16x16 (&wt)[3][8], const unsigned mf, const unsigned wl, int ix, f32x4& a0, f32x4& a1, f32x4& a2, S sync, F side, M mid) {
    bf16x8 A[8], AX[2];
    bf16x16 W[6];
    MEMB();
    W[0] = WLT(0); W[1] = WLT(1);
    a0 = Z4; a1 = Z4; a2 = Z4;
    asm volatile("" : "+v"(a0), "+v"(a1), "+v"(a2));
    SB0();
    sync();
    A[0] = LDS(bf16x8, mf); A[1] = LDS(bf16x8, mf + 1024); AX[0] = LDS(bf16x8, mf + 8192); A[2] = LDS(bf16x8, mf + 2048);
    SB0();
    side();
    SB0();
    bf16x8 Wh[6][2];
#define WH(i, h_) Wh[i][h_] = LDS(bf16x8, wl + (((i) / 3) * 6 + (h_) * 3 + (i) % 3) * 1024);
#define PMS(d, x, y) { d = SMFMA(x, y, d, ix); asm volatile("" : "+v"(d)); SB0(); }
    static_for<0, 8>([&](auto ic) __attribute__((always_inline)) {
        constexpr int i = decltype(ic)::v;
        constexpr int q = i == 2 || i == 3 ? i : i == 5 || i == 6 ? i - 1 : 0;
        mid(ic);
        if constexpr (i == 7) { PMS(a0, A[i], wt[0][i]) PMS(a1, AX[1], cat16(Wh[4][0], Wh[4][1])) PMS(a1, A[i], wt[1][i]) PMS(a2, A[i], wt[2][i]) PMS(a2, AX[1], cat16(Wh[5][0], Wh[5][1])) }
        else {
        PMS(a0, A[i], wt[0][i])
        if constexpr (i + 3 < 8) A[i + 3] = LDS(bf16x8, mf + (i + 3) * 1024);
        if (i >= 5 && q) WH(q, 0)
        SB0();
        PMS(a1, A[i], wt[1][i])
        if (i == 1) AX[1] = LDS(bf16x8, mf + 9216);
        if (q) WH(q, i >= 5)
        SB0();
        PMS(a2, A[i], wt[2][i])
        if (i < 5 && q) WH(q, 1)
        SB0();
        if (i == 1) PMS(a0, AX[0], W[0])
        if (i == 2) PMS(a1, AX[0], W[1])
        if (i == 4) PMS(a2, AX[0], cat16(Wh[2][0], Wh[2][1]))
        if (i == 5) PMS(a0, AX[1], cat16(Wh[3][0], Wh[3][1]))
        }
    });
}
#define NOF [&]() __attribute__((always_inline)) {}
#define NOM [&](auto) __attribute__((always_inline)) {}
#define BAREBAR() { asm volatile("s_barrier" ::: "memory"); SB0(); }
__device__ __forceinline__ void wait_fine(unsigned t) {
    asm volatile("s_branch 2f\n1:\n\ts_sleep 1\n2:\n\ts_memrealtime vcc\n\ts_waitcnt lgkmcnt(0)\n\ts_sub_i32 vcc_lo, %0, vcc_lo\n\ts_cmp_gt_i32 vcc_lo, 8\n\ts_cbranch_scc1 1b\n\t"
                 "s_cmp_gt_i32 vcc_lo, 0\n\ts_cbranch_scc1 2b" :: "s"(t) : "memory", "scc", "vcc");
}
__device__ __forceinline__ void dma6(u32x4 rd, unsigned dst, unsigned voff, unsigned soff) {
    unsigned d2, s2;
    asm volatile("s_mov_b32 m0, %2\n\ts_add_u32 %0, %2, 0x1000\n\tbuffer_load_dwordx4 %3, %4, %5 offen sc1 lds\n\tbuffer_load_dwordx4 %3, %4, %5 offen offset:1024 sc1 lds\n\t"
                 "buffer_load_dwordx4 %3, %4, %5 offen offset:2048 sc1 lds\n\tbuffer_load_dwordx4 %3, %4, %5 offen offset:3072 sc1 lds\n\t"
                 "s_mov_b32 m0, %0\n\ts_add_u32 %1, %5, 0x1000\n\tbuffer_load_dwordx4 %3, %4, %1 offen sc1 lds\n\tbuffer_load_dwordx4 %3, %4, %1 offen offset:1024 sc1 lds"
                 : "=&s"(d2), "=&s"(s2) : "s"(dst), "v"(voff), "s"(rd), "s"(soff) : "memory", "scc");
}
__device__ __forceinline__ void st64(rsrc_t rs, unsigned voff, unsigned soff, u32x2 v) { __builtin_amdgcn_raw_buffer_store_b64(v, rs, (int)voff, (int)soff, AUX_SC1); }

#define QM_MF 0
#define QM_QM 10240
#define QM_KM 13312
#define QM_WL 16384
#define QM_QK 114688
#define KWIN 5
#define AWIN 4
#define BQP 2
template <int D> __device__ __forceinline__ int ev_id(int e) {
    const int d = e - D, id = (d >> 1) + ((d & 1) ? 64 : D);
    return (unsigned)d > (unsigned)(127 - 2 * D) ? e : id;
}
__device__ __forceinline__ unsigned fold1(f32x4 a) { return pack2(a[0] + a[2], a[1] + a[3]); }
typedef __attribute__((ext_vector_type(32))) unsigned u32x32;
typedef __attribute__((ext_vector_type(16))) unsigned u32x16;
typedef __attribute__((ext_vector_type(8))) unsigned u32x8;
__device__ __forceinline__ u32x32 cat4(bf16x16 a, bf16x16 b, bf16x16 c, bf16x16 d) {
    const u32x8 A = __builtin_bit_cast(u32x8, a), B = __builtin_bit_cast(u32x8, b), C = __builtin_bit_cast(u32x8, c), D = __builtin_bit_cast(u32x8, d);
    const u32x16 x = __builtin_shufflevector(A, B, 0, 1, 2, 3, 4, 5, 6, 7, 8, 9, 10, 11, 12, 13, 14, 15), y = __builtin_shufflevector(C, D, 0, 1, 2, 3, 4, 5, 6, 7, 8, 9, 10, 11, 12, 13, 14, 15);
    return __builtin_shufflevector(x, y, 0, 1, 2, 3, 4, 5, 6, 7, 8, 9, 10, 11, 12, 13, 14, 15, 16, 17, 18, 19, 20, 21, 22, 23, 24, 25, 26, 27, 28, 29, 30, 31);
}
#define WIN(t) "{v[0:31]}"(cat4(t[0], t[1], t[2], t[3])), "{v[32:63]}"(cat4(t[4], t[5], t[6], t[7])), "{v[64:95]}"(cat4(t[8], t[9], t[10], t[11])), \
               "{v[96:127]}"(cat4(t[12], t[13], t[14], t[15])), "{v[128:159]}"(cat4(t[16], t[17], t[18], t[19])), "{v[160:191]}"(cat4(t[20], t[21], t[22], t[23]))
#define UNI(x) ((unsigned)__builtin_amdgcn_readfirstlane((unsigned)(x)))
#define ACLOB "memory", "vcc", "scc", "v255", "s99"
__device__ __forceinline__ void qkm_role(const Params& p, char* smem, const int w, const int h) {
    bf16x16 wt[3][8];
    qk_weights<QM_WL, QM_QK>(p.Wq + h * 192, p.Wk + h * 192, smem, w, lane_id(), wt);
    SU(3)
    const rsrc_t rs = __builtin_amdgcn_make_buffer_rsrc((void*)p.ws, 0, 0x7fffffff, 0x00020000);
    const unsigned hm = hot_page(p, rs, 2), hp = hm ^ RFX;
    RAWRD()
    const unsigned lo = lane_id() * 16;
    const unsigned dr = QM_QM + (w >> 2) * 3072 + (lo >> 8) * 4;
    unsigned po1, po2 = 0xC0000000u;
    const unsigned g8 = (lo >> 5) & 8;
    if (w == 0) { po1 = ((lo >> 1) & 0x100) + (lo & 0x60) * 2 + ((lo & 0x80) >> 2) + (lo & 0x10) + g8 + (h & 3) * 512; if (!(lo & 0x280)) po2 = (lo & 0x70) + g8 + 2048 + (h & 3) * 128; }
    else if (w == 1) { po1 = (lo & 0x2f0) + g8 + (h & 2) * 512 + (h & 1) * 256; if (lo < 512) po2 = (lo & 0xf0) + g8 + 2048 + (h & 3) * 256; }
    else if (w == 2) { po1 = lo + 3072 + (h & 3) * 1024; if (lo < 512) po2 = (lo & 0xf0) + g8 + 7168 + (h & 3) * 256; }
    else if (w == 4) po1 = lo < 512 ? lo + (h & 3) * 512 : lo < 640 ? lo + 1536 + (h & 3) * 128 : 0xC0000000u;
    else if (w == 5) { po1 = (lo & 256) ? 0xC0000000u : lo + (h & 2) * 512 + (h & 1) * 256; if (lo < 256) po2 = lo + 2048 + (h & 3) * 256; }
    else if (w == 6) { po1 = lo + 3072 + (h & 3) * 1024; if (lo < 256) po2 = lo + 7168 + (h & 3) * 256; }
    else po1 = lo < 128 ? lo : 0xC0000000u;
    const bool pm = !(w & 3);
    const unsigned pbs = pm ? hp + HB_PM + (h >> 3) * 16384 + ((h >> 2) & 1) * 2560 : hp + HB_PT + (h >> 2) * 16384;
    const int psh = pm ? 16 : 17;
    if (w >= 4 && w < 7) {
        const u32x4 ps = poison4();
#pragma unroll
        for (int i = 0; i < 4; ++i) { const unsigned b_ = pbs + ((unsigned)((i & 1) + (i >> 1) * 4) << psh); st128(rs, po1, b_, ps); st128(rs, po2, b_, ps); }
    }
    const u32x8 lc = {lo, 0x4444 + ((lo >> 5) & 1) * 0xAAAA, dr + (w & 3) * 768 + (lo & 0xf0), dr + (w & 3) * 768 + (lo & 0xf0) + 512,
                      w < 4 ? troff(lo) : QM_WL + w * 12288 + lo, po1, po2, lo ? 0xC0000000u : h * 4u};
    const int mk = (w >> 1) & 1, p0 = h * 12 + (w & 1) * 6, sw = (p0 & 7) == 6;
    const u32x16 sc = {UNI(w), UNI(pbs), UNI(psh), UNI(hm + HB_MEM + ((w - 3) >> 2) * 16384 + ((w - 3) & 3) * 2048), UNI(OFF_QK + mk * 786432u + blk(p0 + sw * 2)),
                       UNI(lb + QM_QK + (w & 3) * 6144 + sw * 2048), UNI(FL_K + h * 256u + mk * 96), UNI(hp + HB_TS + h * 128),
                       UNI(OFF_QK + mk * 786432u + blk(p0 + (sw ? 0 : 4))), UNI(sw ? -2048 : 4096), UNI(near_sel(hm) ^ (mk ? KSX : QSX)), 0u, 0u, 0u, 0u, 0u};
    const bf16x16* t = &wt[0][0];
    asm volatile("s_nop 4\n" QKM_ASM :: WIN(t), "{v[192:199]}"(lc), "{s[16:31]}"(sc), "{s[8:11]}"(rd), [lag] "n"(LAG), [dq] "n"(DQ), [dq3] "n"(DQ3), [rmask] "n"(RMASK) : ACLOB);
}

__device__ __forceinline__ void g_role(const Params& p, char* smem, const int w, const int j, const int a0, const int a1, const int b0) {
    const int l = lane_id(), m = j / 24, c0 = (j - m * 24) * 256;
    const rsrc_t rw = __builtin_amdgcn_make_buffer_rsrc((void*)((m == 0 ? p.Wq : m == 1 ? p.Wk : p.Wv) + c0), 0, 639 * 12288 + 512, 0x00020000);
    bf16x8 wt[2][20];
#define G_ISSUE(r_) { if ((r_) < 10) { _Pragma("unroll") for (int i_ = 0; i_ < 4; ++i_) { \
        const unsigned ix_ = (unsigned)((w * 4 + i_) * 64 + l), rw_ = ix_ >> 5, cc_ = (ix_ & 31) ^ ((rw_ & 3) << 1); \
        __builtin_amdgcn_raw_ptr_buffer_load_lds(rw, (lds_ptr_t)(smem + ((r_) & 3) * 32768 + (w * 4 + i_) * 1024), 16, (int)(rw_ * 12288 + cc_ * 16), (r_) * 64 * 12288, 0, WAUX); } } }
    G_ISSUE(0) G_ISSUE(1) G_ISSUE(2)
#pragma unroll
    for (int r = 0; r < 10; ++r) {
        if (r < 8) WAITVN(8) else if (r == 8) WAITVN(4) else WAITV0();
        RAWBAR()
        G_ISSUE(r + 3)
#pragma unroll
        for (int kk = 0; kk < 2; ++kk)
#pragma unroll
            for (int i = 0; i < 2; ++i) wt[i][r * 2 + kk] = tr_frag<32, false>(smem + (r & 3) * 32768, kk * 32, (unsigned)(w * 64 + i * 32 + (l & 3) * 8), l);
    }
    RAWBAR()
    if (w == 0) *(unsigned*)(smem + 81920 + (l & 1) * 4) = 0u;
    RAWBAR()
    SU(3)
    RAWRD()
    const unsigned lo = lane_id() * 16;
    const int pc = (j - m * 24) * 8 + w, h = pc / 6, i = pc - h * 6;
    const unsigned ns = near_sel(hot_page(p, __builtin_amdgcn_make_buffer_rsrc((void*)p.ws, 0, 0x7fffffff, 0x00020000), 2));
    const u32x4 lc = {lo, lo < (m == 2 ? 64 : 16) ? lo : 0xC0000000u, FL_N + ((lo << 2) & 960), j == 30 ? lo : 0xC0000000u};
    const u32x16 sc = {UNI(w), UNI(OFF_QK + (m & 1) * 786432u + blk(h * 12 + i)), UNI(OFF_QK + (m & 1) * 786432u + blk(h * 12 + 6 + i)), UNI(ns ^ (m == 1 ? KSX : m ? QSX ^ 0x2000 : QSX)),
                      UNI(m == 2 ? FL_V + h * 512u + i * 64 : FL_K + h * 256u + m * 96 + i * 16), UNI(m == 2 ? 16384 : 8192), UNI(OFF_XN + (w & 3) * 5120), UNI(lb + (w & 3) * 5120),
                       UNI(a0), UNI(a1), UNI(a1 + 128), UNI(b0), 0u, 0u, 0u, 0u};
    bf16x16 t[20];
#pragma unroll
    for (int k = 0; k < 20; ++k) t[k] = cat16(wt[k / 10][(k % 10) * 2], wt[k / 10][(k % 10) * 2 + 1]);
    asm volatile("s_nop 4\n" G_ASM :: "{v[0:31]}"(cat4(t[0], t[1], t[2], t[3])), "{v[32:63]}"(cat4(t[4], t[5], t[6], t[7])), "{v[64:95]}"(cat4(t[8], t[9], t[10], t[11])),
                 "{v[96:127]}"(cat4(t[12], t[13], t[14], t[15])), "{v[128:159]}"(cat4(t[16], t[17], t[18], t[19])), "{v[160:163]}"(lc), "{s[16:31]}"(sc), "{s[8:11]}"(rd), [rmask] "n"(RMASK)
                 : "memory", "vcc", "scc", "m0", "v164", "v165", "v166", "v167", "v168", "v169", "v170", "v171", "v172", "v173", "v174", "v175", "v176", "v177", "v178", "v179", "v180", "v181", "v182", "v183", "v184", "v185", "v186", "v187", "v188", "v189", "v190", "v191", "v192", "v193", "v194", "v195", "v196", "v197", "v198", "v199", "v200", "v201", "v202", "v203", "v204", "v205", "v206", "v207", "v208", "v209", "v210", "v211", "v212", "v213", "v214", "v215", "s32", "s33", "s34", "s35", "s36", "s37", "s38", "s39", "s40", "s41", "s42", "s43", "s44", "s45", "s46", "s47", "s48", "s70", "s71", "s72", "s73", "s74", "s75", "s76", "s77", "s78", "s79", "s80", "s81", "s82", "s83", "s84", "s85", "s86", "s87", "s88", "s89", "s90", "s91");
    RAWBAR()
}

#define UA_VM 10240
#define UA_Z 13312
#define VO_WL 65536
__device__ __forceinline__ void ua_role(const Params& p, char* smem, const int w, const int h, const int far) {
    const rsrc_t rs = __builtin_amdgcn_make_buffer_rsrc((void*)p.ws, 0, 0x7fffffff, 0x00020000);
    const rsrc_t ro_ = __builtin_amdgcn_make_buffer_rsrc((void*)p.Wo, 0, 6144 * 640 * 2, 0x00020000);
    const rsrc_t rv_ = __builtin_amdgcn_make_buffer_rsrc((void*)p.Wv, 0, 640 * 6144 * 2, 0x00020000);
#define VO_ISSUE(r_) { \
        if ((r_) < 6) { _Pragma("unroll") for (int i_ = 0; i_ < 5; ++i_) { \
            const unsigned ix_ = (unsigned)((w * 5 + i_) * 64 + l), rw_ = ix_ / 80, cc_ = (ix_ % 80) ^ ((rw_ & 3) << 1); \
            __builtin_amdgcn_raw_ptr_buffer_load_lds(ro_, (lds_ptr_t)(smem + ((r_) % 3) * 49152 + (w * 5 + i_) * 1024), 16, (int)(rw_ * 1280 + cc_ * 16), (h * 192 + (r_) * 32) * 1280, 0, 0); } } \
        else if ((r_) < 11) { _Pragma("unroll") for (int i_ = 0; i_ < 6; ++i_) { \
            const unsigned ix_ = (unsigned)((w * 6 + i_) * 64 + l), rw_ = ix_ / 24, cc_ = (ix_ % 24) ^ ((rw_ & 3) << 1); \
            __builtin_amdgcn_raw_ptr_buffer_load_lds(rv_, (lds_ptr_t)(smem + ((r_) % 3) * 49152 + (w * 6 + i_) * 1024), 16, (int)(rw_ * 12288 + cc_ * 16), ((r_) - 6) * 128 * 12288 + h * 384, 0, 0); } } }
#define VO_STAGE(BODY_O, BODY_V) { \
        VO_ISSUE(0) VO_ISSUE(1) \
        _Pragma("unroll") for (int rr = 0; rr < 11; ++rr) { \
            if (rr + 1 < 6) WAITVN(5) else if (rr + 1 < 11) WAITVN(6) else WAITV0(); \
            RAWBAR() \
            VO_ISSUE(rr + 2) \
            const char* sb = smem + (rr % 3) * 49152; \
            if (rr < 6) { const int r = rr; BODY_O } else { const int r = rr - 6; BODY_V } \
        } \
        RAWBAR() }
    const int wq = w ^ 4;
    if (w < 4) {
        const int l = lane_id(), tid = w * 64 + l;
        const unsigned lo_ = l * 16;
        bf16x16 wt[3][8];
        const unsigned c01 = (unsigned)(w * 96 + (l & 3) * 8);
        VO_STAGE({ (void)r; (void)sb; }, {
            _Pragma("unroll") for (int kk = 0; kk < 2; ++kk)
                _Pragma("unroll") for (int i = 0; i < 3; ++i) {
                    const bf16x8 f0 = (tr_frag<24, false>(sb, kk * 64, c01 + i * 32, l));
                    const bf16x8 f1 = (tr_frag<24, false>(sb, kk * 64 + 32, c01 + i * 32, l));
                    if (r < 4) wt[i][(r * 2 + kk) & 7] = cat16(f0, f1);
                    else { *(bf16x8*)(smem + VO_WL + wq * 12288 + (kk * 6 + i) * 1024 + lo_) = f0; *(bf16x8*)(smem + VO_WL + wq * 12288 + (kk * 6 + 3 + i) * 1024 + lo_) = f1; }
                }
        })
        *(unsigned*)(smem + UA_Z + tid * 4) = 0u;
        SU(3)
        const unsigned hm = hot_page(p, rs, 2);
        RAWRD()
        RAWBAR()
        const unsigned lo = lane_id() * 16;
        const unsigned dr = UA_VM + (lo & 0x10) * 2 + (lo >> 8) * 8 + ((lo >> 3) & 4);
        const u32x8 lc = {lo, 0x4444 + ((lo >> 5) & 1) * 0xAAAA, dr + w * 768 + (lo & 0xc0), dr + w * 768 + (lo & 0xc0) + 512,
                          VO_WL + wq * 12288 + lo, lo + w * 2048, lo + 8192 + (w >> 1) * 1024, (lo & 0x1f0) * 8};
        const u32x8 sc = {UNI(w), UNI(hm + HB_MEM + w * 2048), UNI(hm + HB_MEM + 16384 + (w >> 1) * 1024), UNI((hm ^ RFX) + HB_TS), 0u, 0u, 0u, 0u};
        const bf16x16* t = &wt[0][0];
        asm volatile("s_nop 4\n" UAV_ASM :: WIN(t), "{v[192:199]}"(lc), "{s[16:23]}"(sc), "{s[8:11]}"(rd), [lag] "n"(LAG), [dv] "n"(DV) : ACLOB);
    } else {
        const int l = lane_id();
        bf16x16 wo[8][3];
        bf16x8 hold[12], ev[8];
        VO_STAGE({
            _Pragma("unroll") for (int i = 0; i < 10; ++i) {
                const int jt = wq * 10 + i;
                const int pp = l & 3;
                const unsigned cb = (unsigned)((jt >> 1) * 32 + (pp & 1) * 16 + 4 * (2 * (jt & 1) + (pp >> 1))) * 2;
                const bf16x8 f = (tr_frag<80, false>(sb, 0, cb, l));
                if (i >= 8) hold[(i - 8) * 6 + r] = f;
                else if (r & 1) wo[i & 7][r >> 1] = cat16(ev[i & 7], f);
                else ev[i & 7] = f;
            }
        }, { (void)r; (void)sb; })
#pragma unroll
        for (int i = 0; i < 12; ++i) *(bf16x8*)(smem + VO_WL + wq * 12288 + i * 1024 + lane_id() * 16) = hold[i];
        RAWRD()
        const unsigned hu0 = hot_page(p, rs, 0) ^ RFX, hu1 = hot_page(p, rs, 1) ^ RFX, hm = hot_page(p, rs, 2) ^ RFX;
        const unsigned lo = lane_id() * 16;
        const unsigned ul = (unsigned)(wq * 10 + (lo >> 8)) * 16384u + h * 256 + (lo & 0xf0);
        {
            const u32x4 ps = poison4();
#pragma unroll
            for (int i = 0; i < 4; ++i) { const unsigned b_ = ((i & 2) ? hu1 : hu0) + (i & 1) * 655360u; st128(rs, ul, b_, ps); st128(rs, ul, b_ + 65536, ps); st128(rs, ul, b_ + 98304, ps); }
        }
        WAITV0();
        RAWBAR()
        const u32x8 lc = {lo, 0x4444 + ((lo >> 5) & 1) * 0xAAAA, VO_WL + wq * 12288 + lo, UA_VM + (lo & 0x200) + (lo & 0x80) * 2 + ((lo & 0x100) >> 1) + ((lo & 0x70) >> 1),
                          (unsigned)(wq * 10 + ((lo >> 8) & 1)) * 16384u + h * 256 + (lo & 0xf0) + (lo >> 9) * 8, ul, (lo < 128 && w == 4) || (lo < 512 && h == 0 && w == 5) ? lo : 0xC0000000u, lo || w != 5 ? 0xC0000000u : h * 4u};
        const bool ck = h == 0 && w == 5;
        const u32x8 sc = {UNI(w), UNI(hu0), UNI(hu1), UNI(ck ? FL_T : hm + HB_TS + 4096 + h * 128), UNI(ck ? 9 : 14), 0u, 0u, 0u};
        const bf16x16* t = &wo[0][0];
        asm volatile("s_nop 4\n" UAO_ASM :: WIN(t), "{v[192:199]}"(lc), "{s[16:23]}"(sc), "{s[8:11]}"(rd), [lag] "n"(LAG), [brn] "n"(BRN) : ACLOB);
    }
}

__device__ __forceinline__ void ob_role(const Params& p, char* smem, const int w, const int h) {
    const int l = lane_id();
    const rsrc_t rs = __builtin_amdgcn_make_buffer_rsrc((void*)p.ws, 0, 0x7fffffff, 0x00020000);
    const rsrc_t ro_ = __builtin_amdgcn_make_buffer_rsrc((void*)p.Wo, 0, 6144 * 640 * 2, 0x00020000);
    bf16x8 wo[5][6];
#define OB_ISSUE(r_) { if ((r_) < 6) { _Pragma("unroll") for (int i_ = 0; i_ < 5; ++i_) { \
            const unsigned ix_ = (unsigned)((w * 5 + i_) * 64 + l), rw_ = ix_ / 80, cc_ = (ix_ % 80) ^ ((rw_ & 3) << 1); \
            __builtin_amdgcn_raw_ptr_buffer_load_lds(ro_, (lds_ptr_t)(smem + ((r_) % 3) * 49152 + (w * 5 + i_) * 1024), 16, (int)(rw_ * 1280 + cc_ * 16), (h * 192 + (r_) * 32) * 1280, 0, 0); } } }
    OB_ISSUE(0) OB_ISSUE(1)
#pragma unroll
    for (int r = 0; r < 6; ++r) {
        if (r + 1 < 6) WAITVN(5) else WAITV0();
        RAWBAR()
        OB_ISSUE(r + 2)
        const char* sb = smem + (r % 3) * 49152;
#pragma unroll
        for (int i = 0; i < 5; ++i) {
            const int jt = w * 5 + i, pp = l & 3;
            const unsigned cb = (unsigned)((jt >> 1) * 32 + (pp & 1) * 16 + 4 * (2 * (jt & 1) + (pp >> 1))) * 2;
            wo[i][r] = tr_frag<80, false>(sb, 0, cb, l);
        }
    }
    RAWBAR()
    if (w == 0) *(unsigned*)(smem + 24576 + (l & 3) * 4) = 0u;
    RAWBAR()
    SU(3)
    RAWRD()
    const unsigned lo = lane_id() * 16;
    const unsigned ns = near_sel(hot_page(p, rs, 2));
    const u32x4 lc = {lo, lo < 64 ? lo : 0xC0000000u, FL_V + h * 512u + (lo & 128) * 128 + min(lo & 112, 80u) * 4, 0u};
    const u32x8 sc = {UNI(w), UNI(OFF_BT + (unsigned)(w * 5) * 65536u + (h >> 3) * 16384 + (h & 7) * 1024), UNI(FL_B + (unsigned)(w * 32 + h) * 64u), UNI(w == 7 ? 0x80000000u : OFF_QK + blk(h * 12 + (w & 3) * 4)), UNI(lb + (w == 7 ? 32768 : (w & 3) * 4096)),
                      UNI(ns ^ (h < BNH ? 0x2000 : 0)), UNI(ns ^ QSX ^ 0x2000), 0u};
    bf16x16 t[15];
#pragma unroll
    for (int k = 0; k < 15; ++k) t[k] = cat16(wo[k / 3][(k % 3) * 2], wo[k / 3][(k % 3) * 2 + 1]);
    asm volatile("s_nop 4\n" OB_ASM :: "{v[0:31]}"(cat4(t[0], t[1], t[2], t[3])), "{v[32:63]}"(cat4(t[4], t[5], t[6], t[7])), "{v[64:95]}"(cat4(t[8], t[9], t[10], t[11])),
                 "{v[96:103]}"(t[12]), "{v[104:111]}"(t[13]), "{v[112:119]}"(t[14]), "{v[120:123]}"(lc), "{s[16:23]}"(sc), "{s[8:11]}"(rd), [brn] "n"(BRN), [rmask] "n"(RMASK) : ACLOB);
}

__device__ __forceinline__ void r_role(const Params& p, char* smem, const int w, const int c, const int r) {
    const rsrc_t rs = __builtin_amdgcn_make_buffer_rsrc((void*)p.ws, 0, 0x7fffffff, 0x00020000);
    const unsigned hu = hot_page(p, rs, c) ^ RFX, hm = hot_page(p, rs, 2), hp = hm ^ RFX;
    RAWRD()
    const unsigned lo = lane_id() * 16;
    const unsigned mb = hm + HB_MEM + (unsigned)(c * 8 + (r >> 4)) * 16384u + (r & 15) * 512;
    if (w == 0) {
        const int l = lo >> 4;
        const unsigned* src = (const unsigned*)(p.mem0 + (((l >> 1) & 6) + (l & 1)) * 640 + r * 32 + ((l >> 4) & 1) * 8 + (l & 2));
        st128(rs, lo < 512 ? lo : 0xC0000000u, mb, (u32x4){nop(src[0]), nop(src[8]), nop(src[2]), nop(src[10])});
    }
    SU(3)
    const unsigned pn = PL == 4 ? 43 : PL >= 5 ? 52 : 13, pl = ((PL == 4 ? r / 3 : PL >= 5 ? r >> 2 : r) * 2 + (w >> 2)) * pn + (lo >> 4);
    const unsigned xl = (unsigned)(w & 1) * 20480u + r * 1024 + lo;
    const u32x16 lc = {lo, mb + (lo & 0x1f0) + (lo >> 9) * 8,
                       lo < 128 ? FL_B + (unsigned)(c * 64) * 16384u + ((lo & 64) ? (2 * r + 1) / 5 : (2 * r) / 5) * 2048 + w * 256 + (lo & 48) * 4 : 0xC0000000u,
                       lo < 128 ? hp + HB_TS + (unsigned)c * 32768u + w * 512 + (lo & 64) * 64 + (lo & 48) * 8 : 0xC0000000u,
                       (lo & 0x20) ? 0xC0000000u : ((lo >> 1) & 0x1e0) + (lo & 0x10) + 2048,
                       w * 1024 + (lo & 0x80) * 4 + (lo >> 8) * 64 + (lo & 0x20) + ((lo & 0x40) >> 3) + ((lo & 0x10) >> 2),
                       (w >> 1) == 1 ? xl : 0xC0000000u, 8192 + (w & 1) * 2048 + (lo >> 8) * 512 + (lo & 0xf0), 8192 + w * 4096 + lo,
                       w == 5 ? (lo < 512 ? mb + lo : 0xC0000000u) : w >= 6 ? xl + OFF_X1 + (c * 64 + PDIST) * 40960u : w == 1 && lo < 128 ? hp + HB_TM + c * 32768 + r * 128 + lo : 0xC0000000u,
                       0x4444 + ((lo >> 5) & 1) * 0xAAAA, lo || w != 1 ? 0xC0000000u : r * 4u, r == 5 ? lo : 0xC0000000u,
                       pl < 512 && lo < pn * 16 ? hp + HB_PT + (c * 32 + (pl >> 6)) * 16384 + (pl & 63) * 128 : 0xC0000000u, lo < 128 ? lo >> 2 : 0xC0000000u, 0u};
    const int dru = DRU - DRL * ((0x102ac >> (4 * (r % 5))) & 15);
    const u32x16 sc = {UNI(w), UNI(hp + HB_PM + (unsigned)(c * 16 + (w >> 1)) * 16384u + (w & 1) * 2560), UNI(hu + (unsigned)(2 * r) * 16384u + w * 1024),
                       UNI(OFF_BT + (unsigned)(c * BRN) * 2621440u + r * 131072u + (w >> 1) * 16384 + (w & 1) * 4096), UNI(hp + HB_PT + (unsigned)(c * 32 + w) * 16384u),
                       UNI(c * 64 * 40960), UNI(dru), UNI(w == 5 ? 3 : 63), UNI(w == 5 ? 32768 : 40960), UNI(c * 64), UNI(r * 8 + w), UNI(near_sel(hm) ^ (w < BNH / 4 ? 0x2000 : 0)), UNI(hp + HB_TS + c * 32768 + w * 512), 0u, 0u, 0u};
    asm volatile("s_nop 4\n" R_ASM :: "{v[0:15]}"(lc), "{s[16:31]}"(sc), "{s[8:11]}"(rd), [brn] "n"(BRN), [dr] "n"(DR), [dh] "n"(DH), [x0] "n"(OFF_X0), [x1] "n"(OFF_X1), [pdist] "n"(PDIST) : ACLOB);
}

#undef RB
#define RB 5
__device__ __forceinline__ void up_role(const Params& p, char* smem, const int w, const int j) {
    const int l = lane_id(), tid = w * 64 + l;
    const rsrc_t rs = __builtin_amdgcn_make_buffer_rsrc((void*)p.ws, 0, 0x7fffffff, 0x00020000);
    const unsigned lo_ = l * 16;
    const int fg = j >> 1, mt = j & 1;
    wait_until(now32() + GLD);
    bf16x8 wt[2][20];
    {
        auto src = [&](int rnd) { return [&p, fg, rnd](unsigned r, unsigned c) { return (c < 16 ? p.wg : p.wu) + (size_t)(rnd * 64 + r) * 1536 + fg * 128 + (c & 15) * 8; }; };
        const unsigned cb = (unsigned)((w >> 1) * 32 + (l & 1) * 16 + 4 * (2 * (w & 1) + ((l & 3) >> 1))) * 2;
        stage_issue<32, 64>(smem + 49152, w, l, src(0));
#pragma unroll
        for (int r = 0; r < 10; ++r) {
            if (r + 1 < 10) { stage_issue<32, 64>(smem + 49152 + ((r + 1) & 1) * 32768, w, l, src(r + 1)); WAITVN(4) } else WAITV0();
            RAWBAR()
#pragma unroll
            for (int kk = 0; kk < 2; ++kk)
#pragma unroll
                for (int i = 0; i < 2; ++i) wt[i][r * 2 + kk] = tr_frag<32, false>(smem + 49152 + (r & 1) * 32768, kk * 32, cb + i * 256, l);
            RAWBAR()
        }
    }
    f32x4 cs0 = Z4, cs1 = Z4;
#pragma unroll
    for (int ks = 0; ks < 20; ++ks) { cs0 = MFMA(wt[0][ks], ONES8, cs0); cs1 = MFMA(wt[1][ks], ONES8, cs1); }
    RAWRD()
    const unsigned lo = lane_id() * 16;
    const u32x8 lc = {lo, lo + w * 1024, j == 5 ? lo : 0xC0000000u, (lo >> 8) == ((lo >> 6) & 3) ? 40960 + w * 128 + ((lo >> 1) & 0x78) : 43008 + (lo >> 1), 40960 + (lo & 0x300) + ((lo >> 1) & 0x78),
                      OFF_HG + (unsigned)(mt * 48 + fg * 4 + (w >> 1)) * 1024u + (w & 1) * 512 + ((lo & 512) >> 1) + (lo & 0xf0) + ((lo >> 5) & 8), lo || w != 7 ? 0xC0000000u : j * 4u, 0u};
    const u32x4 sc = {UNI(w), UNI(OFF_X1 + mt * 20480 + w * 1024), UNI(FL_T + 128), 0u};
    bf16x16 t[20];
#pragma unroll
    for (int k = 0; k < 20; ++k) t[k] = cat16(wt[k / 10][(k % 10) * 2], wt[k / 10][(k % 10) * 2 + 1]);
    asm volatile("s_nop 7\n" UP_ASM :: "{v[0:31]}"(cat4(t[0], t[1], t[2], t[3])), "{v[32:63]}"(cat4(t[4], t[5], t[6], t[7])), "{v[64:95]}"(cat4(t[8], t[9], t[10], t[11])),
                 "{v[96:127]}"(cat4(t[12], t[13], t[14], t[15])), "{v[128:159]}"(cat4(t[16], t[17], t[18], t[19])), "{v[160:163]}"(cs0), "{v[164:167]}"(cs1), "{v[168:175]}"(lc),
                 "{s[16:19]}"(sc), "{s[8:11]}"(rd), [dx] "n"(DX), [dlast] "n"(DLAST), [eup] "n"(EUP), [pdist] "n"(PDIST) : ACLOB);
}

#undef RB
#define RB 6
__device__ __forceinline__ void dn_role(const Params& p, char* smem, const int w, const int j) {
    const int l = lane_id(), tid = w * 64 + l;
    const rsrc_t rs = __builtin_amdgcn_make_buffer_rsrc((void*)p.ws, 0, 0x7fffffff, 0x00020000);
    const int cg = j >> 1, mt = j & 1, c0 = cg * 96;
    const unsigned tst = now32();
    if (j == 0) cal_role(p, w, 2);
    wait_until(tst + GLD);
    bf16x8 wt[6][6];
    {
        auto src = [&](int rnd) { return [&p, c0, rnd](unsigned r, unsigned c) { return p.wd + (size_t)((r >> 5) * 192 + rnd * 32 + (r & 31)) * 640 + min(c0 + (int)c * 8, 632); }; };
        stage_issue<16, 256>(smem, w, l, src(0));
#pragma unroll
        for (int r = 0; r < 6; ++r) {
            if (r + 1 < 6) { stage_issue<16, 256>(smem + ((r + 1) & 1) * 65536, w, l, src(r + 1)); WAITVN(8) } else WAITV0();
            RAWBAR()
#pragma unroll
            for (int i = 0; i < 6; ++i) {
                const int pp = l & 3;
                const unsigned cb = (unsigned)((i >> 1) * 32 + (pp & 1) * 16 + 4 * (2 * (i & 1) + (pp >> 1))) * 2;
                wt[i][r] = tr_frag<16, false>(smem + (r & 1) * 65536, w * 32, cb, l);
            }
            RAWBAR()
        }
    }
    RAWRD()
    const u32x4 ro = (u32x4){(unsigned)(size_t)p.out, (unsigned)((size_t)p.out >> 32) & 0xffffu, 2048u * 640 * 2, 0x00020000u};
    const unsigned lo = lane_id() * 16;
    const int pc = cg * 3 + (w >> 1);
    const bool fv = w < 6 && pc < 20;
    const u32x8 lc = {lo, w * 6144 + lo, w * 1024 + lo, fv ? mt * 20480 + pc * 1024 + (w & 1) * 512 + ((lo & 512) >> 1) + (lo & 0xf0) + ((lo >> 5) & 8) : 0xC0000000u,
                      fv ? (unsigned)(mt * 16 + ((lo >> 4) & 15)) * 1280u + pc * 64 + ((lo >> 3) & 32) + (w & 1) * 16 + ((lo >> 6) & 8) : 0xC0000000u,
                      lo || w ? 0xC0000000u : j * 4u, j == 5 ? lo : 0xC0000000u, 0u};
    const u32x4 sc = {UNI(w), UNI(OFF_HG + (unsigned)(mt * 48 + w * 6) * 1024u), UNI(FL_T + 256), 0u};
    bf16x16 t[18];
#pragma unroll
    for (int k = 0; k < 18; ++k) t[k] = cat16(wt[k / 3][(k % 3) * 2], wt[k / 3][(k % 3) * 2 + 1]);
    asm volatile("s_nop 4\n" DN_ASM :: "{v[0:31]}"(cat4(t[0], t[1], t[2], t[3])), "{v[32:63]}"(cat4(t[4], t[5], t[6], t[7])), "{v[64:95]}"(cat4(t[8], t[9], t[10], t[11])),
                 "{v[96:127]}"(cat4(t[12], t[13], t[14], t[15])), "{v[128:135]}"(t[16]), "{v[136:143]}"(t[17]), "{v[144:151]}"(lc), "{s[16:19]}"(sc), "{s[8:11]}"(rd), "{s[12:15]}"(ro),
                 [dxd] "n"(DXD), [dlast] "n"(DLAST), [eup] "n"(EUP), [pdist] "n"(PDIST), [x1] "n"(OFF_X1), [x2] "n"(OFF_X2) : ACLOB);
}

__device__ __forceinline__ void ln_role(const Params& p, char* smem, const int w_, const int mt) {
    const int tid = w_ * 64 + lane_id(), w = w_ & 3, od = w_ >> 2;
    const rsrc_t rs = __builtin_amdgcn_make_buffer_rsrc((void*)p.ws, 0, 0x7fffffff, 0x00020000);
    if (tid < 2) *(unsigned*)(smem + 2048 + tid * 4) = 0u;
    cal_role(p, w_, mt);
    RAWBAR()
    for (int s = od; s < NSEG; s += 2) {
        OPAQUE_LO()
        const int n = (lo >> 4) & 15, g = lo >> 8;
        u32x4 v[5];
        ck_wait(p, rs, s, 3, DXL);
        gather<5>(p, rs, lo, OFF_X2 + (unsigned)s * 40960u + mt * 20480 + w * 5120, v, true);
        f32x4 sm = Z4, sq = Z4;
#pragma unroll
        for (int i = 0; i < 5; ++i) { sm = MFMA(ONES8, asbf(v[i]), sm); sq = MFMA(asbf(v[i]), asbf(v[i]), sq); }
        const float q = (n & 2) ? ((n & 1) ? sq[3] : sq[2]) : ((n & 1) ? sq[1] : sq[0]);
        char* sb = smem + (s & 3) * 512;
        if (g == (n >> 2)) *(f32x2*)(sb + w * 128 + n * 8) = (f32x2){sm[0], q};
        if (od) LADD(2052) else LADD(2048)
        LWAIT(2048 + od * 4, 4 * (s >> 1) + 4)
        const char* sp = sb + n * 8;
        const f32x2 st = ln_stat((*(const f32x2*)sp + *(const f32x2*)(sp + 128)) + (*(const f32x2*)(sp + 256) + *(const f32x2*)(sp + 384)));
#pragma unroll
        for (int i = 0; i < 5; ++i) {
#pragma unroll
            for (int e = 0; e < 4; ++e) {
                const unsigned d = pack2(bflo(v[i][e]) - st[0], bfhi(v[i][e]) - st[0]);
                v[i][e] = pack2(bflo(d) * st[1], bfhi(d) * st[1]);
            }
            st128(rs, lo + i * 1024, OFF_XN + (unsigned)(64 + s) * 40960u + mt * 20480 + w * 5120, v[i]);
        }
        if (w == 0) { FTS(36864 + s * 32 + mt) }
        WAITV0();
        if (w == 0) { FTS(36864 + s * 32 + 2 + mt) }
        st128(rs, lo < 64 ? lo : 0xC0000000u, FL_N + (unsigned)s * 512u + mt * 256 + w * 64, FLV);
    }
    WAITV0();
}

#define MEGA_LDS 163840
__global__ __launch_bounds__(512, 2) void mega_kernel(Params p) {
    extern __shared__ __attribute__((aligned(16))) char smem[];
    const int b = blockIdx.x, x = b & 7, r = b >> 3, i = r * 4 + (x & 3), hi = x >> 2;
    const int w = __builtin_amdgcn_readfirstlane(threadIdx.x >> 6);
    int role, a;
    if (x >= 4) {
        if (i < 32) { role = 8; a = i; } else if (i < 104) { role = 3; a = i - 32; } else if (i < 118) { role = 6; a = i - 104; } else if (i < 120) { role = 7; a = i - 118; } else { role = 3; a = i - 48; }
    }
    else if (x == 0) { role = 0; a = r; }
    else { const int j = r * 3 + x - 1; if (j < 40) { role = 1; a = (j & 1) * 20 + (j >> 1); } else if (j < 72) { role = 2; a = j - 40; } else { role = 5; a = j - 72; } }
    if (role == 5) role = 3; else if (role == 3 && a < 24) role = 5;
    SU(2)
    if (role == 0) qkm_role(p, smem, w, a);
    else if (role == 1) r_role(p, smem, w, a >= 20, a >= 20 ? a - 20 : a);
    else if (role == 2) ua_role(p, smem, w, a, hi);
    else if (role == 8) ob_role(p, smem, w, a);
    else if (role == 3) {
        const bool hp = a >= 72;
        if (hp) { if (!GH) return; wait_until(now32() + GHD); }
#pragma unroll 1
        for (int k = 0; k < (hp ? 6 : 1); ++k) g_role(p, smem, w, hp ? 143 - a - 8 * k : a, hp ? 128 - GH : 0, hp || a < 24 ? 128 : 128 - GH, hp ? 128 : 0);
    }
    else if (role == 5) up_role(p, smem, w, a);
    else if (role == 6) dn_role(p, smem, w, a);
    else ln_role(p, smem, w, a);
    SU(4)
}

extern "C" void kernel_launch(void* const* d_in, const int* in_sizes, int n_in,
                              void* d_out, int out_size, void* d_ws, size_t ws_size,
                              hipStream_t stream) {
    Params p;
    p.x = (const u16*)d_in[0]; p.mem0 = (const u16*)d_in[1];
    p.Wq = (const u16*)d_in[2]; p.Wk = (const u16*)d_in[3]; p.Wv = (const u16*)d_in[4]; p.Wo = (const u16*)d_in[5];
    p.wg = (const u16*)d_in[8]; p.wu = (const u16*)d_in[9]; p.wd = (const u16*)d_in[10];
    p.ws = (char*)(((size_t)d_ws + 0x1FFFFF) & ~(size_t)0x1FFFFF); p.out = (u16*)d_out;
    (void)hipFuncSetAttribute((const void*)mega_kernel, hipFuncAttributeMaxDynamicSharedMemorySize, MEGA_LDS);
    init_kernel<<<256, 512, 32768, stream>>>(p);
    mega_kernel<<<256, 512, MEGA_LDS, stream>>>(p);
}
```

```cpp
#include <hip/hip_runtime.h>
#include <stdint.h>

typedef __attribute__((ext_vector_type(8))) short bf16x8;
typedef __attribute__((ext_vector_type(16))) short bf16x16;
typedef __attribute__((ext_vector_type(4))) float f32x4;
typedef __attribute__((ext_vector_type(2))) float f32x2;
typedef __attribute__((ext_vector_type(4))) unsigned u32x4;
typedef __attribute__((ext_vector_type(2))) unsigned u32x2;
typedef __attribute__((ext_vector_type(2))) __bf16 bf16v2;
typedef unsigned short u16;
typedef __attribute__((address_space(3))) void* lds_ptr_t;
typedef __attribute__((address_space(1))) const void* gptr_t;
typedef __amdgpu_buffer_rsrc_t rsrc_t;

#define NSEG 64
#define LAG 8
#define EUP (LAG + 2)
#define VAH 5
#define QAH 4
#define VHD 20
#define OBD (LAG + 1)
#define HM 0
#define HELPS 0
#define BRN 16
#define C1D 600
#define GLD 700
#define DW 200
#define UPK 8
#define DRL 1
#define DX 120
#define UDW 0
#define USL 3
#define DXD 320
#define DXL 500
#define DCK 250
#define DQ 196
#define DV 196
#define DR 118
#define DRU 104
#define BE 2
#define NCAL 6
#define PFL 0
#define RMASK 79
#define RING(id) ((id) & RMASK)
static_assert(LAG <= 13 && QAH <= 13, "rings too small");
#define QS0 4
#define QS1 4
#define WFS 2
#define WAUX 2
#define DQ3 27
#define PL 4
#define GH 24
#define GHD 1200
#define DLAST 230
#define GSW 1
#define DH 70
#define BNH 24
#define RFX (PL == 7 ? 0x2000 : 0)
#define QSX 0
#define KSX 0x2000
#define SDQ 0
#define SDU 0
#define SDO 0
#define SDA 0
#define SDB 0
#define SDV 0
#define GAUX 0
#define PFD 0
#define AXQ 0
#define AXG 0
#define AXV 0
#define AXO 2
#define AXUO 2
#define AXUV 0
#define UVF 4
#define GHN 6
#define HSEG 30
#define RHD 1200
#define AUX_SC1 16
#define POISON 0xFFFFFFFFu
#define SPIN_CAP (1u << 17)

constexpr unsigned FL_T = 2048;
constexpr unsigned FL_V = 4096;
constexpr unsigned FL_N = FL_V + 3u * 16384;
constexpr unsigned CTRL_BYTES = FL_N + 66u * 1024;
constexpr unsigned HS_D = CTRL_BYTES, HS_N = HS_D + 1024, HS_G = HS_N + 3072;
constexpr unsigned OFF_X1 = HS_G + 4096;
constexpr unsigned OFF_X3 = OFF_X1 + 64u * 40960;
constexpr unsigned OFF_X0 = OFF_X3 + 64u * 40960;
constexpr unsigned OFF_X2 = OFF_X0 + 64u * 40960;
constexpr unsigned OFF_XN = OFF_X2 + 64u * 40960;
constexpr unsigned OFF_HG = OFF_XN + 128u * 40960;
constexpr unsigned OFF_HOT = (OFF_HG + 2u * 64 * 98304 + 0x1FFFFFu) & ~0x1FFFFFu;
constexpr unsigned OFF_BT = OFF_HOT + 3u * 2097152;
constexpr unsigned OFF_QK = OFF_BT + 2u * BRN * 2621440;
constexpr unsigned OFF_END = OFF_QK + 80u * 1572864;
__host__ __device__ constexpr unsigned blk(unsigned piece) { return (piece >> 3) * 16384u + (piece & 7) * 1024u; }
constexpr unsigned HB_MEM = 0;
constexpr unsigned HB_PM = 16u * 16384;
constexpr unsigned HB_PT = 48u * 16384;
constexpr unsigned HB_TS = 112u * 16384;
constexpr unsigned HB_FB = 116u * 16384;
constexpr unsigned HB_FK = 120u * 16384;
constexpr unsigned HB_END = 124u * 16384;
static_assert(OFF_END + 2097152u <= 262000000u, "workspace too large");

__device__ __forceinline__ unsigned pack2(float a, float b) {
    f32x2 v = {a, b};
    bf16v2 r = __builtin_convertvector(v, bf16v2);
    return __builtin_bit_cast(unsigned, r);
}
__device__ __forceinline__ float bflo(unsigned u) { return __uint_as_float(u << 16); }
__device__ __forceinline__ float bfhi(unsigned u) { return __uint_as_float(u & 0xFFFF0000u); }
__device__ __forceinline__ float bfr(float f) { return bflo(pack2(f, 0.f)); }
__device__ __forceinline__ unsigned nop(unsigned d) { return min(d, 0xFFFFFFFEu); }
__device__ __forceinline__ u32x4 ld128(rsrc_t rs, unsigned voff, unsigned soff) { return __builtin_amdgcn_raw_buffer_load_b128(rs, (int)voff, (int)soff, AUX_SC1); }
__device__ __forceinline__ unsigned ld32(rsrc_t rs, unsigned voff, unsigned soff) { return __builtin_amdgcn_raw_buffer_load_b32(rs, (int)voff, (int)soff, AUX_SC1); }
__device__ __forceinline__ void st128(rsrc_t rs, unsigned voff, unsigned soff, u32x4 v) { __builtin_amdgcn_raw_buffer_store_b128(v, rs, (int)(voff + soff), 0, AUX_SC1); }
__device__ __forceinline__ void st32(rsrc_t rs, unsigned voff, unsigned soff, unsigned v) { __builtin_amdgcn_raw_buffer_store_b32(v, rs, (int)voff, (int)soff, AUX_SC1); }
__device__ __forceinline__ bf16x8 asbf(u32x4 v) { return __builtin_bit_cast(bf16x8, v); }
__device__ __forceinline__ unsigned mx4(u32x4 v) { return max(max(v[0], v[1]), max(v[2], v[3])); }
__device__ __forceinline__ unsigned mn4(u32x4 v) { return min(min(v[0], v[1]), min(v[2], v[3])); }
__device__ __forceinline__ u32x4 poison4() { unsigned z = POISON; asm volatile("" : "+v"(z)); return (u32x4){z, z, z, z}; }
__device__ __forceinline__ u32x4 ones4() { unsigned z = 1u; asm volatile("" : "+v"(z)); return (u32x4){z, z, z, z}; }
#define MFMA(a, b, c) __builtin_amdgcn_mfma_f32_16x16x32_bf16((a), (b), (c), 0, 0, 0)
#define MFMAP(d, a, b) { d = MFMA(a, b, d); asm volatile("" : "+v"(d)); }
#define Z4 ((f32x4){0.f, 0.f, 0.f, 0.f})
#define RAWBAR() { asm volatile("s_waitcnt lgkmcnt(0)" ::: "memory"); __builtin_amdgcn_s_barrier(); asm volatile("" ::: "memory"); }
#define WAITV0() { __builtin_amdgcn_s_waitcnt(0x0F70); asm volatile("" ::: "memory"); }
#define WAITVN(n) { __builtin_amdgcn_s_waitcnt(0x0F70 | (n)); asm volatile("" ::: "memory"); }
#define WAITL0() asm volatile("s_waitcnt lgkmcnt(0)" ::: "memory")
#define SB0() __builtin_amdgcn_sched_barrier(0)
#define MEMB() asm volatile("" ::: "memory")
#define ROR8U(x) ((unsigned)__builtin_amdgcn_update_dpp(0, (int)(x), 0x128, 0xf, 0xf, false))
#define ROR8(x) __uint_as_float(ROR8U(__float_as_uint(x)))
#define QX1U(x) ((unsigned)__builtin_amdgcn_update_dpp(0, (int)(x), 0xB1, 0xf, 0xf, false))
#define QEV(x) ((unsigned)__builtin_amdgcn_update_dpp(0, (int)(x), 0xA0, 0xf, 0xf, false))
#define QOD(x) ((unsigned)__builtin_amdgcn_update_dpp(0, (int)(x), 0xF5, 0xf, 0xf, false))
#define DMA(dst, voff, soff) __builtin_amdgcn_raw_ptr_buffer_load_lds(rs, (lds_ptr_t)(dst), 16, (int)(voff), (int)(soff), 0, AUX_SC1)
#define DMAI(dst, voff, soff, imm) __builtin_amdgcn_raw_ptr_buffer_load_lds(rs, (lds_ptr_t)(dst), 16, (int)(voff), (int)(soff), imm, AUX_SC1)
#define OPAQUE_LO() unsigned lo; asm volatile("v_mbcnt_lo_u32_b32 %0, -1, 0\n\tv_mbcnt_hi_u32_b32 %0, -1, %0\n\tv_lshlrev_b32 %0, 4, %0" : "=v"(lo));
__device__ __forceinline__ int lane_id() { int l; asm volatile("v_mbcnt_lo_u32_b32 %0, -1, 0\n\tv_mbcnt_hi_u32_b32 %0, -1, %0" : "=v"(l)); return l; }
__device__ __forceinline__ unsigned now32() { return (unsigned)__builtin_amdgcn_s_memrealtime(); }
#define FTS(slot)
#define FLV ones4()
__device__ __forceinline__ void wait_until(unsigned t) { while ((int)(t - now32()) > 0) __builtin_amdgcn_s_sleep(1); }
__device__ __forceinline__ float xmax(float v) {
    float t;
    asm("v_mov_b32 %1, %0\n\ts_nop 1\n\tv_permlane16_swap_b32 %0, %1\n\tv_max_f32 %0, %0, %1\n\t"
        "v_mov_b32 %1, %0\n\ts_nop 1\n\tv_permlane32_swap_b32 %0, %1\n\tv_max_f32 %0, %0, %1" : "+v"(v), "=&v"(t));
    return v;
}
__device__ __forceinline__ float max3(float a, float b, float c) { return fmaxf(fmaxf(a, b), c); }
__device__ __forceinline__ float xsum(float v) {
    unsigned a = __float_as_uint(v);
    auto r = __builtin_amdgcn_permlane16_swap(a, a, false, false);
    v = __uint_as_float(r[0]) + __uint_as_float(r[1]);
    a = __float_as_uint(v);
    auto s = __builtin_amdgcn_permlane32_swap(a, a, false, false);
    return __uint_as_float(s[0]) + __uint_as_float(s[1]);
}

struct Params {
    const u16* x; const u16* mem0; const u16* Wq; const u16* Wk; const u16* Wv; const u16* Wo;
    const u16* wg; const u16* wu; const u16* wd;
    char* ws; u16* out;
};
__device__ __forceinline__ char* wsp(const Params& p) { return (char*)(((size_t)p.ws + 0x1FFFFF) & ~(size_t)0x1FFFFF); }

__device__ __forceinline__ bool spin_fail(const Params& p, unsigned& spins) {
    ++spins;
    if ((spins & 255u) == 0) {
        unsigned* a = (unsigned*)wsp(p);
        if (spins > SPIN_CAP) { __hip_atomic_store(a, 1u, __ATOMIC_RELAXED, __HIP_MEMORY_SCOPE_AGENT); return true; }
        if (__hip_atomic_load(a, __ATOMIC_RELAXED, __HIP_MEMORY_SCOPE_AGENT)) return true;
    }
    return false;
}
#define POLL(BODY) { unsigned sp_ = 0; bool bad; \
    do { int z_ = 0; do { MEMB(); BODY } while (bad && ++z_ < 16); } while (__builtin_expect(bad, 0) && !spin_fail16(p, sp_)); }
__device__ __forceinline__ bool spin_fail16(const Params& p, unsigned& spins) { spins += 15; return spin_fail(p, spins); }
__device__ __forceinline__ void wait_flags(const Params& p, rsrc_t rs, unsigned voff, unsigned soff, bool use) {
    unsigned spins = 0;
    for (;;) {
        const unsigned f = use ? ld32(rs, voff, soff) : 1u;
        if (__all(f != 0u)) break;
        MEMB();
        __builtin_amdgcn_s_sleep(WFS);
        if (spin_fail(p, spins)) break;
    }
    MEMB();
}
template <int N> __device__ __forceinline__ bool bad_any(const u32x4 (&v)[N]) {
    unsigned m = mx4(v[0]);
#pragma unroll
    for (int i = 1; i < N; ++i) m = max(m, mx4(v[i]));
    return __any(m == POISON);
}
template <int N>
__device__ __forceinline__ void regather(const Params& p, rsrc_t rs, unsigned lo, unsigned soff, u32x4 (&v)[N], bool slp) {
    unsigned spins = 0;
    do {
        if (slp) __builtin_amdgcn_s_sleep(4);
#pragma unroll
        for (int i = 0; i < N; ++i) if (mx4(v[i]) == POISON) v[i] = ld128(rs, lo + i * 1024, soff);
        WAITV0();
    } while (bad_any(v) && !spin_fail(p, spins));
}
template <int N>
__device__ __forceinline__ void gather(const Params& p, rsrc_t rs, unsigned lo, unsigned soff, u32x4 (&v)[N], bool slp) {
#pragma unroll
    for (int i = 0; i < N; ++i) v[i] = ld128(rs, lo + i * 1024, soff);
    if (__builtin_expect(bad_any(v), 0)) regather(p, rs, lo, soff, v, slp);
}

template <int CPR, int NROW, typename SrcF>
__device__ __forceinline__ void stage_issue(char* buf, int w, int l, SrcF src  ) {
    constexpr int PPW = CPR * NROW / 512;
    static_assert(CPR * NROW % 512 == 0 && CPR % 8 == 0, "chunk size");
#pragma unroll
    for (int i = 0; i < PPW; ++i) {
        const unsigned idx = (unsigned)((w * PPW + i) * 64 + l), r = idx / CPR, c = (idx % CPR) ^ ((r & 3) << 1);
        __builtin_amdgcn_global_load_lds((gptr_t)src(r, c), (lds_ptr_t)(buf + (w * PPW + i) * 1024), 16, 0, GAUX);
    }
}
template <int CPR, bool NAT>
__device__ __forceinline__ bf16x8 tr_frag(const char* buf, int r0, unsigned cb, int l) {
    const int g = l >> 4, q = (l & 15) >> 2;
    const unsigned ra = r0 + (NAT ? 8 * g : 4 * g) + q;
    const unsigned co = (((cb >> 4) ^ (q << 1)) << 4) + (cb & 15);
    const unsigned ad = (unsigned)(size_t)(lds_ptr_t)buf + ra * (CPR * 16) + co;
    u32x2 a, b;
    asm volatile("ds_read_b64_tr_b16 %0, %2\n\tds_read_b64_tr_b16 %1, %2 offset:%3\n\ts_waitcnt lgkmcnt(0)"
                 : "=&v"(a), "=&v"(b) : "v"(ad), "i"((NAT ? 4 : 16) * CPR * 16) : "memory");
    return asbf((u32x4){a[0], a[1], b[0], b[1]});
}
template <int CPR>
__device__ __forceinline__ void tr_frag6(bf16x8 (&f)[6], const char* buf, unsigned cb, int l) {
    const int g = l >> 4, q = (l & 15) >> 2;
    const unsigned ad = (unsigned)(size_t)(lds_ptr_t)buf + (8 * g + q) * (CPR * 16) + (((cb >> 4) ^ (q << 1)) << 4) + (cb & 15);
    u32x2 a[12];
    static_assert(CPR == 16, "offsets below");
    asm volatile("ds_read_b64_tr_b16 %0, %12\n\tds_read_b64_tr_b16 %1, %12 offset:1024\n\t"
                 "ds_read_b64_tr_b16 %2, %12 offset:8192\n\tds_read_b64_tr_b16 %3, %12 offset:9216\n\t"
                 "ds_read_b64_tr_b16 %4, %12 offset:16384\n\tds_read_b64_tr_b16 %5, %12 offset:17408\n\t"
                 "ds_read_b64_tr_b16 %6, %12 offset:24576\n\tds_read_b64_tr_b16 %7, %12 offset:25600\n\t"
                 "ds_read_b64_tr_b16 %8, %12 offset:32768\n\tds_read_b64_tr_b16 %9, %12 offset:33792\n\t"
                 "ds_read_b64_tr_b16 %10, %12 offset:40960\n\tds_read_b64_tr_b16 %11, %12 offset:41984\n\ts_waitcnt lgkmcnt(0)"
                 : "=&v"(a[0]), "=&v"(a[1]), "=&v"(a[2]), "=&v"(a[3]), "=&v"(a[4]), "=&v"(a[5]), "=&v"(a[6]), "=&v"(a[7]), "=&v"(a[8]), "=&v"(a[9]), "=&v"(a[10]), "=&v"(a[11])
                 : "v"(ad) : "memory");
#pragma unroll
    for (int i = 0; i < 6; ++i) f[i] = asbf((u32x4){a[2 * i][0], a[2 * i][1], a[2 * i + 1][0], a[2 * i + 1][1]});
}
#define ONES8 ((bf16x8){0x3f80, 0x3f80, 0x3f80, 0x3f80, 0x3f80, 0x3f80, 0x3f80, 0x3f80})

#define SMFMA(a, b, c, ix) __builtin_amdgcn_smfmac_f32_16x16x64_bf16((a), (b), (c), (ix), 0, 0)
__device__ __forceinline__ bf16x16 cat16(bf16x8 a, bf16x8 b) { return __builtin_shufflevector(a, b, 0, 1, 2, 3, 4, 5, 6, 7, 8, 9, 10, 11, 12, 13, 14, 15); }
__device__ __forceinline__ bf16x8 half16(bf16x16 a, int hi) {
    return hi ? __builtin_shufflevector(a, a, 8, 9, 10, 11, 12, 13, 14, 15) : __builtin_shufflevector(a, a, 0, 1, 2, 3, 4, 5, 6, 7);
}
__device__ __forceinline__ u32x2 fold2(f32x4 a, f32x4 b) {
    float s[4];
#pragma unroll
    for (int i = 0; i < 4; ++i) {
        auto r = __builtin_amdgcn_permlane32_swap(__float_as_uint(a[i]), __float_as_uint(b[i]), false, false);
        s[i] = __uint_as_float(r[0]) + __uint_as_float(r[1]);
    }
    return (u32x2){pack2(s[0], s[1]), pack2(s[2], s[3])};
}
typedef __attribute__((ext_vector_type(4))) short s16x4;
__device__ __forceinline__ unsigned troff(unsigned lo) { return ((lo >> 6) << 4) + ((lo >> 1) & 8); }
__device__ __forceinline__ bf16x8 trf(const char* base, int i) {
    typedef __attribute__((address_space(3))) s16x4* tp_t;
    const s16x4 a = __builtin_amdgcn_ds_read_tr16_b64_v4i16((tp_t)(base + i * 512)), b = __builtin_amdgcn_ds_read_tr16_b64_v4i16((tp_t)(base + i * 512 + 256));
    return __builtin_shufflevector(a, b, 0, 1, 2, 3, 4, 5, 6, 7);
}
#define LDS(T, a) (*(__attribute__((address_space(3))) T*)(unsigned)(a))
__device__ __forceinline__ bf16x8 trfa(unsigned a) {
    typedef __attribute__((address_space(3))) s16x4* tp_t;
    const s16x4 x = __builtin_amdgcn_ds_read_tr16_b64_v4i16((tp_t)a), y = __builtin_amdgcn_ds_read_tr16_b64_v4i16((tp_t)(a + 256));
    return __builtin_shufflevector(x, y, 0, 1, 2, 3, 4, 5, 6, 7);
}
#define LSPIN(off, val) { for (;;) { MEMB(); if (__builtin_amdgcn_readfirstlane(mn4(*(const u32x4*)(smem + (off)))) >= (unsigned)(val)) break; } MEMB(); }
#define LADD(off) { unsigned o_; \
    asm volatile("v_mov_b32 %0, 1\n\ts_mov_b64 exec, 1\n\tds_add_u32 %1, %0 offset:%2\n\ts_mov_b64 exec, -1" : "=&v"(o_) : "v"(lo), "i"(off) : "memory"); }
#define LADDV(off, sv) { unsigned o_; \
    asm volatile("v_mov_b32 %0, %3\n\ts_mov_b64 exec, 1\n\tds_add_u32 %1, %0 offset:%2\n\ts_mov_b64 exec, -1" : "=&v"(o_) : "v"(lo), "i"(off), "s"(sv) : "memory"); }
#define LWORD(off) (*(unsigned*)(smem + (off) + (lo >> 10)))
template <int IMM>
__device__ __forceinline__ void dma_raw(u32x4 rd, unsigned dst, unsigned voff, unsigned soff) {
    unsigned keep;
    asm volatile("s_mov_b32 %0, m0\n\ts_mov_b32 m0, %1\n\ts_nop 0\n\tbuffer_load_dwordx4 %2, %3, %4 offen offset:%5 sc1 lds\n\ts_mov_b32 m0, %0"
                 : "=&s"(keep) : "s"(__builtin_amdgcn_readfirstlane(dst)), "v"(voff), "s"(rd), "s"(__builtin_amdgcn_readfirstlane(soff)), "i"(IMM) : "memory");
}
#define UNI(x) ((unsigned)__builtin_amdgcn_readfirstlane((unsigned)(x)))
#define RAWRD() const u32x4 rd = (u32x4){UNI((size_t)wsp(p)), UNI(((size_t)wsp(p) >> 32) & 0xffffu), 0x7fffffffu, 0x00020000u}; \
    const unsigned lb = (unsigned)(size_t)(lds_ptr_t)smem;
#define LWAITX(off, val, slp) { unsigned a_, d_, s_, n_; \
    asm volatile("v_mov_b32 %0, %4\n\ts_mov_b32 %3, 0x400000\n1:\n\tds_read_b32 %1, %0\n\ts_waitcnt lgkmcnt(0)\n\tv_readfirstlane_b32 %2, %1\n\t" \
                 "s_cmp_ge_u32 %2, %5\n\ts_cbranch_scc1 2f\n\t" slp "s_sub_u32 %3, %3, 1\n\ts_cbranch_scc0 1b\n2:" \
                 : "=&v"(a_), "=&v"(d_), "=&s"(s_), "=&s"(n_) : "s"(__builtin_amdgcn_readfirstlane((unsigned)(off))), "s"(__builtin_amdgcn_readfirstlane((unsigned)(val))) : "memory", "scc"); }
#define LWAIT(off, val) LWAITX(off, val, "")
#define LWAITS(off, val) LWAITX(off, val, "s_sleep 1\n\t")
#define FLRD(off) { MEMB(); fl = LWORD(off); }
#define FLOK(val) (__builtin_amdgcn_readfirstlane(fl) >= (unsigned)(val))
#define LSET(off, val) { MEMB(); LWORD(off) = (unsigned)(val); MEMB(); }
__device__ __forceinline__ f32x2 ln_stat(f32x2 a) {
    const float mean = a[0] * (1.f / 640.f);
    const float var = bfr(fmaxf(__builtin_fmaf(-mean, mean, a[1] * (1.f / 640.f)), 0.f));
    const float den = bfr(__builtin_amdgcn_sqrtf(bfr(var + 1e-5f)));
    return (f32x2){bfr(mean), __builtin_amdgcn_rcpf(den)};
}
#define KSO(i) ((i) % 5 == 4 ? 16 + (i) / 5 : (i) - (i) / 5)
#define XPC(dst, src, gq, i) { if ((i) < 4) DMAI((dst) + (gq) * 5120, lo, (src) + (unsigned)(gq) * 4096u, ((i) & 3) * 1024); else DMA((dst) + (gq) * 5120 + 4096, lo, (src) + (unsigned)(16 + (gq)) * 1024u); }
#define XGRP(dst, src, gq) { XPC(dst, src, gq, 0) XPC(dst, src, gq, 1) XPC(dst, src, gq, 2) XPC(dst, src, gq, 3) XPC(dst, src, gq, 4) }
template <int I> struct IC { static constexpr int v = I; };
template <int I, int N, typename F> __device__ __forceinline__ void static_for(F f) { if constexpr (I < N) { f(IC<I>{}); static_for<I + 1, N>(f); } }
template <typename F, typename E>
__device__ __forceinline__ void proj2(const bf16x16 (&wt)[3][8], const char* xf, const char* xg, const char* wl, F side, E epi) {
    bf16x8 B[40], W[8][3];
    f32x4 a[2][3] = {{Z4, Z4, Z4}, {Z4, Z4, Z4}};
#pragma unroll
    for (int i = 0; i < 3; ++i) B[i] = *(const bf16x8*)(xf + i * 1024);
    static_for<0, 40>([&](auto ic) __attribute__((always_inline)) {
        constexpr int i = decltype(ic)::v, k = i % 20, tt = i / 20;
        side(ic);
        SB0();
#pragma unroll
        for (int j = 0; j < 3; ++j) {
            a[tt][j] = MFMA(i % 5 == 4 ? W[i / 5][j] : half16(wt[j][(KSO(k) & 15) >> 1], KSO(k) & 1), B[i], a[tt][j]);
            asm volatile("" : "+v"(a[tt][j]));
            SB0();
            if (j == 0) { if constexpr (i + 3 < 40) B[i + 3] = *(const bf16x8*)((i + 3 < 20 ? xf : xg) + (i + 3) * 1024); }
            if (j == 1) { if constexpr (i % 5 < 3 && i != 20) W[i / 5][i % 5] = *(const bf16x8*)(wl + ((k / 5) * 3 + i % 5) * 1024); }
            if (j == 2) { if constexpr (i == 21) W[4][0] = *(const bf16x8*)wl; }
            SB0();
        }
        if constexpr (i == 20) { epi(0, 0, a[0][0]); epi(0, 1, a[0][1]); }
        if constexpr (i == 21) epi(0, 2, a[0][2]);
        SB0();
    });
#pragma unroll
    for (int j = 0; j < 3; ++j) epi(1, j, a[1][j]);
}

template <typename F, typename E>
__device__ __forceinline__ void proj1(const bf16x16 (&wt)[3][8], const unsigned xf, const unsigned wl, F side, E epi) {
    bf16x8 B[20], W[4][3];
    f32x4 a[3] = {Z4, Z4, Z4};
#pragma unroll
    for (int i = 0; i < 3; ++i) B[i] = LDS(bf16x8, xf + i * 1024);
    static_for<0, 20>([&](auto ic) __attribute__((always_inline)) {
        constexpr int i = decltype(ic)::v;
        side(ic);
        SB0();
#pragma unroll
        for (int j = 0; j < 3; ++j) {
            a[j] = MFMA(i % 5 == 4 ? W[i / 5][j] : half16(wt[j][(KSO(i) & 15) >> 1], KSO(i) & 1), B[i], a[j]);
            asm volatile("" : "+v"(a[j]));
            SB0();
            if (j == 0) { if constexpr (i + 3 < 20) B[i + 3] = LDS(bf16x8, xf + (i + 3) * 1024); }
            if (j == 1) { if constexpr (i % 5 < 3) W[i / 5][i % 5] = LDS(bf16x8, wl + ((i / 5) * 3 + i % 5) * 1024); }
            SB0();
        }
    });
    epi((u32x4){pack2(a[0][0], a[0][1]), pack2(a[0][2], a[0][3]), pack2(a[1][0], a[1][1]), pack2(a[1][2], a[1][3])}, (u32x2){pack2(a[2][0], a[2][1]), pack2(a[2][2], a[2][3])});
}

template <typename F, typename E>
__device__ __forceinline__ void proj1s(const bf16x16 (&wt)[3][8], bf16x8 (&hd)[3], const unsigned xf, const unsigned& xn, const unsigned wl, F side, E epi) {
    bf16x8 B[23], W[4][3];
    f32x4 a[3];
#pragma unroll
    for (int i = 0; i < 3; ++i) B[i] = hd[i];
    static_for<0, 20>([&](auto ic) __attribute__((always_inline)) {
        constexpr int i = decltype(ic)::v;
        side(ic);
        SB0();
#pragma unroll
        for (int j = 0; j < 3; ++j) {
            a[j] = MFMA(i % 5 == 4 ? W[i / 5][j] : half16(wt[j][(KSO(i) & 15) >> 1], KSO(i) & 1), B[i], i ? a[j] : Z4);
            asm volatile("" : "+v"(a[j]));
            SB0();
            if (j == 0) B[i + 3] = LDS(bf16x8, i + 3 < 20 ? xf + (i + 3) * 1024 : xn + (i - 17) * 1024);
            if (j == 1) { if constexpr (i % 5 < 3) W[i / 5][i % 5] = LDS(bf16x8, wl + ((i / 5) * 3 + i % 5) * 1024); }
            SB0();
        }
    });
#pragma unroll
    for (int i = 0; i < 3; ++i) hd[i] = B[20 + i];
    epi((u32x4){pack2(a[0][0], a[0][1]), pack2(a[0][2], a[0][3]), pack2(a[1][0], a[1][1]), pack2(a[1][2], a[1][3])}, (u32x2){pack2(a[2][0], a[2][1]), pack2(a[2][2], a[2][3])});
}

template <int I0, typename S>
__device__ __forceinline__ void projh(const bf16x16 (&wt)[3][8], const unsigned xf, const unsigned wl, f32x4 (&a)[3], S sync) {
    bf16x8 B[20], W[4][3];
#pragma unroll
    for (int i = I0; i < I0 + 3; ++i) B[i] = LDS(bf16x8, xf + i * 1024);
    sync();
    static_for<I0, I0 + 10>([&](auto ic) __attribute__((always_inline)) {
        constexpr int i = decltype(ic)::v;
        SB0();
#pragma unroll
        for (int j = 0; j < 3; ++j) {
            a[j] = MFMA(i % 5 == 4 ? W[i / 5][j] : half16(wt[j][(KSO(i) & 15) >> 1], KSO(i) & 1), B[i], i ? a[j] : Z4);
            asm volatile("" : "+v"(a[j]));
            SB0();
            if (j == 0) { if constexpr (i + 3 < I0 + 10) B[i + 3] = LDS(bf16x8, xf + (i + 3) * 1024); }
            if (j == 1) { if constexpr (i % 5 < 3) W[i / 5][i % 5] = LDS(bf16x8, wl + ((i / 5) * 3 + i % 5) * 1024); }
            SB0();
        }
    });
}

#define PDIST 4
#define NI 86
__device__ __forceinline__ void fill16(rsrc_t rs, unsigned dst, unsigned bytes, unsigned gt, unsigned val) {
    if (gt >= NI * 512) gt -= NI * 512;
    for (unsigned i = gt; i < bytes / 16; i += NI * 512) st128(rs, dst + i * 16, 0, (u32x4){val, val, val, val});
}
#define RORF(x, n) __uint_as_float((unsigned)__builtin_amdgcn_update_dpp(0, (int)__float_as_uint(x), 0x120 + (n), 0xf, 0xf, false))
__device__ __forceinline__ float wsum(float v) { v += RORF(v, 8); v += RORF(v, 4); v += RORF(v, 2); v += RORF(v, 1); return xsum(v); }
__device__ __forceinline__ void init_work(const Params& p, char* smem, const int w, const int k) {
    const rsrc_t rs = __builtin_amdgcn_make_buffer_rsrc((void*)wsp(p), 0, 0x7fffffff, 0x00020000);
    const int l = lane_id(), tid = w * 64 + l;
    const unsigned gt = k * 512 + tid;
    unsigned xd[3][5];
#pragma unroll
    for (int j = 0; j < 3; ++j) {
        const unsigned* xr = (const unsigned*)(p.x + (size_t)(min(k + j * NI, 255) * 8 + w) * 640);
#pragma unroll
        for (int i = 0; i < 5; ++i) xd[j][i] = __builtin_nontemporal_load(xr + i * 64 + l);
    }
    fill16(rs, 0, CTRL_BYTES, gt, 0u);
    fill16(rs, OFF_X1, PDIST * 40960, gt + 30208, POISON);
    fill16(rs, OFF_X2, PDIST * 40960, gt + 19968, POISON);
    fill16(rs, OFF_X3, PDIST * 40960, gt + 9728, POISON);
    fill16(rs, OFF_HG, PDIST * 98304, gt, POISON);
    fill16(rs, OFF_HG + 64u * 98304, PDIST * 98304, gt + 19456, POISON);
    fill16(rs, OFF_HOT + 4194304 + HB_MEM, 16u * 16384, gt + 38912, POISON);
    fill16(rs, OFF_HOT + 4194304 + HB_TS, 4u * 16384, gt + 22528, POISON);
    fill16(rs, OFF_HOT + 4194304 + HB_FB, 7u * 16384, gt + 33280, 0u);
#pragma unroll
    for (int j = 0; j < 3; ++j) {
        float v[10], sm = 0.f;
#pragma unroll
        for (int i = 0; i < 5; ++i) { v[2 * i] = bflo(xd[j][i]); v[2 * i + 1] = bfhi(xd[j][i]); sm += v[2 * i] + v[2 * i + 1]; }
        sm = wsum(sm);
        const float mean = sm * (1.f / 640.f);
        float q = 0.f;
#pragma unroll
        for (int i = 0; i < 10; ++i) { const float d = v[i] - mean; q += d * d; }
        q = wsum(q);
        const float rd = __builtin_amdgcn_rcpf(bfr(__builtin_amdgcn_sqrtf(bfr(bfr(q * (1.f / 640.f)) + 1e-5f)))), mb = bfr(mean);
        unsigned* xn = (unsigned*)(smem + j * 20480 + w * 1280);
#pragma unroll
        for (int i = 0; i < 5; ++i) { xn[i * 64 + l] = pack2(bfr(v[2 * i] - mb) * rd, bfr(v[2 * i + 1] - mb) * rd); xn[2560 + i * 64 + l] = xd[j][i]; }
    }
    RAWBAR()
    for (int i = tid; i < 3840; i += 512) {
        const int j = i / 1280, b = k + j * NI, e = i - j * 1280, raw = e >= 640, m = e - raw * 640, r = m & 7, u = m >> 3;
        const char* src = smem + j * 20480 + raw * 10240 + r * 1280 + ((u >> 2) * 32 + (u & 3) * 4) * 2;
        const u32x2 y0 = *(const u32x2*)src, y1 = *(const u32x2*)(src + 32);
        st128(rs, b < 256 ? (raw ? OFF_X0 : OFF_XN) + (b >> 1) * 20480 + u * 256 + ((b & 1) * 8 + r) * 16 : 0xC0000000u, 0, (u32x4){y0[0], y0[1], y1[0], y1[1]});
    }
    WAITV0();
    RAWBAR()
}
#define HS_KEY 0x5A5A5A5Au
__device__ __forceinline__ unsigned start_wait(const Params& p, char* smem, const int w, const u32x2 n, const unsigned dv = 1u) {
    const rsrc_t rs = __builtin_amdgcn_make_buffer_rsrc((void*)wsp(p), 0, 0x7fffffff, 0x00020000);
    if (w == 0) {
        bool sent = false;
        unsigned par = 0u;
        for (unsigned sp = 0; sp < SPIN_CAP; ++sp) {
            MEMB();
            const u32x4 g = ld128(rs, 0, HS_G + blockIdx.x * 16);
            if (__all(g[0] == n[0] && g[1] == n[1])) {
                if (!sent) { if (lane_id() == 0) st32(rs, 0, HS_D + blockIdx.x * 4, dv); sent = true; }
                par = g[2] ^ n[0] ^ HS_KEY;
                if (__all(par < 2u && g[3] == ~n[1])) break;
            }
            __builtin_amdgcn_s_sleep(2);
        }
        *(unsigned*)smem = par & 1u;
    }
    RAWBAR()
    const unsigned par = __builtin_amdgcn_readfirstlane(*(const unsigned*)smem);
    RAWBAR()
    return par * 0x2000u;
}
template <typename F>
__device__ __forceinline__ void start_master(const Params& p, F needed  ) {
    const rsrc_t rs = __builtin_amdgcn_make_buffer_rsrc((void*)wsp(p), 0, 0x7fffffff, 0x00020000);
    const int l = lane_id();
    st128(rs, l * 16, HS_D, (u32x4){0u, 0u, 0u, 0u});
    WAITV0();
    u32x2 last[4] = {{0u, 0u}, {0u, 0u}, {0u, 0u}, {0u, 0u}};
    int go = 0;
    unsigned par = 0u;
    for (unsigned sp = 0; sp < SPIN_CAP; ++sp) {
        MEMB();
        unsigned d[4];
        u32x2 n[4];
#pragma unroll
        for (int i = 0; i < 4; ++i) d[i] = ld32(rs, l * 4 + i * 256, HS_D);
#pragma unroll
        for (int i = 0; i < 4; ++i) n[i] = __builtin_amdgcn_raw_buffer_load_b64(rs, l * 8 + i * 512, HS_N, AUX_SC1);
        bool ini = true, all = true, chg = sp == 0, odd = false;
#pragma unroll
        for (int i = 0; i < 4; ++i) {
            const int q = needed(l + i * 64);
            ini = ini && (!q || d[i]); all = all && d[i]; odd = odd || (q == 2 && (d[i] & 1u));
            chg = chg || n[i][0] != last[i][0] || n[i][1] != last[i][1];
            last[i] = n[i];
        }
        if (!go && __all(ini)) { go = 1; par = __any(odd) ? 1u : 0u; chg = true; }
        if (__any(chg)) {
#pragma unroll
            for (int i = 0; i < 4; ++i) st128(rs, l * 16 + i * 1024, HS_G, (u32x4){n[i][0], n[i][1], go ? n[i][0] ^ HS_KEY ^ par : ~n[i][0], go ? ~n[i][1] : n[i][1]});
        }
        if (__all(all)) break;
        if (go) __builtin_amdgcn_s_sleep(8); else __builtin_amdgcn_s_sleep(2);
    }
    WAITV0();
}

#define SU(k)
#define TDECL(c)
#define TDECLL(c, off)
#define STAMP(k)
#define STFLUSH(id, it)
#define DBGDECL(bit, c, off)

__device__ __forceinline__ unsigned hot_page(const unsigned ns, const int pg) { const unsigned a = OFF_HOT + pg * 2097152u; return a + (((a >> 8) ^ ns) & 0x2000u); }
__device__ __forceinline__ unsigned calibrate(const Params& p) {
    const int tid = lane_id();
    const rsrc_t rs = __builtin_amdgcn_make_buffer_rsrc((void*)wsp(p), 0, 0x7fffffff, 0x00020000);
    const unsigned base = OFF_HOT + 4194304 + HB_END;
#pragma unroll
    for (int i = 0; i < 16; ++i) st128(rs, tid * 16, base + i * 2048, (u32x4){0u, 0u, 0u, 0u});
    WAITV0();
    unsigned best[2] = {~0u, ~0u};
#pragma unroll 1
    for (int i = 0; i < 32 && (i < NCAL || abs((int)(best[0] - best[1])) < 150 || max(best[0], best[1]) > 1000u); ++i) {
#pragma unroll
        for (int k = 0; k < 2; ++k) {
            const unsigned t0 = (unsigned)__builtin_amdgcn_s_memtime();
            unsigned v = ld32(rs, 0, base + k * 8192 + (i & 3) * 2048 + ((i >> 2) & 1) * 16384 + (i >> 3) * 128);
            WAITV0();
            asm volatile("" : "+v"(v));
            best[k] = min(best[k], (unsigned)__builtin_amdgcn_s_memtime() - t0);
        }
    }
    return ((best[1] < best[0] ? 0u : 1u) ^ (base >> 21)) & 1u;
}

#define QKM_ASM \
    "s_mov_b32 s32, 0\n" \
    "s_mov_b32 s34, 0\n" \
    "s_mov_b32 s35, 1\n" \
    "s_mov_b32 s47, 0x40000\n" \
    "s_mov_b32 s45, 0\n" \
    "s_memrealtime s[50:51]\n" \
    "s_waitcnt lgkmcnt(0)\n" \
    "s_sub_u32 s48, s50, 1000000\n" \
    "s_mov_b32 s49, s48\n" \
    "s_cmp_gt_u32 s16, 3\n" \
    "s_cbranch_scc1 .Lqkm_w4_%=\n" \
    "s_cmp_eq_u32 s16, 3\n" \
    "s_cbranch_scc1 .Lqkm_w3_%=\n" \
    "s_cmp_eq_u32 s16, 2\n" \
    "s_cbranch_scc1 .Lqkm_w2_%=\n" \
    "s_cmp_eq_u32 s16, 1\n" \
    "s_cbranch_scc1 .Lqkm_w1_%=\n" \
    ".Lqkm_w0_%=:\n" \
    "s_mov_b32 s39, 139264\n" \
    "s_mov_b32 s52, 0xff00ff00\n" \
    "s_mov_b32 s53, 0xff00ff00\n" \
    ".Lqkm_l0_%=:\n" \
    "ds_read_b128 v[240:243], v192 offset:16384\n" \
    "ds_read_b128 v[244:247], v192 offset:19456\n" \
    "ds_read_b128 v[248:251], v192 offset:17408\n" \
    "ds_read_b128 v[252:255], v192 offset:20480\n" \
    "v_mov_b64 v[204:205], 0\n" \
    "v_mov_b64 v[206:207], 0\n" \
    "v_mov_b64 v[208:209], 0\n" \
    "v_mov_b64 v[210:211], 0\n" \
    "v_mov_b64 v[212:213], 0\n" \
    "v_mov_b64 v[214:215], 0\n" \
    "s_barrier\n" \
    "ds_read_b128 v[216:219], v192 offset:0\n" \
    "ds_read_b128 v[220:223], v192 offset:1024\n" \
    "ds_read_b128 v[232:235], v192 offset:8192\n" \
    "ds_read_b128 v[224:227], v192 offset:2048\n" \
    "s_xor_b32 s39, s39, 253952\n" \
    "v_add_u32 v200, s39, v192\n" \
    "s_mov_b32 s33, s34\n" \
    "s_add_u32 s37, s32, 1\n" \
    "s_sub_i32 s36, s37, %[lag]\n" \
    "s_bitcmp1_b32 s36, 0\n" \
    "s_cselect_b32 s34, 128, 2*%[lag]\n" \
    "s_add_i32 s34, s34, s36\n" \
    "s_lshr_b32 s34, s34, 1\n" \
    "s_cmp_gt_u32 s36, 127-2*%[lag]\n" \
    "s_cselect_b32 s34, s37, s34\n" \
    "s_lshr_b32 s36, s33, 6\n" \
    "s_lshl_b32 s36, s36, 2\n" \
    "s_and_b32 s37, s33, 3\n" \
    "s_or_b32 s36, s36, s37\n" \
    "s_waitcnt lgkmcnt(3)\n" \
    "v_smfmac_f32_16x16x64_bf16 v[204:207], v[216:219], v[0:7], v193\n" \
    "ds_read_b128 v[228:231], v192 offset:3072\n" \
    "v_smfmac_f32_16x16x64_bf16 v[208:211], v[216:219], v[64:71], v193\n" \
    "v_smfmac_f32_16x16x64_bf16 v[212:215], v[216:219], v[128:135], v193\n" \
    "s_waitcnt lgkmcnt(3)\n" \
    "v_smfmac_f32_16x16x64_bf16 v[204:207], v[220:223], v[8:15], v193\n" \
    "ds_read_b128 v[216:219], v192 offset:4096\n" \
    "v_smfmac_f32_16x16x64_bf16 v[208:211], v[220:223], v[72:79], v193\n" \
    "ds_read_b128 v[236:239], v192 offset:9216\n" \
    "v_smfmac_f32_16x16x64_bf16 v[212:215], v[220:223], v[136:143], v193\n" \
    "s_waitcnt lgkmcnt(4)\n" \
    "v_smfmac_f32_16x16x64_bf16 v[204:207], v[232:235], v[240:247], v193\n" \
    "s_waitcnt lgkmcnt(3)\n" \
    "v_smfmac_f32_16x16x64_bf16 v[204:207], v[224:227], v[16:23], v193\n" \
    "ds_read_b128 v[220:223], v192 offset:5120\n" \
    "v_smfmac_f32_16x16x64_bf16 v[208:211], v[224:227], v[80:87], v193\n" \
    "ds_read_b128 v[240:243], v192 offset:18432\n" \
    "v_smfmac_f32_16x16x64_bf16 v[212:215], v[224:227], v[144:151], v193\n" \
    "ds_read_b128 v[244:247], v192 offset:21504\n" \
    "v_smfmac_f32_16x16x64_bf16 v[208:211], v[232:235], v[248:255], v193\n" \
    "s_waitcnt lgkmcnt(5)\n" \
    "v_smfmac_f32_16x16x64_bf16 v[204:207], v[228:231], v[24:31], v193\n" \
    "ds_read_b128 v[224:227], v192 offset:6144\n" \
    "v_smfmac_f32_16x16x64_bf16 v[208:211], v[228:231], v[88:95], v193\n" \
    "ds_read_b128 v[248:251], v192 offset:22528\n" \
    "v_smfmac_f32_16x16x64_bf16 v[212:215], v[228:231], v[152:159], v193\n" \
    "ds_read_b128 v[252:255], v192 offset:25600\n" \
    "s_waitcnt lgkmcnt(7)\n" \
    "v_smfmac_f32_16x16x64_bf16 v[204:207], v[216:219], v[32:39], v193\n" \
    "ds_read_b128 v[228:231], v192 offset:7168\n" \
    "v_smfmac_f32_16x16x64_bf16 v[208:211], v[216:219], v[96:103], v193\n" \
    "v_smfmac_f32_16x16x64_bf16 v[212:215], v[216:219], v[160:167], v193\n" \
    "s_waitcnt lgkmcnt(4)\n" \
    "v_smfmac_f32_16x16x64_bf16 v[212:215], v[232:235], v[240:247], v193\n" \
    "s_waitcnt lgkmcnt(1)\n" \
    "v_smfmac_f32_16x16x64_bf16 v[204:207], v[236:239], v[248:255], v193\n" \
    "ds_read_b128 v[240:243], v192 offset:23552\n" \
    "v_smfmac_f32_16x16x64_bf16 v[204:207], v[220:223], v[40:47], v193\n" \
    "ds_read_b128 v[244:247], v192 offset:26624\n" \
    "v_smfmac_f32_16x16x64_bf16 v[208:211], v[220:223], v[104:111], v193\n" \
    "ds_read_b128 v[248:251], v192 offset:24576\n" \
    "v_smfmac_f32_16x16x64_bf16 v[212:215], v[220:223], v[168:175], v193\n" \
    "ds_read_b128 v[252:255], v192 offset:27648\n" \
    "v_smfmac_f32_16x16x64_bf16 v[204:207], v[224:227], v[48:55], v193\n" \
    "v_smfmac_f32_16x16x64_bf16 v[208:211], v[224:227], v[112:119], v193\n" \
    "v_smfmac_f32_16x16x64_bf16 v[212:215], v[224:227], v[176:183], v193\n" \
    "s_waitcnt lgkmcnt(4)\n" \
    "v_smfmac_f32_16x16x64_bf16 v[204:207], v[228:231], v[56:63], v193\n" \
    "s_waitcnt lgkmcnt(2)\n" \
    "v_smfmac_f32_16x16x64_bf16 v[208:211], v[236:239], v[240:247], v193\n" \
    "v_smfmac_f32_16x16x64_bf16 v[208:211], v[228:231], v[120:127], v193\n" \
    "s_waitcnt lgkmcnt(0)\n" \
    "v_smfmac_f32_16x16x64_bf16 v[212:215], v[236:239], v[248:255], v193\n" \
    "v_smfmac_f32_16x16x64_bf16 v[212:215], v[228:231], v[184:191], v193\n" \
    "ds_read_b128 v[216:219], v200 offset:13312\n" \
    "ds_read_b128 v[220:223], v200 offset:14336\n" \
    "ds_read_b128 v[224:227], v200 offset:15360\n" \
    "ds_read_b128 v[228:231], v200 offset:16384\n" \
    "s_lshl_b32 s36, s36, s18\n" \
    "s_add_u32 s38, s17, s36\n" \
    "s_add_u32 s32, s32, 1\n" \
    "v_pk_add_f32 v[204:205], v[204:205], v[206:207]\n" \
    "v_pk_add_f32 v[208:209], v[208:209], v[210:211]\n" \
    "v_pk_add_f32 v[212:213], v[212:213], v[214:215]\n" \
    "v_cvt_pk_bf16_f32 v204, v204, v205\n" \
    "v_cvt_pk_bf16_f32 v208, v208, v209\n" \
    "ds_write2st64_b32 v194, v204, v208 offset1:1\n" \
    "v_cvt_pk_bf16_f32 v212, v212, v213\n" \
    "ds_write_b32 v195, v212\n" \
    "ds_read_b128 v[212:215], v200 offset:12288\n" \
    "s_waitcnt lgkmcnt(1)\n" \
    "s_barrier\n" \
    "ds_read_b64_tr_b16 v[232:233], v196 offset:10240\n" \
    "ds_read_b64_tr_b16 v[234:235], v196 offset:10496\n" \
    "ds_read_b64_tr_b16 v[236:237], v196 offset:10752\n" \
    "ds_read_b64_tr_b16 v[238:239], v196 offset:11008\n" \
    "ds_read_b64_tr_b16 v[240:241], v196 offset:11264\n" \
    "ds_read_b64_tr_b16 v[242:243], v196 offset:11520\n" \
    "ds_read_b64_tr_b16 v[244:245], v196 offset:11776\n" \
    "ds_read_b64_tr_b16 v[246:247], v196 offset:12032\n" \
    "ds_read_b64_tr_b16 v[248:249], v196 offset:12288\n" \
    "ds_read_b64_tr_b16 v[250:251], v196 offset:12544\n" \
    "ds_read_b64_tr_b16 v[252:253], v196 offset:12800\n" \
    "ds_read_b64_tr_b16 v[254:255], v196 offset:13056\n" \
    "s_waitcnt lgkmcnt(10)\n" \
    "v_mfma_f32_16x16x32_bf16 v[204:207], v[212:215], v[232:235], 0\n" \
    "ds_read_b128 v[212:215], v200 offset:17408\n" \
    "s_waitcnt lgkmcnt(9)\n" \
    "v_mfma_f32_16x16x32_bf16 v[204:207], v[216:219], v[236:239], v[204:207]\n" \
    "ds_read_b128 v[216:219], v200 offset:18432\n" \
    "s_waitcnt lgkmcnt(8)\n" \
    "v_mfma_f32_16x16x32_bf16 v[204:207], v[220:223], v[240:243], v[204:207]\n" \
    "ds_read_b128 v[220:223], v200 offset:19456\n" \
    "s_waitcnt lgkmcnt(7)\n" \
    "v_mfma_f32_16x16x32_bf16 v[204:207], v[224:227], v[244:247], v[204:207]\n" \
    "ds_read_b128 v[224:227], v200 offset:20480\n" \
    "s_waitcnt lgkmcnt(6)\n" \
    "v_mfma_f32_16x16x32_bf16 v[204:207], v[228:231], v[248:251], v[204:207]\n" \
    "ds_read_b128 v[228:231], v200 offset:21504\n" \
    "s_waitcnt lgkmcnt(4)\n" \
    "v_mfma_f32_16x16x32_bf16 v[204:207], v[212:215], v[252:255], v[204:207]\n" \
    "ds_read_b128 v[212:215], v200 offset:22528\n" \
    "s_waitcnt lgkmcnt(4)\n" \
    "v_mfma_f32_16x16x32_bf16 v[208:211], v[216:219], v[232:235], 0\n" \
    "ds_read_b128 v[216:219], v200 offset:23552\n" \
    "s_waitcnt lgkmcnt(4)\n" \
    "v_mfma_f32_16x16x32_bf16 v[208:211], v[220:223], v[236:239], v[208:211]\n" \
    "ds_read_b64_tr_b16 v[220:221], v196 offset:13312\n" \
    "ds_read_b64_tr_b16 v[222:223], v196 offset:13568\n" \
    "s_waitcnt lgkmcnt(5)\n" \
    "v_mfma_f32_16x16x32_bf16 v[208:211], v[224:227], v[240:243], v[208:211]\n" \
    "ds_read_b64_tr_b16 v[224:225], v196 offset:13824\n" \
    "ds_read_b64_tr_b16 v[226:227], v196 offset:14080\n" \
    "s_waitcnt lgkmcnt(6)\n" \
    "v_mfma_f32_16x16x32_bf16 v[208:211], v[228:231], v[244:247], v[208:211]\n" \
    "ds_read_b64_tr_b16 v[228:229], v196 offset:14336\n" \
    "ds_read_b64_tr_b16 v[230:231], v196 offset:14592\n" \
    "s_waitcnt lgkmcnt(7)\n" \
    "v_mfma_f32_16x16x32_bf16 v[208:211], v[212:215], v[248:251], v[208:211]\n" \
    "ds_read_b64_tr_b16 v[212:213], v196 offset:14848\n" \
    "ds_read_b64_tr_b16 v[214:215], v196 offset:15104\n" \
    "s_waitcnt lgkmcnt(8)\n" \
    "v_mfma_f32_16x16x32_bf16 v[208:211], v[216:219], v[252:255], v[208:211]\n" \
    "ds_read_b64_tr_b16 v[216:217], v196 offset:15360\n" \
    "ds_read_b64_tr_b16 v[218:219], v196 offset:15616\n" \
    "s_waitcnt lgkmcnt(8)\n" \
    "v_mfma_f32_16x16x32_bf16 v[232:235], v[220:223], v[232:235], 0\n" \
    "ds_read_b64_tr_b16 v[220:221], v196 offset:15872\n" \
    "ds_read_b64_tr_b16 v[222:223], v196 offset:16128\n" \
    "s_nop 1\n" \
    "s_waitcnt lgkmcnt(8)\n" \
    "v_mfma_f32_16x16x32_bf16 v[232:235], v[224:227], v[236:239], v[232:235]\n" \
    "s_nop 1\n" \
    "s_waitcnt lgkmcnt(6)\n" \
    "v_mfma_f32_16x16x32_bf16 v[232:235], v[228:231], v[240:243], v[232:235]\n" \
    "v_cndmask_b32 v208, v204, v208, s[52:53]\n" \
    "v_cndmask_b32 v209, v205, v209, s[52:53]\n" \
    "v_cndmask_b32 v210, v206, v210, s[52:53]\n" \
    "s_waitcnt lgkmcnt(4)\n" \
    "v_mfma_f32_16x16x32_bf16 v[232:235], v[212:215], v[244:247], v[232:235]\n" \
    "v_cndmask_b32 v211, v207, v211, s[52:53]\n" \
    "v_max3_f32 v200, v208, v209, v210\n" \
    "v_max_f32 v200, v200, v211\n" \
    "s_waitcnt lgkmcnt(2)\n" \
    "v_mfma_f32_16x16x32_bf16 v[232:235], v[216:219], v[248:251], v[232:235]\n" \
    "v_max_f32_dpp v201, v200, v200 row_ror:8 row_mask:0xf bank_mask:0xf bound_ctrl:1\n" \
    "v_max_f32_dpp v203, v200, v200 row_ror:8 row_mask:0xf bank_mask:0xf bound_ctrl:1\n" \
    "s_nop 1\n" \
    "v_permlane16_swap_b32 v201, v203\n" \
    "s_waitcnt lgkmcnt(0)\n" \
    "v_mfma_f32_16x16x32_bf16 v[232:235], v[220:223], v[252:255], v[232:235]\n" \
    "v_max_f32 v200, v201, v203\n" \
    "v_max_f32 v201, v201, v203\n" \
    "s_nop 1\n" \
    "v_permlane32_swap_b32 v201, v200\n" \
    "v_max_f32 v201, v201, v200\n" \
    "s_nop 1\n" \
    "v_max3_f32 v236, v232, v233, v201\n" \
    "v_max3_f32 v237, v236, v234, v235\n" \
    "v_max3_f32 v236, v236, v234, v235\n" \
    "s_nop 1\n" \
    "v_permlane16_swap_b32 v236, v237\n" \
    "v_max_f32 v236, v236, v237\n" \
    "v_mul_f32 v236, 0xbdd53b94, v236\n" \
    "v_fmamk_f32 v208, v208, 0x3dd53b94, v236\n" \
    "v_fmamk_f32 v209, v209, 0x3dd53b94, v236\n" \
    "v_fmamk_f32 v210, v210, 0x3dd53b94, v236\n" \
    "v_fmamk_f32 v211, v211, 0x3dd53b94, v236\n" \
    "v_fmamk_f32 v232, v232, 0x3dd53b94, v236\n" \
    "v_fmamk_f32 v233, v233, 0x3dd53b94, v236\n" \
    "v_fmamk_f32 v234, v234, 0x3dd53b94, v236\n" \
    "v_fmamk_f32 v235, v235, 0x3dd53b94, v236\n" \
    "v_exp_f32 v208, v208\n" \
    "v_exp_f32 v209, v209\n" \
    "v_exp_f32 v210, v210\n" \
    "v_exp_f32 v211, v211\n" \
    "v_exp_f32 v232, v232\n" \
    "v_add_f32 v200, v208, v209\n" \
    "v_add_f32 v203, v210, v211\n" \
    "v_exp_f32 v233, v233\n" \
    "v_add_f32 v200, v200, v203\n" \
    "v_exp_f32 v234, v234\n" \
    "v_exp_f32 v235, v235\n" \
    "v_add_f32_dpp v202, v200, v200 row_ror:8 row_mask:0xf bank_mask:0xf bound_ctrl:1\n" \
    "v_add_f32_dpp v203, v200, v200 row_ror:8 row_mask:0xf bank_mask:0xf bound_ctrl:1\n" \
    "v_add_f32 v236, v232, v233\n" \
    "v_add_f32 v237, v234, v235\n" \
    "v_permlane32_swap_b32 v202, v203\n" \
    "v_add_f32 v236, v236, v202\n" \
    "v_add_f32 v237, v237, v203\n" \
    "v_add_f32 v238, v236, v237\n" \
    "v_add_f32 v236, v236, v237\n" \
    "s_nop 1\n" \
    "v_permlane16_swap_b32 v236, v238\n" \
    "v_add_f32 v236, v236, v238\n" \
    "v_rcp_f32 v236, v236\n" \
    "s_nop 0\n" \
    "v_pk_mul_f32 v[208:209], v[208:209], v[236:237] op_sel_hi:[1,0]\n" \
    "v_pk_mul_f32 v[210:211], v[210:211], v[236:237] op_sel_hi:[1,0]\n" \
    "v_pk_mul_f32 v[232:233], v[232:233], v[236:237] op_sel_hi:[1,0]\n" \
    "v_pk_mul_f32 v[234:235], v[234:235], v[236:237] op_sel_hi:[1,0]\n" \
    "v_cvt_pk_bf16_f32 v216, v208, v209\n" \
    "v_cvt_pk_bf16_f32 v217, v210, v211\n" \
    "buffer_store_dwordx2 v[216:217], v197, s[8:11], s38 offen sc1\n" \
    "v_cvt_pk_bf16_f32 v220, v232, v233\n" \
    "v_cvt_pk_bf16_f32 v221, v234, v235\n" \
    "buffer_store_dwordx2 v[220:221], v198, s[8:11], s38 offen sc1\n" \
    "s_cmp_lt_u32 s32, 128\n" \
    "s_cbranch_scc1 .Lqkm_l0_%=\n" \
    "s_branch .Lqkm_end_%=\n" \
    ".Lqkm_w1_%=:\n" \
    "s_mov_b32 s39, 139264\n" \
    "v_bfe_u32 v201, v192, 4, 4\n" \
    "v_lshrrev_b32 v202, 6, v192\n" \
    "v_and_b32 v202, 12, v202\n" \
    "v_sub_u32 v201, v201, v202\n" \
    "v_cmp_gt_i32 s[54:55], 0, v201\n" \
    "v_cmp_gt_i32 s[56:57], 1, v201\n" \
    "v_cmp_gt_i32 s[58:59], 2, v201\n" \
    "v_cmp_gt_i32 s[60:61], 3, v201\n" \
    ".Lqkm_l1_%=:\n" \
    "ds_read_b128 v[240:243], v192 offset:28672\n" \
    "ds_read_b128 v[244:247], v192 offset:31744\n" \
    "ds_read_b128 v[248:251], v192 offset:29696\n" \
    "ds_read_b128 v[252:255], v192 offset:32768\n" \
    "v_mov_b64 v[204:205], 0\n" \
    "v_mov_b64 v[206:207], 0\n" \
    "v_mov_b64 v[208:209], 0\n" \
    "v_mov_b64 v[210:211], 0\n" \
    "v_mov_b64 v[212:213], 0\n" \
    "v_mov_b64 v[214:215], 0\n" \
    "s_barrier\n" \
    "ds_read_b128 v[216:219], v192 offset:0\n" \
    "ds_read_b128 v[220:223], v192 offset:1024\n" \
    "ds_read_b128 v[232:235], v192 offset:8192\n" \
    "ds_read_b128 v[224:227], v192 offset:2048\n" \
    "s_xor_b32 s39, s39, 253952\n" \
    "v_add_u32 v200, s39, v192\n" \
    "v_mov_b32 v203, 0xf149f2ca\n" \
    "s_mov_b32 s33, s34\n" \
    "s_add_u32 s37, s32, 1\n" \
    "s_sub_i32 s36, s37, %[lag]\n" \
    "s_bitcmp1_b32 s36, 0\n" \
    "s_cselect_b32 s34, 128, 2*%[lag]\n" \
    "s_add_i32 s34, s34, s36\n" \
    "s_lshr_b32 s34, s34, 1\n" \
    "s_cmp_gt_u32 s36, 127-2*%[lag]\n" \
    "s_cselect_b32 s34, s37, s34\n" \
    "s_lshr_b32 s36, s33, 6\n" \
    "s_lshl_b32 s36, s36, 2\n" \
    "s_and_b32 s37, s33, 3\n" \
    "s_or_b32 s36, s36, s37\n" \
    "s_waitcnt lgkmcnt(3)\n" \
    "v_smfmac_f32_16x16x64_bf16 v[204:207], v[216:219], v[0:7], v193\n" \
    "ds_read_b128 v[228:231], v192 offset:3072\n" \
    "v_smfmac_f32_16x16x64_bf16 v[208:211], v[216:219], v[64:71], v193\n" \
    "v_smfmac_f32_16x16x64_bf16 v[212:215], v[216:219], v[128:135], v193\n" \
    "s_waitcnt lgkmcnt(3)\n" \
    "v_smfmac_f32_16x16x64_bf16 v[204:207], v[220:223], v[8:15], v193\n" \
    "ds_read_b128 v[216:219], v192 offset:4096\n" \
    "v_smfmac_f32_16x16x64_bf16 v[208:211], v[220:223], v[72:79], v193\n" \
    "ds_read_b128 v[236:239], v192 offset:9216\n" \
    "v_smfmac_f32_16x16x64_bf16 v[212:215], v[220:223], v[136:143], v193\n" \
    "s_waitcnt lgkmcnt(4)\n" \
    "v_smfmac_f32_16x16x64_bf16 v[204:207], v[232:235], v[240:247], v193\n" \
    "s_waitcnt lgkmcnt(3)\n" \
    "v_smfmac_f32_16x16x64_bf16 v[204:207], v[224:227], v[16:23], v193\n" \
    "ds_read_b128 v[220:223], v192 offset:5120\n" \
    "v_smfmac_f32_16x16x64_bf16 v[208:211], v[224:227], v[80:87], v193\n" \
    "ds_read_b128 v[240:243], v192 offset:30720\n" \
    "v_smfmac_f32_16x16x64_bf16 v[212:215], v[224:227], v[144:151], v193\n" \
    "ds_read_b128 v[244:247], v192 offset:33792\n" \
    "v_smfmac_f32_16x16x64_bf16 v[208:211], v[232:235], v[248:255], v193\n" \
    "s_waitcnt lgkmcnt(5)\n" \
    "v_smfmac_f32_16x16x64_bf16 v[204:207], v[228:231], v[24:31], v193\n" \
    "ds_read_b128 v[224:227], v192 offset:6144\n" \
    "v_smfmac_f32_16x16x64_bf16 v[208:211], v[228:231], v[88:95], v193\n" \
    "ds_read_b128 v[248:251], v192 offset:34816\n" \
    "v_smfmac_f32_16x16x64_bf16 v[212:215], v[228:231], v[152:159], v193\n" \
    "ds_read_b128 v[252:255], v192 offset:37888\n" \
    "s_waitcnt lgkmcnt(7)\n" \
    "v_smfmac_f32_16x16x64_bf16 v[204:207], v[216:219], v[32:39], v193\n" \
    "ds_read_b128 v[228:231], v192 offset:7168\n" \
    "v_smfmac_f32_16x16x64_bf16 v[208:211], v[216:219], v[96:103], v193\n" \
    "v_smfmac_f32_16x16x64_bf16 v[212:215], v[216:219], v[160:167], v193\n" \
    "s_waitcnt lgkmcnt(4)\n" \
    "v_smfmac_f32_16x16x64_bf16 v[212:215], v[232:235], v[240:247], v193\n" \
    "s_waitcnt lgkmcnt(1)\n" \
    "v_smfmac_f32_16x16x64_bf16 v[204:207], v[236:239], v[248:255], v193\n" \
    "ds_read_b128 v[240:243], v192 offset:35840\n" \
    "v_smfmac_f32_16x16x64_bf16 v[204:207], v[220:223], v[40:47], v193\n" \
    "ds_read_b128 v[244:247], v192 offset:38912\n" \
    "v_smfmac_f32_16x16x64_bf16 v[208:211], v[220:223], v[104:111], v193\n" \
    "ds_read_b128 v[248:251], v192 offset:36864\n" \
    "v_smfmac_f32_16x16x64_bf16 v[212:215], v[220:223], v[168:175], v193\n" \
    "ds_read_b128 v[252:255], v192 offset:39936\n" \
    "v_smfmac_f32_16x16x64_bf16 v[204:207], v[224:227], v[48:55], v193\n" \
    "v_smfmac_f32_16x16x64_bf16 v[208:211], v[224:227], v[112:119], v193\n" \
    "v_smfmac_f32_16x16x64_bf16 v[212:215], v[224:227], v[176:183], v193\n" \
    "s_waitcnt lgkmcnt(4)\n" \
    "v_smfmac_f32_16x16x64_bf16 v[204:207], v[228:231], v[56:63], v193\n" \
    "s_waitcnt lgkmcnt(2)\n" \
    "v_smfmac_f32_16x16x64_bf16 v[208:211], v[236:239], v[240:247], v193\n" \
    "v_smfmac_f32_16x16x64_bf16 v[208:211], v[228:231], v[120:127], v193\n" \
    "s_waitcnt lgkmcnt(0)\n" \
    "v_smfmac_f32_16x16x64_bf16 v[212:215], v[236:239], v[248:255], v193\n" \
    "v_smfmac_f32_16x16x64_bf16 v[212:215], v[228:231], v[184:191], v193\n" \
    "ds_read_b128 v[216:219], v200 offset:13312\n" \
    "ds_read_b128 v[220:223], v200 offset:14336\n" \
    "ds_read_b128 v[224:227], v200 offset:15360\n" \
    "ds_read_b128 v[228:231], v200 offset:16384\n" \
    "ds_read_b128 v[232:235], v200 offset:0\n" \
    "ds_read_b128 v[236:239], v200 offset:1024\n" \
    "ds_read_b128 v[240:243], v200 offset:2048\n" \
    "ds_read_b128 v[244:247], v200 offset:3072\n" \
    "ds_read_b128 v[248:251], v200 offset:4096\n" \
    "ds_read_b128 v[252:255], v200 offset:5120\n" \
    "s_lshl_b32 s36, s36, s18\n" \
    "s_add_u32 s38, s17, s36\n" \
    "s_add_u32 s32, s32, 1\n" \
    "v_pk_add_f32 v[204:205], v[204:205], v[206:207]\n" \
    "v_pk_add_f32 v[208:209], v[208:209], v[210:211]\n" \
    "v_pk_add_f32 v[212:213], v[212:213], v[214:215]\n" \
    "v_cvt_pk_bf16_f32 v204, v204, v205\n" \
    "v_cvt_pk_bf16_f32 v208, v208, v209\n" \
    "ds_write2st64_b32 v194, v204, v208 offset1:1\n" \
    "v_cvt_pk_bf16_f32 v212, v212, v213\n" \
    "ds_write_b32 v195, v212\n" \
    "ds_read_b128 v[212:215], v200 offset:12288\n" \
    "s_waitcnt lgkmcnt(1)\n" \
    "s_barrier\n" \
    "v_cndmask_b32 v208, 0, v203, s[54:55]\n" \
    "v_cndmask_b32 v209, 0, v203, s[56:57]\n" \
    "v_cndmask_b32 v210, 0, v203, s[58:59]\n" \
    "v_cndmask_b32 v211, 0, v203, s[60:61]\n" \
    "s_waitcnt lgkmcnt(0)\n" \
    "s_nop 0\n" \
    "v_mfma_f32_16x16x32_bf16 v[208:211], v[212:215], v[232:235], v[208:211]\n" \
    "ds_read_b128 v[212:215], v200 offset:17408\n" \
    "v_mfma_f32_16x16x32_bf16 v[208:211], v[216:219], v[236:239], v[208:211]\n" \
    "ds_read_b64_tr_b16 v[216:217], v196 offset:13312\n" \
    "ds_read_b64_tr_b16 v[218:219], v196 offset:13568\n" \
    "v_mfma_f32_16x16x32_bf16 v[208:211], v[220:223], v[240:243], v[208:211]\n" \
    "ds_read_b64_tr_b16 v[220:221], v196 offset:13824\n" \
    "ds_read_b64_tr_b16 v[222:223], v196 offset:14080\n" \
    "v_mfma_f32_16x16x32_bf16 v[208:211], v[224:227], v[244:247], v[208:211]\n" \
    "ds_read_b64_tr_b16 v[224:225], v196 offset:14336\n" \
    "ds_read_b64_tr_b16 v[226:227], v196 offset:14592\n" \
    "v_mfma_f32_16x16x32_bf16 v[208:211], v[228:231], v[248:251], v[208:211]\n" \
    "ds_read_b64_tr_b16 v[228:229], v196 offset:14848\n" \
    "ds_read_b64_tr_b16 v[230:231], v196 offset:15104\n" \
    "s_waitcnt lgkmcnt(8)\n" \
    "v_mfma_f32_16x16x32_bf16 v[208:211], v[212:215], v[252:255], v[208:211]\n" \
    "ds_read_b64_tr_b16 v[212:213], v196 offset:15360\n" \
    "ds_read_b64_tr_b16 v[214:215], v196 offset:15616\n" \
    "s_waitcnt lgkmcnt(8)\n" \
    "v_mfma_f32_16x16x32_bf16 v[204:207], v[216:219], v[232:235], 0\n" \
    "ds_read_b64_tr_b16 v[216:217], v196 offset:15872\n" \
    "ds_read_b64_tr_b16 v[218:219], v196 offset:16128\n" \
    "s_nop 1\n" \
    "s_waitcnt lgkmcnt(8)\n" \
    "v_mfma_f32_16x16x32_bf16 v[204:207], v[220:223], v[236:239], v[204:207]\n" \
    "s_nop 1\n" \
    "s_waitcnt lgkmcnt(6)\n" \
    "v_mfma_f32_16x16x32_bf16 v[204:207], v[224:227], v[240:243], v[204:207]\n" \
    "v_max3_f32 v200, v208, v209, v210\n" \
    "v_max_f32 v201, v200, v211\n" \
    "v_max_f32 v203, v200, v211\n" \
    "s_waitcnt lgkmcnt(4)\n" \
    "v_mfma_f32_16x16x32_bf16 v[204:207], v[228:231], v[244:247], v[204:207]\n" \
    "v_permlane16_swap_b32 v201, v203\n" \
    "v_max_f32 v200, v201, v203\n" \
    "v_max_f32 v201, v201, v203\n" \
    "s_waitcnt lgkmcnt(2)\n" \
    "v_mfma_f32_16x16x32_bf16 v[204:207], v[212:215], v[248:251], v[204:207]\n" \
    "v_permlane32_swap_b32 v201, v200\n" \
    "v_max_f32 v201, v201, v200\n" \
    "s_waitcnt lgkmcnt(0)\n" \
    "v_mfma_f32_16x16x32_bf16 v[204:207], v[216:219], v[252:255], v[204:207]\n" \
    "s_nop 7\n" \
    "v_max3_f32 v236, v204, v205, v201\n" \
    "v_max3_f32 v237, v236, v206, v207\n" \
    "v_max3_f32 v236, v236, v206, v207\n" \
    "s_nop 1\n" \
    "v_permlane16_swap_b32 v236, v237\n" \
    "v_max_f32 v236, v236, v237\n" \
    "v_mul_f32 v236, 0xbdd53b94, v236\n" \
    "v_fmamk_f32 v208, v208, 0x3dd53b94, v236\n" \
    "v_fmamk_f32 v209, v209, 0x3dd53b94, v236\n" \
    "v_fmamk_f32 v210, v210, 0x3dd53b94, v236\n" \
    "v_fmamk_f32 v211, v211, 0x3dd53b94, v236\n" \
    "v_fmamk_f32 v204, v204, 0x3dd53b94, v236\n" \
    "v_fmamk_f32 v205, v205, 0x3dd53b94, v236\n" \
    "v_fmamk_f32 v206, v206, 0x3dd53b94, v236\n" \
    "v_fmamk_f32 v207, v207, 0x3dd53b94, v236\n" \
    "v_exp_f32 v208, v208\n" \
    "v_exp_f32 v209, v209\n" \
    "v_exp_f32 v210, v210\n" \
    "v_exp_f32 v211, v211\n" \
    "v_exp_f32 v204, v204\n" \
    "v_add_f32 v200, v208, v209\n" \
    "v_add_f32 v203, v210, v211\n" \
    "v_exp_f32 v205, v205\n" \
    "v_add_f32 v202, v200, v203\n" \
    "v_add_f32 v203, v200, v203\n" \
    "v_exp_f32 v206, v206\n" \
    "v_exp_f32 v207, v207\n" \
    "v_add_f32 v236, v204, v205\n" \
    "v_add_f32 v237, v206, v207\n" \
    "v_permlane32_swap_b32 v202, v203\n" \
    "v_add_f32 v236, v236, v202\n" \
    "v_add_f32 v237, v237, v203\n" \
    "v_add_f32 v238, v236, v237\n" \
    "v_add_f32 v236, v236, v237\n" \
    "s_nop 1\n" \
    "v_permlane16_swap_b32 v236, v238\n" \
    "v_add_f32 v236, v236, v238\n" \
    "v_rcp_f32 v236, v236\n" \
    "s_nop 0\n" \
    "v_pk_mul_f32 v[208:209], v[208:209], v[236:237] op_sel_hi:[1,0]\n" \
    "v_pk_mul_f32 v[210:211], v[210:211], v[236:237] op_sel_hi:[1,0]\n" \
    "v_pk_mul_f32 v[204:205], v[204:205], v[236:237] op_sel_hi:[1,0]\n" \
    "v_pk_mul_f32 v[206:207], v[206:207], v[236:237] op_sel_hi:[1,0]\n" \
    "v_cvt_pk_bf16_f32 v216, v208, v209\n" \
    "v_cvt_pk_bf16_f32 v217, v210, v211\n" \
    "buffer_store_dwordx2 v[216:217], v197, s[8:11], s38 offen sc1\n" \
    "v_cvt_pk_bf16_f32 v220, v204, v205\n" \
    "v_cvt_pk_bf16_f32 v221, v206, v207\n" \
    "buffer_store_dwordx2 v[220:221], v198, s[8:11], s38 offen sc1\n" \
    "s_cmp_lt_u32 s32, 128\n" \
    "s_cbranch_scc1 .Lqkm_l1_%=\n" \
    "s_branch .Lqkm_end_%=\n" \
    ".Lqkm_w2_%=:\n" \
    "s_mov_b32 s39, 139264\n" \
    "v_bfe_u32 v201, v192, 4, 4\n" \
    "v_lshrrev_b32 v202, 6, v192\n" \
    "v_and_b32 v202, 12, v202\n" \
    "v_sub_u32 v201, v201, v202\n" \
    "v_cmp_gt_i32 s[54:55], 0, v201\n" \
    "v_cmp_gt_i32 s[56:57], 1, v201\n" \
    "v_cmp_gt_i32 s[58:59], 2, v201\n" \
    "v_cmp_gt_i32 s[60:61], 3, v201\n" \
    ".Lqkm_l2_%=:\n" \
    "ds_read_b128 v[240:243], v192 offset:40960\n" \
    "ds_read_b128 v[244:247], v192 offset:44032\n" \
    "ds_read_b128 v[248:251], v192 offset:41984\n" \
    "ds_read_b128 v[252:255], v192 offset:45056\n" \
    "v_mov_b64 v[204:205], 0\n" \
    "v_mov_b64 v[206:207], 0\n" \
    "v_mov_b64 v[208:209], 0\n" \
    "v_mov_b64 v[210:211], 0\n" \
    "v_mov_b64 v[212:213], 0\n" \
    "v_mov_b64 v[214:215], 0\n" \
    "s_barrier\n" \
    "ds_read_b128 v[216:219], v192 offset:0\n" \
    "ds_read_b128 v[220:223], v192 offset:1024\n" \
    "ds_read_b128 v[232:235], v192 offset:8192\n" \
    "ds_read_b128 v[224:227], v192 offset:2048\n" \
    "s_xor_b32 s39, s39, 253952\n" \
    "v_add_u32 v200, s39, v192\n" \
    "v_mov_b32 v203, 0xf149f2ca\n" \
    "s_mov_b32 s33, s34\n" \
    "s_add_u32 s37, s32, 1\n" \
    "s_sub_i32 s36, s37, %[lag]\n" \
    "s_bitcmp1_b32 s36, 0\n" \
    "s_cselect_b32 s34, 128, 2*%[lag]\n" \
    "s_add_i32 s34, s34, s36\n" \
    "s_lshr_b32 s34, s34, 1\n" \
    "s_cmp_gt_u32 s36, 127-2*%[lag]\n" \
    "s_cselect_b32 s34, s37, s34\n" \
    "s_lshr_b32 s36, s33, 6\n" \
    "s_lshl_b32 s36, s36, 2\n" \
    "s_and_b32 s37, s33, 3\n" \
    "s_or_b32 s36, s36, s37\n" \
    "s_waitcnt lgkmcnt(3)\n" \
    "v_smfmac_f32_16x16x64_bf16 v[204:207], v[216:219], v[0:7], v193\n" \
    "ds_read_b128 v[228:231], v192 offset:3072\n" \
    "v_smfmac_f32_16x16x64_bf16 v[208:211], v[216:219], v[64:71], v193\n" \
    "v_smfmac_f32_16x16x64_bf16 v[212:215], v[216:219], v[128:135], v193\n" \
    "s_waitcnt lgkmcnt(3)\n" \
    "v_smfmac_f32_16x16x64_bf16 v[204:207], v[220:223], v[8:15], v193\n" \
    "ds_read_b128 v[216:219], v192 offset:4096\n" \
    "v_smfmac_f32_16x16x64_bf16 v[208:211], v[220:223], v[72:79], v193\n" \
    "ds_read_b128 v[236:239], v192 offset:9216\n" \
    "v_smfmac_f32_16x16x64_bf16 v[212:215], v[220:223], v[136:143], v193\n" \
    "s_waitcnt lgkmcnt(4)\n" \
    "v_smfmac_f32_16x16x64_bf16 v[204:207], v[232:235], v[240:247], v193\n" \
    "s_waitcnt lgkmcnt(3)\n" \
    "v_smfmac_f32_16x16x64_bf16 v[204:207], v[224:227], v[16:23], v193\n" \
    "ds_read_b128 v[220:223], v192 offset:5120\n" \
    "v_smfmac_f32_16x16x64_bf16 v[208:211], v[224:227], v[80:87], v193\n" \
    "ds_read_b128 v[240:243], v192 offset:43008\n" \
    "v_smfmac_f32_16x16x64_bf16 v[212:215], v[224:227], v[144:151], v193\n" \
    "ds_read_b128 v[244:247], v192 offset:46080\n" \
    "v_smfmac_f32_16x16x64_bf16 v[208:211], v[232:235], v[248:255], v193\n" \
    "s_waitcnt lgkmcnt(5)\n" \
    "v_smfmac_f32_16x16x64_bf16 v[204:207], v[228:231], v[24:31], v193\n" \
    "ds_read_b128 v[224:227], v192 offset:6144\n" \
    "v_smfmac_f32_16x16x64_bf16 v[208:211], v[228:231], v[88:95], v193\n" \
    "ds_read_b128 v[248:251], v192 offset:47104\n" \
    "v_smfmac_f32_16x16x64_bf16 v[212:215], v[228:231], v[152:159], v193\n" \
    "ds_read_b128 v[252:255], v192 offset:50176\n" \
    "s_waitcnt lgkmcnt(7)\n" \
    "v_smfmac_f32_16x16x64_bf16 v[204:207], v[216:219], v[32:39], v193\n" \
    "ds_read_b128 v[228:231], v192 offset:7168\n" \
    "v_smfmac_f32_16x16x64_bf16 v[208:211], v[216:219], v[96:103], v193\n" \
    "v_smfmac_f32_16x16x64_bf16 v[212:215], v[216:219], v[160:167], v193\n" \
    "s_waitcnt lgkmcnt(4)\n" \
    "v_smfmac_f32_16x16x64_bf16 v[212:215], v[232:235], v[240:247], v193\n" \
    "s_waitcnt lgkmcnt(1)\n" \
    "v_smfmac_f32_16x16x64_bf16 v[204:207], v[236:239], v[248:255], v193\n" \
    "ds_read_b128 v[240:243], v192 offset:48128\n" \
    "v_smfmac_f32_16x16x64_bf16 v[204:207], v[220:223], v[40:47], v193\n" \
    "ds_read_b128 v[244:247], v192 offset:51200\n" \
    "v_smfmac_f32_16x16x64_bf16 v[208:211], v[220:223], v[104:111], v193\n" \
    "ds_read_b128 v[248:251], v192 offset:49152\n" \
    "v_smfmac_f32_16x16x64_bf16 v[212:215], v[220:223], v[168:175], v193\n" \
    "ds_read_b128 v[252:255], v192 offset:52224\n" \
    "v_smfmac_f32_16x16x64_bf16 v[204:207], v[224:227], v[48:55], v193\n" \
    "v_smfmac_f32_16x16x64_bf16 v[208:211], v[224:227], v[112:119], v193\n" \
    "v_smfmac_f32_16x16x64_bf16 v[212:215], v[224:227], v[176:183], v193\n" \
    "s_waitcnt lgkmcnt(4)\n" \
    "v_smfmac_f32_16x16x64_bf16 v[204:207], v[228:231], v[56:63], v193\n" \
    "s_waitcnt lgkmcnt(2)\n" \
    "v_smfmac_f32_16x16x64_bf16 v[208:211], v[236:239], v[240:247], v193\n" \
    "v_smfmac_f32_16x16x64_bf16 v[208:211], v[228:231], v[120:127], v193\n" \
    "s_waitcnt lgkmcnt(0)\n" \
    "v_smfmac_f32_16x16x64_bf16 v[212:215], v[236:239], v[248:255], v193\n" \
    "v_smfmac_f32_16x16x64_bf16 v[212:215], v[228:231], v[184:191], v193\n" \
    "ds_read_b128 v[216:219], v200 offset:13312\n" \
    "ds_read_b128 v[220:223], v200 offset:14336\n" \
    "ds_read_b128 v[224:227], v200 offset:15360\n" \
    "ds_read_b128 v[228:231], v200 offset:16384\n" \
    "ds_read_b128 v[232:235], v200 offset:6144\n" \
    "ds_read_b128 v[236:239], v200 offset:7168\n" \
    "ds_read_b128 v[240:243], v200 offset:8192\n" \
    "ds_read_b128 v[244:247], v200 offset:9216\n" \
    "ds_read_b128 v[248:251], v200 offset:10240\n" \
    "ds_read_b128 v[252:255], v200 offset:11264\n" \
    "s_lshl_b32 s36, s36, s18\n" \
    "s_add_u32 s38, s17, s36\n" \
    "s_add_u32 s32, s32, 1\n" \
    "v_pk_add_f32 v[204:205], v[204:205], v[206:207]\n" \
    "v_pk_add_f32 v[208:209], v[208:209], v[210:211]\n" \
    "v_pk_add_f32 v[212:213], v[212:213], v[214:215]\n" \
    "v_cvt_pk_bf16_f32 v204, v204, v205\n" \
    "v_cvt_pk_bf16_f32 v208, v208, v209\n" \
    "ds_write2st64_b32 v194, v204, v208 offset1:1\n" \
    "v_cvt_pk_bf16_f32 v212, v212, v213\n" \
    "ds_write_b32 v195, v212\n" \
    "ds_read_b128 v[212:215], v200 offset:12288\n" \
    "s_waitcnt lgkmcnt(1)\n" \
    "s_barrier\n" \
    "s_waitcnt lgkmcnt(0)\n" \
    "v_mfma_f32_16x16x32_bf16 v[204:207], v[212:215], v[232:235], 0\n" \
    "ds_read_b128 v[212:215], v200 offset:17408\n" \
    "v_cndmask_b32 v208, 0, v203, s[54:55]\n" \
    "v_mfma_f32_16x16x32_bf16 v[204:207], v[216:219], v[236:239], v[204:207]\n" \
    "ds_read_b128 v[216:219], v200 offset:18432\n" \
    "v_cndmask_b32 v209, 0, v203, s[56:57]\n" \
    "v_mfma_f32_16x16x32_bf16 v[204:207], v[220:223], v[240:243], v[204:207]\n" \
    "ds_read_b128 v[220:223], v200 offset:19456\n" \
    "v_cndmask_b32 v210, 0, v203, s[58:59]\n" \
    "v_mfma_f32_16x16x32_bf16 v[204:207], v[224:227], v[244:247], v[204:207]\n" \
    "ds_read_b128 v[224:227], v200 offset:20480\n" \
    "v_cndmask_b32 v211, 0, v203, s[60:61]\n" \
    "v_mfma_f32_16x16x32_bf16 v[204:207], v[228:231], v[248:251], v[204:207]\n" \
    "ds_read_b128 v[228:231], v200 offset:21504\n" \
    "s_waitcnt lgkmcnt(4)\n" \
    "v_mfma_f32_16x16x32_bf16 v[204:207], v[212:215], v[252:255], v[204:207]\n" \
    "ds_read_b128 v[212:215], v200 offset:22528\n" \
    "s_waitcnt lgkmcnt(4)\n" \
    "v_mfma_f32_16x16x32_bf16 v[208:211], v[216:219], v[232:235], v[208:211]\n" \
    "ds_read_b128 v[216:219], v200 offset:23552\n" \
    "s_waitcnt lgkmcnt(4)\n" \
    "v_mfma_f32_16x16x32_bf16 v[208:211], v[220:223], v[236:239], v[208:211]\n" \
    "ds_read_b64_tr_b16 v[220:221], v196 offset:13312\n" \
    "ds_read_b64_tr_b16 v[222:223], v196 offset:13568\n" \
    "s_waitcnt lgkmcnt(5)\n" \
    "v_mfma_f32_16x16x32_bf16 v[208:211], v[224:227], v[240:243], v[208:211]\n" \
    "ds_read_b64_tr_b16 v[224:225], v196 offset:13824\n" \
    "ds_read_b64_tr_b16 v[226:227], v196 offset:14080\n" \
    "s_waitcnt lgkmcnt(6)\n" \
    "v_mfma_f32_16x16x32_bf16 v[208:211], v[228:231], v[244:247], v[208:211]\n" \
    "ds_read_b64_tr_b16 v[228:229], v196 offset:14336\n" \
    "ds_read_b64_tr_b16 v[230:231], v196 offset:14592\n" \
    "v_max3_f32 v203, v204, v205, v206\n" \
    "s_waitcnt lgkmcnt(7)\n" \
    "v_mfma_f32_16x16x32_bf16 v[208:211], v[212:215], v[248:251], v[208:211]\n" \
    "ds_read_b64_tr_b16 v[212:213], v196 offset:14848\n" \
    "ds_read_b64_tr_b16 v[214:215], v196 offset:15104\n" \
    "v_max_f32 v203, v203, v207\n" \
    "s_waitcnt lgkmcnt(8)\n" \
    "v_mfma_f32_16x16x32_bf16 v[208:211], v[216:219], v[252:255], v[208:211]\n" \
    "ds_read_b64_tr_b16 v[216:217], v196 offset:15360\n" \
    "ds_read_b64_tr_b16 v[218:219], v196 offset:15616\n" \
    "s_waitcnt lgkmcnt(8)\n" \
    "v_mfma_f32_16x16x32_bf16 v[232:235], v[220:223], v[232:235], 0\n" \
    "ds_read_b64_tr_b16 v[220:221], v196 offset:15872\n" \
    "ds_read_b64_tr_b16 v[222:223], v196 offset:16128\n" \
    "s_nop 1\n" \
    "s_waitcnt lgkmcnt(8)\n" \
    "v_mfma_f32_16x16x32_bf16 v[232:235], v[224:227], v[236:239], v[232:235]\n" \
    "s_nop 1\n" \
    "s_waitcnt lgkmcnt(6)\n" \
    "v_mfma_f32_16x16x32_bf16 v[232:235], v[228:231], v[240:243], v[232:235]\n" \
    "v_max3_f32 v200, v203, v208, v209\n" \
    "v_max3_f32 v201, v200, v210, v211\n" \
    "v_max3_f32 v203, v200, v210, v211\n" \
    "s_waitcnt lgkmcnt(4)\n" \
    "v_mfma_f32_16x16x32_bf16 v[232:235], v[212:215], v[244:247], v[232:235]\n" \
    "v_permlane16_swap_b32 v201, v203\n" \
    "v_max_f32 v200, v201, v203\n" \
    "v_max_f32 v201, v201, v203\n" \
    "s_waitcnt lgkmcnt(2)\n" \
    "v_mfma_f32_16x16x32_bf16 v[232:235], v[216:219], v[248:251], v[232:235]\n" \
    "v_permlane32_swap_b32 v201, v200\n" \
    "v_max_f32 v201, v201, v200\n" \
    "s_waitcnt lgkmcnt(0)\n" \
    "v_mfma_f32_16x16x32_bf16 v[232:235], v[220:223], v[252:255], v[232:235]\n" \
    "s_nop 7\n" \
    "v_max3_f32 v236, v232, v233, v201\n" \
    "v_max3_f32 v237, v236, v234, v235\n" \
    "v_max3_f32 v236, v236, v234, v235\n" \
    "s_nop 1\n" \
    "v_permlane16_swap_b32 v236, v237\n" \
    "v_max_f32 v236, v236, v237\n" \
    "v_mul_f32 v236, 0xbdd53b94, v236\n" \
    "v_fmamk_f32 v208, v208, 0x3dd53b94, v236\n" \
    "v_fmamk_f32 v209, v209, 0x3dd53b94, v236\n" \
    "v_fmamk_f32 v210, v210, 0x3dd53b94, v236\n" \
    "v_fmamk_f32 v211, v211, 0x3dd53b94, v236\n" \
    "v_fmamk_f32 v204, v204, 0x3dd53b94, v236\n" \
    "v_fmamk_f32 v205, v205, 0x3dd53b94, v236\n" \
    "v_fmamk_f32 v206, v206, 0x3dd53b94, v236\n" \
    "v_fmamk_f32 v207, v207, 0x3dd53b94, v236\n" \
    "v_fmamk_f32 v232, v232, 0x3dd53b94, v236\n" \
    "v_fmamk_f32 v233, v233, 0x3dd53b94, v236\n" \
    "v_fmamk_f32 v234, v234, 0x3dd53b94, v236\n" \
    "v_fmamk_f32 v235, v235, 0x3dd53b94, v236\n" \
    "v_exp_f32 v208, v208\n" \
    "v_exp_f32 v209, v209\n" \
    "v_exp_f32 v210, v210\n" \
    "v_exp_f32 v211, v211\n" \
    "v_exp_f32 v204, v204\n" \
    "v_exp_f32 v205, v205\n" \
    "v_exp_f32 v206, v206\n" \
    "v_exp_f32 v207, v207\n" \
    "v_exp_f32 v232, v232\n" \
    "v_add_f32 v200, v208, v209\n" \
    "v_add_f32 v203, v210, v211\n" \
    "v_exp_f32 v233, v233\n" \
    "v_add_f32 v200, v200, v204\n" \
    "v_add_f32 v203, v203, v205\n" \
    "v_add_f32 v200, v200, v206\n" \
    "v_add_f32 v203, v203, v207\n" \
    "v_add_f32 v202, v200, v203\n" \
    "v_add_f32 v203, v200, v203\n" \
    "v_exp_f32 v234, v234\n" \
    "v_exp_f32 v235, v235\n" \
    "v_add_f32 v236, v232, v233\n" \
    "v_add_f32 v237, v234, v235\n" \
    "v_permlane32_swap_b32 v202, v203\n" \
    "v_add_f32 v236, v236, v202\n" \
    "v_add_f32 v237, v237, v203\n" \
    "v_add_f32 v238, v236, v237\n" \
    "v_add_f32 v236, v236, v237\n" \
    "s_nop 1\n" \
    "v_permlane16_swap_b32 v236, v238\n" \
    "v_add_f32 v236, v236, v238\n" \
    "v_rcp_f32 v236, v236\n" \
    "s_nop 0\n" \
    "v_pk_mul_f32 v[204:205], v[204:205], v[236:237] op_sel_hi:[1,0]\n" \
    "v_pk_mul_f32 v[208:209], v[208:209], v[236:237] op_sel_hi:[1,0]\n" \
    "v_pk_mul_f32 v[206:207], v[206:207], v[236:237] op_sel_hi:[1,0]\n" \
    "v_pk_mul_f32 v[210:211], v[210:211], v[236:237] op_sel_hi:[1,0]\n" \
    "v_pk_mul_f32 v[232:233], v[232:233], v[236:237] op_sel_hi:[1,0]\n" \
    "v_pk_mul_f32 v[234:235], v[234:235], v[236:237] op_sel_hi:[1,0]\n" \
    "v_cvt_pk_bf16_f32 v216, v204, v205\n" \
    "v_cvt_pk_bf16_f32 v217, v208, v209\n" \
    "v_cvt_pk_bf16_f32 v218, v206, v207\n" \
    "v_cvt_pk_bf16_f32 v219, v210, v211\n" \
    "buffer_store_dwordx4 v[216:219], v197, s[8:11], s38 offen sc1\n" \
    "v_cvt_pk_bf16_f32 v220, v232, v233\n" \
    "v_cvt_pk_bf16_f32 v221, v234, v235\n" \
    "buffer_store_dwordx2 v[220:221], v198, s[8:11], s38 offen sc1\n" \
    "s_cmp_lt_u32 s32, 128\n" \
    "s_cbranch_scc1 .Lqkm_l2_%=\n" \
    "s_branch .Lqkm_end_%=\n" \
    ".Lqkm_w3_%=:\n" \
    "s_sub_u32 s36, s16, 3\n" \
    "s_lshl_b32 s36, s36, 11\n" \
    "v_add_u32 v200, s36, v192\n" \
    "s_mov_b32 s33, s34\n" \
    "s_lshr_b32 s63, s33, 6\n" \
    "s_lshl_b32 s63, s63, 2\n" \
    "s_and_b32 s37, s33, 3\n" \
    "s_or_b32 s63, s63, s37\n" \
    "s_lshl_b32 s40, s63, 15\n" \
    "s_add_u32 s40, s40, s19\n" \
    "s_cmp_gt_u32 s33, 63\n" \
    "s_cselect_b32 s41, s49, s48\n" \
    "s_add_u32 s41, s41, %[dq]\n" \
    ".Lqkm_l3_%=:\n" \
    "s_branch .Lqkm_t3b_%=\n" \
    ".Lqkm_t3a_%=:\n" \
    "s_sleep 1\n" \
    ".Lqkm_t3b_%=:\n" \
    "s_memrealtime vcc\n" \
    "s_waitcnt lgkmcnt(0)\n" \
    "s_sub_i32 vcc_lo, s41, vcc_lo\n" \
    "s_cmp_gt_i32 vcc_lo, 8\n" \
    "s_cbranch_scc1 .Lqkm_t3a_%=\n" \
    "s_cmp_gt_i32 vcc_lo, 0\n" \
    "s_cbranch_scc1 .Lqkm_t3b_%=\n" \
    "buffer_load_dwordx4 v[216:219], v192, s[8:11], s40 offen sc1\n" \
    "buffer_load_dwordx4 v[220:223], v192, s[8:11], s40 offen offset:1024 sc1\n" \
    "ds_read_b128 v[240:243], v192 offset:53248\n" \
    "ds_read_b128 v[244:247], v192 offset:56320\n" \
    "ds_read_b128 v[248:251], v192 offset:54272\n" \
    "ds_read_b128 v[252:255], v192 offset:57344\n" \
    "v_mov_b64 v[204:205], 0\n" \
    "v_mov_b64 v[206:207], 0\n" \
    "v_mov_b64 v[208:209], 0\n" \
    "v_mov_b64 v[210:211], 0\n" \
    "v_mov_b64 v[212:213], 0\n" \
    "v_mov_b64 v[214:215], 0\n" \
    "s_add_u32 s37, s32, 1\n" \
    "s_sub_i32 s36, s37, %[lag]\n" \
    "s_bitcmp1_b32 s36, 0\n" \
    "s_cselect_b32 s34, 128, 2*%[lag]\n" \
    "s_add_i32 s34, s34, s36\n" \
    "s_lshr_b32 s34, s34, 1\n" \
    "s_cmp_gt_u32 s36, 127-2*%[lag]\n" \
    "s_cselect_b32 s34, s37, s34\n" \
    "s_waitcnt vmcnt(1)\n" \
    "ds_write_b128 v200, v[216:219]\n" \
    "v_max3_u32 v224, v216, v217, v218\n" \
    "s_waitcnt vmcnt(0)\n" \
    "ds_write_b128 v200, v[220:223] offset:1024\n" \
    "v_max3_u32 v224, v224, v219, v220\n" \
    "v_max3_u32 v225, v221, v222, v223\n" \
    "v_max_u32 v224, v224, v225\n" \
    "v_cmp_eq_u32 vcc, -1, v224\n" \
    "s_nop 0\n" \
    "s_cbranch_vccnz .Lqkm_cg3_%=\n" \
    ".Lqkm_cgr3_%=:\n" \
    "s_waitcnt lgkmcnt(0)\n" \
    "s_barrier\n" \
    "ds_read_b128 v[216:219], v192 offset:0\n" \
    "ds_read_b128 v[220:223], v192 offset:1024\n" \
    "ds_read_b128 v[232:235], v192 offset:8192\n" \
    "ds_read_b128 v[224:227], v192 offset:2048\n" \
    "s_waitcnt lgkmcnt(3)\n" \
    "v_smfmac_f32_16x16x64_bf16 v[204:207], v[216:219], v[0:7], v193\n" \
    "ds_read_b128 v[228:231], v192 offset:3072\n" \
    "v_smfmac_f32_16x16x64_bf16 v[208:211], v[216:219], v[64:71], v193\n" \
    "v_smfmac_f32_16x16x64_bf16 v[212:215], v[216:219], v[128:135], v193\n" \
    "s_waitcnt lgkmcnt(3)\n" \
    "v_smfmac_f32_16x16x64_bf16 v[204:207], v[220:223], v[8:15], v193\n" \
    "ds_read_b128 v[216:219], v192 offset:4096\n" \
    "v_smfmac_f32_16x16x64_bf16 v[208:211], v[220:223], v[72:79], v193\n" \
    "ds_read_b128 v[236:239], v192 offset:9216\n" \
    "v_smfmac_f32_16x16x64_bf16 v[212:215], v[220:223], v[136:143], v193\n" \
    "s_waitcnt lgkmcnt(4)\n" \
    "v_smfmac_f32_16x16x64_bf16 v[204:207], v[232:235], v[240:247], v193\n" \
    "s_waitcnt lgkmcnt(3)\n" \
    "v_smfmac_f32_16x16x64_bf16 v[204:207], v[224:227], v[16:23], v193\n" \
    "ds_read_b128 v[220:223], v192 offset:5120\n" \
    "v_smfmac_f32_16x16x64_bf16 v[208:211], v[224:227], v[80:87], v193\n" \
    "ds_read_b128 v[240:243], v192 offset:55296\n" \
    "v_smfmac_f32_16x16x64_bf16 v[212:215], v[224:227], v[144:151], v193\n" \
    "ds_read_b128 v[244:247], v192 offset:58368\n" \
    "v_smfmac_f32_16x16x64_bf16 v[208:211], v[232:235], v[248:255], v193\n" \
    "s_waitcnt lgkmcnt(5)\n" \
    "v_smfmac_f32_16x16x64_bf16 v[204:207], v[228:231], v[24:31], v193\n" \
    "ds_read_b128 v[224:227], v192 offset:6144\n" \
    "v_smfmac_f32_16x16x64_bf16 v[208:211], v[228:231], v[88:95], v193\n" \
    "ds_read_b128 v[248:251], v192 offset:59392\n" \
    "v_smfmac_f32_16x16x64_bf16 v[212:215], v[228:231], v[152:159], v193\n" \
    "ds_read_b128 v[252:255], v192 offset:62464\n" \
    "s_waitcnt lgkmcnt(7)\n" \
    "v_smfmac_f32_16x16x64_bf16 v[204:207], v[216:219], v[32:39], v193\n" \
    "ds_read_b128 v[228:231], v192 offset:7168\n" \
    "v_smfmac_f32_16x16x64_bf16 v[208:211], v[216:219], v[96:103], v193\n" \
    "v_smfmac_f32_16x16x64_bf16 v[212:215], v[216:219], v[160:167], v193\n" \
    "s_waitcnt lgkmcnt(4)\n" \
    "v_smfmac_f32_16x16x64_bf16 v[212:215], v[232:235], v[240:247], v193\n" \
    "s_waitcnt lgkmcnt(1)\n" \
    "v_smfmac_f32_16x16x64_bf16 v[204:207], v[236:239], v[248:255], v193\n" \
    "ds_read_b128 v[240:243], v192 offset:60416\n" \
    "v_smfmac_f32_16x16x64_bf16 v[204:207], v[220:223], v[40:47], v193\n" \
    "ds_read_b128 v[244:247], v192 offset:63488\n" \
    "v_smfmac_f32_16x16x64_bf16 v[208:211], v[220:223], v[104:111], v193\n" \
    "ds_read_b128 v[248:251], v192 offset:61440\n" \
    "v_smfmac_f32_16x16x64_bf16 v[212:215], v[220:223], v[168:175], v193\n" \
    "ds_read_b128 v[252:255], v192 offset:64512\n" \
    "v_smfmac_f32_16x16x64_bf16 v[204:207], v[224:227], v[48:55], v193\n" \
    "v_smfmac_f32_16x16x64_bf16 v[208:211], v[224:227], v[112:119], v193\n" \
    "v_smfmac_f32_16x16x64_bf16 v[212:215], v[224:227], v[176:183], v193\n" \
    "s_waitcnt lgkmcnt(4)\n" \
    "v_smfmac_f32_16x16x64_bf16 v[204:207], v[228:231], v[56:63], v193\n" \
    "s_waitcnt lgkmcnt(2)\n" \
    "v_smfmac_f32_16x16x64_bf16 v[208:211], v[236:239], v[240:247], v193\n" \
    "v_smfmac_f32_16x16x64_bf16 v[208:211], v[228:231], v[120:127], v193\n" \
    "s_waitcnt lgkmcnt(0)\n" \
    "v_smfmac_f32_16x16x64_bf16 v[212:215], v[236:239], v[248:255], v193\n" \
    "v_smfmac_f32_16x16x64_bf16 v[212:215], v[228:231], v[184:191], v193\n" \
    "s_memrealtime s[50:51]\n" \
    "s_waitcnt lgkmcnt(0)\n" \
    "s_sub_u32 s50, s50, %[dq3]\n" \
    "s_cmp_gt_u32 s33, 63\n" \
    "s_cselect_b32 s49, s50, s49\n" \
    "s_cselect_b32 s48, s48, s50\n" \
    "s_add_u32 s32, s32, 1\n" \
    "s_mov_b32 s65, s33\n" \
    "s_mov_b32 s33, s34\n" \
    "s_lshr_b32 s63, s33, 6\n" \
    "s_lshl_b32 s63, s63, 2\n" \
    "s_and_b32 s37, s33, 3\n" \
    "s_or_b32 s63, s63, s37\n" \
    "s_lshl_b32 s40, s63, 15\n" \
    "s_add_u32 s40, s40, s19\n" \
    "s_cmp_gt_u32 s33, 63\n" \
    "s_cselect_b32 s41, s49, s48\n" \
    "s_add_u32 s41, s41, %[dq]\n" \
    "v_pk_add_f32 v[204:205], v[204:205], v[206:207]\n" \
    "v_pk_add_f32 v[208:209], v[208:209], v[210:211]\n" \
    "v_pk_add_f32 v[212:213], v[212:213], v[214:215]\n" \
    "v_cvt_pk_bf16_f32 v204, v204, v205\n" \
    "v_cvt_pk_bf16_f32 v208, v208, v209\n" \
    "ds_write2st64_b32 v194, v204, v208 offset1:1\n" \
    "v_cvt_pk_bf16_f32 v212, v212, v213\n" \
    "ds_write_b32 v195, v212\n" \
    "s_waitcnt lgkmcnt(0)\n" \
    "s_barrier\n" \
    "s_cmp_lt_u32 s32, 128\n" \
    "s_cbranch_scc1 .Lqkm_l3_%=\n" \
    "s_branch .Lqkm_end_%=\n" \
    ".Lqkm_cg3_%=:\n" \
    "s_mov_b32 s46, 0\n" \
    ".Lqkm_cg23_%=:\n" \
    "buffer_load_dwordx4 v[216:219], v192, s[8:11], s40 offen sc1\n" \
    "buffer_load_dwordx4 v[220:223], v192, s[8:11], s40 offen offset:1024 sc1\n" \
    "s_waitcnt vmcnt(1)\n" \
    "ds_write_b128 v200, v[216:219]\n" \
    "v_max3_u32 v224, v216, v217, v218\n" \
    "s_waitcnt vmcnt(0)\n" \
    "ds_write_b128 v200, v[220:223] offset:1024\n" \
    "v_max3_u32 v224, v224, v219, v220\n" \
    "v_max3_u32 v225, v221, v222, v223\n" \
    "v_max_u32 v224, v224, v225\n" \
    "v_cmp_eq_u32 vcc, -1, v224\n" \
    "s_nop 0\n" \
    "s_cbranch_vccz .Lqkm_cgr3_%=\n" \
    "s_add_u32 s46, s46, 1\n" \
    "s_cmp_lt_u32 s46, s47\n" \
    "s_cbranch_scc1 .Lqkm_cg23_%=\n" \
    "s_mov_b32 s47, 0\n" \
    "s_branch .Lqkm_cgr3_%=\n" \
    ".Lqkm_w4_%=:\n" \
    "s_sub_u32 s36, s16, 3\n" \
    "s_lshl_b32 s36, s36, 11\n" \
    "v_add_u32 v200, s36, v192\n" \
    "v_mov_b32 v201, 0\n" \
    "v_min_u32 v202, 80, v192\n" \
    "s_mov_b32 s33, s34\n" \
    "s_lshr_b32 s63, s33, 6\n" \
    "s_lshl_b32 s63, s63, 2\n" \
    "s_and_b32 s37, s33, 3\n" \
    "s_or_b32 s63, s63, s37\n" \
    "s_lshl_b32 s40, s63, 15\n" \
    "s_add_u32 s40, s40, s19\n" \
    "s_cmp_gt_u32 s33, 63\n" \
    "s_cselect_b32 s41, s49, s48\n" \
    "s_add_u32 s41, s41, %[dq]\n" \
    ".Lqkm_l4_%=:\n" \
    "s_cmp_eq_u32 s45, 0\n" \
    "s_cbranch_scc1 .Lqkm_cq_%=\n" \
    ".Lqkm_cqr_%=:\n" \
    "s_branch .Lqkm_t4b_%=\n" \
    ".Lqkm_t4a_%=:\n" \
    "s_sleep 1\n" \
    ".Lqkm_t4b_%=:\n" \
    "s_memrealtime vcc\n" \
    "s_waitcnt lgkmcnt(0)\n" \
    "s_sub_i32 vcc_lo, s41, vcc_lo\n" \
    "s_cmp_gt_i32 vcc_lo, 8\n" \
    "s_cbranch_scc1 .Lqkm_t4a_%=\n" \
    "s_cmp_gt_i32 vcc_lo, 0\n" \
    "s_cbranch_scc1 .Lqkm_t4b_%=\n" \
    "buffer_load_dwordx4 v[216:219], v192, s[8:11], s40 offen sc1\n" \
    "buffer_load_dwordx4 v[220:223], v192, s[8:11], s40 offen offset:1024 sc1\n" \
    "ds_read_b128 v[240:243], v196 offset:0\n" \
    "ds_read_b128 v[244:247], v196 offset:3072\n" \
    "ds_read_b128 v[248:251], v196 offset:1024\n" \
    "ds_read_b128 v[252:255], v196 offset:4096\n" \
    "v_mov_b64 v[204:205], 0\n" \
    "v_mov_b64 v[206:207], 0\n" \
    "v_mov_b64 v[208:209], 0\n" \
    "v_mov_b64 v[210:211], 0\n" \
    "v_mov_b64 v[212:213], 0\n" \
    "v_mov_b64 v[214:215], 0\n" \
    "s_and_b32 s36, s35, 63\n" \
    "s_mov_b32 s34, s35\n" \
    "s_waitcnt vmcnt(2)\n" \
    "v_cmp_lt_u32 vcc, s36, v201\n" \
    "s_add_u32 s37, s32, 2\n" \
    "s_sub_i32 s36, s37, %[lag]\n" \
    "s_bitcmp1_b32 s36, 0\n" \
    "s_cselect_b32 s35, 128, 2*%[lag]\n" \
    "s_add_i32 s35, s35, s36\n" \
    "s_lshr_b32 s35, s35, 1\n" \
    "s_cmp_gt_u32 s36, 127-2*%[lag]\n" \
    "s_cselect_b32 s35, s37, s35\n" \
    "s_xor_b32 s36, s63, 2\n" \
    "s_lshl_b32 s36, s36, s18\n" \
    "s_add_u32 s38, s17, s36\n" \
    "s_lshr_b32 s36, s33, 6\n" \
    "s_lshl_b32 s36, s36, 1\n" \
    "s_and_b32 s37, s33, 1\n" \
    "s_or_b32 s36, s36, s37\n" \
    "s_lshl_b32 s36, s36, 14\n" \
    "s_add_u32 s36, s36, s23\n" \
    "s_and_b32 s64, s33, 63\n" \
    "s_add_u32 s64, s64, 1\n" \
    "s_cmp_eq_u32 s16, 7\n" \
    "s_cselect_b32 s38, s36, s38\n" \
    "s_cselect_b32 s64, s64, -1\n" \
    "v_mov_b32 v229, s64\n" \
    "v_mov_b32 v230, -1\n" \
    "v_mov_b32 v231, -1\n" \
    "s_cmp_gt_u32 s34, 63\n" \
    "s_cselect_b32 s36, %[rmask], 63\n" \
    "s_and_b32 s36, s34, s36\n" \
    "s_mul_i32 s42, s36, 1572864\n" \
    "s_add_u32 s68, s42, s24\n" \
    "s_add_u32 s42, s42, s20\n" \
    "s_lshr_b32 s36, s42, 8\n" \
    "s_xor_b32 s36, s36, s26\n" \
    "s_and_b32 s36, s36, 0x2000\n" \
    "s_or_b32 s42, s42, s36\n" \
    "s_lshr_b32 s36, s68, 8\n" \
    "s_xor_b32 s36, s36, s26\n" \
    "s_and_b32 s36, s36, 0x2000\n" \
    "s_or_b32 s68, s68, s36\n" \
    "s_bitcmp0_b32 s32, 0\n" \
    "s_cselect_b32 s43, 24576, 0\n" \
    "s_add_u32 s43, s43, s21\n" \
    "s_sub_u32 s36, s35, s27\n" \
    "s_cmp_lt_u32 s36, %[hn]\n" \
    "s_cselect_b32 s44, 32768, 0\n" \
    "s_cmp_gt_u32 s35, 63\n" \
    "s_cselect_b32 s44, 16384, s44\n" \
    "s_add_u32 s44, s44, s22\n" \
    "s_cmp_eq_u64 vcc, -1\n" \
    "s_cselect_b32 s45, 1, 0\n" \
    "s_cmp_lt_u32 s34, 128\n" \
    "s_cselect_b32 s45, s45, 0\n" \
    "s_cmp_eq_u32 s45, 0\n" \
    "s_cselect_b32 s42, 0x80000000, s42\n" \
    "s_cselect_b32 s68, 0x80000000, s68\n" \
    "s_waitcnt vmcnt(1)\n" \
    "ds_write_b128 v200, v[216:219]\n" \
    "v_max3_u32 v224, v216, v217, v218\n" \
    "s_waitcnt vmcnt(0)\n" \
    "ds_write_b128 v200, v[220:223] offset:1024\n" \
    "v_max3_u32 v224, v224, v219, v220\n" \
    "v_max3_u32 v225, v221, v222, v223\n" \
    "v_max_u32 v224, v224, v225\n" \
    "v_cmp_eq_u32 vcc, -1, v224\n" \
    "s_nop 0\n" \
    "s_cbranch_vccnz .Lqkm_cg4_%=\n" \
    ".Lqkm_cgr4_%=:\n" \
    "s_waitcnt lgkmcnt(0)\n" \
    "s_barrier\n" \
    "s_memrealtime s[50:51]\n" \
    "s_cmp_eq_u32 s16, 7\n" \
    "s_waitcnt lgkmcnt(0)\n" \
    "s_cselect_b32 s36, s50, -1\n" \
    "v_mov_b32 v228, s36\n" \
    "buffer_store_dwordx4 v[228:231], v197, s[8:11], s38 offen sc1\n" \
    "ds_read_b128 v[216:219], v192 offset:0\n" \
    "ds_read_b128 v[220:223], v192 offset:1024\n" \
    "ds_read_b128 v[232:235], v192 offset:8192\n" \
    "ds_read_b128 v[224:227], v192 offset:2048\n" \
    "buffer_store_dwordx4 v[228:231], v198, s[8:11], s38 offen sc1\n" \
    "s_mov_b32 m0, s43\n" \
    "s_add_u32 s43, s43, s25\n" \
    "buffer_load_dwordx4 v192, s[8:11], s42 offen offset:0 sc1 lds\n" \
    "buffer_load_dwordx4 v192, s[8:11], s42 offen offset:1024 sc1 lds\n" \
    "buffer_load_dwordx4 v192, s[8:11], s42 offen offset:2048 sc1 lds\n" \
    "buffer_load_dwordx4 v192, s[8:11], s42 offen offset:3072 sc1 lds\n" \
    "s_mov_b32 m0, s43\n" \
    "s_nop 0\n" \
    "buffer_load_dwordx4 v192, s[8:11], s68 offen offset:0 sc1 lds\n" \
    "buffer_load_dwordx4 v192, s[8:11], s68 offen offset:1024 sc1 lds\n" \
    "buffer_load_dword v201, v202, s[8:11], s44 offen sc1\n" \
    "s_cmp_gt_u32 s33, 63\n" \
    "s_cselect_b32 s49, s50, s49\n" \
    "s_cselect_b32 s48, s48, s50\n" \
    "s_add_u32 s32, s32, 1\n" \
    "s_mov_b32 s65, s33\n" \
    "s_mov_b32 s33, s34\n" \
    "s_lshr_b32 s63, s33, 6\n" \
    "s_lshl_b32 s63, s63, 2\n" \
    "s_and_b32 s37, s33, 3\n" \
    "s_or_b32 s63, s63, s37\n" \
    "s_lshl_b32 s40, s63, 15\n" \
    "s_add_u32 s40, s40, s19\n" \
    "s_cmp_gt_u32 s33, 63\n" \
    "s_cselect_b32 s41, s49, s48\n" \
    "s_add_u32 s41, s41, %[dq]\n" \
    "s_waitcnt lgkmcnt(3)\n" \
    "v_smfmac_f32_16x16x64_bf16 v[204:207], v[216:219], v[0:7], v193\n" \
    "ds_read_b128 v[228:231], v192 offset:3072\n" \
    "v_smfmac_f32_16x16x64_bf16 v[208:211], v[216:219], v[64:71], v193\n" \
    "v_smfmac_f32_16x16x64_bf16 v[212:215], v[216:219], v[128:135], v193\n" \
    "s_waitcnt lgkmcnt(3)\n" \
    "v_smfmac_f32_16x16x64_bf16 v[204:207], v[220:223], v[8:15], v193\n" \
    "ds_read_b128 v[216:219], v192 offset:4096\n" \
    "v_smfmac_f32_16x16x64_bf16 v[208:211], v[220:223], v[72:79], v193\n" \
    "ds_read_b128 v[236:239], v192 offset:9216\n" \
    "v_smfmac_f32_16x16x64_bf16 v[212:215], v[220:223], v[136:143], v193\n" \
    "s_waitcnt lgkmcnt(4)\n" \
    "v_smfmac_f32_16x16x64_bf16 v[204:207], v[232:235], v[240:247], v193\n" \
    "s_waitcnt lgkmcnt(3)\n" \
    "v_smfmac_f32_16x16x64_bf16 v[204:207], v[224:227], v[16:23], v193\n" \
    "ds_read_b128 v[220:223], v192 offset:5120\n" \
    "v_smfmac_f32_16x16x64_bf16 v[208:211], v[224:227], v[80:87], v193\n" \
    "ds_read_b128 v[240:243], v196 offset:2048\n" \
    "v_smfmac_f32_16x16x64_bf16 v[212:215], v[224:227], v[144:151], v193\n" \
    "ds_read_b128 v[244:247], v196 offset:5120\n" \
    "v_smfmac_f32_16x16x64_bf16 v[208:211], v[232:235], v[248:255], v193\n" \
    "s_waitcnt lgkmcnt(5)\n" \
    "v_smfmac_f32_16x16x64_bf16 v[204:207], v[228:231], v[24:31], v193\n" \
    "ds_read_b128 v[224:227], v192 offset:6144\n" \
    "v_smfmac_f32_16x16x64_bf16 v[208:211], v[228:231], v[88:95], v193\n" \
    "ds_read_b128 v[248:251], v196 offset:6144\n" \
    "v_smfmac_f32_16x16x64_bf16 v[212:215], v[228:231], v[152:159], v193\n" \
    "ds_read_b128 v[252:255], v196 offset:9216\n" \
    "s_waitcnt lgkmcnt(7)\n" \
    "v_smfmac_f32_16x16x64_bf16 v[204:207], v[216:219], v[32:39], v193\n" \
    "ds_read_b128 v[228:231], v192 offset:7168\n" \
    "v_smfmac_f32_16x16x64_bf16 v[208:211], v[216:219], v[96:103], v193\n" \
    "v_smfmac_f32_16x16x64_bf16 v[212:215], v[216:219], v[160:167], v193\n" \
    "s_waitcnt lgkmcnt(4)\n" \
    "v_smfmac_f32_16x16x64_bf16 v[212:215], v[232:235], v[240:247], v193\n" \
    "s_waitcnt lgkmcnt(1)\n" \
    "v_smfmac_f32_16x16x64_bf16 v[204:207], v[236:239], v[248:255], v193\n" \
    "ds_read_b128 v[240:243], v196 offset:7168\n" \
    "v_smfmac_f32_16x16x64_bf16 v[204:207], v[220:223], v[40:47], v193\n" \
    "ds_read_b128 v[244:247], v196 offset:10240\n" \
    "v_smfmac_f32_16x16x64_bf16 v[208:211], v[220:223], v[104:111], v193\n" \
    "ds_read_b128 v[248:251], v196 offset:8192\n" \
    "v_smfmac_f32_16x16x64_bf16 v[212:215], v[220:223], v[168:175], v193\n" \
    "ds_read_b128 v[252:255], v196 offset:11264\n" \
    "v_smfmac_f32_16x16x64_bf16 v[204:207], v[224:227], v[48:55], v193\n" \
    "v_smfmac_f32_16x16x64_bf16 v[208:211], v[224:227], v[112:119], v193\n" \
    "v_smfmac_f32_16x16x64_bf16 v[212:215], v[224:227], v[176:183], v193\n" \
    "s_waitcnt lgkmcnt(4)\n" \
    "v_smfmac_f32_16x16x64_bf16 v[204:207], v[228:231], v[56:63], v193\n" \
    "s_waitcnt lgkmcnt(2)\n" \
    "v_smfmac_f32_16x16x64_bf16 v[208:211], v[236:239], v[240:247], v193\n" \
    "v_smfmac_f32_16x16x64_bf16 v[208:211], v[228:231], v[120:127], v193\n" \
    "s_waitcnt lgkmcnt(0)\n" \
    "v_smfmac_f32_16x16x64_bf16 v[212:215], v[236:239], v[248:255], v193\n" \
    "v_smfmac_f32_16x16x64_bf16 v[212:215], v[228:231], v[184:191], v193\n" \
    "s_nop 1\n" \
    "v_pk_add_f32 v[204:205], v[204:205], v[206:207]\n" \
    "s_nop 1\n" \
    "v_pk_add_f32 v[208:209], v[208:209], v[210:211]\n" \
    "s_nop 1\n" \
    "v_pk_add_f32 v[212:213], v[212:213], v[214:215]\n" \
    "v_cvt_pk_bf16_f32 v204, v204, v205\n" \
    "v_cvt_pk_bf16_f32 v208, v208, v209\n" \
    "ds_write2st64_b32 v194, v204, v208 offset1:1\n" \
    "v_cvt_pk_bf16_f32 v212, v212, v213\n" \
    "ds_write_b32 v195, v212\n" \
    "s_waitcnt lgkmcnt(0)\n" \
    "s_barrier\n" \
    "s_cmp_lt_u32 s32, 128\n" \
    "s_cbranch_scc1 .Lqkm_l4_%=\n" \
    "s_branch .Lqkm_end_%=\n" \
    ".Lqkm_cg4_%=:\n" \
    "s_mov_b32 s46, 0\n" \
    ".Lqkm_cg24_%=:\n" \
    "buffer_load_dwordx4 v[216:219], v192, s[8:11], s40 offen sc1\n" \
    "buffer_load_dwordx4 v[220:223], v192, s[8:11], s40 offen offset:1024 sc1\n" \
    "s_waitcnt vmcnt(1)\n" \
    "ds_write_b128 v200, v[216:219]\n" \
    "v_max3_u32 v224, v216, v217, v218\n" \
    "s_waitcnt vmcnt(0)\n" \
    "ds_write_b128 v200, v[220:223] offset:1024\n" \
    "v_max3_u32 v224, v224, v219, v220\n" \
    "v_max3_u32 v225, v221, v222, v223\n" \
    "v_max_u32 v224, v224, v225\n" \
    "v_cmp_eq_u32 vcc, -1, v224\n" \
    "s_nop 0\n" \
    "s_cbranch_vccz .Lqkm_cgr4_%=\n" \
    "s_add_u32 s46, s46, 1\n" \
    "s_cmp_lt_u32 s46, s47\n" \
    "s_cbranch_scc1 .Lqkm_cg24_%=\n" \
    "s_mov_b32 s47, 0\n" \
    "s_branch .Lqkm_cgr4_%=\n" \
    ".Lqkm_cq_%=:\n" \
    "s_sub_u32 s36, s33, s27\n" \
    "s_cmp_lt_u32 s36, %[hn]\n" \
    "s_cselect_b32 s37, 32768, 0\n" \
    "s_cmp_gt_u32 s33, 63\n" \
    "s_cselect_b32 s37, 16384, s37\n" \
    "s_add_u32 s37, s37, s22\n" \
    "s_and_b32 s36, s33, 63\n" \
    "s_mov_b32 s46, 0\n" \
    ".Lqkm_cq2_%=:\n" \
    "buffer_load_dword v216, v202, s[8:11], s37 offen sc1\n" \
    "s_waitcnt vmcnt(0)\n" \
    "v_cmp_lt_u32 s[66:67], s36, v216\n" \
    "s_nop 0\n" \
    "s_cmp_eq_u64 s[66:67], -1\n" \
    "s_cbranch_scc1 .Lqkm_cq3_%=\n" \
    "s_sleep 2\n" \
    "s_add_u32 s46, s46, 1\n" \
    "s_cmp_lt_u32 s46, s47\n" \
    "s_cbranch_scc1 .Lqkm_cq2_%=\n" \
    "s_mov_b32 s47, 0\n" \
    "s_branch .Lqkm_cq3_%=\n" \
    ".Lqkm_cq3_%=:\n" \
    "s_cmp_gt_u32 s33, 63\n" \
    "s_cselect_b32 s37, %[rmask], 63\n" \
    "s_and_b32 s37, s33, s37\n" \
    "s_mul_i32 s36, s37, 1572864\n" \
    "s_add_u32 s69, s36, s24\n" \
    "s_add_u32 s36, s36, s20\n" \
    "s_lshr_b32 s37, s36, 8\n" \
    "s_xor_b32 s37, s37, s26\n" \
    "s_and_b32 s37, s37, 0x2000\n" \
    "s_or_b32 s36, s36, s37\n" \
    "s_lshr_b32 s37, s69, 8\n" \
    "s_xor_b32 s37, s37, s26\n" \
    "s_and_b32 s37, s37, 0x2000\n" \
    "s_or_b32 s69, s69, s37\n" \
    "s_bitcmp1_b32 s32, 0\n" \
    "s_cselect_b32 s37, 24576, 0\n" \
    "s_add_u32 s37, s37, s21\n" \
    "s_mov_b32 m0, s37\n" \
    "s_add_u32 s37, s37, s25\n" \
    "buffer_load_dwordx4 v192, s[8:11], s36 offen offset:0 sc1 lds\n" \
    "buffer_load_dwordx4 v192, s[8:11], s36 offen offset:1024 sc1 lds\n" \
    "buffer_load_dwordx4 v192, s[8:11], s36 offen offset:2048 sc1 lds\n" \
    "buffer_load_dwordx4 v192, s[8:11], s36 offen offset:3072 sc1 lds\n" \
    "s_mov_b32 m0, s37\n" \
    "s_nop 0\n" \
    "buffer_load_dwordx4 v192, s[8:11], s69 offen offset:0 sc1 lds\n" \
    "buffer_load_dwordx4 v192, s[8:11], s69 offen offset:1024 sc1 lds\n" \
    "s_branch .Lqkm_cqr_%=\n" \
    ".Lqkm_end_%=:\n" \
    "s_waitcnt vmcnt(0)\n" \
    "s_endpgm\n" \
    ""
#define UAV_ASM \
    "s_mov_b32 s32, 0\n" \
    "s_mov_b32 s42, 0x40000\n" \
    "s_memrealtime s[44:45]\n" \
    "s_waitcnt lgkmcnt(0)\n" \
    "s_sub_u32 s44, s44, 100000\n" \
    "s_mov_b32 s45, s44\n" \
    ".Luav_l_%=:\n" \
    "ds_read_b128 v[240:243], v196 offset:0\n" \
    "ds_read_b128 v[244:247], v196 offset:3072\n" \
    "ds_read_b128 v[248:251], v196 offset:1024\n" \
    "ds_read_b128 v[252:255], v196 offset:4096\n" \
    "ds_read_b128 v[204:207], v192 offset:13312\n" \
    "ds_read_b128 v[208:211], v192 offset:13312\n" \
    "ds_read_b128 v[212:215], v192 offset:13312\n" \
    "s_sub_i32 s34, s32, %[lag]\n" \
    "s_bitcmp1_b32 s34, 0\n" \
    "s_cselect_b32 s33, 128, 2*%[lag]\n" \
    "s_add_i32 s33, s33, s34\n" \
    "s_lshr_b32 s33, s33, 1\n" \
    "s_cmp_gt_u32 s34, 127-2*%[lag]\n" \
    "s_cselect_b32 s33, s32, s33\n" \
    "s_add_u32 s32, s32, 1\n" \
    "s_lshr_b32 s34, s33, 6\n" \
    "s_lshl_b32 s36, s34, 2\n" \
    "s_and_b32 s35, s33, 3\n" \
    "s_or_b32 s36, s36, s35\n" \
    "s_lshl_b32 s36, s36, 15\n" \
    "s_add_u32 s37, s36, s18\n" \
    "s_add_u32 s36, s36, s17\n" \
    "s_lshl_b32 s39, s34, 1\n" \
    "s_and_b32 s35, s33, 1\n" \
    "s_xor_b32 s35, s35, 1\n" \
    "s_or_b32 s39, s39, s35\n" \
    "s_lshl_b32 s39, s39, 14\n" \
    "s_add_u32 s39, s39, s19\n" \
    "s_mov_b32 s41, 0\n" \
    "s_and_b32 s40, s33, 63\n" \
    "s_cbranch_scc0 .Luav_go_%=\n" \
    ".Luav_an_%=:\n" \
    "buffer_load_dwordx2 v[216:217], v199, s[8:11], s39 offen sc1\n" \
    "s_memrealtime s[50:51]\n" \
    "s_waitcnt vmcnt(0) lgkmcnt(0)\n" \
    "s_waitcnt vmcnt(0)\n" \
    "v_cmp_ne_u32 vcc, s40, v217\n" \
    "s_waitcnt vmcnt(0) lgkmcnt(0)\n" \
    "v_subrev_u32 v216, s50, v216\n" \
    "s_cbranch_vccnz .Luav_can_%=\n" \
    "s_waitcnt vmcnt(0)\n" \
    "v_max_i32_dpp v216, v216, v216 row_ror:8 row_mask:0xf bank_mask:0xf bound_ctrl:1\n" \
    "s_waitcnt vmcnt(0)\n" \
    "s_nop 0\n" \
    "v_max_i32_dpp v216, v216, v216 row_ror:4 row_mask:0xf bank_mask:0xf bound_ctrl:1\n" \
    "s_waitcnt vmcnt(0)\n" \
    "s_nop 0\n" \
    "v_max_i32_dpp v216, v216, v216 row_ror:2 row_mask:0xf bank_mask:0xf bound_ctrl:1\n" \
    "s_waitcnt vmcnt(0)\n" \
    "s_nop 0\n" \
    "v_max_i32_dpp v216, v216, v216 row_ror:1 row_mask:0xf bank_mask:0xf bound_ctrl:1\n" \
    "s_waitcnt vmcnt(0)\n" \
    "v_mov_b32 v217, v216\n" \
    "s_waitcnt vmcnt(0)\n" \
    "s_nop 0\n" \
    "v_permlane16_swap_b32 v216, v217\n" \
    "s_waitcnt vmcnt(0)\n" \
    "v_max_i32 v216, v216, v217\n" \
    "s_waitcnt vmcnt(0)\n" \
    "v_readfirstlane_b32 s38, v216\n" \
    "s_waitcnt lgkmcnt(0)\n" \
    "s_add_u32 s38, s38, s50\n" \
    "s_add_u32 s38, s38, %[dv]\n" \
    "s_branch .Luav_tb_%=\n" \
    ".Luav_ta_%=:\n" \
    "s_sleep 1\n" \
    ".Luav_tb_%=:\n" \
    "s_memrealtime vcc\n" \
    "s_waitcnt lgkmcnt(0)\n" \
    "s_waitcnt lgkmcnt(0)\n" \
    "s_sub_i32 vcc_lo, s38, vcc_lo\n" \
    "s_waitcnt lgkmcnt(0)\n" \
    "s_cmp_gt_i32 vcc_lo, 8\n" \
    "s_cbranch_scc1 .Luav_ta_%=\n" \
    "s_waitcnt lgkmcnt(0)\n" \
    "s_cmp_gt_i32 vcc_lo, 0\n" \
    "s_cbranch_scc1 .Luav_tb_%=\n" \
    ".Luav_go_%=:\n" \
    "s_waitcnt vmcnt(0)\n" \
    "buffer_load_dwordx4 v[216:219], v192, s[8:11], s36 offen sc1\n" \
    "buffer_load_dwordx4 v[220:223], v192, s[8:11], s36 offen offset:1024 sc1\n" \
    "buffer_load_dwordx4 v[224:227], v192, s[8:11], s37 offen sc1\n" \
    "s_waitcnt vmcnt(2)\n" \
    "ds_write_b128 v197, v[216:219]\n" \
    "v_max3_u32 v228, v216, v217, v218\n" \
    "s_waitcnt vmcnt(1)\n" \
    "ds_write_b128 v197, v[220:223] offset:1024\n" \
    "v_max3_u32 v228, v228, v219, v220\n" \
    "v_max3_u32 v229, v221, v222, v223\n" \
    "s_waitcnt vmcnt(0)\n" \
    "ds_write_b128 v198, v[224:227]\n" \
    "v_max3_u32 v228, v228, v224, v225\n" \
    "v_max3_u32 v229, v229, v226, v227\n" \
    "v_max_u32 v228, v228, v229\n" \
    "s_waitcnt lgkmcnt(0)\n" \
    "v_cmp_eq_u32 vcc, -1, v228\n" \
    "s_nop 0\n" \
    "s_cbranch_vccnz .Luav_cg_%=\n" \
    ".Luav_cgr_%=:\n" \
    "s_waitcnt lgkmcnt(0)\n" \
    "s_barrier\n" \
    "ds_read_b128 v[216:219], v192 offset:0\n" \
    "ds_read_b128 v[220:223], v192 offset:1024\n" \
    "ds_read_b128 v[232:235], v192 offset:8192\n" \
    "ds_read_b128 v[224:227], v192 offset:2048\n" \
    "s_waitcnt lgkmcnt(3)\n" \
    "v_smfmac_f32_16x16x64_bf16 v[204:207], v[216:219], v[0:7], v193\n" \
    "ds_read_b128 v[228:231], v192 offset:3072\n" \
    "v_smfmac_f32_16x16x64_bf16 v[208:211], v[216:219], v[64:71], v193\n" \
    "v_smfmac_f32_16x16x64_bf16 v[212:215], v[216:219], v[128:135], v193\n" \
    "s_waitcnt lgkmcnt(3)\n" \
    "v_smfmac_f32_16x16x64_bf16 v[204:207], v[220:223], v[8:15], v193\n" \
    "ds_read_b128 v[216:219], v192 offset:4096\n" \
    "v_smfmac_f32_16x16x64_bf16 v[208:211], v[220:223], v[72:79], v193\n" \
    "ds_read_b128 v[236:239], v192 offset:9216\n" \
    "v_smfmac_f32_16x16x64_bf16 v[212:215], v[220:223], v[136:143], v193\n" \
    "s_waitcnt lgkmcnt(4)\n" \
    "v_smfmac_f32_16x16x64_bf16 v[204:207], v[232:235], v[240:247], v193\n" \
    "s_waitcnt lgkmcnt(3)\n" \
    "v_smfmac_f32_16x16x64_bf16 v[204:207], v[224:227], v[16:23], v193\n" \
    "ds_read_b128 v[220:223], v192 offset:5120\n" \
    "v_smfmac_f32_16x16x64_bf16 v[208:211], v[224:227], v[80:87], v193\n" \
    "ds_read_b128 v[240:243], v196 offset:2048\n" \
    "v_smfmac_f32_16x16x64_bf16 v[212:215], v[224:227], v[144:151], v193\n" \
    "ds_read_b128 v[244:247], v196 offset:5120\n" \
    "v_smfmac_f32_16x16x64_bf16 v[208:211], v[232:235], v[248:255], v193\n" \
    "s_waitcnt lgkmcnt(5)\n" \
    "v_smfmac_f32_16x16x64_bf16 v[204:207], v[228:231], v[24:31], v193\n" \
    "ds_read_b128 v[224:227], v192 offset:6144\n" \
    "v_smfmac_f32_16x16x64_bf16 v[208:211], v[228:231], v[88:95], v193\n" \
    "ds_read_b128 v[248:251], v196 offset:6144\n" \
    "v_smfmac_f32_16x16x64_bf16 v[212:215], v[228:231], v[152:159], v193\n" \
    "ds_read_b128 v[252:255], v196 offset:9216\n" \
    "s_waitcnt lgkmcnt(7)\n" \
    "v_smfmac_f32_16x16x64_bf16 v[204:207], v[216:219], v[32:39], v193\n" \
    "ds_read_b128 v[228:231], v192 offset:7168\n" \
    "v_smfmac_f32_16x16x64_bf16 v[208:211], v[216:219], v[96:103], v193\n" \
    "v_smfmac_f32_16x16x64_bf16 v[212:215], v[216:219], v[160:167], v193\n" \
    "s_waitcnt lgkmcnt(4)\n" \
    "v_smfmac_f32_16x16x64_bf16 v[212:215], v[232:235], v[240:247], v193\n" \
    "s_waitcnt lgkmcnt(1)\n" \
    "v_smfmac_f32_16x16x64_bf16 v[204:207], v[236:239], v[248:255], v193\n" \
    "ds_read_b128 v[240:243], v196 offset:7168\n" \
    "v_smfmac_f32_16x16x64_bf16 v[204:207], v[220:223], v[40:47], v193\n" \
    "ds_read_b128 v[244:247], v196 offset:10240\n" \
    "v_smfmac_f32_16x16x64_bf16 v[208:211], v[220:223], v[104:111], v193\n" \
    "ds_read_b128 v[248:251], v196 offset:8192\n" \
    "v_smfmac_f32_16x16x64_bf16 v[212:215], v[220:223], v[168:175], v193\n" \
    "ds_read_b128 v[252:255], v196 offset:11264\n" \
    "v_smfmac_f32_16x16x64_bf16 v[204:207], v[224:227], v[48:55], v193\n" \
    "v_smfmac_f32_16x16x64_bf16 v[208:211], v[224:227], v[112:119], v193\n" \
    "v_smfmac_f32_16x16x64_bf16 v[212:215], v[224:227], v[176:183], v193\n" \
    "s_waitcnt lgkmcnt(4)\n" \
    "v_smfmac_f32_16x16x64_bf16 v[204:207], v[228:231], v[56:63], v193\n" \
    "s_waitcnt lgkmcnt(2)\n" \
    "v_smfmac_f32_16x16x64_bf16 v[208:211], v[236:239], v[240:247], v193\n" \
    "v_smfmac_f32_16x16x64_bf16 v[208:211], v[228:231], v[120:127], v193\n" \
    "s_waitcnt lgkmcnt(0)\n" \
    "v_smfmac_f32_16x16x64_bf16 v[212:215], v[236:239], v[248:255], v193\n" \
    "v_smfmac_f32_16x16x64_bf16 v[212:215], v[228:231], v[184:191], v193\n" \
    "s_nop 1\n" \
    "v_pk_add_f32 v[204:205], v[204:205], v[206:207]\n" \
    "s_nop 1\n" \
    "v_pk_add_f32 v[208:209], v[208:209], v[210:211]\n" \
    "s_nop 1\n" \
    "v_pk_add_f32 v[212:213], v[212:213], v[214:215]\n" \
    "v_cvt_pk_bf16_f32 v204, v204, v205\n" \
    "v_cvt_pk_bf16_f32 v208, v208, v209\n" \
    "ds_write2st64_b32 v194, v204, v208 offset1:1\n" \
    "v_cvt_pk_bf16_f32 v212, v212, v213\n" \
    "ds_write_b32 v195, v212\n" \
    "s_waitcnt lgkmcnt(0)\n" \
    "s_barrier\n" \
    "s_cmp_lt_u32 s32, 128\n" \
    "s_cbranch_scc1 .Luav_l_%=\n" \
    "s_endpgm\n" \
    ".Luav_cg_%=:\n" \
    "buffer_load_dwordx4 v[216:219], v192, s[8:11], s36 offen sc1\n" \
    "buffer_load_dwordx4 v[220:223], v192, s[8:11], s36 offen offset:1024 sc1\n" \
    "buffer_load_dwordx4 v[224:227], v192, s[8:11], s37 offen sc1\n" \
    "s_waitcnt vmcnt(2)\n" \
    "ds_write_b128 v197, v[216:219]\n" \
    "v_max3_u32 v228, v216, v217, v218\n" \
    "s_waitcnt vmcnt(1)\n" \
    "ds_write_b128 v197, v[220:223] offset:1024\n" \
    "v_max3_u32 v228, v228, v219, v220\n" \
    "v_max3_u32 v229, v221, v222, v223\n" \
    "s_waitcnt vmcnt(0)\n" \
    "ds_write_b128 v198, v[224:227]\n" \
    "v_max3_u32 v228, v228, v224, v225\n" \
    "v_max3_u32 v229, v229, v226, v227\n" \
    "v_max_u32 v228, v228, v229\n" \
    "v_cmp_eq_u32 vcc, -1, v228\n" \
    "s_nop 0\n" \
    "s_cbranch_vccz .Luav_cgr_%=\n" \
    "s_add_u32 s41, s41, 1\n" \
    "s_cmp_lt_u32 s41, s42\n" \
    "s_cbranch_scc1 .Luav_cg_%=\n" \
    "s_mov_b32 s42, 0\n" \
    "s_branch .Luav_cgr_%=\n" \
    ".Luav_can_%=:\n" \
    "s_sleep 2\n" \
    "s_add_u32 s41, s41, 1\n" \
    "s_cmp_lt_u32 s41, s42\n" \
    "s_cbranch_scc1 .Luav_an_%=\n" \
    "s_mov_b32 s42, 0\n" \
    "s_branch .Luav_go_%=\n" \
    ""
#define UAO_ASM \
    "s_mov_b32 s32, 0\n" \
    "s_mov_b32 s39, 0\n" \
    "s_mov_b32 s40, 0\n" \
    "s_setprio 2\n" \
    ".Luao_l_%=:\n" \
    "s_sub_i32 s34, s32, %[lag]\n" \
    "s_bitcmp1_b32 s34, 0\n" \
    "s_cselect_b32 s33, 128, 2*%[lag]\n" \
    "s_add_i32 s33, s33, s34\n" \
    "s_lshr_b32 s33, s33, 1\n" \
    "s_cmp_gt_u32 s34, 127-2*%[lag]\n" \
    "s_cselect_b32 s33, s32, s33\n" \
    "ds_read_b128 v[236:239], v194 offset:0\n" \
    "ds_read_b128 v[240:243], v194 offset:1024\n" \
    "ds_read_b128 v[244:247], v194 offset:2048\n" \
    "ds_read_b128 v[248:251], v194 offset:3072\n" \
    "ds_read_b128 v[228:231], v192 offset:13312\n" \
    "ds_read_b128 v[212:215], v192 offset:13312\n" \
    "ds_read_b128 v[216:219], v192 offset:13312\n" \
    "ds_read_b128 v[232:235], v192 offset:13312\n" \
    "ds_read_b128 v[220:223], v192 offset:13312\n" \
    "ds_read_b128 v[224:227], v192 offset:13312\n" \
    "ds_read_b128 v[252:255], v192 offset:13312\n" \
    "s_add_u32 s32, s32, 1\n" \
    "s_cmp_gt_u32 s33, 63\n" \
    "s_cselect_b32 s34, s40, s39\n" \
    "s_cselect_b32 s35, s18, s17\n" \
    "s_add_u32 s36, s34, s35\n" \
    "s_sub_u32 s37, s34, 655360\n" \
    "s_cmp_eq_u32 s34, 0\n" \
    "s_cselect_b32 s37, 1310720, s37\n" \
    "s_add_u32 s37, s37, s35\n" \
    "s_add_u32 s34, s34, 655360\n" \
    "s_cmp_eq_u32 s34, 1966080\n" \
    "s_cselect_b32 s34, 0, s34\n" \
    "s_cmp_gt_u32 s33, 63\n" \
    "s_cselect_b32 s40, s34, s40\n" \
    "s_cselect_b32 s39, s39, s34\n" \
    "s_lshr_b32 s34, s33, 6\n" \
    "s_lshl_b32 s34, s34, 1\n" \
    "s_and_b32 s35, s33, 1\n" \
    "s_or_b32 s34, s34, s35\n" \
    "s_lshl_b32 s38, s34, s20\n" \
    "s_add_u32 s38, s38, s19\n" \
    "s_and_b32 s35, s33, 63\n" \
    "s_add_u32 s35, s35, 1\n" \
    "v_mov_b32 v201, s35\n" \
    "v_mov_b32 v204, -1\n" \
    "v_mov_b32 v205, -1\n" \
    "v_mov_b32 v206, -1\n" \
    "v_mov_b32 v207, -1\n" \
    "s_waitcnt vmcnt(5) lgkmcnt(0)\n" \
    "s_barrier\n" \
    "s_memrealtime s[50:51]\n" \
    "s_waitcnt lgkmcnt(0)\n" \
    "v_mov_b32 v200, s50\n" \
    "buffer_store_dwordx4 v[200:203], v198, s[8:11], s38 offen sc1\n" \
    "buffer_store_dwordx4 v[204:207], v197, s[8:11], s37 offen sc1\n" \
    "s_add_u32 s37, s37, 65536\n" \
    "buffer_store_dwordx4 v[204:207], v197, s[8:11], s37 offen sc1\n" \
    "s_add_u32 s37, s37, 32768\n" \
    "buffer_store_dwordx4 v[204:207], v197, s[8:11], s37 offen sc1\n" \
    "s_add_u32 s41, s36, 32768\n" \
    "s_add_u32 s42, s36, 65536\n" \
    "s_add_u32 s43, s36, 98304\n" \
    "s_add_u32 s44, s36, 131072\n" \
    "s_barrier\n" \
    "ds_read_b64_tr_b16 v[200:201], v195 offset:0\n" \
    "ds_read_b64_tr_b16 v[202:203], v195 offset:64\n" \
    "ds_read_b64_tr_b16 v[204:205], v195 offset:1024\n" \
    "ds_read_b64_tr_b16 v[206:207], v195 offset:1088\n" \
    "ds_read_b64_tr_b16 v[208:209], v195 offset:2048\n" \
    "ds_read_b64_tr_b16 v[210:211], v195 offset:2112\n" \
    "s_waitcnt lgkmcnt(4)\n" \
    "v_smfmac_f32_16x16x64_bf16 v[212:215], v[200:203], v[0:7], v193\n" \
    "s_waitcnt lgkmcnt(2)\n" \
    "v_smfmac_f32_16x16x64_bf16 v[212:215], v[204:207], v[8:15], v193\n" \
    "s_waitcnt lgkmcnt(0)\n" \
    "v_smfmac_f32_16x16x64_bf16 v[212:215], v[208:211], v[16:23], v193\n" \
    "v_smfmac_f32_16x16x64_bf16 v[216:219], v[200:203], v[24:31], v193\n" \
    "v_smfmac_f32_16x16x64_bf16 v[216:219], v[204:207], v[32:39], v193\n" \
    "v_smfmac_f32_16x16x64_bf16 v[216:219], v[208:211], v[40:47], v193\n" \
    "v_smfmac_f32_16x16x64_bf16 v[228:231], v[200:203], v[236:243], v193\n" \
    "v_smfmac_f32_16x16x64_bf16 v[228:231], v[204:207], v[244:251], v193\n" \
    "ds_read_b128 v[236:239], v194 offset:4096\n" \
    "ds_read_b128 v[240:243], v194 offset:5120\n" \
    "s_nop 0\n" \
    "v_add_f32 v212, v212, v214\n" \
    "v_add_f32 v213, v213, v215\n" \
    "v_smfmac_f32_16x16x64_bf16 v[220:223], v[200:203], v[48:55], v193\n" \
    "ds_read_b128 v[244:247], v194 offset:6144\n" \
    "ds_read_b128 v[248:251], v194 offset:7168\n" \
    "v_smfmac_f32_16x16x64_bf16 v[220:223], v[204:207], v[56:63], v193\n" \
    "v_cvt_pk_bf16_f32 v212, v212, v213\n" \
    "v_add_f32 v216, v216, v218\n" \
    "v_add_f32 v217, v217, v219\n" \
    "v_smfmac_f32_16x16x64_bf16 v[220:223], v[208:211], v[64:71], v193\n" \
    "v_cvt_pk_bf16_f32 v213, v216, v217\n" \
    "v_smfmac_f32_16x16x64_bf16 v[224:227], v[200:203], v[72:79], v193\n" \
    "ds_read_b128 v[216:219], v192 offset:13312\n" \
    "v_smfmac_f32_16x16x64_bf16 v[224:227], v[204:207], v[80:87], v193\n" \
    "v_permlane16_swap_b32 v212, v213\n" \
    "v_smfmac_f32_16x16x64_bf16 v[224:227], v[208:211], v[88:95], v193\n" \
    "buffer_store_dwordx2 v[212:213], v196, s[8:11], s36 offen sc1\n" \
    "s_waitcnt lgkmcnt(3)\n" \
    "v_smfmac_f32_16x16x64_bf16 v[228:231], v[208:211], v[236:243], v193\n" \
    "ds_read_b128 v[212:215], v192 offset:13312\n" \
    "s_waitcnt lgkmcnt(2)\n" \
    "v_smfmac_f32_16x16x64_bf16 v[232:235], v[200:203], v[244:251], v193\n" \
    "ds_read_b128 v[236:239], v194 offset:8192\n" \
    "ds_read_b128 v[240:243], v194 offset:9216\n" \
    "v_add_f32 v220, v220, v222\n" \
    "v_add_f32 v221, v221, v223\n" \
    "v_smfmac_f32_16x16x64_bf16 v[252:255], v[200:203], v[96:103], v193\n" \
    "ds_read_b128 v[244:247], v194 offset:10240\n" \
    "ds_read_b128 v[248:251], v194 offset:11264\n" \
    "v_smfmac_f32_16x16x64_bf16 v[252:255], v[204:207], v[104:111], v193\n" \
    "v_cvt_pk_bf16_f32 v220, v220, v221\n" \
    "v_add_f32 v224, v224, v226\n" \
    "v_add_f32 v225, v225, v227\n" \
    "v_smfmac_f32_16x16x64_bf16 v[252:255], v[208:211], v[112:119], v193\n" \
    "v_cvt_pk_bf16_f32 v221, v224, v225\n" \
    "s_waitcnt lgkmcnt(5)\n" \
    "v_smfmac_f32_16x16x64_bf16 v[216:219], v[200:203], v[120:127], v193\n" \
    "ds_read_b128 v[224:227], v192 offset:13312\n" \
    "v_permlane16_swap_b32 v220, v221\n" \
    "v_smfmac_f32_16x16x64_bf16 v[216:219], v[204:207], v[128:135], v193\n" \
    "buffer_store_dwordx2 v[220:221], v196, s[8:11], s41 offen sc1\n" \
    "v_add_f32 v228, v228, v230\n" \
    "v_add_f32 v229, v229, v231\n" \
    "v_smfmac_f32_16x16x64_bf16 v[216:219], v[208:211], v[136:143], v193\n" \
    "v_cvt_pk_bf16_f32 v228, v228, v229\n" \
    "s_waitcnt lgkmcnt(3)\n" \
    "v_smfmac_f32_16x16x64_bf16 v[232:235], v[204:207], v[236:243], v193\n" \
    "s_waitcnt lgkmcnt(1)\n" \
    "v_smfmac_f32_16x16x64_bf16 v[232:235], v[208:211], v[244:251], v193\n" \
    "v_add_f32 v252, v252, v254\n" \
    "v_add_f32 v253, v253, v255\n" \
    "v_smfmac_f32_16x16x64_bf16 v[212:215], v[200:203], v[144:151], v193\n" \
    "v_cvt_pk_bf16_f32 v252, v252, v253\n" \
    "v_smfmac_f32_16x16x64_bf16 v[212:215], v[204:207], v[152:159], v193\n" \
    "v_add_f32 v216, v216, v218\n" \
    "v_add_f32 v217, v217, v219\n" \
    "v_smfmac_f32_16x16x64_bf16 v[212:215], v[208:211], v[160:167], v193\n" \
    "v_cvt_pk_bf16_f32 v253, v216, v217\n" \
    "s_waitcnt lgkmcnt(0)\n" \
    "v_smfmac_f32_16x16x64_bf16 v[224:227], v[200:203], v[168:175], v193\n" \
    "v_add_f32 v232, v232, v234\n" \
    "v_add_f32 v233, v233, v235\n" \
    "v_permlane16_swap_b32 v252, v253\n" \
    "v_smfmac_f32_16x16x64_bf16 v[224:227], v[204:207], v[176:183], v193\n" \
    "buffer_store_dwordx2 v[252:253], v196, s[8:11], s42 offen sc1\n" \
    "v_smfmac_f32_16x16x64_bf16 v[224:227], v[208:211], v[184:191], v193\n" \
    "v_cvt_pk_bf16_f32 v229, v232, v233\n" \
    "v_add_f32 v212, v212, v214\n" \
    "v_add_f32 v213, v213, v215\n" \
    "v_permlane16_swap_b32 v228, v229\n" \
    "buffer_store_dwordx2 v[228:229], v196, s[8:11], s44 offen sc1\n" \
    "v_cvt_pk_bf16_f32 v212, v212, v213\n" \
    "s_nop 1\n" \
    "v_add_f32 v224, v224, v226\n" \
    "v_add_f32 v225, v225, v227\n" \
    "v_cvt_pk_bf16_f32 v213, v224, v225\n" \
    "s_nop 1\n" \
    "v_permlane16_swap_b32 v212, v213\n" \
    "buffer_store_dwordx2 v[212:213], v196, s[8:11], s43 offen sc1\n" \
    "s_cmp_lt_u32 s32, 128\n" \
    "s_cbranch_scc1 .Luao_l_%=\n" \
    "s_waitcnt vmcnt(0)\n" \
    "s_endpgm\n" \
    ""
#define R_ASM \
    "s_mov_b32 s32, 0\n" \
    "s_mov_b32 s46, 0\n" \
    "s_mov_b32 s53, 0x40000\n" \
    "s_mov_b64 s[56:57], 0xff\n" \
    "s_mov_b32 s50, 0\n" \
    "s_mov_b32 s51, 0\n" \
    "s_mov_b32 s45, 0x80000000\n" \
    "s_lshr_b32 s33, s19, 8\n" \
    "s_xor_b32 s33, s33, s27\n" \
    "s_and_b32 s33, s33, 0x2000\n" \
    "s_or_b32 s39, s19, s33\n" \
    "s_add_u32 s40, s39, 65536\n" \
    "v_mov_b32 v160, -1\n" \
    "v_mov_b32 v161, -1\n" \
    "v_mov_b32 v162, -1\n" \
    "v_mov_b32 v163, -1\n" \
    "s_mov_b32 s52, 0\n" \
    ".Lr_b0_%=:\n" \
    "buffer_load_dword v18, v2, s[8:11], 0 offen sc1\n" \
    "s_waitcnt vmcnt(0)\n" \
    "v_cmp_ge_u32 vcc, s50, v18\n" \
    "s_nop 0\n" \
    "s_and_b64 vcc, vcc, s[56:57]\n" \
    "s_cbranch_scc0 .Lr_b1_%=\n" \
    "s_sleep 2\n" \
    "s_add_u32 s52, s52, 1\n" \
    "s_cmp_lt_u32 s52, s53\n" \
    "s_cbranch_scc1 .Lr_b0_%=\n" \
    "s_mov_b32 s53, 0\n" \
    "s_branch .Lr_b1_%=\n" \
    ".Lr_b1_%=:\n" \
    "buffer_load_dwordx4 v[192:195], v0, s[8:11], s39 offen offset:0 sc1\n" \
    "buffer_load_dwordx4 v[196:199], v0, s[8:11], s39 offen offset:1024 sc1\n" \
    "buffer_load_dwordx4 v[200:203], v0, s[8:11], s39 offen offset:2048 sc1\n" \
    "buffer_load_dwordx4 v[204:207], v0, s[8:11], s39 offen offset:3072 sc1\n" \
    "buffer_load_dwordx4 v[208:211], v0, s[8:11], s40 offen offset:0 sc1\n" \
    "buffer_load_dwordx4 v[212:215], v0, s[8:11], s40 offen offset:1024 sc1\n" \
    "buffer_load_dwordx4 v[216:219], v0, s[8:11], s40 offen offset:2048 sc1\n" \
    "buffer_load_dwordx4 v[220:223], v0, s[8:11], s40 offen offset:3072 sc1\n" \
    "s_mov_b32 s52, 0\n" \
    "buffer_load_dwordx2 v[16:17], v3, s[8:11], s51 offen sc1\n" \
    "s_memrealtime s[62:63]\n" \
    "s_waitcnt vmcnt(0) lgkmcnt(0)\n" \
    "s_mov_b32 s55, s62\n" \
    "s_cmp_eq_u32 s16, 0\n" \
    "s_cbranch_scc1 .Lr_lA_%=\n" \
    "s_cmp_gt_u32 s16, 4\n" \
    "s_cbranch_scc1 .Lr_lC_%=\n" \
    "s_bitcmp1_b32 s16, 1\n" \
    "s_cbranch_scc1 .Lr_lB_%=\n" \
    ".Lr_lD_%=:\n" \
    "s_add_u32 s49, s32, 1\n" \
    "s_add_u32 s36, s18, s46\n" \
    "s_add_u32 s37, s36, 16384\n" \
    "s_and_b32 s33, s32, 3\n" \
    "s_lshl_b32 s35, s33, 16\n" \
    "s_add_u32 s35, s35, s17\n" \
    ".Lr_tvD0_%=:\n" \
    "s_waitcnt vmcnt(0)\n" \
    "v_cmp_ne_u32 vcc, s49, v17\n" \
    "v_subrev_u32 v19, s55, v16\n" \
    "s_and_b64 vcc, vcc, s[56:57]\n" \
    "s_cbranch_scc1 .Lr_ctvD0_%=\n" \
    "v_max_i32_dpp v19, v19, v19 quad_perm:[1,0,3,2] row_mask:0xf bank_mask:0xf bound_ctrl:1\n" \
    "s_nop 1\n" \
    "v_max_i32_dpp v19, v19, v19 quad_perm:[2,3,0,1] row_mask:0xf bank_mask:0xf bound_ctrl:1\n" \
    "s_nop 0\n" \
    "v_readlane_b32 s48, v19, 4\n" \
    "v_readlane_b32 s47, v19, 0\n" \
    "s_add_u32 s48, s48, s55\n" \
    "s_add_u32 s48, s48, s22\n" \
    ".Lr_goD0_%=:\n" \
    "s_branch .Lr_uD0b_%=\n" \
    ".Lr_uD0a_%=:\n" \
    "s_sleep 1\n" \
    ".Lr_uD0b_%=:\n" \
    "s_memrealtime vcc\n" \
    "s_waitcnt lgkmcnt(0)\n" \
    "s_sub_i32 vcc_lo, s48, vcc_lo\n" \
    "s_cmp_gt_i32 vcc_lo, 8\n" \
    "s_cbranch_scc1 .Lr_uD0a_%=\n" \
    "s_cmp_gt_i32 vcc_lo, 0\n" \
    "s_cbranch_scc1 .Lr_uD0b_%=\n" \
    "buffer_load_dwordx4 v[36:39], v0, s[8:11], s36 offen sc1\n" \
    "buffer_load_dwordx4 v[40:43], v0, s[8:11], s37 offen sc1\n" \
    "s_add_u32 s47, s47, s55\n" \
    "s_add_u32 s47, s47, %[dr]\n" \
    "s_branch .Lr_pD0b_%=\n" \
    ".Lr_pD0a_%=:\n" \
    "s_sleep 1\n" \
    ".Lr_pD0b_%=:\n" \
    "s_memrealtime vcc\n" \
    "s_waitcnt lgkmcnt(0)\n" \
    "s_sub_i32 vcc_lo, s47, vcc_lo\n" \
    "s_cmp_gt_i32 vcc_lo, 8\n" \
    "s_cbranch_scc1 .Lr_pD0a_%=\n" \
    "s_cmp_gt_i32 vcc_lo, 0\n" \
    "s_cbranch_scc1 .Lr_pD0b_%=\n" \
    "buffer_load_dwordx4 v[32:35], v4, s[8:11], s35 offen sc1\n" \
    "buffer_load_dwordx4 v[24:27], v0, s[8:11], s35 offen sc1\n" \
    "buffer_load_dwordx4 v[28:31], v0, s[8:11], s35 offen offset:1024 sc1\n" \
    "s_and_b32 s33, s32, 3\n" \
    "s_lshl_b32 s61, s33, 17\n" \
    "s_add_u32 s41, s61, s20\n" \
    "s_add_u32 s42, s41, 4096\n" \
    "s_barrier\n" \
    "buffer_load_dword v18, v2, s[8:11], 0 offen sc1\n" \
    "buffer_load_dwordx4 v[52:55], v0, s[8:11], s41 offen offset:2048 sc1\n" \
    "buffer_load_dwordx4 v[44:47], v0, s[8:11], s41 offen offset:0 sc1\n" \
    "buffer_load_dwordx4 v[48:51], v0, s[8:11], s41 offen offset:1024 sc1\n" \
    "buffer_load_dwordx4 v[56:59], v0, s[8:11], s41 offen offset:3072 sc1\n" \
    "buffer_load_dword v21, v13, s[8:11], s61 offen sc1\n" \
    "s_add_u32 s46, s46, 655360\n" \
    "s_cmp_eq_u32 s46, 1966080\n" \
    "s_cselect_b32 s46, 0, s46\n" \
    "s_and_b32 s33, s49, 3\n" \
    "s_lshl_b32 s38, s33, 15\n" \
    "s_and_b32 s33, s49, %[brn]-1\n" \
    "s_mul_i32 s39, s33, 2621440\n" \
    "s_add_u32 s39, s39, s19\n" \
    "s_lshr_b32 s33, s39, 8\n" \
    "s_xor_b32 s33, s33, s27\n" \
    "s_and_b32 s33, s33, 0x2000\n" \
    "s_or_b32 s39, s39, s33\n" \
    "s_mul_i32 s43, s32, 40960\n" \
    "s_add_u32 s43, s43, s21\n" \
    "s_cmp_eq_u32 s32, 63\n" \
    "s_cselect_b32 s38, 0x80000000, s38\n" \
    "s_waitcnt vmcnt(10)\n" \
    "v_max3_u32 v19, v36, v37, v38\n" \
    "s_waitcnt vmcnt(9)\n" \
    "v_max3_u32 v19, v19, v39, v40\n" \
    "v_max3_u32 v19, v19, v41, v42\n" \
    "v_max_u32 v19, v19, v43\n" \
    "s_waitcnt vmcnt(8)\n" \
    "v_mfma_f32_16x16x32_bf16 v[80:83], v[32:35], v[36:39], 0\n" \
    "v_max3_u32 v20, v32, v33, v34\n" \
    "v_mfma_f32_16x16x32_bf16 v[84:87], v[32:35], v[40:43], 0\n" \
    "v_max_u32 v20, v20, v35\n" \
    "s_waitcnt vmcnt(7)\n" \
    "v_smfmac_f32_16x16x64_bf16 v[80:83], v[24:27], v[192:199], v10\n" \
    "v_smfmac_f32_16x16x64_bf16 v[84:87], v[24:27], v[208:215], v10\n" \
    "v_max3_u32 v19, v19, v24, v25\n" \
    "v_max3_u32 v19, v19, v26, v27\n" \
    "s_waitcnt vmcnt(6)\n" \
    "v_smfmac_f32_16x16x64_bf16 v[80:83], v[28:31], v[200:207], v10\n" \
    "v_smfmac_f32_16x16x64_bf16 v[84:87], v[28:31], v[216:223], v10\n" \
    "v_max3_u32 v20, v20, v28, v29\n" \
    "v_max3_u32 v20, v20, v30, v31\n" \
    "v_max_u32 v19, v19, v20\n" \
    "v_cmp_eq_u32 vcc, -1, v19\n" \
    "s_nop 0\n" \
    "s_cbranch_vccnz .Lr_cpD0_%=\n" \
    ".Lr_cprD0_%=:\n" \
    "s_nop 0\n" \
    "v_pk_add_f32 v[80:81], v[80:81], v[82:83]\n" \
    "v_pk_add_f32 v[84:85], v[84:85], v[86:87]\n" \
    "ds_write2_b32 v5, v80, v81 offset0:0 offset1:4\n" \
    "ds_write2_b32 v5, v84, v85 offset0:64 offset1:68\n" \
    "s_waitcnt lgkmcnt(0)\n" \
    "s_barrier\n" \
    "s_barrier\n" \
    "s_memrealtime s[62:63]\n" \
    "buffer_load_dwordx4 v[60:63], v0, s[8:11], s42 offen offset:0 sc1\n" \
    "buffer_load_dwordx4 v[64:67], v0, s[8:11], s42 offen offset:1024 sc1\n" \
    "buffer_load_dwordx4 v[68:71], v0, s[8:11], s42 offen offset:2048 sc1\n" \
    "buffer_load_dwordx4 v[72:75], v0, s[8:11], s42 offen offset:3072 sc1\n" \
    "s_waitcnt vmcnt(9)\n" \
    "v_cmp_ge_u32 vcc, s49, v18\n" \
    "s_nop 0\n" \
    "s_and_b64 vcc, vcc, s[56:57]\n" \
    "s_cselect_b32 s54, 0, 1\n" \
    "s_cmp_eq_u32 s32, 63\n" \
    "s_cselect_b32 s54, 2, s54\n" \
    "s_cmp_eq_u32 s54, 1\n" \
    "s_cselect_b32 s39, s39, 0x80000000\n" \
    "s_add_u32 s40, s39, 65536\n" \
    "s_barrier\n" \
    "buffer_load_dwordx4 v[224:227], v0, s[8:11], s39 offen offset:0 sc1\n" \
    "buffer_load_dwordx4 v[228:231], v0, s[8:11], s39 offen offset:1024 sc1\n" \
    "buffer_load_dwordx4 v[232:235], v0, s[8:11], s39 offen offset:2048 sc1\n" \
    "buffer_load_dwordx4 v[236:239], v0, s[8:11], s39 offen offset:3072 sc1\n" \
    "buffer_load_dwordx4 v[240:243], v0, s[8:11], s40 offen offset:0 sc1\n" \
    "buffer_load_dwordx4 v[244:247], v0, s[8:11], s40 offen offset:1024 sc1\n" \
    "buffer_load_dwordx4 v[248:251], v0, s[8:11], s40 offen offset:2048 sc1\n" \
    "buffer_load_dwordx4 v[252:255], v0, s[8:11], s40 offen offset:3072 sc1\n" \
    "s_and_b32 s33, s32, s23\n" \
    "s_mul_i32 s45, s33, s24\n" \
    "s_cmp_lt_u32 s33, 64-%[pdist]\n" \
    "s_cselect_b32 s45, s45, 0x80000000\n" \
    "s_add_u32 s44, s43, %[x1]\n" \
    "s_xor_b32 s51, s51, 16384\n" \
    "s_waitcnt vmcnt(15)\n" \
    "v_max3_u32 v19, v44, v45, v46\n" \
    "s_waitcnt vmcnt(14)\n" \
    "v_max3_u32 v19, v19, v47, v48\n" \
    "v_max3_u32 v19, v19, v49, v50\n" \
    "v_max3_u32 v19, v19, v51, v52\n" \
    "v_max3_u32 v19, v19, v53, v54\n" \
    "s_waitcnt vmcnt(13)\n" \
    "v_max3_u32 v19, v19, v55, v56\n" \
    "v_max3_u32 v19, v19, v57, v58\n" \
    "s_waitcnt vmcnt(11)\n" \
    "v_max3_u32 v19, v19, v59, v60\n" \
    "v_max3_u32 v19, v19, v61, v62\n" \
    "s_waitcnt vmcnt(10)\n" \
    "v_max3_u32 v19, v19, v63, v64\n" \
    "v_max3_u32 v19, v19, v65, v66\n" \
    "s_waitcnt vmcnt(9)\n" \
    "v_max3_u32 v19, v19, v67, v68\n" \
    "v_max3_u32 v19, v19, v69, v70\n" \
    "s_waitcnt vmcnt(8)\n" \
    "v_max3_u32 v19, v19, v71, v72\n" \
    "v_max3_u32 v19, v19, v73, v74\n" \
    "v_max_u32 v19, v19, v75\n" \
    "v_cmp_eq_u32 vcc, -1, v19\n" \
    "s_nop 0\n" \
    "s_cbranch_vccnz .Lr_ctD0_%=\n" \
    ".Lr_ctrD0_%=:\n" \
    "v_mfma_f32_16x16x32_bf16 v[88:91], v[36:39], v[72:75], 0\n" \
    "v_mfma_f32_16x16x32_bf16 v[88:91], v[192:195], v[56:59], v[88:91]\n" \
    "v_mfma_f32_16x16x32_bf16 v[88:91], v[196:199], v[60:63], v[88:91]\n" \
    "v_mfma_f32_16x16x32_bf16 v[88:91], v[200:203], v[64:67], v[88:91]\n" \
    "v_mfma_f32_16x16x32_bf16 v[88:91], v[204:207], v[68:71], v[88:91]\n" \
    "v_mfma_f32_16x16x32_bf16 v[92:95], v[40:43], v[72:75], 0\n" \
    "v_mfma_f32_16x16x32_bf16 v[92:95], v[208:211], v[56:59], v[92:95]\n" \
    "v_mfma_f32_16x16x32_bf16 v[92:95], v[212:215], v[60:63], v[92:95]\n" \
    "v_mfma_f32_16x16x32_bf16 v[92:95], v[216:219], v[64:67], v[92:95]\n" \
    "v_mfma_f32_16x16x32_bf16 v[92:95], v[220:223], v[68:71], v[92:95]\n" \
    "v_mfma_f32_16x16x32_bf16 v[80:83], v[36:39], v[52:55], 0\n" \
    "v_mfma_f32_16x16x32_bf16 v[84:87], v[40:43], v[52:55], 0\n" \
    "v_mov_b32 v193, v194\n" \
    "v_mov_b32 v194, v196\n" \
    "v_mov_b32 v195, v198\n" \
    "v_mov_b32 v201, v202\n" \
    "v_mov_b32 v202, v204\n" \
    "v_mov_b32 v203, v206\n" \
    "v_mov_b32 v209, v210\n" \
    "v_mov_b32 v210, v212\n" \
    "v_mov_b32 v211, v214\n" \
    "v_mov_b32 v217, v218\n" \
    "v_mov_b32 v218, v220\n" \
    "v_mov_b32 v219, v222\n" \
    "ds_write_b128 v8, v[88:91] offset:2048\n" \
    "ds_write_b128 v8, v[92:95] offset:3072\n" \
    "v_permlane16_swap_b32 v192, v194\n" \
    "v_permlane16_swap_b32 v193, v195\n" \
    "v_permlane16_swap_b32 v200, v202\n" \
    "v_permlane16_swap_b32 v201, v203\n" \
    "v_permlane16_swap_b32 v208, v210\n" \
    "v_permlane16_swap_b32 v209, v211\n" \
    "v_permlane16_swap_b32 v216, v218\n" \
    "v_permlane16_swap_b32 v217, v219\n" \
    "v_mfma_f32_16x16x32_bf16 v[80:83], v[192:195], v[44:47], v[80:83]\n" \
    "v_mfma_f32_16x16x32_bf16 v[84:87], v[208:211], v[44:47], v[84:87]\n" \
    "v_mfma_f32_16x16x32_bf16 v[80:83], v[200:203], v[48:51], v[80:83]\n" \
    "v_mfma_f32_16x16x32_bf16 v[84:87], v[216:219], v[48:51], v[84:87]\n" \
    "s_nop 6\n" \
    "ds_write_b128 v8, v[80:83] offset:0\n" \
    "ds_write_b128 v8, v[84:87] offset:1024\n" \
    "s_waitcnt lgkmcnt(0)\n" \
    "s_barrier\n" \
    "s_mov_b32 s55, s62\n" \
    "buffer_load_dwordx2 v[16:17], v3, s[8:11], s51 offen sc1\n" \
    "s_cmp_eq_u32 s54, 0\n" \
    "s_cbranch_scc1 .Lr_cbD0_%=\n" \
    ".Lr_cbrD0_%=:\n" \
    "s_add_u32 s32, s32, 1\n" \
    "s_add_u32 s49, s32, 1\n" \
    "s_add_u32 s36, s18, s46\n" \
    "s_add_u32 s37, s36, 16384\n" \
    "s_and_b32 s33, s32, 3\n" \
    "s_lshl_b32 s35, s33, 16\n" \
    "s_add_u32 s35, s35, s17\n" \
    ".Lr_tvD1_%=:\n" \
    "s_waitcnt vmcnt(0)\n" \
    "v_cmp_ne_u32 vcc, s49, v17\n" \
    "v_subrev_u32 v19, s55, v16\n" \
    "s_and_b64 vcc, vcc, s[56:57]\n" \
    "s_cbranch_scc1 .Lr_ctvD1_%=\n" \
    "v_max_i32_dpp v19, v19, v19 quad_perm:[1,0,3,2] row_mask:0xf bank_mask:0xf bound_ctrl:1\n" \
    "s_nop 1\n" \
    "v_max_i32_dpp v19, v19, v19 quad_perm:[2,3,0,1] row_mask:0xf bank_mask:0xf bound_ctrl:1\n" \
    "s_nop 0\n" \
    "v_readlane_b32 s48, v19, 4\n" \
    "v_readlane_b32 s47, v19, 0\n" \
    "s_add_u32 s48, s48, s55\n" \
    "s_add_u32 s48, s48, s22\n" \
    ".Lr_goD1_%=:\n" \
    "s_branch .Lr_uD1b_%=\n" \
    ".Lr_uD1a_%=:\n" \
    "s_sleep 1\n" \
    ".Lr_uD1b_%=:\n" \
    "s_memrealtime vcc\n" \
    "s_waitcnt lgkmcnt(0)\n" \
    "s_sub_i32 vcc_lo, s48, vcc_lo\n" \
    "s_cmp_gt_i32 vcc_lo, 8\n" \
    "s_cbranch_scc1 .Lr_uD1a_%=\n" \
    "s_cmp_gt_i32 vcc_lo, 0\n" \
    "s_cbranch_scc1 .Lr_uD1b_%=\n" \
    "buffer_load_dwordx4 v[36:39], v0, s[8:11], s36 offen sc1\n" \
    "buffer_load_dwordx4 v[40:43], v0, s[8:11], s37 offen sc1\n" \
    "s_add_u32 s47, s47, s55\n" \
    "s_add_u32 s47, s47, %[dr]\n" \
    "s_branch .Lr_pD1b_%=\n" \
    ".Lr_pD1a_%=:\n" \
    "s_sleep 1\n" \
    ".Lr_pD1b_%=:\n" \
    "s_memrealtime vcc\n" \
    "s_waitcnt lgkmcnt(0)\n" \
    "s_sub_i32 vcc_lo, s47, vcc_lo\n" \
    "s_cmp_gt_i32 vcc_lo, 8\n" \
    "s_cbranch_scc1 .Lr_pD1a_%=\n" \
    "s_cmp_gt_i32 vcc_lo, 0\n" \
    "s_cbranch_scc1 .Lr_pD1b_%=\n" \
    "buffer_load_dwordx4 v[32:35], v4, s[8:11], s35 offen sc1\n" \
    "buffer_load_dwordx4 v[24:27], v0, s[8:11], s35 offen sc1\n" \
    "buffer_load_dwordx4 v[28:31], v0, s[8:11], s35 offen offset:1024 sc1\n" \
    "s_and_b32 s33, s32, 3\n" \
    "s_lshl_b32 s61, s33, 17\n" \
    "s_add_u32 s41, s61, s20\n" \
    "s_add_u32 s42, s41, 4096\n" \
    "s_barrier\n" \
    "buffer_load_dword v18, v2, s[8:11], 0 offen sc1\n" \
    "buffer_load_dwordx4 v[52:55], v0, s[8:11], s41 offen offset:2048 sc1\n" \
    "buffer_load_dwordx4 v[44:47], v0, s[8:11], s41 offen offset:0 sc1\n" \
    "buffer_load_dwordx4 v[48:51], v0, s[8:11], s41 offen offset:1024 sc1\n" \
    "buffer_load_dwordx4 v[56:59], v0, s[8:11], s41 offen offset:3072 sc1\n" \
    "buffer_load_dword v21, v13, s[8:11], s61 offen sc1\n" \
    "s_add_u32 s46, s46, 655360\n" \
    "s_cmp_eq_u32 s46, 1966080\n" \
    "s_cselect_b32 s46, 0, s46\n" \
    "s_and_b32 s33, s49, 3\n" \
    "s_lshl_b32 s38, s33, 15\n" \
    "s_and_b32 s33, s49, %[brn]-1\n" \
    "s_mul_i32 s39, s33, 2621440\n" \
    "s_add_u32 s39, s39, s19\n" \
    "s_lshr_b32 s33, s39, 8\n" \
    "s_xor_b32 s33, s33, s27\n" \
    "s_and_b32 s33, s33, 0x2000\n" \
    "s_or_b32 s39, s39, s33\n" \
    "s_mul_i32 s43, s32, 40960\n" \
    "s_add_u32 s43, s43, s21\n" \
    "s_cmp_eq_u32 s32, 63\n" \
    "s_cselect_b32 s38, 0x80000000, s38\n" \
    "s_waitcnt vmcnt(10)\n" \
    "v_max3_u32 v19, v36, v37, v38\n" \
    "s_waitcnt vmcnt(9)\n" \
    "v_max3_u32 v19, v19, v39, v40\n" \
    "v_max3_u32 v19, v19, v41, v42\n" \
    "v_max_u32 v19, v19, v43\n" \
    "s_waitcnt vmcnt(8)\n" \
    "v_mfma_f32_16x16x32_bf16 v[80:83], v[32:35], v[36:39], 0\n" \
    "v_max3_u32 v20, v32, v33, v34\n" \
    "v_mfma_f32_16x16x32_bf16 v[84:87], v[32:35], v[40:43], 0\n" \
    "v_max_u32 v20, v20, v35\n" \
    "s_waitcnt vmcnt(7)\n" \
    "v_smfmac_f32_16x16x64_bf16 v[80:83], v[24:27], v[224:231], v10\n" \
    "v_smfmac_f32_16x16x64_bf16 v[84:87], v[24:27], v[240:247], v10\n" \
    "v_max3_u32 v19, v19, v24, v25\n" \
    "v_max3_u32 v19, v19, v26, v27\n" \
    "s_waitcnt vmcnt(6)\n" \
    "v_smfmac_f32_16x16x64_bf16 v[80:83], v[28:31], v[232:239], v10\n" \
    "v_smfmac_f32_16x16x64_bf16 v[84:87], v[28:31], v[248:255], v10\n" \
    "v_max3_u32 v20, v20, v28, v29\n" \
    "v_max3_u32 v20, v20, v30, v31\n" \
    "v_max_u32 v19, v19, v20\n" \
    "v_cmp_eq_u32 vcc, -1, v19\n" \
    "s_nop 0\n" \
    "s_cbranch_vccnz .Lr_cpD1_%=\n" \
    ".Lr_cprD1_%=:\n" \
    "s_nop 0\n" \
    "v_pk_add_f32 v[80:81], v[80:81], v[82:83]\n" \
    "v_pk_add_f32 v[84:85], v[84:85], v[86:87]\n" \
    "ds_write2_b32 v5, v80, v81 offset0:0 offset1:4\n" \
    "ds_write2_b32 v5, v84, v85 offset0:64 offset1:68\n" \
    "s_waitcnt lgkmcnt(0)\n" \
    "s_barrier\n" \
    "s_barrier\n" \
    "s_memrealtime s[62:63]\n" \
    "buffer_load_dwordx4 v[60:63], v0, s[8:11], s42 offen offset:0 sc1\n" \
    "buffer_load_dwordx4 v[64:67], v0, s[8:11], s42 offen offset:1024 sc1\n" \
    "buffer_load_dwordx4 v[68:71], v0, s[8:11], s42 offen offset:2048 sc1\n" \
    "buffer_load_dwordx4 v[72:75], v0, s[8:11], s42 offen offset:3072 sc1\n" \
    "s_waitcnt vmcnt(9)\n" \
    "v_cmp_ge_u32 vcc, s49, v18\n" \
    "s_nop 0\n" \
    "s_and_b64 vcc, vcc, s[56:57]\n" \
    "s_cselect_b32 s54, 0, 1\n" \
    "s_cmp_eq_u32 s32, 63\n" \
    "s_cselect_b32 s54, 2, s54\n" \
    "s_cmp_eq_u32 s54, 1\n" \
    "s_cselect_b32 s39, s39, 0x80000000\n" \
    "s_add_u32 s40, s39, 65536\n" \
    "s_barrier\n" \
    "buffer_load_dwordx4 v[192:195], v0, s[8:11], s39 offen offset:0 sc1\n" \
    "buffer_load_dwordx4 v[196:199], v0, s[8:11], s39 offen offset:1024 sc1\n" \
    "buffer_load_dwordx4 v[200:203], v0, s[8:11], s39 offen offset:2048 sc1\n" \
    "buffer_load_dwordx4 v[204:207], v0, s[8:11], s39 offen offset:3072 sc1\n" \
    "buffer_load_dwordx4 v[208:211], v0, s[8:11], s40 offen offset:0 sc1\n" \
    "buffer_load_dwordx4 v[212:215], v0, s[8:11], s40 offen offset:1024 sc1\n" \
    "buffer_load_dwordx4 v[216:219], v0, s[8:11], s40 offen offset:2048 sc1\n" \
    "buffer_load_dwordx4 v[220:223], v0, s[8:11], s40 offen offset:3072 sc1\n" \
    "s_and_b32 s33, s32, s23\n" \
    "s_mul_i32 s45, s33, s24\n" \
    "s_cmp_lt_u32 s33, 64-%[pdist]\n" \
    "s_cselect_b32 s45, s45, 0x80000000\n" \
    "s_add_u32 s44, s43, %[x1]\n" \
    "s_xor_b32 s51, s51, 16384\n" \
    "s_waitcnt vmcnt(15)\n" \
    "v_max3_u32 v19, v44, v45, v46\n" \
    "s_waitcnt vmcnt(14)\n" \
    "v_max3_u32 v19, v19, v47, v48\n" \
    "v_max3_u32 v19, v19, v49, v50\n" \
    "v_max3_u32 v19, v19, v51, v52\n" \
    "v_max3_u32 v19, v19, v53, v54\n" \
    "s_waitcnt vmcnt(13)\n" \
    "v_max3_u32 v19, v19, v55, v56\n" \
    "v_max3_u32 v19, v19, v57, v58\n" \
    "s_waitcnt vmcnt(11)\n" \
    "v_max3_u32 v19, v19, v59, v60\n" \
    "v_max3_u32 v19, v19, v61, v62\n" \
    "s_waitcnt vmcnt(10)\n" \
    "v_max3_u32 v19, v19, v63, v64\n" \
    "v_max3_u32 v19, v19, v65, v66\n" \
    "s_waitcnt vmcnt(9)\n" \
    "v_max3_u32 v19, v19, v67, v68\n" \
    "v_max3_u32 v19, v19, v69, v70\n" \
    "s_waitcnt vmcnt(8)\n" \
    "v_max3_u32 v19, v19, v71, v72\n" \
    "v_max3_u32 v19, v19, v73, v74\n" \
    "v_max_u32 v19, v19, v75\n" \
    "v_cmp_eq_u32 vcc, -1, v19\n" \
    "s_nop 0\n" \
    "s_cbranch_vccnz .Lr_ctD1_%=\n" \
    ".Lr_ctrD1_%=:\n" \
    "v_mfma_f32_16x16x32_bf16 v[88:91], v[36:39], v[72:75], 0\n" \
    "v_mfma_f32_16x16x32_bf16 v[88:91], v[224:227], v[56:59], v[88:91]\n" \
    "v_mfma_f32_16x16x32_bf16 v[88:91], v[228:231], v[60:63], v[88:91]\n" \
    "v_mfma_f32_16x16x32_bf16 v[88:91], v[232:235], v[64:67], v[88:91]\n" \
    "v_mfma_f32_16x16x32_bf16 v[88:91], v[236:239], v[68:71], v[88:91]\n" \
    "v_mfma_f32_16x16x32_bf16 v[92:95], v[40:43], v[72:75], 0\n" \
    "v_mfma_f32_16x16x32_bf16 v[92:95], v[240:243], v[56:59], v[92:95]\n" \
    "v_mfma_f32_16x16x32_bf16 v[92:95], v[244:247], v[60:63], v[92:95]\n" \
    "v_mfma_f32_16x16x32_bf16 v[92:95], v[248:251], v[64:67], v[92:95]\n" \
    "v_mfma_f32_16x16x32_bf16 v[92:95], v[252:255], v[68:71], v[92:95]\n" \
    "v_mfma_f32_16x16x32_bf16 v[80:83], v[36:39], v[52:55], 0\n" \
    "v_mfma_f32_16x16x32_bf16 v[84:87], v[40:43], v[52:55], 0\n" \
    "v_mov_b32 v225, v226\n" \
    "v_mov_b32 v226, v228\n" \
    "v_mov_b32 v227, v230\n" \
    "v_mov_b32 v233, v234\n" \
    "v_mov_b32 v234, v236\n" \
    "v_mov_b32 v235, v238\n" \
    "v_mov_b32 v241, v242\n" \
    "v_mov_b32 v242, v244\n" \
    "v_mov_b32 v243, v246\n" \
    "v_mov_b32 v249, v250\n" \
    "v_mov_b32 v250, v252\n" \
    "v_mov_b32 v251, v254\n" \
    "ds_write_b128 v8, v[88:91] offset:2048\n" \
    "ds_write_b128 v8, v[92:95] offset:3072\n" \
    "v_permlane16_swap_b32 v224, v226\n" \
    "v_permlane16_swap_b32 v225, v227\n" \
    "v_permlane16_swap_b32 v232, v234\n" \
    "v_permlane16_swap_b32 v233, v235\n" \
    "v_permlane16_swap_b32 v240, v242\n" \
    "v_permlane16_swap_b32 v241, v243\n" \
    "v_permlane16_swap_b32 v248, v250\n" \
    "v_permlane16_swap_b32 v249, v251\n" \
    "v_mfma_f32_16x16x32_bf16 v[80:83], v[224:227], v[44:47], v[80:83]\n" \
    "v_mfma_f32_16x16x32_bf16 v[84:87], v[240:243], v[44:47], v[84:87]\n" \
    "v_mfma_f32_16x16x32_bf16 v[80:83], v[232:235], v[48:51], v[80:83]\n" \
    "v_mfma_f32_16x16x32_bf16 v[84:87], v[248:251], v[48:51], v[84:87]\n" \
    "s_nop 6\n" \
    "ds_write_b128 v8, v[80:83] offset:0\n" \
    "ds_write_b128 v8, v[84:87] offset:1024\n" \
    "s_waitcnt lgkmcnt(0)\n" \
    "s_barrier\n" \
    "s_mov_b32 s55, s62\n" \
    "buffer_load_dwordx2 v[16:17], v3, s[8:11], s51 offen sc1\n" \
    "s_cmp_eq_u32 s54, 0\n" \
    "s_cbranch_scc1 .Lr_cbD1_%=\n" \
    ".Lr_cbrD1_%=:\n" \
    "s_add_u32 s32, s32, 1\n" \
    "s_cmp_lt_u32 s32, 64\n" \
    "s_cbranch_scc1 .Lr_lD_%=\n" \
    "s_waitcnt vmcnt(0)\n" \
    "s_endpgm\n" \
    ".Lr_lA_%=:\n" \
    "s_add_u32 s49, s32, 1\n" \
    "s_add_u32 s36, s18, s46\n" \
    "s_add_u32 s37, s36, 16384\n" \
    "s_and_b32 s33, s32, 3\n" \
    "s_lshl_b32 s35, s33, 16\n" \
    "s_add_u32 s35, s35, s17\n" \
    ".Lr_tvA0_%=:\n" \
    "s_waitcnt vmcnt(0)\n" \
    "v_cmp_ne_u32 vcc, s49, v17\n" \
    "v_subrev_u32 v19, s55, v16\n" \
    "s_and_b64 vcc, vcc, s[56:57]\n" \
    "s_cbranch_scc1 .Lr_ctvA0_%=\n" \
    "v_max_i32_dpp v19, v19, v19 quad_perm:[1,0,3,2] row_mask:0xf bank_mask:0xf bound_ctrl:1\n" \
    "s_nop 1\n" \
    "v_max_i32_dpp v19, v19, v19 quad_perm:[2,3,0,1] row_mask:0xf bank_mask:0xf bound_ctrl:1\n" \
    "s_nop 0\n" \
    "v_readlane_b32 s48, v19, 4\n" \
    "v_readlane_b32 s47, v19, 0\n" \
    "s_add_u32 s48, s48, s55\n" \
    "s_add_u32 s48, s48, s22\n" \
    ".Lr_goA0_%=:\n" \
    "s_branch .Lr_uA0b_%=\n" \
    ".Lr_uA0a_%=:\n" \
    "s_sleep 1\n" \
    ".Lr_uA0b_%=:\n" \
    "s_memrealtime vcc\n" \
    "s_waitcnt lgkmcnt(0)\n" \
    "s_sub_i32 vcc_lo, s48, vcc_lo\n" \
    "s_cmp_gt_i32 vcc_lo, 8\n" \
    "s_cbranch_scc1 .Lr_uA0a_%=\n" \
    "s_cmp_gt_i32 vcc_lo, 0\n" \
    "s_cbranch_scc1 .Lr_uA0b_%=\n" \
    "buffer_load_dwordx4 v[36:39], v0, s[8:11], s36 offen sc1\n" \
    "buffer_load_dwordx4 v[40:43], v0, s[8:11], s37 offen sc1\n" \
    "s_add_u32 s47, s47, s55\n" \
    "s_add_u32 s47, s47, %[dr]\n" \
    "s_branch .Lr_pA0b_%=\n" \
    ".Lr_pA0a_%=:\n" \
    "s_sleep 1\n" \
    ".Lr_pA0b_%=:\n" \
    "s_memrealtime vcc\n" \
    "s_waitcnt lgkmcnt(0)\n" \
    "s_sub_i32 vcc_lo, s47, vcc_lo\n" \
    "s_cmp_gt_i32 vcc_lo, 8\n" \
    "s_cbranch_scc1 .Lr_pA0a_%=\n" \
    "s_cmp_gt_i32 vcc_lo, 0\n" \
    "s_cbranch_scc1 .Lr_pA0b_%=\n" \
    "buffer_load_dwordx4 v[32:35], v4, s[8:11], s35 offen sc1\n" \
    "buffer_load_dwordx4 v[24:27], v0, s[8:11], s35 offen sc1\n" \
    "buffer_load_dwordx4 v[28:31], v0, s[8:11], s35 offen offset:1024 sc1\n" \
    "s_and_b32 s33, s32, 3\n" \
    "s_lshl_b32 s61, s33, 17\n" \
    "s_add_u32 s41, s61, s20\n" \
    "s_add_u32 s42, s41, 4096\n" \
    "s_barrier\n" \
    "buffer_load_dword v18, v2, s[8:11], 0 offen sc1\n" \
    "buffer_load_dwordx4 v[52:55], v0, s[8:11], s41 offen offset:2048 sc1\n" \
    "buffer_load_dwordx4 v[44:47], v0, s[8:11], s41 offen offset:0 sc1\n" \
    "buffer_load_dwordx4 v[48:51], v0, s[8:11], s41 offen offset:1024 sc1\n" \
    "buffer_load_dwordx4 v[56:59], v0, s[8:11], s41 offen offset:3072 sc1\n" \
    "s_add_u32 s46, s46, 655360\n" \
    "s_cmp_eq_u32 s46, 1966080\n" \
    "s_cselect_b32 s46, 0, s46\n" \
    "s_and_b32 s33, s49, 3\n" \
    "s_lshl_b32 s38, s33, 15\n" \
    "s_and_b32 s33, s49, %[brn]-1\n" \
    "s_mul_i32 s39, s33, 2621440\n" \
    "s_add_u32 s39, s39, s19\n" \
    "s_lshr_b32 s33, s39, 8\n" \
    "s_xor_b32 s33, s33, s27\n" \
    "s_and_b32 s33, s33, 0x2000\n" \
    "s_or_b32 s39, s39, s33\n" \
    "s_mul_i32 s43, s32, 40960\n" \
    "s_add_u32 s43, s43, s21\n" \
    "s_cmp_eq_u32 s32, 63\n" \
    "s_cselect_b32 s38, 0x80000000, s38\n" \
    "s_waitcnt vmcnt(9)\n" \
    "v_max3_u32 v19, v36, v37, v38\n" \
    "s_waitcnt vmcnt(8)\n" \
    "v_max3_u32 v19, v19, v39, v40\n" \
    "v_max3_u32 v19, v19, v41, v42\n" \
    "v_max_u32 v19, v19, v43\n" \
    "s_waitcnt vmcnt(7)\n" \
    "v_mfma_f32_16x16x32_bf16 v[80:83], v[32:35], v[36:39], 0\n" \
    "v_max3_u32 v20, v32, v33, v34\n" \
    "v_mfma_f32_16x16x32_bf16 v[84:87], v[32:35], v[40:43], 0\n" \
    "v_max_u32 v20, v20, v35\n" \
    "s_waitcnt vmcnt(6)\n" \
    "v_smfmac_f32_16x16x64_bf16 v[80:83], v[24:27], v[192:199], v10\n" \
    "v_smfmac_f32_16x16x64_bf16 v[84:87], v[24:27], v[208:215], v10\n" \
    "v_max3_u32 v19, v19, v24, v25\n" \
    "v_max3_u32 v19, v19, v26, v27\n" \
    "s_waitcnt vmcnt(5)\n" \
    "v_smfmac_f32_16x16x64_bf16 v[80:83], v[28:31], v[200:207], v10\n" \
    "v_smfmac_f32_16x16x64_bf16 v[84:87], v[28:31], v[216:223], v10\n" \
    "v_max3_u32 v20, v20, v28, v29\n" \
    "v_max3_u32 v20, v20, v30, v31\n" \
    "v_max_u32 v19, v19, v20\n" \
    "v_cmp_eq_u32 vcc, -1, v19\n" \
    "s_nop 0\n" \
    "s_cbranch_vccnz .Lr_cpA0_%=\n" \
    ".Lr_cprA0_%=:\n" \
    "s_nop 0\n" \
    "v_pk_add_f32 v[80:81], v[80:81], v[82:83]\n" \
    "v_pk_add_f32 v[84:85], v[84:85], v[86:87]\n" \
    "ds_write2_b32 v5, v80, v81 offset0:0 offset1:4\n" \
    "ds_write2_b32 v5, v84, v85 offset0:64 offset1:68\n" \
    "s_waitcnt lgkmcnt(0)\n" \
    "s_barrier\n" \
    "ds_read_b128 v[96:99], v0 offset:0\n" \
    "ds_read_b128 v[100:103], v0 offset:1024\n" \
    "ds_read_b128 v[104:107], v0 offset:2048\n" \
    "ds_read_b128 v[108:111], v0 offset:3072\n" \
    "ds_read_b128 v[112:115], v0 offset:4096\n" \
    "ds_read_b128 v[116:119], v0 offset:5120\n" \
    "ds_read_b128 v[120:123], v0 offset:6144\n" \
    "ds_read_b128 v[124:127], v0 offset:7168\n" \
    "s_waitcnt lgkmcnt(6)\n" \
    "v_pk_add_f32 v[96:97], v[96:97], v[100:101]\n" \
    "v_pk_add_f32 v[98:99], v[98:99], v[102:103]\n" \
    "s_waitcnt lgkmcnt(4)\n" \
    "v_pk_add_f32 v[104:105], v[104:105], v[108:109]\n" \
    "v_pk_add_f32 v[106:107], v[106:107], v[110:111]\n" \
    "s_waitcnt lgkmcnt(2)\n" \
    "v_pk_add_f32 v[112:113], v[112:113], v[116:117]\n" \
    "v_pk_add_f32 v[114:115], v[114:115], v[118:119]\n" \
    "v_pk_add_f32 v[96:97], v[96:97], v[104:105]\n" \
    "v_pk_add_f32 v[98:99], v[98:99], v[106:107]\n" \
    "s_waitcnt lgkmcnt(0)\n" \
    "v_pk_add_f32 v[120:121], v[120:121], v[124:125]\n" \
    "v_pk_add_f32 v[122:123], v[122:123], v[126:127]\n" \
    "v_pk_add_f32 v[96:97], v[96:97], v[112:113]\n" \
    "v_pk_add_f32 v[98:99], v[98:99], v[114:115]\n" \
    "v_pk_add_f32 v[96:97], v[96:97], v[120:121]\n" \
    "v_pk_add_f32 v[98:99], v[98:99], v[122:123]\n" \
    "v_cvt_pk_bf16_f32 v96, v96, v97\n" \
    "v_cvt_pk_bf16_f32 v97, v98, v99\n" \
    "buffer_store_dwordx2 v[96:97], v1, s[8:11], s38 offen sc1\n" \
    "s_barrier\n" \
    "s_memrealtime s[62:63]\n" \
    "buffer_load_dwordx4 v[60:63], v0, s[8:11], s42 offen offset:0 sc1\n" \
    "buffer_load_dwordx4 v[64:67], v0, s[8:11], s42 offen offset:1024 sc1\n" \
    "buffer_load_dwordx4 v[68:71], v0, s[8:11], s42 offen offset:2048 sc1\n" \
    "buffer_load_dwordx4 v[72:75], v0, s[8:11], s42 offen offset:3072 sc1\n" \
    "s_waitcnt vmcnt(9)\n" \
    "v_cmp_ge_u32 vcc, s49, v18\n" \
    "s_nop 0\n" \
    "s_and_b64 vcc, vcc, s[56:57]\n" \
    "s_cselect_b32 s54, 0, 1\n" \
    "s_cmp_eq_u32 s32, 63\n" \
    "s_cselect_b32 s54, 2, s54\n" \
    "s_cmp_eq_u32 s54, 1\n" \
    "s_cselect_b32 s39, s39, 0x80000000\n" \
    "s_add_u32 s40, s39, 65536\n" \
    "s_barrier\n" \
    "buffer_load_dwordx4 v[224:227], v0, s[8:11], s39 offen offset:0 sc1\n" \
    "buffer_load_dwordx4 v[228:231], v0, s[8:11], s39 offen offset:1024 sc1\n" \
    "buffer_load_dwordx4 v[232:235], v0, s[8:11], s39 offen offset:2048 sc1\n" \
    "buffer_load_dwordx4 v[236:239], v0, s[8:11], s39 offen offset:3072 sc1\n" \
    "buffer_load_dwordx4 v[240:243], v0, s[8:11], s40 offen offset:0 sc1\n" \
    "buffer_load_dwordx4 v[244:247], v0, s[8:11], s40 offen offset:1024 sc1\n" \
    "buffer_load_dwordx4 v[248:251], v0, s[8:11], s40 offen offset:2048 sc1\n" \
    "buffer_load_dwordx4 v[252:255], v0, s[8:11], s40 offen offset:3072 sc1\n" \
    "s_and_b32 s33, s32, s23\n" \
    "s_mul_i32 s45, s33, s24\n" \
    "s_cmp_lt_u32 s33, 64-%[pdist]\n" \
    "s_cselect_b32 s45, s45, 0x80000000\n" \
    "s_add_u32 s44, s43, %[x1]\n" \
    "s_xor_b32 s51, s51, 16384\n" \
    "s_waitcnt vmcnt(15)\n" \
    "v_max3_u32 v19, v44, v45, v46\n" \
    "s_waitcnt vmcnt(14)\n" \
    "v_max3_u32 v19, v19, v47, v48\n" \
    "v_max3_u32 v19, v19, v49, v50\n" \
    "v_max3_u32 v19, v19, v51, v52\n" \
    "v_max3_u32 v19, v19, v53, v54\n" \
    "s_waitcnt vmcnt(13)\n" \
    "v_max3_u32 v19, v19, v55, v56\n" \
    "v_max3_u32 v19, v19, v57, v58\n" \
    "s_waitcnt vmcnt(11)\n" \
    "v_max3_u32 v19, v19, v59, v60\n" \
    "v_max3_u32 v19, v19, v61, v62\n" \
    "s_waitcnt vmcnt(10)\n" \
    "v_max3_u32 v19, v19, v63, v64\n" \
    "v_max3_u32 v19, v19, v65, v66\n" \
    "s_waitcnt vmcnt(9)\n" \
    "v_max3_u32 v19, v19, v67, v68\n" \
    "v_max3_u32 v19, v19, v69, v70\n" \
    "s_waitcnt vmcnt(8)\n" \
    "v_max3_u32 v19, v19, v71, v72\n" \
    "v_max3_u32 v19, v19, v73, v74\n" \
    "v_max_u32 v19, v19, v75\n" \
    "v_cmp_eq_u32 vcc, -1, v19\n" \
    "s_nop 0\n" \
    "s_cbranch_vccnz .Lr_ctA0_%=\n" \
    ".Lr_ctrA0_%=:\n" \
    "v_mfma_f32_16x16x32_bf16 v[88:91], v[36:39], v[72:75], 0\n" \
    "v_mfma_f32_16x16x32_bf16 v[88:91], v[192:195], v[56:59], v[88:91]\n" \
    "v_mfma_f32_16x16x32_bf16 v[88:91], v[196:199], v[60:63], v[88:91]\n" \
    "v_mfma_f32_16x16x32_bf16 v[88:91], v[200:203], v[64:67], v[88:91]\n" \
    "v_mfma_f32_16x16x32_bf16 v[88:91], v[204:207], v[68:71], v[88:91]\n" \
    "v_mfma_f32_16x16x32_bf16 v[92:95], v[40:43], v[72:75], 0\n" \
    "v_mfma_f32_16x16x32_bf16 v[92:95], v[208:211], v[56:59], v[92:95]\n" \
    "v_mfma_f32_16x16x32_bf16 v[92:95], v[212:215], v[60:63], v[92:95]\n" \
    "v_mfma_f32_16x16x32_bf16 v[92:95], v[216:219], v[64:67], v[92:95]\n" \
    "v_mfma_f32_16x16x32_bf16 v[92:95], v[220:223], v[68:71], v[92:95]\n" \
    "v_mfma_f32_16x16x32_bf16 v[80:83], v[36:39], v[52:55], 0\n" \
    "v_mfma_f32_16x16x32_bf16 v[84:87], v[40:43], v[52:55], 0\n" \
    "v_mov_b32 v193, v194\n" \
    "v_mov_b32 v194, v196\n" \
    "v_mov_b32 v195, v198\n" \
    "v_mov_b32 v201, v202\n" \
    "v_mov_b32 v202, v204\n" \
    "v_mov_b32 v203, v206\n" \
    "v_mov_b32 v209, v210\n" \
    "v_mov_b32 v210, v212\n" \
    "v_mov_b32 v211, v214\n" \
    "v_mov_b32 v217, v218\n" \
    "v_mov_b32 v218, v220\n" \
    "v_mov_b32 v219, v222\n" \
    "ds_write_b128 v8, v[88:91] offset:2048\n" \
    "ds_write_b128 v8, v[92:95] offset:3072\n" \
    "v_permlane16_swap_b32 v192, v194\n" \
    "v_permlane16_swap_b32 v193, v195\n" \
    "v_permlane16_swap_b32 v200, v202\n" \
    "v_permlane16_swap_b32 v201, v203\n" \
    "v_permlane16_swap_b32 v208, v210\n" \
    "v_permlane16_swap_b32 v209, v211\n" \
    "v_permlane16_swap_b32 v216, v218\n" \
    "v_permlane16_swap_b32 v217, v219\n" \
    "v_mfma_f32_16x16x32_bf16 v[80:83], v[192:195], v[44:47], v[80:83]\n" \
    "v_mfma_f32_16x16x32_bf16 v[84:87], v[208:211], v[44:47], v[84:87]\n" \
    "v_mfma_f32_16x16x32_bf16 v[80:83], v[200:203], v[48:51], v[80:83]\n" \
    "v_mfma_f32_16x16x32_bf16 v[84:87], v[216:219], v[48:51], v[84:87]\n" \
    "s_nop 6\n" \
    "ds_write_b128 v8, v[80:83] offset:0\n" \
    "ds_write_b128 v8, v[84:87] offset:1024\n" \
    "s_waitcnt lgkmcnt(0)\n" \
    "s_barrier\n" \
    "s_mov_b32 s55, s62\n" \
    "buffer_load_dwordx2 v[16:17], v3, s[8:11], s51 offen sc1\n" \
    "s_cmp_eq_u32 s54, 0\n" \
    "s_cbranch_scc1 .Lr_cbA0_%=\n" \
    ".Lr_cbrA0_%=:\n" \
    "s_add_u32 s32, s32, 1\n" \
    "s_add_u32 s49, s32, 1\n" \
    "s_add_u32 s36, s18, s46\n" \
    "s_add_u32 s37, s36, 16384\n" \
    "s_and_b32 s33, s32, 3\n" \
    "s_lshl_b32 s35, s33, 16\n" \
    "s_add_u32 s35, s35, s17\n" \
    ".Lr_tvA1_%=:\n" \
    "s_waitcnt vmcnt(0)\n" \
    "v_cmp_ne_u32 vcc, s49, v17\n" \
    "v_subrev_u32 v19, s55, v16\n" \
    "s_and_b64 vcc, vcc, s[56:57]\n" \
    "s_cbranch_scc1 .Lr_ctvA1_%=\n" \
    "v_max_i32_dpp v19, v19, v19 quad_perm:[1,0,3,2] row_mask:0xf bank_mask:0xf bound_ctrl:1\n" \
    "s_nop 1\n" \
    "v_max_i32_dpp v19, v19, v19 quad_perm:[2,3,0,1] row_mask:0xf bank_mask:0xf bound_ctrl:1\n" \
    "s_nop 0\n" \
    "v_readlane_b32 s48, v19, 4\n" \
    "v_readlane_b32 s47, v19, 0\n" \
    "s_add_u32 s48, s48, s55\n" \
    "s_add_u32 s48, s48, s22\n" \
    ".Lr_goA1_%=:\n" \
    "s_branch .Lr_uA1b_%=\n" \
    ".Lr_uA1a_%=:\n" \
    "s_sleep 1\n" \
    ".Lr_uA1b_%=:\n" \
    "s_memrealtime vcc\n" \
    "s_waitcnt lgkmcnt(0)\n" \
    "s_sub_i32 vcc_lo, s48, vcc_lo\n" \
    "s_cmp_gt_i32 vcc_lo, 8\n" \
    "s_cbranch_scc1 .Lr_uA1a_%=\n" \
    "s_cmp_gt_i32 vcc_lo, 0\n" \
    "s_cbranch_scc1 .Lr_uA1b_%=\n" \
    "buffer_load_dwordx4 v[36:39], v0, s[8:11], s36 offen sc1\n" \
    "buffer_load_dwordx4 v[40:43], v0, s[8:11], s37 offen sc1\n" \
    "s_add_u32 s47, s47, s55\n" \
    "s_add_u32 s47, s47, %[dr]\n" \
    "s_branch .Lr_pA1b_%=\n" \
    ".Lr_pA1a_%=:\n" \
    "s_sleep 1\n" \
    ".Lr_pA1b_%=:\n" \
    "s_memrealtime vcc\n" \
    "s_waitcnt lgkmcnt(0)\n" \
    "s_sub_i32 vcc_lo, s47, vcc_lo\n" \
    "s_cmp_gt_i32 vcc_lo, 8\n" \
    "s_cbranch_scc1 .Lr_pA1a_%=\n" \
    "s_cmp_gt_i32 vcc_lo, 0\n" \
    "s_cbranch_scc1 .Lr_pA1b_%=\n" \
    "buffer_load_dwordx4 v[32:35], v4, s[8:11], s35 offen sc1\n" \
    "buffer_load_dwordx4 v[24:27], v0, s[8:11], s35 offen sc1\n" \
    "buffer_load_dwordx4 v[28:31], v0, s[8:11], s35 offen offset:1024 sc1\n" \
    "s_and_b32 s33, s32, 3\n" \
    "s_lshl_b32 s61, s33, 17\n" \
    "s_add_u32 s41, s61, s20\n" \
    "s_add_u32 s42, s41, 4096\n" \
    "s_barrier\n" \
    "buffer_load_dword v18, v2, s[8:11], 0 offen sc1\n" \
    "buffer_load_dwordx4 v[52:55], v0, s[8:11], s41 offen offset:2048 sc1\n" \
    "buffer_load_dwordx4 v[44:47], v0, s[8:11], s41 offen offset:0 sc1\n" \
    "buffer_load_dwordx4 v[48:51], v0, s[8:11], s41 offen offset:1024 sc1\n" \
    "buffer_load_dwordx4 v[56:59], v0, s[8:11], s41 offen offset:3072 sc1\n" \
    "s_add_u32 s46, s46, 655360\n" \
    "s_cmp_eq_u32 s46, 1966080\n" \
    "s_cselect_b32 s46, 0, s46\n" \
    "s_and_b32 s33, s49, 3\n" \
    "s_lshl_b32 s38, s33, 15\n" \
    "s_and_b32 s33, s49, %[brn]-1\n" \
    "s_mul_i32 s39, s33, 2621440\n" \
    "s_add_u32 s39, s39, s19\n" \
    "s_lshr_b32 s33, s39, 8\n" \
    "s_xor_b32 s33, s33, s27\n" \
    "s_and_b32 s33, s33, 0x2000\n" \
    "s_or_b32 s39, s39, s33\n" \
    "s_mul_i32 s43, s32, 40960\n" \
    "s_add_u32 s43, s43, s21\n" \
    "s_cmp_eq_u32 s32, 63\n" \
    "s_cselect_b32 s38, 0x80000000, s38\n" \
    "s_waitcnt vmcnt(9)\n" \
    "v_max3_u32 v19, v36, v37, v38\n" \
    "s_waitcnt vmcnt(8)\n" \
    "v_max3_u32 v19, v19, v39, v40\n" \
    "v_max3_u32 v19, v19, v41, v42\n" \
    "v_max_u32 v19, v19, v43\n" \
    "s_waitcnt vmcnt(7)\n" \
    "v_mfma_f32_16x16x32_bf16 v[80:83], v[32:35], v[36:39], 0\n" \
    "v_max3_u32 v20, v32, v33, v34\n" \
    "v_mfma_f32_16x16x32_bf16 v[84:87], v[32:35], v[40:43], 0\n" \
    "v_max_u32 v20, v20, v35\n" \
    "s_waitcnt vmcnt(6)\n" \
    "v_smfmac_f32_16x16x64_bf16 v[80:83], v[24:27], v[224:231], v10\n" \
    "v_smfmac_f32_16x16x64_bf16 v[84:87], v[24:27], v[240:247], v10\n" \
    "v_max3_u32 v19, v19, v24, v25\n" \
    "v_max3_u32 v19, v19, v26, v27\n" \
    "s_waitcnt vmcnt(5)\n" \
    "v_smfmac_f32_16x16x64_bf16 v[80:83], v[28:31], v[232:239], v10\n" \
    "v_smfmac_f32_16x16x64_bf16 v[84:87], v[28:31], v[248:255], v10\n" \
    "v_max3_u32 v20, v20, v28, v29\n" \
    "v_max3_u32 v20, v20, v30, v31\n" \
    "v_max_u32 v19, v19, v20\n" \
    "v_cmp_eq_u32 vcc, -1, v19\n" \
    "s_nop 0\n" \
    "s_cbranch_vccnz .Lr_cpA1_%=\n" \
    ".Lr_cprA1_%=:\n" \
    "s_nop 0\n" \
    "v_pk_add_f32 v[80:81], v[80:81], v[82:83]\n" \
    "v_pk_add_f32 v[84:85], v[84:85], v[86:87]\n" \
    "ds_write2_b32 v5, v80, v81 offset0:0 offset1:4\n" \
    "ds_write2_b32 v5, v84, v85 offset0:64 offset1:68\n" \
    "s_waitcnt lgkmcnt(0)\n" \
    "s_barrier\n" \
    "ds_read_b128 v[96:99], v0 offset:0\n" \
    "ds_read_b128 v[100:103], v0 offset:1024\n" \
    "ds_read_b128 v[104:107], v0 offset:2048\n" \
    "ds_read_b128 v[108:111], v0 offset:3072\n" \
    "ds_read_b128 v[112:115], v0 offset:4096\n" \
    "ds_read_b128 v[116:119], v0 offset:5120\n" \
    "ds_read_b128 v[120:123], v0 offset:6144\n" \
    "ds_read_b128 v[124:127], v0 offset:7168\n" \
    "s_waitcnt lgkmcnt(6)\n" \
    "v_pk_add_f32 v[96:97], v[96:97], v[100:101]\n" \
    "v_pk_add_f32 v[98:99], v[98:99], v[102:103]\n" \
    "s_waitcnt lgkmcnt(4)\n" \
    "v_pk_add_f32 v[104:105], v[104:105], v[108:109]\n" \
    "v_pk_add_f32 v[106:107], v[106:107], v[110:111]\n" \
    "s_waitcnt lgkmcnt(2)\n" \
    "v_pk_add_f32 v[112:113], v[112:113], v[116:117]\n" \
    "v_pk_add_f32 v[114:115], v[114:115], v[118:119]\n" \
    "v_pk_add_f32 v[96:97], v[96:97], v[104:105]\n" \
    "v_pk_add_f32 v[98:99], v[98:99], v[106:107]\n" \
    "s_waitcnt lgkmcnt(0)\n" \
    "v_pk_add_f32 v[120:121], v[120:121], v[124:125]\n" \
    "v_pk_add_f32 v[122:123], v[122:123], v[126:127]\n" \
    "v_pk_add_f32 v[96:97], v[96:97], v[112:113]\n" \
    "v_pk_add_f32 v[98:99], v[98:99], v[114:115]\n" \
    "v_pk_add_f32 v[96:97], v[96:97], v[120:121]\n" \
    "v_pk_add_f32 v[98:99], v[98:99], v[122:123]\n" \
    "v_cvt_pk_bf16_f32 v96, v96, v97\n" \
    "v_cvt_pk_bf16_f32 v97, v98, v99\n" \
    "buffer_store_dwordx2 v[96:97], v1, s[8:11], s38 offen sc1\n" \
    "s_barrier\n" \
    "s_memrealtime s[62:63]\n" \
    "buffer_load_dwordx4 v[60:63], v0, s[8:11], s42 offen offset:0 sc1\n" \
    "buffer_load_dwordx4 v[64:67], v0, s[8:11], s42 offen offset:1024 sc1\n" \
    "buffer_load_dwordx4 v[68:71], v0, s[8:11], s42 offen offset:2048 sc1\n" \
    "buffer_load_dwordx4 v[72:75], v0, s[8:11], s42 offen offset:3072 sc1\n" \
    "s_waitcnt vmcnt(9)\n" \
    "v_cmp_ge_u32 vcc, s49, v18\n" \
    "s_nop 0\n" \
    "s_and_b64 vcc, vcc, s[56:57]\n" \
    "s_cselect_b32 s54, 0, 1\n" \
    "s_cmp_eq_u32 s32, 63\n" \
    "s_cselect_b32 s54, 2, s54\n" \
    "s_cmp_eq_u32 s54, 1\n" \
    "s_cselect_b32 s39, s39, 0x80000000\n" \
    "s_add_u32 s40, s39, 65536\n" \
    "s_barrier\n" \
    "buffer_load_dwordx4 v[192:195], v0, s[8:11], s39 offen offset:0 sc1\n" \
    "buffer_load_dwordx4 v[196:199], v0, s[8:11], s39 offen offset:1024 sc1\n" \
    "buffer_load_dwordx4 v[200:203], v0, s[8:11], s39 offen offset:2048 sc1\n" \
    "buffer_load_dwordx4 v[204:207], v0, s[8:11], s39 offen offset:3072 sc1\n" \
    "buffer_load_dwordx4 v[208:211], v0, s[8:11], s40 offen offset:0 sc1\n" \
    "buffer_load_dwordx4 v[212:215], v0, s[8:11], s40 offen offset:1024 sc1\n" \
    "buffer_load_dwordx4 v[216:219], v0, s[8:11], s40 offen offset:2048 sc1\n" \
    "buffer_load_dwordx4 v[220:223], v0, s[8:11], s40 offen offset:3072 sc1\n" \
    "s_and_b32 s33, s32, s23\n" \
    "s_mul_i32 s45, s33, s24\n" \
    "s_cmp_lt_u32 s33, 64-%[pdist]\n" \
    "s_cselect_b32 s45, s45, 0x80000000\n" \
    "s_add_u32 s44, s43, %[x1]\n" \
    "s_xor_b32 s51, s51, 16384\n" \
    "s_waitcnt vmcnt(15)\n" \
    "v_max3_u32 v19, v44, v45, v46\n" \
    "s_waitcnt vmcnt(14)\n" \
    "v_max3_u32 v19, v19, v47, v48\n" \
    "v_max3_u32 v19, v19, v49, v50\n" \
    "v_max3_u32 v19, v19, v51, v52\n" \
    "v_max3_u32 v19, v19, v53, v54\n" \
    "s_waitcnt vmcnt(13)\n" \
    "v_max3_u32 v19, v19, v55, v56\n" \
    "v_max3_u32 v19, v19, v57, v58\n" \
    "s_waitcnt vmcnt(11)\n" \
    "v_max3_u32 v19, v19, v59, v60\n" \
    "v_max3_u32 v19, v19, v61, v62\n" \
    "s_waitcnt vmcnt(10)\n" \
    "v_max3_u32 v19, v19, v63, v64\n" \
    "v_max3_u32 v19, v19, v65, v66\n" \
    "s_waitcnt vmcnt(9)\n" \
    "v_max3_u32 v19, v19, v67, v68\n" \
    "v_max3_u32 v19, v19, v69, v70\n" \
    "s_waitcnt vmcnt(8)\n" \
    "v_max3_u32 v19, v19, v71, v72\n" \
    "v_max3_u32 v19, v19, v73, v74\n" \
    "v_max_u32 v19, v19, v75\n" \
    "v_cmp_eq_u32 vcc, -1, v19\n" \
    "s_nop 0\n" \
    "s_cbranch_vccnz .Lr_ctA1_%=\n" \
    ".Lr_ctrA1_%=:\n" \
    "v_mfma_f32_16x16x32_bf16 v[88:91], v[36:39], v[72:75], 0\n" \
    "v_mfma_f32_16x16x32_bf16 v[88:91], v[224:227], v[56:59], v[88:91]\n" \
    "v_mfma_f32_16x16x32_bf16 v[88:91], v[228:231], v[60:63], v[88:91]\n" \
    "v_mfma_f32_16x16x32_bf16 v[88:91], v[232:235], v[64:67], v[88:91]\n" \
    "v_mfma_f32_16x16x32_bf16 v[88:91], v[236:239], v[68:71], v[88:91]\n" \
    "v_mfma_f32_16x16x32_bf16 v[92:95], v[40:43], v[72:75], 0\n" \
    "v_mfma_f32_16x16x32_bf16 v[92:95], v[240:243], v[56:59], v[92:95]\n" \
    "v_mfma_f32_16x16x32_bf16 v[92:95], v[244:247], v[60:63], v[92:95]\n" \
    "v_mfma_f32_16x16x32_bf16 v[92:95], v[248:251], v[64:67], v[92:95]\n" \
    "v_mfma_f32_16x16x32_bf16 v[92:95], v[252:255], v[68:71], v[92:95]\n" \
    "v_mfma_f32_16x16x32_bf16 v[80:83], v[36:39], v[52:55], 0\n" \
    "v_mfma_f32_16x16x32_bf16 v[84:87], v[40:43], v[52:55], 0\n" \
    "v_mov_b32 v225, v226\n" \
    "v_mov_b32 v226, v228\n" \
    "v_mov_b32 v227, v230\n" \
    "v_mov_b32 v233, v234\n" \
    "v_mov_b32 v234, v236\n" \
    "v_mov_b32 v235, v238\n" \
    "v_mov_b32 v241, v242\n" \
    "v_mov_b32 v242, v244\n" \
    "v_mov_b32 v243, v246\n" \
    "v_mov_b32 v249, v250\n" \
    "v_mov_b32 v250, v252\n" \
    "v_mov_b32 v251, v254\n" \
    "ds_write_b128 v8, v[88:91] offset:2048\n" \
    "ds_write_b128 v8, v[92:95] offset:3072\n" \
    "v_permlane16_swap_b32 v224, v226\n" \
    "v_permlane16_swap_b32 v225, v227\n" \
    "v_permlane16_swap_b32 v232, v234\n" \
    "v_permlane16_swap_b32 v233, v235\n" \
    "v_permlane16_swap_b32 v240, v242\n" \
    "v_permlane16_swap_b32 v241, v243\n" \
    "v_permlane16_swap_b32 v248, v250\n" \
    "v_permlane16_swap_b32 v249, v251\n" \
    "v_mfma_f32_16x16x32_bf16 v[80:83], v[224:227], v[44:47], v[80:83]\n" \
    "v_mfma_f32_16x16x32_bf16 v[84:87], v[240:243], v[44:47], v[84:87]\n" \
    "v_mfma_f32_16x16x32_bf16 v[80:83], v[232:235], v[48:51], v[80:83]\n" \
    "v_mfma_f32_16x16x32_bf16 v[84:87], v[248:251], v[48:51], v[84:87]\n" \
    "s_nop 6\n" \
    "ds_write_b128 v8, v[80:83] offset:0\n" \
    "ds_write_b128 v8, v[84:87] offset:1024\n" \
    "s_waitcnt lgkmcnt(0)\n" \
    "s_barrier\n" \
    "s_mov_b32 s55, s62\n" \
    "buffer_load_dwordx2 v[16:17], v3, s[8:11], s51 offen sc1\n" \
    "s_cmp_eq_u32 s54, 0\n" \
    "s_cbranch_scc1 .Lr_cbA1_%=\n" \
    ".Lr_cbrA1_%=:\n" \
    "s_add_u32 s32, s32, 1\n" \
    "s_cmp_lt_u32 s32, 64\n" \
    "s_cbranch_scc1 .Lr_lA_%=\n" \
    "s_waitcnt vmcnt(0)\n" \
    "s_endpgm\n" \
    ".Lr_lB_%=:\n" \
    "s_add_u32 s49, s32, 1\n" \
    "s_add_u32 s36, s18, s46\n" \
    "s_add_u32 s37, s36, 16384\n" \
    "s_and_b32 s33, s32, 3\n" \
    "s_lshl_b32 s35, s33, 16\n" \
    "s_add_u32 s35, s35, s17\n" \
    ".Lr_tvB0_%=:\n" \
    "s_waitcnt vmcnt(1)\n" \
    "v_cmp_ne_u32 vcc, s49, v17\n" \
    "v_subrev_u32 v19, s55, v16\n" \
    "s_and_b64 vcc, vcc, s[56:57]\n" \
    "s_cbranch_scc1 .Lr_ctvB0_%=\n" \
    "v_max_i32_dpp v19, v19, v19 quad_perm:[1,0,3,2] row_mask:0xf bank_mask:0xf bound_ctrl:1\n" \
    "s_nop 1\n" \
    "v_max_i32_dpp v19, v19, v19 quad_perm:[2,3,0,1] row_mask:0xf bank_mask:0xf bound_ctrl:1\n" \
    "s_nop 0\n" \
    "v_readlane_b32 s48, v19, 4\n" \
    "v_readlane_b32 s47, v19, 0\n" \
    "s_add_u32 s48, s48, s55\n" \
    "s_add_u32 s48, s48, s22\n" \
    ".Lr_goB0_%=:\n" \
    "s_branch .Lr_uB0b_%=\n" \
    ".Lr_uB0a_%=:\n" \
    "s_sleep 1\n" \
    ".Lr_uB0b_%=:\n" \
    "s_memrealtime vcc\n" \
    "s_waitcnt lgkmcnt(0)\n" \
    "s_sub_i32 vcc_lo, s48, vcc_lo\n" \
    "s_cmp_gt_i32 vcc_lo, 8\n" \
    "s_cbranch_scc1 .Lr_uB0a_%=\n" \
    "s_cmp_gt_i32 vcc_lo, 0\n" \
    "s_cbranch_scc1 .Lr_uB0b_%=\n" \
    "buffer_load_dwordx4 v[36:39], v0, s[8:11], s36 offen sc1\n" \
    "buffer_load_dwordx4 v[40:43], v0, s[8:11], s37 offen sc1\n" \
    "s_add_u32 s47, s47, s55\n" \
    "s_add_u32 s47, s47, %[dr]\n" \
    "s_branch .Lr_pB0b_%=\n" \
    ".Lr_pB0a_%=:\n" \
    "s_sleep 1\n" \
    ".Lr_pB0b_%=:\n" \
    "s_memrealtime vcc\n" \
    "s_waitcnt lgkmcnt(0)\n" \
    "s_sub_i32 vcc_lo, s47, vcc_lo\n" \
    "s_cmp_gt_i32 vcc_lo, 8\n" \
    "s_cbranch_scc1 .Lr_pB0a_%=\n" \
    "s_cmp_gt_i32 vcc_lo, 0\n" \
    "s_cbranch_scc1 .Lr_pB0b_%=\n" \
    "buffer_load_dwordx4 v[32:35], v4, s[8:11], s35 offen sc1\n" \
    "buffer_load_dwordx4 v[24:27], v0, s[8:11], s35 offen sc1\n" \
    "buffer_load_dwordx4 v[28:31], v0, s[8:11], s35 offen offset:1024 sc1\n" \
    "s_and_b32 s33, s32, 3\n" \
    "s_lshl_b32 s61, s33, 17\n" \
    "s_add_u32 s41, s61, s20\n" \
    "s_add_u32 s42, s41, 4096\n" \
    "s_barrier\n" \
    "buffer_load_dword v18, v2, s[8:11], 0 offen sc1\n" \
    "buffer_load_dwordx4 v[52:55], v0, s[8:11], s41 offen offset:2048 sc1\n" \
    "buffer_load_dwordx4 v[44:47], v0, s[8:11], s41 offen offset:0 sc1\n" \
    "buffer_load_dwordx4 v[48:51], v0, s[8:11], s41 offen offset:1024 sc1\n" \
    "buffer_load_dwordx4 v[56:59], v0, s[8:11], s41 offen offset:3072 sc1\n" \
    "s_add_u32 s46, s46, 655360\n" \
    "s_cmp_eq_u32 s46, 1966080\n" \
    "s_cselect_b32 s46, 0, s46\n" \
    "s_and_b32 s33, s49, 3\n" \
    "s_lshl_b32 s38, s33, 15\n" \
    "s_and_b32 s33, s49, %[brn]-1\n" \
    "s_mul_i32 s39, s33, 2621440\n" \
    "s_add_u32 s39, s39, s19\n" \
    "s_lshr_b32 s33, s39, 8\n" \
    "s_xor_b32 s33, s33, s27\n" \
    "s_and_b32 s33, s33, 0x2000\n" \
    "s_or_b32 s39, s39, s33\n" \
    "s_mul_i32 s43, s32, 40960\n" \
    "s_add_u32 s43, s43, s21\n" \
    "s_cmp_eq_u32 s32, 63\n" \
    "s_cselect_b32 s38, 0x80000000, s38\n" \
    "s_waitcnt vmcnt(9)\n" \
    "v_max3_u32 v19, v36, v37, v38\n" \
    "s_waitcnt vmcnt(8)\n" \
    "v_max3_u32 v19, v19, v39, v40\n" \
    "v_max3_u32 v19, v19, v41, v42\n" \
    "v_max_u32 v19, v19, v43\n" \
    "s_waitcnt vmcnt(7)\n" \
    "v_mfma_f32_16x16x32_bf16 v[80:83], v[32:35], v[36:39], 0\n" \
    "v_max3_u32 v20, v32, v33, v34\n" \
    "v_mfma_f32_16x16x32_bf16 v[84:87], v[32:35], v[40:43], 0\n" \
    "v_max_u32 v20, v20, v35\n" \
    "s_waitcnt vmcnt(6)\n" \
    "v_smfmac_f32_16x16x64_bf16 v[80:83], v[24:27], v[192:199], v10\n" \
    "v_smfmac_f32_16x16x64_bf16 v[84:87], v[24:27], v[208:215], v10\n" \
    "v_max3_u32 v19, v19, v24, v25\n" \
    "v_max3_u32 v19, v19, v26, v27\n" \
    "s_waitcnt vmcnt(5)\n" \
    "v_smfmac_f32_16x16x64_bf16 v[80:83], v[28:31], v[200:207], v10\n" \
    "v_smfmac_f32_16x16x64_bf16 v[84:87], v[28:31], v[216:223], v10\n" \
    "v_max3_u32 v20, v20, v28, v29\n" \
    "v_max3_u32 v20, v20, v30, v31\n" \
    "v_max_u32 v19, v19, v20\n" \
    "v_cmp_eq_u32 vcc, -1, v19\n" \
    "s_nop 0\n" \
    "s_cbranch_vccnz .Lr_cpB0_%=\n" \
    ".Lr_cprB0_%=:\n" \
    "s_nop 0\n" \
    "v_pk_add_f32 v[80:81], v[80:81], v[82:83]\n" \
    "v_pk_add_f32 v[84:85], v[84:85], v[86:87]\n" \
    "ds_write2_b32 v5, v80, v81 offset0:0 offset1:4\n" \
    "ds_write2_b32 v5, v84, v85 offset0:64 offset1:68\n" \
    "s_waitcnt lgkmcnt(0)\n" \
    "s_barrier\n" \
    "s_barrier\n" \
    "s_memrealtime s[62:63]\n" \
    "buffer_load_dwordx4 v[60:63], v0, s[8:11], s42 offen offset:0 sc1\n" \
    "buffer_load_dwordx4 v[64:67], v0, s[8:11], s42 offen offset:1024 sc1\n" \
    "buffer_load_dwordx4 v[68:71], v0, s[8:11], s42 offen offset:2048 sc1\n" \
    "buffer_load_dwordx4 v[72:75], v0, s[8:11], s42 offen offset:3072 sc1\n" \
    "s_waitcnt vmcnt(8)\n" \
    "v_cmp_ge_u32 vcc, s49, v18\n" \
    "s_nop 0\n" \
    "s_and_b64 vcc, vcc, s[56:57]\n" \
    "s_cselect_b32 s54, 0, 1\n" \
    "s_cmp_eq_u32 s32, 63\n" \
    "s_cselect_b32 s54, 2, s54\n" \
    "s_cmp_eq_u32 s54, 1\n" \
    "s_cselect_b32 s39, s39, 0x80000000\n" \
    "s_add_u32 s40, s39, 65536\n" \
    "s_barrier\n" \
    "s_add_u32 s33, s43, %[x0]\n" \
    "buffer_load_dwordx4 v[76:79], v6, s[8:11], s33 offen sc1\n" \
    "buffer_load_dwordx4 v[224:227], v0, s[8:11], s39 offen offset:0 sc1\n" \
    "buffer_load_dwordx4 v[228:231], v0, s[8:11], s39 offen offset:1024 sc1\n" \
    "buffer_load_dwordx4 v[232:235], v0, s[8:11], s39 offen offset:2048 sc1\n" \
    "buffer_load_dwordx4 v[236:239], v0, s[8:11], s39 offen offset:3072 sc1\n" \
    "buffer_load_dwordx4 v[240:243], v0, s[8:11], s40 offen offset:0 sc1\n" \
    "buffer_load_dwordx4 v[244:247], v0, s[8:11], s40 offen offset:1024 sc1\n" \
    "buffer_load_dwordx4 v[248:251], v0, s[8:11], s40 offen offset:2048 sc1\n" \
    "buffer_load_dwordx4 v[252:255], v0, s[8:11], s40 offen offset:3072 sc1\n" \
    "s_and_b32 s33, s32, s23\n" \
    "s_mul_i32 s45, s33, s24\n" \
    "s_cmp_lt_u32 s33, 64-%[pdist]\n" \
    "s_cselect_b32 s45, s45, 0x80000000\n" \
    "s_add_u32 s44, s43, %[x1]\n" \
    "s_xor_b32 s51, s51, 16384\n" \
    "s_waitcnt vmcnt(15)\n" \
    "v_max3_u32 v19, v44, v45, v46\n" \
    "s_waitcnt vmcnt(14)\n" \
    "v_max3_u32 v19, v19, v47, v48\n" \
    "v_max3_u32 v19, v19, v49, v50\n" \
    "v_max3_u32 v19, v19, v51, v52\n" \
    "v_max3_u32 v19, v19, v53, v54\n" \
    "s_waitcnt vmcnt(13)\n" \
    "v_max3_u32 v19, v19, v55, v56\n" \
    "v_max3_u32 v19, v19, v57, v58\n" \
    "s_waitcnt vmcnt(12)\n" \
    "v_max3_u32 v19, v19, v59, v60\n" \
    "v_max3_u32 v19, v19, v61, v62\n" \
    "s_waitcnt vmcnt(11)\n" \
    "v_max3_u32 v19, v19, v63, v64\n" \
    "v_max3_u32 v19, v19, v65, v66\n" \
    "s_waitcnt vmcnt(10)\n" \
    "v_max3_u32 v19, v19, v67, v68\n" \
    "v_max3_u32 v19, v19, v69, v70\n" \
    "s_waitcnt vmcnt(9)\n" \
    "v_max3_u32 v19, v19, v71, v72\n" \
    "v_max3_u32 v19, v19, v73, v74\n" \
    "v_max_u32 v19, v19, v75\n" \
    "v_cmp_eq_u32 vcc, -1, v19\n" \
    "s_nop 0\n" \
    "s_cbranch_vccnz .Lr_ctB0_%=\n" \
    ".Lr_ctrB0_%=:\n" \
    "v_mfma_f32_16x16x32_bf16 v[88:91], v[36:39], v[72:75], 0\n" \
    "v_mfma_f32_16x16x32_bf16 v[88:91], v[192:195], v[56:59], v[88:91]\n" \
    "v_mfma_f32_16x16x32_bf16 v[88:91], v[196:199], v[60:63], v[88:91]\n" \
    "v_mfma_f32_16x16x32_bf16 v[88:91], v[200:203], v[64:67], v[88:91]\n" \
    "v_mfma_f32_16x16x32_bf16 v[88:91], v[204:207], v[68:71], v[88:91]\n" \
    "v_mfma_f32_16x16x32_bf16 v[92:95], v[40:43], v[72:75], 0\n" \
    "v_mfma_f32_16x16x32_bf16 v[92:95], v[208:211], v[56:59], v[92:95]\n" \
    "v_mfma_f32_16x16x32_bf16 v[92:95], v[212:215], v[60:63], v[92:95]\n" \
    "v_mfma_f32_16x16x32_bf16 v[92:95], v[216:219], v[64:67], v[92:95]\n" \
    "v_mfma_f32_16x16x32_bf16 v[92:95], v[220:223], v[68:71], v[92:95]\n" \
    "v_mfma_f32_16x16x32_bf16 v[80:83], v[36:39], v[52:55], 0\n" \
    "v_mfma_f32_16x16x32_bf16 v[84:87], v[40:43], v[52:55], 0\n" \
    "v_mov_b32 v193, v194\n" \
    "v_mov_b32 v194, v196\n" \
    "v_mov_b32 v195, v198\n" \
    "v_mov_b32 v201, v202\n" \
    "v_mov_b32 v202, v204\n" \
    "v_mov_b32 v203, v206\n" \
    "v_mov_b32 v209, v210\n" \
    "v_mov_b32 v210, v212\n" \
    "v_mov_b32 v211, v214\n" \
    "v_mov_b32 v217, v218\n" \
    "v_mov_b32 v218, v220\n" \
    "v_mov_b32 v219, v222\n" \
    "ds_write_b128 v8, v[88:91] offset:2048\n" \
    "ds_write_b128 v8, v[92:95] offset:3072\n" \
    "v_permlane16_swap_b32 v192, v194\n" \
    "v_permlane16_swap_b32 v193, v195\n" \
    "v_permlane16_swap_b32 v200, v202\n" \
    "v_permlane16_swap_b32 v201, v203\n" \
    "v_permlane16_swap_b32 v208, v210\n" \
    "v_permlane16_swap_b32 v209, v211\n" \
    "v_permlane16_swap_b32 v216, v218\n" \
    "v_permlane16_swap_b32 v217, v219\n" \
    "v_mfma_f32_16x16x32_bf16 v[80:83], v[192:195], v[44:47], v[80:83]\n" \
    "v_mfma_f32_16x16x32_bf16 v[84:87], v[208:211], v[44:47], v[84:87]\n" \
    "v_mfma_f32_16x16x32_bf16 v[80:83], v[200:203], v[48:51], v[80:83]\n" \
    "v_mfma_f32_16x16x32_bf16 v[84:87], v[216:219], v[48:51], v[84:87]\n" \
    "s_nop 6\n" \
    "ds_write_b128 v8, v[80:83] offset:0\n" \
    "ds_write_b128 v8, v[84:87] offset:1024\n" \
    "s_waitcnt lgkmcnt(0)\n" \
    "s_barrier\n" \
    "s_mov_b32 s55, s62\n" \
    "buffer_load_dwordx2 v[16:17], v3, s[8:11], s51 offen sc1\n" \
    "ds_read_b128 v[96:99], v7 offset:0\n" \
    "ds_read_b128 v[100:103], v7 offset:256\n" \
    "ds_read_b128 v[104:107], v7 offset:4096\n" \
    "ds_read_b128 v[108:111], v7 offset:4352\n" \
    "ds_read_b128 v[112:115], v7 offset:8192\n" \
    "ds_read_b128 v[116:119], v7 offset:8448\n" \
    "ds_read_b128 v[120:123], v7 offset:12288\n" \
    "ds_read_b128 v[124:127], v7 offset:12544\n" \
    "ds_read_b128 v[128:131], v7 offset:16384\n" \
    "ds_read_b128 v[132:135], v7 offset:16640\n" \
    "ds_read_b128 v[136:139], v7 offset:20480\n" \
    "ds_read_b128 v[140:143], v7 offset:20736\n" \
    "ds_read_b128 v[144:147], v7 offset:24576\n" \
    "ds_read_b128 v[148:151], v7 offset:24832\n" \
    "ds_read_b128 v[152:155], v7 offset:28672\n" \
    "ds_read_b128 v[156:159], v7 offset:28928\n" \
    "s_waitcnt lgkmcnt(13)\n" \
    "v_pk_add_f32 v[96:97], v[96:97], v[104:105]\n" \
    "v_pk_add_f32 v[98:99], v[98:99], v[106:107]\n" \
    "s_waitcnt lgkmcnt(12)\n" \
    "v_pk_add_f32 v[100:101], v[100:101], v[108:109]\n" \
    "v_pk_add_f32 v[102:103], v[102:103], v[110:111]\n" \
    "s_waitcnt lgkmcnt(9)\n" \
    "v_pk_add_f32 v[112:113], v[112:113], v[120:121]\n" \
    "v_pk_add_f32 v[114:115], v[114:115], v[122:123]\n" \
    "s_waitcnt lgkmcnt(8)\n" \
    "v_pk_add_f32 v[116:117], v[116:117], v[124:125]\n" \
    "v_pk_add_f32 v[118:119], v[118:119], v[126:127]\n" \
    "v_pk_add_f32 v[96:97], v[96:97], v[112:113]\n" \
    "v_pk_add_f32 v[98:99], v[98:99], v[114:115]\n" \
    "v_pk_add_f32 v[100:101], v[100:101], v[116:117]\n" \
    "v_pk_add_f32 v[102:103], v[102:103], v[118:119]\n" \
    "s_waitcnt lgkmcnt(5)\n" \
    "v_pk_add_f32 v[128:129], v[128:129], v[136:137]\n" \
    "v_pk_add_f32 v[130:131], v[130:131], v[138:139]\n" \
    "s_waitcnt lgkmcnt(4)\n" \
    "v_pk_add_f32 v[132:133], v[132:133], v[140:141]\n" \
    "v_pk_add_f32 v[134:135], v[134:135], v[142:143]\n" \
    "s_waitcnt lgkmcnt(1)\n" \
    "v_pk_add_f32 v[144:145], v[144:145], v[152:153]\n" \
    "v_pk_add_f32 v[146:147], v[146:147], v[154:155]\n" \
    "s_waitcnt lgkmcnt(0)\n" \
    "v_pk_add_f32 v[148:149], v[148:149], v[156:157]\n" \
    "v_pk_add_f32 v[150:151], v[150:151], v[158:159]\n" \
    "v_pk_add_f32 v[128:129], v[128:129], v[144:145]\n" \
    "v_pk_add_f32 v[130:131], v[130:131], v[146:147]\n" \
    "v_pk_add_f32 v[132:133], v[132:133], v[148:149]\n" \
    "v_pk_add_f32 v[134:135], v[134:135], v[150:151]\n" \
    "v_pk_add_f32 v[96:97], v[96:97], v[128:129]\n" \
    "v_pk_add_f32 v[98:99], v[98:99], v[130:131]\n" \
    "v_pk_add_f32 v[100:101], v[100:101], v[132:133]\n" \
    "v_pk_add_f32 v[102:103], v[102:103], v[134:135]\n" \
    "v_cvt_pk_bf16_f32 v104, v96, v97\n" \
    "v_cvt_pk_bf16_f32 v105, v98, v99\n" \
    "v_cvt_pk_bf16_f32 v106, v100, v101\n" \
    "v_cvt_pk_bf16_f32 v107, v102, v103\n" \
    "v_lshlrev_b32 v96, 16, v104\n" \
    "v_and_b32 v97, 0xffff0000, v104\n" \
    "s_waitcnt vmcnt(9)\n" \
    "v_lshlrev_b32 v112, 16, v76\n" \
    "v_and_b32 v113, 0xffff0000, v76\n" \
    "v_lshlrev_b32 v98, 16, v105\n" \
    "v_and_b32 v99, 0xffff0000, v105\n" \
    "v_lshlrev_b32 v114, 16, v77\n" \
    "v_and_b32 v115, 0xffff0000, v77\n" \
    "v_lshlrev_b32 v100, 16, v106\n" \
    "v_and_b32 v101, 0xffff0000, v106\n" \
    "v_lshlrev_b32 v116, 16, v78\n" \
    "v_and_b32 v117, 0xffff0000, v78\n" \
    "v_lshlrev_b32 v102, 16, v107\n" \
    "v_and_b32 v103, 0xffff0000, v107\n" \
    "v_lshlrev_b32 v118, 16, v79\n" \
    "v_and_b32 v119, 0xffff0000, v79\n" \
    "v_pk_add_f32 v[96:97], v[96:97], v[112:113]\n" \
    "v_pk_add_f32 v[98:99], v[98:99], v[114:115]\n" \
    "v_pk_add_f32 v[100:101], v[100:101], v[116:117]\n" \
    "v_pk_add_f32 v[102:103], v[102:103], v[118:119]\n" \
    "v_cvt_pk_bf16_f32 v76, v96, v97\n" \
    "v_cvt_pk_bf16_f32 v77, v98, v99\n" \
    "v_cvt_pk_bf16_f32 v78, v100, v101\n" \
    "v_cvt_pk_bf16_f32 v79, v102, v103\n" \
    "v_min_u32 v76, -2, v76\n" \
    "v_min_u32 v77, -2, v77\n" \
    "v_min_u32 v78, -2, v78\n" \
    "v_min_u32 v79, -2, v79\n" \
    "buffer_store_dwordx4 v[76:79], v6, s[8:11], s44 offen sc1\n" \
    "s_cmp_eq_u32 s54, 0\n" \
    "s_cbranch_scc1 .Lr_cbB0_%=\n" \
    ".Lr_cbrB0_%=:\n" \
    "s_add_u32 s32, s32, 1\n" \
    "s_add_u32 s49, s32, 1\n" \
    "s_add_u32 s36, s18, s46\n" \
    "s_add_u32 s37, s36, 16384\n" \
    "s_and_b32 s33, s32, 3\n" \
    "s_lshl_b32 s35, s33, 16\n" \
    "s_add_u32 s35, s35, s17\n" \
    ".Lr_tvB1_%=:\n" \
    "s_waitcnt vmcnt(1)\n" \
    "v_cmp_ne_u32 vcc, s49, v17\n" \
    "v_subrev_u32 v19, s55, v16\n" \
    "s_and_b64 vcc, vcc, s[56:57]\n" \
    "s_cbranch_scc1 .Lr_ctvB1_%=\n" \
    "v_max_i32_dpp v19, v19, v19 quad_perm:[1,0,3,2] row_mask:0xf bank_mask:0xf bound_ctrl:1\n" \
    "s_nop 1\n" \
    "v_max_i32_dpp v19, v19, v19 quad_perm:[2,3,0,1] row_mask:0xf bank_mask:0xf bound_ctrl:1\n" \
    "s_nop 0\n" \
    "v_readlane_b32 s48, v19, 4\n" \
    "v_readlane_b32 s47, v19, 0\n" \
    "s_add_u32 s48, s48, s55\n" \
    "s_add_u32 s48, s48, s22\n" \
    ".Lr_goB1_%=:\n" \
    "s_branch .Lr_uB1b_%=\n" \
    ".Lr_uB1a_%=:\n" \
    "s_sleep 1\n" \
    ".Lr_uB1b_%=:\n" \
    "s_memrealtime vcc\n" \
    "s_waitcnt lgkmcnt(0)\n" \
    "s_sub_i32 vcc_lo, s48, vcc_lo\n" \
    "s_cmp_gt_i32 vcc_lo, 8\n" \
    "s_cbranch_scc1 .Lr_uB1a_%=\n" \
    "s_cmp_gt_i32 vcc_lo, 0\n" \
    "s_cbranch_scc1 .Lr_uB1b_%=\n" \
    "buffer_load_dwordx4 v[36:39], v0, s[8:11], s36 offen sc1\n" \
    "buffer_load_dwordx4 v[40:43], v0, s[8:11], s37 offen sc1\n" \
    "s_add_u32 s47, s47, s55\n" \
    "s_add_u32 s47, s47, %[dr]\n" \
    "s_branch .Lr_pB1b_%=\n" \
    ".Lr_pB1a_%=:\n" \
    "s_sleep 1\n" \
    ".Lr_pB1b_%=:\n" \
    "s_memrealtime vcc\n" \
    "s_waitcnt lgkmcnt(0)\n" \
    "s_sub_i32 vcc_lo, s47, vcc_lo\n" \
    "s_cmp_gt_i32 vcc_lo, 8\n" \
    "s_cbranch_scc1 .Lr_pB1a_%=\n" \
    "s_cmp_gt_i32 vcc_lo, 0\n" \
    "s_cbranch_scc1 .Lr_pB1b_%=\n" \
    "buffer_load_dwordx4 v[32:35], v4, s[8:11], s35 offen sc1\n" \
    "buffer_load_dwordx4 v[24:27], v0, s[8:11], s35 offen sc1\n" \
    "buffer_load_dwordx4 v[28:31], v0, s[8:11], s35 offen offset:1024 sc1\n" \
    "s_and_b32 s33, s32, 3\n" \
    "s_lshl_b32 s61, s33, 17\n" \
    "s_add_u32 s41, s61, s20\n" \
    "s_add_u32 s42, s41, 4096\n" \
    "s_barrier\n" \
    "buffer_load_dword v18, v2, s[8:11], 0 offen sc1\n" \
    "buffer_load_dwordx4 v[52:55], v0, s[8:11], s41 offen offset:2048 sc1\n" \
    "buffer_load_dwordx4 v[44:47], v0, s[8:11], s41 offen offset:0 sc1\n" \
    "buffer_load_dwordx4 v[48:51], v0, s[8:11], s41 offen offset:1024 sc1\n" \
    "buffer_load_dwordx4 v[56:59], v0, s[8:11], s41 offen offset:3072 sc1\n" \
    "s_add_u32 s46, s46, 655360\n" \
    "s_cmp_eq_u32 s46, 1966080\n" \
    "s_cselect_b32 s46, 0, s46\n" \
    "s_and_b32 s33, s49, 3\n" \
    "s_lshl_b32 s38, s33, 15\n" \
    "s_and_b32 s33, s49, %[brn]-1\n" \
    "s_mul_i32 s39, s33, 2621440\n" \
    "s_add_u32 s39, s39, s19\n" \
    "s_lshr_b32 s33, s39, 8\n" \
    "s_xor_b32 s33, s33, s27\n" \
    "s_and_b32 s33, s33, 0x2000\n" \
    "s_or_b32 s39, s39, s33\n" \
    "s_mul_i32 s43, s32, 40960\n" \
    "s_add_u32 s43, s43, s21\n" \
    "s_cmp_eq_u32 s32, 63\n" \
    "s_cselect_b32 s38, 0x80000000, s38\n" \
    "s_waitcnt vmcnt(9)\n" \
    "v_max3_u32 v19, v36, v37, v38\n" \
    "s_waitcnt vmcnt(8)\n" \
    "v_max3_u32 v19, v19, v39, v40\n" \
    "v_max3_u32 v19, v19, v41, v42\n" \
    "v_max_u32 v19, v19, v43\n" \
    "s_waitcnt vmcnt(7)\n" \
    "v_mfma_f32_16x16x32_bf16 v[80:83], v[32:35], v[36:39], 0\n" \
    "v_max3_u32 v20, v32, v33, v34\n" \
    "v_mfma_f32_16x16x32_bf16 v[84:87], v[32:35], v[40:43], 0\n" \
    "v_max_u32 v20, v20, v35\n" \
    "s_waitcnt vmcnt(6)\n" \
    "v_smfmac_f32_16x16x64_bf16 v[80:83], v[24:27], v[224:231], v10\n" \
    "v_smfmac_f32_16x16x64_bf16 v[84:87], v[24:27], v[240:247], v10\n" \
    "v_max3_u32 v19, v19, v24, v25\n" \
    "v_max3_u32 v19, v19, v26, v27\n" \
    "s_waitcnt vmcnt(5)\n" \
    "v_smfmac_f32_16x16x64_bf16 v[80:83], v[28:31], v[232:239], v10\n" \
    "v_smfmac_f32_16x16x64_bf16 v[84:87], v[28:31], v[248:255], v10\n" \
    "v_max3_u32 v20, v20, v28, v29\n" \
    "v_max3_u32 v20, v20, v30, v31\n" \
    "v_max_u32 v19, v19, v20\n" \
    "v_cmp_eq_u32 vcc, -1, v19\n" \
    "s_nop 0\n" \
    "s_cbranch_vccnz .Lr_cpB1_%=\n" \
    ".Lr_cprB1_%=:\n" \
    "s_nop 0\n" \
    "v_pk_add_f32 v[80:81], v[80:81], v[82:83]\n" \
    "v_pk_add_f32 v[84:85], v[84:85], v[86:87]\n" \
    "ds_write2_b32 v5, v80, v81 offset0:0 offset1:4\n" \
    "ds_write2_b32 v5, v84, v85 offset0:64 offset1:68\n" \
    "s_waitcnt lgkmcnt(0)\n" \
    "s_barrier\n" \
    "s_barrier\n" \
    "s_memrealtime s[62:63]\n" \
    "buffer_load_dwordx4 v[60:63], v0, s[8:11], s42 offen offset:0 sc1\n" \
    "buffer_load_dwordx4 v[64:67], v0, s[8:11], s42 offen offset:1024 sc1\n" \
    "buffer_load_dwordx4 v[68:71], v0, s[8:11], s42 offen offset:2048 sc1\n" \
    "buffer_load_dwordx4 v[72:75], v0, s[8:11], s42 offen offset:3072 sc1\n" \
    "s_waitcnt vmcnt(8)\n" \
    "v_cmp_ge_u32 vcc, s49, v18\n" \
    "s_nop 0\n" \
    "s_and_b64 vcc, vcc, s[56:57]\n" \
    "s_cselect_b32 s54, 0, 1\n" \
    "s_cmp_eq_u32 s32, 63\n" \
    "s_cselect_b32 s54, 2, s54\n" \
    "s_cmp_eq_u32 s54, 1\n" \
    "s_cselect_b32 s39, s39, 0x80000000\n" \
    "s_add_u32 s40, s39, 65536\n" \
    "s_barrier\n" \
    "s_add_u32 s33, s43, %[x0]\n" \
    "buffer_load_dwordx4 v[76:79], v6, s[8:11], s33 offen sc1\n" \
    "buffer_load_dwordx4 v[192:195], v0, s[8:11], s39 offen offset:0 sc1\n" \
    "buffer_load_dwordx4 v[196:199], v0, s[8:11], s39 offen offset:1024 sc1\n" \
    "buffer_load_dwordx4 v[200:203], v0, s[8:11], s39 offen offset:2048 sc1\n" \
    "buffer_load_dwordx4 v[204:207], v0, s[8:11], s39 offen offset:3072 sc1\n" \
    "buffer_load_dwordx4 v[208:211], v0, s[8:11], s40 offen offset:0 sc1\n" \
    "buffer_load_dwordx4 v[212:215], v0, s[8:11], s40 offen offset:1024 sc1\n" \
    "buffer_load_dwordx4 v[216:219], v0, s[8:11], s40 offen offset:2048 sc1\n" \
    "buffer_load_dwordx4 v[220:223], v0, s[8:11], s40 offen offset:3072 sc1\n" \
    "s_and_b32 s33, s32, s23\n" \
    "s_mul_i32 s45, s33, s24\n" \
    "s_cmp_lt_u32 s33, 64-%[pdist]\n" \
    "s_cselect_b32 s45, s45, 0x80000000\n" \
    "s_add_u32 s44, s43, %[x1]\n" \
    "s_xor_b32 s51, s51, 16384\n" \
    "s_waitcnt vmcnt(15)\n" \
    "v_max3_u32 v19, v44, v45, v46\n" \
    "s_waitcnt vmcnt(14)\n" \
    "v_max3_u32 v19, v19, v47, v48\n" \
    "v_max3_u32 v19, v19, v49, v50\n" \
    "v_max3_u32 v19, v19, v51, v52\n" \
    "v_max3_u32 v19, v19, v53, v54\n" \
    "s_waitcnt vmcnt(13)\n" \
    "v_max3_u32 v19, v19, v55, v56\n" \
    "v_max3_u32 v19, v19, v57, v58\n" \
    "s_waitcnt vmcnt(12)\n" \
    "v_max3_u32 v19, v19, v59, v60\n" \
    "v_max3_u32 v19, v19, v61, v62\n" \
    "s_waitcnt vmcnt(11)\n" \
    "v_max3_u32 v19, v19, v63, v64\n" \
    "v_max3_u32 v19, v19, v65, v66\n" \
    "s_waitcnt vmcnt(10)\n" \
    "v_max3_u32 v19, v19, v67, v68\n" \
    "v_max3_u32 v19, v19, v69, v70\n" \
    "s_waitcnt vmcnt(9)\n" \
    "v_max3_u32 v19, v19, v71, v72\n" \
    "v_max3_u32 v19, v19, v73, v74\n" \
    "v_max_u32 v19, v19, v75\n" \
    "v_cmp_eq_u32 vcc, -1, v19\n" \
    "s_nop 0\n" \
    "s_cbranch_vccnz .Lr_ctB1_%=\n" \
    ".Lr_ctrB1_%=:\n" \
    "v_mfma_f32_16x16x32_bf16 v[88:91], v[36:39], v[72:75], 0\n" \
    "v_mfma_f32_16x16x32_bf16 v[88:91], v[224:227], v[56:59], v[88:91]\n" \
    "v_mfma_f32_16x16x32_bf16 v[88:91], v[228:231], v[60:63], v[88:91]\n" \
    "v_mfma_f32_16x16x32_bf16 v[88:91], v[232:235], v[64:67], v[88:91]\n" \
    "v_mfma_f32_16x16x32_bf16 v[88:91], v[236:239], v[68:71], v[88:91]\n" \
    "v_mfma_f32_16x16x32_bf16 v[92:95], v[40:43], v[72:75], 0\n" \
    "v_mfma_f32_16x16x32_bf16 v[92:95], v[240:243], v[56:59], v[92:95]\n" \
    "v_mfma_f32_16x16x32_bf16 v[92:95], v[244:247], v[60:63], v[92:95]\n" \
    "v_mfma_f32_16x16x32_bf16 v[92:95], v[248:251], v[64:67], v[92:95]\n" \
    "v_mfma_f32_16x16x32_bf16 v[92:95], v[252:255], v[68:71], v[92:95]\n" \
    "v_mfma_f32_16x16x32_bf16 v[80:83], v[36:39], v[52:55], 0\n" \
    "v_mfma_f32_16x16x32_bf16 v[84:87], v[40:43], v[52:55], 0\n" \
    "v_mov_b32 v225, v226\n" \
    "v_mov_b32 v226, v228\n" \
    "v_mov_b32 v227, v230\n" \
    "v_mov_b32 v233, v234\n" \
    "v_mov_b32 v234, v236\n" \
    "v_mov_b32 v235, v238\n" \
    "v_mov_b32 v241, v242\n" \
    "v_mov_b32 v242, v244\n" \
    "v_mov_b32 v243, v246\n" \
    "v_mov_b32 v249, v250\n" \
    "v_mov_b32 v250, v252\n" \
    "v_mov_b32 v251, v254\n" \
    "ds_write_b128 v8, v[88:91] offset:2048\n" \
    "ds_write_b128 v8, v[92:95] offset:3072\n" \
    "v_permlane16_swap_b32 v224, v226\n" \
    "v_permlane16_swap_b32 v225, v227\n" \
    "v_permlane16_swap_b32 v232, v234\n" \
    "v_permlane16_swap_b32 v233, v235\n" \
    "v_permlane16_swap_b32 v240, v242\n" \
    "v_permlane16_swap_b32 v241, v243\n" \
    "v_permlane16_swap_b32 v248, v250\n" \
    "v_permlane16_swap_b32 v249, v251\n" \
    "v_mfma_f32_16x16x32_bf16 v[80:83], v[224:227], v[44:47], v[80:83]\n" \
    "v_mfma_f32_16x16x32_bf16 v[84:87], v[240:243], v[44:47], v[84:87]\n" \
    "v_mfma_f32_16x16x32_bf16 v[80:83], v[232:235], v[48:51], v[80:83]\n" \
    "v_mfma_f32_16x16x32_bf16 v[84:87], v[248:251], v[48:51], v[84:87]\n" \
    "s_nop 6\n" \
    "ds_write_b128 v8, v[80:83] offset:0\n" \
    "ds_write_b128 v8, v[84:87] offset:1024\n" \
    "s_waitcnt lgkmcnt(0)\n" \
    "s_barrier\n" \
    "s_mov_b32 s55, s62\n" \
    "buffer_load_dwordx2 v[16:17], v3, s[8:11], s51 offen sc1\n" \
    "ds_read_b128 v[96:99], v7 offset:0\n" \
    "ds_read_b128 v[100:103], v7 offset:256\n" \
    "ds_read_b128 v[104:107], v7 offset:4096\n" \
    "ds_read_b128 v[108:111], v7 offset:4352\n" \
    "ds_read_b128 v[112:115], v7 offset:8192\n" \
    "ds_read_b128 v[116:119], v7 offset:8448\n" \
    "ds_read_b128 v[120:123], v7 offset:12288\n" \
    "ds_read_b128 v[124:127], v7 offset:12544\n" \
    "ds_read_b128 v[128:131], v7 offset:16384\n" \
    "ds_read_b128 v[132:135], v7 offset:16640\n" \
    "ds_read_b128 v[136:139], v7 offset:20480\n" \
    "ds_read_b128 v[140:143], v7 offset:20736\n" \
    "ds_read_b128 v[144:147], v7 offset:24576\n" \
    "ds_read_b128 v[148:151], v7 offset:24832\n" \
    "ds_read_b128 v[152:155], v7 offset:28672\n" \
    "ds_read_b128 v[156:159], v7 offset:28928\n" \
    "s_waitcnt lgkmcnt(13)\n" \
    "v_pk_add_f32 v[96:97], v[96:97], v[104:105]\n" \
    "v_pk_add_f32 v[98:99], v[98:99], v[106:107]\n" \
    "s_waitcnt lgkmcnt(12)\n" \
    "v_pk_add_f32 v[100:101], v[100:101], v[108:109]\n" \
    "v_pk_add_f32 v[102:103], v[102:103], v[110:111]\n" \
    "s_waitcnt lgkmcnt(9)\n" \
    "v_pk_add_f32 v[112:113], v[112:113], v[120:121]\n" \
    "v_pk_add_f32 v[114:115], v[114:115], v[122:123]\n" \
    "s_waitcnt lgkmcnt(8)\n" \
    "v_pk_add_f32 v[116:117], v[116:117], v[124:125]\n" \
    "v_pk_add_f32 v[118:119], v[118:119], v[126:127]\n" \
    "v_pk_add_f32 v[96:97], v[96:97], v[112:113]\n" \
    "v_pk_add_f32 v[98:99], v[98:99], v[114:115]\n" \
    "v_pk_add_f32 v[100:101], v[100:101], v[116:117]\n" \
    "v_pk_add_f32 v[102:103], v[102:103], v[118:119]\n" \
    "s_waitcnt lgkmcnt(5)\n" \
    "v_pk_add_f32 v[128:129], v[128:129], v[136:137]\n" \
    "v_pk_add_f32 v[130:131], v[130:131], v[138:139]\n" \
    "s_waitcnt lgkmcnt(4)\n" \
    "v_pk_add_f32 v[132:133], v[132:133], v[140:141]\n" \
    "v_pk_add_f32 v[134:135], v[134:135], v[142:143]\n" \
    "s_waitcnt lgkmcnt(1)\n" \
    "v_pk_add_f32 v[144:145], v[144:145], v[152:153]\n" \
    "v_pk_add_f32 v[146:147], v[146:147], v[154:155]\n" \
    "s_waitcnt lgkmcnt(0)\n" \
    "v_pk_add_f32 v[148:149], v[148:149], v[156:157]\n" \
    "v_pk_add_f32 v[150:151], v[150:151], v[158:159]\n" \
    "v_pk_add_f32 v[128:129], v[128:129], v[144:145]\n" \
    "v_pk_add_f32 v[130:131], v[130:131], v[146:147]\n" \
    "v_pk_add_f32 v[132:133], v[132:133], v[148:149]\n" \
    "v_pk_add_f32 v[134:135], v[134:135], v[150:151]\n" \
    "v_pk_add_f32 v[96:97], v[96:97], v[128:129]\n" \
    "v_pk_add_f32 v[98:99], v[98:99], v[130:131]\n" \
    "v_pk_add_f32 v[100:101], v[100:101], v[132:133]\n" \
    "v_pk_add_f32 v[102:103], v[102:103], v[134:135]\n" \
    "v_cvt_pk_bf16_f32 v104, v96, v97\n" \
    "v_cvt_pk_bf16_f32 v105, v98, v99\n" \
    "v_cvt_pk_bf16_f32 v106, v100, v101\n" \
    "v_cvt_pk_bf16_f32 v107, v102, v103\n" \
    "v_lshlrev_b32 v96, 16, v104\n" \
    "v_and_b32 v97, 0xffff0000, v104\n" \
    "s_waitcnt vmcnt(9)\n" \
    "v_lshlrev_b32 v112, 16, v76\n" \
    "v_and_b32 v113, 0xffff0000, v76\n" \
    "v_lshlrev_b32 v98, 16, v105\n" \
    "v_and_b32 v99, 0xffff0000, v105\n" \
    "v_lshlrev_b32 v114, 16, v77\n" \
    "v_and_b32 v115, 0xffff0000, v77\n" \
    "v_lshlrev_b32 v100, 16, v106\n" \
    "v_and_b32 v101, 0xffff0000, v106\n" \
    "v_lshlrev_b32 v116, 16, v78\n" \
    "v_and_b32 v117, 0xffff0000, v78\n" \
    "v_lshlrev_b32 v102, 16, v107\n" \
    "v_and_b32 v103, 0xffff0000, v107\n" \
    "v_lshlrev_b32 v118, 16, v79\n" \
    "v_and_b32 v119, 0xffff0000, v79\n" \
    "v_pk_add_f32 v[96:97], v[96:97], v[112:113]\n" \
    "v_pk_add_f32 v[98:99], v[98:99], v[114:115]\n" \
    "v_pk_add_f32 v[100:101], v[100:101], v[116:117]\n" \
    "v_pk_add_f32 v[102:103], v[102:103], v[118:119]\n" \
    "v_cvt_pk_bf16_f32 v76, v96, v97\n" \
    "v_cvt_pk_bf16_f32 v77, v98, v99\n" \
    "v_cvt_pk_bf16_f32 v78, v100, v101\n" \
    "v_cvt_pk_bf16_f32 v79, v102, v103\n" \
    "v_min_u32 v76, -2, v76\n" \
    "v_min_u32 v77, -2, v77\n" \
    "v_min_u32 v78, -2, v78\n" \
    "v_min_u32 v79, -2, v79\n" \
    "buffer_store_dwordx4 v[76:79], v6, s[8:11], s44 offen sc1\n" \
    "s_cmp_eq_u32 s54, 0\n" \
    "s_cbranch_scc1 .Lr_cbB1_%=\n" \
    ".Lr_cbrB1_%=:\n" \
    "s_add_u32 s32, s32, 1\n" \
    "s_cmp_lt_u32 s32, 64\n" \
    "s_cbranch_scc1 .Lr_lB_%=\n" \
    "s_waitcnt vmcnt(0)\n" \
    "s_endpgm\n" \
    ".Lr_lC_%=:\n" \
    "s_add_u32 s49, s32, 1\n" \
    "s_add_u32 s36, s18, s46\n" \
    "s_add_u32 s37, s36, 16384\n" \
    "s_and_b32 s33, s32, 3\n" \
    "s_lshl_b32 s35, s33, 16\n" \
    "s_add_u32 s35, s35, s17\n" \
    ".Lr_tvC0_%=:\n" \
    "s_waitcnt vmcnt(0)\n" \
    "v_cmp_ne_u32 vcc, s49, v17\n" \
    "v_subrev_u32 v19, s55, v16\n" \
    "s_and_b64 vcc, vcc, s[56:57]\n" \
    "s_cbranch_scc1 .Lr_ctvC0_%=\n" \
    "v_max_i32_dpp v19, v19, v19 quad_perm:[1,0,3,2] row_mask:0xf bank_mask:0xf bound_ctrl:1\n" \
    "s_nop 1\n" \
    "v_max_i32_dpp v19, v19, v19 quad_perm:[2,3,0,1] row_mask:0xf bank_mask:0xf bound_ctrl:1\n" \
    "s_nop 0\n" \
    "v_readlane_b32 s48, v19, 4\n" \
    "v_readlane_b32 s47, v19, 0\n" \
    "s_add_u32 s48, s48, s55\n" \
    "s_add_u32 s48, s48, s22\n" \
    ".Lr_goC0_%=:\n" \
    "s_branch .Lr_uC0b_%=\n" \
    ".Lr_uC0a_%=:\n" \
    "s_sleep 1\n" \
    ".Lr_uC0b_%=:\n" \
    "s_memrealtime vcc\n" \
    "s_waitcnt lgkmcnt(0)\n" \
    "s_sub_i32 vcc_lo, s48, vcc_lo\n" \
    "s_cmp_gt_i32 vcc_lo, 8\n" \
    "s_cbranch_scc1 .Lr_uC0a_%=\n" \
    "s_cmp_gt_i32 vcc_lo, 0\n" \
    "s_cbranch_scc1 .Lr_uC0b_%=\n" \
    "buffer_load_dwordx4 v[36:39], v0, s[8:11], s36 offen sc1\n" \
    "buffer_load_dwordx4 v[40:43], v0, s[8:11], s37 offen sc1\n" \
    "s_add_u32 s47, s47, s55\n" \
    "s_add_u32 s47, s47, %[dr]\n" \
    "s_branch .Lr_pC0b_%=\n" \
    ".Lr_pC0a_%=:\n" \
    "s_sleep 1\n" \
    ".Lr_pC0b_%=:\n" \
    "s_memrealtime vcc\n" \
    "s_waitcnt lgkmcnt(0)\n" \
    "s_sub_i32 vcc_lo, s47, vcc_lo\n" \
    "s_cmp_gt_i32 vcc_lo, 8\n" \
    "s_cbranch_scc1 .Lr_pC0a_%=\n" \
    "s_cmp_gt_i32 vcc_lo, 0\n" \
    "s_cbranch_scc1 .Lr_pC0b_%=\n" \
    "buffer_load_dwordx4 v[32:35], v4, s[8:11], s35 offen sc1\n" \
    "buffer_load_dwordx4 v[24:27], v0, s[8:11], s35 offen sc1\n" \
    "buffer_load_dwordx4 v[28:31], v0, s[8:11], s35 offen offset:1024 sc1\n" \
    "s_and_b32 s33, s32, 3\n" \
    "s_lshl_b32 s61, s33, 17\n" \
    "s_add_u32 s41, s61, s20\n" \
    "s_add_u32 s42, s41, 4096\n" \
    "s_barrier\n" \
    "buffer_load_dword v18, v2, s[8:11], 0 offen sc1\n" \
    "buffer_load_dwordx4 v[52:55], v0, s[8:11], s41 offen offset:2048 sc1\n" \
    "buffer_load_dwordx4 v[44:47], v0, s[8:11], s41 offen offset:0 sc1\n" \
    "buffer_load_dwordx4 v[48:51], v0, s[8:11], s41 offen offset:1024 sc1\n" \
    "buffer_load_dwordx4 v[56:59], v0, s[8:11], s41 offen offset:3072 sc1\n" \
    "s_add_u32 s46, s46, 655360\n" \
    "s_cmp_eq_u32 s46, 1966080\n" \
    "s_cselect_b32 s46, 0, s46\n" \
    "s_and_b32 s33, s49, 3\n" \
    "s_lshl_b32 s38, s33, 15\n" \
    "s_and_b32 s33, s49, %[brn]-1\n" \
    "s_mul_i32 s39, s33, 2621440\n" \
    "s_add_u32 s39, s39, s19\n" \
    "s_lshr_b32 s33, s39, 8\n" \
    "s_xor_b32 s33, s33, s27\n" \
    "s_and_b32 s33, s33, 0x2000\n" \
    "s_or_b32 s39, s39, s33\n" \
    "s_mul_i32 s43, s32, 40960\n" \
    "s_add_u32 s43, s43, s21\n" \
    "s_cmp_eq_u32 s32, 63\n" \
    "s_cselect_b32 s38, 0x80000000, s38\n" \
    "s_waitcnt vmcnt(9)\n" \
    "v_max3_u32 v19, v36, v37, v38\n" \
    "s_waitcnt vmcnt(8)\n" \
    "v_max3_u32 v19, v19, v39, v40\n" \
    "v_max3_u32 v19, v19, v41, v42\n" \
    "v_max_u32 v19, v19, v43\n" \
    "s_waitcnt vmcnt(7)\n" \
    "v_mfma_f32_16x16x32_bf16 v[80:83], v[32:35], v[36:39], 0\n" \
    "v_max3_u32 v20, v32, v33, v34\n" \
    "v_mfma_f32_16x16x32_bf16 v[84:87], v[32:35], v[40:43], 0\n" \
    "v_max_u32 v20, v20, v35\n" \
    "s_waitcnt vmcnt(6)\n" \
    "v_smfmac_f32_16x16x64_bf16 v[80:83], v[24:27], v[192:199], v10\n" \
    "v_smfmac_f32_16x16x64_bf16 v[84:87], v[24:27], v[208:215], v10\n" \
    "v_max3_u32 v19, v19, v24, v25\n" \
    "v_max3_u32 v19, v19, v26, v27\n" \
    "s_waitcnt vmcnt(5)\n" \
    "v_smfmac_f32_16x16x64_bf16 v[80:83], v[28:31], v[200:207], v10\n" \
    "v_smfmac_f32_16x16x64_bf16 v[84:87], v[28:31], v[216:223], v10\n" \
    "v_max3_u32 v20, v20, v28, v29\n" \
    "v_max3_u32 v20, v20, v30, v31\n" \
    "v_max_u32 v19, v19, v20\n" \
    "v_cmp_eq_u32 vcc, -1, v19\n" \
    "s_nop 0\n" \
    "s_cbranch_vccnz .Lr_cpC0_%=\n" \
    ".Lr_cprC0_%=:\n" \
    "s_nop 0\n" \
    "v_pk_add_f32 v[80:81], v[80:81], v[82:83]\n" \
    "v_pk_add_f32 v[84:85], v[84:85], v[86:87]\n" \
    "ds_write2_b32 v5, v80, v81 offset0:0 offset1:4\n" \
    "ds_write2_b32 v5, v84, v85 offset0:64 offset1:68\n" \
    "s_waitcnt lgkmcnt(0)\n" \
    "s_barrier\n" \
    "s_barrier\n" \
    "s_memrealtime s[62:63]\n" \
    "buffer_load_dwordx4 v[60:63], v0, s[8:11], s42 offen offset:0 sc1\n" \
    "buffer_load_dwordx4 v[64:67], v0, s[8:11], s42 offen offset:1024 sc1\n" \
    "buffer_load_dwordx4 v[68:71], v0, s[8:11], s42 offen offset:2048 sc1\n" \
    "buffer_load_dwordx4 v[72:75], v0, s[8:11], s42 offen offset:3072 sc1\n" \
    "s_waitcnt vmcnt(8)\n" \
    "v_cmp_ge_u32 vcc, s49, v18\n" \
    "s_nop 0\n" \
    "s_and_b64 vcc, vcc, s[56:57]\n" \
    "s_cselect_b32 s54, 0, 1\n" \
    "s_cmp_eq_u32 s32, 63\n" \
    "s_cselect_b32 s54, 2, s54\n" \
    "s_cmp_eq_u32 s54, 1\n" \
    "s_cselect_b32 s39, s39, 0x80000000\n" \
    "s_add_u32 s40, s39, 65536\n" \
    "s_barrier\n" \
    "buffer_store_dwordx4 v[160:163], v9, s[8:11], s45 offen sc1\n" \
    "buffer_load_dwordx4 v[224:227], v0, s[8:11], s39 offen offset:0 sc1\n" \
    "buffer_load_dwordx4 v[228:231], v0, s[8:11], s39 offen offset:1024 sc1\n" \
    "buffer_load_dwordx4 v[232:235], v0, s[8:11], s39 offen offset:2048 sc1\n" \
    "buffer_load_dwordx4 v[236:239], v0, s[8:11], s39 offen offset:3072 sc1\n" \
    "buffer_load_dwordx4 v[240:243], v0, s[8:11], s40 offen offset:0 sc1\n" \
    "buffer_load_dwordx4 v[244:247], v0, s[8:11], s40 offen offset:1024 sc1\n" \
    "buffer_load_dwordx4 v[248:251], v0, s[8:11], s40 offen offset:2048 sc1\n" \
    "buffer_load_dwordx4 v[252:255], v0, s[8:11], s40 offen offset:3072 sc1\n" \
    "s_and_b32 s33, s32, s23\n" \
    "s_mul_i32 s45, s33, s24\n" \
    "s_cmp_lt_u32 s33, 64-%[pdist]\n" \
    "s_cselect_b32 s45, s45, 0x80000000\n" \
    "s_add_u32 s44, s43, %[x1]\n" \
    "s_xor_b32 s51, s51, 16384\n" \
    "s_waitcnt vmcnt(15)\n" \
    "v_max3_u32 v19, v44, v45, v46\n" \
    "s_waitcnt vmcnt(14)\n" \
    "v_max3_u32 v19, v19, v47, v48\n" \
    "v_max3_u32 v19, v19, v49, v50\n" \
    "v_max3_u32 v19, v19, v51, v52\n" \
    "v_max3_u32 v19, v19, v53, v54\n" \
    "s_waitcnt vmcnt(13)\n" \
    "v_max3_u32 v19, v19, v55, v56\n" \
    "v_max3_u32 v19, v19, v57, v58\n" \
    "s_waitcnt vmcnt(12)\n" \
    "v_max3_u32 v19, v19, v59, v60\n" \
    "v_max3_u32 v19, v19, v61, v62\n" \
    "s_waitcnt vmcnt(11)\n" \
    "v_max3_u32 v19, v19, v63, v64\n" \
    "v_max3_u32 v19, v19, v65, v66\n" \
    "s_waitcnt vmcnt(10)\n" \
    "v_max3_u32 v19, v19, v67, v68\n" \
    "v_max3_u32 v19, v19, v69, v70\n" \
    "s_waitcnt vmcnt(9)\n" \
    "v_max3_u32 v19, v19, v71, v72\n" \
    "v_max3_u32 v19, v19, v73, v74\n" \
    "v_max_u32 v19, v19, v75\n" \
    "v_cmp_eq_u32 vcc, -1, v19\n" \
    "s_nop 0\n" \
    "s_cbranch_vccnz .Lr_ctC0_%=\n" \
    ".Lr_ctrC0_%=:\n" \
    "v_mfma_f32_16x16x32_bf16 v[88:91], v[36:39], v[72:75], 0\n" \
    "v_mfma_f32_16x16x32_bf16 v[88:91], v[192:195], v[56:59], v[88:91]\n" \
    "v_mfma_f32_16x16x32_bf16 v[88:91], v[196:199], v[60:63], v[88:91]\n" \
    "v_mfma_f32_16x16x32_bf16 v[88:91], v[200:203], v[64:67], v[88:91]\n" \
    "v_mfma_f32_16x16x32_bf16 v[88:91], v[204:207], v[68:71], v[88:91]\n" \
    "v_mfma_f32_16x16x32_bf16 v[92:95], v[40:43], v[72:75], 0\n" \
    "v_mfma_f32_16x16x32_bf16 v[92:95], v[208:211], v[56:59], v[92:95]\n" \
    "v_mfma_f32_16x16x32_bf16 v[92:95], v[212:215], v[60:63], v[92:95]\n" \
    "v_mfma_f32_16x16x32_bf16 v[92:95], v[216:219], v[64:67], v[92:95]\n" \
    "v_mfma_f32_16x16x32_bf16 v[92:95], v[220:223], v[68:71], v[92:95]\n" \
    "v_mfma_f32_16x16x32_bf16 v[80:83], v[36:39], v[52:55], 0\n" \
    "v_mfma_f32_16x16x32_bf16 v[84:87], v[40:43], v[52:55], 0\n" \
    "v_mov_b32 v193, v194\n" \
    "v_mov_b32 v194, v196\n" \
    "v_mov_b32 v195, v198\n" \
    "v_mov_b32 v201, v202\n" \
    "v_mov_b32 v202, v204\n" \
    "v_mov_b32 v203, v206\n" \
    "v_mov_b32 v209, v210\n" \
    "v_mov_b32 v210, v212\n" \
    "v_mov_b32 v211, v214\n" \
    "v_mov_b32 v217, v218\n" \
    "v_mov_b32 v218, v220\n" \
    "v_mov_b32 v219, v222\n" \
    "ds_write_b128 v8, v[88:91] offset:2048\n" \
    "ds_write_b128 v8, v[92:95] offset:3072\n" \
    "v_permlane16_swap_b32 v192, v194\n" \
    "v_permlane16_swap_b32 v193, v195\n" \
    "v_permlane16_swap_b32 v200, v202\n" \
    "v_permlane16_swap_b32 v201, v203\n" \
    "v_permlane16_swap_b32 v208, v210\n" \
    "v_permlane16_swap_b32 v209, v211\n" \
    "v_permlane16_swap_b32 v216, v218\n" \
    "v_permlane16_swap_b32 v217, v219\n" \
    "v_mfma_f32_16x16x32_bf16 v[80:83], v[192:195], v[44:47], v[80:83]\n" \
    "v_mfma_f32_16x16x32_bf16 v[84:87], v[208:211], v[44:47], v[84:87]\n" \
    "v_mfma_f32_16x16x32_bf16 v[80:83], v[200:203], v[48:51], v[80:83]\n" \
    "v_mfma_f32_16x16x32_bf16 v[84:87], v[216:219], v[48:51], v[84:87]\n" \
    "s_nop 6\n" \
    "ds_write_b128 v8, v[80:83] offset:0\n" \
    "ds_write_b128 v8, v[84:87] offset:1024\n" \
    "s_waitcnt lgkmcnt(0)\n" \
    "s_barrier\n" \
    "s_mov_b32 s55, s62\n" \
    "buffer_load_dwordx2 v[16:17], v3, s[8:11], s51 offen sc1\n" \
    "s_cmp_eq_u32 s54, 0\n" \
    "s_cbranch_scc1 .Lr_cbC0_%=\n" \
    ".Lr_cbrC0_%=:\n" \
    "s_add_u32 s32, s32, 1\n" \
    "s_add_u32 s49, s32, 1\n" \
    "s_add_u32 s36, s18, s46\n" \
    "s_add_u32 s37, s36, 16384\n" \
    "s_and_b32 s33, s32, 3\n" \
    "s_lshl_b32 s35, s33, 16\n" \
    "s_add_u32 s35, s35, s17\n" \
    ".Lr_tvC1_%=:\n" \
    "s_waitcnt vmcnt(0)\n" \
    "v_cmp_ne_u32 vcc, s49, v17\n" \
    "v_subrev_u32 v19, s55, v16\n" \
    "s_and_b64 vcc, vcc, s[56:57]\n" \
    "s_cbranch_scc1 .Lr_ctvC1_%=\n" \
    "v_max_i32_dpp v19, v19, v19 quad_perm:[1,0,3,2] row_mask:0xf bank_mask:0xf bound_ctrl:1\n" \
    "s_nop 1\n" \
    "v_max_i32_dpp v19, v19, v19 quad_perm:[2,3,0,1] row_mask:0xf bank_mask:0xf bound_ctrl:1\n" \
    "s_nop 0\n" \
    "v_readlane_b32 s48, v19, 4\n" \
    "v_readlane_b32 s47, v19, 0\n" \
    "s_add_u32 s48, s48, s55\n" \
    "s_add_u32 s48, s48, s22\n" \
    ".Lr_goC1_%=:\n" \
    "s_branch .Lr_uC1b_%=\n" \
    ".Lr_uC1a_%=:\n" \
    "s_sleep 1\n" \
    ".Lr_uC1b_%=:\n" \
    "s_memrealtime vcc\n" \
    "s_waitcnt lgkmcnt(0)\n" \
    "s_sub_i32 vcc_lo, s48, vcc_lo\n" \
    "s_cmp_gt_i32 vcc_lo, 8\n" \
    "s_cbranch_scc1 .Lr_uC1a_%=\n" \
    "s_cmp_gt_i32 vcc_lo, 0\n" \
    "s_cbranch_scc1 .Lr_uC1b_%=\n" \
    "buffer_load_dwordx4 v[36:39], v0, s[8:11], s36 offen sc1\n" \
    "buffer_load_dwordx4 v[40:43], v0, s[8:11], s37 offen sc1\n" \
    "s_add_u32 s47, s47, s55\n" \
    "s_add_u32 s47, s47, %[dr]\n" \
    "s_branch .Lr_pC1b_%=\n" \
    ".Lr_pC1a_%=:\n" \
    "s_sleep 1\n" \
    ".Lr_pC1b_%=:\n" \
    "s_memrealtime vcc\n" \
    "s_waitcnt lgkmcnt(0)\n" \
    "s_sub_i32 vcc_lo, s47, vcc_lo\n" \
    "s_cmp_gt_i32 vcc_lo, 8\n" \
    "s_cbranch_scc1 .Lr_pC1a_%=\n" \
    "s_cmp_gt_i32 vcc_lo, 0\n" \
    "s_cbranch_scc1 .Lr_pC1b_%=\n" \
    "buffer_load_dwordx4 v[32:35], v4, s[8:11], s35 offen sc1\n" \
    "buffer_load_dwordx4 v[24:27], v0, s[8:11], s35 offen sc1\n" \
    "buffer_load_dwordx4 v[28:31], v0, s[8:11], s35 offen offset:1024 sc1\n" \
    "s_and_b32 s33, s32, 3\n" \
    "s_lshl_b32 s61, s33, 17\n" \
    "s_add_u32 s41, s61, s20\n" \
    "s_add_u32 s42, s41, 4096\n" \
    "s_barrier\n" \
    "buffer_load_dword v18, v2, s[8:11], 0 offen sc1\n" \
    "buffer_load_dwordx4 v[52:55], v0, s[8:11], s41 offen offset:2048 sc1\n" \
    "buffer_load_dwordx4 v[44:47], v0, s[8:11], s41 offen offset:0 sc1\n" \
    "buffer_load_dwordx4 v[48:51], v0, s[8:11], s41 offen offset:1024 sc1\n" \
    "buffer_load_dwordx4 v[56:59], v0, s[8:11], s41 offen offset:3072 sc1\n" \
    "s_add_u32 s46, s46, 655360\n" \
    "s_cmp_eq_u32 s46, 1966080\n" \
    "s_cselect_b32 s46, 0, s46\n" \
    "s_and_b32 s33, s49, 3\n" \
    "s_lshl_b32 s38, s33, 15\n" \
    "s_and_b32 s33, s49, %[brn]-1\n" \
    "s_mul_i32 s39, s33, 2621440\n" \
    "s_add_u32 s39, s39, s19\n" \
    "s_lshr_b32 s33, s39, 8\n" \
    "s_xor_b32 s33, s33, s27\n" \
    "s_and_b32 s33, s33, 0x2000\n" \
    "s_or_b32 s39, s39, s33\n" \
    "s_mul_i32 s43, s32, 40960\n" \
    "s_add_u32 s43, s43, s21\n" \
    "s_cmp_eq_u32 s32, 63\n" \
    "s_cselect_b32 s38, 0x80000000, s38\n" \
    "s_waitcnt vmcnt(9)\n" \
    "v_max3_u32 v19, v36, v37, v38\n" \
    "s_waitcnt vmcnt(8)\n" \
    "v_max3_u32 v19, v19, v39, v40\n" \
    "v_max3_u32 v19, v19, v41, v42\n" \
    "v_max_u32 v19, v19, v43\n" \
    "s_waitcnt vmcnt(7)\n" \
    "v_mfma_f32_16x16x32_bf16 v[80:83], v[32:35], v[36:39], 0\n" \
    "v_max3_u32 v20, v32, v33, v34\n" \
    "v_mfma_f32_16x16x32_bf16 v[84:87], v[32:35], v[40:43], 0\n" \
    "v_max_u32 v20, v20, v35\n" \
    "s_waitcnt vmcnt(6)\n" \
    "v_smfmac_f32_16x16x64_bf16 v[80:83], v[24:27], v[224:231], v10\n" \
    "v_smfmac_f32_16x16x64_bf16 v[84:87], v[24:27], v[240:247], v10\n" \
    "v_max3_u32 v19, v19, v24, v25\n" \
    "v_max3_u32 v19, v19, v26, v27\n" \
    "s_waitcnt vmcnt(5)\n" \
    "v_smfmac_f32_16x16x64_bf16 v[80:83], v[28:31], v[232:239], v10\n" \
    "v_smfmac_f32_16x16x64_bf16 v[84:87], v[28:31], v[248:255], v10\n" \
    "v_max3_u32 v20, v20, v28, v29\n" \
    "v_max3_u32 v20, v20, v30, v31\n" \
    "v_max_u32 v19, v19, v20\n" \
    "v_cmp_eq_u32 vcc, -1, v19\n" \
    "s_nop 0\n" \
    "s_cbranch_vccnz .Lr_cpC1_%=\n" \
    ".Lr_cprC1_%=:\n" \
    "s_nop 0\n" \
    "v_pk_add_f32 v[80:81], v[80:81], v[82:83]\n" \
    "v_pk_add_f32 v[84:85], v[84:85], v[86:87]\n" \
    "ds_write2_b32 v5, v80, v81 offset0:0 offset1:4\n" \
    "ds_write2_b32 v5, v84, v85 offset0:64 offset1:68\n" \
    "s_waitcnt lgkmcnt(0)\n" \
    "s_barrier\n" \
    "s_barrier\n" \
    "s_memrealtime s[62:63]\n" \
    "buffer_load_dwordx4 v[60:63], v0, s[8:11], s42 offen offset:0 sc1\n" \
    "buffer_load_dwordx4 v[64:67], v0, s[8:11], s42 offen offset:1024 sc1\n" \
    "buffer_load_dwordx4 v[68:71], v0, s[8:11], s42 offen offset:2048 sc1\n" \
    "buffer_load_dwordx4 v[72:75], v0, s[8:11], s42 offen offset:3072 sc1\n" \
    "s_waitcnt vmcnt(8)\n" \
    "v_cmp_ge_u32 vcc, s49, v18\n" \
    "s_nop 0\n" \
    "s_and_b64 vcc, vcc, s[56:57]\n" \
    "s_cselect_b32 s54, 0, 1\n" \
    "s_cmp_eq_u32 s32, 63\n" \
    "s_cselect_b32 s54, 2, s54\n" \
    "s_cmp_eq_u32 s54, 1\n" \
    "s_cselect_b32 s39, s39, 0x80000000\n" \
    "s_add_u32 s40, s39, 65536\n" \
    "s_barrier\n" \
    "buffer_store_dwordx4 v[160:163], v9, s[8:11], s45 offen sc1\n" \
    "buffer_load_dwordx4 v[192:195], v0, s[8:11], s39 offen offset:0 sc1\n" \
    "buffer_load_dwordx4 v[196:199], v0, s[8:11], s39 offen offset:1024 sc1\n" \
    "buffer_load_dwordx4 v[200:203], v0, s[8:11], s39 offen offset:2048 sc1\n" \
    "buffer_load_dwordx4 v[204:207], v0, s[8:11], s39 offen offset:3072 sc1\n" \
    "buffer_load_dwordx4 v[208:211], v0, s[8:11], s40 offen offset:0 sc1\n" \
    "buffer_load_dwordx4 v[212:215], v0, s[8:11], s40 offen offset:1024 sc1\n" \
    "buffer_load_dwordx4 v[216:219], v0, s[8:11], s40 offen offset:2048 sc1\n" \
    "buffer_load_dwordx4 v[220:223], v0, s[8:11], s40 offen offset:3072 sc1\n" \
    "s_and_b32 s33, s32, s23\n" \
    "s_mul_i32 s45, s33, s24\n" \
    "s_cmp_lt_u32 s33, 64-%[pdist]\n" \
    "s_cselect_b32 s45, s45, 0x80000000\n" \
    "s_add_u32 s44, s43, %[x1]\n" \
    "s_xor_b32 s51, s51, 16384\n" \
    "s_waitcnt vmcnt(15)\n" \
    "v_max3_u32 v19, v44, v45, v46\n" \
    "s_waitcnt vmcnt(14)\n" \
    "v_max3_u32 v19, v19, v47, v48\n" \
    "v_max3_u32 v19, v19, v49, v50\n" \
    "v_max3_u32 v19, v19, v51, v52\n" \
    "v_max3_u32 v19, v19, v53, v54\n" \
    "s_waitcnt vmcnt(13)\n" \
    "v_max3_u32 v19, v19, v55, v56\n" \
    "v_max3_u32 v19, v19, v57, v58\n" \
    "s_waitcnt vmcnt(12)\n" \
    "v_max3_u32 v19, v19, v59, v60\n" \
    "v_max3_u32 v19, v19, v61, v62\n" \
    "s_waitcnt vmcnt(11)\n" \
    "v_max3_u32 v19, v19, v63, v64\n" \
    "v_max3_u32 v19, v19, v65, v66\n" \
    "s_waitcnt vmcnt(10)\n" \
    "v_max3_u32 v19, v19, v67, v68\n" \
    "v_max3_u32 v19, v19, v69, v70\n" \
    "s_waitcnt vmcnt(9)\n" \
    "v_max3_u32 v19, v19, v71, v72\n" \
    "v_max3_u32 v19, v19, v73, v74\n" \
    "v_max_u32 v19, v19, v75\n" \
    "v_cmp_eq_u32 vcc, -1, v19\n" \
    "s_nop 0\n" \
    "s_cbranch_vccnz .Lr_ctC1_%=\n" \
    ".Lr_ctrC1_%=:\n" \
    "v_mfma_f32_16x16x32_bf16 v[88:91], v[36:39], v[72:75], 0\n" \
    "v_mfma_f32_16x16x32_bf16 v[88:91], v[224:227], v[56:59], v[88:91]\n" \
    "v_mfma_f32_16x16x32_bf16 v[88:91], v[228:231], v[60:63], v[88:91]\n" \
    "v_mfma_f32_16x16x32_bf16 v[88:91], v[232:235], v[64:67], v[88:91]\n" \
    "v_mfma_f32_16x16x32_bf16 v[88:91], v[236:239], v[68:71], v[88:91]\n" \
    "v_mfma_f32_16x16x32_bf16 v[92:95], v[40:43], v[72:75], 0\n" \
    "v_mfma_f32_16x16x32_bf16 v[92:95], v[240:243], v[56:59], v[92:95]\n" \
    "v_mfma_f32_16x16x32_bf16 v[92:95], v[244:247], v[60:63], v[92:95]\n" \
    "v_mfma_f32_16x16x32_bf16 v[92:95], v[248:251], v[64:67], v[92:95]\n" \
    "v_mfma_f32_16x16x32_bf16 v[92:95], v[252:255], v[68:71], v[92:95]\n" \
    "v_mfma_f32_16x16x32_bf16 v[80:83], v[36:39], v[52:55], 0\n" \
    "v_mfma_f32_16x16x32_bf16 v[84:87], v[40:43], v[52:55], 0\n" \
    "v_mov_b32 v225, v226\n" \
    "v_mov_b32 v226, v228\n" \
    "v_mov_b32 v227, v230\n" \
    "v_mov_b32 v233, v234\n" \
    "v_mov_b32 v234, v236\n" \
    "v_mov_b32 v235, v238\n" \
    "v_mov_b32 v241, v242\n" \
    "v_mov_b32 v242, v244\n" \
    "v_mov_b32 v243, v246\n" \
    "v_mov_b32 v249, v250\n" \
    "v_mov_b32 v250, v252\n" \
    "v_mov_b32 v251, v254\n" \
    "ds_write_b128 v8, v[88:91] offset:2048\n" \
    "ds_write_b128 v8, v[92:95] offset:3072\n" \
    "v_permlane16_swap_b32 v224, v226\n" \
    "v_permlane16_swap_b32 v225, v227\n" \
    "v_permlane16_swap_b32 v232, v234\n" \
    "v_permlane16_swap_b32 v233, v235\n" \
    "v_permlane16_swap_b32 v240, v242\n" \
    "v_permlane16_swap_b32 v241, v243\n" \
    "v_permlane16_swap_b32 v248, v250\n" \
    "v_permlane16_swap_b32 v249, v251\n" \
    "v_mfma_f32_16x16x32_bf16 v[80:83], v[224:227], v[44:47], v[80:83]\n" \
    "v_mfma_f32_16x16x32_bf16 v[84:87], v[240:243], v[44:47], v[84:87]\n" \
    "v_mfma_f32_16x16x32_bf16 v[80:83], v[232:235], v[48:51], v[80:83]\n" \
    "v_mfma_f32_16x16x32_bf16 v[84:87], v[248:251], v[48:51], v[84:87]\n" \
    "s_nop 6\n" \
    "ds_write_b128 v8, v[80:83] offset:0\n" \
    "ds_write_b128 v8, v[84:87] offset:1024\n" \
    "s_waitcnt lgkmcnt(0)\n" \
    "s_barrier\n" \
    "s_mov_b32 s55, s62\n" \
    "buffer_load_dwordx2 v[16:17], v3, s[8:11], s51 offen sc1\n" \
    "s_cmp_eq_u32 s54, 0\n" \
    "s_cbranch_scc1 .Lr_cbC1_%=\n" \
    ".Lr_cbrC1_%=:\n" \
    "s_add_u32 s32, s32, 1\n" \
    "s_cmp_lt_u32 s32, 64\n" \
    "s_cbranch_scc1 .Lr_lC_%=\n" \
    "s_waitcnt vmcnt(0)\n" \
    "s_endpgm\n" \
    ".Lr_ctvD0_%=:\n" \
    "s_mov_b32 s52, 0\n" \
    "s_add_u32 s73, s32, s25\n" \
    ".Lr_ctwD0_%=:\n" \
    "buffer_load_dwordx2 v[16:17], v3, s[8:11], s51 offen sc1\n" \
    "s_waitcnt vmcnt(0)\n" \
    "v_cmp_ne_u32 vcc, s49, v17\n" \
    "s_nop 0\n" \
    "s_and_b64 vcc, vcc, s[56:57]\n" \
    "s_cbranch_scc0 .Lr_tvD0_%=\n" \
    "s_add_u32 s52, s52, 1\n" \
    "s_cmp_lt_u32 s52, s53\n" \
    "s_cbranch_scc1 .Lr_ctwD0_%=\n" \
    "s_mov_b32 s53, 0\n" \
    "v_mov_b32 v17, s49\n" \
    "v_mov_b32 v16, s55\n" \
    "s_branch .Lr_tvD0_%=\n" \
    ".Lr_cpD0_%=:\n" \
    "s_mov_b32 s52, 0\n" \
    "s_nop 7\n" \
    ".Lr_cqD0_%=:\n" \
    "buffer_load_dwordx4 v[36:39], v0, s[8:11], s36 offen sc1\n" \
    "buffer_load_dwordx4 v[40:43], v0, s[8:11], s37 offen sc1\n" \
    "buffer_load_dwordx4 v[32:35], v4, s[8:11], s35 offen sc1\n" \
    "buffer_load_dwordx4 v[24:27], v0, s[8:11], s35 offen sc1\n" \
    "buffer_load_dwordx4 v[28:31], v0, s[8:11], s35 offen offset:1024 sc1\n" \
    "s_waitcnt vmcnt(4)\n" \
    "v_max3_u32 v19, v36, v37, v38\n" \
    "s_waitcnt vmcnt(3)\n" \
    "v_max3_u32 v19, v19, v39, v40\n" \
    "v_max3_u32 v19, v19, v41, v42\n" \
    "v_max_u32 v19, v19, v43\n" \
    "s_waitcnt vmcnt(2)\n" \
    "v_mfma_f32_16x16x32_bf16 v[80:83], v[32:35], v[36:39], 0\n" \
    "v_max3_u32 v20, v32, v33, v34\n" \
    "v_mfma_f32_16x16x32_bf16 v[84:87], v[32:35], v[40:43], 0\n" \
    "v_max_u32 v20, v20, v35\n" \
    "s_waitcnt vmcnt(1)\n" \
    "v_smfmac_f32_16x16x64_bf16 v[80:83], v[24:27], v[192:199], v10\n" \
    "v_smfmac_f32_16x16x64_bf16 v[84:87], v[24:27], v[208:215], v10\n" \
    "v_max3_u32 v19, v19, v24, v25\n" \
    "v_max3_u32 v19, v19, v26, v27\n" \
    "s_waitcnt vmcnt(0)\n" \
    "v_smfmac_f32_16x16x64_bf16 v[80:83], v[28:31], v[200:207], v10\n" \
    "v_smfmac_f32_16x16x64_bf16 v[84:87], v[28:31], v[216:223], v10\n" \
    "v_max3_u32 v20, v20, v28, v29\n" \
    "v_max3_u32 v20, v20, v30, v31\n" \
    "v_max_u32 v19, v19, v20\n" \
    "v_cmp_eq_u32 vcc, -1, v19\n" \
    "s_nop 7\n" \
    "s_cbranch_vccz .Lr_cprD0_%=\n" \
    "s_add_u32 s52, s52, 1\n" \
    "s_cmp_lt_u32 s52, s53\n" \
    "s_cbranch_scc1 .Lr_cqD0_%=\n" \
    "s_mov_b32 s53, 0\n" \
    "s_branch .Lr_cprD0_%=\n" \
    ".Lr_ctD0_%=:\n" \
    "s_mov_b32 s52, 0\n" \
    "s_add_u32 s73, s32, s25\n" \
    ".Lr_cuD0_%=:\n" \
    "s_sleep 4\n" \
    "buffer_load_dwordx4 v[44:47], v0, s[8:11], s41 offen offset:0 sc1\n" \
    "buffer_load_dwordx4 v[48:51], v0, s[8:11], s41 offen offset:1024 sc1\n" \
    "buffer_load_dwordx4 v[52:55], v0, s[8:11], s41 offen offset:2048 sc1\n" \
    "buffer_load_dwordx4 v[56:59], v0, s[8:11], s41 offen offset:3072 sc1\n" \
    "buffer_load_dwordx4 v[60:63], v0, s[8:11], s42 offen offset:0 sc1\n" \
    "buffer_load_dwordx4 v[64:67], v0, s[8:11], s42 offen offset:1024 sc1\n" \
    "buffer_load_dwordx4 v[68:71], v0, s[8:11], s42 offen offset:2048 sc1\n" \
    "buffer_load_dwordx4 v[72:75], v0, s[8:11], s42 offen offset:3072 sc1\n" \
    "s_waitcnt vmcnt(7)\n" \
    "v_max3_u32 v19, v44, v45, v46\n" \
    "s_waitcnt vmcnt(6)\n" \
    "v_max3_u32 v19, v19, v47, v48\n" \
    "v_max3_u32 v19, v19, v49, v50\n" \
    "s_waitcnt vmcnt(5)\n" \
    "v_max3_u32 v19, v19, v51, v52\n" \
    "v_max3_u32 v19, v19, v53, v54\n" \
    "s_waitcnt vmcnt(4)\n" \
    "v_max3_u32 v19, v19, v55, v56\n" \
    "v_max3_u32 v19, v19, v57, v58\n" \
    "s_waitcnt vmcnt(3)\n" \
    "v_max3_u32 v19, v19, v59, v60\n" \
    "v_max3_u32 v19, v19, v61, v62\n" \
    "s_waitcnt vmcnt(2)\n" \
    "v_max3_u32 v19, v19, v63, v64\n" \
    "v_max3_u32 v19, v19, v65, v66\n" \
    "s_waitcnt vmcnt(1)\n" \
    "v_max3_u32 v19, v19, v67, v68\n" \
    "v_max3_u32 v19, v19, v69, v70\n" \
    "s_waitcnt vmcnt(0)\n" \
    "v_max3_u32 v19, v19, v71, v72\n" \
    "v_max3_u32 v19, v19, v73, v74\n" \
    "v_max_u32 v19, v19, v75\n" \
    "v_cmp_eq_u32 vcc, -1, v19\n" \
    "s_nop 0\n" \
    "s_cbranch_vccz .Lr_ctrD0_%=\n" \
    "s_add_u32 s52, s52, 1\n" \
    "s_cmp_lt_u32 s52, s53\n" \
    "s_cbranch_scc1 .Lr_cuD0_%=\n" \
    "s_mov_b32 s53, 0\n" \
    "s_branch .Lr_ctrD0_%=\n" \
    ".Lr_cbD0_%=:\n" \
    "s_add_u32 s73, s32, s25\n" \
    "s_mov_b32 s50, s49\n" \
    "s_mov_b32 s52, 0\n" \
    ".Lr_ccD0_%=:\n" \
    "buffer_load_dword v18, v2, s[8:11], 0 offen sc1\n" \
    "s_waitcnt vmcnt(0)\n" \
    "v_cmp_ge_u32 vcc, s50, v18\n" \
    "s_nop 0\n" \
    "s_and_b64 vcc, vcc, s[56:57]\n" \
    "s_cbranch_scc0 .Lr_cdD0_%=\n" \
    "s_sleep 2\n" \
    "s_add_u32 s52, s52, 1\n" \
    "s_cmp_lt_u32 s52, s53\n" \
    "s_cbranch_scc1 .Lr_ccD0_%=\n" \
    "s_mov_b32 s53, 0\n" \
    "s_branch .Lr_cdD0_%=\n" \
    ".Lr_cdD0_%=:\n" \
    "s_and_b32 s33, s49, %[brn]-1\n" \
    "s_mul_i32 s39, s33, 2621440\n" \
    "s_add_u32 s39, s39, s19\n" \
    "s_lshr_b32 s33, s39, 8\n" \
    "s_xor_b32 s33, s33, s27\n" \
    "s_and_b32 s33, s33, 0x2000\n" \
    "s_or_b32 s39, s39, s33\n" \
    "s_add_u32 s40, s39, 65536\n" \
    "s_waitcnt vmcnt(0)\n" \
    "buffer_load_dwordx4 v[224:227], v0, s[8:11], s39 offen offset:0 sc1\n" \
    "buffer_load_dwordx4 v[228:231], v0, s[8:11], s39 offen offset:1024 sc1\n" \
    "buffer_load_dwordx4 v[232:235], v0, s[8:11], s39 offen offset:2048 sc1\n" \
    "buffer_load_dwordx4 v[236:239], v0, s[8:11], s39 offen offset:3072 sc1\n" \
    "buffer_load_dwordx4 v[240:243], v0, s[8:11], s40 offen offset:0 sc1\n" \
    "buffer_load_dwordx4 v[244:247], v0, s[8:11], s40 offen offset:1024 sc1\n" \
    "buffer_load_dwordx4 v[248:251], v0, s[8:11], s40 offen offset:2048 sc1\n" \
    "buffer_load_dwordx4 v[252:255], v0, s[8:11], s40 offen offset:3072 sc1\n" \
    "s_waitcnt vmcnt(0)\n" \
    "s_branch .Lr_cbrD0_%=\n" \
    ".Lr_ctvD1_%=:\n" \
    "s_mov_b32 s52, 0\n" \
    "s_add_u32 s73, s32, s25\n" \
    ".Lr_ctwD1_%=:\n" \
    "buffer_load_dwordx2 v[16:17], v3, s[8:11], s51 offen sc1\n" \
    "s_waitcnt vmcnt(0)\n" \
    "v_cmp_ne_u32 vcc, s49, v17\n" \
    "s_nop 0\n" \
    "s_and_b64 vcc, vcc, s[56:57]\n" \
    "s_cbranch_scc0 .Lr_tvD1_%=\n" \
    "s_add_u32 s52, s52, 1\n" \
    "s_cmp_lt_u32 s52, s53\n" \
    "s_cbranch_scc1 .Lr_ctwD1_%=\n" \
    "s_mov_b32 s53, 0\n" \
    "v_mov_b32 v17, s49\n" \
    "v_mov_b32 v16, s55\n" \
    "s_branch .Lr_tvD1_%=\n" \
    ".Lr_cpD1_%=:\n" \
    "s_mov_b32 s52, 0\n" \
    "s_nop 7\n" \
    ".Lr_cqD1_%=:\n" \
    "buffer_load_dwordx4 v[36:39], v0, s[8:11], s36 offen sc1\n" \
    "buffer_load_dwordx4 v[40:43], v0, s[8:11], s37 offen sc1\n" \
    "buffer_load_dwordx4 v[32:35], v4, s[8:11], s35 offen sc1\n" \
    "buffer_load_dwordx4 v[24:27], v0, s[8:11], s35 offen sc1\n" \
    "buffer_load_dwordx4 v[28:31], v0, s[8:11], s35 offen offset:1024 sc1\n" \
    "s_waitcnt vmcnt(4)\n" \
    "v_max3_u32 v19, v36, v37, v38\n" \
    "s_waitcnt vmcnt(3)\n" \
    "v_max3_u32 v19, v19, v39, v40\n" \
    "v_max3_u32 v19, v19, v41, v42\n" \
    "v_max_u32 v19, v19, v43\n" \
    "s_waitcnt vmcnt(2)\n" \
    "v_mfma_f32_16x16x32_bf16 v[80:83], v[32:35], v[36:39], 0\n" \
    "v_max3_u32 v20, v32, v33, v34\n" \
    "v_mfma_f32_16x16x32_bf16 v[84:87], v[32:35], v[40:43], 0\n" \
    "v_max_u32 v20, v20, v35\n" \
    "s_waitcnt vmcnt(1)\n" \
    "v_smfmac_f32_16x16x64_bf16 v[80:83], v[24:27], v[224:231], v10\n" \
    "v_smfmac_f32_16x16x64_bf16 v[84:87], v[24:27], v[240:247], v10\n" \
    "v_max3_u32 v19, v19, v24, v25\n" \
    "v_max3_u32 v19, v19, v26, v27\n" \
    "s_waitcnt vmcnt(0)\n" \
    "v_smfmac_f32_16x16x64_bf16 v[80:83], v[28:31], v[232:239], v10\n" \
    "v_smfmac_f32_16x16x64_bf16 v[84:87], v[28:31], v[248:255], v10\n" \
    "v_max3_u32 v20, v20, v28, v29\n" \
    "v_max3_u32 v20, v20, v30, v31\n" \
    "v_max_u32 v19, v19, v20\n" \
    "v_cmp_eq_u32 vcc, -1, v19\n" \
    "s_nop 7\n" \
    "s_cbranch_vccz .Lr_cprD1_%=\n" \
    "s_add_u32 s52, s52, 1\n" \
    "s_cmp_lt_u32 s52, s53\n" \
    "s_cbranch_scc1 .Lr_cqD1_%=\n" \
    "s_mov_b32 s53, 0\n" \
    "s_branch .Lr_cprD1_%=\n" \
    ".Lr_ctD1_%=:\n" \
    "s_mov_b32 s52, 0\n" \
    "s_add_u32 s73, s32, s25\n" \
    ".Lr_cuD1_%=:\n" \
    "s_sleep 4\n" \
    "buffer_load_dwordx4 v[44:47], v0, s[8:11], s41 offen offset:0 sc1\n" \
    "buffer_load_dwordx4 v[48:51], v0, s[8:11], s41 offen offset:1024 sc1\n" \
    "buffer_load_dwordx4 v[52:55], v0, s[8:11], s41 offen offset:2048 sc1\n" \
    "buffer_load_dwordx4 v[56:59], v0, s[8:11], s41 offen offset:3072 sc1\n" \
    "buffer_load_dwordx4 v[60:63], v0, s[8:11], s42 offen offset:0 sc1\n" \
    "buffer_load_dwordx4 v[64:67], v0, s[8:11], s42 offen offset:1024 sc1\n" \
    "buffer_load_dwordx4 v[68:71], v0, s[8:11], s42 offen offset:2048 sc1\n" \
    "buffer_load_dwordx4 v[72:75], v0, s[8:11], s42 offen offset:3072 sc1\n" \
    "s_waitcnt vmcnt(7)\n" \
    "v_max3_u32 v19, v44, v45, v46\n" \
    "s_waitcnt vmcnt(6)\n" \
    "v_max3_u32 v19, v19, v47, v48\n" \
    "v_max3_u32 v19, v19, v49, v50\n" \
    "s_waitcnt vmcnt(5)\n" \
    "v_max3_u32 v19, v19, v51, v52\n" \
    "v_max3_u32 v19, v19, v53, v54\n" \
    "s_waitcnt vmcnt(4)\n" \
    "v_max3_u32 v19, v19, v55, v56\n" \
    "v_max3_u32 v19, v19, v57, v58\n" \
    "s_waitcnt vmcnt(3)\n" \
    "v_max3_u32 v19, v19, v59, v60\n" \
    "v_max3_u32 v19, v19, v61, v62\n" \
    "s_waitcnt vmcnt(2)\n" \
    "v_max3_u32 v19, v19, v63, v64\n" \
    "v_max3_u32 v19, v19, v65, v66\n" \
    "s_waitcnt vmcnt(1)\n" \
    "v_max3_u32 v19, v19, v67, v68\n" \
    "v_max3_u32 v19, v19, v69, v70\n" \
    "s_waitcnt vmcnt(0)\n" \
    "v_max3_u32 v19, v19, v71, v72\n" \
    "v_max3_u32 v19, v19, v73, v74\n" \
    "v_max_u32 v19, v19, v75\n" \
    "v_cmp_eq_u32 vcc, -1, v19\n" \
    "s_nop 0\n" \
    "s_cbranch_vccz .Lr_ctrD1_%=\n" \
    "s_add_u32 s52, s52, 1\n" \
    "s_cmp_lt_u32 s52, s53\n" \
    "s_cbranch_scc1 .Lr_cuD1_%=\n" \
    "s_mov_b32 s53, 0\n" \
    "s_branch .Lr_ctrD1_%=\n" \
    ".Lr_cbD1_%=:\n" \
    "s_add_u32 s73, s32, s25\n" \
    "s_mov_b32 s50, s49\n" \
    "s_mov_b32 s52, 0\n" \
    ".Lr_ccD1_%=:\n" \
    "buffer_load_dword v18, v2, s[8:11], 0 offen sc1\n" \
    "s_waitcnt vmcnt(0)\n" \
    "v_cmp_ge_u32 vcc, s50, v18\n" \
    "s_nop 0\n" \
    "s_and_b64 vcc, vcc, s[56:57]\n" \
    "s_cbranch_scc0 .Lr_cdD1_%=\n" \
    "s_sleep 2\n" \
    "s_add_u32 s52, s52, 1\n" \
    "s_cmp_lt_u32 s52, s53\n" \
    "s_cbranch_scc1 .Lr_ccD1_%=\n" \
    "s_mov_b32 s53, 0\n" \
    "s_branch .Lr_cdD1_%=\n" \
    ".Lr_cdD1_%=:\n" \
    "s_and_b32 s33, s49, %[brn]-1\n" \
    "s_mul_i32 s39, s33, 2621440\n" \
    "s_add_u32 s39, s39, s19\n" \
    "s_lshr_b32 s33, s39, 8\n" \
    "s_xor_b32 s33, s33, s27\n" \
    "s_and_b32 s33, s33, 0x2000\n" \
    "s_or_b32 s39, s39, s33\n" \
    "s_add_u32 s40, s39, 65536\n" \
    "s_waitcnt vmcnt(0)\n" \
    "buffer_load_dwordx4 v[192:195], v0, s[8:11], s39 offen offset:0 sc1\n" \
    "buffer_load_dwordx4 v[196:199], v0, s[8:11], s39 offen offset:1024 sc1\n" \
    "buffer_load_dwordx4 v[200:203], v0, s[8:11], s39 offen offset:2048 sc1\n" \
    "buffer_load_dwordx4 v[204:207], v0, s[8:11], s39 offen offset:3072 sc1\n" \
    "buffer_load_dwordx4 v[208:211], v0, s[8:11], s40 offen offset:0 sc1\n" \
    "buffer_load_dwordx4 v[212:215], v0, s[8:11], s40 offen offset:1024 sc1\n" \
    "buffer_load_dwordx4 v[216:219], v0, s[8:11], s40 offen offset:2048 sc1\n" \
    "buffer_load_dwordx4 v[220:223], v0, s[8:11], s40 offen offset:3072 sc1\n" \
    "s_waitcnt vmcnt(0)\n" \
    "s_branch .Lr_cbrD1_%=\n" \
    ".Lr_ctvA0_%=:\n" \
    "s_mov_b32 s52, 0\n" \
    "s_add_u32 s73, s32, s25\n" \
    ".Lr_ctwA0_%=:\n" \
    "buffer_load_dwordx2 v[16:17], v3, s[8:11], s51 offen sc1\n" \
    "s_waitcnt vmcnt(0)\n" \
    "v_cmp_ne_u32 vcc, s49, v17\n" \
    "s_nop 0\n" \
    "s_and_b64 vcc, vcc, s[56:57]\n" \
    "s_cbranch_scc0 .Lr_tvA0_%=\n" \
    "s_add_u32 s52, s52, 1\n" \
    "s_cmp_lt_u32 s52, s53\n" \
    "s_cbranch_scc1 .Lr_ctwA0_%=\n" \
    "s_mov_b32 s53, 0\n" \
    "v_mov_b32 v17, s49\n" \
    "v_mov_b32 v16, s55\n" \
    "s_branch .Lr_tvA0_%=\n" \
    ".Lr_cpA0_%=:\n" \
    "s_mov_b32 s52, 0\n" \
    "s_nop 7\n" \
    ".Lr_cqA0_%=:\n" \
    "buffer_load_dwordx4 v[36:39], v0, s[8:11], s36 offen sc1\n" \
    "buffer_load_dwordx4 v[40:43], v0, s[8:11], s37 offen sc1\n" \
    "buffer_load_dwordx4 v[32:35], v4, s[8:11], s35 offen sc1\n" \
    "buffer_load_dwordx4 v[24:27], v0, s[8:11], s35 offen sc1\n" \
    "buffer_load_dwordx4 v[28:31], v0, s[8:11], s35 offen offset:1024 sc1\n" \
    "s_waitcnt vmcnt(4)\n" \
    "v_max3_u32 v19, v36, v37, v38\n" \
    "s_waitcnt vmcnt(3)\n" \
    "v_max3_u32 v19, v19, v39, v40\n" \
    "v_max3_u32 v19, v19, v41, v42\n" \
    "v_max_u32 v19, v19, v43\n" \
    "s_waitcnt vmcnt(2)\n" \
    "v_mfma_f32_16x16x32_bf16 v[80:83], v[32:35], v[36:39], 0\n" \
    "v_max3_u32 v20, v32, v33, v34\n" \
    "v_mfma_f32_16x16x32_bf16 v[84:87], v[32:35], v[40:43], 0\n" \
    "v_max_u32 v20, v20, v35\n" \
    "s_waitcnt vmcnt(1)\n" \
    "v_smfmac_f32_16x16x64_bf16 v[80:83], v[24:27], v[192:199], v10\n" \
    "v_smfmac_f32_16x16x64_bf16 v[84:87], v[24:27], v[208:215], v10\n" \
    "v_max3_u32 v19, v19, v24, v25\n" \
    "v_max3_u32 v19, v19, v26, v27\n" \
    "s_waitcnt vmcnt(0)\n" \
    "v_smfmac_f32_16x16x64_bf16 v[80:83], v[28:31], v[200:207], v10\n" \
    "v_smfmac_f32_16x16x64_bf16 v[84:87], v[28:31], v[216:223], v10\n" \
    "v_max3_u32 v20, v20, v28, v29\n" \
    "v_max3_u32 v20, v20, v30, v31\n" \
    "v_max_u32 v19, v19, v20\n" \
    "v_cmp_eq_u32 vcc, -1, v19\n" \
    "s_nop 7\n" \
    "s_cbranch_vccz .Lr_cprA0_%=\n" \
    "s_add_u32 s52, s52, 1\n" \
    "s_cmp_lt_u32 s52, s53\n" \
    "s_cbranch_scc1 .Lr_cqA0_%=\n" \
    "s_mov_b32 s53, 0\n" \
    "s_branch .Lr_cprA0_%=\n" \
    ".Lr_ctA0_%=:\n" \
    "s_mov_b32 s52, 0\n" \
    "s_add_u32 s73, s32, s25\n" \
    ".Lr_cuA0_%=:\n" \
    "s_sleep 4\n" \
    "buffer_load_dwordx4 v[44:47], v0, s[8:11], s41 offen offset:0 sc1\n" \
    "buffer_load_dwordx4 v[48:51], v0, s[8:11], s41 offen offset:1024 sc1\n" \
    "buffer_load_dwordx4 v[52:55], v0, s[8:11], s41 offen offset:2048 sc1\n" \
    "buffer_load_dwordx4 v[56:59], v0, s[8:11], s41 offen offset:3072 sc1\n" \
    "buffer_load_dwordx4 v[60:63], v0, s[8:11], s42 offen offset:0 sc1\n" \
    "buffer_load_dwordx4 v[64:67], v0, s[8:11], s42 offen offset:1024 sc1\n" \
    "buffer_load_dwordx4 v[68:71], v0, s[8:11], s42 offen offset:2048 sc1\n" \
    "buffer_load_dwordx4 v[72:75], v0, s[8:11], s42 offen offset:3072 sc1\n" \
    "s_waitcnt vmcnt(7)\n" \
    "v_max3_u32 v19, v44, v45, v46\n" \
    "s_waitcnt vmcnt(6)\n" \
    "v_max3_u32 v19, v19, v47, v48\n" \
    "v_max3_u32 v19, v19, v49, v50\n" \
    "s_waitcnt vmcnt(5)\n" \
    "v_max3_u32 v19, v19, v51, v52\n" \
    "v_max3_u32 v19, v19, v53, v54\n" \
    "s_waitcnt vmcnt(4)\n" \
    "v_max3_u32 v19, v19, v55, v56\n" \
    "v_max3_u32 v19, v19, v57, v58\n" \
    "s_waitcnt vmcnt(3)\n" \
    "v_max3_u32 v19, v19, v59, v60\n" \
    "v_max3_u32 v19, v19, v61, v62\n" \
    "s_waitcnt vmcnt(2)\n" \
    "v_max3_u32 v19, v19, v63, v64\n" \
    "v_max3_u32 v19, v19, v65, v66\n" \
    "s_waitcnt vmcnt(1)\n" \
    "v_max3_u32 v19, v19, v67, v68\n" \
    "v_max3_u32 v19, v19, v69, v70\n" \
    "s_waitcnt vmcnt(0)\n" \
    "v_max3_u32 v19, v19, v71, v72\n" \
    "v_max3_u32 v19, v19, v73, v74\n" \
    "v_max_u32 v19, v19, v75\n" \
    "v_cmp_eq_u32 vcc, -1, v19\n" \
    "s_nop 0\n" \
    "s_cbranch_vccz .Lr_ctrA0_%=\n" \
    "s_add_u32 s52, s52, 1\n" \
    "s_cmp_lt_u32 s52, s53\n" \
    "s_cbranch_scc1 .Lr_cuA0_%=\n" \
    "s_mov_b32 s53, 0\n" \
    "s_branch .Lr_ctrA0_%=\n" \
    ".Lr_cbA0_%=:\n" \
    "s_add_u32 s73, s32, s25\n" \
    "s_mov_b32 s50, s49\n" \
    "s_mov_b32 s52, 0\n" \
    ".Lr_ccA0_%=:\n" \
    "buffer_load_dword v18, v2, s[8:11], 0 offen sc1\n" \
    "s_waitcnt vmcnt(0)\n" \
    "v_cmp_ge_u32 vcc, s50, v18\n" \
    "s_nop 0\n" \
    "s_and_b64 vcc, vcc, s[56:57]\n" \
    "s_cbranch_scc0 .Lr_cdA0_%=\n" \
    "s_sleep 2\n" \
    "s_add_u32 s52, s52, 1\n" \
    "s_cmp_lt_u32 s52, s53\n" \
    "s_cbranch_scc1 .Lr_ccA0_%=\n" \
    "s_mov_b32 s53, 0\n" \
    "s_branch .Lr_cdA0_%=\n" \
    ".Lr_cdA0_%=:\n" \
    "s_and_b32 s33, s49, %[brn]-1\n" \
    "s_mul_i32 s39, s33, 2621440\n" \
    "s_add_u32 s39, s39, s19\n" \
    "s_lshr_b32 s33, s39, 8\n" \
    "s_xor_b32 s33, s33, s27\n" \
    "s_and_b32 s33, s33, 0x2000\n" \
    "s_or_b32 s39, s39, s33\n" \
    "s_add_u32 s40, s39, 65536\n" \
    "s_waitcnt vmcnt(0)\n" \
    "buffer_load_dwordx4 v[224:227], v0, s[8:11], s39 offen offset:0 sc1\n" \
    "buffer_load_dwordx4 v[228:231], v0, s[8:11], s39 offen offset:1024 sc1\n" \
    "buffer_load_dwordx4 v[232:235], v0, s[8:11], s39 offen offset:2048 sc1\n" \
    "buffer_load_dwordx4 v[236:239], v0, s[8:11], s39 offen offset:3072 sc1\n" \
    "buffer_load_dwordx4 v[240:243], v0, s[8:11], s40 offen offset:0 sc1\n" \
    "buffer_load_dwordx4 v[244:247], v0, s[8:11], s40 offen offset:1024 sc1\n" \
    "buffer_load_dwordx4 v[248:251], v0, s[8:11], s40 offen offset:2048 sc1\n" \
    "buffer_load_dwordx4 v[252:255], v0, s[8:11], s40 offen offset:3072 sc1\n" \
    "s_waitcnt vmcnt(0)\n" \
    "s_branch .Lr_cbrA0_%=\n" \
    ".Lr_ctvA1_%=:\n" \
    "s_mov_b32 s52, 0\n" \
    "s_add_u32 s73, s32, s25\n" \
    ".Lr_ctwA1_%=:\n" \
    "buffer_load_dwordx2 v[16:17], v3, s[8:11], s51 offen sc1\n" \
    "s_waitcnt vmcnt(0)\n" \
    "v_cmp_ne_u32 vcc, s49, v17\n" \
    "s_nop 0\n" \
    "s_and_b64 vcc, vcc, s[56:57]\n" \
    "s_cbranch_scc0 .Lr_tvA1_%=\n" \
    "s_add_u32 s52, s52, 1\n" \
    "s_cmp_lt_u32 s52, s53\n" \
    "s_cbranch_scc1 .Lr_ctwA1_%=\n" \
    "s_mov_b32 s53, 0\n" \
    "v_mov_b32 v17, s49\n" \
    "v_mov_b32 v16, s55\n" \
    "s_branch .Lr_tvA1_%=\n" \
    ".Lr_cpA1_%=:\n" \
    "s_mov_b32 s52, 0\n" \
    "s_nop 7\n" \
    ".Lr_cqA1_%=:\n" \
    "buffer_load_dwordx4 v[36:39], v0, s[8:11], s36 offen sc1\n" \
    "buffer_load_dwordx4 v[40:43], v0, s[8:11], s37 offen sc1\n" \
    "buffer_load_dwordx4 v[32:35], v4, s[8:11], s35 offen sc1\n" \
    "buffer_load_dwordx4 v[24:27], v0, s[8:11], s35 offen sc1\n" \
    "buffer_load_dwordx4 v[28:31], v0, s[8:11], s35 offen offset:1024 sc1\n" \
    "s_waitcnt vmcnt(4)\n" \
    "v_max3_u32 v19, v36, v37, v38\n" \
    "s_waitcnt vmcnt(3)\n" \
    "v_max3_u32 v19, v19, v39, v40\n" \
    "v_max3_u32 v19, v19, v41, v42\n" \
    "v_max_u32 v19, v19, v43\n" \
    "s_waitcnt vmcnt(2)\n" \
    "v_mfma_f32_16x16x32_bf16 v[80:83], v[32:35], v[36:39], 0\n" \
    "v_max3_u32 v20, v32, v33, v34\n" \
    "v_mfma_f32_16x16x32_bf16 v[84:87], v[32:35], v[40:43], 0\n" \
    "v_max_u32 v20, v20, v35\n" \
    "s_waitcnt vmcnt(1)\n" \
    "v_smfmac_f32_16x16x64_bf16 v[80:83], v[24:27], v[224:231], v10\n" \
    "v_smfmac_f32_16x16x64_bf16 v[84:87], v[24:27], v[240:247], v10\n" \
    "v_max3_u32 v19, v19, v24, v25\n" \
    "v_max3_u32 v19, v19, v26, v27\n" \
    "s_waitcnt vmcnt(0)\n" \
    "v_smfmac_f32_16x16x64_bf16 v[80:83], v[28:31], v[232:239], v10\n" \
    "v_smfmac_f32_16x16x64_bf16 v[84:87], v[28:31], v[248:255], v10\n" \
    "v_max3_u32 v20, v20, v28, v29\n" \
    "v_max3_u32 v20, v20, v30, v31\n" \
    "v_max_u32 v19, v19, v20\n" \
    "v_cmp_eq_u32 vcc, -1, v19\n" \
    "s_nop 7\n" \
    "s_cbranch_vccz .Lr_cprA1_%=\n" \
    "s_add_u32 s52, s52, 1\n" \
    "s_cmp_lt_u32 s52, s53\n" \
    "s_cbranch_scc1 .Lr_cqA1_%=\n" \
    "s_mov_b32 s53, 0\n" \
    "s_branch .Lr_cprA1_%=\n" \
    ".Lr_ctA1_%=:\n" \
    "s_mov_b32 s52, 0\n" \
    "s_add_u32 s73, s32, s25\n" \
    ".Lr_cuA1_%=:\n" \
    "s_sleep 4\n" \
    "buffer_load_dwordx4 v[44:47], v0, s[8:11], s41 offen offset:0 sc1\n" \
    "buffer_load_dwordx4 v[48:51], v0, s[8:11], s41 offen offset:1024 sc1\n" \
    "buffer_load_dwordx4 v[52:55], v0, s[8:11], s41 offen offset:2048 sc1\n" \
    "buffer_load_dwordx4 v[56:59], v0, s[8:11], s41 offen offset:3072 sc1\n" \
    "buffer_load_dwordx4 v[60:63], v0, s[8:11], s42 offen offset:0 sc1\n" \
    "buffer_load_dwordx4 v[64:67], v0, s[8:11], s42 offen offset:1024 sc1\n" \
    "buffer_load_dwordx4 v[68:71], v0, s[8:11], s42 offen offset:2048 sc1\n" \
    "buffer_load_dwordx4 v[72:75], v0, s[8:11], s42 offen offset:3072 sc1\n" \
    "s_waitcnt vmcnt(7)\n" \
    "v_max3_u32 v19, v44, v45, v46\n" \
    "s_waitcnt vmcnt(6)\n" \
    "v_max3_u32 v19, v19, v47, v48\n" \
    "v_max3_u32 v19, v19, v49, v50\n" \
    "s_waitcnt vmcnt(5)\n" \
    "v_max3_u32 v19, v19, v51, v52\n" \
    "v_max3_u32 v19, v19, v53, v54\n" \
    "s_waitcnt vmcnt(4)\n" \
    "v_max3_u32 v19, v19, v55, v56\n" \
    "v_max3_u32 v19, v19, v57, v58\n" \
    "s_waitcnt vmcnt(3)\n" \
    "v_max3_u32 v19, v19, v59, v60\n" \
    "v_max3_u32 v19, v19, v61, v62\n" \
    "s_waitcnt vmcnt(2)\n" \
    "v_max3_u32 v19, v19, v63, v64\n" \
    "v_max3_u32 v19, v19, v65, v66\n" \
    "s_waitcnt vmcnt(1)\n" \
    "v_max3_u32 v19, v19, v67, v68\n" \
    "v_max3_u32 v19, v19, v69, v70\n" \
    "s_waitcnt vmcnt(0)\n" \
    "v_max3_u32 v19, v19, v71, v72\n" \
    "v_max3_u32 v19, v19, v73, v74\n" \
    "v_max_u32 v19, v19, v75\n" \
    "v_cmp_eq_u32 vcc, -1, v19\n" \
    "s_nop 0\n" \
    "s_cbranch_vccz .Lr_ctrA1_%=\n" \
    "s_add_u32 s52, s52, 1\n" \
    "s_cmp_lt_u32 s52, s53\n" \
    "s_cbranch_scc1 .Lr_cuA1_%=\n" \
    "s_mov_b32 s53, 0\n" \
    "s_branch .Lr_ctrA1_%=\n" \
    ".Lr_cbA1_%=:\n" \
    "s_add_u32 s73, s32, s25\n" \
    "s_mov_b32 s50, s49\n" \
    "s_mov_b32 s52, 0\n" \
    ".Lr_ccA1_%=:\n" \
    "buffer_load_dword v18, v2, s[8:11], 0 offen sc1\n" \
    "s_waitcnt vmcnt(0)\n" \
    "v_cmp_ge_u32 vcc, s50, v18\n" \
    "s_nop 0\n" \
    "s_and_b64 vcc, vcc, s[56:57]\n" \
    "s_cbranch_scc0 .Lr_cdA1_%=\n" \
    "s_sleep 2\n" \
    "s_add_u32 s52, s52, 1\n" \
    "s_cmp_lt_u32 s52, s53\n" \
    "s_cbranch_scc1 .Lr_ccA1_%=\n" \
    "s_mov_b32 s53, 0\n" \
    "s_branch .Lr_cdA1_%=\n" \
    ".Lr_cdA1_%=:\n" \
    "s_and_b32 s33, s49, %[brn]-1\n" \
    "s_mul_i32 s39, s33, 2621440\n" \
    "s_add_u32 s39, s39, s19\n" \
    "s_lshr_b32 s33, s39, 8\n" \
    "s_xor_b32 s33, s33, s27\n" \
    "s_and_b32 s33, s33, 0x2000\n" \
    "s_or_b32 s39, s39, s33\n" \
    "s_add_u32 s40, s39, 65536\n" \
    "s_waitcnt vmcnt(0)\n" \
    "buffer_load_dwordx4 v[192:195], v0, s[8:11], s39 offen offset:0 sc1\n" \
    "buffer_load_dwordx4 v[196:199], v0, s[8:11], s39 offen offset:1024 sc1\n" \
    "buffer_load_dwordx4 v[200:203], v0, s[8:11], s39 offen offset:2048 sc1\n" \
    "buffer_load_dwordx4 v[204:207], v0, s[8:11], s39 offen offset:3072 sc1\n" \
    "buffer_load_dwordx4 v[208:211], v0, s[8:11], s40 offen offset:0 sc1\n" \
    "buffer_load_dwordx4 v[212:215], v0, s[8:11], s40 offen offset:1024 sc1\n" \
    "buffer_load_dwordx4 v[216:219], v0, s[8:11], s40 offen offset:2048 sc1\n" \
    "buffer_load_dwordx4 v[220:223], v0, s[8:11], s40 offen offset:3072 sc1\n" \
    "s_waitcnt vmcnt(0)\n" \
    "s_branch .Lr_cbrA1_%=\n" \
    ".Lr_ctvB0_%=:\n" \
    "s_mov_b32 s52, 0\n" \
    "s_add_u32 s73, s32, s25\n" \
    ".Lr_ctwB0_%=:\n" \
    "buffer_load_dwordx2 v[16:17], v3, s[8:11], s51 offen sc1\n" \
    "s_waitcnt vmcnt(0)\n" \
    "v_cmp_ne_u32 vcc, s49, v17\n" \
    "s_nop 0\n" \
    "s_and_b64 vcc, vcc, s[56:57]\n" \
    "s_cbranch_scc0 .Lr_tvB0_%=\n" \
    "s_add_u32 s52, s52, 1\n" \
    "s_cmp_lt_u32 s52, s53\n" \
    "s_cbranch_scc1 .Lr_ctwB0_%=\n" \
    "s_mov_b32 s53, 0\n" \
    "v_mov_b32 v17, s49\n" \
    "v_mov_b32 v16, s55\n" \
    "s_branch .Lr_tvB0_%=\n" \
    ".Lr_cpB0_%=:\n" \
    "s_mov_b32 s52, 0\n" \
    "s_nop 7\n" \
    ".Lr_cqB0_%=:\n" \
    "buffer_load_dwordx4 v[36:39], v0, s[8:11], s36 offen sc1\n" \
    "buffer_load_dwordx4 v[40:43], v0, s[8:11], s37 offen sc1\n" \
    "buffer_load_dwordx4 v[32:35], v4, s[8:11], s35 offen sc1\n" \
    "buffer_load_dwordx4 v[24:27], v0, s[8:11], s35 offen sc1\n" \
    "buffer_load_dwordx4 v[28:31], v0, s[8:11], s35 offen offset:1024 sc1\n" \
    "s_waitcnt vmcnt(4)\n" \
    "v_max3_u32 v19, v36, v37, v38\n" \
    "s_waitcnt vmcnt(3)\n" \
    "v_max3_u32 v19, v19, v39, v40\n" \
    "v_max3_u32 v19, v19, v41, v42\n" \
    "v_max_u32 v19, v19, v43\n" \
    "s_waitcnt vmcnt(2)\n" \
    "v_mfma_f32_16x16x32_bf16 v[80:83], v[32:35], v[36:39], 0\n" \
    "v_max3_u32 v20, v32, v33, v34\n" \
    "v_mfma_f32_16x16x32_bf16 v[84:87], v[32:35], v[40:43], 0\n" \
    "v_max_u32 v20, v20, v35\n" \
    "s_waitcnt vmcnt(1)\n" \
    "v_smfmac_f32_16x16x64_bf16 v[80:83], v[24:27], v[192:199], v10\n" \
    "v_smfmac_f32_16x16x64_bf16 v[84:87], v[24:27], v[208:215], v10\n" \
    "v_max3_u32 v19, v19, v24, v25\n" \
    "v_max3_u32 v19, v19, v26, v27\n" \
    "s_waitcnt vmcnt(0)\n" \
    "v_smfmac_f32_16x16x64_bf16 v[80:83], v[28:31], v[200:207], v10\n" \
    "v_smfmac_f32_16x16x64_bf16 v[84:87], v[28:31], v[216:223], v10\n" \
    "v_max3_u32 v20, v20, v28, v29\n" \
    "v_max3_u32 v20, v20, v30, v31\n" \
    "v_max_u32 v19, v19, v20\n" \
    "v_cmp_eq_u32 vcc, -1, v19\n" \
    "s_nop 7\n" \
    "s_cbranch_vccz .Lr_cprB0_%=\n" \
    "s_add_u32 s52, s52, 1\n" \
    "s_cmp_lt_u32 s52, s53\n" \
    "s_cbranch_scc1 .Lr_cqB0_%=\n" \
    "s_mov_b32 s53, 0\n" \
    "s_branch .Lr_cprB0_%=\n" \
    ".Lr_ctB0_%=:\n" \
    "s_mov_b32 s52, 0\n" \
    "s_add_u32 s73, s32, s25\n" \
    ".Lr_cuB0_%=:\n" \
    "s_sleep 4\n" \
    "buffer_load_dwordx4 v[44:47], v0, s[8:11], s41 offen offset:0 sc1\n" \
    "buffer_load_dwordx4 v[48:51], v0, s[8:11], s41 offen offset:1024 sc1\n" \
    "buffer_load_dwordx4 v[52:55], v0, s[8:11], s41 offen offset:2048 sc1\n" \
    "buffer_load_dwordx4 v[56:59], v0, s[8:11], s41 offen offset:3072 sc1\n" \
    "buffer_load_dwordx4 v[60:63], v0, s[8:11], s42 offen offset:0 sc1\n" \
    "buffer_load_dwordx4 v[64:67], v0, s[8:11], s42 offen offset:1024 sc1\n" \
    "buffer_load_dwordx4 v[68:71], v0, s[8:11], s42 offen offset:2048 sc1\n" \
    "buffer_load_dwordx4 v[72:75], v0, s[8:11], s42 offen offset:3072 sc1\n" \
    "s_waitcnt vmcnt(7)\n" \
    "v_max3_u32 v19, v44, v45, v46\n" \
    "s_waitcnt vmcnt(6)\n" \
    "v_max3_u32 v19, v19, v47, v48\n" \
    "v_max3_u32 v19, v19, v49, v50\n" \
    "s_waitcnt vmcnt(5)\n" \
    "v_max3_u32 v19, v19, v51, v52\n" \
    "v_max3_u32 v19, v19, v53, v54\n" \
    "s_waitcnt vmcnt(4)\n" \
    "v_max3_u32 v19, v19, v55, v56\n" \
    "v_max3_u32 v19, v19, v57, v58\n" \
    "s_waitcnt vmcnt(3)\n" \
    "v_max3_u32 v19, v19, v59, v60\n" \
    "v_max3_u32 v19, v19, v61, v62\n" \
    "s_waitcnt vmcnt(2)\n" \
    "v_max3_u32 v19, v19, v63, v64\n" \
    "v_max3_u32 v19, v19, v65, v66\n" \
    "s_waitcnt vmcnt(1)\n" \
    "v_max3_u32 v19, v19, v67, v68\n" \
    "v_max3_u32 v19, v19, v69, v70\n" \
    "s_waitcnt vmcnt(0)\n" \
    "v_max3_u32 v19, v19, v71, v72\n" \
    "v_max3_u32 v19, v19, v73, v74\n" \
    "v_max_u32 v19, v19, v75\n" \
    "v_cmp_eq_u32 vcc, -1, v19\n" \
    "s_nop 0\n" \
    "s_cbranch_vccz .Lr_ctrB0_%=\n" \
    "s_add_u32 s52, s52, 1\n" \
    "s_cmp_lt_u32 s52, s53\n" \
    "s_cbranch_scc1 .Lr_cuB0_%=\n" \
    "s_mov_b32 s53, 0\n" \
    "s_branch .Lr_ctrB0_%=\n" \
    ".Lr_cbB0_%=:\n" \
    "s_add_u32 s73, s32, s25\n" \
    "s_mov_b32 s50, s49\n" \
    "s_mov_b32 s52, 0\n" \
    ".Lr_ccB0_%=:\n" \
    "buffer_load_dword v18, v2, s[8:11], 0 offen sc1\n" \
    "s_waitcnt vmcnt(0)\n" \
    "v_cmp_ge_u32 vcc, s50, v18\n" \
    "s_nop 0\n" \
    "s_and_b64 vcc, vcc, s[56:57]\n" \
    "s_cbranch_scc0 .Lr_cdB0_%=\n" \
    "s_sleep 2\n" \
    "s_add_u32 s52, s52, 1\n" \
    "s_cmp_lt_u32 s52, s53\n" \
    "s_cbranch_scc1 .Lr_ccB0_%=\n" \
    "s_mov_b32 s53, 0\n" \
    "s_branch .Lr_cdB0_%=\n" \
    ".Lr_cdB0_%=:\n" \
    "s_and_b32 s33, s49, %[brn]-1\n" \
    "s_mul_i32 s39, s33, 2621440\n" \
    "s_add_u32 s39, s39, s19\n" \
    "s_lshr_b32 s33, s39, 8\n" \
    "s_xor_b32 s33, s33, s27\n" \
    "s_and_b32 s33, s33, 0x2000\n" \
    "s_or_b32 s39, s39, s33\n" \
    "s_add_u32 s40, s39, 65536\n" \
    "s_waitcnt vmcnt(0)\n" \
    "buffer_load_dwordx4 v[224:227], v0, s[8:11], s39 offen offset:0 sc1\n" \
    "buffer_load_dwordx4 v[228:231], v0, s[8:11], s39 offen offset:1024 sc1\n" \
    "buffer_load_dwordx4 v[232:235], v0, s[8:11], s39 offen offset:2048 sc1\n" \
    "buffer_load_dwordx4 v[236:239], v0, s[8:11], s39 offen offset:3072 sc1\n" \
    "buffer_load_dwordx4 v[240:243], v0, s[8:11], s40 offen offset:0 sc1\n" \
    "buffer_load_dwordx4 v[244:247], v0, s[8:11], s40 offen offset:1024 sc1\n" \
    "buffer_load_dwordx4 v[248:251], v0, s[8:11], s40 offen offset:2048 sc1\n" \
    "buffer_load_dwordx4 v[252:255], v0, s[8:11], s40 offen offset:3072 sc1\n" \
    "s_waitcnt vmcnt(0)\n" \
    "s_branch .Lr_cbrB0_%=\n" \
    ".Lr_ctvB1_%=:\n" \
    "s_mov_b32 s52, 0\n" \
    "s_add_u32 s73, s32, s25\n" \
    ".Lr_ctwB1_%=:\n" \
    "buffer_load_dwordx2 v[16:17], v3, s[8:11], s51 offen sc1\n" \
    "s_waitcnt vmcnt(0)\n" \
    "v_cmp_ne_u32 vcc, s49, v17\n" \
    "s_nop 0\n" \
    "s_and_b64 vcc, vcc, s[56:57]\n" \
    "s_cbranch_scc0 .Lr_tvB1_%=\n" \
    "s_add_u32 s52, s52, 1\n" \
    "s_cmp_lt_u32 s52, s53\n" \
    "s_cbranch_scc1 .Lr_ctwB1_%=\n" \
    "s_mov_b32 s53, 0\n" \
    "v_mov_b32 v17, s49\n" \
    "v_mov_b32 v16, s55\n" \
    "s_branch .Lr_tvB1_%=\n" \
    ".Lr_cpB1_%=:\n" \
    "s_mov_b32 s52, 0\n" \
    "s_nop 7\n" \
    ".Lr_cqB1_%=:\n" \
    "buffer_load_dwordx4 v[36:39], v0, s[8:11], s36 offen sc1\n" \
    "buffer_load_dwordx4 v[40:43], v0, s[8:11], s37 offen sc1\n" \
    "buffer_load_dwordx4 v[32:35], v4, s[8:11], s35 offen sc1\n" \
    "buffer_load_dwordx4 v[24:27], v0, s[8:11], s35 offen sc1\n" \
    "buffer_load_dwordx4 v[28:31], v0, s[8:11], s35 offen offset:1024 sc1\n" \
    "s_waitcnt vmcnt(4)\n" \
    "v_max3_u32 v19, v36, v37, v38\n" \
    "s_waitcnt vmcnt(3)\n" \
    "v_max3_u32 v19, v19, v39, v40\n" \
    "v_max3_u32 v19, v19, v41, v42\n" \
    "v_max_u32 v19, v19, v43\n" \
    "s_waitcnt vmcnt(2)\n" \
    "v_mfma_f32_16x16x32_bf16 v[80:83], v[32:35], v[36:39], 0\n" \
    "v_max3_u32 v20, v32, v33, v34\n" \
    "v_mfma_f32_16x16x32_bf16 v[84:87], v[32:35], v[40:43], 0\n" \
    "v_max_u32 v20, v20, v35\n" \
    "s_waitcnt vmcnt(1)\n" \
    "v_smfmac_f32_16x16x64_bf16 v[80:83], v[24:27], v[224:231], v10\n" \
    "v_smfmac_f32_16x16x64_bf16 v[84:87], v[24:27], v[240:247], v10\n" \
    "v_max3_u32 v19, v19, v24, v25\n" \
    "v_max3_u32 v19, v19, v26, v27\n" \
    "s_waitcnt vmcnt(0)\n" \
    "v_smfmac_f32_16x16x64_bf16 v[80:83], v[28:31], v[232:239], v10\n" \
    "v_smfmac_f32_16x16x64_bf16 v[84:87], v[28:31], v[248:255], v10\n" \
    "v_max3_u32 v20, v20, v28, v29\n" \
    "v_max3_u32 v20, v20, v30, v31\n" \
    "v_max_u32 v19, v19, v20\n" \
    "v_cmp_eq_u32 vcc, -1, v19\n" \
    "s_nop 7\n" \
    "s_cbranch_vccz .Lr_cprB1_%=\n" \
    "s_add_u32 s52, s52, 1\n" \
    "s_cmp_lt_u32 s52, s53\n" \
    "s_cbranch_scc1 .Lr_cqB1_%=\n" \
    "s_mov_b32 s53, 0\n" \
    "s_branch .Lr_cprB1_%=\n" \
    ".Lr_ctB1_%=:\n" \
    "s_mov_b32 s52, 0\n" \
    "s_add_u32 s73, s32, s25\n" \
    ".Lr_cuB1_%=:\n" \
    "s_sleep 4\n" \
    "buffer_load_dwordx4 v[44:47], v0, s[8:11], s41 offen offset:0 sc1\n" \
    "buffer_load_dwordx4 v[48:51], v0, s[8:11], s41 offen offset:1024 sc1\n" \
    "buffer_load_dwordx4 v[52:55], v0, s[8:11], s41 offen offset:2048 sc1\n" \
    "buffer_load_dwordx4 v[56:59], v0, s[8:11], s41 offen offset:3072 sc1\n" \
    "buffer_load_dwordx4 v[60:63], v0, s[8:11], s42 offen offset:0 sc1\n" \
    "buffer_load_dwordx4 v[64:67], v0, s[8:11], s42 offen offset:1024 sc1\n" \
    "buffer_load_dwordx4 v[68:71], v0, s[8:11], s42 offen offset:2048 sc1\n" \
    "buffer_load_dwordx4 v[72:75], v0, s[8:11], s42 offen offset:3072 sc1\n" \
    "s_waitcnt vmcnt(7)\n" \
    "v_max3_u32 v19, v44, v45, v46\n" \
    "s_waitcnt vmcnt(6)\n" \
    "v_max3_u32 v19, v19, v47, v48\n" \
    "v_max3_u32 v19, v19, v49, v50\n" \
    "s_waitcnt vmcnt(5)\n" \
    "v_max3_u32 v19, v19, v51, v52\n" \
    "v_max3_u32 v19, v19, v53, v54\n" \
    "s_waitcnt vmcnt(4)\n" \
    "v_max3_u32 v19, v19, v55, v56\n" \
    "v_max3_u32 v19, v19, v57, v58\n" \
    "s_waitcnt vmcnt(3)\n" \
    "v_max3_u32 v19, v19, v59, v60\n" \
    "v_max3_u32 v19, v19, v61, v62\n" \
    "s_waitcnt vmcnt(2)\n" \
    "v_max3_u32 v19, v19, v63, v64\n" \
    "v_max3_u32 v19, v19, v65, v66\n" \
    "s_waitcnt vmcnt(1)\n" \
    "v_max3_u32 v19, v19, v67, v68\n" \
    "v_max3_u32 v19, v19, v69, v70\n" \
    "s_waitcnt vmcnt(0)\n" \
    "v_max3_u32 v19, v19, v71, v72\n" \
    "v_max3_u32 v19, v19, v73, v74\n" \
    "v_max_u32 v19, v19, v75\n" \
    "v_cmp_eq_u32 vcc, -1, v19\n" \
    "s_nop 0\n" \
    "s_cbranch_vccz .Lr_ctrB1_%=\n" \
    "s_add_u32 s52, s52, 1\n" \
    "s_cmp_lt_u32 s52, s53\n" \
    "s_cbranch_scc1 .Lr_cuB1_%=\n" \
    "s_mov_b32 s53, 0\n" \
    "s_branch .Lr_ctrB1_%=\n" \
    ".Lr_cbB1_%=:\n" \
    "s_add_u32 s73, s32, s25\n" \
    "s_mov_b32 s50, s49\n" \
    "s_mov_b32 s52, 0\n" \
    ".Lr_ccB1_%=:\n" \
    "buffer_load_dword v18, v2, s[8:11], 0 offen sc1\n" \
    "s_waitcnt vmcnt(0)\n" \
    "v_cmp_ge_u32 vcc, s50, v18\n" \
    "s_nop 0\n" \
    "s_and_b64 vcc, vcc, s[56:57]\n" \
    "s_cbranch_scc0 .Lr_cdB1_%=\n" \
    "s_sleep 2\n" \
    "s_add_u32 s52, s52, 1\n" \
    "s_cmp_lt_u32 s52, s53\n" \
    "s_cbranch_scc1 .Lr_ccB1_%=\n" \
    "s_mov_b32 s53, 0\n" \
    "s_branch .Lr_cdB1_%=\n" \
    ".Lr_cdB1_%=:\n" \
    "s_and_b32 s33, s49, %[brn]-1\n" \
    "s_mul_i32 s39, s33, 2621440\n" \
    "s_add_u32 s39, s39, s19\n" \
    "s_lshr_b32 s33, s39, 8\n" \
    "s_xor_b32 s33, s33, s27\n" \
    "s_and_b32 s33, s33, 0x2000\n" \
    "s_or_b32 s39, s39, s33\n" \
    "s_add_u32 s40, s39, 65536\n" \
    "s_waitcnt vmcnt(0)\n" \
    "buffer_load_dwordx4 v[192:195], v0, s[8:11], s39 offen offset:0 sc1\n" \
    "buffer_load_dwordx4 v[196:199], v0, s[8:11], s39 offen offset:1024 sc1\n" \
    "buffer_load_dwordx4 v[200:203], v0, s[8:11], s39 offen offset:2048 sc1\n" \
    "buffer_load_dwordx4 v[204:207], v0, s[8:11], s39 offen offset:3072 sc1\n" \
    "buffer_load_dwordx4 v[208:211], v0, s[8:11], s40 offen offset:0 sc1\n" \
    "buffer_load_dwordx4 v[212:215], v0, s[8:11], s40 offen offset:1024 sc1\n" \
    "buffer_load_dwordx4 v[216:219], v0, s[8:11], s40 offen offset:2048 sc1\n" \
    "buffer_load_dwordx4 v[220:223], v0, s[8:11], s40 offen offset:3072 sc1\n" \
    "s_waitcnt vmcnt(0)\n" \
    "s_branch .Lr_cbrB1_%=\n" \
    ".Lr_ctvC0_%=:\n" \
    "s_mov_b32 s52, 0\n" \
    "s_add_u32 s73, s32, s25\n" \
    ".Lr_ctwC0_%=:\n" \
    "buffer_load_dwordx2 v[16:17], v3, s[8:11], s51 offen sc1\n" \
    "s_waitcnt vmcnt(0)\n" \
    "v_cmp_ne_u32 vcc, s49, v17\n" \
    "s_nop 0\n" \
    "s_and_b64 vcc, vcc, s[56:57]\n" \
    "s_cbranch_scc0 .Lr_tvC0_%=\n" \
    "s_add_u32 s52, s52, 1\n" \
    "s_cmp_lt_u32 s52, s53\n" \
    "s_cbranch_scc1 .Lr_ctwC0_%=\n" \
    "s_mov_b32 s53, 0\n" \
    "v_mov_b32 v17, s49\n" \
    "v_mov_b32 v16, s55\n" \
    "s_branch .Lr_tvC0_%=\n" \
    ".Lr_cpC0_%=:\n" \
    "s_mov_b32 s52, 0\n" \
    "s_nop 7\n" \
    ".Lr_cqC0_%=:\n" \
    "buffer_load_dwordx4 v[36:39], v0, s[8:11], s36 offen sc1\n" \
    "buffer_load_dwordx4 v[40:43], v0, s[8:11], s37 offen sc1\n" \
    "buffer_load_dwordx4 v[32:35], v4, s[8:11], s35 offen sc1\n" \
    "buffer_load_dwordx4 v[24:27], v0, s[8:11], s35 offen sc1\n" \
    "buffer_load_dwordx4 v[28:31], v0, s[8:11], s35 offen offset:1024 sc1\n" \
    "s_waitcnt vmcnt(4)\n" \
    "v_max3_u32 v19, v36, v37, v38\n" \
    "s_waitcnt vmcnt(3)\n" \
    "v_max3_u32 v19, v19, v39, v40\n" \
    "v_max3_u32 v19, v19, v41, v42\n" \
    "v_max_u32 v19, v19, v43\n" \
    "s_waitcnt vmcnt(2)\n" \
    "v_mfma_f32_16x16x32_bf16 v[80:83], v[32:35], v[36:39], 0\n" \
    "v_max3_u32 v20, v32, v33, v34\n" \
    "v_mfma_f32_16x16x32_bf16 v[84:87], v[32:35], v[40:43], 0\n" \
    "v_max_u32 v20, v20, v35\n" \
    "s_waitcnt vmcnt(1)\n" \
    "v_smfmac_f32_16x16x64_bf16 v[80:83], v[24:27], v[192:199], v10\n" \
    "v_smfmac_f32_16x16x64_bf16 v[84:87], v[24:27], v[208:215], v10\n" \
    "v_max3_u32 v19, v19, v24, v25\n" \
    "v_max3_u32 v19, v19, v26, v27\n" \
    "s_waitcnt vmcnt(0)\n" \
    "v_smfmac_f32_16x16x64_bf16 v[80:83], v[28:31], v[200:207], v10\n" \
    "v_smfmac_f32_16x16x64_bf16 v[84:87], v[28:31], v[216:223], v10\n" \
    "v_max3_u32 v20, v20, v28, v29\n" \
    "v_max3_u32 v20, v20, v30, v31\n" \
    "v_max_u32 v19, v19, v20\n" \
    "v_cmp_eq_u32 vcc, -1, v19\n" \
    "s_nop 7\n" \
    "s_cbranch_vccz .Lr_cprC0_%=\n" \
    "s_add_u32 s52, s52, 1\n" \
    "s_cmp_lt_u32 s52, s53\n" \
    "s_cbranch_scc1 .Lr_cqC0_%=\n" \
    "s_mov_b32 s53, 0\n" \
    "s_branch .Lr_cprC0_%=\n" \
    ".Lr_ctC0_%=:\n" \
    "s_mov_b32 s52, 0\n" \
    "s_add_u32 s73, s32, s25\n" \
    ".Lr_cuC0_%=:\n" \
    "s_sleep 4\n" \
    "buffer_load_dwordx4 v[44:47], v0, s[8:11], s41 offen offset:0 sc1\n" \
    "buffer_load_dwordx4 v[48:51], v0, s[8:11], s41 offen offset:1024 sc1\n" \
    "buffer_load_dwordx4 v[52:55], v0, s[8:11], s41 offen offset:2048 sc1\n" \
    "buffer_load_dwordx4 v[56:59], v0, s[8:11], s41 offen offset:3072 sc1\n" \
    "buffer_load_dwordx4 v[60:63], v0, s[8:11], s42 offen offset:0 sc1\n" \
    "buffer_load_dwordx4 v[64:67], v0, s[8:11], s42 offen offset:1024 sc1\n" \
    "buffer_load_dwordx4 v[68:71], v0, s[8:11], s42 offen offset:2048 sc1\n" \
    "buffer_load_dwordx4 v[72:75], v0, s[8:11], s42 offen offset:3072 sc1\n" \
    "s_waitcnt vmcnt(7)\n" \
    "v_max3_u32 v19, v44, v45, v46\n" \
    "s_waitcnt vmcnt(6)\n" \
    "v_max3_u32 v19, v19, v47, v48\n" \
    "v_max3_u32 v19, v19, v49, v50\n" \
    "s_waitcnt vmcnt(5)\n" \
    "v_max3_u32 v19, v19, v51, v52\n" \
    "v_max3_u32 v19, v19, v53, v54\n" \
    "s_waitcnt vmcnt(4)\n" \
    "v_max3_u32 v19, v19, v55, v56\n" \
    "v_max3_u32 v19, v19, v57, v58\n" \
    "s_waitcnt vmcnt(3)\n" \
    "v_max3_u32 v19, v19, v59, v60\n" \
    "v_max3_u32 v19, v19, v61, v62\n" \
    "s_waitcnt vmcnt(2)\n" \
    "v_max3_u32 v19, v19, v63, v64\n" \
    "v_max3_u32 v19, v19, v65, v66\n" \
    "s_waitcnt vmcnt(1)\n" \
    "v_max3_u32 v19, v19, v67, v68\n" \
    "v_max3_u32 v19, v19, v69, v70\n" \
    "s_waitcnt vmcnt(0)\n" \
    "v_max3_u32 v19, v19, v71, v72\n" \
    "v_max3_u32 v19, v19, v73, v74\n" \
    "v_max_u32 v19, v19, v75\n" \
    "v_cmp_eq_u32 vcc, -1, v19\n" \
    "s_nop 0\n" \
    "s_cbranch_vccz .Lr_ctrC0_%=\n" \
    "s_add_u32 s52, s52, 1\n" \
    "s_cmp_lt_u32 s52, s53\n" \
    "s_cbranch_scc1 .Lr_cuC0_%=\n" \
    "s_mov_b32 s53, 0\n" \
    "s_branch .Lr_ctrC0_%=\n" \
    ".Lr_cbC0_%=:\n" \
    "s_add_u32 s73, s32, s25\n" \
    "s_mov_b32 s50, s49\n" \
    "s_mov_b32 s52, 0\n" \
    ".Lr_ccC0_%=:\n" \
    "buffer_load_dword v18, v2, s[8:11], 0 offen sc1\n" \
    "s_waitcnt vmcnt(0)\n" \
    "v_cmp_ge_u32 vcc, s50, v18\n" \
    "s_nop 0\n" \
    "s_and_b64 vcc, vcc, s[56:57]\n" \
    "s_cbranch_scc0 .Lr_cdC0_%=\n" \
    "s_sleep 2\n" \
    "s_add_u32 s52, s52, 1\n" \
    "s_cmp_lt_u32 s52, s53\n" \
    "s_cbranch_scc1 .Lr_ccC0_%=\n" \
    "s_mov_b32 s53, 0\n" \
    "s_branch .Lr_cdC0_%=\n" \
    ".Lr_cdC0_%=:\n" \
    "s_and_b32 s33, s49, %[brn]-1\n" \
    "s_mul_i32 s39, s33, 2621440\n" \
    "s_add_u32 s39, s39, s19\n" \
    "s_lshr_b32 s33, s39, 8\n" \
    "s_xor_b32 s33, s33, s27\n" \
    "s_and_b32 s33, s33, 0x2000\n" \
    "s_or_b32 s39, s39, s33\n" \
    "s_add_u32 s40, s39, 65536\n" \
    "s_waitcnt vmcnt(0)\n" \
    "buffer_load_dwordx4 v[224:227], v0, s[8:11], s39 offen offset:0 sc1\n" \
    "buffer_load_dwordx4 v[228:231], v0, s[8:11], s39 offen offset:1024 sc1\n" \
    "buffer_load_dwordx4 v[232:235], v0, s[8:11], s39 offen offset:2048 sc1\n" \
    "buffer_load_dwordx4 v[236:239], v0, s[8:11], s39 offen offset:3072 sc1\n" \
    "buffer_load_dwordx4 v[240:243], v0, s[8:11], s40 offen offset:0 sc1\n" \
    "buffer_load_dwordx4 v[244:247], v0, s[8:11], s40 offen offset:1024 sc1\n" \
    "buffer_load_dwordx4 v[248:251], v0, s[8:11], s40 offen offset:2048 sc1\n" \
    "buffer_load_dwordx4 v[252:255], v0, s[8:11], s40 offen offset:3072 sc1\n" \
    "s_waitcnt vmcnt(0)\n" \
    "s_branch .Lr_cbrC0_%=\n" \
    ".Lr_ctvC1_%=:\n" \
    "s_mov_b32 s52, 0\n" \
    "s_add_u32 s73, s32, s25\n" \
    ".Lr_ctwC1_%=:\n" \
    "buffer_load_dwordx2 v[16:17], v3, s[8:11], s51 offen sc1\n" \
    "s_waitcnt vmcnt(0)\n" \
    "v_cmp_ne_u32 vcc, s49, v17\n" \
    "s_nop 0\n" \
    "s_and_b64 vcc, vcc, s[56:57]\n" \
    "s_cbranch_scc0 .Lr_tvC1_%=\n" \
    "s_add_u32 s52, s52, 1\n" \
    "s_cmp_lt_u32 s52, s53\n" \
    "s_cbranch_scc1 .Lr_ctwC1_%=\n" \
    "s_mov_b32 s53, 0\n" \
    "v_mov_b32 v17, s49\n" \
    "v_mov_b32 v16, s55\n" \
    "s_branch .Lr_tvC1_%=\n" \
    ".Lr_cpC1_%=:\n" \
    "s_mov_b32 s52, 0\n" \
    "s_nop 7\n" \
    ".Lr_cqC1_%=:\n" \
    "buffer_load_dwordx4 v[36:39], v0, s[8:11], s36 offen sc1\n" \
    "buffer_load_dwordx4 v[40:43], v0, s[8:11], s37 offen sc1\n" \
    "buffer_load_dwordx4 v[32:35], v4, s[8:11], s35 offen sc1\n" \
    "buffer_load_dwordx4 v[24:27], v0, s[8:11], s35 offen sc1\n" \
    "buffer_load_dwordx4 v[28:31], v0, s[8:11], s35 offen offset:1024 sc1\n" \
    "s_waitcnt vmcnt(4)\n" \
    "v_max3_u32 v19, v36, v37, v38\n" \
    "s_waitcnt vmcnt(3)\n" \
    "v_max3_u32 v19, v19, v39, v40\n" \
    "v_max3_u32 v19, v19, v41, v42\n" \
    "v_max_u32 v19, v19, v43\n" \
    "s_waitcnt vmcnt(2)\n" \
    "v_mfma_f32_16x16x32_bf16 v[80:83], v[32:35], v[36:39], 0\n" \
    "v_max3_u32 v20, v32, v33, v34\n" \
    "v_mfma_f32_16x16x32_bf16 v[84:87], v[32:35], v[40:43], 0\n" \
    "v_max_u32 v20, v20, v35\n" \
    "s_waitcnt vmcnt(1)\n" \
    "v_smfmac_f32_16x16x64_bf16 v[80:83], v[24:27], v[224:231], v10\n" \
    "v_smfmac_f32_16x16x64_bf16 v[84:87], v[24:27], v[240:247], v10\n" \
    "v_max3_u32 v19, v19, v24, v25\n" \
    "v_max3_u32 v19, v19, v26, v27\n" \
    "s_waitcnt vmcnt(0)\n" \
    "v_smfmac_f32_16x16x64_bf16 v[80:83], v[28:31], v[232:239], v10\n" \
    "v_smfmac_f32_16x16x64_bf16 v[84:87], v[28:31], v[248:255], v10\n" \
    "v_max3_u32 v20, v20, v28, v29\n" \
    "v_max3_u32 v20, v20, v30, v31\n" \
    "v_max_u32 v19, v19, v20\n" \
    "v_cmp_eq_u32 vcc, -1, v19\n" \
    "s_nop 7\n" \
    "s_cbranch_vccz .Lr_cprC1_%=\n" \
    "s_add_u32 s52, s52, 1\n" \
    "s_cmp_lt_u32 s52, s53\n" \
    "s_cbranch_scc1 .Lr_cqC1_%=\n" \
    "s_mov_b32 s53, 0\n" \
    "s_branch .Lr_cprC1_%=\n" \
    ".Lr_ctC1_%=:\n" \
    "s_mov_b32 s52, 0\n" \
    "s_add_u32 s73, s32, s25\n" \
    ".Lr_cuC1_%=:\n" \
    "s_sleep 4\n" \
    "buffer_load_dwordx4 v[44:47], v0, s[8:11], s41 offen offset:0 sc1\n" \
    "buffer_load_dwordx4 v[48:51], v0, s[8:11], s41 offen offset:1024 sc1\n" \
    "buffer_load_dwordx4 v[52:55], v0, s[8:11], s41 offen offset:2048 sc1\n" \
    "buffer_load_dwordx4 v[56:59], v0, s[8:11], s41 offen offset:3072 sc1\n" \
    "buffer_load_dwordx4 v[60:63], v0, s[8:11], s42 offen offset:0 sc1\n" \
    "buffer_load_dwordx4 v[64:67], v0, s[8:11], s42 offen offset:1024 sc1\n" \
    "buffer_load_dwordx4 v[68:71], v0, s[8:11], s42 offen offset:2048 sc1\n" \
    "buffer_load_dwordx4 v[72:75], v0, s[8:11], s42 offen offset:3072 sc1\n" \
    "s_waitcnt vmcnt(7)\n" \
    "v_max3_u32 v19, v44, v45, v46\n" \
    "s_waitcnt vmcnt(6)\n" \
    "v_max3_u32 v19, v19, v47, v48\n" \
    "v_max3_u32 v19, v19, v49, v50\n" \
    "s_waitcnt vmcnt(5)\n" \
    "v_max3_u32 v19, v19, v51, v52\n" \
    "v_max3_u32 v19, v19, v53, v54\n" \
    "s_waitcnt vmcnt(4)\n" \
    "v_max3_u32 v19, v19, v55, v56\n" \
    "v_max3_u32 v19, v19, v57, v58\n" \
    "s_waitcnt vmcnt(3)\n" \
    "v_max3_u32 v19, v19, v59, v60\n" \
    "v_max3_u32 v19, v19, v61, v62\n" \
    "s_waitcnt vmcnt(2)\n" \
    "v_max3_u32 v19, v19, v63, v64\n" \
    "v_max3_u32 v19, v19, v65, v66\n" \
    "s_waitcnt vmcnt(1)\n" \
    "v_max3_u32 v19, v19, v67, v68\n" \
    "v_max3_u32 v19, v19, v69, v70\n" \
    "s_waitcnt vmcnt(0)\n" \
    "v_max3_u32 v19, v19, v71, v72\n" \
    "v_max3_u32 v19, v19, v73, v74\n" \
    "v_max_u32 v19, v19, v75\n" \
    "v_cmp_eq_u32 vcc, -1, v19\n" \
    "s_nop 0\n" \
    "s_cbranch_vccz .Lr_ctrC1_%=\n" \
    "s_add_u32 s52, s52, 1\n" \
    "s_cmp_lt_u32 s52, s53\n" \
    "s_cbranch_scc1 .Lr_cuC1_%=\n" \
    "s_mov_b32 s53, 0\n" \
    "s_branch .Lr_ctrC1_%=\n" \
    ".Lr_cbC1_%=:\n" \
    "s_add_u32 s73, s32, s25\n" \
    "s_mov_b32 s50, s49\n" \
    "s_mov_b32 s52, 0\n" \
    ".Lr_ccC1_%=:\n" \
    "buffer_load_dword v18, v2, s[8:11], 0 offen sc1\n" \
    "s_waitcnt vmcnt(0)\n" \
    "v_cmp_ge_u32 vcc, s50, v18\n" \
    "s_nop 0\n" \
    "s_and_b64 vcc, vcc, s[56:57]\n" \
    "s_cbranch_scc0 .Lr_cdC1_%=\n" \
    "s_sleep 2\n" \
    "s_add_u32 s52, s52, 1\n" \
    "s_cmp_lt_u32 s52, s53\n" \
    "s_cbranch_scc1 .Lr_ccC1_%=\n" \
    "s_mov_b32 s53, 0\n" \
    "s_branch .Lr_cdC1_%=\n" \
    ".Lr_cdC1_%=:\n" \
    "s_and_b32 s33, s49, %[brn]-1\n" \
    "s_mul_i32 s39, s33, 2621440\n" \
    "s_add_u32 s39, s39, s19\n" \
    "s_lshr_b32 s33, s39, 8\n" \
    "s_xor_b32 s33, s33, s27\n" \
    "s_and_b32 s33, s33, 0x2000\n" \
    "s_or_b32 s39, s39, s33\n" \
    "s_add_u32 s40, s39, 65536\n" \
    "s_waitcnt vmcnt(0)\n" \
    "buffer_load_dwordx4 v[192:195], v0, s[8:11], s39 offen offset:0 sc1\n" \
    "buffer_load_dwordx4 v[196:199], v0, s[8:11], s39 offen offset:1024 sc1\n" \
    "buffer_load_dwordx4 v[200:203], v0, s[8:11], s39 offen offset:2048 sc1\n" \
    "buffer_load_dwordx4 v[204:207], v0, s[8:11], s39 offen offset:3072 sc1\n" \
    "buffer_load_dwordx4 v[208:211], v0, s[8:11], s40 offen offset:0 sc1\n" \
    "buffer_load_dwordx4 v[212:215], v0, s[8:11], s40 offen offset:1024 sc1\n" \
    "buffer_load_dwordx4 v[216:219], v0, s[8:11], s40 offen offset:2048 sc1\n" \
    "buffer_load_dwordx4 v[220:223], v0, s[8:11], s40 offen offset:3072 sc1\n" \
    "s_waitcnt vmcnt(0)\n" \
    "s_branch .Lr_cbrC1_%=\n" \
    ""
#define G_ASM \
    "s_mov_b32 s32, s24\n" \
    "s_mov_b32 s33, s27\n" \
    "s_mov_b32 s34, -1\n" \
    "s_mov_b32 s35, -1\n" \
    "s_mov_b32 s37, 0\n" \
    "s_mov_b32 s45, 0x80000000\n" \
    "s_mov_b32 s38, 0x80000000\n" \
    "s_mov_b32 s41, 0\n" \
    "s_mov_b32 s49, 256\n" \
    "s_mov_b32 s43, 0\n" \
    "s_mov_b32 s46, 0\n" \
    "s_mov_b32 s69, 0x80000000\n" \
    "v_mov_b32 v165, 81920\n" \
    "v_mov_b32 v168, 1\n" \
    "v_mov_b32 v169, 1\n" \
    "v_mov_b32 v170, 1\n" \
    "v_mov_b32 v171, 1\n" \
    "s_cmp_eq_u32 s16, 0\n" \
    "s_cbranch_scc1 .Lg_lS_%=\n" \
    "s_cmp_lt_u32 s16, 4\n" \
    "s_cbranch_scc1 .Lg_lO_%=\n" \
    "s_barrier\n" \
    "s_add_u32 s41, s41, 20480\n" \
    ".Lg_lY_%=:\n" \
    "s_barrier\n" \
    "ds_read_b32 v167, v165\n" \
    ".Lg_nt_%=:\n" \
    "v_add_u32 v164, s41, v160\n" \
    "s_setprio 0\n" \
    "s_waitcnt vmcnt(63)\n" \
    "buffer_store_dwordx4 v[168:171], v161, s[8:11], s38 offen sc1\n" \
    "s_mov_b32 s38, 0x80000000\n" \
    "s_waitcnt lgkmcnt(0)\n" \
    "v_readfirstlane_b32 s44, v167\n" \
    "s_nop 0\n" \
    "s_and_b32 s44, s44, 255\n" \
    "s_cmp_lt_u32 s32, s25\n" \
    "s_cselect_b32 s36, s32, -1\n" \
    "s_add_u32 s39, s33, 128\n" \
    "s_cmp_lt_u32 s33, s44\n" \
    "s_cselect_b32 s36, s39, s36\n" \
    "s_addc_u32 s33, s33, 0\n" \
    "s_cmp_lt_u32 s36, 128\n" \
    "s_addc_u32 s32, s32, 0\n" \
    "s_cmp_eq_u32 s32, s29\n" \
    "s_cselect_b32 s32, s30, s32\n" \
    "s_add_u32 s48, s41, 20480\n" \
    "s_cmp_ge_u32 s48, 81920\n" \
    "s_cselect_b32 s40, 81920, 0\n" \
    "s_sub_u32 s48, s48, s40\n" \
    "s_add_u32 m0, s48, s23\n" \
    "s_mul_i32 s40, s36, 20480\n" \
    "s_add_u32 s40, s40, s22\n" \
    "s_cmp_eq_u32 s36, -1\n" \
    "s_cselect_b32 s40, 0x80000000, s40\n" \
    "buffer_load_dwordx4 v160, s[8:11], s40 offen offset:0 sc1 lds\n" \
    "v_cvt_pk_bf16_f32 v172, v208, v209\n" \
    "v_cvt_pk_bf16_f32 v173, v210, v211\n" \
    "v_cvt_pk_bf16_f32 v174, v212, v213\n" \
    "v_cvt_pk_bf16_f32 v175, v214, v215\n" \
    "buffer_store_dwordx4 v[172:175], v160, s[8:11], s45 offen sc1\n" \
    "s_cmp_gt_u32 s37, 127\n" \
    "s_cselect_b32 s38, s21, s20\n" \
    "s_bitcmp1_b32 s37, 0\n" \
    "s_cselect_b32 s38, s38, 0x80000000\n" \
    "s_bfe_u32 s39, s37, 0x60001\n" \
    "s_add_u32 s39, s39, 1\n" \
    "v_mov_b32 v168, s39\n" \
    "s_add_u32 s41, s41, 20480\n" \
    "s_cmp_eq_u32 s41, 81920\n" \
    "s_cselect_b32 s41, 0, s41\n" \
    "s_add_u32 s49, s49, 256\n" \
    "v_xor_b32 v165, 4, v165\n" \
    "s_mov_b32 s37, s34\n" \
    "s_mov_b32 s34, s36\n" \
    "s_cmp_eq_u32 s37, -1\n" \
    "s_cbranch_scc1 .Lg_eY_%=\n" \
    "ds_read_b128 v[176:179], v164 offset:0\n" \
    "ds_read_b128 v[180:183], v164 offset:1024\n" \
    "ds_read_b128 v[184:187], v164 offset:2048\n" \
    "ds_read_b128 v[188:191], v164 offset:3072\n" \
    "ds_read_b128 v[192:195], v164 offset:4096\n" \
    "s_lshr_b32 s39, s37, 1\n" \
    "s_cmp_gt_u32 s39, 63\n" \
    "s_cselect_b32 s40, %[rmask], 63\n" \
    "s_and_b32 s40, s40, s39\n" \
    "s_mul_i32 s45, s40, 1572864\n" \
    "s_bitcmp1_b32 s37, 0\n" \
    "s_cselect_b32 s40, s18, s17\n" \
    "s_add_u32 s45, s45, s40\n" \
    "s_lshr_b32 s40, s45, 8\n" \
    "s_xor_b32 s40, s40, s19\n" \
    "s_and_b32 s40, s40, 0x2000\n" \
    "s_or_b32 s45, s45, s40\n" \
    "s_mov_b32 s46, 0\n" \
    "s_waitcnt lgkmcnt(4)\n" \
    "v_mfma_f32_16x16x32_bf16 v[208:211], v[0:3], v[176:179], 0\n" \
    "ds_read_b128 v[196:199], v164 offset:5120\n" \
    "v_mfma_f32_16x16x32_bf16 v[212:215], v[80:83], v[176:179], 0\n" \
    "s_waitcnt lgkmcnt(4)\n" \
    "v_mfma_f32_16x16x32_bf16 v[208:211], v[4:7], v[180:183], v[208:211]\n" \
    "ds_read_b128 v[200:203], v164 offset:6144\n" \
    "v_mfma_f32_16x16x32_bf16 v[212:215], v[84:87], v[180:183], v[212:215]\n" \
    "s_waitcnt lgkmcnt(4)\n" \
    "v_mfma_f32_16x16x32_bf16 v[208:211], v[8:11], v[184:187], v[208:211]\n" \
    "ds_read_b128 v[204:207], v164 offset:7168\n" \
    "v_mfma_f32_16x16x32_bf16 v[212:215], v[88:91], v[184:187], v[212:215]\n" \
    "s_waitcnt lgkmcnt(4)\n" \
    "v_mfma_f32_16x16x32_bf16 v[208:211], v[12:15], v[188:191], v[208:211]\n" \
    "ds_read_b128 v[176:179], v164 offset:8192\n" \
    "v_mfma_f32_16x16x32_bf16 v[212:215], v[92:95], v[188:191], v[212:215]\n" \
    "s_waitcnt lgkmcnt(4)\n" \
    "v_mfma_f32_16x16x32_bf16 v[208:211], v[16:19], v[192:195], v[208:211]\n" \
    "ds_read_b128 v[180:183], v164 offset:9216\n" \
    "v_mfma_f32_16x16x32_bf16 v[212:215], v[96:99], v[192:195], v[212:215]\n" \
    "s_waitcnt lgkmcnt(4)\n" \
    "v_mfma_f32_16x16x32_bf16 v[208:211], v[20:23], v[196:199], v[208:211]\n" \
    "ds_read_b128 v[184:187], v164 offset:10240\n" \
    "v_mfma_f32_16x16x32_bf16 v[212:215], v[100:103], v[196:199], v[212:215]\n" \
    "s_waitcnt lgkmcnt(4)\n" \
    "v_mfma_f32_16x16x32_bf16 v[208:211], v[24:27], v[200:203], v[208:211]\n" \
    "ds_read_b128 v[188:191], v164 offset:11264\n" \
    "v_mfma_f32_16x16x32_bf16 v[212:215], v[104:107], v[200:203], v[212:215]\n" \
    "s_waitcnt lgkmcnt(4)\n" \
    "v_mfma_f32_16x16x32_bf16 v[208:211], v[28:31], v[204:207], v[208:211]\n" \
    "ds_read_b128 v[192:195], v164 offset:12288\n" \
    "v_mfma_f32_16x16x32_bf16 v[212:215], v[108:111], v[204:207], v[212:215]\n" \
    "s_waitcnt lgkmcnt(4)\n" \
    "v_mfma_f32_16x16x32_bf16 v[208:211], v[32:35], v[176:179], v[208:211]\n" \
    "ds_read_b128 v[196:199], v164 offset:13312\n" \
    "v_mfma_f32_16x16x32_bf16 v[212:215], v[112:115], v[176:179], v[212:215]\n" \
    "s_waitcnt lgkmcnt(4)\n" \
    "v_mfma_f32_16x16x32_bf16 v[208:211], v[36:39], v[180:183], v[208:211]\n" \
    "ds_read_b128 v[200:203], v164 offset:14336\n" \
    "v_mfma_f32_16x16x32_bf16 v[212:215], v[116:119], v[180:183], v[212:215]\n" \
    "s_waitcnt lgkmcnt(4)\n" \
    "v_mfma_f32_16x16x32_bf16 v[208:211], v[40:43], v[184:187], v[208:211]\n" \
    "ds_read_b128 v[204:207], v164 offset:15360\n" \
    "v_mfma_f32_16x16x32_bf16 v[212:215], v[120:123], v[184:187], v[212:215]\n" \
    "s_waitcnt lgkmcnt(4)\n" \
    "v_mfma_f32_16x16x32_bf16 v[208:211], v[44:47], v[188:191], v[208:211]\n" \
    "ds_read_b128 v[176:179], v164 offset:16384\n" \
    "v_mfma_f32_16x16x32_bf16 v[212:215], v[124:127], v[188:191], v[212:215]\n" \
    "s_waitcnt lgkmcnt(4)\n" \
    "v_mfma_f32_16x16x32_bf16 v[208:211], v[48:51], v[192:195], v[208:211]\n" \
    "ds_read_b128 v[180:183], v164 offset:17408\n" \
    "v_mfma_f32_16x16x32_bf16 v[212:215], v[128:131], v[192:195], v[212:215]\n" \
    "s_waitcnt lgkmcnt(4)\n" \
    "v_mfma_f32_16x16x32_bf16 v[208:211], v[52:55], v[196:199], v[208:211]\n" \
    "ds_read_b128 v[184:187], v164 offset:18432\n" \
    "v_mfma_f32_16x16x32_bf16 v[212:215], v[132:135], v[196:199], v[212:215]\n" \
    "s_waitcnt lgkmcnt(4)\n" \
    "v_mfma_f32_16x16x32_bf16 v[208:211], v[56:59], v[200:203], v[208:211]\n" \
    "ds_read_b128 v[188:191], v164 offset:19456\n" \
    "v_mfma_f32_16x16x32_bf16 v[212:215], v[136:139], v[200:203], v[212:215]\n" \
    "s_setprio 1\n" \
    "s_waitcnt vmcnt(1)\n" \
    "s_waitcnt lgkmcnt(4)\n" \
    "v_mfma_f32_16x16x32_bf16 v[208:211], v[60:63], v[204:207], v[208:211]\n" \
    "v_mfma_f32_16x16x32_bf16 v[212:215], v[140:143], v[204:207], v[212:215]\n" \
    "s_barrier\n" \
    "s_waitcnt lgkmcnt(3)\n" \
    "v_mfma_f32_16x16x32_bf16 v[208:211], v[64:67], v[176:179], v[208:211]\n" \
    "v_mfma_f32_16x16x32_bf16 v[212:215], v[144:147], v[176:179], v[212:215]\n" \
    "ds_read_b32 v167, v165\n" \
    "s_waitcnt lgkmcnt(3)\n" \
    "v_mfma_f32_16x16x32_bf16 v[208:211], v[68:71], v[180:183], v[208:211]\n" \
    "v_mfma_f32_16x16x32_bf16 v[212:215], v[148:151], v[180:183], v[212:215]\n" \
    "s_waitcnt lgkmcnt(2)\n" \
    "v_mfma_f32_16x16x32_bf16 v[208:211], v[72:75], v[184:187], v[208:211]\n" \
    "v_mfma_f32_16x16x32_bf16 v[212:215], v[152:155], v[184:187], v[212:215]\n" \
    "s_waitcnt lgkmcnt(1)\n" \
    "v_mfma_f32_16x16x32_bf16 v[208:211], v[76:79], v[188:191], v[208:211]\n" \
    "v_mfma_f32_16x16x32_bf16 v[212:215], v[156:159], v[188:191], v[212:215]\n" \
    "s_waitcnt vmcnt(0)\n" \
    "s_branch .Lg_nt_%=\n" \
    ".Lg_eY_%=:\n" \
    "s_waitcnt vmcnt(0)\n" \
    "buffer_store_dwordx4 v[168:171], v161, s[8:11], s38 offen sc1\n" \
    "s_mov_b32 s38, 0x80000000\n" \
    "s_mov_b32 s45, 0x80000000\n" \
    "s_mov_b32 s37, 0\n" \
    "s_add_u32 s40, s32, s33\n" \
    "s_cmp_eq_u32 s34, -1\n" \
    "s_cselect_b32 s40, s40, 0\n" \
    "s_cmp_eq_u32 s40, s26\n" \
    "s_cbranch_scc1 .Lg_end_%=\n" \
    "s_add_u32 s46, s46, 1\n" \
    "s_cmp_lt_u32 s46, 0x20000\n" \
    "s_cbranch_scc1 .Lg_lY_%=\n" \
    "s_branch .Lg_end_%=\n" \
    ".Lg_lO_%=:\n" \
    "v_add_u32 v164, s41, v160\n" \
    "s_cmp_eq_u32 s34, -1\n" \
    "s_barrier\n" \
    "s_cbranch_scc1 .Lg_eO_%=\n" \
    "ds_read_b128 v[176:179], v164 offset:0\n" \
    "ds_read_b128 v[180:183], v164 offset:1024\n" \
    "ds_read_b128 v[184:187], v164 offset:2048\n" \
    "ds_read_b128 v[188:191], v164 offset:3072\n" \
    "ds_read_b128 v[192:195], v164 offset:4096\n" \
    "s_add_u32 s48, s41, 40960\n" \
    "s_cmp_ge_u32 s48, 81920\n" \
    "s_cselect_b32 s40, 81920, 0\n" \
    "s_sub_u32 s48, s48, s40\n" \
    "s_add_u32 m0, s48, s23\n" \
    "s_waitcnt lgkmcnt(4)\n" \
    "v_mfma_f32_16x16x32_bf16 v[208:211], v[0:3], v[176:179], 0\n" \
    "ds_read_b128 v[196:199], v164 offset:5120\n" \
    "v_mfma_f32_16x16x32_bf16 v[212:215], v[80:83], v[176:179], 0\n" \
    "s_waitcnt lgkmcnt(4)\n" \
    "v_mfma_f32_16x16x32_bf16 v[208:211], v[4:7], v[180:183], v[208:211]\n" \
    "ds_read_b128 v[200:203], v164 offset:6144\n" \
    "v_mfma_f32_16x16x32_bf16 v[212:215], v[84:87], v[180:183], v[212:215]\n" \
    "s_lshr_b32 s39, s34, 1\n" \
    "s_waitcnt lgkmcnt(4)\n" \
    "v_mfma_f32_16x16x32_bf16 v[208:211], v[8:11], v[184:187], v[208:211]\n" \
    "ds_read_b128 v[204:207], v164 offset:7168\n" \
    "v_mfma_f32_16x16x32_bf16 v[212:215], v[88:91], v[184:187], v[212:215]\n" \
    "s_cmp_gt_u32 s39, 63\n" \
    "s_cselect_b32 s40, %[rmask], 63\n" \
    "s_waitcnt lgkmcnt(4)\n" \
    "v_mfma_f32_16x16x32_bf16 v[208:211], v[12:15], v[188:191], v[208:211]\n" \
    "ds_read_b128 v[176:179], v164 offset:8192\n" \
    "v_mfma_f32_16x16x32_bf16 v[212:215], v[92:95], v[188:191], v[212:215]\n" \
    "s_and_b32 s40, s40, s39\n" \
    "s_waitcnt lgkmcnt(4)\n" \
    "v_mfma_f32_16x16x32_bf16 v[208:211], v[16:19], v[192:195], v[208:211]\n" \
    "ds_read_b128 v[180:183], v164 offset:9216\n" \
    "v_mfma_f32_16x16x32_bf16 v[212:215], v[96:99], v[192:195], v[212:215]\n" \
    "s_mul_i32 s45, s40, 1572864\n" \
    "s_waitcnt lgkmcnt(4)\n" \
    "v_mfma_f32_16x16x32_bf16 v[208:211], v[20:23], v[196:199], v[208:211]\n" \
    "ds_read_b128 v[184:187], v164 offset:10240\n" \
    "v_mfma_f32_16x16x32_bf16 v[212:215], v[100:103], v[196:199], v[212:215]\n" \
    "s_bitcmp1_b32 s34, 0\n" \
    "s_cselect_b32 s40, s18, s17\n" \
    "s_waitcnt lgkmcnt(4)\n" \
    "v_mfma_f32_16x16x32_bf16 v[208:211], v[24:27], v[200:203], v[208:211]\n" \
    "ds_read_b128 v[188:191], v164 offset:11264\n" \
    "v_mfma_f32_16x16x32_bf16 v[212:215], v[104:107], v[200:203], v[212:215]\n" \
    "s_add_u32 s45, s45, s40\n" \
    "s_waitcnt lgkmcnt(4)\n" \
    "v_mfma_f32_16x16x32_bf16 v[208:211], v[28:31], v[204:207], v[208:211]\n" \
    "ds_read_b128 v[192:195], v164 offset:12288\n" \
    "v_mfma_f32_16x16x32_bf16 v[212:215], v[108:111], v[204:207], v[212:215]\n" \
    "s_lshr_b32 s40, s45, 8\n" \
    "s_waitcnt lgkmcnt(4)\n" \
    "v_mfma_f32_16x16x32_bf16 v[208:211], v[32:35], v[176:179], v[208:211]\n" \
    "ds_read_b128 v[196:199], v164 offset:13312\n" \
    "v_mfma_f32_16x16x32_bf16 v[212:215], v[112:115], v[176:179], v[212:215]\n" \
    "s_xor_b32 s40, s40, s19\n" \
    "s_waitcnt lgkmcnt(4)\n" \
    "v_mfma_f32_16x16x32_bf16 v[208:211], v[36:39], v[180:183], v[208:211]\n" \
    "ds_read_b128 v[200:203], v164 offset:14336\n" \
    "v_mfma_f32_16x16x32_bf16 v[212:215], v[116:119], v[180:183], v[212:215]\n" \
    "s_and_b32 s40, s40, 0x2000\n" \
    "s_waitcnt lgkmcnt(4)\n" \
    "v_mfma_f32_16x16x32_bf16 v[208:211], v[40:43], v[184:187], v[208:211]\n" \
    "ds_read_b128 v[204:207], v164 offset:15360\n" \
    "v_mfma_f32_16x16x32_bf16 v[212:215], v[120:123], v[184:187], v[212:215]\n" \
    "s_or_b32 s45, s45, s40\n" \
    "s_waitcnt lgkmcnt(4)\n" \
    "v_mfma_f32_16x16x32_bf16 v[208:211], v[44:47], v[188:191], v[208:211]\n" \
    "ds_read_b128 v[176:179], v164 offset:16384\n" \
    "v_mfma_f32_16x16x32_bf16 v[212:215], v[124:127], v[188:191], v[212:215]\n" \
    "s_waitcnt lgkmcnt(4)\n" \
    "v_mfma_f32_16x16x32_bf16 v[208:211], v[48:51], v[192:195], v[208:211]\n" \
    "ds_read_b128 v[180:183], v164 offset:17408\n" \
    "v_mfma_f32_16x16x32_bf16 v[212:215], v[128:131], v[192:195], v[212:215]\n" \
    "s_waitcnt lgkmcnt(4)\n" \
    "v_mfma_f32_16x16x32_bf16 v[208:211], v[52:55], v[196:199], v[208:211]\n" \
    "ds_read_b128 v[184:187], v164 offset:18432\n" \
    "v_mfma_f32_16x16x32_bf16 v[212:215], v[132:135], v[196:199], v[212:215]\n" \
    "s_waitcnt lgkmcnt(4)\n" \
    "v_mfma_f32_16x16x32_bf16 v[208:211], v[56:59], v[200:203], v[208:211]\n" \
    "ds_read_b128 v[188:191], v164 offset:19456\n" \
    "v_mfma_f32_16x16x32_bf16 v[212:215], v[136:139], v[200:203], v[212:215]\n" \
    "s_waitcnt lgkmcnt(4)\n" \
    "v_mfma_f32_16x16x32_bf16 v[208:211], v[60:63], v[204:207], v[208:211]\n" \
    "v_mfma_f32_16x16x32_bf16 v[212:215], v[140:143], v[204:207], v[212:215]\n" \
    "s_waitcnt lgkmcnt(3)\n" \
    "v_mfma_f32_16x16x32_bf16 v[208:211], v[64:67], v[176:179], v[208:211]\n" \
    "v_mfma_f32_16x16x32_bf16 v[212:215], v[144:147], v[176:179], v[212:215]\n" \
    "s_waitcnt lgkmcnt(2)\n" \
    "v_mfma_f32_16x16x32_bf16 v[208:211], v[68:71], v[180:183], v[208:211]\n" \
    "v_mfma_f32_16x16x32_bf16 v[212:215], v[148:151], v[180:183], v[212:215]\n" \
    "s_waitcnt lgkmcnt(1)\n" \
    "v_mfma_f32_16x16x32_bf16 v[208:211], v[72:75], v[184:187], v[208:211]\n" \
    "v_mfma_f32_16x16x32_bf16 v[212:215], v[152:155], v[184:187], v[212:215]\n" \
    "s_waitcnt lgkmcnt(0)\n" \
    "v_mfma_f32_16x16x32_bf16 v[208:211], v[76:79], v[188:191], v[208:211]\n" \
    "v_mfma_f32_16x16x32_bf16 v[212:215], v[156:159], v[188:191], v[212:215]\n" \
    "ds_read_b32 v167, v165\n" \
    "s_waitcnt vmcnt(0)\n" \
    "buffer_store_dwordx4 v[168:171], v161, s[8:11], s38 offen sc1\n" \
    "s_mov_b32 s38, 0x80000000\n" \
    "s_nop 2\n" \
    "v_cvt_pk_bf16_f32 v172, v208, v209\n" \
    "v_cvt_pk_bf16_f32 v173, v210, v211\n" \
    "v_cvt_pk_bf16_f32 v174, v212, v213\n" \
    "v_cvt_pk_bf16_f32 v175, v214, v215\n" \
    "buffer_store_dwordx4 v[172:175], v160, s[8:11], s45 offen sc1\n" \
    "s_waitcnt lgkmcnt(0)\n" \
    "v_readfirstlane_b32 s44, v167\n" \
    "s_nop 0\n" \
    "s_cmp_ge_u32 s44, s49\n" \
    "s_cbranch_scc1 .Lg_vO_%=\n" \
    ".Lg_wO_%=:\n" \
    "ds_read_b32 v167, v165\n" \
    "s_waitcnt lgkmcnt(0)\n" \
    "v_readfirstlane_b32 s44, v167\n" \
    "s_nop 0\n" \
    "s_cmp_lt_u32 s44, s49\n" \
    "s_cbranch_scc1 .Lg_wO_%=\n" \
    ".Lg_vO_%=:\n" \
    "s_and_b32 s44, s44, 255\n" \
    "s_cmp_lt_u32 s32, s25\n" \
    "s_cselect_b32 s36, s32, -1\n" \
    "s_add_u32 s39, s33, 128\n" \
    "s_cmp_lt_u32 s33, s44\n" \
    "s_cselect_b32 s36, s39, s36\n" \
    "s_addc_u32 s33, s33, 0\n" \
    "s_cmp_lt_u32 s36, 128\n" \
    "s_addc_u32 s32, s32, 0\n" \
    "s_cmp_eq_u32 s32, s29\n" \
    "s_cselect_b32 s32, s30, s32\n" \
    "s_mul_i32 s40, s36, 20480\n" \
    "s_add_u32 s40, s40, s22\n" \
    "s_cmp_eq_u32 s36, -1\n" \
    "s_cselect_b32 s40, 0x80000000, s40\n" \
    "buffer_load_dwordx4 v160, s[8:11], s40 offen offset:0 sc1 lds\n" \
    "buffer_load_dwordx4 v160, s[8:11], s40 offen offset:1024 sc1 lds\n" \
    "buffer_load_dwordx4 v160, s[8:11], s40 offen offset:2048 sc1 lds\n" \
    "buffer_load_dwordx4 v160, s[8:11], s40 offen offset:3072 sc1 lds\n" \
    "s_cmp_gt_u32 s34, 127\n" \
    "s_cselect_b32 s38, s21, s20\n" \
    "s_bitcmp1_b32 s34, 0\n" \
    "s_cselect_b32 s38, s38, 0x80000000\n" \
    "s_bfe_u32 s39, s34, 0x60001\n" \
    "s_add_u32 s39, s39, 1\n" \
    "v_mov_b32 v168, s39\n" \
    "s_mov_b32 s46, 0\n" \
    "s_mov_b32 s34, s35\n" \
    "s_mov_b32 s35, s36\n" \
    "s_add_u32 s41, s41, 20480\n" \
    "s_cmp_eq_u32 s41, 81920\n" \
    "s_cselect_b32 s41, 0, s41\n" \
    "s_add_u32 s49, s49, 256\n" \
    "v_xor_b32 v165, 4, v165\n" \
    "s_waitcnt lgkmcnt(0)\n" \
    "s_branch .Lg_lO_%=\n" \
    ".Lg_eO_%=:\n" \
    "s_waitcnt vmcnt(0)\n" \
    "buffer_store_dwordx4 v[168:171], v161, s[8:11], s38 offen sc1\n" \
    "s_mov_b32 s38, 0x80000000\n" \
    ".Lg_xO_%=:\n" \
    "ds_read_b32 v167, v165\n" \
    "s_waitcnt lgkmcnt(0)\n" \
    "v_readfirstlane_b32 s44, v167\n" \
    "s_nop 0\n" \
    "s_cmp_lt_u32 s44, s49\n" \
    "s_cbranch_scc1 .Lg_xO_%=\n" \
    "s_and_b32 s44, s44, 255\n" \
    "s_cmp_lt_u32 s32, s25\n" \
    "s_cselect_b32 s36, s32, -1\n" \
    "s_add_u32 s39, s33, 128\n" \
    "s_cmp_lt_u32 s33, s44\n" \
    "s_cselect_b32 s36, s39, s36\n" \
    "s_addc_u32 s33, s33, 0\n" \
    "s_cmp_lt_u32 s36, 128\n" \
    "s_addc_u32 s32, s32, 0\n" \
    "s_cmp_eq_u32 s32, s29\n" \
    "s_cselect_b32 s32, s30, s32\n" \
    "s_add_u32 s48, s41, 40960\n" \
    "s_cmp_ge_u32 s48, 81920\n" \
    "s_cselect_b32 s40, 81920, 0\n" \
    "s_sub_u32 s48, s48, s40\n" \
    "s_add_u32 m0, s48, s23\n" \
    "s_mul_i32 s40, s36, 20480\n" \
    "s_add_u32 s40, s40, s22\n" \
    "s_cmp_eq_u32 s36, -1\n" \
    "s_cselect_b32 s40, 0x80000000, s40\n" \
    "buffer_load_dwordx4 v160, s[8:11], s40 offen offset:0 sc1 lds\n" \
    "buffer_load_dwordx4 v160, s[8:11], s40 offen offset:1024 sc1 lds\n" \
    "buffer_load_dwordx4 v160, s[8:11], s40 offen offset:2048 sc1 lds\n" \
    "buffer_load_dwordx4 v160, s[8:11], s40 offen offset:3072 sc1 lds\n" \
    "s_mov_b32 s34, s35\n" \
    "s_mov_b32 s35, s36\n" \
    "s_add_u32 s41, s41, 20480\n" \
    "s_cmp_eq_u32 s41, 81920\n" \
    "s_cselect_b32 s41, 0, s41\n" \
    "s_add_u32 s49, s49, 256\n" \
    "v_xor_b32 v165, 4, v165\n" \
    "s_and_b32 s39, s34, s35\n" \
    "s_waitcnt lgkmcnt(0)\n" \
    "s_add_u32 s40, s32, s33\n" \
    "s_cmp_eq_u32 s39, -1\n" \
    "s_cselect_b32 s40, s40, 0\n" \
    "s_cmp_eq_u32 s40, s26\n" \
    "s_cbranch_scc1 .Lg_end_%=\n" \
    "s_add_u32 s46, s46, 1\n" \
    "s_cmp_lt_u32 s46, 0x20000\n" \
    "s_cbranch_scc1 .Lg_lO_%=\n" \
    "s_branch .Lg_end_%=\n" \
    ".Lg_lS_%=:\n" \
    "v_add_u32 v164, s41, v160\n" \
    "s_cmp_eq_u32 s34, -1\n" \
    "s_barrier\n" \
    "s_cbranch_scc1 .Lg_eS_%=\n" \
    "ds_read_b128 v[176:179], v164 offset:0\n" \
    "ds_read_b128 v[180:183], v164 offset:1024\n" \
    "ds_read_b128 v[184:187], v164 offset:2048\n" \
    "ds_read_b128 v[188:191], v164 offset:3072\n" \
    "ds_read_b128 v[192:195], v164 offset:4096\n" \
    "s_lshl_b32 s42, s43, 9\n" \
    "buffer_load_dword v166, v162, s[8:11], s42 offen sc1\n" \
    "s_add_u32 s48, s41, 40960\n" \
    "s_cmp_ge_u32 s48, 81920\n" \
    "s_cselect_b32 s40, 81920, 0\n" \
    "s_sub_u32 s48, s48, s40\n" \
    "s_add_u32 m0, s48, s23\n" \
    "s_waitcnt lgkmcnt(4)\n" \
    "v_mfma_f32_16x16x32_bf16 v[208:211], v[0:3], v[176:179], 0\n" \
    "ds_read_b128 v[196:199], v164 offset:5120\n" \
    "v_mfma_f32_16x16x32_bf16 v[212:215], v[80:83], v[176:179], 0\n" \
    "s_waitcnt lgkmcnt(4)\n" \
    "v_mfma_f32_16x16x32_bf16 v[208:211], v[4:7], v[180:183], v[208:211]\n" \
    "ds_read_b128 v[200:203], v164 offset:6144\n" \
    "v_mfma_f32_16x16x32_bf16 v[212:215], v[84:87], v[180:183], v[212:215]\n" \
    "s_lshr_b32 s39, s34, 1\n" \
    "s_waitcnt lgkmcnt(4)\n" \
    "v_mfma_f32_16x16x32_bf16 v[208:211], v[8:11], v[184:187], v[208:211]\n" \
    "ds_read_b128 v[204:207], v164 offset:7168\n" \
    "v_mfma_f32_16x16x32_bf16 v[212:215], v[88:91], v[184:187], v[212:215]\n" \
    "s_cmp_gt_u32 s39, 63\n" \
    "s_cselect_b32 s40, %[rmask], 63\n" \
    "s_waitcnt lgkmcnt(4)\n" \
    "v_mfma_f32_16x16x32_bf16 v[208:211], v[12:15], v[188:191], v[208:211]\n" \
    "ds_read_b128 v[176:179], v164 offset:8192\n" \
    "v_mfma_f32_16x16x32_bf16 v[212:215], v[92:95], v[188:191], v[212:215]\n" \
    "s_and_b32 s40, s40, s39\n" \
    "s_waitcnt lgkmcnt(4)\n" \
    "v_mfma_f32_16x16x32_bf16 v[208:211], v[16:19], v[192:195], v[208:211]\n" \
    "ds_read_b128 v[180:183], v164 offset:9216\n" \
    "v_mfma_f32_16x16x32_bf16 v[212:215], v[96:99], v[192:195], v[212:215]\n" \
    "s_mul_i32 s45, s40, 1572864\n" \
    "s_waitcnt lgkmcnt(4)\n" \
    "v_mfma_f32_16x16x32_bf16 v[208:211], v[20:23], v[196:199], v[208:211]\n" \
    "ds_read_b128 v[184:187], v164 offset:10240\n" \
    "v_mfma_f32_16x16x32_bf16 v[212:215], v[100:103], v[196:199], v[212:215]\n" \
    "s_bitcmp1_b32 s34, 0\n" \
    "s_cselect_b32 s40, s18, s17\n" \
    "s_waitcnt lgkmcnt(4)\n" \
    "v_mfma_f32_16x16x32_bf16 v[208:211], v[24:27], v[200:203], v[208:211]\n" \
    "ds_read_b128 v[188:191], v164 offset:11264\n" \
    "v_mfma_f32_16x16x32_bf16 v[212:215], v[104:107], v[200:203], v[212:215]\n" \
    "s_add_u32 s45, s45, s40\n" \
    "s_waitcnt lgkmcnt(4)\n" \
    "v_mfma_f32_16x16x32_bf16 v[208:211], v[28:31], v[204:207], v[208:211]\n" \
    "ds_read_b128 v[192:195], v164 offset:12288\n" \
    "v_mfma_f32_16x16x32_bf16 v[212:215], v[108:111], v[204:207], v[212:215]\n" \
    "s_lshr_b32 s40, s45, 8\n" \
    "s_waitcnt lgkmcnt(4)\n" \
    "v_mfma_f32_16x16x32_bf16 v[208:211], v[32:35], v[176:179], v[208:211]\n" \
    "ds_read_b128 v[196:199], v164 offset:13312\n" \
    "v_mfma_f32_16x16x32_bf16 v[212:215], v[112:115], v[176:179], v[212:215]\n" \
    "s_xor_b32 s40, s40, s19\n" \
    "s_waitcnt lgkmcnt(4)\n" \
    "v_mfma_f32_16x16x32_bf16 v[208:211], v[36:39], v[180:183], v[208:211]\n" \
    "ds_read_b128 v[200:203], v164 offset:14336\n" \
    "v_mfma_f32_16x16x32_bf16 v[212:215], v[116:119], v[180:183], v[212:215]\n" \
    "s_and_b32 s40, s40, 0x2000\n" \
    "s_waitcnt lgkmcnt(4)\n" \
    "v_mfma_f32_16x16x32_bf16 v[208:211], v[40:43], v[184:187], v[208:211]\n" \
    "ds_read_b128 v[204:207], v164 offset:15360\n" \
    "v_mfma_f32_16x16x32_bf16 v[212:215], v[120:123], v[184:187], v[212:215]\n" \
    "s_or_b32 s45, s45, s40\n" \
    "s_waitcnt lgkmcnt(4)\n" \
    "v_mfma_f32_16x16x32_bf16 v[208:211], v[44:47], v[188:191], v[208:211]\n" \
    "ds_read_b128 v[176:179], v164 offset:16384\n" \
    "v_mfma_f32_16x16x32_bf16 v[212:215], v[124:127], v[188:191], v[212:215]\n" \
    "s_waitcnt lgkmcnt(4)\n" \
    "v_mfma_f32_16x16x32_bf16 v[208:211], v[48:51], v[192:195], v[208:211]\n" \
    "ds_read_b128 v[180:183], v164 offset:17408\n" \
    "v_mfma_f32_16x16x32_bf16 v[212:215], v[128:131], v[192:195], v[212:215]\n" \
    "s_waitcnt lgkmcnt(4)\n" \
    "v_mfma_f32_16x16x32_bf16 v[208:211], v[52:55], v[196:199], v[208:211]\n" \
    "ds_read_b128 v[184:187], v164 offset:18432\n" \
    "v_mfma_f32_16x16x32_bf16 v[212:215], v[132:135], v[196:199], v[212:215]\n" \
    "s_waitcnt lgkmcnt(4)\n" \
    "v_mfma_f32_16x16x32_bf16 v[208:211], v[56:59], v[200:203], v[208:211]\n" \
    "ds_read_b128 v[188:191], v164 offset:19456\n" \
    "v_mfma_f32_16x16x32_bf16 v[212:215], v[136:139], v[200:203], v[212:215]\n" \
    "s_waitcnt lgkmcnt(4)\n" \
    "v_mfma_f32_16x16x32_bf16 v[208:211], v[60:63], v[204:207], v[208:211]\n" \
    "v_mfma_f32_16x16x32_bf16 v[212:215], v[140:143], v[204:207], v[212:215]\n" \
    "s_waitcnt lgkmcnt(3)\n" \
    "v_mfma_f32_16x16x32_bf16 v[208:211], v[64:67], v[176:179], v[208:211]\n" \
    "v_mfma_f32_16x16x32_bf16 v[212:215], v[144:147], v[176:179], v[212:215]\n" \
    "s_waitcnt vmcnt(0)\n" \
    "v_cmp_ne_u32 vcc, 0, v166\n" \
    "s_waitcnt lgkmcnt(2)\n" \
    "v_mfma_f32_16x16x32_bf16 v[208:211], v[68:71], v[180:183], v[208:211]\n" \
    "v_mfma_f32_16x16x32_bf16 v[212:215], v[148:151], v[180:183], v[212:215]\n" \
    "s_not_b32 s39, vcc_lo\n" \
    "s_bitset1_b32 s39, 24\n" \
    "s_ff1_i32_b32 s39, s39\n" \
    "s_waitcnt lgkmcnt(1)\n" \
    "v_mfma_f32_16x16x32_bf16 v[208:211], v[72:75], v[184:187], v[208:211]\n" \
    "v_mfma_f32_16x16x32_bf16 v[212:215], v[152:155], v[184:187], v[212:215]\n" \
    "s_lshr_b32 s39, s39, 3\n" \
    "s_add_u32 s43, s43, s39\n" \
    "s_min_u32 s43, s43, 128\n" \
    "s_waitcnt lgkmcnt(0)\n" \
    "v_mfma_f32_16x16x32_bf16 v[208:211], v[76:79], v[188:191], v[208:211]\n" \
    "v_mfma_f32_16x16x32_bf16 v[212:215], v[156:159], v[188:191], v[212:215]\n" \
    "s_or_b32 s44, s43, s49\n" \
    "v_mov_b32 v167, s44\n" \
    "ds_write_b32 v165, v167\n" \
    "s_waitcnt vmcnt(0)\n" \
    "buffer_store_dwordx4 v[168:171], v161, s[8:11], s38 offen sc1\n" \
    "s_mov_b32 s38, 0x80000000\n" \
    "s_nop 0\n" \
    "v_cvt_pk_bf16_f32 v172, v208, v209\n" \
    "v_cvt_pk_bf16_f32 v173, v210, v211\n" \
    "v_cvt_pk_bf16_f32 v174, v212, v213\n" \
    "v_cvt_pk_bf16_f32 v175, v214, v215\n" \
    "buffer_store_dwordx4 v[172:175], v160, s[8:11], s45 offen sc1\n" \
    "s_and_b32 s44, s44, 255\n" \
    "s_cmp_lt_u32 s32, s25\n" \
    "s_cselect_b32 s36, s32, -1\n" \
    "s_add_u32 s39, s33, 128\n" \
    "s_cmp_lt_u32 s33, s44\n" \
    "s_cselect_b32 s36, s39, s36\n" \
    "s_addc_u32 s33, s33, 0\n" \
    "s_cmp_lt_u32 s36, 128\n" \
    "s_addc_u32 s32, s32, 0\n" \
    "s_cmp_eq_u32 s32, s29\n" \
    "s_cselect_b32 s32, s30, s32\n" \
    "s_mul_i32 s40, s36, 20480\n" \
    "s_add_u32 s40, s40, s22\n" \
    "s_cmp_eq_u32 s36, -1\n" \
    "s_cselect_b32 s40, 0x80000000, s40\n" \
    "buffer_load_dwordx4 v160, s[8:11], s40 offen offset:0 sc1 lds\n" \
    "buffer_load_dwordx4 v160, s[8:11], s40 offen offset:1024 sc1 lds\n" \
    "buffer_load_dwordx4 v160, s[8:11], s40 offen offset:2048 sc1 lds\n" \
    "buffer_load_dwordx4 v160, s[8:11], s40 offen offset:3072 sc1 lds\n" \
    "s_cmp_gt_u32 s34, 127\n" \
    "s_cselect_b32 s38, s21, s20\n" \
    "s_bitcmp1_b32 s34, 0\n" \
    "s_cselect_b32 s38, s38, 0x80000000\n" \
    "s_bfe_u32 s39, s34, 0x60001\n" \
    "s_add_u32 s39, s39, 1\n" \
    "v_mov_b32 v168, s39\n" \
    "s_mov_b32 s46, 0\n" \
    "s_mov_b32 s34, s35\n" \
    "s_mov_b32 s35, s36\n" \
    "s_add_u32 s41, s41, 20480\n" \
    "s_cmp_eq_u32 s41, 81920\n" \
    "s_cselect_b32 s41, 0, s41\n" \
    "s_add_u32 s49, s49, 256\n" \
    "v_xor_b32 v165, 4, v165\n" \
    "s_waitcnt lgkmcnt(0)\n" \
    "s_branch .Lg_lS_%=\n" \
    ".Lg_eS_%=:\n" \
    "s_and_b32 s39, s35, s46\n" \
    "s_cmp_lt_u32 s39, 2\n" \
    "s_cbranch_scc1 .Lg_ns_%=\n" \
    "s_sleep 2\n" \
    ".Lg_ns_%=:\n" \
    "s_lshl_b32 s42, s43, 9\n" \
    "buffer_load_dword v166, v162, s[8:11], s42 offen sc1\n" \
    "s_waitcnt vmcnt(0)\n" \
    "v_cmp_ne_u32 vcc, 0, v166\n" \
    "s_nop 0\n" \
    "s_not_b32 s39, vcc_lo\n" \
    "s_bitset1_b32 s39, 24\n" \
    "s_ff1_i32_b32 s39, s39\n" \
    "s_lshr_b32 s39, s39, 3\n" \
    "s_add_u32 s43, s43, s39\n" \
    "s_min_u32 s43, s43, 128\n" \
    "s_or_b32 s44, s43, s49\n" \
    "v_mov_b32 v167, s44\n" \
    "ds_write_b32 v165, v167\n" \
    "s_waitcnt vmcnt(0)\n" \
    "buffer_store_dwordx4 v[168:171], v161, s[8:11], s38 offen sc1\n" \
    "s_mov_b32 s38, 0x80000000\n" \
    "s_and_b32 s44, s44, 255\n" \
    "s_cmp_lt_u32 s32, s25\n" \
    "s_cselect_b32 s36, s32, -1\n" \
    "s_add_u32 s39, s33, 128\n" \
    "s_cmp_lt_u32 s33, s44\n" \
    "s_cselect_b32 s36, s39, s36\n" \
    "s_addc_u32 s33, s33, 0\n" \
    "s_cmp_lt_u32 s36, 128\n" \
    "s_addc_u32 s32, s32, 0\n" \
    "s_cmp_eq_u32 s32, s29\n" \
    "s_cselect_b32 s32, s30, s32\n" \
    "s_add_u32 s48, s41, 40960\n" \
    "s_cmp_ge_u32 s48, 81920\n" \
    "s_cselect_b32 s40, 81920, 0\n" \
    "s_sub_u32 s48, s48, s40\n" \
    "s_add_u32 m0, s48, s23\n" \
    "s_mul_i32 s40, s36, 20480\n" \
    "s_add_u32 s40, s40, s22\n" \
    "s_cmp_eq_u32 s36, -1\n" \
    "s_cselect_b32 s40, 0x80000000, s40\n" \
    "buffer_load_dwordx4 v160, s[8:11], s40 offen offset:0 sc1 lds\n" \
    "buffer_load_dwordx4 v160, s[8:11], s40 offen offset:1024 sc1 lds\n" \
    "buffer_load_dwordx4 v160, s[8:11], s40 offen offset:2048 sc1 lds\n" \
    "buffer_load_dwordx4 v160, s[8:11], s40 offen offset:3072 sc1 lds\n" \
    "s_mov_b32 s34, s35\n" \
    "s_mov_b32 s35, s36\n" \
    "s_add_u32 s41, s41, 20480\n" \
    "s_cmp_eq_u32 s41, 81920\n" \
    "s_cselect_b32 s41, 0, s41\n" \
    "s_add_u32 s49, s49, 256\n" \
    "v_xor_b32 v165, 4, v165\n" \
    "s_and_b32 s39, s34, s35\n" \
    "s_waitcnt lgkmcnt(0)\n" \
    "s_add_u32 s40, s32, s33\n" \
    "s_cmp_eq_u32 s39, -1\n" \
    "s_cselect_b32 s40, s40, 0\n" \
    "s_cmp_eq_u32 s40, s26\n" \
    "s_cbranch_scc1 .Lg_end_%=\n" \
    "s_add_u32 s46, s46, 1\n" \
    "s_cmp_lt_u32 s46, 0x20000\n" \
    "s_cbranch_scc1 .Lg_lS_%=\n" \
    "s_branch .Lg_end_%=\n" \
    ".Lg_end_%=:\n" \
    "s_setprio 0\n" \
    "s_waitcnt vmcnt(0)\n" \
    ""
#define OB_ASM \
    "s_mov_b32 s32, 0\n" \
    "s_mov_b32 s33, 0\n" \
    "s_mov_b32 s34, -1\n" \
    "s_mov_b32 s36, 0x80000000\n" \
    "s_mov_b32 s39, 0\n" \
    "s_mov_b32 s69, 0x80000000\n" \
    "s_mov_b32 s45, 0\n" \
    "s_mov_b32 s44, 0x10000\n" \
    "v_mov_b32 v125, 24576\n" \
    "v_mov_b32 v132, 1\n" \
    "v_mov_b32 v133, 1\n" \
    "v_mov_b32 v134, 1\n" \
    "v_mov_b32 v135, 1\n" \
    "s_cmp_eq_u32 s16, 0\n" \
    "s_cbranch_scc1 .Lo_lS_%=\n" \
    "s_cmp_lt_u32 s16, 4\n" \
    "s_cbranch_scc1 .Lo_lO_%=\n" \
    ".Lo_lY_%=:\n" \
    "s_barrier\n" \
    "ds_read_b32 v129, v125\n" \
    ".Lo_nt_%=:\n" \
    "s_waitcnt lgkmcnt(0)\n" \
    "v_readfirstlane_b32 s42, v129\n" \
    "s_nop 0\n" \
    "s_bfe_u32 s43, s42, 0x80008\n" \
    "s_and_b32 s42, s42, 255\n" \
    "s_add_u32 s37, s33, %[brn]\n" \
    "s_min_u32 s42, s42, s37\n" \
    "s_cmp_lt_u32 s32, s42\n" \
    "s_cselect_b32 s35, s32, -1\n" \
    "s_add_u32 s37, s33, 64\n" \
    "s_cmp_lt_u32 s33, s43\n" \
    "s_cselect_b32 s35, s37, s35\n" \
    "s_addc_u32 s33, s33, 0\n" \
    "s_cmp_lt_u32 s35, 64\n" \
    "s_addc_u32 s32, s32, 0\n" \
    "s_xor_b32 s37, s39, 12288\n" \
    "s_add_u32 m0, s37, s20\n" \
    "s_cmp_gt_u32 s35, 63\n" \
    "s_cselect_b32 s38, %[rmask], 63\n" \
    "s_and_b32 s38, s38, s35\n" \
    "s_mul_i32 s38, s38, 1572864\n" \
    "s_add_u32 s38, s38, s19\n" \
    "s_lshr_b32 s37, s38, 8\n" \
    "s_xor_b32 s37, s37, s22\n" \
    "s_and_b32 s37, s37, 0x2000\n" \
    "s_or_b32 s38, s38, s37\n" \
    "s_cmp_eq_u32 s35, -1\n" \
    "s_cselect_b32 s38, 0x80000000, s38\n" \
    "buffer_load_dwordx4 v120, s[8:11], s38 offen offset:0 sc1 lds\n" \
    "buffer_load_dwordx4 v120, s[8:11], s38 offen offset:1024 sc1 lds\n" \
    "buffer_load_dwordx4 v120, s[8:11], s38 offen offset:2048 sc1 lds\n" \
    "buffer_load_dwordx4 v120, s[8:11], s38 offen offset:3072 sc1 lds\n" \
    "v_add_u32 v124, s39, v120\n" \
    "s_xor_b32 s39, s39, 12288\n" \
    "s_add_u32 s44, s44, 0x10000\n" \
    "v_xor_b32 v125, 4, v125\n" \
    "s_cmp_eq_u32 s34, -1\n" \
    "s_cbranch_scc1 .Lo_eY_%=\n" \
    "ds_read_b128 v[160:163], v124 offset:0\n" \
    "ds_read_b128 v[184:187], v124 offset:6144\n" \
    "ds_read_b128 v[164:167], v124 offset:1024\n" \
    "ds_read_b128 v[188:191], v124 offset:7168\n" \
    "ds_read_b128 v[168:171], v124 offset:2048\n" \
    "ds_read_b128 v[192:195], v124 offset:8192\n" \
    "ds_read_b128 v[172:175], v124 offset:3072\n" \
    "ds_read_b128 v[196:199], v124 offset:9216\n" \
    "ds_read_b128 v[176:179], v124 offset:4096\n" \
    "ds_read_b128 v[200:203], v124 offset:10240\n" \
    "ds_read_b128 v[180:183], v124 offset:5120\n" \
    "ds_read_b128 v[204:207], v124 offset:11264\n" \
    "s_lshr_b32 s37, s34, 6\n" \
    "s_mul_i32 s37, s37, %[brn]\n" \
    "s_and_b32 s38, s34, %[brn]-1\n" \
    "s_add_u32 s37, s37, s38\n" \
    "s_mul_i32 s37, s37, 2621440\n" \
    "s_add_u32 s37, s37, s17\n" \
    "s_lshr_b32 s38, s37, 8\n" \
    "s_xor_b32 s38, s38, s21\n" \
    "s_and_b32 s38, s38, 0x2000\n" \
    "s_or_b32 s46, s37, s38\n" \
    "s_add_u32 s37, s37, 65536\n" \
    "s_lshr_b32 s38, s37, 8\n" \
    "s_xor_b32 s38, s38, s21\n" \
    "s_and_b32 s38, s38, 0x2000\n" \
    "s_or_b32 s47, s37, s38\n" \
    "s_add_u32 s37, s37, 65536\n" \
    "s_lshr_b32 s38, s37, 8\n" \
    "s_xor_b32 s38, s38, s21\n" \
    "s_and_b32 s38, s38, 0x2000\n" \
    "s_or_b32 s48, s37, s38\n" \
    "s_add_u32 s37, s37, 65536\n" \
    "s_lshr_b32 s38, s37, 8\n" \
    "s_xor_b32 s38, s38, s21\n" \
    "s_and_b32 s38, s38, 0x2000\n" \
    "s_or_b32 s49, s37, s38\n" \
    "s_add_u32 s37, s37, 65536\n" \
    "s_lshr_b32 s38, s37, 8\n" \
    "s_xor_b32 s38, s38, s21\n" \
    "s_and_b32 s38, s38, 0x2000\n" \
    "s_or_b32 s50, s37, s38\n" \
    "s_waitcnt vmcnt(4)\n" \
    "buffer_store_dwordx4 v[132:135], v121, s[8:11], s36 offen sc1\n" \
    "s_mov_b32 s36, 0x80000000\n" \
    "s_mov_b32 s45, 0\n" \
    "s_waitcnt lgkmcnt(11)\n" \
    "v_mfma_f32_16x16x32_bf16 v[144:147], v[160:163], v[0:3], 0\n" \
    "s_waitcnt lgkmcnt(10)\n" \
    "v_mfma_f32_16x16x32_bf16 v[148:151], v[184:187], v[0:3], 0\n" \
    "s_waitcnt lgkmcnt(9)\n" \
    "v_mfma_f32_16x16x32_bf16 v[144:147], v[164:167], v[4:7], v[144:147]\n" \
    "s_waitcnt lgkmcnt(8)\n" \
    "v_mfma_f32_16x16x32_bf16 v[148:151], v[188:191], v[4:7], v[148:151]\n" \
    "s_waitcnt lgkmcnt(7)\n" \
    "v_mfma_f32_16x16x32_bf16 v[144:147], v[168:171], v[8:11], v[144:147]\n" \
    "s_waitcnt lgkmcnt(6)\n" \
    "v_mfma_f32_16x16x32_bf16 v[148:151], v[192:195], v[8:11], v[148:151]\n" \
    "s_waitcnt lgkmcnt(5)\n" \
    "v_mfma_f32_16x16x32_bf16 v[144:147], v[172:175], v[12:15], v[144:147]\n" \
    "s_waitcnt lgkmcnt(4)\n" \
    "v_mfma_f32_16x16x32_bf16 v[148:151], v[196:199], v[12:15], v[148:151]\n" \
    "s_waitcnt lgkmcnt(3)\n" \
    "v_mfma_f32_16x16x32_bf16 v[144:147], v[176:179], v[16:19], v[144:147]\n" \
    "s_waitcnt lgkmcnt(2)\n" \
    "v_mfma_f32_16x16x32_bf16 v[148:151], v[200:203], v[16:19], v[148:151]\n" \
    "s_waitcnt lgkmcnt(1)\n" \
    "v_mfma_f32_16x16x32_bf16 v[144:147], v[180:183], v[20:23], v[144:147]\n" \
    "s_waitcnt lgkmcnt(0)\n" \
    "v_mfma_f32_16x16x32_bf16 v[148:151], v[204:207], v[20:23], v[148:151]\n" \
    "v_mfma_f32_16x16x32_bf16 v[152:155], v[160:163], v[24:27], 0\n" \
    "v_mfma_f32_16x16x32_bf16 v[156:159], v[184:187], v[24:27], 0\n" \
    "v_mfma_f32_16x16x32_bf16 v[152:155], v[164:167], v[28:31], v[152:155]\n" \
    "v_mfma_f32_16x16x32_bf16 v[156:159], v[188:191], v[28:31], v[156:159]\n" \
    "v_mfma_f32_16x16x32_bf16 v[152:155], v[168:171], v[32:35], v[152:155]\n" \
    "v_mfma_f32_16x16x32_bf16 v[156:159], v[192:195], v[32:35], v[156:159]\n" \
    "v_mfma_f32_16x16x32_bf16 v[152:155], v[172:175], v[36:39], v[152:155]\n" \
    "v_mfma_f32_16x16x32_bf16 v[156:159], v[196:199], v[36:39], v[156:159]\n" \
    "v_cvt_pk_bf16_f32 v136, v144, v145\n" \
    "v_mfma_f32_16x16x32_bf16 v[152:155], v[176:179], v[40:43], v[152:155]\n" \
    "v_cvt_pk_bf16_f32 v137, v148, v149\n" \
    "v_mfma_f32_16x16x32_bf16 v[156:159], v[200:203], v[40:43], v[156:159]\n" \
    "v_cvt_pk_bf16_f32 v138, v146, v147\n" \
    "v_mfma_f32_16x16x32_bf16 v[152:155], v[180:183], v[44:47], v[152:155]\n" \
    "v_cvt_pk_bf16_f32 v139, v150, v151\n" \
    "v_mfma_f32_16x16x32_bf16 v[156:159], v[204:207], v[44:47], v[156:159]\n" \
    "buffer_store_dwordx4 v[136:139], v120, s[8:11], s46 offen sc1\n" \
    "v_mfma_f32_16x16x32_bf16 v[144:147], v[160:163], v[48:51], 0\n" \
    "v_mfma_f32_16x16x32_bf16 v[148:151], v[184:187], v[48:51], 0\n" \
    "v_mfma_f32_16x16x32_bf16 v[144:147], v[164:167], v[52:55], v[144:147]\n" \
    "v_mfma_f32_16x16x32_bf16 v[148:151], v[188:191], v[52:55], v[148:151]\n" \
    "v_mfma_f32_16x16x32_bf16 v[144:147], v[168:171], v[56:59], v[144:147]\n" \
    "v_mfma_f32_16x16x32_bf16 v[148:151], v[192:195], v[56:59], v[148:151]\n" \
    "v_mfma_f32_16x16x32_bf16 v[144:147], v[172:175], v[60:63], v[144:147]\n" \
    "v_mfma_f32_16x16x32_bf16 v[148:151], v[196:199], v[60:63], v[148:151]\n" \
    "v_cvt_pk_bf16_f32 v140, v152, v153\n" \
    "v_mfma_f32_16x16x32_bf16 v[144:147], v[176:179], v[64:67], v[144:147]\n" \
    "v_cvt_pk_bf16_f32 v141, v156, v157\n" \
    "v_mfma_f32_16x16x32_bf16 v[148:151], v[200:203], v[64:67], v[148:151]\n" \
    "v_cvt_pk_bf16_f32 v142, v154, v155\n" \
    "v_mfma_f32_16x16x32_bf16 v[144:147], v[180:183], v[68:71], v[144:147]\n" \
    "v_cvt_pk_bf16_f32 v143, v158, v159\n" \
    "v_mfma_f32_16x16x32_bf16 v[148:151], v[204:207], v[68:71], v[148:151]\n" \
    "buffer_store_dwordx4 v[140:143], v120, s[8:11], s47 offen sc1\n" \
    "v_mfma_f32_16x16x32_bf16 v[152:155], v[160:163], v[72:75], 0\n" \
    "v_mfma_f32_16x16x32_bf16 v[156:159], v[184:187], v[72:75], 0\n" \
    "v_mfma_f32_16x16x32_bf16 v[152:155], v[164:167], v[76:79], v[152:155]\n" \
    "v_mfma_f32_16x16x32_bf16 v[156:159], v[188:191], v[76:79], v[156:159]\n" \
    "v_mfma_f32_16x16x32_bf16 v[152:155], v[168:171], v[80:83], v[152:155]\n" \
    "v_mfma_f32_16x16x32_bf16 v[156:159], v[192:195], v[80:83], v[156:159]\n" \
    "v_mfma_f32_16x16x32_bf16 v[152:155], v[172:175], v[84:87], v[152:155]\n" \
    "v_mfma_f32_16x16x32_bf16 v[156:159], v[196:199], v[84:87], v[156:159]\n" \
    "v_cvt_pk_bf16_f32 v136, v144, v145\n" \
    "v_mfma_f32_16x16x32_bf16 v[152:155], v[176:179], v[88:91], v[152:155]\n" \
    "v_cvt_pk_bf16_f32 v137, v148, v149\n" \
    "v_mfma_f32_16x16x32_bf16 v[156:159], v[200:203], v[88:91], v[156:159]\n" \
    "v_cvt_pk_bf16_f32 v138, v146, v147\n" \
    "v_mfma_f32_16x16x32_bf16 v[152:155], v[180:183], v[92:95], v[152:155]\n" \
    "v_cvt_pk_bf16_f32 v139, v150, v151\n" \
    "s_setprio 1\n" \
    "s_waitcnt vmcnt(3)\n" \
    "v_mfma_f32_16x16x32_bf16 v[156:159], v[204:207], v[92:95], v[156:159]\n" \
    "buffer_store_dwordx4 v[136:139], v120, s[8:11], s48 offen sc1\n" \
    "s_barrier\n" \
    "v_mfma_f32_16x16x32_bf16 v[144:147], v[160:163], v[96:99], 0\n" \
    "ds_read_b32 v129, v125\n" \
    "v_mfma_f32_16x16x32_bf16 v[148:151], v[184:187], v[96:99], 0\n" \
    "v_mfma_f32_16x16x32_bf16 v[144:147], v[164:167], v[100:103], v[144:147]\n" \
    "v_mfma_f32_16x16x32_bf16 v[148:151], v[188:191], v[100:103], v[148:151]\n" \
    "v_mfma_f32_16x16x32_bf16 v[144:147], v[168:171], v[104:107], v[144:147]\n" \
    "v_mfma_f32_16x16x32_bf16 v[148:151], v[192:195], v[104:107], v[148:151]\n" \
    "v_mfma_f32_16x16x32_bf16 v[144:147], v[172:175], v[108:111], v[144:147]\n" \
    "v_mfma_f32_16x16x32_bf16 v[148:151], v[196:199], v[108:111], v[148:151]\n" \
    "v_cvt_pk_bf16_f32 v140, v152, v153\n" \
    "v_mfma_f32_16x16x32_bf16 v[144:147], v[176:179], v[112:115], v[144:147]\n" \
    "v_cvt_pk_bf16_f32 v141, v156, v157\n" \
    "v_mfma_f32_16x16x32_bf16 v[148:151], v[200:203], v[112:115], v[148:151]\n" \
    "v_cvt_pk_bf16_f32 v142, v154, v155\n" \
    "v_mfma_f32_16x16x32_bf16 v[144:147], v[180:183], v[116:119], v[144:147]\n" \
    "v_cvt_pk_bf16_f32 v143, v158, v159\n" \
    "v_mfma_f32_16x16x32_bf16 v[148:151], v[204:207], v[116:119], v[148:151]\n" \
    "buffer_store_dwordx4 v[140:143], v120, s[8:11], s49 offen sc1\n" \
    "s_setprio 0\n" \
    "s_lshr_b32 s36, s34, 6\n" \
    "s_lshl_b32 s36, s36, 15\n" \
    "s_add_u32 s36, s36, s18\n" \
    "s_and_b32 s37, s34, 63\n" \
    "s_add_u32 s37, s37, 1\n" \
    "v_mov_b32 v132, s37\n" \
    "v_cvt_pk_bf16_f32 v136, v144, v145\n" \
    "v_cvt_pk_bf16_f32 v137, v148, v149\n" \
    "v_cvt_pk_bf16_f32 v138, v146, v147\n" \
    "v_cvt_pk_bf16_f32 v139, v150, v151\n" \
    "buffer_store_dwordx4 v[136:139], v120, s[8:11], s50 offen sc1\n" \
    "s_mov_b32 s34, s35\n" \
    "s_branch .Lo_nt_%=\n" \
    ".Lo_eY_%=:\n" \
    "s_waitcnt vmcnt(4)\n" \
    "buffer_store_dwordx4 v[132:135], v121, s[8:11], s36 offen sc1\n" \
    "s_mov_b32 s36, 0x80000000\n" \
    "s_mov_b32 s34, s35\n" \
    "s_waitcnt vmcnt(1)\n" \
    "s_add_u32 s38, s32, s33\n" \
    "s_cmp_eq_u32 s34, -1\n" \
    "s_cselect_b32 s38, s38, 0\n" \
    "s_cmp_eq_u32 s38, 128\n" \
    "s_cbranch_scc1 .Lo_end_%=\n" \
    "s_add_u32 s45, s45, 1\n" \
    "s_cmp_lt_u32 s45, 0x8000\n" \
    "s_cbranch_scc1 .Lo_lY_%=\n" \
    "s_branch .Lo_end_%=\n" \
    ".Lo_lO_%=:\n" \
    "v_add_u32 v124, s39, v120\n" \
    "s_cmp_eq_u32 s34, -1\n" \
    "s_barrier\n" \
    "s_cbranch_scc1 .Lo_eO_%=\n" \
    "ds_read_b128 v[160:163], v124 offset:0\n" \
    "ds_read_b128 v[184:187], v124 offset:6144\n" \
    "ds_read_b128 v[164:167], v124 offset:1024\n" \
    "ds_read_b128 v[188:191], v124 offset:7168\n" \
    "ds_read_b128 v[168:171], v124 offset:2048\n" \
    "ds_read_b128 v[192:195], v124 offset:8192\n" \
    "ds_read_b128 v[172:175], v124 offset:3072\n" \
    "ds_read_b128 v[196:199], v124 offset:9216\n" \
    "ds_read_b128 v[176:179], v124 offset:4096\n" \
    "ds_read_b128 v[200:203], v124 offset:10240\n" \
    "ds_read_b128 v[180:183], v124 offset:5120\n" \
    "ds_read_b128 v[204:207], v124 offset:11264\n" \
    "ds_read_b32 v129, v125\n" \
    "s_waitcnt lgkmcnt(12)\n" \
    "v_mfma_f32_16x16x32_bf16 v[144:147], v[160:163], v[0:3], 0\n" \
    "s_waitcnt lgkmcnt(11)\n" \
    "v_mfma_f32_16x16x32_bf16 v[148:151], v[184:187], v[0:3], 0\n" \
    "s_waitcnt lgkmcnt(10)\n" \
    "v_mfma_f32_16x16x32_bf16 v[144:147], v[164:167], v[4:7], v[144:147]\n" \
    "s_waitcnt lgkmcnt(9)\n" \
    "v_mfma_f32_16x16x32_bf16 v[148:151], v[188:191], v[4:7], v[148:151]\n" \
    "s_waitcnt lgkmcnt(8)\n" \
    "v_mfma_f32_16x16x32_bf16 v[144:147], v[168:171], v[8:11], v[144:147]\n" \
    "s_waitcnt vmcnt(0)\n" \
    "buffer_store_dwordx4 v[132:135], v121, s[8:11], s36 offen sc1\n" \
    "s_waitcnt lgkmcnt(7)\n" \
    "v_mfma_f32_16x16x32_bf16 v[148:151], v[192:195], v[8:11], v[148:151]\n" \
    "s_waitcnt lgkmcnt(6)\n" \
    "v_mfma_f32_16x16x32_bf16 v[144:147], v[172:175], v[12:15], v[144:147]\n" \
    "s_waitcnt lgkmcnt(5)\n" \
    "v_mfma_f32_16x16x32_bf16 v[148:151], v[196:199], v[12:15], v[148:151]\n" \
    "s_waitcnt lgkmcnt(4)\n" \
    "v_mfma_f32_16x16x32_bf16 v[144:147], v[176:179], v[16:19], v[144:147]\n" \
    "s_waitcnt lgkmcnt(3)\n" \
    "v_mfma_f32_16x16x32_bf16 v[148:151], v[200:203], v[16:19], v[148:151]\n" \
    "s_waitcnt lgkmcnt(2)\n" \
    "v_mfma_f32_16x16x32_bf16 v[144:147], v[180:183], v[20:23], v[144:147]\n" \
    "s_waitcnt lgkmcnt(1)\n" \
    "v_mfma_f32_16x16x32_bf16 v[148:151], v[204:207], v[20:23], v[148:151]\n" \
    "v_mfma_f32_16x16x32_bf16 v[152:155], v[160:163], v[24:27], 0\n" \
    "v_mfma_f32_16x16x32_bf16 v[156:159], v[184:187], v[24:27], 0\n" \
    "v_mfma_f32_16x16x32_bf16 v[152:155], v[164:167], v[28:31], v[152:155]\n" \
    "v_mfma_f32_16x16x32_bf16 v[156:159], v[188:191], v[28:31], v[156:159]\n" \
    "v_mfma_f32_16x16x32_bf16 v[152:155], v[168:171], v[32:35], v[152:155]\n" \
    "v_mfma_f32_16x16x32_bf16 v[156:159], v[192:195], v[32:35], v[156:159]\n" \
    "v_mfma_f32_16x16x32_bf16 v[152:155], v[172:175], v[36:39], v[152:155]\n" \
    "v_mfma_f32_16x16x32_bf16 v[156:159], v[196:199], v[36:39], v[156:159]\n" \
    "v_cvt_pk_bf16_f32 v136, v144, v145\n" \
    "v_mfma_f32_16x16x32_bf16 v[152:155], v[176:179], v[40:43], v[152:155]\n" \
    "v_cvt_pk_bf16_f32 v137, v148, v149\n" \
    "v_mfma_f32_16x16x32_bf16 v[156:159], v[200:203], v[40:43], v[156:159]\n" \
    "v_cvt_pk_bf16_f32 v138, v146, v147\n" \
    "v_mfma_f32_16x16x32_bf16 v[152:155], v[180:183], v[44:47], v[152:155]\n" \
    "v_cvt_pk_bf16_f32 v139, v150, v151\n" \
    "v_mfma_f32_16x16x32_bf16 v[156:159], v[204:207], v[44:47], v[156:159]\n" \
    "buffer_store_dwordx4 v[136:139], v120, s[8:11], s46 offen sc1\n" \
    "v_mfma_f32_16x16x32_bf16 v[144:147], v[160:163], v[48:51], 0\n" \
    "v_mfma_f32_16x16x32_bf16 v[148:151], v[184:187], v[48:51], 0\n" \
    "v_mfma_f32_16x16x32_bf16 v[144:147], v[164:167], v[52:55], v[144:147]\n" \
    "v_mfma_f32_16x16x32_bf16 v[148:151], v[188:191], v[52:55], v[148:151]\n" \
    "v_mfma_f32_16x16x32_bf16 v[144:147], v[168:171], v[56:59], v[144:147]\n" \
    "v_mfma_f32_16x16x32_bf16 v[148:151], v[192:195], v[56:59], v[148:151]\n" \
    "v_mfma_f32_16x16x32_bf16 v[144:147], v[172:175], v[60:63], v[144:147]\n" \
    "v_mfma_f32_16x16x32_bf16 v[148:151], v[196:199], v[60:63], v[148:151]\n" \
    "v_cvt_pk_bf16_f32 v140, v152, v153\n" \
    "v_mfma_f32_16x16x32_bf16 v[144:147], v[176:179], v[64:67], v[144:147]\n" \
    "v_cvt_pk_bf16_f32 v141, v156, v157\n" \
    "v_mfma_f32_16x16x32_bf16 v[148:151], v[200:203], v[64:67], v[148:151]\n" \
    "v_cvt_pk_bf16_f32 v142, v154, v155\n" \
    "v_mfma_f32_16x16x32_bf16 v[144:147], v[180:183], v[68:71], v[144:147]\n" \
    "v_cvt_pk_bf16_f32 v143, v158, v159\n" \
    "v_mfma_f32_16x16x32_bf16 v[148:151], v[204:207], v[68:71], v[148:151]\n" \
    "buffer_store_dwordx4 v[140:143], v120, s[8:11], s47 offen sc1\n" \
    "v_mfma_f32_16x16x32_bf16 v[152:155], v[160:163], v[72:75], 0\n" \
    "v_mfma_f32_16x16x32_bf16 v[156:159], v[184:187], v[72:75], 0\n" \
    "v_mfma_f32_16x16x32_bf16 v[152:155], v[164:167], v[76:79], v[152:155]\n" \
    "v_mfma_f32_16x16x32_bf16 v[156:159], v[188:191], v[76:79], v[156:159]\n" \
    "v_mfma_f32_16x16x32_bf16 v[152:155], v[168:171], v[80:83], v[152:155]\n" \
    "v_mfma_f32_16x16x32_bf16 v[156:159], v[192:195], v[80:83], v[156:159]\n" \
    "v_mfma_f32_16x16x32_bf16 v[152:155], v[172:175], v[84:87], v[152:155]\n" \
    "v_mfma_f32_16x16x32_bf16 v[156:159], v[196:199], v[84:87], v[156:159]\n" \
    "v_cvt_pk_bf16_f32 v136, v144, v145\n" \
    "v_mfma_f32_16x16x32_bf16 v[152:155], v[176:179], v[88:91], v[152:155]\n" \
    "v_cvt_pk_bf16_f32 v137, v148, v149\n" \
    "v_mfma_f32_16x16x32_bf16 v[156:159], v[200:203], v[88:91], v[156:159]\n" \
    "v_cvt_pk_bf16_f32 v138, v146, v147\n" \
    "v_mfma_f32_16x16x32_bf16 v[152:155], v[180:183], v[92:95], v[152:155]\n" \
    "v_cvt_pk_bf16_f32 v139, v150, v151\n" \
    "v_mfma_f32_16x16x32_bf16 v[156:159], v[204:207], v[92:95], v[156:159]\n" \
    "buffer_store_dwordx4 v[136:139], v120, s[8:11], s48 offen sc1\n" \
    "v_mfma_f32_16x16x32_bf16 v[144:147], v[160:163], v[96:99], 0\n" \
    "v_mfma_f32_16x16x32_bf16 v[148:151], v[184:187], v[96:99], 0\n" \
    "v_mfma_f32_16x16x32_bf16 v[144:147], v[164:167], v[100:103], v[144:147]\n" \
    "v_mfma_f32_16x16x32_bf16 v[148:151], v[188:191], v[100:103], v[148:151]\n" \
    "v_mfma_f32_16x16x32_bf16 v[144:147], v[168:171], v[104:107], v[144:147]\n" \
    "v_mfma_f32_16x16x32_bf16 v[148:151], v[192:195], v[104:107], v[148:151]\n" \
    "v_mfma_f32_16x16x32_bf16 v[144:147], v[172:175], v[108:111], v[144:147]\n" \
    "v_mfma_f32_16x16x32_bf16 v[148:151], v[196:199], v[108:111], v[148:151]\n" \
    "v_cvt_pk_bf16_f32 v140, v152, v153\n" \
    "v_mfma_f32_16x16x32_bf16 v[144:147], v[176:179], v[112:115], v[144:147]\n" \
    "v_cvt_pk_bf16_f32 v141, v156, v157\n" \
    "v_mfma_f32_16x16x32_bf16 v[148:151], v[200:203], v[112:115], v[148:151]\n" \
    "v_cvt_pk_bf16_f32 v142, v154, v155\n" \
    "v_mfma_f32_16x16x32_bf16 v[144:147], v[180:183], v[116:119], v[144:147]\n" \
    "v_cvt_pk_bf16_f32 v143, v158, v159\n" \
    "v_mfma_f32_16x16x32_bf16 v[148:151], v[204:207], v[116:119], v[148:151]\n" \
    "buffer_store_dwordx4 v[140:143], v120, s[8:11], s49 offen sc1\n" \
    "s_lshr_b32 s36, s34, 6\n" \
    "s_lshl_b32 s36, s36, 15\n" \
    "s_add_u32 s36, s36, s18\n" \
    "s_and_b32 s37, s34, 63\n" \
    "s_add_u32 s37, s37, 1\n" \
    "v_mov_b32 v132, s37\n" \
    "v_cvt_pk_bf16_f32 v136, v144, v145\n" \
    "v_cvt_pk_bf16_f32 v137, v148, v149\n" \
    "v_cvt_pk_bf16_f32 v138, v146, v147\n" \
    "v_cvt_pk_bf16_f32 v139, v150, v151\n" \
    "buffer_store_dwordx4 v[136:139], v120, s[8:11], s50 offen sc1\n" \
    "s_waitcnt lgkmcnt(0)\n" \
    "v_readfirstlane_b32 s42, v129\n" \
    "s_nop 0\n" \
    "s_bfe_u32 s43, s42, 0x80008\n" \
    "s_and_b32 s42, s42, 255\n" \
    "s_add_u32 s37, s33, %[brn]\n" \
    "s_min_u32 s42, s42, s37\n" \
    "s_cmp_lt_u32 s32, s42\n" \
    "s_cselect_b32 s34, s32, -1\n" \
    "s_add_u32 s37, s33, 64\n" \
    "s_cmp_lt_u32 s33, s43\n" \
    "s_cselect_b32 s34, s37, s34\n" \
    "s_addc_u32 s33, s33, 0\n" \
    "s_cmp_lt_u32 s34, 64\n" \
    "s_addc_u32 s32, s32, 0\n" \
    "s_mov_b32 s45, 0\n" \
    "s_xor_b32 s39, s39, 12288\n" \
    "s_add_u32 s44, s44, 0x10000\n" \
    "v_xor_b32 v125, 4, v125\n" \
    "s_lshr_b32 s37, s34, 6\n" \
    "s_mul_i32 s37, s37, %[brn]\n" \
    "s_and_b32 s38, s34, %[brn]-1\n" \
    "s_add_u32 s37, s37, s38\n" \
    "s_mul_i32 s37, s37, 2621440\n" \
    "s_add_u32 s37, s37, s17\n" \
    "s_lshr_b32 s38, s37, 8\n" \
    "s_xor_b32 s38, s38, s21\n" \
    "s_and_b32 s38, s38, 0x2000\n" \
    "s_or_b32 s46, s37, s38\n" \
    "s_add_u32 s37, s37, 65536\n" \
    "s_lshr_b32 s38, s37, 8\n" \
    "s_xor_b32 s38, s38, s21\n" \
    "s_and_b32 s38, s38, 0x2000\n" \
    "s_or_b32 s47, s37, s38\n" \
    "s_add_u32 s37, s37, 65536\n" \
    "s_lshr_b32 s38, s37, 8\n" \
    "s_xor_b32 s38, s38, s21\n" \
    "s_and_b32 s38, s38, 0x2000\n" \
    "s_or_b32 s48, s37, s38\n" \
    "s_add_u32 s37, s37, 65536\n" \
    "s_lshr_b32 s38, s37, 8\n" \
    "s_xor_b32 s38, s38, s21\n" \
    "s_and_b32 s38, s38, 0x2000\n" \
    "s_or_b32 s49, s37, s38\n" \
    "s_add_u32 s37, s37, 65536\n" \
    "s_lshr_b32 s38, s37, 8\n" \
    "s_xor_b32 s38, s38, s21\n" \
    "s_and_b32 s38, s38, 0x2000\n" \
    "s_or_b32 s50, s37, s38\n" \
    "s_waitcnt lgkmcnt(0)\n" \
    "s_branch .Lo_lO_%=\n" \
    ".Lo_eO_%=:\n" \
    "ds_read_b32 v129, v125\n" \
    "s_waitcnt lgkmcnt(0)\n" \
    "v_readfirstlane_b32 s42, v129\n" \
    "s_nop 0\n" \
    "s_bfe_u32 s43, s42, 0x80008\n" \
    "s_and_b32 s42, s42, 255\n" \
    "s_add_u32 s37, s33, %[brn]\n" \
    "s_min_u32 s42, s42, s37\n" \
    "s_cmp_lt_u32 s32, s42\n" \
    "s_cselect_b32 s34, s32, -1\n" \
    "s_add_u32 s37, s33, 64\n" \
    "s_cmp_lt_u32 s33, s43\n" \
    "s_cselect_b32 s34, s37, s34\n" \
    "s_addc_u32 s33, s33, 0\n" \
    "s_cmp_lt_u32 s34, 64\n" \
    "s_addc_u32 s32, s32, 0\n" \
    "s_waitcnt vmcnt(0)\n" \
    "buffer_store_dwordx4 v[132:135], v121, s[8:11], s36 offen sc1\n" \
    "s_mov_b32 s36, 0x80000000\n" \
    "s_xor_b32 s39, s39, 12288\n" \
    "s_add_u32 s44, s44, 0x10000\n" \
    "v_xor_b32 v125, 4, v125\n" \
    "s_lshr_b32 s37, s34, 6\n" \
    "s_mul_i32 s37, s37, %[brn]\n" \
    "s_and_b32 s38, s34, %[brn]-1\n" \
    "s_add_u32 s37, s37, s38\n" \
    "s_mul_i32 s37, s37, 2621440\n" \
    "s_add_u32 s37, s37, s17\n" \
    "s_lshr_b32 s38, s37, 8\n" \
    "s_xor_b32 s38, s38, s21\n" \
    "s_and_b32 s38, s38, 0x2000\n" \
    "s_or_b32 s46, s37, s38\n" \
    "s_add_u32 s37, s37, 65536\n" \
    "s_lshr_b32 s38, s37, 8\n" \
    "s_xor_b32 s38, s38, s21\n" \
    "s_and_b32 s38, s38, 0x2000\n" \
    "s_or_b32 s47, s37, s38\n" \
    "s_add_u32 s37, s37, 65536\n" \
    "s_lshr_b32 s38, s37, 8\n" \
    "s_xor_b32 s38, s38, s21\n" \
    "s_and_b32 s38, s38, 0x2000\n" \
    "s_or_b32 s48, s37, s38\n" \
    "s_add_u32 s37, s37, 65536\n" \
    "s_lshr_b32 s38, s37, 8\n" \
    "s_xor_b32 s38, s38, s21\n" \
    "s_and_b32 s38, s38, 0x2000\n" \
    "s_or_b32 s49, s37, s38\n" \
    "s_add_u32 s37, s37, 65536\n" \
    "s_lshr_b32 s38, s37, 8\n" \
    "s_xor_b32 s38, s38, s21\n" \
    "s_and_b32 s38, s38, 0x2000\n" \
    "s_or_b32 s50, s37, s38\n" \
    "s_waitcnt lgkmcnt(0)\n" \
    "s_add_u32 s38, s32, s33\n" \
    "s_cmp_eq_u32 s34, -1\n" \
    "s_cselect_b32 s38, s38, 0\n" \
    "s_cmp_eq_u32 s38, 128\n" \
    "s_cbranch_scc1 .Lo_end_%=\n" \
    "s_add_u32 s45, s45, 1\n" \
    "s_cmp_lt_u32 s45, 0x8000\n" \
    "s_cbranch_scc1 .Lo_lO_%=\n" \
    "s_branch .Lo_end_%=\n" \
    ".Lo_lS_%=:\n" \
    "v_add_u32 v124, s39, v120\n" \
    "s_cmp_eq_u32 s34, -1\n" \
    "s_barrier\n" \
    "s_cbranch_scc1 .Lo_eS_%=\n" \
    "ds_read_b128 v[160:163], v124 offset:0\n" \
    "ds_read_b128 v[184:187], v124 offset:6144\n" \
    "ds_read_b128 v[164:167], v124 offset:1024\n" \
    "ds_read_b128 v[188:191], v124 offset:7168\n" \
    "ds_read_b128 v[168:171], v124 offset:2048\n" \
    "ds_read_b128 v[192:195], v124 offset:8192\n" \
    "ds_read_b128 v[172:175], v124 offset:3072\n" \
    "ds_read_b128 v[196:199], v124 offset:9216\n" \
    "ds_read_b128 v[176:179], v124 offset:4096\n" \
    "ds_read_b128 v[200:203], v124 offset:10240\n" \
    "ds_read_b128 v[180:183], v124 offset:5120\n" \
    "ds_read_b128 v[204:207], v124 offset:11264\n" \
    "ds_read_b32 v129, v125\n" \
    "s_waitcnt lgkmcnt(12)\n" \
    "v_mfma_f32_16x16x32_bf16 v[144:147], v[160:163], v[0:3], 0\n" \
    "s_waitcnt lgkmcnt(11)\n" \
    "v_mfma_f32_16x16x32_bf16 v[148:151], v[184:187], v[0:3], 0\n" \
    "s_waitcnt lgkmcnt(10)\n" \
    "v_mfma_f32_16x16x32_bf16 v[144:147], v[164:167], v[4:7], v[144:147]\n" \
    "s_waitcnt lgkmcnt(9)\n" \
    "v_mfma_f32_16x16x32_bf16 v[148:151], v[188:191], v[4:7], v[148:151]\n" \
    "s_waitcnt lgkmcnt(8)\n" \
    "v_mfma_f32_16x16x32_bf16 v[144:147], v[168:171], v[8:11], v[144:147]\n" \
    "s_waitcnt vmcnt(0)\n" \
    "buffer_store_dwordx4 v[132:135], v121, s[8:11], s36 offen sc1\n" \
    "s_waitcnt lgkmcnt(7)\n" \
    "v_mfma_f32_16x16x32_bf16 v[148:151], v[192:195], v[8:11], v[148:151]\n" \
    "s_waitcnt lgkmcnt(6)\n" \
    "v_mfma_f32_16x16x32_bf16 v[144:147], v[172:175], v[12:15], v[144:147]\n" \
    "s_waitcnt lgkmcnt(5)\n" \
    "v_mfma_f32_16x16x32_bf16 v[148:151], v[196:199], v[12:15], v[148:151]\n" \
    "s_waitcnt lgkmcnt(4)\n" \
    "v_mfma_f32_16x16x32_bf16 v[144:147], v[176:179], v[16:19], v[144:147]\n" \
    "s_waitcnt lgkmcnt(3)\n" \
    "v_mfma_f32_16x16x32_bf16 v[148:151], v[200:203], v[16:19], v[148:151]\n" \
    "s_waitcnt lgkmcnt(2)\n" \
    "v_mfma_f32_16x16x32_bf16 v[144:147], v[180:183], v[20:23], v[144:147]\n" \
    "s_waitcnt lgkmcnt(1)\n" \
    "v_mfma_f32_16x16x32_bf16 v[148:151], v[204:207], v[20:23], v[148:151]\n" \
    "v_mfma_f32_16x16x32_bf16 v[152:155], v[160:163], v[24:27], 0\n" \
    "v_mfma_f32_16x16x32_bf16 v[156:159], v[184:187], v[24:27], 0\n" \
    "v_mfma_f32_16x16x32_bf16 v[152:155], v[164:167], v[28:31], v[152:155]\n" \
    "v_mfma_f32_16x16x32_bf16 v[156:159], v[188:191], v[28:31], v[156:159]\n" \
    "v_mfma_f32_16x16x32_bf16 v[152:155], v[168:171], v[32:35], v[152:155]\n" \
    "v_mfma_f32_16x16x32_bf16 v[156:159], v[192:195], v[32:35], v[156:159]\n" \
    "v_mfma_f32_16x16x32_bf16 v[152:155], v[172:175], v[36:39], v[152:155]\n" \
    "v_mfma_f32_16x16x32_bf16 v[156:159], v[196:199], v[36:39], v[156:159]\n" \
    "v_cvt_pk_bf16_f32 v136, v144, v145\n" \
    "v_mfma_f32_16x16x32_bf16 v[152:155], v[176:179], v[40:43], v[152:155]\n" \
    "v_cvt_pk_bf16_f32 v137, v148, v149\n" \
    "v_mfma_f32_16x16x32_bf16 v[156:159], v[200:203], v[40:43], v[156:159]\n" \
    "v_cvt_pk_bf16_f32 v138, v146, v147\n" \
    "v_mfma_f32_16x16x32_bf16 v[152:155], v[180:183], v[44:47], v[152:155]\n" \
    "v_cvt_pk_bf16_f32 v139, v150, v151\n" \
    "v_mfma_f32_16x16x32_bf16 v[156:159], v[204:207], v[44:47], v[156:159]\n" \
    "buffer_store_dwordx4 v[136:139], v120, s[8:11], s46 offen sc1\n" \
    "v_mfma_f32_16x16x32_bf16 v[144:147], v[160:163], v[48:51], 0\n" \
    "v_mfma_f32_16x16x32_bf16 v[148:151], v[184:187], v[48:51], 0\n" \
    "v_mfma_f32_16x16x32_bf16 v[144:147], v[164:167], v[52:55], v[144:147]\n" \
    "v_mfma_f32_16x16x32_bf16 v[148:151], v[188:191], v[52:55], v[148:151]\n" \
    "v_mfma_f32_16x16x32_bf16 v[144:147], v[168:171], v[56:59], v[144:147]\n" \
    "v_mfma_f32_16x16x32_bf16 v[148:151], v[192:195], v[56:59], v[148:151]\n" \
    "v_mfma_f32_16x16x32_bf16 v[144:147], v[172:175], v[60:63], v[144:147]\n" \
    "v_mfma_f32_16x16x32_bf16 v[148:151], v[196:199], v[60:63], v[148:151]\n" \
    "v_cvt_pk_bf16_f32 v140, v152, v153\n" \
    "v_mfma_f32_16x16x32_bf16 v[144:147], v[176:179], v[64:67], v[144:147]\n" \
    "v_cvt_pk_bf16_f32 v141, v156, v157\n" \
    "v_mfma_f32_16x16x32_bf16 v[148:151], v[200:203], v[64:67], v[148:151]\n" \
    "v_cvt_pk_bf16_f32 v142, v154, v155\n" \
    "v_mfma_f32_16x16x32_bf16 v[144:147], v[180:183], v[68:71], v[144:147]\n" \
    "v_cvt_pk_bf16_f32 v143, v158, v159\n" \
    "v_mfma_f32_16x16x32_bf16 v[148:151], v[204:207], v[68:71], v[148:151]\n" \
    "buffer_store_dwordx4 v[140:143], v120, s[8:11], s47 offen sc1\n" \
    "v_mfma_f32_16x16x32_bf16 v[152:155], v[160:163], v[72:75], 0\n" \
    "v_mfma_f32_16x16x32_bf16 v[156:159], v[184:187], v[72:75], 0\n" \
    "v_mfma_f32_16x16x32_bf16 v[152:155], v[164:167], v[76:79], v[152:155]\n" \
    "v_mfma_f32_16x16x32_bf16 v[156:159], v[188:191], v[76:79], v[156:159]\n" \
    "v_mfma_f32_16x16x32_bf16 v[152:155], v[168:171], v[80:83], v[152:155]\n" \
    "v_mfma_f32_16x16x32_bf16 v[156:159], v[192:195], v[80:83], v[156:159]\n" \
    "v_mfma_f32_16x16x32_bf16 v[152:155], v[172:175], v[84:87], v[152:155]\n" \
    "v_mfma_f32_16x16x32_bf16 v[156:159], v[196:199], v[84:87], v[156:159]\n" \
    "v_cvt_pk_bf16_f32 v136, v144, v145\n" \
    "v_mfma_f32_16x16x32_bf16 v[152:155], v[176:179], v[88:91], v[152:155]\n" \
    "v_cvt_pk_bf16_f32 v137, v148, v149\n" \
    "v_mfma_f32_16x16x32_bf16 v[156:159], v[200:203], v[88:91], v[156:159]\n" \
    "v_cvt_pk_bf16_f32 v138, v146, v147\n" \
    "v_mfma_f32_16x16x32_bf16 v[152:155], v[180:183], v[92:95], v[152:155]\n" \
    "v_cvt_pk_bf16_f32 v139, v150, v151\n" \
    "v_mfma_f32_16x16x32_bf16 v[156:159], v[204:207], v[92:95], v[156:159]\n" \
    "buffer_store_dwordx4 v[136:139], v120, s[8:11], s48 offen sc1\n" \
    "v_mfma_f32_16x16x32_bf16 v[144:147], v[160:163], v[96:99], 0\n" \
    "v_mfma_f32_16x16x32_bf16 v[148:151], v[184:187], v[96:99], 0\n" \
    "v_mfma_f32_16x16x32_bf16 v[144:147], v[164:167], v[100:103], v[144:147]\n" \
    "buffer_load_dword v126, v122, s[8:11], 0 offen sc1\n" \
    "v_mfma_f32_16x16x32_bf16 v[148:151], v[188:191], v[100:103], v[148:151]\n" \
    "v_mfma_f32_16x16x32_bf16 v[144:147], v[168:171], v[104:107], v[144:147]\n" \
    "v_mfma_f32_16x16x32_bf16 v[148:151], v[192:195], v[104:107], v[148:151]\n" \
    "v_mfma_f32_16x16x32_bf16 v[144:147], v[172:175], v[108:111], v[144:147]\n" \
    "v_mfma_f32_16x16x32_bf16 v[148:151], v[196:199], v[108:111], v[148:151]\n" \
    "v_cvt_pk_bf16_f32 v140, v152, v153\n" \
    "v_mfma_f32_16x16x32_bf16 v[144:147], v[176:179], v[112:115], v[144:147]\n" \
    "v_cvt_pk_bf16_f32 v141, v156, v157\n" \
    "v_mfma_f32_16x16x32_bf16 v[148:151], v[200:203], v[112:115], v[148:151]\n" \
    "v_cvt_pk_bf16_f32 v142, v154, v155\n" \
    "v_mfma_f32_16x16x32_bf16 v[144:147], v[180:183], v[116:119], v[144:147]\n" \
    "v_cvt_pk_bf16_f32 v143, v158, v159\n" \
    "v_mfma_f32_16x16x32_bf16 v[148:151], v[204:207], v[116:119], v[148:151]\n" \
    "buffer_store_dwordx4 v[140:143], v120, s[8:11], s49 offen sc1\n" \
    "s_lshr_b32 s36, s34, 6\n" \
    "s_lshl_b32 s36, s36, 15\n" \
    "s_add_u32 s36, s36, s18\n" \
    "s_and_b32 s37, s34, 63\n" \
    "s_add_u32 s37, s37, 1\n" \
    "v_mov_b32 v132, s37\n" \
    "v_cvt_pk_bf16_f32 v136, v144, v145\n" \
    "v_cvt_pk_bf16_f32 v137, v148, v149\n" \
    "v_cvt_pk_bf16_f32 v138, v146, v147\n" \
    "v_cvt_pk_bf16_f32 v139, v150, v151\n" \
    "buffer_store_dwordx4 v[136:139], v120, s[8:11], s50 offen sc1\n" \
    "s_waitcnt lgkmcnt(0)\n" \
    "v_readfirstlane_b32 s42, v129\n" \
    "s_nop 0\n" \
    "s_bfe_u32 s43, s42, 0x80008\n" \
    "s_and_b32 s42, s42, 255\n" \
    "s_add_u32 s37, s33, %[brn]\n" \
    "s_min_u32 s42, s42, s37\n" \
    "s_cmp_lt_u32 s32, s42\n" \
    "s_cselect_b32 s34, s32, -1\n" \
    "s_add_u32 s37, s33, 64\n" \
    "s_cmp_lt_u32 s33, s43\n" \
    "s_cselect_b32 s34, s37, s34\n" \
    "s_addc_u32 s33, s33, 0\n" \
    "s_cmp_lt_u32 s34, 64\n" \
    "s_addc_u32 s32, s32, 0\n" \
    "s_mov_b32 s45, 0\n" \
    "s_xor_b32 s39, s39, 12288\n" \
    "s_add_u32 s44, s44, 0x10000\n" \
    "v_xor_b32 v125, 4, v125\n" \
    "s_lshr_b32 s37, s34, 6\n" \
    "s_mul_i32 s37, s37, %[brn]\n" \
    "s_and_b32 s38, s34, %[brn]-1\n" \
    "s_add_u32 s37, s37, s38\n" \
    "s_mul_i32 s37, s37, 2621440\n" \
    "s_add_u32 s37, s37, s17\n" \
    "s_lshr_b32 s38, s37, 8\n" \
    "s_xor_b32 s38, s38, s21\n" \
    "s_and_b32 s38, s38, 0x2000\n" \
    "s_or_b32 s46, s37, s38\n" \
    "s_add_u32 s37, s37, 65536\n" \
    "s_lshr_b32 s38, s37, 8\n" \
    "s_xor_b32 s38, s38, s21\n" \
    "s_and_b32 s38, s38, 0x2000\n" \
    "s_or_b32 s47, s37, s38\n" \
    "s_add_u32 s37, s37, 65536\n" \
    "s_lshr_b32 s38, s37, 8\n" \
    "s_xor_b32 s38, s38, s21\n" \
    "s_and_b32 s38, s38, 0x2000\n" \
    "s_or_b32 s48, s37, s38\n" \
    "s_add_u32 s37, s37, 65536\n" \
    "s_lshr_b32 s38, s37, 8\n" \
    "s_xor_b32 s38, s38, s21\n" \
    "s_and_b32 s38, s38, 0x2000\n" \
    "s_or_b32 s49, s37, s38\n" \
    "s_add_u32 s37, s37, 65536\n" \
    "s_lshr_b32 s38, s37, 8\n" \
    "s_xor_b32 s38, s38, s21\n" \
    "s_and_b32 s38, s38, 0x2000\n" \
    "s_or_b32 s50, s37, s38\n" \
    "s_waitcnt vmcnt(2)\n" \
    "v_min_u32_dpp v126, v126, v126 quad_perm:[1,0,3,2] row_mask:0xf bank_mask:0xf bound_ctrl:1\n" \
    "s_nop 1\n" \
    "v_min_u32_dpp v126, v126, v126 quad_perm:[2,3,0,1] row_mask:0xf bank_mask:0xf bound_ctrl:1\n" \
    "s_nop 1\n" \
    "v_min_u32_dpp v126, v126, v126 row_half_mirror row_mask:0xf bank_mask:0xf bound_ctrl:1\n" \
    "s_nop 0\n" \
    "v_readlane_b32 s40, v126, 0\n" \
    "v_readlane_b32 s37, v126, 16\n" \
    "v_readlane_b32 s41, v126, 8\n" \
    "s_max_u32 s38, s40, %[hs]+%[hn]\n" \
    "s_cmp_lt_u32 s37, %[hs]+%[hn]\n" \
    "s_cselect_b32 s37, s37, s38\n" \
    "s_max_u32 s37, s37, %[hs]\n" \
    "s_cmp_ge_u32 s40, %[hs]\n" \
    "s_cselect_b32 s40, s37, s40\n" \
    "s_lshl_b32 s41, s41, 8\n" \
    "s_or_b32 s42, s40, s41\n" \
    "s_or_b32 s42, s42, s44\n" \
    "v_mov_b32 v128, s42\n" \
    "ds_write_b32 v125, v128\n" \
    "s_waitcnt lgkmcnt(0)\n" \
    "s_branch .Lo_lS_%=\n" \
    ".Lo_eS_%=:\n" \
    "ds_read_b32 v129, v125\n" \
    "s_waitcnt lgkmcnt(0)\n" \
    "v_readfirstlane_b32 s42, v129\n" \
    "s_nop 0\n" \
    "s_bfe_u32 s43, s42, 0x80008\n" \
    "s_and_b32 s42, s42, 255\n" \
    "s_add_u32 s37, s33, %[brn]\n" \
    "s_min_u32 s42, s42, s37\n" \
    "s_cmp_lt_u32 s32, s42\n" \
    "s_cselect_b32 s34, s32, -1\n" \
    "s_add_u32 s37, s33, 64\n" \
    "s_cmp_lt_u32 s33, s43\n" \
    "s_cselect_b32 s34, s37, s34\n" \
    "s_addc_u32 s33, s33, 0\n" \
    "s_cmp_lt_u32 s34, 64\n" \
    "s_addc_u32 s32, s32, 0\n" \
    "s_cmp_lg_u32 s34, -1\n" \
    "s_cbranch_scc1 .Lo_ns_%=\n" \
    "s_sleep 2\n" \
    ".Lo_ns_%=:\n" \
    "buffer_load_dword v126, v122, s[8:11], 0 offen sc1\n" \
    "s_waitcnt vmcnt(1)\n" \
    "buffer_store_dwordx4 v[132:135], v121, s[8:11], s36 offen sc1\n" \
    "s_mov_b32 s36, 0x80000000\n" \
    "s_xor_b32 s39, s39, 12288\n" \
    "s_add_u32 s44, s44, 0x10000\n" \
    "v_xor_b32 v125, 4, v125\n" \
    "s_lshr_b32 s37, s34, 6\n" \
    "s_mul_i32 s37, s37, %[brn]\n" \
    "s_and_b32 s38, s34, %[brn]-1\n" \
    "s_add_u32 s37, s37, s38\n" \
    "s_mul_i32 s37, s37, 2621440\n" \
    "s_add_u32 s37, s37, s17\n" \
    "s_lshr_b32 s38, s37, 8\n" \
    "s_xor_b32 s38, s38, s21\n" \
    "s_and_b32 s38, s38, 0x2000\n" \
    "s_or_b32 s46, s37, s38\n" \
    "s_add_u32 s37, s37, 65536\n" \
    "s_lshr_b32 s38, s37, 8\n" \
    "s_xor_b32 s38, s38, s21\n" \
    "s_and_b32 s38, s38, 0x2000\n" \
    "s_or_b32 s47, s37, s38\n" \
    "s_add_u32 s37, s37, 65536\n" \
    "s_lshr_b32 s38, s37, 8\n" \
    "s_xor_b32 s38, s38, s21\n" \
    "s_and_b32 s38, s38, 0x2000\n" \
    "s_or_b32 s48, s37, s38\n" \
    "s_add_u32 s37, s37, 65536\n" \
    "s_lshr_b32 s38, s37, 8\n" \
    "s_xor_b32 s38, s38, s21\n" \
    "s_and_b32 s38, s38, 0x2000\n" \
    "s_or_b32 s49, s37, s38\n" \
    "s_add_u32 s37, s37, 65536\n" \
    "s_lshr_b32 s38, s37, 8\n" \
    "s_xor_b32 s38, s38, s21\n" \
    "s_and_b32 s38, s38, 0x2000\n" \
    "s_or_b32 s50, s37, s38\n" \
    "s_waitcnt vmcnt(1)\n" \
    "v_min_u32_dpp v126, v126, v126 quad_perm:[1,0,3,2] row_mask:0xf bank_mask:0xf bound_ctrl:1\n" \
    "s_nop 1\n" \
    "v_min_u32_dpp v126, v126, v126 quad_perm:[2,3,0,1] row_mask:0xf bank_mask:0xf bound_ctrl:1\n" \
    "s_nop 1\n" \
    "v_min_u32_dpp v126, v126, v126 row_half_mirror row_mask:0xf bank_mask:0xf bound_ctrl:1\n" \
    "s_nop 0\n" \
    "v_readlane_b32 s40, v126, 0\n" \
    "v_readlane_b32 s37, v126, 16\n" \
    "v_readlane_b32 s41, v126, 8\n" \
    "s_max_u32 s38, s40, %[hs]+%[hn]\n" \
    "s_cmp_lt_u32 s37, %[hs]+%[hn]\n" \
    "s_cselect_b32 s37, s37, s38\n" \
    "s_max_u32 s37, s37, %[hs]\n" \
    "s_cmp_ge_u32 s40, %[hs]\n" \
    "s_cselect_b32 s40, s37, s40\n" \
    "s_lshl_b32 s41, s41, 8\n" \
    "s_or_b32 s42, s40, s41\n" \
    "s_or_b32 s42, s42, s44\n" \
    "v_mov_b32 v128, s42\n" \
    "ds_write_b32 v125, v128\n" \
    "s_waitcnt lgkmcnt(0)\n" \
    "s_add_u32 s38, s32, s33\n" \
    "s_cmp_eq_u32 s34, -1\n" \
    "s_cselect_b32 s38, s38, 0\n" \
    "s_cmp_eq_u32 s38, 128\n" \
    "s_cbranch_scc1 .Lo_end_%=\n" \
    "s_add_u32 s45, s45, 1\n" \
    "s_cmp_lt_u32 s45, 0x8000\n" \
    "s_cbranch_scc1 .Lo_lS_%=\n" \
    "s_branch .Lo_end_%=\n" \
    ".Lo_end_%=:\n" \
    "s_waitcnt vmcnt(0)\n" \
    "s_endpgm\n" \
    ""
#define UP_ASM \
    "s_mov_b32 s32, 0\n" \
    "s_mov_b32 s38, 0x40000\n" \
    "s_mov_b32 s44, 0xaaaaaaaa\n" \
    "s_mov_b32 s45, 0xaaaaaaaa\n" \
    "s_mov_b32 s46, 0xcccccccc\n" \
    "s_mov_b32 s47, 0xcccccccc\n" \
    "v_mov_b32 v182, -1\n" \
    "v_mov_b32 v183, -1\n" \
    "v_mov_b32 v178, 0x3f803f80\n" \
    "v_mov_b32 v179, 0x3f803f80\n" \
    "v_mov_b32 v180, 0x3f803f80\n" \
    "v_mov_b32 v181, 0x3f803f80\n" \
    "s_cmp_lt_u32 s16, 4\n" \
    "s_cbranch_scc1 .Lup_lO_%=\n" \
    ".Lup_lY_%=:\n" \
    "s_add_u32 s48, s32, 2\n" \
    "s_min_u32 s48, s48, 64\n" \
    "s_and_b32 s49, s48, 1\n" \
    "s_xor_b32 s49, s49, 1\n" \
    "s_lshl_b32 s49, s49, 9\n" \
    "s_add_u32 s49, s49, s18\n" \
    "buffer_load_dwordx2 v[176:177], off, s[8:11], s49 sc1\n" \
    "s_mov_b32 s33, 0\n" \
    "s_waitcnt vmcnt(0)\n" \
    ".Lup_kY_%=:\n" \
    "s_mov_b32 s37, 0\n" \
    "s_waitcnt vmcnt(2)\n" \
    ".Lup_Yap_%=:\n" \
    "v_readfirstlane_b32 s36, v177\n" \
    "s_nop 0\n" \
    "s_cmp_gt_i32 s36, s48\n" \
    "s_cbranch_scc1 .Lup_Yax_%=\n" \
    "s_cmp_eq_u32 s36, s48\n" \
    "s_cbranch_scc1 .Lup_Yaw_%=\n" \
    "s_sleep 3\n" \
    "buffer_load_dwordx2 v[176:177], off, s[8:11], s49 sc1\n" \
    "s_waitcnt vmcnt(0)\n" \
    "s_add_u32 s37, s37, 1\n" \
    "s_cmp_lt_u32 s37, s38\n" \
    "s_cbranch_scc1 .Lup_Yap_%=\n" \
    "s_mov_b32 s38, 0\n" \
    "s_branch .Lup_Yax_%=\n" \
    ".Lup_Yaw_%=:\n" \
    "v_readfirstlane_b32 s36, v176\n" \
    "s_cmp_eq_u32 s32, 63\n" \
    "s_cselect_b32 s34, %[dlast], 0\n" \
    "s_add_u32 s36, s36, s34\n" \
    "s_add_u32 s36, s36, %[dx]\n" \
    "s_branch .Lup_Yatb_%=\n" \
    ".Lup_Yata_%=:\n" \
    "s_sleep 1\n" \
    ".Lup_Yatb_%=:\n" \
    "s_memrealtime vcc\n" \
    "s_waitcnt lgkmcnt(0)\n" \
    "s_sub_i32 vcc_lo, s36, vcc_lo\n" \
    "s_cmp_gt_i32 vcc_lo, 8\n" \
    "s_cbranch_scc1 .Lup_Yata_%=\n" \
    "s_cmp_gt_i32 vcc_lo, 0\n" \
    "s_cbranch_scc1 .Lup_Yatb_%=\n" \
    ".Lup_Yax_%=:\n" \
    "s_mul_i32 s41, s32, 40960\n" \
    "s_add_u32 s41, s41, s17\n" \
    "buffer_load_dwordx4 v[216:219], v168, s[8:11], s41 offen sc1\n" \
    "s_add_u32 s41, s41, 8192\n" \
    "buffer_load_dwordx4 v[220:223], v168, s[8:11], s41 offen sc1\n" \
    "s_mov_b32 s37, 0\n" \
    ".Lup_srYa_%=:\n" \
    "s_waitcnt vmcnt(1)\n" \
    "v_max3_u32 v254, v216, v217, v218\n" \
    "s_waitcnt vmcnt(0)\n" \
    "v_max3_u32 v254, v254, v219, v220\n" \
    "v_max3_u32 v254, v254, v221, v222\n" \
    "v_max_u32 v254, v254, v223\n" \
    "v_cmp_eq_u32 vcc, -1, v254\n" \
    "s_nop 0\n" \
    "s_cbranch_vccnz .Lup_scYa_%=\n" \
    ".Lup_sxYa_%=:\n" \
    "v_mfma_f32_16x16x32_bf16 v[244:247], v[216:219], v[216:219], 0\n" \
    "ds_write_b128 v169, v[216:219] offset:0\n" \
    "v_mfma_f32_16x16x32_bf16 v[244:247], v[220:223], v[220:223], v[244:247]\n" \
    "ds_write_b128 v169, v[220:223] offset:8192\n" \
    "v_mfma_f32_16x16x32_bf16 v[240:243], v[178:181], v[216:219], 0\n" \
    "v_mfma_f32_16x16x32_bf16 v[240:243], v[178:181], v[220:223], v[240:243]\n" \
    "s_nop 4\n" \
    "v_cndmask_b32 v254, v244, v245, s[44:45]\n" \
    "v_cndmask_b32 v255, v246, v247, s[44:45]\n" \
    "s_nop 0\n" \
    "v_cndmask_b32 v241, v254, v255, s[46:47]\n" \
    "ds_write_b64 v171, v[240:241] offset:0\n" \
    "s_waitcnt lgkmcnt(0)\n" \
    "s_barrier\n" \
    "ds_read_b128 v[184:187], v168 offset:0\n" \
    "ds_read_b128 v[188:191], v168 offset:1024\n" \
    "ds_read_b128 v[192:195], v168 offset:2048\n" \
    "ds_read_b128 v[196:199], v168 offset:3072\n" \
    "ds_read_b128 v[200:203], v168 offset:4096\n" \
    "ds_read2_b64 v[248:251], v172 offset0:0 offset1:16\n" \
    "s_waitcnt lgkmcnt(5)\n" \
    "v_mfma_f32_16x16x32_bf16 v[208:211], v[0:3], v[184:187], 0\n" \
    "ds_read_b128 v[204:207], v168 offset:5120\n" \
    "v_mfma_f32_16x16x32_bf16 v[212:215], v[80:83], v[184:187], 0\n" \
    "s_mul_i32 s41, s33, 40960\n" \
    "s_add_u32 s41, s41, s17\n" \
    "s_add_u32 s41, s41, 2621440\n" \
    "buffer_load_dwordx4 v[228:231], v168, s[8:11], s41 offen sc1\n" \
    "s_add_u32 s41, s41, 8192\n" \
    "buffer_load_dwordx4 v[232:235], v168, s[8:11], s41 offen sc1\n" \
    "s_waitcnt lgkmcnt(5)\n" \
    "v_mfma_f32_16x16x32_bf16 v[208:211], v[4:7], v[188:191], v[208:211]\n" \
    "ds_read_b128 v[184:187], v168 offset:6144\n" \
    "v_mfma_f32_16x16x32_bf16 v[212:215], v[84:87], v[188:191], v[212:215]\n" \
    "s_mul_i32 s39, s32, 98304\n" \
    "s_add_u32 s39, s39, 0\n" \
    "s_waitcnt lgkmcnt(5)\n" \
    "v_mfma_f32_16x16x32_bf16 v[208:211], v[8:11], v[192:195], v[208:211]\n" \
    "ds_read_b128 v[188:191], v168 offset:7168\n" \
    "v_mfma_f32_16x16x32_bf16 v[212:215], v[88:91], v[192:195], v[212:215]\n" \
    "s_add_u32 s40, s39, %[pdist]*98304\n" \
    "s_cmp_lt_u32 s32, 64-%[pdist]\n" \
    "s_cselect_b32 s40, s40, 0x80000000\n" \
    "s_waitcnt lgkmcnt(5)\n" \
    "v_mfma_f32_16x16x32_bf16 v[208:211], v[12:15], v[196:199], v[208:211]\n" \
    "ds_read_b128 v[192:195], v168 offset:8192\n" \
    "v_mfma_f32_16x16x32_bf16 v[212:215], v[92:95], v[196:199], v[212:215]\n" \
    "s_waitcnt lgkmcnt(4)\n" \
    "v_add_f32 v248, v248, v250\n" \
    "v_mfma_f32_16x16x32_bf16 v[208:211], v[16:19], v[200:203], v[208:211]\n" \
    "ds_read_b128 v[196:199], v168 offset:9216\n" \
    "v_mfma_f32_16x16x32_bf16 v[212:215], v[96:99], v[200:203], v[212:215]\n" \
    "v_add_f32 v249, v249, v251\n" \
    "s_waitcnt lgkmcnt(4)\n" \
    "v_mfma_f32_16x16x32_bf16 v[208:211], v[20:23], v[204:207], v[208:211]\n" \
    "ds_read_b128 v[200:203], v168 offset:10240\n" \
    "v_mfma_f32_16x16x32_bf16 v[212:215], v[100:103], v[204:207], v[212:215]\n" \
    "v_permlane16_swap_b32 v248, v249\n" \
    "s_waitcnt lgkmcnt(4)\n" \
    "v_mfma_f32_16x16x32_bf16 v[208:211], v[24:27], v[184:187], v[208:211]\n" \
    "ds_read_b128 v[204:207], v168 offset:11264\n" \
    "v_mfma_f32_16x16x32_bf16 v[212:215], v[104:107], v[184:187], v[212:215]\n" \
    "v_add_f32 v250, v248, v249\n" \
    "s_waitcnt lgkmcnt(4)\n" \
    "v_mfma_f32_16x16x32_bf16 v[208:211], v[28:31], v[188:191], v[208:211]\n" \
    "ds_read_b128 v[184:187], v168 offset:12288\n" \
    "v_mfma_f32_16x16x32_bf16 v[212:215], v[108:111], v[188:191], v[212:215]\n" \
    "v_add_f32 v248, v248, v249\n" \
    "s_waitcnt lgkmcnt(4)\n" \
    "v_mfma_f32_16x16x32_bf16 v[208:211], v[32:35], v[192:195], v[208:211]\n" \
    "ds_read_b128 v[188:191], v168 offset:13312\n" \
    "v_mfma_f32_16x16x32_bf16 v[212:215], v[112:115], v[192:195], v[212:215]\n" \
    "v_permlane32_swap_b32 v248, v250\n" \
    "s_waitcnt lgkmcnt(4)\n" \
    "v_mfma_f32_16x16x32_bf16 v[208:211], v[36:39], v[196:199], v[208:211]\n" \
    "ds_read_b128 v[192:195], v168 offset:14336\n" \
    "v_mfma_f32_16x16x32_bf16 v[212:215], v[116:119], v[196:199], v[212:215]\n" \
    "v_add_f32 v249, v248, v250\n" \
    "s_waitcnt lgkmcnt(4)\n" \
    "v_mfma_f32_16x16x32_bf16 v[208:211], v[40:43], v[200:203], v[208:211]\n" \
    "ds_read_b128 v[196:199], v168 offset:15360\n" \
    "v_mfma_f32_16x16x32_bf16 v[212:215], v[120:123], v[200:203], v[212:215]\n" \
    "v_add_f32 v248, v248, v250\n" \
    "s_add_u32 s48, s32, 3\n" \
    "s_min_u32 s48, s48, 64\n" \
    "s_and_b32 s49, s48, 1\n" \
    "s_xor_b32 s49, s49, 1\n" \
    "s_lshl_b32 s49, s49, 9\n" \
    "s_add_u32 s49, s49, s18\n" \
    "buffer_load_dwordx2 v[176:177], off, s[8:11], s49 sc1\n" \
    "s_waitcnt lgkmcnt(4)\n" \
    "v_mfma_f32_16x16x32_bf16 v[208:211], v[44:47], v[204:207], v[208:211]\n" \
    "ds_read_b128 v[200:203], v168 offset:16384\n" \
    "v_mfma_f32_16x16x32_bf16 v[212:215], v[124:127], v[204:207], v[212:215]\n" \
    "v_permlane16_swap_b32 v248, v249\n" \
    "s_waitcnt lgkmcnt(4)\n" \
    "v_mfma_f32_16x16x32_bf16 v[208:211], v[48:51], v[184:187], v[208:211]\n" \
    "ds_read_b128 v[204:207], v168 offset:17408\n" \
    "v_mfma_f32_16x16x32_bf16 v[212:215], v[128:131], v[184:187], v[212:215]\n" \
    "v_mul_f32 v252, 0x3acccccd, v248\n" \
    "s_waitcnt lgkmcnt(4)\n" \
    "v_mfma_f32_16x16x32_bf16 v[208:211], v[52:55], v[188:191], v[208:211]\n" \
    "ds_read_b128 v[184:187], v168 offset:18432\n" \
    "v_mfma_f32_16x16x32_bf16 v[212:215], v[132:135], v[188:191], v[212:215]\n" \
    "v_mul_f32 v249, 0x3acccccd, v249\n" \
    "s_waitcnt lgkmcnt(4)\n" \
    "v_mfma_f32_16x16x32_bf16 v[208:211], v[56:59], v[192:195], v[208:211]\n" \
    "ds_read_b128 v[188:191], v168 offset:19456\n" \
    "v_mfma_f32_16x16x32_bf16 v[212:215], v[136:139], v[192:195], v[212:215]\n" \
    "v_fma_f32 v249, -v252, v252, v249\n" \
    "s_waitcnt lgkmcnt(4)\n" \
    "v_mfma_f32_16x16x32_bf16 v[208:211], v[60:63], v[196:199], v[208:211]\n" \
    "v_mfma_f32_16x16x32_bf16 v[212:215], v[140:143], v[196:199], v[212:215]\n" \
    "v_max_f32 v249, 0, v249\n" \
    "s_waitcnt lgkmcnt(3)\n" \
    "v_mfma_f32_16x16x32_bf16 v[208:211], v[64:67], v[200:203], v[208:211]\n" \
    "v_mfma_f32_16x16x32_bf16 v[212:215], v[144:147], v[200:203], v[212:215]\n" \
    "v_add_f32 v249, 0x3727c5ac, v249\n" \
    "s_waitcnt lgkmcnt(2)\n" \
    "v_mfma_f32_16x16x32_bf16 v[208:211], v[68:71], v[204:207], v[208:211]\n" \
    "v_mfma_f32_16x16x32_bf16 v[212:215], v[148:151], v[204:207], v[212:215]\n" \
    "v_rsq_f32 v253, v249\n" \
    "s_waitcnt lgkmcnt(1)\n" \
    "v_mfma_f32_16x16x32_bf16 v[208:211], v[72:75], v[184:187], v[208:211]\n" \
    "v_mfma_f32_16x16x32_bf16 v[212:215], v[152:155], v[184:187], v[212:215]\n" \
    "v_mul_f32 v251, 0xbfb8aa3b, v253\n" \
    "s_waitcnt lgkmcnt(0)\n" \
    "v_mfma_f32_16x16x32_bf16 v[208:211], v[76:79], v[188:191], v[208:211]\n" \
    "v_mfma_f32_16x16x32_bf16 v[212:215], v[156:159], v[188:191], v[212:215]\n" \
    "s_nop 6\n" \
    "v_fma_f32 v208, -v252, v160, v208\n" \
    "v_fma_f32 v209, -v252, v161, v209\n" \
    "v_fma_f32 v210, -v252, v162, v210\n" \
    "v_fma_f32 v211, -v252, v163, v211\n" \
    "v_mul_f32 v208, v251, v208\n" \
    "v_mul_f32 v209, v251, v209\n" \
    "v_mul_f32 v210, v251, v210\n" \
    "v_mul_f32 v211, v251, v211\n" \
    "v_exp_f32 v208, v208\n" \
    "v_fma_f32 v212, -v252, v164, v212\n" \
    "v_exp_f32 v209, v209\n" \
    "v_fma_f32 v213, -v252, v165, v213\n" \
    "v_exp_f32 v210, v210\n" \
    "v_fma_f32 v214, -v252, v166, v214\n" \
    "v_exp_f32 v211, v211\n" \
    "v_fma_f32 v215, -v252, v167, v215\n" \
    "v_add_f32 v208, 1.0, v208\n" \
    "v_add_f32 v209, 1.0, v209\n" \
    "v_add_f32 v210, 1.0, v210\n" \
    "v_add_f32 v211, 1.0, v211\n" \
    "v_rcp_f32 v208, v208\n" \
    "v_mul_f32 v212, v253, v212\n" \
    "v_rcp_f32 v209, v209\n" \
    "v_mul_f32 v213, v253, v213\n" \
    "v_rcp_f32 v210, v210\n" \
    "v_mul_f32 v214, v253, v214\n" \
    "v_rcp_f32 v211, v211\n" \
    "v_mul_f32 v215, v253, v215\n" \
    "v_mul_f32 v208, v208, v212\n" \
    "v_mul_f32 v209, v209, v213\n" \
    "v_mul_f32 v210, v210, v214\n" \
    "v_mul_f32 v211, v211, v215\n" \
    "v_cvt_pk_bf16_f32 v240, v208, v209\n" \
    "v_cvt_pk_bf16_f32 v241, v210, v211\n" \
    "v_min_u32 v240, -2, v240\n" \
    "v_min_u32 v241, -2, v241\n" \
    "buffer_store_dwordx2 v[240:241], v173, s[8:11], s39 offen sc1\n" \
    "buffer_store_dwordx2 v[182:183], v173, s[8:11], s40 offen sc1\n" \
    "s_add_u32 s32, s32, 1\n" \
    "s_cmp_lt_u32 s32, %[eup]\n" \
    "s_barrier\n" \
    "s_cbranch_scc1 .Lup_kY_%=\n" \
    ".Lup_mY_%=:\n" \
    "s_mov_b32 s37, 0\n" \
    "s_waitcnt vmcnt(2)\n" \
    ".Lup_Ybp_%=:\n" \
    "v_readfirstlane_b32 s36, v177\n" \
    "s_nop 0\n" \
    "s_cmp_gt_i32 s36, s48\n" \
    "s_cbranch_scc1 .Lup_Ybx_%=\n" \
    "s_cmp_eq_u32 s36, s48\n" \
    "s_cbranch_scc1 .Lup_Ybw_%=\n" \
    "s_sleep 3\n" \
    "buffer_load_dwordx2 v[176:177], off, s[8:11], s49 sc1\n" \
    "s_waitcnt vmcnt(0)\n" \
    "s_add_u32 s37, s37, 1\n" \
    "s_cmp_lt_u32 s37, s38\n" \
    "s_cbranch_scc1 .Lup_Ybp_%=\n" \
    "s_mov_b32 s38, 0\n" \
    "s_branch .Lup_Ybx_%=\n" \
    ".Lup_Ybw_%=:\n" \
    "v_readfirstlane_b32 s36, v176\n" \
    "s_cmp_eq_u32 s32, 63\n" \
    "s_cselect_b32 s34, %[dlast], 0\n" \
    "s_add_u32 s36, s36, s34\n" \
    "s_add_u32 s36, s36, %[dx]\n" \
    "s_branch .Lup_Ybtb_%=\n" \
    ".Lup_Ybta_%=:\n" \
    "s_sleep 1\n" \
    ".Lup_Ybtb_%=:\n" \
    "s_memrealtime vcc\n" \
    "s_waitcnt lgkmcnt(0)\n" \
    "s_sub_i32 vcc_lo, s36, vcc_lo\n" \
    "s_cmp_gt_i32 vcc_lo, 8\n" \
    "s_cbranch_scc1 .Lup_Ybta_%=\n" \
    "s_cmp_gt_i32 vcc_lo, 0\n" \
    "s_cbranch_scc1 .Lup_Ybtb_%=\n" \
    ".Lup_Ybx_%=:\n" \
    "s_mul_i32 s41, s32, 40960\n" \
    "s_add_u32 s41, s41, s17\n" \
    "buffer_load_dwordx4 v[216:219], v168, s[8:11], s41 offen sc1\n" \
    "s_add_u32 s41, s41, 8192\n" \
    "buffer_load_dwordx4 v[220:223], v168, s[8:11], s41 offen sc1\n" \
    "s_mov_b32 s37, 0\n" \
    ".Lup_srYb_%=:\n" \
    "s_waitcnt vmcnt(1)\n" \
    "v_max3_u32 v254, v216, v217, v218\n" \
    "s_waitcnt vmcnt(0)\n" \
    "v_max3_u32 v254, v254, v219, v220\n" \
    "v_max3_u32 v254, v254, v221, v222\n" \
    "v_max_u32 v254, v254, v223\n" \
    "v_cmp_eq_u32 vcc, -1, v254\n" \
    "s_nop 0\n" \
    "s_cbranch_vccnz .Lup_scYb_%=\n" \
    ".Lup_sxYb_%=:\n" \
    "v_mfma_f32_16x16x32_bf16 v[244:247], v[216:219], v[216:219], 0\n" \
    "ds_write_b128 v169, v[216:219] offset:0\n" \
    "v_mfma_f32_16x16x32_bf16 v[244:247], v[220:223], v[220:223], v[244:247]\n" \
    "ds_write_b128 v169, v[220:223] offset:8192\n" \
    "v_mfma_f32_16x16x32_bf16 v[240:243], v[178:181], v[216:219], 0\n" \
    "v_mfma_f32_16x16x32_bf16 v[240:243], v[178:181], v[220:223], v[240:243]\n" \
    "s_nop 4\n" \
    "v_cndmask_b32 v254, v244, v245, s[44:45]\n" \
    "v_cndmask_b32 v255, v246, v247, s[44:45]\n" \
    "s_nop 0\n" \
    "v_cndmask_b32 v241, v254, v255, s[46:47]\n" \
    "ds_write_b64 v171, v[240:241] offset:0\n" \
    "s_waitcnt lgkmcnt(0)\n" \
    "s_barrier\n" \
    ".Lup_srYc_%=:\n" \
    "v_max3_u32 v254, v228, v229, v230\n" \
    "v_max3_u32 v254, v254, v231, v232\n" \
    "v_max3_u32 v254, v254, v233, v234\n" \
    "v_max_u32 v254, v254, v235\n" \
    "v_cmp_eq_u32 vcc, -1, v254\n" \
    "s_nop 0\n" \
    "s_cbranch_vccnz .Lup_scYc_%=\n" \
    ".Lup_sxYc_%=:\n" \
    "v_mfma_f32_16x16x32_bf16 v[244:247], v[228:231], v[228:231], 0\n" \
    "ds_write_b128 v169, v[228:231] offset:20480\n" \
    "v_mfma_f32_16x16x32_bf16 v[244:247], v[232:235], v[232:235], v[244:247]\n" \
    "ds_write_b128 v169, v[232:235] offset:28672\n" \
    "v_mfma_f32_16x16x32_bf16 v[240:243], v[178:181], v[228:231], 0\n" \
    "v_mfma_f32_16x16x32_bf16 v[240:243], v[178:181], v[232:235], v[240:243]\n" \
    "s_nop 4\n" \
    "v_cndmask_b32 v254, v244, v245, s[44:45]\n" \
    "v_cndmask_b32 v255, v246, v247, s[44:45]\n" \
    "s_nop 0\n" \
    "v_cndmask_b32 v241, v254, v255, s[46:47]\n" \
    "ds_write_b64 v171, v[240:241] offset:1024\n" \
    "ds_read_b128 v[184:187], v168 offset:0\n" \
    "ds_read_b128 v[188:191], v168 offset:1024\n" \
    "ds_read_b128 v[192:195], v168 offset:2048\n" \
    "ds_read_b128 v[196:199], v168 offset:3072\n" \
    "ds_read_b128 v[200:203], v168 offset:4096\n" \
    "ds_read2_b64 v[248:251], v172 offset0:0 offset1:16\n" \
    "s_waitcnt lgkmcnt(5)\n" \
    "v_mfma_f32_16x16x32_bf16 v[208:211], v[0:3], v[184:187], 0\n" \
    "ds_read_b128 v[204:207], v168 offset:5120\n" \
    "v_mfma_f32_16x16x32_bf16 v[212:215], v[80:83], v[184:187], 0\n" \
    "s_waitcnt lgkmcnt(5)\n" \
    "v_mfma_f32_16x16x32_bf16 v[208:211], v[4:7], v[188:191], v[208:211]\n" \
    "ds_read_b128 v[184:187], v168 offset:6144\n" \
    "v_mfma_f32_16x16x32_bf16 v[212:215], v[84:87], v[188:191], v[212:215]\n" \
    "s_mul_i32 s39, s32, 98304\n" \
    "s_add_u32 s39, s39, 0\n" \
    "s_waitcnt lgkmcnt(5)\n" \
    "v_mfma_f32_16x16x32_bf16 v[208:211], v[8:11], v[192:195], v[208:211]\n" \
    "ds_read_b128 v[188:191], v168 offset:7168\n" \
    "v_mfma_f32_16x16x32_bf16 v[212:215], v[88:91], v[192:195], v[212:215]\n" \
    "s_add_u32 s40, s39, %[pdist]*98304\n" \
    "s_cmp_lt_u32 s32, 64-%[pdist]\n" \
    "s_cselect_b32 s40, s40, 0x80000000\n" \
    "s_waitcnt lgkmcnt(5)\n" \
    "v_mfma_f32_16x16x32_bf16 v[208:211], v[12:15], v[196:199], v[208:211]\n" \
    "ds_read_b128 v[192:195], v168 offset:8192\n" \
    "v_mfma_f32_16x16x32_bf16 v[212:215], v[92:95], v[196:199], v[212:215]\n" \
    "s_waitcnt lgkmcnt(4)\n" \
    "v_add_f32 v248, v248, v250\n" \
    "v_mfma_f32_16x16x32_bf16 v[208:211], v[16:19], v[200:203], v[208:211]\n" \
    "ds_read_b128 v[196:199], v168 offset:9216\n" \
    "v_mfma_f32_16x16x32_bf16 v[212:215], v[96:99], v[200:203], v[212:215]\n" \
    "v_add_f32 v249, v249, v251\n" \
    "s_waitcnt lgkmcnt(4)\n" \
    "v_mfma_f32_16x16x32_bf16 v[208:211], v[20:23], v[204:207], v[208:211]\n" \
    "ds_read_b128 v[200:203], v168 offset:10240\n" \
    "v_mfma_f32_16x16x32_bf16 v[212:215], v[100:103], v[204:207], v[212:215]\n" \
    "v_permlane16_swap_b32 v248, v249\n" \
    "s_waitcnt lgkmcnt(4)\n" \
    "v_mfma_f32_16x16x32_bf16 v[208:211], v[24:27], v[184:187], v[208:211]\n" \
    "ds_read_b128 v[204:207], v168 offset:11264\n" \
    "v_mfma_f32_16x16x32_bf16 v[212:215], v[104:107], v[184:187], v[212:215]\n" \
    "v_add_f32 v250, v248, v249\n" \
    "s_waitcnt lgkmcnt(4)\n" \
    "v_mfma_f32_16x16x32_bf16 v[208:211], v[28:31], v[188:191], v[208:211]\n" \
    "ds_read_b128 v[184:187], v168 offset:12288\n" \
    "v_mfma_f32_16x16x32_bf16 v[212:215], v[108:111], v[188:191], v[212:215]\n" \
    "v_add_f32 v248, v248, v249\n" \
    "s_waitcnt lgkmcnt(4)\n" \
    "v_mfma_f32_16x16x32_bf16 v[208:211], v[32:35], v[192:195], v[208:211]\n" \
    "ds_read_b128 v[188:191], v168 offset:13312\n" \
    "v_mfma_f32_16x16x32_bf16 v[212:215], v[112:115], v[192:195], v[212:215]\n" \
    "v_permlane32_swap_b32 v248, v250\n" \
    "s_waitcnt lgkmcnt(4)\n" \
    "v_mfma_f32_16x16x32_bf16 v[208:211], v[36:39], v[196:199], v[208:211]\n" \
    "ds_read_b128 v[192:195], v168 offset:14336\n" \
    "v_mfma_f32_16x16x32_bf16 v[212:215], v[116:119], v[196:199], v[212:215]\n" \
    "v_add_f32 v249, v248, v250\n" \
    "s_waitcnt lgkmcnt(4)\n" \
    "v_mfma_f32_16x16x32_bf16 v[208:211], v[40:43], v[200:203], v[208:211]\n" \
    "ds_read_b128 v[196:199], v168 offset:15360\n" \
    "v_mfma_f32_16x16x32_bf16 v[212:215], v[120:123], v[200:203], v[212:215]\n" \
    "v_add_f32 v248, v248, v250\n" \
    "s_waitcnt lgkmcnt(4)\n" \
    "v_mfma_f32_16x16x32_bf16 v[208:211], v[44:47], v[204:207], v[208:211]\n" \
    "ds_read_b128 v[200:203], v168 offset:16384\n" \
    "v_mfma_f32_16x16x32_bf16 v[212:215], v[124:127], v[204:207], v[212:215]\n" \
    "v_permlane16_swap_b32 v248, v249\n" \
    "s_waitcnt lgkmcnt(4)\n" \
    "v_mfma_f32_16x16x32_bf16 v[208:211], v[48:51], v[184:187], v[208:211]\n" \
    "ds_read_b128 v[204:207], v168 offset:17408\n" \
    "v_mfma_f32_16x16x32_bf16 v[212:215], v[128:131], v[184:187], v[212:215]\n" \
    "v_mul_f32 v252, 0x3acccccd, v248\n" \
    "s_waitcnt lgkmcnt(4)\n" \
    "v_mfma_f32_16x16x32_bf16 v[208:211], v[52:55], v[188:191], v[208:211]\n" \
    "ds_read_b128 v[184:187], v168 offset:18432\n" \
    "v_mfma_f32_16x16x32_bf16 v[212:215], v[132:135], v[188:191], v[212:215]\n" \
    "v_mul_f32 v249, 0x3acccccd, v249\n" \
    "s_waitcnt lgkmcnt(4)\n" \
    "v_mfma_f32_16x16x32_bf16 v[208:211], v[56:59], v[192:195], v[208:211]\n" \
    "ds_read_b128 v[188:191], v168 offset:19456\n" \
    "v_mfma_f32_16x16x32_bf16 v[212:215], v[136:139], v[192:195], v[212:215]\n" \
    "v_fma_f32 v249, -v252, v252, v249\n" \
    "s_waitcnt lgkmcnt(4)\n" \
    "v_mfma_f32_16x16x32_bf16 v[208:211], v[60:63], v[196:199], v[208:211]\n" \
    "v_mfma_f32_16x16x32_bf16 v[212:215], v[140:143], v[196:199], v[212:215]\n" \
    "v_max_f32 v249, 0, v249\n" \
    "s_waitcnt lgkmcnt(3)\n" \
    "v_mfma_f32_16x16x32_bf16 v[208:211], v[64:67], v[200:203], v[208:211]\n" \
    "v_mfma_f32_16x16x32_bf16 v[212:215], v[144:147], v[200:203], v[212:215]\n" \
    "v_add_f32 v249, 0x3727c5ac, v249\n" \
    "s_waitcnt lgkmcnt(2)\n" \
    "v_mfma_f32_16x16x32_bf16 v[208:211], v[68:71], v[204:207], v[208:211]\n" \
    "v_mfma_f32_16x16x32_bf16 v[212:215], v[148:151], v[204:207], v[212:215]\n" \
    "v_rsq_f32 v253, v249\n" \
    "s_waitcnt lgkmcnt(1)\n" \
    "v_mfma_f32_16x16x32_bf16 v[208:211], v[72:75], v[184:187], v[208:211]\n" \
    "v_mfma_f32_16x16x32_bf16 v[212:215], v[152:155], v[184:187], v[212:215]\n" \
    "v_mul_f32 v251, 0xbfb8aa3b, v253\n" \
    "s_waitcnt lgkmcnt(0)\n" \
    "v_mfma_f32_16x16x32_bf16 v[208:211], v[76:79], v[188:191], v[208:211]\n" \
    "v_mfma_f32_16x16x32_bf16 v[212:215], v[156:159], v[188:191], v[212:215]\n" \
    "s_waitcnt lgkmcnt(0)\n" \
    "s_barrier\n" \
    "s_nop 4\n" \
    "v_fma_f32 v208, -v252, v160, v208\n" \
    "v_fma_f32 v209, -v252, v161, v209\n" \
    "v_fma_f32 v210, -v252, v162, v210\n" \
    "v_fma_f32 v211, -v252, v163, v211\n" \
    "v_mul_f32 v208, v251, v208\n" \
    "v_mul_f32 v209, v251, v209\n" \
    "v_mul_f32 v210, v251, v210\n" \
    "v_mul_f32 v211, v251, v211\n" \
    "v_exp_f32 v208, v208\n" \
    "v_fma_f32 v212, -v252, v164, v212\n" \
    "v_exp_f32 v209, v209\n" \
    "v_fma_f32 v213, -v252, v165, v213\n" \
    "v_exp_f32 v210, v210\n" \
    "v_fma_f32 v214, -v252, v166, v214\n" \
    "v_exp_f32 v211, v211\n" \
    "v_fma_f32 v215, -v252, v167, v215\n" \
    "v_add_f32 v208, 1.0, v208\n" \
    "v_add_f32 v209, 1.0, v209\n" \
    "v_add_f32 v210, 1.0, v210\n" \
    "v_add_f32 v211, 1.0, v211\n" \
    "v_rcp_f32 v208, v208\n" \
    "v_mul_f32 v212, v253, v212\n" \
    "v_rcp_f32 v209, v209\n" \
    "v_mul_f32 v213, v253, v213\n" \
    "v_rcp_f32 v210, v210\n" \
    "v_mul_f32 v214, v253, v214\n" \
    "v_rcp_f32 v211, v211\n" \
    "v_mul_f32 v215, v253, v215\n" \
    "v_mul_f32 v208, v208, v212\n" \
    "v_mul_f32 v209, v209, v213\n" \
    "v_mul_f32 v210, v210, v214\n" \
    "v_mul_f32 v211, v211, v215\n" \
    "v_cvt_pk_bf16_f32 v240, v208, v209\n" \
    "v_cvt_pk_bf16_f32 v241, v210, v211\n" \
    "v_min_u32 v240, -2, v240\n" \
    "v_min_u32 v241, -2, v241\n" \
    "buffer_store_dwordx2 v[240:241], v173, s[8:11], s39 offen sc1\n" \
    "buffer_store_dwordx2 v[182:183], v173, s[8:11], s40 offen sc1\n" \
    "s_add_u32 s36, s33, 1\n" \
    "ds_read_b128 v[184:187], v168 offset:20480\n" \
    "ds_read_b128 v[188:191], v168 offset:21504\n" \
    "ds_read_b128 v[192:195], v168 offset:22528\n" \
    "ds_read_b128 v[196:199], v168 offset:23552\n" \
    "ds_read_b128 v[200:203], v168 offset:24576\n" \
    "ds_read2_b64 v[248:251], v172 offset0:128 offset1:144\n" \
    "s_waitcnt lgkmcnt(5)\n" \
    "v_mfma_f32_16x16x32_bf16 v[208:211], v[0:3], v[184:187], 0\n" \
    "ds_read_b128 v[204:207], v168 offset:25600\n" \
    "v_mfma_f32_16x16x32_bf16 v[212:215], v[80:83], v[184:187], 0\n" \
    "s_mul_i32 s41, s36, 40960\n" \
    "s_add_u32 s41, s41, s17\n" \
    "s_add_u32 s41, s41, 2621440\n" \
    "buffer_load_dwordx4 v[228:231], v168, s[8:11], s41 offen sc1\n" \
    "s_add_u32 s41, s41, 8192\n" \
    "buffer_load_dwordx4 v[232:235], v168, s[8:11], s41 offen sc1\n" \
    "s_waitcnt lgkmcnt(5)\n" \
    "v_mfma_f32_16x16x32_bf16 v[208:211], v[4:7], v[188:191], v[208:211]\n" \
    "ds_read_b128 v[184:187], v168 offset:26624\n" \
    "v_mfma_f32_16x16x32_bf16 v[212:215], v[84:87], v[188:191], v[212:215]\n" \
    "s_mul_i32 s39, s33, 98304\n" \
    "s_add_u32 s39, s39, 6291456\n" \
    "s_waitcnt lgkmcnt(5)\n" \
    "v_mfma_f32_16x16x32_bf16 v[208:211], v[8:11], v[192:195], v[208:211]\n" \
    "ds_read_b128 v[188:191], v168 offset:27648\n" \
    "v_mfma_f32_16x16x32_bf16 v[212:215], v[88:91], v[192:195], v[212:215]\n" \
    "s_add_u32 s40, s39, %[pdist]*98304\n" \
    "s_cmp_lt_u32 s33, 64-%[pdist]\n" \
    "s_cselect_b32 s40, s40, 0x80000000\n" \
    "s_waitcnt lgkmcnt(5)\n" \
    "v_mfma_f32_16x16x32_bf16 v[208:211], v[12:15], v[196:199], v[208:211]\n" \
    "ds_read_b128 v[192:195], v168 offset:28672\n" \
    "v_mfma_f32_16x16x32_bf16 v[212:215], v[92:95], v[196:199], v[212:215]\n" \
    "s_waitcnt lgkmcnt(4)\n" \
    "v_add_f32 v248, v248, v250\n" \
    "v_mfma_f32_16x16x32_bf16 v[208:211], v[16:19], v[200:203], v[208:211]\n" \
    "ds_read_b128 v[196:199], v168 offset:29696\n" \
    "v_mfma_f32_16x16x32_bf16 v[212:215], v[96:99], v[200:203], v[212:215]\n" \
    "v_add_f32 v249, v249, v251\n" \
    "s_waitcnt lgkmcnt(4)\n" \
    "v_mfma_f32_16x16x32_bf16 v[208:211], v[20:23], v[204:207], v[208:211]\n" \
    "ds_read_b128 v[200:203], v168 offset:30720\n" \
    "v_mfma_f32_16x16x32_bf16 v[212:215], v[100:103], v[204:207], v[212:215]\n" \
    "v_permlane16_swap_b32 v248, v249\n" \
    "s_waitcnt lgkmcnt(4)\n" \
    "v_mfma_f32_16x16x32_bf16 v[208:211], v[24:27], v[184:187], v[208:211]\n" \
    "ds_read_b128 v[204:207], v168 offset:31744\n" \
    "v_mfma_f32_16x16x32_bf16 v[212:215], v[104:107], v[184:187], v[212:215]\n" \
    "v_add_f32 v250, v248, v249\n" \
    "s_waitcnt lgkmcnt(4)\n" \
    "v_mfma_f32_16x16x32_bf16 v[208:211], v[28:31], v[188:191], v[208:211]\n" \
    "ds_read_b128 v[184:187], v168 offset:32768\n" \
    "v_mfma_f32_16x16x32_bf16 v[212:215], v[108:111], v[188:191], v[212:215]\n" \
    "v_add_f32 v248, v248, v249\n" \
    "s_waitcnt lgkmcnt(4)\n" \
    "v_mfma_f32_16x16x32_bf16 v[208:211], v[32:35], v[192:195], v[208:211]\n" \
    "ds_read_b128 v[188:191], v168 offset:33792\n" \
    "v_mfma_f32_16x16x32_bf16 v[212:215], v[112:115], v[192:195], v[212:215]\n" \
    "v_permlane32_swap_b32 v248, v250\n" \
    "s_waitcnt lgkmcnt(4)\n" \
    "v_mfma_f32_16x16x32_bf16 v[208:211], v[36:39], v[196:199], v[208:211]\n" \
    "ds_read_b128 v[192:195], v168 offset:34816\n" \
    "v_mfma_f32_16x16x32_bf16 v[212:215], v[116:119], v[196:199], v[212:215]\n" \
    "v_add_f32 v249, v248, v250\n" \
    "s_waitcnt lgkmcnt(4)\n" \
    "v_mfma_f32_16x16x32_bf16 v[208:211], v[40:43], v[200:203], v[208:211]\n" \
    "ds_read_b128 v[196:199], v168 offset:35840\n" \
    "v_mfma_f32_16x16x32_bf16 v[212:215], v[120:123], v[200:203], v[212:215]\n" \
    "v_add_f32 v248, v248, v250\n" \
    "s_add_u32 s48, s32, 3\n" \
    "s_min_u32 s48, s48, 64\n" \
    "s_and_b32 s49, s48, 1\n" \
    "s_xor_b32 s49, s49, 1\n" \
    "s_lshl_b32 s49, s49, 9\n" \
    "s_add_u32 s49, s49, s18\n" \
    "buffer_load_dwordx2 v[176:177], off, s[8:11], s49 sc1\n" \
    "s_waitcnt lgkmcnt(4)\n" \
    "v_mfma_f32_16x16x32_bf16 v[208:211], v[44:47], v[204:207], v[208:211]\n" \
    "ds_read_b128 v[200:203], v168 offset:36864\n" \
    "v_mfma_f32_16x16x32_bf16 v[212:215], v[124:127], v[204:207], v[212:215]\n" \
    "v_permlane16_swap_b32 v248, v249\n" \
    "s_waitcnt lgkmcnt(4)\n" \
    "v_mfma_f32_16x16x32_bf16 v[208:211], v[48:51], v[184:187], v[208:211]\n" \
    "ds_read_b128 v[204:207], v168 offset:37888\n" \
    "v_mfma_f32_16x16x32_bf16 v[212:215], v[128:131], v[184:187], v[212:215]\n" \
    "v_mul_f32 v252, 0x3acccccd, v248\n" \
    "s_waitcnt lgkmcnt(4)\n" \
    "v_mfma_f32_16x16x32_bf16 v[208:211], v[52:55], v[188:191], v[208:211]\n" \
    "ds_read_b128 v[184:187], v168 offset:38912\n" \
    "v_mfma_f32_16x16x32_bf16 v[212:215], v[132:135], v[188:191], v[212:215]\n" \
    "v_mul_f32 v249, 0x3acccccd, v249\n" \
    "s_waitcnt lgkmcnt(4)\n" \
    "v_mfma_f32_16x16x32_bf16 v[208:211], v[56:59], v[192:195], v[208:211]\n" \
    "ds_read_b128 v[188:191], v168 offset:39936\n" \
    "v_mfma_f32_16x16x32_bf16 v[212:215], v[136:139], v[192:195], v[212:215]\n" \
    "v_fma_f32 v249, -v252, v252, v249\n" \
    "s_waitcnt lgkmcnt(4)\n" \
    "v_mfma_f32_16x16x32_bf16 v[208:211], v[60:63], v[196:199], v[208:211]\n" \
    "v_mfma_f32_16x16x32_bf16 v[212:215], v[140:143], v[196:199], v[212:215]\n" \
    "v_max_f32 v249, 0, v249\n" \
    "s_waitcnt lgkmcnt(3)\n" \
    "v_mfma_f32_16x16x32_bf16 v[208:211], v[64:67], v[200:203], v[208:211]\n" \
    "v_mfma_f32_16x16x32_bf16 v[212:215], v[144:147], v[200:203], v[212:215]\n" \
    "v_add_f32 v249, 0x3727c5ac, v249\n" \
    "s_waitcnt lgkmcnt(2)\n" \
    "v_mfma_f32_16x16x32_bf16 v[208:211], v[68:71], v[204:207], v[208:211]\n" \
    "v_mfma_f32_16x16x32_bf16 v[212:215], v[148:151], v[204:207], v[212:215]\n" \
    "v_rsq_f32 v253, v249\n" \
    "s_waitcnt lgkmcnt(1)\n" \
    "v_mfma_f32_16x16x32_bf16 v[208:211], v[72:75], v[184:187], v[208:211]\n" \
    "v_mfma_f32_16x16x32_bf16 v[212:215], v[152:155], v[184:187], v[212:215]\n" \
    "v_mul_f32 v251, 0xbfb8aa3b, v253\n" \
    "s_waitcnt lgkmcnt(0)\n" \
    "v_mfma_f32_16x16x32_bf16 v[208:211], v[76:79], v[188:191], v[208:211]\n" \
    "v_mfma_f32_16x16x32_bf16 v[212:215], v[156:159], v[188:191], v[212:215]\n" \
    "s_nop 6\n" \
    "v_fma_f32 v208, -v252, v160, v208\n" \
    "v_fma_f32 v209, -v252, v161, v209\n" \
    "v_fma_f32 v210, -v252, v162, v210\n" \
    "v_fma_f32 v211, -v252, v163, v211\n" \
    "v_mul_f32 v208, v251, v208\n" \
    "v_mul_f32 v209, v251, v209\n" \
    "v_mul_f32 v210, v251, v210\n" \
    "v_mul_f32 v211, v251, v211\n" \
    "v_exp_f32 v208, v208\n" \
    "v_fma_f32 v212, -v252, v164, v212\n" \
    "v_exp_f32 v209, v209\n" \
    "v_fma_f32 v213, -v252, v165, v213\n" \
    "v_exp_f32 v210, v210\n" \
    "v_fma_f32 v214, -v252, v166, v214\n" \
    "v_exp_f32 v211, v211\n" \
    "v_fma_f32 v215, -v252, v167, v215\n" \
    "v_add_f32 v208, 1.0, v208\n" \
    "v_add_f32 v209, 1.0, v209\n" \
    "v_add_f32 v210, 1.0, v210\n" \
    "v_add_f32 v211, 1.0, v211\n" \
    "v_rcp_f32 v208, v208\n" \
    "v_mul_f32 v212, v253, v212\n" \
    "v_rcp_f32 v209, v209\n" \
    "v_mul_f32 v213, v253, v213\n" \
    "v_rcp_f32 v210, v210\n" \
    "v_mul_f32 v214, v253, v214\n" \
    "v_rcp_f32 v211, v211\n" \
    "v_mul_f32 v215, v253, v215\n" \
    "v_mul_f32 v208, v208, v212\n" \
    "v_mul_f32 v209, v209, v213\n" \
    "v_mul_f32 v210, v210, v214\n" \
    "v_mul_f32 v211, v211, v215\n" \
    "v_cvt_pk_bf16_f32 v240, v208, v209\n" \
    "v_cvt_pk_bf16_f32 v241, v210, v211\n" \
    "v_min_u32 v240, -2, v240\n" \
    "v_min_u32 v241, -2, v241\n" \
    "buffer_store_dwordx2 v[240:241], v173, s[8:11], s39 offen sc1\n" \
    "buffer_store_dwordx2 v[182:183], v173, s[8:11], s40 offen sc1\n" \
    "s_add_u32 s33, s33, 1\n" \
    "s_add_u32 s32, s32, 1\n" \
    "s_cmp_lt_u32 s32, 64\n" \
    "s_cbranch_scc1 .Lup_mY_%=\n" \
    "s_add_u32 s18, s18, 1024\n" \
    "s_waitcnt vmcnt(0)\n" \
    "s_add_u32 s48, s33, 2\n" \
    "s_min_u32 s48, s48, 64\n" \
    "s_and_b32 s49, s48, 1\n" \
    "s_xor_b32 s49, s49, 1\n" \
    "s_lshl_b32 s49, s49, 9\n" \
    "s_add_u32 s49, s49, s18\n" \
    "buffer_load_dwordx2 v[176:177], off, s[8:11], s49 sc1\n" \
    "s_waitcnt vmcnt(0)\n" \
    ".Lup_nY_%=:\n" \
    "s_barrier\n" \
    "s_mov_b32 s37, 0\n" \
    "s_waitcnt vmcnt(2)\n" \
    ".Lup_Ydp_%=:\n" \
    "v_readfirstlane_b32 s36, v177\n" \
    "s_nop 0\n" \
    "s_cmp_gt_i32 s36, s48\n" \
    "s_cbranch_scc1 .Lup_Ydx_%=\n" \
    "s_cmp_eq_u32 s36, s48\n" \
    "s_cbranch_scc1 .Lup_Ydw_%=\n" \
    "s_sleep 3\n" \
    "buffer_load_dwordx2 v[176:177], off, s[8:11], s49 sc1\n" \
    "s_waitcnt vmcnt(0)\n" \
    "s_add_u32 s37, s37, 1\n" \
    "s_cmp_lt_u32 s37, s38\n" \
    "s_cbranch_scc1 .Lup_Ydp_%=\n" \
    "s_mov_b32 s38, 0\n" \
    "s_branch .Lup_Ydx_%=\n" \
    ".Lup_Ydw_%=:\n" \
    "v_readfirstlane_b32 s36, v176\n" \
    "s_cmp_eq_u32 s33, 63\n" \
    "s_cselect_b32 s34, %[dlast], 0\n" \
    "s_add_u32 s36, s36, s34\n" \
    "s_add_u32 s36, s36, %[dx]\n" \
    "s_branch .Lup_Ydtb_%=\n" \
    ".Lup_Ydta_%=:\n" \
    "s_sleep 1\n" \
    ".Lup_Ydtb_%=:\n" \
    "s_memrealtime vcc\n" \
    "s_waitcnt lgkmcnt(0)\n" \
    "s_sub_i32 vcc_lo, s36, vcc_lo\n" \
    "s_cmp_gt_i32 vcc_lo, 8\n" \
    "s_cbranch_scc1 .Lup_Ydta_%=\n" \
    "s_cmp_gt_i32 vcc_lo, 0\n" \
    "s_cbranch_scc1 .Lup_Ydtb_%=\n" \
    ".Lup_Ydx_%=:\n" \
    "s_mul_i32 s41, s33, 40960\n" \
    "s_add_u32 s41, s41, s17\n" \
    "s_add_u32 s41, s41, 2621440\n" \
    "buffer_load_dwordx4 v[228:231], v168, s[8:11], s41 offen sc1\n" \
    "s_add_u32 s41, s41, 8192\n" \
    "buffer_load_dwordx4 v[232:235], v168, s[8:11], s41 offen sc1\n" \
    "s_mov_b32 s37, 0\n" \
    ".Lup_srYd_%=:\n" \
    "s_waitcnt vmcnt(1)\n" \
    "v_max3_u32 v254, v228, v229, v230\n" \
    "s_waitcnt vmcnt(0)\n" \
    "v_max3_u32 v254, v254, v231, v232\n" \
    "v_max3_u32 v254, v254, v233, v234\n" \
    "v_max_u32 v254, v254, v235\n" \
    "v_cmp_eq_u32 vcc, -1, v254\n" \
    "s_nop 0\n" \
    "s_cbranch_vccnz .Lup_scYd_%=\n" \
    ".Lup_sxYd_%=:\n" \
    "v_mfma_f32_16x16x32_bf16 v[244:247], v[228:231], v[228:231], 0\n" \
    "ds_write_b128 v169, v[228:231] offset:20480\n" \
    "v_mfma_f32_16x16x32_bf16 v[244:247], v[232:235], v[232:235], v[244:247]\n" \
    "ds_write_b128 v169, v[232:235] offset:28672\n" \
    "v_mfma_f32_16x16x32_bf16 v[240:243], v[178:181], v[228:231], 0\n" \
    "v_mfma_f32_16x16x32_bf16 v[240:243], v[178:181], v[232:235], v[240:243]\n" \
    "s_nop 4\n" \
    "v_cndmask_b32 v254, v244, v245, s[44:45]\n" \
    "v_cndmask_b32 v255, v246, v247, s[44:45]\n" \
    "s_nop 0\n" \
    "v_cndmask_b32 v241, v254, v255, s[46:47]\n" \
    "ds_write_b64 v171, v[240:241] offset:1024\n" \
    "s_waitcnt lgkmcnt(0)\n" \
    "s_barrier\n" \
    "ds_read_b128 v[184:187], v168 offset:20480\n" \
    "ds_read_b128 v[188:191], v168 offset:21504\n" \
    "ds_read_b128 v[192:195], v168 offset:22528\n" \
    "ds_read_b128 v[196:199], v168 offset:23552\n" \
    "ds_read_b128 v[200:203], v168 offset:24576\n" \
    "ds_read2_b64 v[248:251], v172 offset0:128 offset1:144\n" \
    "s_waitcnt lgkmcnt(5)\n" \
    "v_mfma_f32_16x16x32_bf16 v[208:211], v[0:3], v[184:187], 0\n" \
    "ds_read_b128 v[204:207], v168 offset:25600\n" \
    "v_mfma_f32_16x16x32_bf16 v[212:215], v[80:83], v[184:187], 0\n" \
    "s_waitcnt lgkmcnt(5)\n" \
    "v_mfma_f32_16x16x32_bf16 v[208:211], v[4:7], v[188:191], v[208:211]\n" \
    "ds_read_b128 v[184:187], v168 offset:26624\n" \
    "v_mfma_f32_16x16x32_bf16 v[212:215], v[84:87], v[188:191], v[212:215]\n" \
    "s_mul_i32 s39, s33, 98304\n" \
    "s_add_u32 s39, s39, 6291456\n" \
    "s_waitcnt lgkmcnt(5)\n" \
    "v_mfma_f32_16x16x32_bf16 v[208:211], v[8:11], v[192:195], v[208:211]\n" \
    "ds_read_b128 v[188:191], v168 offset:27648\n" \
    "v_mfma_f32_16x16x32_bf16 v[212:215], v[88:91], v[192:195], v[212:215]\n" \
    "s_add_u32 s40, s39, %[pdist]*98304\n" \
    "s_cmp_lt_u32 s33, 64-%[pdist]\n" \
    "s_cselect_b32 s40, s40, 0x80000000\n" \
    "s_waitcnt lgkmcnt(5)\n" \
    "v_mfma_f32_16x16x32_bf16 v[208:211], v[12:15], v[196:199], v[208:211]\n" \
    "ds_read_b128 v[192:195], v168 offset:28672\n" \
    "v_mfma_f32_16x16x32_bf16 v[212:215], v[92:95], v[196:199], v[212:215]\n" \
    "s_waitcnt lgkmcnt(4)\n" \
    "v_add_f32 v248, v248, v250\n" \
    "v_mfma_f32_16x16x32_bf16 v[208:211], v[16:19], v[200:203], v[208:211]\n" \
    "ds_read_b128 v[196:199], v168 offset:29696\n" \
    "v_mfma_f32_16x16x32_bf16 v[212:215], v[96:99], v[200:203], v[212:215]\n" \
    "v_add_f32 v249, v249, v251\n" \
    "s_waitcnt lgkmcnt(4)\n" \
    "v_mfma_f32_16x16x32_bf16 v[208:211], v[20:23], v[204:207], v[208:211]\n" \
    "ds_read_b128 v[200:203], v168 offset:30720\n" \
    "v_mfma_f32_16x16x32_bf16 v[212:215], v[100:103], v[204:207], v[212:215]\n" \
    "v_permlane16_swap_b32 v248, v249\n" \
    "s_waitcnt lgkmcnt(4)\n" \
    "v_mfma_f32_16x16x32_bf16 v[208:211], v[24:27], v[184:187], v[208:211]\n" \
    "ds_read_b128 v[204:207], v168 offset:31744\n" \
    "v_mfma_f32_16x16x32_bf16 v[212:215], v[104:107], v[184:187], v[212:215]\n" \
    "v_add_f32 v250, v248, v249\n" \
    "s_waitcnt lgkmcnt(4)\n" \
    "v_mfma_f32_16x16x32_bf16 v[208:211], v[28:31], v[188:191], v[208:211]\n" \
    "ds_read_b128 v[184:187], v168 offset:32768\n" \
    "v_mfma_f32_16x16x32_bf16 v[212:215], v[108:111], v[188:191], v[212:215]\n" \
    "v_add_f32 v248, v248, v249\n" \
    "s_waitcnt lgkmcnt(4)\n" \
    "v_mfma_f32_16x16x32_bf16 v[208:211], v[32:35], v[192:195], v[208:211]\n" \
    "ds_read_b128 v[188:191], v168 offset:33792\n" \
    "v_mfma_f32_16x16x32_bf16 v[212:215], v[112:115], v[192:195], v[212:215]\n" \
    "v_permlane32_swap_b32 v248, v250\n" \
    "s_waitcnt lgkmcnt(4)\n" \
    "v_mfma_f32_16x16x32_bf16 v[208:211], v[36:39], v[196:199], v[208:211]\n" \
    "ds_read_b128 v[192:195], v168 offset:34816\n" \
    "v_mfma_f32_16x16x32_bf16 v[212:215], v[116:119], v[196:199], v[212:215]\n" \
    "v_add_f32 v249, v248, v250\n" \
    "s_waitcnt lgkmcnt(4)\n" \
    "v_mfma_f32_16x16x32_bf16 v[208:211], v[40:43], v[200:203], v[208:211]\n" \
    "ds_read_b128 v[196:199], v168 offset:35840\n" \
    "v_mfma_f32_16x16x32_bf16 v[212:215], v[120:123], v[200:203], v[212:215]\n" \
    "v_add_f32 v248, v248, v250\n" \
    "s_add_u32 s48, s33, 3\n" \
    "s_min_u32 s48, s48, 64\n" \
    "s_and_b32 s49, s48, 1\n" \
    "s_xor_b32 s49, s49, 1\n" \
    "s_lshl_b32 s49, s49, 9\n" \
    "s_add_u32 s49, s49, s18\n" \
    "buffer_load_dwordx2 v[176:177], off, s[8:11], s49 sc1\n" \
    "s_waitcnt lgkmcnt(4)\n" \
    "v_mfma_f32_16x16x32_bf16 v[208:211], v[44:47], v[204:207], v[208:211]\n" \
    "ds_read_b128 v[200:203], v168 offset:36864\n" \
    "v_mfma_f32_16x16x32_bf16 v[212:215], v[124:127], v[204:207], v[212:215]\n" \
    "v_permlane16_swap_b32 v248, v249\n" \
    "s_waitcnt lgkmcnt(4)\n" \
    "v_mfma_f32_16x16x32_bf16 v[208:211], v[48:51], v[184:187], v[208:211]\n" \
    "ds_read_b128 v[204:207], v168 offset:37888\n" \
    "v_mfma_f32_16x16x32_bf16 v[212:215], v[128:131], v[184:187], v[212:215]\n" \
    "v_mul_f32 v252, 0x3acccccd, v248\n" \
    "s_waitcnt lgkmcnt(4)\n" \
    "v_mfma_f32_16x16x32_bf16 v[208:211], v[52:55], v[188:191], v[208:211]\n" \
    "ds_read_b128 v[184:187], v168 offset:38912\n" \
    "v_mfma_f32_16x16x32_bf16 v[212:215], v[132:135], v[188:191], v[212:215]\n" \
    "v_mul_f32 v249, 0x3acccccd, v249\n" \
    "s_waitcnt lgkmcnt(4)\n" \
    "v_mfma_f32_16x16x32_bf16 v[208:211], v[56:59], v[192:195], v[208:211]\n" \
    "ds_read_b128 v[188:191], v168 offset:39936\n" \
    "v_mfma_f32_16x16x32_bf16 v[212:215], v[136:139], v[192:195], v[212:215]\n" \
    "v_fma_f32 v249, -v252, v252, v249\n" \
    "s_waitcnt lgkmcnt(4)\n" \
    "v_mfma_f32_16x16x32_bf16 v[208:211], v[60:63], v[196:199], v[208:211]\n" \
    "v_mfma_f32_16x16x32_bf16 v[212:215], v[140:143], v[196:199], v[212:215]\n" \
    "v_max_f32 v249, 0, v249\n" \
    "s_waitcnt lgkmcnt(3)\n" \
    "v_mfma_f32_16x16x32_bf16 v[208:211], v[64:67], v[200:203], v[208:211]\n" \
    "v_mfma_f32_16x16x32_bf16 v[212:215], v[144:147], v[200:203], v[212:215]\n" \
    "v_add_f32 v249, 0x3727c5ac, v249\n" \
    "s_waitcnt lgkmcnt(2)\n" \
    "v_mfma_f32_16x16x32_bf16 v[208:211], v[68:71], v[204:207], v[208:211]\n" \
    "v_mfma_f32_16x16x32_bf16 v[212:215], v[148:151], v[204:207], v[212:215]\n" \
    "v_rsq_f32 v253, v249\n" \
    "s_waitcnt lgkmcnt(1)\n" \
    "v_mfma_f32_16x16x32_bf16 v[208:211], v[72:75], v[184:187], v[208:211]\n" \
    "v_mfma_f32_16x16x32_bf16 v[212:215], v[152:155], v[184:187], v[212:215]\n" \
    "v_mul_f32 v251, 0xbfb8aa3b, v253\n" \
    "s_waitcnt lgkmcnt(0)\n" \
    "v_mfma_f32_16x16x32_bf16 v[208:211], v[76:79], v[188:191], v[208:211]\n" \
    "v_mfma_f32_16x16x32_bf16 v[212:215], v[156:159], v[188:191], v[212:215]\n" \
    "s_nop 6\n" \
    "v_fma_f32 v208, -v252, v160, v208\n" \
    "v_fma_f32 v209, -v252, v161, v209\n" \
    "v_fma_f32 v210, -v252, v162, v210\n" \
    "v_fma_f32 v211, -v252, v163, v211\n" \
    "v_mul_f32 v208, v251, v208\n" \
    "v_mul_f32 v209, v251, v209\n" \
    "v_mul_f32 v210, v251, v210\n" \
    "v_mul_f32 v211, v251, v211\n" \
    "v_exp_f32 v208, v208\n" \
    "v_fma_f32 v212, -v252, v164, v212\n" \
    "v_exp_f32 v209, v209\n" \
    "v_fma_f32 v213, -v252, v165, v213\n" \
    "v_exp_f32 v210, v210\n" \
    "v_fma_f32 v214, -v252, v166, v214\n" \
    "v_exp_f32 v211, v211\n" \
    "v_fma_f32 v215, -v252, v167, v215\n" \
    "v_add_f32 v208, 1.0, v208\n" \
    "v_add_f32 v209, 1.0, v209\n" \
    "v_add_f32 v210, 1.0, v210\n" \
    "v_add_f32 v211, 1.0, v211\n" \
    "v_rcp_f32 v208, v208\n" \
    "v_mul_f32 v212, v253, v212\n" \
    "v_rcp_f32 v209, v209\n" \
    "v_mul_f32 v213, v253, v213\n" \
    "v_rcp_f32 v210, v210\n" \
    "v_mul_f32 v214, v253, v214\n" \
    "v_rcp_f32 v211, v211\n" \
    "v_mul_f32 v215, v253, v215\n" \
    "v_mul_f32 v208, v208, v212\n" \
    "v_mul_f32 v209, v209, v213\n" \
    "v_mul_f32 v210, v210, v214\n" \
    "v_mul_f32 v211, v211, v215\n" \
    "v_cvt_pk_bf16_f32 v240, v208, v209\n" \
    "v_cvt_pk_bf16_f32 v241, v210, v211\n" \
    "v_min_u32 v240, -2, v240\n" \
    "v_min_u32 v241, -2, v241\n" \
    "buffer_store_dwordx2 v[240:241], v173, s[8:11], s39 offen sc1\n" \
    "buffer_store_dwordx2 v[182:183], v173, s[8:11], s40 offen sc1\n" \
    "s_add_u32 s33, s33, 1\n" \
    "s_cmp_lt_u32 s33, 64\n" \
    "s_cbranch_scc1 .Lup_nY_%=\n" \
    "s_waitcnt vmcnt(0)\n" \
    "s_endpgm\n" \
    ".Lup_lO_%=:\n" \
    "s_add_u32 s48, s32, 2\n" \
    "s_min_u32 s48, s48, 64\n" \
    "s_and_b32 s49, s48, 1\n" \
    "s_xor_b32 s49, s49, 1\n" \
    "s_lshl_b32 s49, s49, 9\n" \
    "s_add_u32 s49, s49, s18\n" \
    "buffer_load_dwordx2 v[176:177], off, s[8:11], s49 sc1\n" \
    "s_mov_b32 s33, 0\n" \
    "s_waitcnt vmcnt(0)\n" \
    ".Lup_kO_%=:\n" \
    "s_mov_b32 s37, 0\n" \
    "s_waitcnt vmcnt(2)\n" \
    ".Lup_Oap_%=:\n" \
    "v_readfirstlane_b32 s36, v177\n" \
    "s_nop 0\n" \
    "s_cmp_gt_i32 s36, s48\n" \
    "s_cbranch_scc1 .Lup_Oax_%=\n" \
    "s_cmp_eq_u32 s36, s48\n" \
    "s_cbranch_scc1 .Lup_Oaw_%=\n" \
    "s_sleep 3\n" \
    "buffer_load_dwordx2 v[176:177], off, s[8:11], s49 sc1\n" \
    "s_waitcnt vmcnt(0)\n" \
    "s_add_u32 s37, s37, 1\n" \
    "s_cmp_lt_u32 s37, s38\n" \
    "s_cbranch_scc1 .Lup_Oap_%=\n" \
    "s_mov_b32 s38, 0\n" \
    "s_branch .Lup_Oax_%=\n" \
    ".Lup_Oaw_%=:\n" \
    "v_readfirstlane_b32 s36, v176\n" \
    "s_cmp_eq_u32 s32, 63\n" \
    "s_cselect_b32 s34, %[dlast], 0\n" \
    "s_add_u32 s36, s36, s34\n" \
    "s_add_u32 s36, s36, %[dx]\n" \
    "s_branch .Lup_Oatb_%=\n" \
    ".Lup_Oata_%=:\n" \
    "s_sleep 1\n" \
    ".Lup_Oatb_%=:\n" \
    "s_memrealtime vcc\n" \
    "s_waitcnt lgkmcnt(0)\n" \
    "s_sub_i32 vcc_lo, s36, vcc_lo\n" \
    "s_cmp_gt_i32 vcc_lo, 8\n" \
    "s_cbranch_scc1 .Lup_Oata_%=\n" \
    "s_cmp_gt_i32 vcc_lo, 0\n" \
    "s_cbranch_scc1 .Lup_Oatb_%=\n" \
    ".Lup_Oax_%=:\n" \
    "s_mul_i32 s41, s32, 40960\n" \
    "s_add_u32 s41, s41, s17\n" \
    "buffer_load_dwordx4 v[216:219], v168, s[8:11], s41 offen sc1\n" \
    "s_add_u32 s41, s41, 8192\n" \
    "buffer_load_dwordx4 v[220:223], v168, s[8:11], s41 offen sc1\n" \
    "s_add_u32 s41, s41, 8192\n" \
    "buffer_load_dwordx4 v[224:227], v168, s[8:11], s41 offen sc1\n" \
    "s_mov_b32 s37, 0\n" \
    ".Lup_srOa_%=:\n" \
    "s_waitcnt vmcnt(2)\n" \
    "v_max3_u32 v254, v216, v217, v218\n" \
    "s_waitcnt vmcnt(1)\n" \
    "v_max3_u32 v254, v254, v219, v220\n" \
    "v_max3_u32 v254, v254, v221, v222\n" \
    "s_waitcnt vmcnt(0)\n" \
    "v_max3_u32 v254, v254, v223, v224\n" \
    "v_max3_u32 v254, v254, v225, v226\n" \
    "v_max_u32 v254, v254, v227\n" \
    "v_cmp_eq_u32 vcc, -1, v254\n" \
    "s_nop 0\n" \
    "s_cbranch_vccnz .Lup_scOa_%=\n" \
    ".Lup_sxOa_%=:\n" \
    "v_mfma_f32_16x16x32_bf16 v[244:247], v[216:219], v[216:219], 0\n" \
    "ds_write_b128 v169, v[216:219] offset:0\n" \
    "v_mfma_f32_16x16x32_bf16 v[244:247], v[220:223], v[220:223], v[244:247]\n" \
    "ds_write_b128 v169, v[220:223] offset:8192\n" \
    "v_mfma_f32_16x16x32_bf16 v[244:247], v[224:227], v[224:227], v[244:247]\n" \
    "ds_write_b128 v169, v[224:227] offset:16384\n" \
    "v_mfma_f32_16x16x32_bf16 v[240:243], v[178:181], v[216:219], 0\n" \
    "v_mfma_f32_16x16x32_bf16 v[240:243], v[178:181], v[220:223], v[240:243]\n" \
    "v_mfma_f32_16x16x32_bf16 v[240:243], v[178:181], v[224:227], v[240:243]\n" \
    "s_nop 3\n" \
    "v_cndmask_b32 v254, v244, v245, s[44:45]\n" \
    "v_cndmask_b32 v255, v246, v247, s[44:45]\n" \
    "s_nop 1\n" \
    "v_cndmask_b32 v241, v254, v255, s[46:47]\n" \
    "ds_write_b64 v171, v[240:241] offset:0\n" \
    "s_waitcnt lgkmcnt(0)\n" \
    "s_barrier\n" \
    "ds_read_b128 v[184:187], v168 offset:0\n" \
    "ds_read_b128 v[188:191], v168 offset:1024\n" \
    "ds_read_b128 v[192:195], v168 offset:2048\n" \
    "ds_read_b128 v[196:199], v168 offset:3072\n" \
    "ds_read_b128 v[200:203], v168 offset:4096\n" \
    "ds_read2_b64 v[248:251], v172 offset0:0 offset1:16\n" \
    "s_waitcnt lgkmcnt(5)\n" \
    "v_mfma_f32_16x16x32_bf16 v[208:211], v[0:3], v[184:187], 0\n" \
    "ds_read_b128 v[204:207], v168 offset:5120\n" \
    "v_mfma_f32_16x16x32_bf16 v[212:215], v[80:83], v[184:187], 0\n" \
    "s_mul_i32 s41, s33, 40960\n" \
    "s_add_u32 s41, s41, s17\n" \
    "s_add_u32 s41, s41, 2621440\n" \
    "buffer_load_dwordx4 v[228:231], v168, s[8:11], s41 offen sc1\n" \
    "s_add_u32 s41, s41, 8192\n" \
    "buffer_load_dwordx4 v[232:235], v168, s[8:11], s41 offen sc1\n" \
    "s_add_u32 s41, s41, 8192\n" \
    "buffer_load_dwordx4 v[236:239], v168, s[8:11], s41 offen sc1\n" \
    "s_waitcnt lgkmcnt(5)\n" \
    "v_mfma_f32_16x16x32_bf16 v[208:211], v[4:7], v[188:191], v[208:211]\n" \
    "ds_read_b128 v[184:187], v168 offset:6144\n" \
    "v_mfma_f32_16x16x32_bf16 v[212:215], v[84:87], v[188:191], v[212:215]\n" \
    "s_mul_i32 s39, s32, 98304\n" \
    "s_add_u32 s39, s39, 0\n" \
    "s_waitcnt lgkmcnt(5)\n" \
    "v_mfma_f32_16x16x32_bf16 v[208:211], v[8:11], v[192:195], v[208:211]\n" \
    "ds_read_b128 v[188:191], v168 offset:7168\n" \
    "v_mfma_f32_16x16x32_bf16 v[212:215], v[88:91], v[192:195], v[212:215]\n" \
    "s_add_u32 s40, s39, %[pdist]*98304\n" \
    "s_cmp_lt_u32 s32, 64-%[pdist]\n" \
    "s_cselect_b32 s40, s40, 0x80000000\n" \
    "s_waitcnt lgkmcnt(5)\n" \
    "v_mfma_f32_16x16x32_bf16 v[208:211], v[12:15], v[196:199], v[208:211]\n" \
    "ds_read_b128 v[192:195], v168 offset:8192\n" \
    "v_mfma_f32_16x16x32_bf16 v[212:215], v[92:95], v[196:199], v[212:215]\n" \
    "s_waitcnt lgkmcnt(4)\n" \
    "v_add_f32 v248, v248, v250\n" \
    "v_mfma_f32_16x16x32_bf16 v[208:211], v[16:19], v[200:203], v[208:211]\n" \
    "ds_read_b128 v[196:199], v168 offset:9216\n" \
    "v_mfma_f32_16x16x32_bf16 v[212:215], v[96:99], v[200:203], v[212:215]\n" \
    "v_add_f32 v249, v249, v251\n" \
    "s_waitcnt lgkmcnt(4)\n" \
    "v_mfma_f32_16x16x32_bf16 v[208:211], v[20:23], v[204:207], v[208:211]\n" \
    "ds_read_b128 v[200:203], v168 offset:10240\n" \
    "v_mfma_f32_16x16x32_bf16 v[212:215], v[100:103], v[204:207], v[212:215]\n" \
    "v_permlane16_swap_b32 v248, v249\n" \
    "s_waitcnt lgkmcnt(4)\n" \
    "v_mfma_f32_16x16x32_bf16 v[208:211], v[24:27], v[184:187], v[208:211]\n" \
    "ds_read_b128 v[204:207], v168 offset:11264\n" \
    "v_mfma_f32_16x16x32_bf16 v[212:215], v[104:107], v[184:187], v[212:215]\n" \
    "v_add_f32 v250, v248, v249\n" \
    "s_waitcnt lgkmcnt(4)\n" \
    "v_mfma_f32_16x16x32_bf16 v[208:211], v[28:31], v[188:191], v[208:211]\n" \
    "ds_read_b128 v[184:187], v168 offset:12288\n" \
    "v_mfma_f32_16x16x32_bf16 v[212:215], v[108:111], v[188:191], v[212:215]\n" \
    "v_add_f32 v248, v248, v249\n" \
    "s_waitcnt lgkmcnt(4)\n" \
    "v_mfma_f32_16x16x32_bf16 v[208:211], v[32:35], v[192:195], v[208:211]\n" \
    "ds_read_b128 v[188:191], v168 offset:13312\n" \
    "v_mfma_f32_16x16x32_bf16 v[212:215], v[112:115], v[192:195], v[212:215]\n" \
    "v_permlane32_swap_b32 v248, v250\n" \
    "s_waitcnt lgkmcnt(4)\n" \
    "v_mfma_f32_16x16x32_bf16 v[208:211], v[36:39], v[196:199], v[208:211]\n" \
    "ds_read_b128 v[192:195], v168 offset:14336\n" \
    "v_mfma_f32_16x16x32_bf16 v[212:215], v[116:119], v[196:199], v[212:215]\n" \
    "v_add_f32 v249, v248, v250\n" \
    "s_waitcnt lgkmcnt(4)\n" \
    "v_mfma_f32_16x16x32_bf16 v[208:211], v[40:43], v[200:203], v[208:211]\n" \
    "ds_read_b128 v[196:199], v168 offset:15360\n" \
    "v_mfma_f32_16x16x32_bf16 v[212:215], v[120:123], v[200:203], v[212:215]\n" \
    "v_add_f32 v248, v248, v250\n" \
    "s_add_u32 s48, s32, 3\n" \
    "s_min_u32 s48, s48, 64\n" \
    "s_and_b32 s49, s48, 1\n" \
    "s_xor_b32 s49, s49, 1\n" \
    "s_lshl_b32 s49, s49, 9\n" \
    "s_add_u32 s49, s49, s18\n" \
    "buffer_load_dwordx2 v[176:177], off, s[8:11], s49 sc1\n" \
    "s_waitcnt lgkmcnt(4)\n" \
    "v_mfma_f32_16x16x32_bf16 v[208:211], v[44:47], v[204:207], v[208:211]\n" \
    "ds_read_b128 v[200:203], v168 offset:16384\n" \
    "v_mfma_f32_16x16x32_bf16 v[212:215], v[124:127], v[204:207], v[212:215]\n" \
    "v_permlane16_swap_b32 v248, v249\n" \
    "s_waitcnt lgkmcnt(4)\n" \
    "v_mfma_f32_16x16x32_bf16 v[208:211], v[48:51], v[184:187], v[208:211]\n" \
    "ds_read_b128 v[204:207], v168 offset:17408\n" \
    "v_mfma_f32_16x16x32_bf16 v[212:215], v[128:131], v[184:187], v[212:215]\n" \
    "v_mul_f32 v252, 0x3acccccd, v248\n" \
    "s_waitcnt lgkmcnt(4)\n" \
    "v_mfma_f32_16x16x32_bf16 v[208:211], v[52:55], v[188:191], v[208:211]\n" \
    "ds_read_b128 v[184:187], v168 offset:18432\n" \
    "v_mfma_f32_16x16x32_bf16 v[212:215], v[132:135], v[188:191], v[212:215]\n" \
    "v_mul_f32 v249, 0x3acccccd, v249\n" \
    "s_waitcnt lgkmcnt(4)\n" \
    "v_mfma_f32_16x16x32_bf16 v[208:211], v[56:59], v[192:195], v[208:211]\n" \
    "ds_read_b128 v[188:191], v168 offset:19456\n" \
    "v_mfma_f32_16x16x32_bf16 v[212:215], v[136:139], v[192:195], v[212:215]\n" \
    "v_fma_f32 v249, -v252, v252, v249\n" \
    "s_waitcnt lgkmcnt(4)\n" \
    "v_mfma_f32_16x16x32_bf16 v[208:211], v[60:63], v[196:199], v[208:211]\n" \
    "v_mfma_f32_16x16x32_bf16 v[212:215], v[140:143], v[196:199], v[212:215]\n" \
    "v_max_f32 v249, 0, v249\n" \
    "s_waitcnt lgkmcnt(3)\n" \
    "v_mfma_f32_16x16x32_bf16 v[208:211], v[64:67], v[200:203], v[208:211]\n" \
    "v_mfma_f32_16x16x32_bf16 v[212:215], v[144:147], v[200:203], v[212:215]\n" \
    "v_add_f32 v249, 0x3727c5ac, v249\n" \
    "s_waitcnt lgkmcnt(2)\n" \
    "v_mfma_f32_16x16x32_bf16 v[208:211], v[68:71], v[204:207], v[208:211]\n" \
    "v_mfma_f32_16x16x32_bf16 v[212:215], v[148:151], v[204:207], v[212:215]\n" \
    "v_rsq_f32 v253, v249\n" \
    "s_waitcnt lgkmcnt(1)\n" \
    "v_mfma_f32_16x16x32_bf16 v[208:211], v[72:75], v[184:187], v[208:211]\n" \
    "v_mfma_f32_16x16x32_bf16 v[212:215], v[152:155], v[184:187], v[212:215]\n" \
    "v_mul_f32 v251, 0xbfb8aa3b, v253\n" \
    "s_waitcnt lgkmcnt(0)\n" \
    "v_mfma_f32_16x16x32_bf16 v[208:211], v[76:79], v[188:191], v[208:211]\n" \
    "v_mfma_f32_16x16x32_bf16 v[212:215], v[156:159], v[188:191], v[212:215]\n" \
    "s_nop 6\n" \
    "v_fma_f32 v208, -v252, v160, v208\n" \
    "v_fma_f32 v209, -v252, v161, v209\n" \
    "v_fma_f32 v210, -v252, v162, v210\n" \
    "v_fma_f32 v211, -v252, v163, v211\n" \
    "v_mul_f32 v208, v251, v208\n" \
    "v_mul_f32 v209, v251, v209\n" \
    "v_mul_f32 v210, v251, v210\n" \
    "v_mul_f32 v211, v251, v211\n" \
    "v_exp_f32 v208, v208\n" \
    "v_fma_f32 v212, -v252, v164, v212\n" \
    "v_exp_f32 v209, v209\n" \
    "v_fma_f32 v213, -v252, v165, v213\n" \
    "v_exp_f32 v210, v210\n" \
    "v_fma_f32 v214, -v252, v166, v214\n" \
    "v_exp_f32 v211, v211\n" \
    "v_fma_f32 v215, -v252, v167, v215\n" \
    "v_add_f32 v208, 1.0, v208\n" \
    "v_add_f32 v209, 1.0, v209\n" \
    "v_add_f32 v210, 1.0, v210\n" \
    "v_add_f32 v211, 1.0, v211\n" \
    "v_rcp_f32 v208, v208\n" \
    "v_mul_f32 v212, v253, v212\n" \
    "v_rcp_f32 v209, v209\n" \
    "v_mul_f32 v213, v253, v213\n" \
    "v_rcp_f32 v210, v210\n" \
    "v_mul_f32 v214, v253, v214\n" \
    "v_rcp_f32 v211, v211\n" \
    "v_mul_f32 v215, v253, v215\n" \
    "v_mul_f32 v208, v208, v212\n" \
    "v_mul_f32 v209, v209, v213\n" \
    "v_mul_f32 v210, v210, v214\n" \
    "v_mul_f32 v211, v211, v215\n" \
    "v_cvt_pk_bf16_f32 v240, v208, v209\n" \
    "v_cvt_pk_bf16_f32 v241, v210, v211\n" \
    "v_min_u32 v240, -2, v240\n" \
    "v_min_u32 v241, -2, v241\n" \
    "buffer_store_dwordx2 v[240:241], v173, s[8:11], s39 offen sc1\n" \
    "buffer_store_dwordx2 v[182:183], v173, s[8:11], s40 offen sc1\n" \
    "s_add_u32 s32, s32, 1\n" \
    "s_cmp_lt_u32 s32, %[eup]\n" \
    "s_barrier\n" \
    "s_cbranch_scc1 .Lup_kO_%=\n" \
    ".Lup_mO_%=:\n" \
    "s_mov_b32 s37, 0\n" \
    "s_waitcnt vmcnt(2)\n" \
    ".Lup_Obp_%=:\n" \
    "v_readfirstlane_b32 s36, v177\n" \
    "s_nop 0\n" \
    "s_cmp_gt_i32 s36, s48\n" \
    "s_cbranch_scc1 .Lup_Obx_%=\n" \
    "s_cmp_eq_u32 s36, s48\n" \
    "s_cbranch_scc1 .Lup_Obw_%=\n" \
    "s_sleep 3\n" \
    "buffer_load_dwordx2 v[176:177], off, s[8:11], s49 sc1\n" \
    "s_waitcnt vmcnt(0)\n" \
    "s_add_u32 s37, s37, 1\n" \
    "s_cmp_lt_u32 s37, s38\n" \
    "s_cbranch_scc1 .Lup_Obp_%=\n" \
    "s_mov_b32 s38, 0\n" \
    "s_branch .Lup_Obx_%=\n" \
    ".Lup_Obw_%=:\n" \
    "v_readfirstlane_b32 s36, v176\n" \
    "s_cmp_eq_u32 s32, 63\n" \
    "s_cselect_b32 s34, %[dlast], 0\n" \
    "s_add_u32 s36, s36, s34\n" \
    "s_add_u32 s36, s36, %[dx]\n" \
    "s_branch .Lup_Obtb_%=\n" \
    ".Lup_Obta_%=:\n" \
    "s_sleep 1\n" \
    ".Lup_Obtb_%=:\n" \
    "s_memrealtime vcc\n" \
    "s_waitcnt lgkmcnt(0)\n" \
    "s_sub_i32 vcc_lo, s36, vcc_lo\n" \
    "s_cmp_gt_i32 vcc_lo, 8\n" \
    "s_cbranch_scc1 .Lup_Obta_%=\n" \
    "s_cmp_gt_i32 vcc_lo, 0\n" \
    "s_cbranch_scc1 .Lup_Obtb_%=\n" \
    ".Lup_Obx_%=:\n" \
    "s_mul_i32 s41, s32, 40960\n" \
    "s_add_u32 s41, s41, s17\n" \
    "buffer_load_dwordx4 v[216:219], v168, s[8:11], s41 offen sc1\n" \
    "s_add_u32 s41, s41, 8192\n" \
    "buffer_load_dwordx4 v[220:223], v168, s[8:11], s41 offen sc1\n" \
    "s_add_u32 s41, s41, 8192\n" \
    "buffer_load_dwordx4 v[224:227], v168, s[8:11], s41 offen sc1\n" \
    "s_mov_b32 s37, 0\n" \
    ".Lup_srOb_%=:\n" \
    "s_waitcnt vmcnt(2)\n" \
    "v_max3_u32 v254, v216, v217, v218\n" \
    "s_waitcnt vmcnt(1)\n" \
    "v_max3_u32 v254, v254, v219, v220\n" \
    "v_max3_u32 v254, v254, v221, v222\n" \
    "s_waitcnt vmcnt(0)\n" \
    "v_max3_u32 v254, v254, v223, v224\n" \
    "v_max3_u32 v254, v254, v225, v226\n" \
    "v_max_u32 v254, v254, v227\n" \
    "v_cmp_eq_u32 vcc, -1, v254\n" \
    "s_nop 0\n" \
    "s_cbranch_vccnz .Lup_scOb_%=\n" \
    ".Lup_sxOb_%=:\n" \
    "v_mfma_f32_16x16x32_bf16 v[244:247], v[216:219], v[216:219], 0\n" \
    "ds_write_b128 v169, v[216:219] offset:0\n" \
    "v_mfma_f32_16x16x32_bf16 v[244:247], v[220:223], v[220:223], v[244:247]\n" \
    "ds_write_b128 v169, v[220:223] offset:8192\n" \
    "v_mfma_f32_16x16x32_bf16 v[244:247], v[224:227], v[224:227], v[244:247]\n" \
    "ds_write_b128 v169, v[224:227] offset:16384\n" \
    "v_mfma_f32_16x16x32_bf16 v[240:243], v[178:181], v[216:219], 0\n" \
    "v_mfma_f32_16x16x32_bf16 v[240:243], v[178:181], v[220:223], v[240:243]\n" \
    "v_mfma_f32_16x16x32_bf16 v[240:243], v[178:181], v[224:227], v[240:243]\n" \
    "s_nop 3\n" \
    "v_cndmask_b32 v254, v244, v245, s[44:45]\n" \
    "v_cndmask_b32 v255, v246, v247, s[44:45]\n" \
    "s_nop 1\n" \
    "v_cndmask_b32 v241, v254, v255, s[46:47]\n" \
    "ds_write_b64 v171, v[240:241] offset:0\n" \
    "s_waitcnt lgkmcnt(0)\n" \
    "s_barrier\n" \
    "ds_read_b128 v[184:187], v168 offset:0\n" \
    "ds_read_b128 v[188:191], v168 offset:1024\n" \
    "ds_read_b128 v[192:195], v168 offset:2048\n" \
    "ds_read_b128 v[196:199], v168 offset:3072\n" \
    "ds_read_b128 v[200:203], v168 offset:4096\n" \
    "ds_read2_b64 v[248:251], v172 offset0:0 offset1:16\n" \
    "s_waitcnt lgkmcnt(5)\n" \
    "v_mfma_f32_16x16x32_bf16 v[208:211], v[0:3], v[184:187], 0\n" \
    "ds_read_b128 v[204:207], v168 offset:5120\n" \
    "v_mfma_f32_16x16x32_bf16 v[212:215], v[80:83], v[184:187], 0\n" \
    "s_waitcnt lgkmcnt(5)\n" \
    "v_mfma_f32_16x16x32_bf16 v[208:211], v[4:7], v[188:191], v[208:211]\n" \
    "ds_read_b128 v[184:187], v168 offset:6144\n" \
    "v_mfma_f32_16x16x32_bf16 v[212:215], v[84:87], v[188:191], v[212:215]\n" \
    "s_mul_i32 s39, s32, 98304\n" \
    "s_add_u32 s39, s39, 0\n" \
    "s_waitcnt lgkmcnt(5)\n" \
    "v_mfma_f32_16x16x32_bf16 v[208:211], v[8:11], v[192:195], v[208:211]\n" \
    "ds_read_b128 v[188:191], v168 offset:7168\n" \
    "v_mfma_f32_16x16x32_bf16 v[212:215], v[88:91], v[192:195], v[212:215]\n" \
    "s_add_u32 s40, s39, %[pdist]*98304\n" \
    "s_cmp_lt_u32 s32, 64-%[pdist]\n" \
    "s_cselect_b32 s40, s40, 0x80000000\n" \
    "s_waitcnt lgkmcnt(5)\n" \
    "v_mfma_f32_16x16x32_bf16 v[208:211], v[12:15], v[196:199], v[208:211]\n" \
    "ds_read_b128 v[192:195], v168 offset:8192\n" \
    "v_mfma_f32_16x16x32_bf16 v[212:215], v[92:95], v[196:199], v[212:215]\n" \
    "s_waitcnt lgkmcnt(4)\n" \
    "v_add_f32 v248, v248, v250\n" \
    "v_mfma_f32_16x16x32_bf16 v[208:211], v[16:19], v[200:203], v[208:211]\n" \
    "ds_read_b128 v[196:199], v168 offset:9216\n" \
    "v_mfma_f32_16x16x32_bf16 v[212:215], v[96:99], v[200:203], v[212:215]\n" \
    "v_add_f32 v249, v249, v251\n" \
    "s_waitcnt lgkmcnt(4)\n" \
    "v_mfma_f32_16x16x32_bf16 v[208:211], v[20:23], v[204:207], v[208:211]\n" \
    "ds_read_b128 v[200:203], v168 offset:10240\n" \
    "v_mfma_f32_16x16x32_bf16 v[212:215], v[100:103], v[204:207], v[212:215]\n" \
    "v_permlane16_swap_b32 v248, v249\n" \
    "s_waitcnt lgkmcnt(4)\n" \
    "v_mfma_f32_16x16x32_bf16 v[208:211], v[24:27], v[184:187], v[208:211]\n" \
    "ds_read_b128 v[204:207], v168 offset:11264\n" \
    "v_mfma_f32_16x16x32_bf16 v[212:215], v[104:107], v[184:187], v[212:215]\n" \
    "v_add_f32 v250, v248, v249\n" \
    "s_waitcnt lgkmcnt(4)\n" \
    "v_mfma_f32_16x16x32_bf16 v[208:211], v[28:31], v[188:191], v[208:211]\n" \
    "ds_read_b128 v[184:187], v168 offset:12288\n" \
    "v_mfma_f32_16x16x32_bf16 v[212:215], v[108:111], v[188:191], v[212:215]\n" \
    "v_add_f32 v248, v248, v249\n" \
    "s_waitcnt lgkmcnt(4)\n" \
    "v_mfma_f32_16x16x32_bf16 v[208:211], v[32:35], v[192:195], v[208:211]\n" \
    "ds_read_b128 v[188:191], v168 offset:13312\n" \
    "v_mfma_f32_16x16x32_bf16 v[212:215], v[112:115], v[192:195], v[212:215]\n" \
    "v_permlane32_swap_b32 v248, v250\n" \
    "s_waitcnt lgkmcnt(4)\n" \
    "v_mfma_f32_16x16x32_bf16 v[208:211], v[36:39], v[196:199], v[208:211]\n" \
    "ds_read_b128 v[192:195], v168 offset:14336\n" \
    "v_mfma_f32_16x16x32_bf16 v[212:215], v[116:119], v[196:199], v[212:215]\n" \
    "v_add_f32 v249, v248, v250\n" \
    "s_waitcnt lgkmcnt(4)\n" \
    "v_mfma_f32_16x16x32_bf16 v[208:211], v[40:43], v[200:203], v[208:211]\n" \
    "ds_read_b128 v[196:199], v168 offset:15360\n" \
    "v_mfma_f32_16x16x32_bf16 v[212:215], v[120:123], v[200:203], v[212:215]\n" \
    "v_add_f32 v248, v248, v250\n" \
    "s_waitcnt lgkmcnt(4)\n" \
    "v_mfma_f32_16x16x32_bf16 v[208:211], v[44:47], v[204:207], v[208:211]\n" \
    "ds_read_b128 v[200:203], v168 offset:16384\n" \
    "v_mfma_f32_16x16x32_bf16 v[212:215], v[124:127], v[204:207], v[212:215]\n" \
    "v_permlane16_swap_b32 v248, v249\n" \
    "s_waitcnt lgkmcnt(4)\n" \
    "v_mfma_f32_16x16x32_bf16 v[208:211], v[48:51], v[184:187], v[208:211]\n" \
    "ds_read_b128 v[204:207], v168 offset:17408\n" \
    "v_mfma_f32_16x16x32_bf16 v[212:215], v[128:131], v[184:187], v[212:215]\n" \
    "v_mul_f32 v252, 0x3acccccd, v248\n" \
    "s_waitcnt lgkmcnt(4)\n" \
    "v_mfma_f32_16x16x32_bf16 v[208:211], v[52:55], v[188:191], v[208:211]\n" \
    "ds_read_b128 v[184:187], v168 offset:18432\n" \
    "v_mfma_f32_16x16x32_bf16 v[212:215], v[132:135], v[188:191], v[212:215]\n" \
    "v_mul_f32 v249, 0x3acccccd, v249\n" \
    "s_waitcnt lgkmcnt(4)\n" \
    "v_mfma_f32_16x16x32_bf16 v[208:211], v[56:59], v[192:195], v[208:211]\n" \
    "ds_read_b128 v[188:191], v168 offset:19456\n" \
    "v_mfma_f32_16x16x32_bf16 v[212:215], v[136:139], v[192:195], v[212:215]\n" \
    "v_fma_f32 v249, -v252, v252, v249\n" \
    "s_waitcnt lgkmcnt(4)\n" \
    "v_mfma_f32_16x16x32_bf16 v[208:211], v[60:63], v[196:199], v[208:211]\n" \
    "v_mfma_f32_16x16x32_bf16 v[212:215], v[140:143], v[196:199], v[212:215]\n" \
    "v_max_f32 v249, 0, v249\n" \
    "s_waitcnt lgkmcnt(3)\n" \
    "v_mfma_f32_16x16x32_bf16 v[208:211], v[64:67], v[200:203], v[208:211]\n" \
    "v_mfma_f32_16x16x32_bf16 v[212:215], v[144:147], v[200:203], v[212:215]\n" \
    "v_add_f32 v249, 0x3727c5ac, v249\n" \
    "s_waitcnt lgkmcnt(2)\n" \
    "v_mfma_f32_16x16x32_bf16 v[208:211], v[68:71], v[204:207], v[208:211]\n" \
    "v_mfma_f32_16x16x32_bf16 v[212:215], v[148:151], v[204:207], v[212:215]\n" \
    "v_rsq_f32 v253, v249\n" \
    "s_waitcnt lgkmcnt(1)\n" \
    "v_mfma_f32_16x16x32_bf16 v[208:211], v[72:75], v[184:187], v[208:211]\n" \
    "v_mfma_f32_16x16x32_bf16 v[212:215], v[152:155], v[184:187], v[212:215]\n" \
    "v_mul_f32 v251, 0xbfb8aa3b, v253\n" \
    "s_waitcnt lgkmcnt(0)\n" \
    "v_mfma_f32_16x16x32_bf16 v[208:211], v[76:79], v[188:191], v[208:211]\n" \
    "v_mfma_f32_16x16x32_bf16 v[212:215], v[156:159], v[188:191], v[212:215]\n" \
    "s_nop 6\n" \
    "v_fma_f32 v208, -v252, v160, v208\n" \
    "v_fma_f32 v209, -v252, v161, v209\n" \
    "v_fma_f32 v210, -v252, v162, v210\n" \
    "v_fma_f32 v211, -v252, v163, v211\n" \
    "v_mul_f32 v208, v251, v208\n" \
    "v_mul_f32 v209, v251, v209\n" \
    "v_mul_f32 v210, v251, v210\n" \
    "v_mul_f32 v211, v251, v211\n" \
    "v_exp_f32 v208, v208\n" \
    "v_fma_f32 v212, -v252, v164, v212\n" \
    "v_exp_f32 v209, v209\n" \
    "v_fma_f32 v213, -v252, v165, v213\n" \
    "v_exp_f32 v210, v210\n" \
    "v_fma_f32 v214, -v252, v166, v214\n" \
    "v_exp_f32 v211, v211\n" \
    "v_fma_f32 v215, -v252, v167, v215\n" \
    "v_add_f32 v208, 1.0, v208\n" \
    "v_add_f32 v209, 1.0, v209\n" \
    "v_add_f32 v210, 1.0, v210\n" \
    "v_add_f32 v211, 1.0, v211\n" \
    "v_rcp_f32 v208, v208\n" \
    "v_mul_f32 v212, v253, v212\n" \
    "v_rcp_f32 v209, v209\n" \
    "v_mul_f32 v213, v253, v213\n" \
    "v_rcp_f32 v210, v210\n" \
    "v_mul_f32 v214, v253, v214\n" \
    "v_rcp_f32 v211, v211\n" \
    "v_mul_f32 v215, v253, v215\n" \
    "v_mul_f32 v208, v208, v212\n" \
    "v_mul_f32 v209, v209, v213\n" \
    "v_mul_f32 v210, v210, v214\n" \
    "v_mul_f32 v211, v211, v215\n" \
    "v_cvt_pk_bf16_f32 v240, v208, v209\n" \
    "v_cvt_pk_bf16_f32 v241, v210, v211\n" \
    "v_min_u32 v240, -2, v240\n" \
    "v_min_u32 v241, -2, v241\n" \
    "buffer_store_dwordx2 v[240:241], v173, s[8:11], s39 offen sc1\n" \
    "buffer_store_dwordx2 v[182:183], v173, s[8:11], s40 offen sc1\n" \
    ".Lup_srOc_%=:\n" \
    "v_max3_u32 v254, v228, v229, v230\n" \
    "v_max3_u32 v254, v254, v231, v232\n" \
    "v_max3_u32 v254, v254, v233, v234\n" \
    "v_max3_u32 v254, v254, v235, v236\n" \
    "v_max3_u32 v254, v254, v237, v238\n" \
    "v_max_u32 v254, v254, v239\n" \
    "v_cmp_eq_u32 vcc, -1, v254\n" \
    "s_nop 0\n" \
    "s_cbranch_vccnz .Lup_scOc_%=\n" \
    ".Lup_sxOc_%=:\n" \
    "v_mfma_f32_16x16x32_bf16 v[244:247], v[228:231], v[228:231], 0\n" \
    "ds_write_b128 v169, v[228:231] offset:20480\n" \
    "v_mfma_f32_16x16x32_bf16 v[244:247], v[232:235], v[232:235], v[244:247]\n" \
    "ds_write_b128 v169, v[232:235] offset:28672\n" \
    "v_mfma_f32_16x16x32_bf16 v[244:247], v[236:239], v[236:239], v[244:247]\n" \
    "ds_write_b128 v169, v[236:239] offset:36864\n" \
    "v_mfma_f32_16x16x32_bf16 v[240:243], v[178:181], v[228:231], 0\n" \
    "v_mfma_f32_16x16x32_bf16 v[240:243], v[178:181], v[232:235], v[240:243]\n" \
    "v_mfma_f32_16x16x32_bf16 v[240:243], v[178:181], v[236:239], v[240:243]\n" \
    "s_nop 3\n" \
    "v_cndmask_b32 v254, v244, v245, s[44:45]\n" \
    "v_cndmask_b32 v255, v246, v247, s[44:45]\n" \
    "s_nop 1\n" \
    "v_cndmask_b32 v241, v254, v255, s[46:47]\n" \
    "ds_write_b64 v171, v[240:241] offset:1024\n" \
    "s_waitcnt lgkmcnt(0)\n" \
    "s_barrier\n" \
    "s_add_u32 s36, s33, 1\n" \
    "ds_read_b128 v[184:187], v168 offset:20480\n" \
    "ds_read_b128 v[188:191], v168 offset:21504\n" \
    "ds_read_b128 v[192:195], v168 offset:22528\n" \
    "ds_read_b128 v[196:199], v168 offset:23552\n" \
    "ds_read_b128 v[200:203], v168 offset:24576\n" \
    "ds_read2_b64 v[248:251], v172 offset0:128 offset1:144\n" \
    "s_waitcnt lgkmcnt(5)\n" \
    "v_mfma_f32_16x16x32_bf16 v[208:211], v[0:3], v[184:187], 0\n" \
    "ds_read_b128 v[204:207], v168 offset:25600\n" \
    "v_mfma_f32_16x16x32_bf16 v[212:215], v[80:83], v[184:187], 0\n" \
    "s_mul_i32 s41, s36, 40960\n" \
    "s_add_u32 s41, s41, s17\n" \
    "s_add_u32 s41, s41, 2621440\n" \
    "buffer_load_dwordx4 v[228:231], v168, s[8:11], s41 offen sc1\n" \
    "s_add_u32 s41, s41, 8192\n" \
    "buffer_load_dwordx4 v[232:235], v168, s[8:11], s41 offen sc1\n" \
    "s_add_u32 s41, s41, 8192\n" \
    "buffer_load_dwordx4 v[236:239], v168, s[8:11], s41 offen sc1\n" \
    "s_waitcnt lgkmcnt(5)\n" \
    "v_mfma_f32_16x16x32_bf16 v[208:211], v[4:7], v[188:191], v[208:211]\n" \
    "ds_read_b128 v[184:187], v168 offset:26624\n" \
    "v_mfma_f32_16x16x32_bf16 v[212:215], v[84:87], v[188:191], v[212:215]\n" \
    "s_mul_i32 s39, s33, 98304\n" \
    "s_add_u32 s39, s39, 6291456\n" \
    "s_waitcnt lgkmcnt(5)\n" \
    "v_mfma_f32_16x16x32_bf16 v[208:211], v[8:11], v[192:195], v[208:211]\n" \
    "ds_read_b128 v[188:191], v168 offset:27648\n" \
    "v_mfma_f32_16x16x32_bf16 v[212:215], v[88:91], v[192:195], v[212:215]\n" \
    "s_add_u32 s40, s39, %[pdist]*98304\n" \
    "s_cmp_lt_u32 s33, 64-%[pdist]\n" \
    "s_cselect_b32 s40, s40, 0x80000000\n" \
    "s_waitcnt lgkmcnt(5)\n" \
    "v_mfma_f32_16x16x32_bf16 v[208:211], v[12:15], v[196:199], v[208:211]\n" \
    "ds_read_b128 v[192:195], v168 offset:28672\n" \
    "v_mfma_f32_16x16x32_bf16 v[212:215], v[92:95], v[196:199], v[212:215]\n" \
    "s_waitcnt lgkmcnt(4)\n" \
    "v_add_f32 v248, v248, v250\n" \
    "v_mfma_f32_16x16x32_bf16 v[208:211], v[16:19], v[200:203], v[208:211]\n" \
    "ds_read_b128 v[196:199], v168 offset:29696\n" \
    "v_mfma_f32_16x16x32_bf16 v[212:215], v[96:99], v[200:203], v[212:215]\n" \
    "v_add_f32 v249, v249, v251\n" \
    "s_waitcnt lgkmcnt(4)\n" \
    "v_mfma_f32_16x16x32_bf16 v[208:211], v[20:23], v[204:207], v[208:211]\n" \
    "ds_read_b128 v[200:203], v168 offset:30720\n" \
    "v_mfma_f32_16x16x32_bf16 v[212:215], v[100:103], v[204:207], v[212:215]\n" \
    "v_permlane16_swap_b32 v248, v249\n" \
    "s_waitcnt lgkmcnt(4)\n" \
    "v_mfma_f32_16x16x32_bf16 v[208:211], v[24:27], v[184:187], v[208:211]\n" \
    "ds_read_b128 v[204:207], v168 offset:31744\n" \
    "v_mfma_f32_16x16x32_bf16 v[212:215], v[104:107], v[184:187], v[212:215]\n" \
    "v_add_f32 v250, v248, v249\n" \
    "s_waitcnt lgkmcnt(4)\n" \
    "v_mfma_f32_16x16x32_bf16 v[208:211], v[28:31], v[188:191], v[208:211]\n" \
    "ds_read_b128 v[184:187], v168 offset:32768\n" \
    "v_mfma_f32_16x16x32_bf16 v[212:215], v[108:111], v[188:191], v[212:215]\n" \
    "v_add_f32 v248, v248, v249\n" \
    "s_waitcnt lgkmcnt(4)\n" \
    "v_mfma_f32_16x16x32_bf16 v[208:211], v[32:35], v[192:195], v[208:211]\n" \
    "ds_read_b128 v[188:191], v168 offset:33792\n" \
    "v_mfma_f32_16x16x32_bf16 v[212:215], v[112:115], v[192:195], v[212:215]\n" \
    "v_permlane32_swap_b32 v248, v250\n" \
    "s_waitcnt lgkmcnt(4)\n" \
    "v_mfma_f32_16x16x32_bf16 v[208:211], v[36:39], v[196:199], v[208:211]\n" \
    "ds_read_b128 v[192:195], v168 offset:34816\n" \
    "v_mfma_f32_16x16x32_bf16 v[212:215], v[116:119], v[196:199], v[212:215]\n" \
    "v_add_f32 v249, v248, v250\n" \
    "s_waitcnt lgkmcnt(4)\n" \
    "v_mfma_f32_16x16x32_bf16 v[208:211], v[40:43], v[200:203], v[208:211]\n" \
    "ds_read_b128 v[196:199], v168 offset:35840\n" \
    "v_mfma_f32_16x16x32_bf16 v[212:215], v[120:123], v[200:203], v[212:215]\n" \
    "v_add_f32 v248, v248, v250\n" \
    "s_add_u32 s48, s32, 3\n" \
    "s_min_u32 s48, s48, 64\n" \
    "s_and_b32 s49, s48, 1\n" \
    "s_xor_b32 s49, s49, 1\n" \
    "s_lshl_b32 s49, s49, 9\n" \
    "s_add_u32 s49, s49, s18\n" \
    "buffer_load_dwordx2 v[176:177], off, s[8:11], s49 sc1\n" \
    "s_waitcnt lgkmcnt(4)\n" \
    "v_mfma_f32_16x16x32_bf16 v[208:211], v[44:47], v[204:207], v[208:211]\n" \
    "ds_read_b128 v[200:203], v168 offset:36864\n" \
    "v_mfma_f32_16x16x32_bf16 v[212:215], v[124:127], v[204:207], v[212:215]\n" \
    "v_permlane16_swap_b32 v248, v249\n" \
    "s_waitcnt lgkmcnt(4)\n" \
    "v_mfma_f32_16x16x32_bf16 v[208:211], v[48:51], v[184:187], v[208:211]\n" \
    "ds_read_b128 v[204:207], v168 offset:37888\n" \
    "v_mfma_f32_16x16x32_bf16 v[212:215], v[128:131], v[184:187], v[212:215]\n" \
    "v_mul_f32 v252, 0x3acccccd, v248\n" \
    "s_waitcnt lgkmcnt(4)\n" \
    "v_mfma_f32_16x16x32_bf16 v[208:211], v[52:55], v[188:191], v[208:211]\n" \
    "ds_read_b128 v[184:187], v168 offset:38912\n" \
    "v_mfma_f32_16x16x32_bf16 v[212:215], v[132:135], v[188:191], v[212:215]\n" \
    "v_mul_f32 v249, 0x3acccccd, v249\n" \
    "s_waitcnt lgkmcnt(4)\n" \
    "v_mfma_f32_16x16x32_bf16 v[208:211], v[56:59], v[192:195], v[208:211]\n" \
    "ds_read_b128 v[188:191], v168 offset:39936\n" \
    "v_mfma_f32_16x16x32_bf16 v[212:215], v[136:139], v[192:195], v[212:215]\n" \
    "v_fma_f32 v249, -v252, v252, v249\n" \
    "s_waitcnt lgkmcnt(4)\n" \
    "v_mfma_f32_16x16x32_bf16 v[208:211], v[60:63], v[196:199], v[208:211]\n" \
    "v_mfma_f32_16x16x32_bf16 v[212:215], v[140:143], v[196:199], v[212:215]\n" \
    "v_max_f32 v249, 0, v249\n" \
    "s_waitcnt lgkmcnt(3)\n" \
    "v_mfma_f32_16x16x32_bf16 v[208:211], v[64:67], v[200:203], v[208:211]\n" \
    "v_mfma_f32_16x16x32_bf16 v[212:215], v[144:147], v[200:203], v[212:215]\n" \
    "v_add_f32 v249, 0x3727c5ac, v249\n" \
    "s_waitcnt lgkmcnt(2)\n" \
    "v_mfma_f32_16x16x32_bf16 v[208:211], v[68:71], v[204:207], v[208:211]\n" \
    "v_mfma_f32_16x16x32_bf16 v[212:215], v[148:151], v[204:207], v[212:215]\n" \
    "v_rsq_f32 v253, v249\n" \
    "s_waitcnt lgkmcnt(1)\n" \
    "v_mfma_f32_16x16x32_bf16 v[208:211], v[72:75], v[184:187], v[208:211]\n" \
    "v_mfma_f32_16x16x32_bf16 v[212:215], v[152:155], v[184:187], v[212:215]\n" \
    "v_mul_f32 v251, 0xbfb8aa3b, v253\n" \
    "s_waitcnt lgkmcnt(0)\n" \
    "v_mfma_f32_16x16x32_bf16 v[208:211], v[76:79], v[188:191], v[208:211]\n" \
    "v_mfma_f32_16x16x32_bf16 v[212:215], v[156:159], v[188:191], v[212:215]\n" \
    "s_nop 6\n" \
    "v_fma_f32 v208, -v252, v160, v208\n" \
    "v_fma_f32 v209, -v252, v161, v209\n" \
    "v_fma_f32 v210, -v252, v162, v210\n" \
    "v_fma_f32 v211, -v252, v163, v211\n" \
    "v_mul_f32 v208, v251, v208\n" \
    "v_mul_f32 v209, v251, v209\n" \
    "v_mul_f32 v210, v251, v210\n" \
    "v_mul_f32 v211, v251, v211\n" \
    "v_exp_f32 v208, v208\n" \
    "v_fma_f32 v212, -v252, v164, v212\n" \
    "v_exp_f32 v209, v209\n" \
    "v_fma_f32 v213, -v252, v165, v213\n" \
    "v_exp_f32 v210, v210\n" \
    "v_fma_f32 v214, -v252, v166, v214\n" \
    "v_exp_f32 v211, v211\n" \
    "v_fma_f32 v215, -v252, v167, v215\n" \
    "v_add_f32 v208, 1.0, v208\n" \
    "v_add_f32 v209, 1.0, v209\n" \
    "v_add_f32 v210, 1.0, v210\n" \
    "v_add_f32 v211, 1.0, v211\n" \
    "v_rcp_f32 v208, v208\n" \
    "v_mul_f32 v212, v253, v212\n" \
    "v_rcp_f32 v209, v209\n" \
    "v_mul_f32 v213, v253, v213\n" \
    "v_rcp_f32 v210, v210\n" \
    "v_mul_f32 v214, v253, v214\n" \
    "v_rcp_f32 v211, v211\n" \
    "v_mul_f32 v215, v253, v215\n" \
    "v_mul_f32 v208, v208, v212\n" \
    "v_mul_f32 v209, v209, v213\n" \
    "v_mul_f32 v210, v210, v214\n" \
    "v_mul_f32 v211, v211, v215\n" \
    "v_cvt_pk_bf16_f32 v240, v208, v209\n" \
    "v_cvt_pk_bf16_f32 v241, v210, v211\n" \
    "v_min_u32 v240, -2, v240\n" \
    "v_min_u32 v241, -2, v241\n" \
    "buffer_store_dwordx2 v[240:241], v173, s[8:11], s39 offen sc1\n" \
    "buffer_store_dwordx2 v[182:183], v173, s[8:11], s40 offen sc1\n" \
    "s_add_u32 s33, s33, 1\n" \
    "s_add_u32 s32, s32, 1\n" \
    "s_cmp_lt_u32 s32, 64\n" \
    "s_cbranch_scc1 .Lup_mO_%=\n" \
    "s_add_u32 s18, s18, 1024\n" \
    "s_waitcnt vmcnt(0)\n" \
    "s_add_u32 s48, s33, 2\n" \
    "s_min_u32 s48, s48, 64\n" \
    "s_and_b32 s49, s48, 1\n" \
    "s_xor_b32 s49, s49, 1\n" \
    "s_lshl_b32 s49, s49, 9\n" \
    "s_add_u32 s49, s49, s18\n" \
    "buffer_load_dwordx2 v[176:177], off, s[8:11], s49 sc1\n" \
    "s_waitcnt vmcnt(0)\n" \
    ".Lup_nO_%=:\n" \
    "s_barrier\n" \
    "s_mov_b32 s37, 0\n" \
    "s_waitcnt vmcnt(2)\n" \
    ".Lup_Odp_%=:\n" \
    "v_readfirstlane_b32 s36, v177\n" \
    "s_nop 0\n" \
    "s_cmp_gt_i32 s36, s48\n" \
    "s_cbranch_scc1 .Lup_Odx_%=\n" \
    "s_cmp_eq_u32 s36, s48\n" \
    "s_cbranch_scc1 .Lup_Odw_%=\n" \
    "s_sleep 3\n" \
    "buffer_load_dwordx2 v[176:177], off, s[8:11], s49 sc1\n" \
    "s_waitcnt vmcnt(0)\n" \
    "s_add_u32 s37, s37, 1\n" \
    "s_cmp_lt_u32 s37, s38\n" \
    "s_cbranch_scc1 .Lup_Odp_%=\n" \
    "s_mov_b32 s38, 0\n" \
    "s_branch .Lup_Odx_%=\n" \
    ".Lup_Odw_%=:\n" \
    "v_readfirstlane_b32 s36, v176\n" \
    "s_cmp_eq_u32 s33, 63\n" \
    "s_cselect_b32 s34, %[dlast], 0\n" \
    "s_add_u32 s36, s36, s34\n" \
    "s_add_u32 s36, s36, %[dx]\n" \
    "s_branch .Lup_Odtb_%=\n" \
    ".Lup_Odta_%=:\n" \
    "s_sleep 1\n" \
    ".Lup_Odtb_%=:\n" \
    "s_memrealtime vcc\n" \
    "s_waitcnt lgkmcnt(0)\n" \
    "s_sub_i32 vcc_lo, s36, vcc_lo\n" \
    "s_cmp_gt_i32 vcc_lo, 8\n" \
    "s_cbranch_scc1 .Lup_Odta_%=\n" \
    "s_cmp_gt_i32 vcc_lo, 0\n" \
    "s_cbranch_scc1 .Lup_Odtb_%=\n" \
    ".Lup_Odx_%=:\n" \
    "s_mul_i32 s41, s33, 40960\n" \
    "s_add_u32 s41, s41, s17\n" \
    "s_add_u32 s41, s41, 2621440\n" \
    "buffer_load_dwordx4 v[228:231], v168, s[8:11], s41 offen sc1\n" \
    "s_add_u32 s41, s41, 8192\n" \
    "buffer_load_dwordx4 v[232:235], v168, s[8:11], s41 offen sc1\n" \
    "s_add_u32 s41, s41, 8192\n" \
    "buffer_load_dwordx4 v[236:239], v168, s[8:11], s41 offen sc1\n" \
    "s_mov_b32 s37, 0\n" \
    ".Lup_srOd_%=:\n" \
    "s_waitcnt vmcnt(2)\n" \
    "v_max3_u32 v254, v228, v229, v230\n" \
    "s_waitcnt vmcnt(1)\n" \
    "v_max3_u32 v254, v254, v231, v232\n" \
    "v_max3_u32 v254, v254, v233, v234\n" \
    "s_waitcnt vmcnt(0)\n" \
    "v_max3_u32 v254, v254, v235, v236\n" \
    "v_max3_u32 v254, v254, v237, v238\n" \
    "v_max_u32 v254, v254, v239\n" \
    "v_cmp_eq_u32 vcc, -1, v254\n" \
    "s_nop 0\n" \
    "s_cbranch_vccnz .Lup_scOd_%=\n" \
    ".Lup_sxOd_%=:\n" \
    "v_mfma_f32_16x16x32_bf16 v[244:247], v[228:231], v[228:231], 0\n" \
    "ds_write_b128 v169, v[228:231] offset:20480\n" \
    "v_mfma_f32_16x16x32_bf16 v[244:247], v[232:235], v[232:235], v[244:247]\n" \
    "ds_write_b128 v169, v[232:235] offset:28672\n" \
    "v_mfma_f32_16x16x32_bf16 v[244:247], v[236:239], v[236:239], v[244:247]\n" \
    "ds_write_b128 v169, v[236:239] offset:36864\n" \
    "v_mfma_f32_16x16x32_bf16 v[240:243], v[178:181], v[228:231], 0\n" \
    "v_mfma_f32_16x16x32_bf16 v[240:243], v[178:181], v[232:235], v[240:243]\n" \
    "v_mfma_f32_16x16x32_bf16 v[240:243], v[178:181], v[236:239], v[240:243]\n" \
    "s_nop 3\n" \
    "v_cndmask_b32 v254, v244, v245, s[44:45]\n" \
    "v_cndmask_b32 v255, v246, v247, s[44:45]\n" \
    "s_nop 1\n" \
    "v_cndmask_b32 v241, v254, v255, s[46:47]\n" \
    "ds_write_b64 v171, v[240:241] offset:1024\n" \
    "s_waitcnt lgkmcnt(0)\n" \
    "s_barrier\n" \
    "ds_read_b128 v[184:187], v168 offset:20480\n" \
    "ds_read_b128 v[188:191], v168 offset:21504\n" \
    "ds_read_b128 v[192:195], v168 offset:22528\n" \
    "ds_read_b128 v[196:199], v168 offset:23552\n" \
    "ds_read_b128 v[200:203], v168 offset:24576\n" \
    "ds_read2_b64 v[248:251], v172 offset0:128 offset1:144\n" \
    "s_waitcnt lgkmcnt(5)\n" \
    "v_mfma_f32_16x16x32_bf16 v[208:211], v[0:3], v[184:187], 0\n" \
    "ds_read_b128 v[204:207], v168 offset:25600\n" \
    "v_mfma_f32_16x16x32_bf16 v[212:215], v[80:83], v[184:187], 0\n" \
    "s_waitcnt lgkmcnt(5)\n" \
    "v_mfma_f32_16x16x32_bf16 v[208:211], v[4:7], v[188:191], v[208:211]\n" \
    "ds_read_b128 v[184:187], v168 offset:26624\n" \
    "v_mfma_f32_16x16x32_bf16 v[212:215], v[84:87], v[188:191], v[212:215]\n" \
    "s_mul_i32 s39, s33, 98304\n" \
    "s_add_u32 s39, s39, 6291456\n" \
    "s_waitcnt lgkmcnt(5)\n" \
    "v_mfma_f32_16x16x32_bf16 v[208:211], v[8:11], v[192:195], v[208:211]\n" \
    "ds_read_b128 v[188:191], v168 offset:27648\n" \
    "v_mfma_f32_16x16x32_bf16 v[212:215], v[88:91], v[192:195], v[212:215]\n" \
    "s_add_u32 s40, s39, %[pdist]*98304\n" \
    "s_cmp_lt_u32 s33, 64-%[pdist]\n" \
    "s_cselect_b32 s40, s40, 0x80000000\n" \
    "s_waitcnt lgkmcnt(5)\n" \
    "v_mfma_f32_16x16x32_bf16 v[208:211], v[12:15], v[196:199], v[208:211]\n" \
    "ds_read_b128 v[192:195], v168 offset:28672\n" \
    "v_mfma_f32_16x16x32_bf16 v[212:215], v[92:95], v[196:199], v[212:215]\n" \
    "s_waitcnt lgkmcnt(4)\n" \
    "v_add_f32 v248, v248, v250\n" \
    "v_mfma_f32_16x16x32_bf16 v[208:211], v[16:19], v[200:203], v[208:211]\n" \
    "ds_read_b128 v[196:199], v168 offset:29696\n" \
    "v_mfma_f32_16x16x32_bf16 v[212:215], v[96:99], v[200:203], v[212:215]\n" \
    "v_add_f32 v249, v249, v251\n" \
    "s_waitcnt lgkmcnt(4)\n" \
    "v_mfma_f32_16x16x32_bf16 v[208:211], v[20:23], v[204:207], v[208:211]\n" \
    "ds_read_b128 v[200:203], v168 offset:30720\n" \
    "v_mfma_f32_16x16x32_bf16 v[212:215], v[100:103], v[204:207], v[212:215]\n" \
    "v_permlane16_swap_b32 v248, v249\n" \
    "s_waitcnt lgkmcnt(4)\n" \
    "v_mfma_f32_16x16x32_bf16 v[208:211], v[24:27], v[184:187], v[208:211]\n" \
    "ds_read_b128 v[204:207], v168 offset:31744\n" \
    "v_mfma_f32_16x16x32_bf16 v[212:215], v[104:107], v[184:187], v[212:215]\n" \
    "v_add_f32 v250, v248, v249\n" \
    "s_waitcnt lgkmcnt(4)\n" \
    "v_mfma_f32_16x16x32_bf16 v[208:211], v[28:31], v[188:191], v[208:211]\n" \
    "ds_read_b128 v[184:187], v168 offset:32768\n" \
    "v_mfma_f32_16x16x32_bf16 v[212:215], v[108:111], v[188:191], v[212:215]\n" \
    "v_add_f32 v248, v248, v249\n" \
    "s_waitcnt lgkmcnt(4)\n" \
    "v_mfma_f32_16x16x32_bf16 v[208:211], v[32:35], v[192:195], v[208:211]\n" \
    "ds_read_b128 v[188:191], v168 offset:33792\n" \
    "v_mfma_f32_16x16x32_bf16 v[212:215], v[112:115], v[192:195], v[212:215]\n" \
    "v_permlane32_swap_b32 v248, v250\n" \
    "s_waitcnt lgkmcnt(4)\n" \
    "v_mfma_f32_16x16x32_bf16 v[208:211], v[36:39], v[196:199], v[208:211]\n" \
    "ds_read_b128 v[192:195], v168 offset:34816\n" \
    "v_mfma_f32_16x16x32_bf16 v[212:215], v[116:119], v[196:199], v[212:215]\n" \
    "v_add_f32 v249, v248, v250\n" \
    "s_waitcnt lgkmcnt(4)\n" \
    "v_mfma_f32_16x16x32_bf16 v[208:211], v[40:43], v[200:203], v[208:211]\n" \
    "ds_read_b128 v[196:199], v168 offset:35840\n" \
    "v_mfma_f32_16x16x32_bf16 v[212:215], v[120:123], v[200:203], v[212:215]\n" \
    "v_add_f32 v248, v248, v250\n" \
    "s_add_u32 s48, s33, 3\n" \
    "s_min_u32 s48, s48, 64\n" \
    "s_and_b32 s49, s48, 1\n" \
    "s_xor_b32 s49, s49, 1\n" \
    "s_lshl_b32 s49, s49, 9\n" \
    "s_add_u32 s49, s49, s18\n" \
    "buffer_load_dwordx2 v[176:177], off, s[8:11], s49 sc1\n" \
    "s_waitcnt lgkmcnt(4)\n" \
    "v_mfma_f32_16x16x32_bf16 v[208:211], v[44:47], v[204:207], v[208:211]\n" \
    "ds_read_b128 v[200:203], v168 offset:36864\n" \
    "v_mfma_f32_16x16x32_bf16 v[212:215], v[124:127], v[204:207], v[212:215]\n" \
    "v_permlane16_swap_b32 v248, v249\n" \
    "s_waitcnt lgkmcnt(4)\n" \
    "v_mfma_f32_16x16x32_bf16 v[208:211], v[48:51], v[184:187], v[208:211]\n" \
    "ds_read_b128 v[204:207], v168 offset:37888\n" \
    "v_mfma_f32_16x16x32_bf16 v[212:215], v[128:131], v[184:187], v[212:215]\n" \
    "v_mul_f32 v252, 0x3acccccd, v248\n" \
    "s_waitcnt lgkmcnt(4)\n" \
    "v_mfma_f32_16x16x32_bf16 v[208:211], v[52:55], v[188:191], v[208:211]\n" \
    "ds_read_b128 v[184:187], v168 offset:38912\n" \
    "v_mfma_f32_16x16x32_bf16 v[212:215], v[132:135], v[188:191], v[212:215]\n" \
    "v_mul_f32 v249, 0x3acccccd, v249\n" \
    "s_waitcnt lgkmcnt(4)\n" \
    "v_mfma_f32_16x16x32_bf16 v[208:211], v[56:59], v[192:195], v[208:211]\n" \
    "ds_read_b128 v[188:191], v168 offset:39936\n" \
    "v_mfma_f32_16x16x32_bf16 v[212:215], v[136:139], v[192:195], v[212:215]\n" \
    "v_fma_f32 v249, -v252, v252, v249\n" \
    "s_waitcnt lgkmcnt(4)\n" \
    "v_mfma_f32_16x16x32_bf16 v[208:211], v[60:63], v[196:199], v[208:211]\n" \
    "v_mfma_f32_16x16x32_bf16 v[212:215], v[140:143], v[196:199], v[212:215]\n" \
    "v_max_f32 v249, 0, v249\n" \
    "s_waitcnt lgkmcnt(3)\n" \
    "v_mfma_f32_16x16x32_bf16 v[208:211], v[64:67], v[200:203], v[208:211]\n" \
    "v_mfma_f32_16x16x32_bf16 v[212:215], v[144:147], v[200:203], v[212:215]\n" \
    "v_add_f32 v249, 0x3727c5ac, v249\n" \
    "s_waitcnt lgkmcnt(2)\n" \
    "v_mfma_f32_16x16x32_bf16 v[208:211], v[68:71], v[204:207], v[208:211]\n" \
    "v_mfma_f32_16x16x32_bf16 v[212:215], v[148:151], v[204:207], v[212:215]\n" \
    "v_rsq_f32 v253, v249\n" \
    "s_waitcnt lgkmcnt(1)\n" \
    "v_mfma_f32_16x16x32_bf16 v[208:211], v[72:75], v[184:187], v[208:211]\n" \
    "v_mfma_f32_16x16x32_bf16 v[212:215], v[152:155], v[184:187], v[212:215]\n" \
    "v_mul_f32 v251, 0xbfb8aa3b, v253\n" \
    "s_waitcnt lgkmcnt(0)\n" \
    "v_mfma_f32_16x16x32_bf16 v[208:211], v[76:79], v[188:191], v[208:211]\n" \
    "v_mfma_f32_16x16x32_bf16 v[212:215], v[156:159], v[188:191], v[212:215]\n" \
    "s_nop 6\n" \
    "v_fma_f32 v208, -v252, v160, v208\n" \
    "v_fma_f32 v209, -v252, v161, v209\n" \
    "v_fma_f32 v210, -v252, v162, v210\n" \
    "v_fma_f32 v211, -v252, v163, v211\n" \
    "v_mul_f32 v208, v251, v208\n" \
    "v_mul_f32 v209, v251, v209\n" \
    "v_mul_f32 v210, v251, v210\n" \
    "v_mul_f32 v211, v251, v211\n" \
    "v_exp_f32 v208, v208\n" \
    "v_fma_f32 v212, -v252, v164, v212\n" \
    "v_exp_f32 v209, v209\n" \
    "v_fma_f32 v213, -v252, v165, v213\n" \
    "v_exp_f32 v210, v210\n" \
    "v_fma_f32 v214, -v252, v166, v214\n" \
    "v_exp_f32 v211, v211\n" \
    "v_fma_f32 v215, -v252, v167, v215\n" \
    "v_add_f32 v208, 1.0, v208\n" \
    "v_add_f32 v209, 1.0, v209\n" \
    "v_add_f32 v210, 1.0, v210\n" \
    "v_add_f32 v211, 1.0, v211\n" \
    "v_rcp_f32 v208, v208\n" \
    "v_mul_f32 v212, v253, v212\n" \
    "v_rcp_f32 v209, v209\n" \
    "v_mul_f32 v213, v253, v213\n" \
    "v_rcp_f32 v210, v210\n" \
    "v_mul_f32 v214, v253, v214\n" \
    "v_rcp_f32 v211, v211\n" \
    "v_mul_f32 v215, v253, v215\n" \
    "v_mul_f32 v208, v208, v212\n" \
    "v_mul_f32 v209, v209, v213\n" \
    "v_mul_f32 v210, v210, v214\n" \
    "v_mul_f32 v211, v211, v215\n" \
    "v_cvt_pk_bf16_f32 v240, v208, v209\n" \
    "v_cvt_pk_bf16_f32 v241, v210, v211\n" \
    "v_min_u32 v240, -2, v240\n" \
    "v_min_u32 v241, -2, v241\n" \
    "buffer_store_dwordx2 v[240:241], v173, s[8:11], s39 offen sc1\n" \
    "buffer_store_dwordx2 v[182:183], v173, s[8:11], s40 offen sc1\n" \
    "s_add_u32 s33, s33, 1\n" \
    "s_cmp_lt_u32 s33, 64\n" \
    "s_cbranch_scc1 .Lup_nO_%=\n" \
    "s_waitcnt vmcnt(0)\n" \
    "s_endpgm\n" \
    ".Lup_scYa_%=:\n" \
    "s_add_u32 s37, s37, 1\n" \
    "s_cmp_lt_u32 s37, s38\n" \
    "s_cselect_b32 s38, s38, 0\n" \
    "s_cbranch_scc0 .Lup_sxYa_%=\n" \
    "s_sleep 3\n" \
    "s_mul_i32 s41, s32, 40960\n" \
    "s_add_u32 s41, s41, s17\n" \
    "buffer_load_dwordx4 v[216:219], v168, s[8:11], s41 offen sc1\n" \
    "s_add_u32 s41, s41, 8192\n" \
    "buffer_load_dwordx4 v[220:223], v168, s[8:11], s41 offen sc1\n" \
    "s_add_u32 s41, s41, 8192\n" \
    "buffer_load_dwordx4 v[224:227], v168, s[8:11], s41 offen sc1\n" \
    "s_waitcnt vmcnt(0)\n" \
    "s_branch .Lup_srYa_%=\n" \
    ".Lup_scYb_%=:\n" \
    "s_add_u32 s37, s37, 1\n" \
    "s_cmp_lt_u32 s37, s38\n" \
    "s_cselect_b32 s38, s38, 0\n" \
    "s_cbranch_scc0 .Lup_sxYb_%=\n" \
    "s_sleep 3\n" \
    "s_mul_i32 s41, s32, 40960\n" \
    "s_add_u32 s41, s41, s17\n" \
    "buffer_load_dwordx4 v[216:219], v168, s[8:11], s41 offen sc1\n" \
    "s_add_u32 s41, s41, 8192\n" \
    "buffer_load_dwordx4 v[220:223], v168, s[8:11], s41 offen sc1\n" \
    "s_add_u32 s41, s41, 8192\n" \
    "buffer_load_dwordx4 v[224:227], v168, s[8:11], s41 offen sc1\n" \
    "s_waitcnt vmcnt(0)\n" \
    "s_branch .Lup_srYb_%=\n" \
    ".Lup_scYc_%=:\n" \
    "s_add_u32 s37, s37, 1\n" \
    "s_cmp_lt_u32 s37, s38\n" \
    "s_cselect_b32 s38, s38, 0\n" \
    "s_cbranch_scc0 .Lup_sxYc_%=\n" \
    "s_sleep 3\n" \
    "s_mul_i32 s41, s33, 40960\n" \
    "s_add_u32 s41, s41, s17\n" \
    "s_add_u32 s41, s41, 2621440\n" \
    "buffer_load_dwordx4 v[228:231], v168, s[8:11], s41 offen sc1\n" \
    "s_add_u32 s41, s41, 8192\n" \
    "buffer_load_dwordx4 v[232:235], v168, s[8:11], s41 offen sc1\n" \
    "s_add_u32 s41, s41, 8192\n" \
    "buffer_load_dwordx4 v[236:239], v168, s[8:11], s41 offen sc1\n" \
    "s_waitcnt vmcnt(0)\n" \
    "s_branch .Lup_srYc_%=\n" \
    ".Lup_scYd_%=:\n" \
    "s_add_u32 s37, s37, 1\n" \
    "s_cmp_lt_u32 s37, s38\n" \
    "s_cselect_b32 s38, s38, 0\n" \
    "s_cbranch_scc0 .Lup_sxYd_%=\n" \
    "s_sleep 3\n" \
    "s_mul_i32 s41, s33, 40960\n" \
    "s_add_u32 s41, s41, s17\n" \
    "s_add_u32 s41, s41, 2621440\n" \
    "buffer_load_dwordx4 v[228:231], v168, s[8:11], s41 offen sc1\n" \
    "s_add_u32 s41, s41, 8192\n" \
    "buffer_load_dwordx4 v[232:235], v168, s[8:11], s41 offen sc1\n" \
    "s_add_u32 s41, s41, 8192\n" \
    "buffer_load_dwordx4 v[236:239], v168, s[8:11], s41 offen sc1\n" \
    "s_waitcnt vmcnt(0)\n" \
    "s_branch .Lup_srYd_%=\n" \
    ".Lup_scOa_%=:\n" \
    "s_add_u32 s37, s37, 1\n" \
    "s_cmp_lt_u32 s37, s38\n" \
    "s_cselect_b32 s38, s38, 0\n" \
    "s_cbranch_scc0 .Lup_sxOa_%=\n" \
    "s_sleep 3\n" \
    "s_mul_i32 s41, s32, 40960\n" \
    "s_add_u32 s41, s41, s17\n" \
    "buffer_load_dwordx4 v[216:219], v168, s[8:11], s41 offen sc1\n" \
    "s_add_u32 s41, s41, 8192\n" \
    "buffer_load_dwordx4 v[220:223], v168, s[8:11], s41 offen sc1\n" \
    "s_add_u32 s41, s41, 8192\n" \
    "buffer_load_dwordx4 v[224:227], v168, s[8:11], s41 offen sc1\n" \
    "s_waitcnt vmcnt(0)\n" \
    "s_branch .Lup_srOa_%=\n" \
    ".Lup_scOb_%=:\n" \
    "s_add_u32 s37, s37, 1\n" \
    "s_cmp_lt_u32 s37, s38\n" \
    "s_cselect_b32 s38, s38, 0\n" \
    "s_cbranch_scc0 .Lup_sxOb_%=\n" \
    "s_sleep 3\n" \
    "s_mul_i32 s41, s32, 40960\n" \
    "s_add_u32 s41, s41, s17\n" \
    "buffer_load_dwordx4 v[216:219], v168, s[8:11], s41 offen sc1\n" \
    "s_add_u32 s41, s41, 8192\n" \
    "buffer_load_dwordx4 v[220:223], v168, s[8:11], s41 offen sc1\n" \
    "s_add_u32 s41, s41, 8192\n" \
    "buffer_load_dwordx4 v[224:227], v168, s[8:11], s41 offen sc1\n" \
    "s_waitcnt vmcnt(0)\n" \
    "s_branch .Lup_srOb_%=\n" \
    ".Lup_scOc_%=:\n" \
    "s_add_u32 s37, s37, 1\n" \
    "s_cmp_lt_u32 s37, s38\n" \
    "s_cselect_b32 s38, s38, 0\n" \
    "s_cbranch_scc0 .Lup_sxOc_%=\n" \
    "s_sleep 3\n" \
    "s_mul_i32 s41, s33, 40960\n" \
    "s_add_u32 s41, s41, s17\n" \
    "s_add_u32 s41, s41, 2621440\n" \
    "buffer_load_dwordx4 v[228:231], v168, s[8:11], s41 offen sc1\n" \
    "s_add_u32 s41, s41, 8192\n" \
    "buffer_load_dwordx4 v[232:235], v168, s[8:11], s41 offen sc1\n" \
    "s_add_u32 s41, s41, 8192\n" \
    "buffer_load_dwordx4 v[236:239], v168, s[8:11], s41 offen sc1\n" \
    "s_waitcnt vmcnt(0)\n" \
    "s_branch .Lup_srOc_%=\n" \
    ".Lup_scOd_%=:\n" \
    "s_add_u32 s37, s37, 1\n" \
    "s_cmp_lt_u32 s37, s38\n" \
    "s_cselect_b32 s38, s38, 0\n" \
    "s_cbranch_scc0 .Lup_sxOd_%=\n" \
    "s_sleep 3\n" \
    "s_mul_i32 s41, s33, 40960\n" \
    "s_add_u32 s41, s41, s17\n" \
    "s_add_u32 s41, s41, 2621440\n" \
    "buffer_load_dwordx4 v[228:231], v168, s[8:11], s41 offen sc1\n" \
    "s_add_u32 s41, s41, 8192\n" \
    "buffer_load_dwordx4 v[232:235], v168, s[8:11], s41 offen sc1\n" \
    "s_add_u32 s41, s41, 8192\n" \
    "buffer_load_dwordx4 v[236:239], v168, s[8:11], s41 offen sc1\n" \
    "s_waitcnt vmcnt(0)\n" \
    "s_branch .Lup_srOd_%=\n" \
    ""
#define DN_ASM \
    "s_mov_b32 s32, 0\n" \
    "s_mov_b32 s33, 0\n" \
    "s_mov_b32 s38, 0x40000\n" \
    "v_mov_b32 v236, -1\n" \
    "v_mov_b32 v237, -1\n" \
    "v_mov_b32 v252, v150\n" \
    "s_add_u32 s48, s32, 2\n" \
    "s_min_u32 s48, s48, 64\n" \
    "s_and_b32 s49, s48, 1\n" \
    "s_xor_b32 s49, s49, 1\n" \
    "s_lshl_b32 s49, s49, 9\n" \
    "s_add_u32 s49, s49, s18\n" \
    "buffer_load_dwordx2 v[150:151], off, s[8:11], s49 sc1\n" \
    ".Ldn_l_%=:\n" \
    "s_mov_b32 s37, 0\n" \
    "s_waitcnt vmcnt(0)\n" \
    ".Ldn_ap_%=:\n" \
    "v_readfirstlane_b32 s36, v151\n" \
    "s_nop 0\n" \
    "s_cmp_gt_i32 s36, s48\n" \
    "s_cbranch_scc1 .Ldn_ax_%=\n" \
    "s_cmp_eq_u32 s36, s48\n" \
    "s_cbranch_scc1 .Ldn_aw_%=\n" \
    "s_sleep 3\n" \
    "buffer_load_dwordx2 v[150:151], off, s[8:11], s49 sc1\n" \
    "s_waitcnt vmcnt(0)\n" \
    "s_add_u32 s37, s37, 1\n" \
    "s_cmp_lt_u32 s37, s38\n" \
    "s_cbranch_scc1 .Ldn_ap_%=\n" \
    "s_mov_b32 s38, 0\n" \
    "s_branch .Ldn_ax_%=\n" \
    ".Ldn_aw_%=:\n" \
    "v_readfirstlane_b32 s36, v150\n" \
    "s_cmp_eq_u32 s32, 63\n" \
    "s_cselect_b32 s34, %[dlast], 0\n" \
    "s_add_u32 s36, s36, s34\n" \
    "s_add_u32 s36, s36, %[dxd]\n" \
    "s_branch .Ldn_atb_%=\n" \
    ".Ldn_ata_%=:\n" \
    "s_sleep 1\n" \
    ".Ldn_atb_%=:\n" \
    "s_memrealtime vcc\n" \
    "s_waitcnt lgkmcnt(0)\n" \
    "s_sub_i32 vcc_lo, s36, vcc_lo\n" \
    "s_cmp_gt_i32 vcc_lo, 8\n" \
    "s_cbranch_scc1 .Ldn_ata_%=\n" \
    "s_cmp_gt_i32 vcc_lo, 0\n" \
    "s_cbranch_scc1 .Ldn_atb_%=\n" \
    ".Ldn_ax_%=:\n" \
    "s_mul_i32 s39, s32, 98304\n" \
    "s_add_u32 s39, s39, s17\n" \
    "buffer_load_dwordx4 v[152:155], v144, s[8:11], s39 offen offset:0 sc1\n" \
    "buffer_load_dwordx4 v[156:159], v144, s[8:11], s39 offen offset:1024 sc1\n" \
    "buffer_load_dwordx4 v[160:163], v144, s[8:11], s39 offen offset:2048 sc1\n" \
    "buffer_load_dwordx4 v[164:167], v144, s[8:11], s39 offen offset:3072 sc1\n" \
    "s_add_u32 s39, s39, 4096\n" \
    "buffer_load_dwordx4 v[168:171], v144, s[8:11], s39 offen offset:0 sc1\n" \
    "buffer_load_dwordx4 v[172:175], v144, s[8:11], s39 offen offset:1024 sc1\n" \
    "s_mul_i32 s39, s32, 40960\n" \
    "s_add_u32 s39, s39, %[x1]+0\n" \
    "buffer_load_dwordx2 v[176:177], v147, s[8:11], s39 offen sc1\n" \
    "s_mov_b32 s37, 0\n" \
    ".Ldn_ra_%=:\n" \
    "s_waitcnt vmcnt(6)\n" \
    "v_mfma_f32_16x16x32_bf16 v[204:207], v[0:3], v[152:155], 0\n" \
    "v_mfma_f32_16x16x32_bf16 v[208:211], v[24:27], v[152:155], 0\n" \
    "v_mfma_f32_16x16x32_bf16 v[212:215], v[48:51], v[152:155], 0\n" \
    "v_mfma_f32_16x16x32_bf16 v[216:219], v[72:75], v[152:155], 0\n" \
    "v_max3_u32 v238, v152, v153, v154\n" \
    "v_mfma_f32_16x16x32_bf16 v[220:223], v[96:99], v[152:155], 0\n" \
    "v_mfma_f32_16x16x32_bf16 v[224:227], v[120:123], v[152:155], 0\n" \
    "s_waitcnt vmcnt(5)\n" \
    "v_mfma_f32_16x16x32_bf16 v[204:207], v[4:7], v[156:159], v[204:207]\n" \
    "v_mfma_f32_16x16x32_bf16 v[208:211], v[28:31], v[156:159], v[208:211]\n" \
    "v_max3_u32 v238, v238, v155, v156\n" \
    "v_mfma_f32_16x16x32_bf16 v[212:215], v[52:55], v[156:159], v[212:215]\n" \
    "v_mfma_f32_16x16x32_bf16 v[216:219], v[76:79], v[156:159], v[216:219]\n" \
    "v_max3_u32 v238, v238, v157, v158\n" \
    "v_mfma_f32_16x16x32_bf16 v[220:223], v[100:103], v[156:159], v[220:223]\n" \
    "v_mfma_f32_16x16x32_bf16 v[224:227], v[124:127], v[156:159], v[224:227]\n" \
    "s_waitcnt vmcnt(4)\n" \
    "v_mfma_f32_16x16x32_bf16 v[204:207], v[8:11], v[160:163], v[204:207]\n" \
    "v_mfma_f32_16x16x32_bf16 v[208:211], v[32:35], v[160:163], v[208:211]\n" \
    "v_max3_u32 v238, v238, v159, v160\n" \
    "v_mfma_f32_16x16x32_bf16 v[212:215], v[56:59], v[160:163], v[212:215]\n" \
    "v_mfma_f32_16x16x32_bf16 v[216:219], v[80:83], v[160:163], v[216:219]\n" \
    "v_max3_u32 v238, v238, v161, v162\n" \
    "v_mfma_f32_16x16x32_bf16 v[220:223], v[104:107], v[160:163], v[220:223]\n" \
    "v_mfma_f32_16x16x32_bf16 v[224:227], v[128:131], v[160:163], v[224:227]\n" \
    "s_waitcnt vmcnt(3)\n" \
    "v_mfma_f32_16x16x32_bf16 v[204:207], v[12:15], v[164:167], v[204:207]\n" \
    "v_mfma_f32_16x16x32_bf16 v[208:211], v[36:39], v[164:167], v[208:211]\n" \
    "v_max3_u32 v238, v238, v163, v164\n" \
    "v_mfma_f32_16x16x32_bf16 v[212:215], v[60:63], v[164:167], v[212:215]\n" \
    "v_mfma_f32_16x16x32_bf16 v[216:219], v[84:87], v[164:167], v[216:219]\n" \
    "v_max3_u32 v238, v238, v165, v166\n" \
    "v_mfma_f32_16x16x32_bf16 v[220:223], v[108:111], v[164:167], v[220:223]\n" \
    "v_mfma_f32_16x16x32_bf16 v[224:227], v[132:135], v[164:167], v[224:227]\n" \
    "s_waitcnt vmcnt(2)\n" \
    "v_mfma_f32_16x16x32_bf16 v[204:207], v[16:19], v[168:171], v[204:207]\n" \
    "s_waitcnt vmcnt(1)\n" \
    "v_mfma_f32_16x16x32_bf16 v[204:207], v[20:23], v[172:175], v[204:207]\n" \
    "v_mfma_f32_16x16x32_bf16 v[208:211], v[40:43], v[168:171], v[208:211]\n" \
    "v_max3_u32 v238, v238, v167, v168\n" \
    "v_mfma_f32_16x16x32_bf16 v[208:211], v[44:47], v[172:175], v[208:211]\n" \
    "v_max3_u32 v238, v238, v169, v170\n" \
    "v_mfma_f32_16x16x32_bf16 v[212:215], v[64:67], v[168:171], v[212:215]\n" \
    "v_max3_u32 v238, v238, v171, v172\n" \
    "v_mfma_f32_16x16x32_bf16 v[212:215], v[68:71], v[172:175], v[212:215]\n" \
    "v_max3_u32 v238, v238, v173, v174\n" \
    "v_mfma_f32_16x16x32_bf16 v[216:219], v[88:91], v[168:171], v[216:219]\n" \
    "s_waitcnt vmcnt(0)\n" \
    "v_max3_u32 v238, v238, v175, v176\n" \
    "v_mfma_f32_16x16x32_bf16 v[216:219], v[92:95], v[172:175], v[216:219]\n" \
    "v_max_u32 v238, v238, v177\n" \
    "ds_write_b128 v145, v[204:207] offset:0\n" \
    "v_mfma_f32_16x16x32_bf16 v[220:223], v[112:115], v[168:171], v[220:223]\n" \
    "v_mfma_f32_16x16x32_bf16 v[220:223], v[116:119], v[172:175], v[220:223]\n" \
    "ds_write_b128 v145, v[208:211] offset:1024\n" \
    "v_mfma_f32_16x16x32_bf16 v[224:227], v[136:139], v[168:171], v[224:227]\n" \
    "v_mfma_f32_16x16x32_bf16 v[224:227], v[140:143], v[172:175], v[224:227]\n" \
    "ds_write_b128 v145, v[212:215] offset:2048\n" \
    "v_cmp_eq_u32 vcc, -1, v238\n" \
    "ds_write_b128 v145, v[216:219] offset:3072\n" \
    "s_nop 1\n" \
    "ds_write_b128 v145, v[220:223] offset:4096\n" \
    "s_nop 1\n" \
    "ds_write_b128 v145, v[224:227] offset:5120\n" \
    "s_cbranch_vccnz .Ldn_ca_%=\n" \
    ".Ldn_xa_%=:\n" \
    "v_lshlrev_b32 v240, 16, v176\n" \
    "v_and_b32 v241, 0xffff0000, v176\n" \
    "v_lshlrev_b32 v242, 16, v177\n" \
    "v_and_b32 v243, 0xffff0000, v177\n" \
    "s_mul_i32 s39, s33, 98304\n" \
    "s_add_u32 s39, s39, s17\n" \
    "s_add_u32 s39, s39, 6291456\n" \
    "buffer_load_dwordx4 v[178:181], v144, s[8:11], s39 offen offset:0 sc1\n" \
    "buffer_load_dwordx4 v[182:185], v144, s[8:11], s39 offen offset:1024 sc1\n" \
    "buffer_load_dwordx4 v[186:189], v144, s[8:11], s39 offen offset:2048 sc1\n" \
    "buffer_load_dwordx4 v[190:193], v144, s[8:11], s39 offen offset:3072 sc1\n" \
    "s_add_u32 s39, s39, 4096\n" \
    "buffer_load_dwordx4 v[194:197], v144, s[8:11], s39 offen offset:0 sc1\n" \
    "buffer_load_dwordx4 v[198:201], v144, s[8:11], s39 offen offset:1024 sc1\n" \
    "s_mul_i32 s39, s33, 40960\n" \
    "s_add_u32 s39, s39, %[x1]+2621440\n" \
    "buffer_load_dwordx2 v[202:203], v147, s[8:11], s39 offen sc1\n" \
    "s_add_u32 s48, s32, 3\n" \
    "s_min_u32 s48, s48, 64\n" \
    "s_and_b32 s49, s48, 1\n" \
    "s_xor_b32 s49, s49, 1\n" \
    "s_lshl_b32 s49, s49, 9\n" \
    "s_add_u32 s49, s49, s18\n" \
    "buffer_load_dwordx2 v[150:151], off, s[8:11], s49 sc1\n" \
    "s_cmp_gt_u32 s16, 5\n" \
    "s_waitcnt lgkmcnt(0)\n" \
    "s_barrier\n" \
    "s_cbranch_scc1 .Ldn_sa_%=\n" \
    "ds_read_b128 v[204:207], v146 offset:0\n" \
    "ds_read_b128 v[208:211], v146 offset:6144\n" \
    "ds_read_b128 v[212:215], v146 offset:12288\n" \
    "ds_read_b128 v[216:219], v146 offset:18432\n" \
    "ds_read_b128 v[220:223], v146 offset:24576\n" \
    "ds_read_b128 v[224:227], v146 offset:30720\n" \
    "ds_read_b128 v[228:231], v146 offset:36864\n" \
    "ds_read_b128 v[232:235], v146 offset:43008\n" \
    "s_mul_i32 s34, s32, 40960\n" \
    "s_waitcnt lgkmcnt(6)\n" \
    "v_pk_add_f32 v[204:205], v[204:205], v[208:209]\n" \
    "v_pk_add_f32 v[206:207], v[206:207], v[210:211]\n" \
    "s_waitcnt lgkmcnt(4)\n" \
    "v_pk_add_f32 v[212:213], v[212:213], v[216:217]\n" \
    "v_pk_add_f32 v[214:215], v[214:215], v[218:219]\n" \
    "v_pk_add_f32 v[204:205], v[204:205], v[212:213]\n" \
    "v_pk_add_f32 v[206:207], v[206:207], v[214:215]\n" \
    "s_waitcnt lgkmcnt(2)\n" \
    "v_pk_add_f32 v[220:221], v[220:221], v[224:225]\n" \
    "v_pk_add_f32 v[222:223], v[222:223], v[226:227]\n" \
    "s_waitcnt lgkmcnt(0)\n" \
    "v_pk_add_f32 v[228:229], v[228:229], v[232:233]\n" \
    "v_pk_add_f32 v[230:231], v[230:231], v[234:235]\n" \
    "v_pk_add_f32 v[220:221], v[220:221], v[228:229]\n" \
    "v_pk_add_f32 v[222:223], v[222:223], v[230:231]\n" \
    "v_pk_add_f32 v[204:205], v[204:205], v[220:221]\n" \
    "v_pk_add_f32 v[206:207], v[206:207], v[222:223]\n" \
    "v_cvt_pk_bf16_f32 v208, v204, v205\n" \
    "v_cvt_pk_bf16_f32 v209, v206, v207\n" \
    "v_lshlrev_b32 v204, 16, v208\n" \
    "v_and_b32 v205, 0xffff0000, v208\n" \
    "v_lshlrev_b32 v206, 16, v209\n" \
    "v_and_b32 v207, 0xffff0000, v209\n" \
    "v_pk_add_f32 v[204:205], v[204:205], v[240:241]\n" \
    "v_pk_add_f32 v[206:207], v[206:207], v[242:243]\n" \
    "v_cvt_pk_bf16_f32 v248, v204, v205\n" \
    "v_cvt_pk_bf16_f32 v249, v206, v207\n" \
    "v_min_u32 v248, -2, v248\n" \
    "v_min_u32 v249, -2, v249\n" \
    "s_add_u32 s34, s34, %[x2]\n" \
    "buffer_store_dwordx2 v[248:249], v147, s[8:11], s34 offen sc1\n" \
    "s_add_u32 s34, s34, %[pdist]*40960\n" \
    "s_cmp_lt_u32 s32, 64-%[pdist]\n" \
    "s_cselect_b32 s34, s34, 0x80000000\n" \
    "buffer_store_dwordx2 v[236:237], v147, s[8:11], s34 offen sc1\n" \
    ".Ldn_sa_%=:\n" \
    "s_barrier\n" \
    "s_add_u32 s32, s32, 1\n" \
    "s_cmp_lt_u32 s32, %[eup]+1\n" \
    "s_cbranch_scc1 .Ldn_l_%=\n" \
    ".Ldn_m_%=:\n" \
    "s_waitcnt vmcnt(0)\n" \
    "s_memrealtime s[50:51]\n" \
    "v_readfirstlane_b32 s36, v151\n" \
    "v_readfirstlane_b32 s35, v150\n" \
    "s_waitcnt lgkmcnt(0)\n" \
    "s_sub_u32 s35, s50, s35\n" \
    "s_cmp_ge_i32 s35, %[dxd]\n" \
    "s_addc_u32 s36, s36, 0\n" \
    "s_cmp_eq_u32 s32, 63\n" \
    "s_cselect_b32 s36, 0, s36\n" \
    "s_cmp_gt_i32 s36, s48\n" \
    "s_cbranch_scc0 .Ldn_nd_%=\n" \
    "s_mul_i32 s39, s32, 98304\n" \
    "s_add_u32 s39, s39, s17\n" \
    "buffer_load_dwordx4 v[152:155], v144, s[8:11], s39 offen offset:0 sc1\n" \
    "buffer_load_dwordx4 v[156:159], v144, s[8:11], s39 offen offset:1024 sc1\n" \
    "buffer_load_dwordx4 v[160:163], v144, s[8:11], s39 offen offset:2048 sc1\n" \
    "buffer_load_dwordx4 v[164:167], v144, s[8:11], s39 offen offset:3072 sc1\n" \
    "s_add_u32 s39, s39, 4096\n" \
    "buffer_load_dwordx4 v[168:171], v144, s[8:11], s39 offen offset:0 sc1\n" \
    "buffer_load_dwordx4 v[172:175], v144, s[8:11], s39 offen offset:1024 sc1\n" \
    "s_mul_i32 s39, s32, 40960\n" \
    "s_add_u32 s39, s39, %[x1]+0\n" \
    "buffer_load_dwordx2 v[176:177], v147, s[8:11], s39 offen sc1\n" \
    "s_mov_b32 s37, 0\n" \
    ".Ldn_rb_%=:\n" \
    "v_mfma_f32_16x16x32_bf16 v[204:207], v[0:3], v[178:181], 0\n" \
    "v_mfma_f32_16x16x32_bf16 v[204:207], v[4:7], v[182:185], v[204:207]\n" \
    "v_mfma_f32_16x16x32_bf16 v[204:207], v[8:11], v[186:189], v[204:207]\n" \
    "v_mfma_f32_16x16x32_bf16 v[204:207], v[12:15], v[190:193], v[204:207]\n" \
    "v_mfma_f32_16x16x32_bf16 v[204:207], v[16:19], v[194:197], v[204:207]\n" \
    "v_mfma_f32_16x16x32_bf16 v[204:207], v[20:23], v[198:201], v[204:207]\n" \
    "v_mfma_f32_16x16x32_bf16 v[208:211], v[24:27], v[178:181], 0\n" \
    "v_max3_u32 v238, v178, v179, v180\n" \
    "v_mfma_f32_16x16x32_bf16 v[208:211], v[28:31], v[182:185], v[208:211]\n" \
    "v_max3_u32 v238, v238, v181, v182\n" \
    "v_mfma_f32_16x16x32_bf16 v[208:211], v[32:35], v[186:189], v[208:211]\n" \
    "v_max3_u32 v238, v238, v183, v184\n" \
    "v_mfma_f32_16x16x32_bf16 v[208:211], v[36:39], v[190:193], v[208:211]\n" \
    "v_max3_u32 v238, v238, v185, v186\n" \
    "v_mfma_f32_16x16x32_bf16 v[208:211], v[40:43], v[194:197], v[208:211]\n" \
    "v_max3_u32 v238, v238, v187, v188\n" \
    "v_mfma_f32_16x16x32_bf16 v[208:211], v[44:47], v[198:201], v[208:211]\n" \
    "v_max3_u32 v238, v238, v189, v190\n" \
    "v_mfma_f32_16x16x32_bf16 v[212:215], v[48:51], v[178:181], 0\n" \
    "v_max3_u32 v238, v238, v191, v192\n" \
    "v_mfma_f32_16x16x32_bf16 v[212:215], v[52:55], v[182:185], v[212:215]\n" \
    "v_max3_u32 v238, v238, v193, v194\n" \
    "v_mfma_f32_16x16x32_bf16 v[212:215], v[56:59], v[186:189], v[212:215]\n" \
    "v_max3_u32 v238, v238, v195, v196\n" \
    "v_mfma_f32_16x16x32_bf16 v[212:215], v[60:63], v[190:193], v[212:215]\n" \
    "v_max3_u32 v238, v238, v197, v198\n" \
    "v_mfma_f32_16x16x32_bf16 v[212:215], v[64:67], v[194:197], v[212:215]\n" \
    "v_max3_u32 v238, v238, v199, v200\n" \
    "v_mfma_f32_16x16x32_bf16 v[212:215], v[68:71], v[198:201], v[212:215]\n" \
    "v_max3_u32 v238, v238, v201, v202\n" \
    "ds_write_b128 v145, v[204:207] offset:49152\n" \
    "v_mfma_f32_16x16x32_bf16 v[216:219], v[72:75], v[178:181], 0\n" \
    "v_max_u32 v238, v238, v203\n" \
    "v_mfma_f32_16x16x32_bf16 v[216:219], v[76:79], v[182:185], v[216:219]\n" \
    "v_mfma_f32_16x16x32_bf16 v[216:219], v[80:83], v[186:189], v[216:219]\n" \
    "v_mfma_f32_16x16x32_bf16 v[216:219], v[84:87], v[190:193], v[216:219]\n" \
    "v_mfma_f32_16x16x32_bf16 v[216:219], v[88:91], v[194:197], v[216:219]\n" \
    "v_mfma_f32_16x16x32_bf16 v[216:219], v[92:95], v[198:201], v[216:219]\n" \
    "ds_write_b128 v145, v[208:211] offset:50176\n" \
    "v_mfma_f32_16x16x32_bf16 v[220:223], v[96:99], v[178:181], 0\n" \
    "v_mfma_f32_16x16x32_bf16 v[220:223], v[100:103], v[182:185], v[220:223]\n" \
    "v_mfma_f32_16x16x32_bf16 v[220:223], v[104:107], v[186:189], v[220:223]\n" \
    "v_mfma_f32_16x16x32_bf16 v[220:223], v[108:111], v[190:193], v[220:223]\n" \
    "v_mfma_f32_16x16x32_bf16 v[220:223], v[112:115], v[194:197], v[220:223]\n" \
    "v_mfma_f32_16x16x32_bf16 v[220:223], v[116:119], v[198:201], v[220:223]\n" \
    "ds_write_b128 v145, v[212:215] offset:51200\n" \
    "v_mfma_f32_16x16x32_bf16 v[224:227], v[120:123], v[178:181], 0\n" \
    "v_mfma_f32_16x16x32_bf16 v[224:227], v[124:127], v[182:185], v[224:227]\n" \
    "v_mfma_f32_16x16x32_bf16 v[224:227], v[128:131], v[186:189], v[224:227]\n" \
    "v_mfma_f32_16x16x32_bf16 v[224:227], v[132:135], v[190:193], v[224:227]\n" \
    "v_mfma_f32_16x16x32_bf16 v[224:227], v[136:139], v[194:197], v[224:227]\n" \
    "v_mfma_f32_16x16x32_bf16 v[224:227], v[140:143], v[198:201], v[224:227]\n" \
    "ds_write_b128 v145, v[216:219] offset:52224\n" \
    "v_cmp_eq_u32 vcc, -1, v238\n" \
    "ds_write_b128 v145, v[220:223] offset:53248\n" \
    "s_nop 4\n" \
    "ds_write_b128 v145, v[224:227] offset:54272\n" \
    "s_cbranch_vccnz .Ldn_cb_%=\n" \
    ".Ldn_xb_%=:\n" \
    "s_branch .Ldn_jn_%=\n" \
    ".Ldn_nd_%=:\n" \
    "s_mov_b32 s37, 0\n" \
    ".Ldn_rc_%=:\n" \
    "v_mfma_f32_16x16x32_bf16 v[204:207], v[0:3], v[178:181], 0\n" \
    "v_mfma_f32_16x16x32_bf16 v[204:207], v[4:7], v[182:185], v[204:207]\n" \
    "v_mfma_f32_16x16x32_bf16 v[204:207], v[8:11], v[186:189], v[204:207]\n" \
    "v_mfma_f32_16x16x32_bf16 v[204:207], v[12:15], v[190:193], v[204:207]\n" \
    "v_mfma_f32_16x16x32_bf16 v[204:207], v[16:19], v[194:197], v[204:207]\n" \
    "v_mfma_f32_16x16x32_bf16 v[204:207], v[20:23], v[198:201], v[204:207]\n" \
    "v_mfma_f32_16x16x32_bf16 v[208:211], v[24:27], v[178:181], 0\n" \
    "v_max3_u32 v238, v178, v179, v180\n" \
    "v_mfma_f32_16x16x32_bf16 v[208:211], v[28:31], v[182:185], v[208:211]\n" \
    "v_max3_u32 v238, v238, v181, v182\n" \
    "v_mfma_f32_16x16x32_bf16 v[208:211], v[32:35], v[186:189], v[208:211]\n" \
    "v_max3_u32 v238, v238, v183, v184\n" \
    "v_mfma_f32_16x16x32_bf16 v[208:211], v[36:39], v[190:193], v[208:211]\n" \
    "v_max3_u32 v238, v238, v185, v186\n" \
    "v_mfma_f32_16x16x32_bf16 v[208:211], v[40:43], v[194:197], v[208:211]\n" \
    "v_max3_u32 v238, v238, v187, v188\n" \
    "v_mfma_f32_16x16x32_bf16 v[208:211], v[44:47], v[198:201], v[208:211]\n" \
    "v_max3_u32 v238, v238, v189, v190\n" \
    "v_mfma_f32_16x16x32_bf16 v[212:215], v[48:51], v[178:181], 0\n" \
    "v_max3_u32 v238, v238, v191, v192\n" \
    "v_mfma_f32_16x16x32_bf16 v[212:215], v[52:55], v[182:185], v[212:215]\n" \
    "v_max3_u32 v238, v238, v193, v194\n" \
    "v_mfma_f32_16x16x32_bf16 v[212:215], v[56:59], v[186:189], v[212:215]\n" \
    "v_max3_u32 v238, v238, v195, v196\n" \
    "v_mfma_f32_16x16x32_bf16 v[212:215], v[60:63], v[190:193], v[212:215]\n" \
    "v_max3_u32 v238, v238, v197, v198\n" \
    "v_mfma_f32_16x16x32_bf16 v[212:215], v[64:67], v[194:197], v[212:215]\n" \
    "v_max3_u32 v238, v238, v199, v200\n" \
    "v_mfma_f32_16x16x32_bf16 v[212:215], v[68:71], v[198:201], v[212:215]\n" \
    "v_max3_u32 v238, v238, v201, v202\n" \
    "ds_write_b128 v145, v[204:207] offset:49152\n" \
    "v_mfma_f32_16x16x32_bf16 v[216:219], v[72:75], v[178:181], 0\n" \
    "v_max_u32 v238, v238, v203\n" \
    "v_mfma_f32_16x16x32_bf16 v[216:219], v[76:79], v[182:185], v[216:219]\n" \
    "v_mfma_f32_16x16x32_bf16 v[216:219], v[80:83], v[186:189], v[216:219]\n" \
    "v_mfma_f32_16x16x32_bf16 v[216:219], v[84:87], v[190:193], v[216:219]\n" \
    "v_mfma_f32_16x16x32_bf16 v[216:219], v[88:91], v[194:197], v[216:219]\n" \
    "v_mfma_f32_16x16x32_bf16 v[216:219], v[92:95], v[198:201], v[216:219]\n" \
    "ds_write_b128 v145, v[208:211] offset:50176\n" \
    "v_mfma_f32_16x16x32_bf16 v[220:223], v[96:99], v[178:181], 0\n" \
    "v_mfma_f32_16x16x32_bf16 v[220:223], v[100:103], v[182:185], v[220:223]\n" \
    "v_mfma_f32_16x16x32_bf16 v[220:223], v[104:107], v[186:189], v[220:223]\n" \
    "v_mfma_f32_16x16x32_bf16 v[220:223], v[108:111], v[190:193], v[220:223]\n" \
    "v_mfma_f32_16x16x32_bf16 v[220:223], v[112:115], v[194:197], v[220:223]\n" \
    "v_mfma_f32_16x16x32_bf16 v[220:223], v[116:119], v[198:201], v[220:223]\n" \
    "ds_write_b128 v145, v[212:215] offset:51200\n" \
    "v_mfma_f32_16x16x32_bf16 v[224:227], v[120:123], v[178:181], 0\n" \
    "v_mfma_f32_16x16x32_bf16 v[224:227], v[124:127], v[182:185], v[224:227]\n" \
    "v_mfma_f32_16x16x32_bf16 v[224:227], v[128:131], v[186:189], v[224:227]\n" \
    "v_mfma_f32_16x16x32_bf16 v[224:227], v[132:135], v[190:193], v[224:227]\n" \
    "v_mfma_f32_16x16x32_bf16 v[224:227], v[136:139], v[194:197], v[224:227]\n" \
    "v_mfma_f32_16x16x32_bf16 v[224:227], v[140:143], v[198:201], v[224:227]\n" \
    "ds_write_b128 v145, v[216:219] offset:52224\n" \
    "v_cmp_eq_u32 vcc, -1, v238\n" \
    "ds_write_b128 v145, v[220:223] offset:53248\n" \
    "s_nop 4\n" \
    "ds_write_b128 v145, v[224:227] offset:54272\n" \
    "s_cbranch_vccnz .Ldn_cc_%=\n" \
    ".Ldn_xc_%=:\n" \
    "s_mov_b32 s37, 0\n" \
    "s_waitcnt vmcnt(0)\n" \
    ".Ldn_ep_%=:\n" \
    "v_readfirstlane_b32 s36, v151\n" \
    "s_nop 0\n" \
    "s_cmp_gt_i32 s36, s48\n" \
    "s_cbranch_scc1 .Ldn_ex_%=\n" \
    "s_cmp_eq_u32 s36, s48\n" \
    "s_cbranch_scc1 .Ldn_ew_%=\n" \
    "s_sleep 3\n" \
    "buffer_load_dwordx2 v[150:151], off, s[8:11], s49 sc1\n" \
    "s_waitcnt vmcnt(0)\n" \
    "s_add_u32 s37, s37, 1\n" \
    "s_cmp_lt_u32 s37, s38\n" \
    "s_cbranch_scc1 .Ldn_ep_%=\n" \
    "s_mov_b32 s38, 0\n" \
    "s_branch .Ldn_ex_%=\n" \
    ".Ldn_ew_%=:\n" \
    "v_readfirstlane_b32 s36, v150\n" \
    "s_cmp_eq_u32 s32, 63\n" \
    "s_cselect_b32 s34, %[dlast], 0\n" \
    "s_add_u32 s36, s36, s34\n" \
    "s_add_u32 s36, s36, %[dxd]\n" \
    "s_branch .Ldn_etb_%=\n" \
    ".Ldn_eta_%=:\n" \
    "s_sleep 1\n" \
    ".Ldn_etb_%=:\n" \
    "s_memrealtime vcc\n" \
    "s_waitcnt lgkmcnt(0)\n" \
    "s_sub_i32 vcc_lo, s36, vcc_lo\n" \
    "s_cmp_gt_i32 vcc_lo, 8\n" \
    "s_cbranch_scc1 .Ldn_eta_%=\n" \
    "s_cmp_gt_i32 vcc_lo, 0\n" \
    "s_cbranch_scc1 .Ldn_etb_%=\n" \
    ".Ldn_ex_%=:\n" \
    "s_mul_i32 s39, s32, 98304\n" \
    "s_add_u32 s39, s39, s17\n" \
    "buffer_load_dwordx4 v[152:155], v144, s[8:11], s39 offen offset:0 sc1\n" \
    "buffer_load_dwordx4 v[156:159], v144, s[8:11], s39 offen offset:1024 sc1\n" \
    "buffer_load_dwordx4 v[160:163], v144, s[8:11], s39 offen offset:2048 sc1\n" \
    "buffer_load_dwordx4 v[164:167], v144, s[8:11], s39 offen offset:3072 sc1\n" \
    "s_add_u32 s39, s39, 4096\n" \
    "buffer_load_dwordx4 v[168:171], v144, s[8:11], s39 offen offset:0 sc1\n" \
    "buffer_load_dwordx4 v[172:175], v144, s[8:11], s39 offen offset:1024 sc1\n" \
    "s_mul_i32 s39, s32, 40960\n" \
    "s_add_u32 s39, s39, %[x1]+0\n" \
    "buffer_load_dwordx2 v[176:177], v147, s[8:11], s39 offen sc1\n" \
    ".Ldn_jn_%=:\n" \
    "v_lshlrev_b32 v244, 16, v202\n" \
    "v_and_b32 v245, 0xffff0000, v202\n" \
    "v_lshlrev_b32 v246, 16, v203\n" \
    "v_and_b32 v247, 0xffff0000, v203\n" \
    "s_mov_b32 s37, 0\n" \
    ".Ldn_rd_%=:\n" \
    "s_waitcnt vmcnt(6)\n" \
    "v_mfma_f32_16x16x32_bf16 v[204:207], v[0:3], v[152:155], 0\n" \
    "v_mfma_f32_16x16x32_bf16 v[208:211], v[24:27], v[152:155], 0\n" \
    "v_mfma_f32_16x16x32_bf16 v[212:215], v[48:51], v[152:155], 0\n" \
    "v_mfma_f32_16x16x32_bf16 v[216:219], v[72:75], v[152:155], 0\n" \
    "v_max3_u32 v238, v152, v153, v154\n" \
    "v_mfma_f32_16x16x32_bf16 v[220:223], v[96:99], v[152:155], 0\n" \
    "v_mfma_f32_16x16x32_bf16 v[224:227], v[120:123], v[152:155], 0\n" \
    "s_waitcnt vmcnt(5)\n" \
    "v_mfma_f32_16x16x32_bf16 v[204:207], v[4:7], v[156:159], v[204:207]\n" \
    "v_mfma_f32_16x16x32_bf16 v[208:211], v[28:31], v[156:159], v[208:211]\n" \
    "v_max3_u32 v238, v238, v155, v156\n" \
    "v_mfma_f32_16x16x32_bf16 v[212:215], v[52:55], v[156:159], v[212:215]\n" \
    "v_mfma_f32_16x16x32_bf16 v[216:219], v[76:79], v[156:159], v[216:219]\n" \
    "v_max3_u32 v238, v238, v157, v158\n" \
    "v_mfma_f32_16x16x32_bf16 v[220:223], v[100:103], v[156:159], v[220:223]\n" \
    "v_mfma_f32_16x16x32_bf16 v[224:227], v[124:127], v[156:159], v[224:227]\n" \
    "s_waitcnt vmcnt(4)\n" \
    "v_mfma_f32_16x16x32_bf16 v[204:207], v[8:11], v[160:163], v[204:207]\n" \
    "v_mfma_f32_16x16x32_bf16 v[208:211], v[32:35], v[160:163], v[208:211]\n" \
    "v_max3_u32 v238, v238, v159, v160\n" \
    "v_mfma_f32_16x16x32_bf16 v[212:215], v[56:59], v[160:163], v[212:215]\n" \
    "v_mfma_f32_16x16x32_bf16 v[216:219], v[80:83], v[160:163], v[216:219]\n" \
    "v_max3_u32 v238, v238, v161, v162\n" \
    "v_mfma_f32_16x16x32_bf16 v[220:223], v[104:107], v[160:163], v[220:223]\n" \
    "v_mfma_f32_16x16x32_bf16 v[224:227], v[128:131], v[160:163], v[224:227]\n" \
    "s_waitcnt vmcnt(3)\n" \
    "v_mfma_f32_16x16x32_bf16 v[204:207], v[12:15], v[164:167], v[204:207]\n" \
    "v_mfma_f32_16x16x32_bf16 v[208:211], v[36:39], v[164:167], v[208:211]\n" \
    "v_max3_u32 v238, v238, v163, v164\n" \
    "v_mfma_f32_16x16x32_bf16 v[212:215], v[60:63], v[164:167], v[212:215]\n" \
    "v_mfma_f32_16x16x32_bf16 v[216:219], v[84:87], v[164:167], v[216:219]\n" \
    "v_max3_u32 v238, v238, v165, v166\n" \
    "v_mfma_f32_16x16x32_bf16 v[220:223], v[108:111], v[164:167], v[220:223]\n" \
    "v_mfma_f32_16x16x32_bf16 v[224:227], v[132:135], v[164:167], v[224:227]\n" \
    "s_waitcnt vmcnt(2)\n" \
    "v_mfma_f32_16x16x32_bf16 v[204:207], v[16:19], v[168:171], v[204:207]\n" \
    "s_waitcnt vmcnt(1)\n" \
    "v_mfma_f32_16x16x32_bf16 v[204:207], v[20:23], v[172:175], v[204:207]\n" \
    "v_mfma_f32_16x16x32_bf16 v[208:211], v[40:43], v[168:171], v[208:211]\n" \
    "v_max3_u32 v238, v238, v167, v168\n" \
    "v_mfma_f32_16x16x32_bf16 v[208:211], v[44:47], v[172:175], v[208:211]\n" \
    "v_max3_u32 v238, v238, v169, v170\n" \
    "v_mfma_f32_16x16x32_bf16 v[212:215], v[64:67], v[168:171], v[212:215]\n" \
    "v_max3_u32 v238, v238, v171, v172\n" \
    "v_mfma_f32_16x16x32_bf16 v[212:215], v[68:71], v[172:175], v[212:215]\n" \
    "v_max3_u32 v238, v238, v173, v174\n" \
    "v_mfma_f32_16x16x32_bf16 v[216:219], v[88:91], v[168:171], v[216:219]\n" \
    "s_waitcnt vmcnt(0)\n" \
    "v_max3_u32 v238, v238, v175, v176\n" \
    "v_mfma_f32_16x16x32_bf16 v[216:219], v[92:95], v[172:175], v[216:219]\n" \
    "v_max_u32 v238, v238, v177\n" \
    "ds_write_b128 v145, v[204:207] offset:0\n" \
    "v_mfma_f32_16x16x32_bf16 v[220:223], v[112:115], v[168:171], v[220:223]\n" \
    "v_mfma_f32_16x16x32_bf16 v[220:223], v[116:119], v[172:175], v[220:223]\n" \
    "ds_write_b128 v145, v[208:211] offset:1024\n" \
    "v_mfma_f32_16x16x32_bf16 v[224:227], v[136:139], v[168:171], v[224:227]\n" \
    "v_mfma_f32_16x16x32_bf16 v[224:227], v[140:143], v[172:175], v[224:227]\n" \
    "ds_write_b128 v145, v[212:215] offset:2048\n" \
    "v_cmp_eq_u32 vcc, -1, v238\n" \
    "ds_write_b128 v145, v[216:219] offset:3072\n" \
    "s_nop 1\n" \
    "ds_write_b128 v145, v[220:223] offset:4096\n" \
    "s_nop 1\n" \
    "ds_write_b128 v145, v[224:227] offset:5120\n" \
    "s_cbranch_vccnz .Ldn_cd_%=\n" \
    ".Ldn_xd_%=:\n" \
    "v_lshlrev_b32 v240, 16, v176\n" \
    "v_and_b32 v241, 0xffff0000, v176\n" \
    "v_lshlrev_b32 v242, 16, v177\n" \
    "v_and_b32 v243, 0xffff0000, v177\n" \
    "s_add_u32 s36, s33, 1\n" \
    "s_mul_i32 s39, s36, 98304\n" \
    "s_add_u32 s39, s39, s17\n" \
    "s_add_u32 s39, s39, 6291456\n" \
    "buffer_load_dwordx4 v[178:181], v144, s[8:11], s39 offen offset:0 sc1\n" \
    "buffer_load_dwordx4 v[182:185], v144, s[8:11], s39 offen offset:1024 sc1\n" \
    "buffer_load_dwordx4 v[186:189], v144, s[8:11], s39 offen offset:2048 sc1\n" \
    "buffer_load_dwordx4 v[190:193], v144, s[8:11], s39 offen offset:3072 sc1\n" \
    "s_add_u32 s39, s39, 4096\n" \
    "buffer_load_dwordx4 v[194:197], v144, s[8:11], s39 offen offset:0 sc1\n" \
    "buffer_load_dwordx4 v[198:201], v144, s[8:11], s39 offen offset:1024 sc1\n" \
    "s_mul_i32 s39, s36, 40960\n" \
    "s_add_u32 s39, s39, %[x1]+2621440\n" \
    "buffer_load_dwordx2 v[202:203], v147, s[8:11], s39 offen sc1\n" \
    "s_add_u32 s48, s32, 3\n" \
    "s_min_u32 s48, s48, 64\n" \
    "s_and_b32 s49, s48, 1\n" \
    "s_xor_b32 s49, s49, 1\n" \
    "s_lshl_b32 s49, s49, 9\n" \
    "s_add_u32 s49, s49, s18\n" \
    "buffer_load_dwordx2 v[150:151], off, s[8:11], s49 sc1\n" \
    "s_cmp_gt_u32 s16, 5\n" \
    "s_waitcnt lgkmcnt(0)\n" \
    "s_barrier\n" \
    "s_cbranch_scc1 .Ldn_sb_%=\n" \
    "ds_read_b128 v[204:207], v146 offset:0\n" \
    "ds_read_b128 v[208:211], v146 offset:6144\n" \
    "ds_read_b128 v[212:215], v146 offset:12288\n" \
    "ds_read_b128 v[216:219], v146 offset:18432\n" \
    "ds_read_b128 v[220:223], v146 offset:24576\n" \
    "ds_read_b128 v[224:227], v146 offset:30720\n" \
    "ds_read_b128 v[228:231], v146 offset:36864\n" \
    "ds_read_b128 v[232:235], v146 offset:43008\n" \
    "s_mul_i32 s34, s32, 40960\n" \
    "s_waitcnt lgkmcnt(6)\n" \
    "v_pk_add_f32 v[204:205], v[204:205], v[208:209]\n" \
    "v_pk_add_f32 v[206:207], v[206:207], v[210:211]\n" \
    "s_waitcnt lgkmcnt(4)\n" \
    "v_pk_add_f32 v[212:213], v[212:213], v[216:217]\n" \
    "v_pk_add_f32 v[214:215], v[214:215], v[218:219]\n" \
    "v_pk_add_f32 v[204:205], v[204:205], v[212:213]\n" \
    "v_pk_add_f32 v[206:207], v[206:207], v[214:215]\n" \
    "s_waitcnt lgkmcnt(2)\n" \
    "v_pk_add_f32 v[220:221], v[220:221], v[224:225]\n" \
    "v_pk_add_f32 v[222:223], v[222:223], v[226:227]\n" \
    "s_waitcnt lgkmcnt(0)\n" \
    "v_pk_add_f32 v[228:229], v[228:229], v[232:233]\n" \
    "v_pk_add_f32 v[230:231], v[230:231], v[234:235]\n" \
    "v_pk_add_f32 v[220:221], v[220:221], v[228:229]\n" \
    "v_pk_add_f32 v[222:223], v[222:223], v[230:231]\n" \
    "v_pk_add_f32 v[204:205], v[204:205], v[220:221]\n" \
    "v_pk_add_f32 v[206:207], v[206:207], v[222:223]\n" \
    "v_cvt_pk_bf16_f32 v208, v204, v205\n" \
    "v_cvt_pk_bf16_f32 v209, v206, v207\n" \
    "v_lshlrev_b32 v204, 16, v208\n" \
    "v_and_b32 v205, 0xffff0000, v208\n" \
    "v_lshlrev_b32 v206, 16, v209\n" \
    "v_and_b32 v207, 0xffff0000, v209\n" \
    "v_pk_add_f32 v[204:205], v[204:205], v[240:241]\n" \
    "v_pk_add_f32 v[206:207], v[206:207], v[242:243]\n" \
    "v_cvt_pk_bf16_f32 v248, v204, v205\n" \
    "v_cvt_pk_bf16_f32 v249, v206, v207\n" \
    "v_min_u32 v248, -2, v248\n" \
    "v_min_u32 v249, -2, v249\n" \
    "s_add_u32 s34, s34, %[x2]\n" \
    "buffer_store_dwordx2 v[248:249], v147, s[8:11], s34 offen sc1\n" \
    "s_add_u32 s34, s34, %[pdist]*40960\n" \
    "s_cmp_lt_u32 s32, 64-%[pdist]\n" \
    "s_cselect_b32 s34, s34, 0x80000000\n" \
    "buffer_store_dwordx2 v[236:237], v147, s[8:11], s34 offen sc1\n" \
    "v_add_u32 v239, 49152, v146\n" \
    "ds_read_b128 v[204:207], v239 offset:0\n" \
    "ds_read_b128 v[208:211], v239 offset:6144\n" \
    "ds_read_b128 v[212:215], v239 offset:12288\n" \
    "ds_read_b128 v[216:219], v239 offset:18432\n" \
    "ds_read_b128 v[220:223], v239 offset:24576\n" \
    "ds_read_b128 v[224:227], v239 offset:30720\n" \
    "ds_read_b128 v[228:231], v239 offset:36864\n" \
    "ds_read_b128 v[232:235], v239 offset:43008\n" \
    "s_mul_i32 s34, s33, 40960\n" \
    "s_waitcnt lgkmcnt(6)\n" \
    "v_pk_add_f32 v[204:205], v[204:205], v[208:209]\n" \
    "v_pk_add_f32 v[206:207], v[206:207], v[210:211]\n" \
    "s_waitcnt lgkmcnt(4)\n" \
    "v_pk_add_f32 v[212:213], v[212:213], v[216:217]\n" \
    "v_pk_add_f32 v[214:215], v[214:215], v[218:219]\n" \
    "v_pk_add_f32 v[204:205], v[204:205], v[212:213]\n" \
    "v_pk_add_f32 v[206:207], v[206:207], v[214:215]\n" \
    "s_waitcnt lgkmcnt(2)\n" \
    "v_pk_add_f32 v[220:221], v[220:221], v[224:225]\n" \
    "v_pk_add_f32 v[222:223], v[222:223], v[226:227]\n" \
    "s_waitcnt lgkmcnt(0)\n" \
    "v_pk_add_f32 v[228:229], v[228:229], v[232:233]\n" \
    "v_pk_add_f32 v[230:231], v[230:231], v[234:235]\n" \
    "v_pk_add_f32 v[220:221], v[220:221], v[228:229]\n" \
    "v_pk_add_f32 v[222:223], v[222:223], v[230:231]\n" \
    "v_pk_add_f32 v[204:205], v[204:205], v[220:221]\n" \
    "v_pk_add_f32 v[206:207], v[206:207], v[222:223]\n" \
    "v_cvt_pk_bf16_f32 v208, v204, v205\n" \
    "v_cvt_pk_bf16_f32 v209, v206, v207\n" \
    "v_lshlrev_b32 v204, 16, v208\n" \
    "v_and_b32 v205, 0xffff0000, v208\n" \
    "v_lshlrev_b32 v206, 16, v209\n" \
    "v_and_b32 v207, 0xffff0000, v209\n" \
    "v_pk_add_f32 v[204:205], v[204:205], v[244:245]\n" \
    "v_pk_add_f32 v[206:207], v[206:207], v[246:247]\n" \
    "v_cvt_pk_bf16_f32 v250, v204, v205\n" \
    "v_cvt_pk_bf16_f32 v251, v206, v207\n" \
    "buffer_store_dwordx2 v[250:251], v148, s[12:15], s34 offen\n" \
    ".Ldn_sb_%=:\n" \
    "s_barrier\n" \
    "s_add_u32 s33, s33, 1\n" \
    "s_add_u32 s32, s32, 1\n" \
    "s_cmp_lt_u32 s32, 64\n" \
    "s_cbranch_scc1 .Ldn_m_%=\n" \
    "s_add_u32 s18, s18, 1024\n" \
    "s_waitcnt vmcnt(0)\n" \
    "s_add_u32 s48, s33, 2\n" \
    "s_min_u32 s48, s48, 64\n" \
    "s_and_b32 s49, s48, 1\n" \
    "s_xor_b32 s49, s49, 1\n" \
    "s_lshl_b32 s49, s49, 9\n" \
    "s_add_u32 s49, s49, s18\n" \
    "buffer_load_dwordx2 v[150:151], off, s[8:11], s49 sc1\n" \
    ".Ldn_n_%=:\n" \
    "s_mov_b32 s37, 0\n" \
    "s_waitcnt vmcnt(0)\n" \
    ".Ldn_fp_%=:\n" \
    "v_readfirstlane_b32 s36, v151\n" \
    "s_nop 0\n" \
    "s_cmp_gt_i32 s36, s48\n" \
    "s_cbranch_scc1 .Ldn_fx_%=\n" \
    "s_cmp_eq_u32 s36, s48\n" \
    "s_cbranch_scc1 .Ldn_fw_%=\n" \
    "s_sleep 3\n" \
    "buffer_load_dwordx2 v[150:151], off, s[8:11], s49 sc1\n" \
    "s_waitcnt vmcnt(0)\n" \
    "s_add_u32 s37, s37, 1\n" \
    "s_cmp_lt_u32 s37, s38\n" \
    "s_cbranch_scc1 .Ldn_fp_%=\n" \
    "s_mov_b32 s38, 0\n" \
    "s_branch .Ldn_fx_%=\n" \
    ".Ldn_fw_%=:\n" \
    "v_readfirstlane_b32 s36, v150\n" \
    "s_cmp_eq_u32 s33, 63\n" \
    "s_cselect_b32 s34, %[dlast], 0\n" \
    "s_add_u32 s36, s36, s34\n" \
    "s_add_u32 s36, s36, %[dxd]\n" \
    "s_branch .Ldn_ftb_%=\n" \
    ".Ldn_fta_%=:\n" \
    "s_sleep 1\n" \
    ".Ldn_ftb_%=:\n" \
    "s_memrealtime vcc\n" \
    "s_waitcnt lgkmcnt(0)\n" \
    "s_sub_i32 vcc_lo, s36, vcc_lo\n" \
    "s_cmp_gt_i32 vcc_lo, 8\n" \
    "s_cbranch_scc1 .Ldn_fta_%=\n" \
    "s_cmp_gt_i32 vcc_lo, 0\n" \
    "s_cbranch_scc1 .Ldn_ftb_%=\n" \
    ".Ldn_fx_%=:\n" \
    "s_mul_i32 s39, s33, 98304\n" \
    "s_add_u32 s39, s39, s17\n" \
    "s_add_u32 s39, s39, 6291456\n" \
    "buffer_load_dwordx4 v[178:181], v144, s[8:11], s39 offen offset:0 sc1\n" \
    "buffer_load_dwordx4 v[182:185], v144, s[8:11], s39 offen offset:1024 sc1\n" \
    "buffer_load_dwordx4 v[186:189], v144, s[8:11], s39 offen offset:2048 sc1\n" \
    "buffer_load_dwordx4 v[190:193], v144, s[8:11], s39 offen offset:3072 sc1\n" \
    "s_add_u32 s39, s39, 4096\n" \
    "buffer_load_dwordx4 v[194:197], v144, s[8:11], s39 offen offset:0 sc1\n" \
    "buffer_load_dwordx4 v[198:201], v144, s[8:11], s39 offen offset:1024 sc1\n" \
    "s_mul_i32 s39, s33, 40960\n" \
    "s_add_u32 s39, s39, %[x1]+2621440\n" \
    "buffer_load_dwordx2 v[202:203], v147, s[8:11], s39 offen sc1\n" \
    "s_mov_b32 s37, 0\n" \
    ".Ldn_rf_%=:\n" \
    "s_waitcnt vmcnt(6)\n" \
    "v_mfma_f32_16x16x32_bf16 v[204:207], v[0:3], v[178:181], 0\n" \
    "v_mfma_f32_16x16x32_bf16 v[208:211], v[24:27], v[178:181], 0\n" \
    "v_mfma_f32_16x16x32_bf16 v[212:215], v[48:51], v[178:181], 0\n" \
    "v_mfma_f32_16x16x32_bf16 v[216:219], v[72:75], v[178:181], 0\n" \
    "v_max3_u32 v238, v178, v179, v180\n" \
    "v_mfma_f32_16x16x32_bf16 v[220:223], v[96:99], v[178:181], 0\n" \
    "v_mfma_f32_16x16x32_bf16 v[224:227], v[120:123], v[178:181], 0\n" \
    "s_waitcnt vmcnt(5)\n" \
    "v_mfma_f32_16x16x32_bf16 v[204:207], v[4:7], v[182:185], v[204:207]\n" \
    "v_mfma_f32_16x16x32_bf16 v[208:211], v[28:31], v[182:185], v[208:211]\n" \
    "v_max3_u32 v238, v238, v181, v182\n" \
    "v_mfma_f32_16x16x32_bf16 v[212:215], v[52:55], v[182:185], v[212:215]\n" \
    "v_mfma_f32_16x16x32_bf16 v[216:219], v[76:79], v[182:185], v[216:219]\n" \
    "v_max3_u32 v238, v238, v183, v184\n" \
    "v_mfma_f32_16x16x32_bf16 v[220:223], v[100:103], v[182:185], v[220:223]\n" \
    "v_mfma_f32_16x16x32_bf16 v[224:227], v[124:127], v[182:185], v[224:227]\n" \
    "s_waitcnt vmcnt(4)\n" \
    "v_mfma_f32_16x16x32_bf16 v[204:207], v[8:11], v[186:189], v[204:207]\n" \
    "v_mfma_f32_16x16x32_bf16 v[208:211], v[32:35], v[186:189], v[208:211]\n" \
    "v_max3_u32 v238, v238, v185, v186\n" \
    "v_mfma_f32_16x16x32_bf16 v[212:215], v[56:59], v[186:189], v[212:215]\n" \
    "v_mfma_f32_16x16x32_bf16 v[216:219], v[80:83], v[186:189], v[216:219]\n" \
    "v_max3_u32 v238, v238, v187, v188\n" \
    "v_mfma_f32_16x16x32_bf16 v[220:223], v[104:107], v[186:189], v[220:223]\n" \
    "v_mfma_f32_16x16x32_bf16 v[224:227], v[128:131], v[186:189], v[224:227]\n" \
    "s_waitcnt vmcnt(3)\n" \
    "v_mfma_f32_16x16x32_bf16 v[204:207], v[12:15], v[190:193], v[204:207]\n" \
    "v_mfma_f32_16x16x32_bf16 v[208:211], v[36:39], v[190:193], v[208:211]\n" \
    "v_max3_u32 v238, v238, v189, v190\n" \
    "v_mfma_f32_16x16x32_bf16 v[212:215], v[60:63], v[190:193], v[212:215]\n" \
    "v_mfma_f32_16x16x32_bf16 v[216:219], v[84:87], v[190:193], v[216:219]\n" \
    "v_max3_u32 v238, v238, v191, v192\n" \
    "v_mfma_f32_16x16x32_bf16 v[220:223], v[108:111], v[190:193], v[220:223]\n" \
    "v_mfma_f32_16x16x32_bf16 v[224:227], v[132:135], v[190:193], v[224:227]\n" \
    "s_waitcnt vmcnt(2)\n" \
    "v_mfma_f32_16x16x32_bf16 v[204:207], v[16:19], v[194:197], v[204:207]\n" \
    "s_waitcnt vmcnt(1)\n" \
    "v_mfma_f32_16x16x32_bf16 v[204:207], v[20:23], v[198:201], v[204:207]\n" \
    "v_mfma_f32_16x16x32_bf16 v[208:211], v[40:43], v[194:197], v[208:211]\n" \
    "v_max3_u32 v238, v238, v193, v194\n" \
    "v_mfma_f32_16x16x32_bf16 v[208:211], v[44:47], v[198:201], v[208:211]\n" \
    "v_max3_u32 v238, v238, v195, v196\n" \
    "v_mfma_f32_16x16x32_bf16 v[212:215], v[64:67], v[194:197], v[212:215]\n" \
    "v_max3_u32 v238, v238, v197, v198\n" \
    "v_mfma_f32_16x16x32_bf16 v[212:215], v[68:71], v[198:201], v[212:215]\n" \
    "v_max3_u32 v238, v238, v199, v200\n" \
    "v_mfma_f32_16x16x32_bf16 v[216:219], v[88:91], v[194:197], v[216:219]\n" \
    "s_waitcnt vmcnt(0)\n" \
    "v_max3_u32 v238, v238, v201, v202\n" \
    "v_mfma_f32_16x16x32_bf16 v[216:219], v[92:95], v[198:201], v[216:219]\n" \
    "v_max_u32 v238, v238, v203\n" \
    "ds_write_b128 v145, v[204:207] offset:49152\n" \
    "v_mfma_f32_16x16x32_bf16 v[220:223], v[112:115], v[194:197], v[220:223]\n" \
    "v_mfma_f32_16x16x32_bf16 v[220:223], v[116:119], v[198:201], v[220:223]\n" \
    "ds_write_b128 v145, v[208:211] offset:50176\n" \
    "v_mfma_f32_16x16x32_bf16 v[224:227], v[136:139], v[194:197], v[224:227]\n" \
    "v_mfma_f32_16x16x32_bf16 v[224:227], v[140:143], v[198:201], v[224:227]\n" \
    "ds_write_b128 v145, v[212:215] offset:51200\n" \
    "v_cmp_eq_u32 vcc, -1, v238\n" \
    "ds_write_b128 v145, v[216:219] offset:52224\n" \
    "s_nop 1\n" \
    "ds_write_b128 v145, v[220:223] offset:53248\n" \
    "s_nop 1\n" \
    "ds_write_b128 v145, v[224:227] offset:54272\n" \
    "s_cbranch_vccnz .Ldn_cf_%=\n" \
    ".Ldn_xf_%=:\n" \
    "v_lshlrev_b32 v244, 16, v202\n" \
    "v_and_b32 v245, 0xffff0000, v202\n" \
    "v_lshlrev_b32 v246, 16, v203\n" \
    "v_and_b32 v247, 0xffff0000, v203\n" \
    "s_add_u32 s48, s33, 3\n" \
    "s_min_u32 s48, s48, 64\n" \
    "s_and_b32 s49, s48, 1\n" \
    "s_xor_b32 s49, s49, 1\n" \
    "s_lshl_b32 s49, s49, 9\n" \
    "s_add_u32 s49, s49, s18\n" \
    "buffer_load_dwordx2 v[150:151], off, s[8:11], s49 sc1\n" \
    "s_cmp_gt_u32 s16, 5\n" \
    "s_waitcnt lgkmcnt(0)\n" \
    "s_barrier\n" \
    "s_cbranch_scc1 .Ldn_sf_%=\n" \
    "v_add_u32 v239, 49152, v146\n" \
    "ds_read_b128 v[204:207], v239 offset:0\n" \
    "ds_read_b128 v[208:211], v239 offset:6144\n" \
    "ds_read_b128 v[212:215], v239 offset:12288\n" \
    "ds_read_b128 v[216:219], v239 offset:18432\n" \
    "ds_read_b128 v[220:223], v239 offset:24576\n" \
    "ds_read_b128 v[224:227], v239 offset:30720\n" \
    "ds_read_b128 v[228:231], v239 offset:36864\n" \
    "ds_read_b128 v[232:235], v239 offset:43008\n" \
    "s_mul_i32 s34, s33, 40960\n" \
    "s_waitcnt lgkmcnt(6)\n" \
    "v_pk_add_f32 v[204:205], v[204:205], v[208:209]\n" \
    "v_pk_add_f32 v[206:207], v[206:207], v[210:211]\n" \
    "s_waitcnt lgkmcnt(4)\n" \
    "v_pk_add_f32 v[212:213], v[212:213], v[216:217]\n" \
    "v_pk_add_f32 v[214:215], v[214:215], v[218:219]\n" \
    "v_pk_add_f32 v[204:205], v[204:205], v[212:213]\n" \
    "v_pk_add_f32 v[206:207], v[206:207], v[214:215]\n" \
    "s_waitcnt lgkmcnt(2)\n" \
    "v_pk_add_f32 v[220:221], v[220:221], v[224:225]\n" \
    "v_pk_add_f32 v[222:223], v[222:223], v[226:227]\n" \
    "s_waitcnt lgkmcnt(0)\n" \
    "v_pk_add_f32 v[228:229], v[228:229], v[232:233]\n" \
    "v_pk_add_f32 v[230:231], v[230:231], v[234:235]\n" \
    "v_pk_add_f32 v[220:221], v[220:221], v[228:229]\n" \
    "v_pk_add_f32 v[222:223], v[222:223], v[230:231]\n" \
    "v_pk_add_f32 v[204:205], v[204:205], v[220:221]\n" \
    "v_pk_add_f32 v[206:207], v[206:207], v[222:223]\n" \
    "v_cvt_pk_bf16_f32 v208, v204, v205\n" \
    "v_cvt_pk_bf16_f32 v209, v206, v207\n" \
    "v_lshlrev_b32 v204, 16, v208\n" \
    "v_and_b32 v205, 0xffff0000, v208\n" \
    "v_lshlrev_b32 v206, 16, v209\n" \
    "v_and_b32 v207, 0xffff0000, v209\n" \
    "v_pk_add_f32 v[204:205], v[204:205], v[244:245]\n" \
    "v_pk_add_f32 v[206:207], v[206:207], v[246:247]\n" \
    "v_cvt_pk_bf16_f32 v250, v204, v205\n" \
    "v_cvt_pk_bf16_f32 v251, v206, v207\n" \
    "buffer_store_dwordx2 v[250:251], v148, s[12:15], s34 offen\n" \
    ".Ldn_sf_%=:\n" \
    "s_barrier\n" \
    "s_add_u32 s33, s33, 1\n" \
    "s_cmp_lt_u32 s33, 64\n" \
    "s_cbranch_scc1 .Ldn_n_%=\n" \
    "s_waitcnt vmcnt(0)\n" \
    "s_endpgm\n" \
    ".Ldn_ca_%=:\n" \
    "s_add_u32 s37, s37, 1\n" \
    "s_cmp_lt_u32 s37, s38\n" \
    "s_cselect_b32 s38, s38, 0\n" \
    "s_cbranch_scc0 .Ldn_xa_%=\n" \
    "s_sleep 4\n" \
    "s_mul_i32 s39, s32, 98304\n" \
    "s_add_u32 s39, s39, s17\n" \
    "buffer_load_dwordx4 v[152:155], v144, s[8:11], s39 offen offset:0 sc1\n" \
    "buffer_load_dwordx4 v[156:159], v144, s[8:11], s39 offen offset:1024 sc1\n" \
    "buffer_load_dwordx4 v[160:163], v144, s[8:11], s39 offen offset:2048 sc1\n" \
    "buffer_load_dwordx4 v[164:167], v144, s[8:11], s39 offen offset:3072 sc1\n" \
    "s_add_u32 s39, s39, 4096\n" \
    "buffer_load_dwordx4 v[168:171], v144, s[8:11], s39 offen offset:0 sc1\n" \
    "buffer_load_dwordx4 v[172:175], v144, s[8:11], s39 offen offset:1024 sc1\n" \
    "s_mul_i32 s39, s32, 40960\n" \
    "s_add_u32 s39, s39, %[x1]+0\n" \
    "buffer_load_dwordx2 v[176:177], v147, s[8:11], s39 offen sc1\n" \
    "s_waitcnt vmcnt(0) lgkmcnt(0)\n" \
    "s_branch .Ldn_ra_%=\n" \
    ".Ldn_cb_%=:\n" \
    "s_add_u32 s37, s37, 1\n" \
    "s_cmp_lt_u32 s37, s38\n" \
    "s_cselect_b32 s38, s38, 0\n" \
    "s_cbranch_scc0 .Ldn_xb_%=\n" \
    "s_sleep 4\n" \
    "s_mul_i32 s39, s33, 98304\n" \
    "s_add_u32 s39, s39, s17\n" \
    "s_add_u32 s39, s39, 6291456\n" \
    "buffer_load_dwordx4 v[178:181], v144, s[8:11], s39 offen offset:0 sc1\n" \
    "buffer_load_dwordx4 v[182:185], v144, s[8:11], s39 offen offset:1024 sc1\n" \
    "buffer_load_dwordx4 v[186:189], v144, s[8:11], s39 offen offset:2048 sc1\n" \
    "buffer_load_dwordx4 v[190:193], v144, s[8:11], s39 offen offset:3072 sc1\n" \
    "s_add_u32 s39, s39, 4096\n" \
    "buffer_load_dwordx4 v[194:197], v144, s[8:11], s39 offen offset:0 sc1\n" \
    "buffer_load_dwordx4 v[198:201], v144, s[8:11], s39 offen offset:1024 sc1\n" \
    "s_mul_i32 s39, s33, 40960\n" \
    "s_add_u32 s39, s39, %[x1]+2621440\n" \
    "buffer_load_dwordx2 v[202:203], v147, s[8:11], s39 offen sc1\n" \
    "s_waitcnt vmcnt(0) lgkmcnt(0)\n" \
    "s_branch .Ldn_rb_%=\n" \
    ".Ldn_cc_%=:\n" \
    "s_add_u32 s37, s37, 1\n" \
    "s_cmp_lt_u32 s37, s38\n" \
    "s_cselect_b32 s38, s38, 0\n" \
    "s_cbranch_scc0 .Ldn_xc_%=\n" \
    "s_sleep 4\n" \
    "s_mul_i32 s39, s33, 98304\n" \
    "s_add_u32 s39, s39, s17\n" \
    "s_add_u32 s39, s39, 6291456\n" \
    "buffer_load_dwordx4 v[178:181], v144, s[8:11], s39 offen offset:0 sc1\n" \
    "buffer_load_dwordx4 v[182:185], v144, s[8:11], s39 offen offset:1024 sc1\n" \
    "buffer_load_dwordx4 v[186:189], v144, s[8:11], s39 offen offset:2048 sc1\n" \
    "buffer_load_dwordx4 v[190:193], v144, s[8:11], s39 offen offset:3072 sc1\n" \
    "s_add_u32 s39, s39, 4096\n" \
    "buffer_load_dwordx4 v[194:197], v144, s[8:11], s39 offen offset:0 sc1\n" \
    "buffer_load_dwordx4 v[198:201], v144, s[8:11], s39 offen offset:1024 sc1\n" \
    "s_mul_i32 s39, s33, 40960\n" \
    "s_add_u32 s39, s39, %[x1]+2621440\n" \
    "buffer_load_dwordx2 v[202:203], v147, s[8:11], s39 offen sc1\n" \
    "s_waitcnt vmcnt(0) lgkmcnt(0)\n" \
    "s_branch .Ldn_rc_%=\n" \
    ".Ldn_cd_%=:\n" \
    "s_add_u32 s37, s37, 1\n" \
    "s_cmp_lt_u32 s37, s38\n" \
    "s_cselect_b32 s38, s38, 0\n" \
    "s_cbranch_scc0 .Ldn_xd_%=\n" \
    "s_sleep 4\n" \
    "s_mul_i32 s39, s32, 98304\n" \
    "s_add_u32 s39, s39, s17\n" \
    "buffer_load_dwordx4 v[152:155], v144, s[8:11], s39 offen offset:0 sc1\n" \
    "buffer_load_dwordx4 v[156:159], v144, s[8:11], s39 offen offset:1024 sc1\n" \
    "buffer_load_dwordx4 v[160:163], v144, s[8:11], s39 offen offset:2048 sc1\n" \
    "buffer_load_dwordx4 v[164:167], v144, s[8:11], s39 offen offset:3072 sc1\n" \
    "s_add_u32 s39, s39, 4096\n" \
    "buffer_load_dwordx4 v[168:171], v144, s[8:11], s39 offen offset:0 sc1\n" \
    "buffer_load_dwordx4 v[172:175], v144, s[8:11], s39 offen offset:1024 sc1\n" \
    "s_mul_i32 s39, s32, 40960\n" \
    "s_add_u32 s39, s39, %[x1]+0\n" \
    "buffer_load_dwordx2 v[176:177], v147, s[8:11], s39 offen sc1\n" \
    "s_waitcnt vmcnt(0) lgkmcnt(0)\n" \
    "s_branch .Ldn_rd_%=\n" \
    ".Ldn_cf_%=:\n" \
    "s_add_u32 s37, s37, 1\n" \
    "s_cmp_lt_u32 s37, s38\n" \
    "s_cselect_b32 s38, s38, 0\n" \
    "s_cbranch_scc0 .Ldn_xf_%=\n" \
    "s_sleep 4\n" \
    "s_mul_i32 s39, s33, 98304\n" \
    "s_add_u32 s39, s39, s17\n" \
    "s_add_u32 s39, s39, 6291456\n" \
    "buffer_load_dwordx4 v[178:181], v144, s[8:11], s39 offen offset:0 sc1\n" \
    "buffer_load_dwordx4 v[182:185], v144, s[8:11], s39 offen offset:1024 sc1\n" \
    "buffer_load_dwordx4 v[186:189], v144, s[8:11], s39 offen offset:2048 sc1\n" \
    "buffer_load_dwordx4 v[190:193], v144, s[8:11], s39 offen offset:3072 sc1\n" \
    "s_add_u32 s39, s39, 4096\n" \
    "buffer_load_dwordx4 v[194:197], v144, s[8:11], s39 offen offset:0 sc1\n" \
    "buffer_load_dwordx4 v[198:201], v144, s[8:11], s39 offen offset:1024 sc1\n" \
    "s_mul_i32 s39, s33, 40960\n" \
    "s_add_u32 s39, s39, %[x1]+2621440\n" \
    "buffer_load_dwordx2 v[202:203], v147, s[8:11], s39 offen sc1\n" \
    "s_waitcnt vmcnt(0) lgkmcnt(0)\n" \
    "s_branch .Ldn_rf_%=\n" \
    ""
#define LN_ASM \
    "s_mov_b32 s32, 0\n" \
    "s_mov_b32 s36, 0x40000\n" \
    "s_mov_b32 s44, 0xaaaaaaaa\n" \
    "s_mov_b32 s45, 0xaaaaaaaa\n" \
    "s_mov_b32 s46, 0xcccccccc\n" \
    "s_mov_b32 s47, 0xcccccccc\n" \
    "v_mov_b32 v12, 0x3f803f80\n" \
    "v_mov_b32 v64, 1\n" \
    "v_mov_b32 v13, 0x3f803f80\n" \
    "v_mov_b32 v65, 1\n" \
    "v_mov_b32 v14, 0x3f803f80\n" \
    "v_mov_b32 v66, 1\n" \
    "v_mov_b32 v15, 0x3f803f80\n" \
    "v_mov_b32 v67, 1\n" \
    "s_add_u32 s48, s32, 2\n" \
    "s_min_u32 s48, s48, 64\n" \
    "s_and_b32 s49, s48, 1\n" \
    "s_xor_b32 s49, s49, 1\n" \
    "s_lshl_b32 s49, s49, 9\n" \
    "s_add_u32 s49, s49, s20\n" \
    "buffer_load_dwordx2 v[8:9], off, s[8:11], s49 sc1\n" \
    ".Lln_l_%=:\n" \
    "s_mov_b32 s35, 0\n" \
    "s_waitcnt vmcnt(0)\n" \
    ".Lln_kp_%=:\n" \
    "v_readfirstlane_b32 s34, v9\n" \
    "s_nop 0\n" \
    "s_cmp_gt_i32 s34, s48\n" \
    "s_cbranch_scc1 .Lln_kx_%=\n" \
    "s_cmp_eq_u32 s34, s48\n" \
    "s_cbranch_scc1 .Lln_kw_%=\n" \
    "s_sleep 3\n" \
    "buffer_load_dwordx2 v[8:9], off, s[8:11], s49 sc1\n" \
    "s_waitcnt vmcnt(0)\n" \
    "s_add_u32 s35, s35, 1\n" \
    "s_cmp_lt_u32 s35, s36\n" \
    "s_cbranch_scc1 .Lln_kp_%=\n" \
    "s_mov_b32 s36, 0\n" \
    "s_branch .Lln_kx_%=\n" \
    ".Lln_kw_%=:\n" \
    "v_readfirstlane_b32 s34, v8\n" \
    "s_cmp_eq_u32 s32, 63\n" \
    "s_cselect_b32 s33, %[dlast], 0\n" \
    "s_add_u32 s34, s34, s33\n" \
    "s_add_u32 s34, s34, %[dxl]\n" \
    "s_branch .Lln_ktb_%=\n" \
    ".Lln_kta_%=:\n" \
    "s_sleep 1\n" \
    ".Lln_ktb_%=:\n" \
    "s_memrealtime vcc\n" \
    "s_waitcnt lgkmcnt(0)\n" \
    "s_sub_i32 vcc_lo, s34, vcc_lo\n" \
    "s_cmp_gt_i32 vcc_lo, 8\n" \
    "s_cbranch_scc1 .Lln_kta_%=\n" \
    "s_cmp_gt_i32 vcc_lo, 0\n" \
    "s_cbranch_scc1 .Lln_ktb_%=\n" \
    ".Lln_kx_%=:\n" \
    "s_mul_i32 s37, s32, 40960\n" \
    "s_add_u32 s37, s37, s17\n" \
    "buffer_load_dwordx4 v[16:19], v0, s[8:11], s37 offen sc1\n" \
    "s_add_u32 s37, s37, 8192\n" \
    "buffer_load_dwordx4 v[20:23], v0, s[8:11], s37 offen sc1\n" \
    "s_add_u32 s37, s37, 8192\n" \
    "buffer_load_dwordx4 v[24:27], v1, s[8:11], s37 offen sc1\n" \
    "s_mov_b32 s35, 0\n" \
    ".Lln_r_%=:\n" \
    "s_waitcnt vmcnt(2)\n" \
    "v_mfma_f32_16x16x32_bf16 v[32:35], v[16:19], v[16:19], 0\n" \
    "v_mfma_f32_16x16x32_bf16 v[28:31], v[12:15], v[16:19], 0\n" \
    "s_waitcnt vmcnt(1)\n" \
    "v_mfma_f32_16x16x32_bf16 v[32:35], v[20:23], v[20:23], v[32:35]\n" \
    "v_mfma_f32_16x16x32_bf16 v[28:31], v[12:15], v[20:23], v[28:31]\n" \
    "s_waitcnt vmcnt(0)\n" \
    "v_mfma_f32_16x16x32_bf16 v[32:35], v[24:27], v[24:27], v[32:35]\n" \
    "v_mfma_f32_16x16x32_bf16 v[28:31], v[12:15], v[24:27], v[28:31]\n" \
    "v_max3_u32 v10, v16, v17, v18\n" \
    "v_max3_u32 v10, v10, v19, v20\n" \
    "v_max3_u32 v10, v10, v21, v22\n" \
    "v_max3_u32 v10, v10, v23, v24\n" \
    "v_max3_u32 v10, v10, v25, v26\n" \
    "v_max_u32 v10, v10, v27\n" \
    "v_cmp_eq_u32 vcc, -1, v10\n" \
    "s_nop 0\n" \
    "s_cbranch_vccnz .Lln_c_%=\n" \
    ".Lln_x_%=:\n" \
    "v_cndmask_b32 v10, v32, v33, s[44:45]\n" \
    "v_cndmask_b32 v11, v34, v35, s[44:45]\n" \
    "v_cndmask_b32 v29, v10, v11, s[46:47]\n" \
    "ds_write_b64 v2, v[28:29]\n" \
    "v_lshlrev_b32 v40, 16, v16\n" \
    "v_and_b32 v41, 0xffff0000, v16\n" \
    "v_lshlrev_b32 v42, 16, v17\n" \
    "v_and_b32 v43, 0xffff0000, v17\n" \
    "v_lshlrev_b32 v44, 16, v18\n" \
    "v_and_b32 v45, 0xffff0000, v18\n" \
    "v_lshlrev_b32 v46, 16, v19\n" \
    "v_and_b32 v47, 0xffff0000, v19\n" \
    "v_lshlrev_b32 v48, 16, v20\n" \
    "v_and_b32 v49, 0xffff0000, v20\n" \
    "v_lshlrev_b32 v50, 16, v21\n" \
    "v_and_b32 v51, 0xffff0000, v21\n" \
    "v_lshlrev_b32 v52, 16, v22\n" \
    "v_and_b32 v53, 0xffff0000, v22\n" \
    "v_lshlrev_b32 v54, 16, v23\n" \
    "v_and_b32 v55, 0xffff0000, v23\n" \
    "v_lshlrev_b32 v56, 16, v24\n" \
    "v_and_b32 v57, 0xffff0000, v24\n" \
    "v_lshlrev_b32 v58, 16, v25\n" \
    "v_and_b32 v59, 0xffff0000, v25\n" \
    "v_lshlrev_b32 v60, 16, v26\n" \
    "v_and_b32 v61, 0xffff0000, v26\n" \
    "v_lshlrev_b32 v62, 16, v27\n" \
    "v_and_b32 v63, 0xffff0000, v27\n" \
    "s_mul_i32 s37, s32, 40960\n" \
    "s_add_u32 s37, s37, s18\n" \
    "s_waitcnt lgkmcnt(0)\n" \
    "s_barrier\n" \
    "ds_read2_b64 v[36:39], v3 offset1:16\n" \
    "v_xor_b32 v2, 1024, v2\n" \
    "v_xor_b32 v3, 1024, v3\n" \
    "s_waitcnt lgkmcnt(0)\n" \
    "v_add_f32 v36, v36, v38\n" \
    "v_add_f32 v37, v37, v39\n" \
    "s_nop 1\n" \
    "v_permlane16_swap_b32 v36, v37\n" \
    "v_add_f32 v38, v36, v37\n" \
    "v_add_f32 v36, v36, v37\n" \
    "s_nop 1\n" \
    "v_permlane32_swap_b32 v36, v38\n" \
    "v_add_f32 v37, v36, v38\n" \
    "v_add_f32 v36, v36, v38\n" \
    "s_nop 1\n" \
    "v_permlane16_swap_b32 v36, v37\n" \
    "v_mul_f32 v36, 0xbacccccd, v36\n" \
    "v_mul_f32 v37, 0x3acccccd, v37\n" \
    "v_fma_f32 v37, -v36, v36, v37\n" \
    "v_max_f32 v37, 0, v37\n" \
    "v_add_f32 v37, 0x3727c5ac, v37\n" \
    "v_rsq_f32 v38, v37\n" \
    "s_nop 0\n" \
    "v_mul_f32 v36, v36, v38\n" \
    "v_fma_f32 v40, v40, v38, v36\n" \
    "v_fma_f32 v41, v41, v38, v36\n" \
    "v_cvt_pk_bf16_f32 v16, v40, v41\n" \
    "v_fma_f32 v42, v42, v38, v36\n" \
    "v_fma_f32 v43, v43, v38, v36\n" \
    "v_cvt_pk_bf16_f32 v17, v42, v43\n" \
    "v_fma_f32 v44, v44, v38, v36\n" \
    "v_fma_f32 v45, v45, v38, v36\n" \
    "v_cvt_pk_bf16_f32 v18, v44, v45\n" \
    "v_fma_f32 v46, v46, v38, v36\n" \
    "v_fma_f32 v47, v47, v38, v36\n" \
    "v_cvt_pk_bf16_f32 v19, v46, v47\n" \
    "buffer_store_dwordx4 v[16:19], v0, s[8:11], s37 offen sc1\n" \
    "s_add_u32 s37, s37, 8192\n" \
    "v_fma_f32 v48, v48, v38, v36\n" \
    "v_fma_f32 v49, v49, v38, v36\n" \
    "v_cvt_pk_bf16_f32 v20, v48, v49\n" \
    "v_fma_f32 v50, v50, v38, v36\n" \
    "v_fma_f32 v51, v51, v38, v36\n" \
    "v_cvt_pk_bf16_f32 v21, v50, v51\n" \
    "v_fma_f32 v52, v52, v38, v36\n" \
    "v_fma_f32 v53, v53, v38, v36\n" \
    "v_cvt_pk_bf16_f32 v22, v52, v53\n" \
    "v_fma_f32 v54, v54, v38, v36\n" \
    "v_fma_f32 v55, v55, v38, v36\n" \
    "v_cvt_pk_bf16_f32 v23, v54, v55\n" \
    "buffer_store_dwordx4 v[20:23], v0, s[8:11], s37 offen sc1\n" \
    "s_add_u32 s37, s37, 8192\n" \
    "v_fma_f32 v56, v56, v38, v36\n" \
    "v_fma_f32 v57, v57, v38, v36\n" \
    "v_cvt_pk_bf16_f32 v24, v56, v57\n" \
    "v_fma_f32 v58, v58, v38, v36\n" \
    "v_fma_f32 v59, v59, v38, v36\n" \
    "v_cvt_pk_bf16_f32 v25, v58, v59\n" \
    "v_fma_f32 v60, v60, v38, v36\n" \
    "v_fma_f32 v61, v61, v38, v36\n" \
    "v_cvt_pk_bf16_f32 v26, v60, v61\n" \
    "v_fma_f32 v62, v62, v38, v36\n" \
    "v_fma_f32 v63, v63, v38, v36\n" \
    "v_cvt_pk_bf16_f32 v27, v62, v63\n" \
    "buffer_store_dwordx4 v[24:27], v1, s[8:11], s37 offen sc1\n" \
    "s_lshl_b32 s33, s32, 10\n" \
    "s_add_u32 s33, s33, s19\n" \
    "s_waitcnt vmcnt(0)\n" \
    "buffer_store_dwordx4 v[64:67], v4, s[8:11], s33 offen sc1\n" \
    "s_add_u32 s48, s32, 3\n" \
    "s_min_u32 s48, s48, 64\n" \
    "s_and_b32 s49, s48, 1\n" \
    "s_xor_b32 s49, s49, 1\n" \
    "s_lshl_b32 s49, s49, 9\n" \
    "s_add_u32 s49, s49, s20\n" \
    "buffer_load_dwordx2 v[8:9], off, s[8:11], s49 sc1\n" \
    "s_add_u32 s32, s32, 1\n" \
    "s_cmp_lt_u32 s32, 64\n" \
    "s_cbranch_scc1 .Lln_l_%=\n" \
    "s_waitcnt vmcnt(0)\n" \
    "s_endpgm\n" \
    ".Lln_c_%=:\n" \
    "s_add_u32 s35, s35, 1\n" \
    "s_cmp_lt_u32 s35, s36\n" \
    "s_cselect_b32 s36, s36, 0\n" \
    "s_cbranch_scc0 .Lln_x_%=\n" \
    "s_sleep 2\n" \
    "s_mul_i32 s37, s32, 40960\n" \
    "s_add_u32 s37, s37, s17\n" \
    "buffer_load_dwordx4 v[16:19], v0, s[8:11], s37 offen sc1\n" \
    "s_add_u32 s37, s37, 8192\n" \
    "buffer_load_dwordx4 v[20:23], v0, s[8:11], s37 offen sc1\n" \
    "s_add_u32 s37, s37, 8192\n" \
    "buffer_load_dwordx4 v[24:27], v1, s[8:11], s37 offen sc1\n" \
    "s_waitcnt vmcnt(0)\n" \
    "s_branch .Lln_r_%=\n" \
    ""
#define QK_WL 65536
template <int WL, int SL2>
__device__ __forceinline__ void qk_weights(const u16* wa, const u16* wb, char* smem, const int w, const int l, bf16x16 (&wt)[3][8]) {
    const rsrc_t rq_ = __builtin_amdgcn_make_buffer_rsrc((void*)(w < 4 ? wa : wb), 0, 639 * 12288 + 384, 0x00020000);
#define QK_SLOT(r_) ((r_) % 3 == 0 ? SL2 : SL2 ? ((r_) % 3 - 1) * 49152 : ((r_) % 3) * 49152)
#define QK_ISSUE(r_) { if ((r_) < 10) { _Pragma("unroll") for (int i_ = 0; i_ < 6; ++i_) { \
        const unsigned ix_ = (unsigned)(((w & 3) * 6 + i_) * 64 + l), rw_ = ix_ / 24, cc_ = (ix_ % 24) ^ ((rw_ & 3) << 1); \
        __builtin_amdgcn_raw_ptr_buffer_load_lds(rq_, (lds_ptr_t)(smem + QK_SLOT(r_) + (w * 6 + i_) * 1024), 16, (int)(rw_ * 12288 + cc_ * 16), (r_) * 64 * 12288, 0, AXQ); } } }
    const unsigned c01 = (unsigned)((w & 3) * 96 + (l & 3) * 8);
    bf16x8 hold[6];
    char* wl = smem + WL + w * 12288 + l * 16;
    QK_ISSUE(0) QK_ISSUE(1)
#pragma unroll
    for (int r = 0; r < 10; ++r) {
        if (r + 1 < 10) WAITVN(6) else WAITV0();
        RAWBAR()
        QK_ISSUE(r + 2)
        const char* sb = smem + QK_SLOT(r) + (w >> 2) * 24576;
        if (r == 9) {
#pragma unroll
            for (int i = 0; i < 6; ++i) *(bf16x8*)(wl + i * 1024) = hold[i];
        }
#pragma unroll
        for (int i = 0; i < 3; ++i) {
            const bf16x8 f0 = tr_frag<24, false>(sb, 0, c01 + i * 32, l), f1 = tr_frag<24, false>(sb, 32, c01 + i * 32, l);
            if (r < 8) wt[i][r & 7] = cat16(f0, f1);
            else if (r == 8) { hold[i] = f0; hold[3 + i] = f1; }
            else { *(bf16x8*)(wl + (6 + i) * 1024) = f0; *(bf16x8*)(wl + (9 + i) * 1024) = f1; }
        }
    }
    RAWBAR()
}
#define WLT(i) cat16(LDS(bf16x8, wl + (((i) / 3) * 6 + (i) % 3) * 1024), LDS(bf16x8, wl + (((i) / 3) * 6 + 3 + (i) % 3) * 1024))
template <typename S, typename F, typename M>
__device__ __forceinline__ void projm(const bf16x16 (&wt)[3][8], const unsigned mf, const unsigned wl, int ix, f32x4& a0, f32x4& a1, f32x4& a2, S sync, F side, M mid) {
    bf16x8 A[8], AX[2];
    bf16x16 W[6];
    MEMB();
    W[0] = WLT(0); W[1] = WLT(1);
    a0 = Z4; a1 = Z4; a2 = Z4;
    asm volatile("" : "+v"(a0), "+v"(a1), "+v"(a2));
    SB0();
    sync();
    A[0] = LDS(bf16x8, mf); A[1] = LDS(bf16x8, mf + 1024); AX[0] = LDS(bf16x8, mf + 8192); A[2] = LDS(bf16x8, mf + 2048);
    SB0();
    side();
    SB0();
    bf16x8 Wh[6][2];
#define WH(i, h_) Wh[i][h_] = LDS(bf16x8, wl + (((i) / 3) * 6 + (h_) * 3 + (i) % 3) * 1024);
#define PMS(d, x, y) { d = SMFMA(x, y, d, ix); asm volatile("" : "+v"(d)); SB0(); }
    static_for<0, 8>([&](auto ic) __attribute__((always_inline)) {
        constexpr int i = decltype(ic)::v;
        constexpr int q = i == 2 || i == 3 ? i : i == 5 || i == 6 ? i - 1 : 0;
        mid(ic);
        if constexpr (i == 7) { PMS(a0, A[i], wt[0][i]) PMS(a1, AX[1], cat16(Wh[4][0], Wh[4][1])) PMS(a1, A[i], wt[1][i]) PMS(a2, A[i], wt[2][i]) PMS(a2, AX[1], cat16(Wh[5][0], Wh[5][1])) }
        else {
        PMS(a0, A[i], wt[0][i])
        if constexpr (i + 3 < 8) A[i + 3] = LDS(bf16x8, mf + (i + 3) * 1024);
        if (i >= 5 && q) WH(q, 0)
        SB0();
        PMS(a1, A[i], wt[1][i])
        if (i == 1) AX[1] = LDS(bf16x8, mf + 9216);
        if (q) WH(q, i >= 5)
        SB0();
        PMS(a2, A[i], wt[2][i])
        if (i < 5 && q) WH(q, 1)
        SB0();
        if (i == 1) PMS(a0, AX[0], W[0])
        if (i == 2) PMS(a1, AX[0], W[1])
        if (i == 4) PMS(a2, AX[0], cat16(Wh[2][0], Wh[2][1]))
        if (i == 5) PMS(a0, AX[1], cat16(Wh[3][0], Wh[3][1]))
        }
    });
}
#define NOF [&]() __attribute__((always_inline)) {}
#define NOM [&](auto) __attribute__((always_inline)) {}
#define BAREBAR() { asm volatile("s_barrier" ::: "memory"); SB0(); }
__device__ __forceinline__ void wait_fine(unsigned t) {
    asm volatile("s_branch 2f\n1:\n\ts_sleep 1\n2:\n\ts_memrealtime vcc\n\ts_waitcnt lgkmcnt(0)\n\ts_sub_i32 vcc_lo, %0, vcc_lo\n\ts_cmp_gt_i32 vcc_lo, 8\n\ts_cbranch_scc1 1b\n\t"
                 "s_cmp_gt_i32 vcc_lo, 0\n\ts_cbranch_scc1 2b" :: "s"(t) : "memory", "scc", "vcc");
}
__device__ __forceinline__ void dma6(u32x4 rd, unsigned dst, unsigned voff, unsigned soff) {
    unsigned d2, s2;
    asm volatile("s_mov_b32 m0, %2\n\ts_add_u32 %0, %2, 0x1000\n\tbuffer_load_dwordx4 %3, %4, %5 offen sc1 lds\n\tbuffer_load_dwordx4 %3, %4, %5 offen offset:1024 sc1 lds\n\t"
                 "buffer_load_dwordx4 %3, %4, %5 offen offset:2048 sc1 lds\n\tbuffer_load_dwordx4 %3, %4, %5 offen offset:3072 sc1 lds\n\t"
                 "s_mov_b32 m0, %0\n\ts_add_u32 %1, %5, 0x1000\n\tbuffer_load_dwordx4 %3, %4, %1 offen sc1 lds\n\tbuffer_load_dwordx4 %3, %4, %1 offen offset:1024 sc1 lds"
                 : "=&s"(d2), "=&s"(s2) : "s"(dst), "v"(voff), "s"(rd), "s"(soff) : "memory", "scc");
}
__device__ __forceinline__ void st64(rsrc_t rs, unsigned voff, unsigned soff, u32x2 v) { __builtin_amdgcn_raw_buffer_store_b64(v, rs, (int)voff, (int)soff, AUX_SC1); }

#define QM_MF 0
#define QM_QM 10240
#define QM_KM 13312
#define QM_WL 16384
#define QM_QK 114688
#define KWIN 5
#define AWIN 4
#define BQP 2
template <int D> __device__ __forceinline__ int ev_id(int e) {
    const int d = e - D, id = (d >> 1) + ((d & 1) ? 64 : D);
    return (unsigned)d > (unsigned)(127 - 2 * D) ? e : id;
}
__device__ __forceinline__ unsigned fold1(f32x4 a) { return pack2(a[0] + a[2], a[1] + a[3]); }
typedef __attribute__((ext_vector_type(32))) unsigned u32x32;
typedef __attribute__((ext_vector_type(16))) unsigned u32x16;
typedef __attribute__((ext_vector_type(8))) unsigned u32x8;
__device__ __forceinline__ u32x32 cat4(bf16x16 a, bf16x16 b, bf16x16 c, bf16x16 d) {
    const u32x8 A = __builtin_bit_cast(u32x8, a), B = __builtin_bit_cast(u32x8, b), C = __builtin_bit_cast(u32x8, c), D = __builtin_bit_cast(u32x8, d);
    const u32x16 x = __builtin_shufflevector(A, B, 0, 1, 2, 3, 4, 5, 6, 7, 8, 9, 10, 11, 12, 13, 14, 15), y = __builtin_shufflevector(C, D, 0, 1, 2, 3, 4, 5, 6, 7, 8, 9, 10, 11, 12, 13, 14, 15);
    return __builtin_shufflevector(x, y, 0, 1, 2, 3, 4, 5, 6, 7, 8, 9, 10, 11, 12, 13, 14, 15, 16, 17, 18, 19, 20, 21, 22, 23, 24, 25, 26, 27, 28, 29, 30, 31);
}
#define WIN(t) "{v[0:31]}"(cat4(t[0], t[1], t[2], t[3])), "{v[32:63]}"(cat4(t[4], t[5], t[6], t[7])), "{v[64:95]}"(cat4(t[8], t[9], t[10], t[11])), \
               "{v[96:127]}"(cat4(t[12], t[13], t[14], t[15])), "{v[128:159]}"(cat4(t[16], t[17], t[18], t[19])), "{v[160:191]}"(cat4(t[20], t[21], t[22], t[23]))
#define ACLOB "memory", "vcc", "scc", "v255", "s99"
__device__ __forceinline__ void qkm_role(const Params& p, char* smem, const int w, const int h, const u32x2 nc) {
    bf16x16 wt[3][8];
    qk_weights<QM_WL, QM_QK>(p.Wq + h * 192, p.Wk + h * 192, smem, w, lane_id(), wt);
    SU(3)
    const rsrc_t rs = __builtin_amdgcn_make_buffer_rsrc((void*)wsp(p), 0, 0x7fffffff, 0x00020000);
    const unsigned ns = start_wait(p, smem, w, nc), hm = hot_page(ns, 2), hp = hm ^ RFX;
    RAWRD()
    const unsigned lo = lane_id() * 16;
    const unsigned dr = QM_QM + (w >> 2) * 3072 + (lo >> 8) * 4;
    unsigned po1, po2 = 0xC0000000u;
    const unsigned g8 = (lo >> 5) & 8;
    if (w == 0) { po1 = ((lo >> 1) & 0x100) + (lo & 0x60) * 2 + ((lo & 0x80) >> 2) + (lo & 0x10) + g8 + (h & 3) * 512; if (!(lo & 0x280)) po2 = (lo & 0x70) + g8 + 2048 + (h & 3) * 128; }
    else if (w == 1) { po1 = (lo & 0x2f0) + g8 + (h & 2) * 512 + (h & 1) * 256; if (lo < 512) po2 = (lo & 0xf0) + g8 + 2048 + (h & 3) * 256; }
    else if (w == 2) { po1 = lo + 3072 + (h & 3) * 1024; if (lo < 512) po2 = (lo & 0xf0) + g8 + 7168 + (h & 3) * 256; }
    else if (w == 4) po1 = lo < 512 ? lo + (h & 3) * 512 : lo < 640 ? lo + 1536 + (h & 3) * 128 : 0xC0000000u;
    else if (w == 5) { po1 = (lo & 256) ? 0xC0000000u : lo + (h & 2) * 512 + (h & 1) * 256; if (lo < 256) po2 = lo + 2048 + (h & 3) * 256; }
    else if (w == 6) { po1 = lo + 3072 + (h & 3) * 1024; if (lo < 256) po2 = lo + 7168 + (h & 3) * 256; }
    else po1 = lo < 128 ? lo : 0xC0000000u;
    const bool pm = !(w & 3);
    const unsigned pbs = pm ? hp + HB_PM + (h >> 3) * 16384 + ((h >> 2) & 1) * 2560 : hp + HB_PT + (h >> 2) * 16384;
    const int psh = pm ? 16 : 17;
    if (w >= 4 && w < 7) {
        const u32x4 ps = poison4();
#pragma unroll
        for (int i = 0; i < 4; ++i) { const unsigned b_ = pbs + ((unsigned)((i & 1) + (i >> 1) * 4) << psh); st128(rs, po1, b_, ps); st128(rs, po2, b_, ps); }
    }
    const u32x8 lc = {lo, 0x4444 + ((lo >> 5) & 1) * 0xAAAA, dr + (w & 3) * 768 + (lo & 0xf0), dr + (w & 3) * 768 + (lo & 0xf0) + 512,
                      w < 4 ? troff(lo) : QM_WL + w * 12288 + lo, po1, po2, lo ? 0xC0000000u : h * 4u};
    const int mk = (w >> 1) & 1, p0 = h * 12 + (w & 1) * 6, sw = (p0 & 7) == 6;
    const u32x16 sc = {UNI(w), UNI(pbs), UNI(psh), UNI(hm + HB_MEM + ((w - 3) >> 2) * 16384 + ((w - 3) & 3) * 2048), UNI(OFF_QK + mk * 786432u + blk(p0 + sw * 2)),
                       UNI(lb + QM_QK + (w & 3) * 6144 + sw * 2048), UNI(hm + HB_FK + h * 256u + mk * 96), UNI(hp + HB_TS + h * 128),
                       UNI(OFF_QK + mk * 786432u + blk(p0 + (sw ? 0 : 4))), UNI(sw ? -2048 : 4096), UNI(ns ^ (mk ? KSX : QSX)), UNI(mk ? HSEG : 1000), 0u, 0u, 0u, 0u};
    const bf16x16* t = &wt[0][0];
    asm volatile("s_nop 4\n" QKM_ASM :: WIN(t), "{v[192:199]}"(lc), "{s[16:31]}"(sc), "{s[8:11]}"(rd), [lag] "n"(LAG), [dq] "n"(DQ), [dq3] "n"(DQ3), [rmask] "n"(RMASK), [hn] "n"(GH / 2) : ACLOB);
}

__device__ __forceinline__ void g_role(const Params& p, char* smem, const int w, const int j, const int a0, const int a1, const int b0, const u32x2 nc, unsigned ns) {
    const int l = lane_id(), m = j / 24, c0 = (j - m * 24) * 256;
    const rsrc_t rw = __builtin_amdgcn_make_buffer_rsrc((void*)((m == 0 ? p.Wq : m == 1 ? p.Wk : p.Wv) + c0), 0, 639 * 12288 + 512, 0x00020000);
    bf16x8 wt[2][20];
#define G_ISS(r_, ax_) { _Pragma("unroll") for (int i_ = 0; i_ < 4; ++i_) { \
        const unsigned ix_ = (unsigned)((w * 4 + i_) * 64 + l), rw_ = ix_ >> 5, cc_ = (ix_ & 31) ^ ((rw_ & 3) << 1); \
        __builtin_amdgcn_raw_ptr_buffer_load_lds(rw, (lds_ptr_t)(smem + ((r_) & 3) * 32768 + (w * 4 + i_) * 1024), 16, (int)(rw_ * 12288 + cc_ * 16), (r_) * 64 * 12288, 0, ax_); } }
#define G_ISSUE(r_) { if ((r_) < 10) { if (m == 2) G_ISS(r_, AXV) else G_ISS(r_, AXG) } }
    G_ISSUE(0) G_ISSUE(1) G_ISSUE(2)
#pragma unroll
    for (int r = 0; r < 10; ++r) {
        if (r < 8) WAITVN(8) else if (r == 8) WAITVN(4) else WAITV0();
        RAWBAR()
        G_ISSUE(r + 3)
#pragma unroll
        for (int kk = 0; kk < 2; ++kk)
#pragma unroll
            for (int i = 0; i < 2; ++i) wt[i][r * 2 + kk] = tr_frag<32, false>(smem + (r & 3) * 32768, kk * 32, (unsigned)(w * 64 + i * 32 + (l & 3) * 8), l);
    }
    RAWBAR()
    if (w == 0) *(unsigned*)(smem + 81920 + (l & 1) * 4) = 0u;
    SU(3)
    if (b0) RAWBAR() else ns = start_wait(p, smem, w, nc);
    RAWRD()
    const unsigned lo = lane_id() * 16;
    const int pc = (j - m * 24) * 8 + w, h = pc / 6, i = pc - h * 6;
    const unsigned fb = m == 2 ? FL_V + h * 512u + i * 64 : hot_page(ns, 2) + HB_FK + h * 256u + m * 96 + i * 16, fs = 16384;
    const u32x4 lc = {lo, lo < (m == 2 ? 64 : 16) ? lo : 0xC0000000u, FL_N + min(lo, 368u) * 4, j == 30 ? lo : 0xC0000000u};
    const u32x16 sc = {UNI(w), UNI(OFF_QK + (m & 1) * 786432u + blk(h * 12 + i)), UNI(OFF_QK + (m & 1) * 786432u + blk(h * 12 + 6 + i)), UNI(ns ^ (m == 1 ? KSX : m ? QSX ^ 0x2000 : QSX)),
                      UNI(fb + (b0 ? 2 * fs : 0)), UNI(fb + fs), UNI(OFF_XN + (w < 4 ? w * 4096 : 12288 + w * 1024)), UNI(lb + (w < 4 ? w * 4096 : 12288 + w * 1024)),
                       UNI(a0), UNI(a1), UNI(a1 + 128), UNI(b0), UNI(263500000 + (j * 8 + w) * 4), UNI(m && !b0 ? 2 * HSEG : 1000), UNI(2 * HSEG + GH), 0u};
    bf16x16 t[20];
#pragma unroll
    for (int k = 0; k < 20; ++k) t[k] = cat16(wt[k / 10][(k % 10) * 2], wt[k / 10][(k % 10) * 2 + 1]);
    asm volatile("s_nop 4\n" G_ASM :: "{v[0:31]}"(cat4(t[0], t[1], t[2], t[3])), "{v[32:63]}"(cat4(t[4], t[5], t[6], t[7])), "{v[64:95]}"(cat4(t[8], t[9], t[10], t[11])),
                 "{v[96:127]}"(cat4(t[12], t[13], t[14], t[15])), "{v[128:159]}"(cat4(t[16], t[17], t[18], t[19])), "{v[160:163]}"(lc), "{s[16:31]}"(sc), "{s[8:11]}"(rd), [rmask] "n"(RMASK)
                 : "memory", "vcc", "scc", "m0", "v164", "v165", "v166", "v167", "v168", "v169", "v170", "v171", "v172", "v173", "v174", "v175", "v176", "v177", "v178", "v179", "v180", "v181", "v182", "v183", "v184", "v185", "v186", "v187", "v188", "v189", "v190", "v191", "v192", "v193", "v194", "v195", "v196", "v197", "v198", "v199", "v200", "v201", "v202", "v203", "v204", "v205", "v206", "v207", "v208", "v209", "v210", "v211", "v212", "v213", "v214", "v215", "s32", "s33", "s34", "s35", "s36", "s37", "s38", "s39", "s40", "s41", "s42", "s43", "s44", "s45", "s46", "s47", "s48", "s49", "s69", "s70", "s71", "s72", "s73", "s74", "s75", "s76", "s77", "s78", "s79", "s80", "s81", "s82", "s83", "s84", "s85", "s86", "s87", "s88", "s89", "s90", "s91");
    RAWBAR()
}

#define UA_VM 10240
#define UA_Z 13312
#define VO_WL 65536
__device__ __forceinline__ void ua_role(const Params& p, char* smem, const int w, const int h, const u32x2 nc) {
    const rsrc_t rs = __builtin_amdgcn_make_buffer_rsrc((void*)wsp(p), 0, 0x7fffffff, 0x00020000);
    const rsrc_t ro_ = __builtin_amdgcn_make_buffer_rsrc((void*)p.Wo, 0, 6144 * 640 * 2, 0x00020000);
    const rsrc_t rv_ = __builtin_amdgcn_make_buffer_rsrc((void*)p.Wv, 0, 640 * 6144 * 2, 0x00020000);
#define VO_ISSUE(r_) { \
        if ((r_) >= UVF && (r_) < UVF + 6) { _Pragma("unroll") for (int i_ = 0; i_ < 5; ++i_) { \
            const unsigned ix_ = (unsigned)((w * 5 + i_) * 64 + l), rw_ = ix_ / 80, cc_ = (ix_ % 80) ^ ((rw_ & 3) << 1); \
            __builtin_amdgcn_raw_ptr_buffer_load_lds(ro_, (lds_ptr_t)(smem + ((r_) % 3) * 49152 + (w * 5 + i_) * 1024), 16, (int)(rw_ * 1280 + cc_ * 16), (h * 192 + ((r_) - UVF) * 32) * 1280, 0, AXUO); } } \
        else if ((r_) < 11) { _Pragma("unroll") for (int i_ = 0; i_ < 6; ++i_) { \
            const unsigned ix_ = (unsigned)((w * 6 + i_) * 64 + l), rw_ = ix_ / 24, cc_ = (ix_ % 24) ^ ((rw_ & 3) << 1); \
            __builtin_amdgcn_raw_ptr_buffer_load_lds(rv_, (lds_ptr_t)(smem + ((r_) % 3) * 49152 + (w * 6 + i_) * 1024), 16, (int)(rw_ * 12288 + cc_ * 16), ((r_) < UVF ? (r_) : (r_) - 6) * 128 * 12288 + h * 384, 0, AXUV); } } }
#define VO_STAGE(BODY_O, BODY_V) { \
        VO_ISSUE(0) VO_ISSUE(1) \
        _Pragma("unroll") for (int rr = 0; rr < 11; ++rr) { \
            if (rr + 1 >= UVF && rr + 1 < UVF + 6) WAITVN(5) else if (rr + 1 < 11) WAITVN(6) else WAITV0(); \
            RAWBAR() \
            VO_ISSUE(rr + 2) \
            const char* sb = smem + (rr % 3) * 49152; \
            if (rr >= UVF && rr < UVF + 6) { const int r = rr - UVF; BODY_O } else { const int r = rr < UVF ? rr : rr - 6; BODY_V } \
        } \
        RAWBAR() }
    const int wq = w ^ 4;
    if (w < 4) {
        const int l = lane_id(), tid = w * 64 + l;
        const unsigned lo_ = l * 16;
        bf16x16 wt[3][8];
        const unsigned c01 = (unsigned)(w * 96 + (l & 3) * 8);
        VO_STAGE({ (void)r; (void)sb; }, {
            _Pragma("unroll") for (int kk = 0; kk < 2; ++kk)
                _Pragma("unroll") for (int i = 0; i < 3; ++i) {
                    const bf16x8 f0 = (tr_frag<24, false>(sb, kk * 64, c01 + i * 32, l));
                    const bf16x8 f1 = (tr_frag<24, false>(sb, kk * 64 + 32, c01 + i * 32, l));
                    if (r < 4) wt[i][(r * 2 + kk) & 7] = cat16(f0, f1);
                    else { *(bf16x8*)(smem + VO_WL + wq * 12288 + (kk * 6 + i) * 1024 + lo_) = f0; *(bf16x8*)(smem + VO_WL + wq * 12288 + (kk * 6 + 3 + i) * 1024 + lo_) = f1; }
                }
        })
        *(unsigned*)(smem + UA_Z + tid * 4) = 0u;
        SU(3)
        const unsigned hm = hot_page(start_wait(p, smem, w, nc), 2);
        RAWRD()
        RAWBAR()
        const unsigned lo = lane_id() * 16;
        const unsigned dr = UA_VM + (lo & 0x10) * 2 + (lo >> 8) * 8 + ((lo >> 3) & 4);
        const u32x8 lc = {lo, 0x4444 + ((lo >> 5) & 1) * 0xAAAA, dr + w * 768 + (lo & 0xc0), dr + w * 768 + (lo & 0xc0) + 512,
                          VO_WL + wq * 12288 + lo, lo + w * 2048, lo + 8192 + (w >> 1) * 1024, (lo & 0x1f0) * 8};
        const u32x8 sc = {UNI(w), UNI(hm + HB_MEM + w * 2048), UNI(hm + HB_MEM + 16384 + (w >> 1) * 1024), UNI((hm ^ RFX) + HB_TS), 0u, 0u, 0u, 0u};
        const bf16x16* t = &wt[0][0];
        asm volatile("s_nop 4\n" UAV_ASM :: WIN(t), "{v[192:199]}"(lc), "{s[16:23]}"(sc), "{s[8:11]}"(rd), [lag] "n"(LAG), [dv] "n"(DV) : ACLOB);
    } else {
        const int l = lane_id();
        bf16x16 wo[8][3];
        bf16x8 hold[12], ev[8];
        VO_STAGE({
            _Pragma("unroll") for (int i = 0; i < 10; ++i) {
                const int jt = wq * 10 + i;
                const int pp = l & 3;
                const unsigned cb = (unsigned)((jt >> 1) * 32 + (pp & 1) * 16 + 4 * (2 * (jt & 1) + (pp >> 1))) * 2;
                const bf16x8 f = (tr_frag<80, false>(sb, 0, cb, l));
                if (i >= 8) hold[(i - 8) * 6 + r] = f;
                else if (r & 1) wo[i & 7][r >> 1] = cat16(ev[i & 7], f);
                else ev[i & 7] = f;
            }
        }, { (void)r; (void)sb; })
#pragma unroll
        for (int i = 0; i < 12; ++i) *(bf16x8*)(smem + VO_WL + wq * 12288 + i * 1024 + lane_id() * 16) = hold[i];
        RAWRD()
        const unsigned ns = start_wait(p, smem, w, nc), hu0 = hot_page(ns, 0) ^ RFX, hu1 = hot_page(ns, 1) ^ RFX, hm = hot_page(ns, 2) ^ RFX;
        const unsigned lo = lane_id() * 16;
        const unsigned ul = (unsigned)(wq * 10 + (lo >> 8)) * 16384u + h * 256 + (lo & 0xf0);
        {
            const u32x4 ps = poison4();
#pragma unroll
            for (int i = 0; i < 4; ++i) { const unsigned b_ = ((i & 2) ? hu1 : hu0) + (i & 1) * 655360u; st128(rs, ul, b_, ps); st128(rs, ul, b_ + 65536, ps); st128(rs, ul, b_ + 98304, ps); }
        }
        WAITV0();
        RAWBAR()
        const u32x8 lc = {lo, 0x4444 + ((lo >> 5) & 1) * 0xAAAA, VO_WL + wq * 12288 + lo, UA_VM + (lo & 0x200) + (lo & 0x80) * 2 + ((lo & 0x100) >> 1) + ((lo & 0x70) >> 1),
                          (unsigned)(wq * 10 + ((lo >> 8) & 1)) * 16384u + h * 256 + (lo & 0xf0) + (lo >> 9) * 8, ul, (lo < 128 && w == 4) || (lo < 512 && h == 0 && w == 5) ? lo : 0xC0000000u, lo || w != 5 ? 0xC0000000u : h * 4u};
        const bool ck = h == 0 && w == 5;
        const u32x8 sc = {UNI(w), UNI(hu0), UNI(hu1), UNI(ck ? FL_T : hm + HB_TS + 4096 + h * 128), UNI(ck ? 9 : 14), 0u, 0u, 0u};
        const bf16x16* t = &wo[0][0];
        asm volatile("s_nop 4\n" UAO_ASM :: WIN(t), "{v[192:199]}"(lc), "{s[16:23]}"(sc), "{s[8:11]}"(rd), [lag] "n"(LAG), [brn] "n"(BRN) : ACLOB);
    }
}

__device__ __forceinline__ void ob_role(const Params& p, char* smem, const int w, const int h, const u32x2 nc) {
    const int l = lane_id();
    const rsrc_t rs = __builtin_amdgcn_make_buffer_rsrc((void*)wsp(p), 0, 0x7fffffff, 0x00020000);
    const rsrc_t ro_ = __builtin_amdgcn_make_buffer_rsrc((void*)p.Wo, 0, 6144 * 640 * 2, 0x00020000);
    bf16x8 wo[5][6];
#define OB_ISSUE(r_) { if ((r_) < 6) { _Pragma("unroll") for (int i_ = 0; i_ < 5; ++i_) { \
            const unsigned ix_ = (unsigned)((w * 5 + i_) * 64 + l), rw_ = ix_ / 80, cc_ = (ix_ % 80) ^ ((rw_ & 3) << 1); \
            __builtin_amdgcn_raw_ptr_buffer_load_lds(ro_, (lds_ptr_t)(smem + ((r_) % 3) * 49152 + (w * 5 + i_) * 1024), 16, (int)(rw_ * 1280 + cc_ * 16), (h * 192 + (r_) * 32) * 1280, 0, AXO); } } }
    OB_ISSUE(0) OB_ISSUE(1)
#pragma unroll
    for (int r = 0; r < 6; ++r) {
        if (r + 1 < 6) WAITVN(5) else WAITV0();
        RAWBAR()
        OB_ISSUE(r + 2)
        const char* sb = smem + (r % 3) * 49152;
#pragma unroll
        for (int i = 0; i < 5; ++i) {
            const int jt = w * 5 + i, pp = l & 3;
            const unsigned cb = (unsigned)((jt >> 1) * 32 + (pp & 1) * 16 + 4 * (2 * (jt & 1) + (pp >> 1))) * 2;
            wo[i][r] = tr_frag<80, false>(sb, 0, cb, l);
        }
    }
    RAWBAR()
    if (w == 0) *(unsigned*)(smem + 24576 + (l & 3) * 4) = 0u;
    SU(3)
    const unsigned ns = start_wait(p, smem, w, nc);
    RAWRD()
    const unsigned lo = lane_id() * 16;
    const u32x4 lc = {lo, lo < 64 ? lo : 0xC0000000u, FL_V + h * 512u + min(lo >> 7, 2u) * 16384 + min(lo & 112, 80u) * 4, h == 7 ? lo : 0xC0000000u};
    const u32x8 sc = {UNI(w), UNI(OFF_BT + (unsigned)(w * 5) * 65536u + (h >> 3) * 16384 + (h & 7) * 1024), UNI((hot_page(ns, 2) ^ RFX) + HB_FB + (w >> 2) * 16384 + (w & 3) * 2048 + h * 64), UNI(w > 4 ? OFF_QK + blk(h * 12 + (w - 5) * 4) : 0x80000000u), UNI(lb + (w > 4 ? (w - 5) * 4096 : 32768)),
                      UNI(ns ^ (h < BNH ? 0 : 0x2000)), UNI(ns ^ QSX ^ 0x2000), UNI(263500000 + (576 + h * 8 + w) * 4)};
    bf16x16 t[15];
#pragma unroll
    for (int k = 0; k < 15; ++k) t[k] = cat16(wo[k / 3][(k % 3) * 2], wo[k / 3][(k % 3) * 2 + 1]);
    asm volatile("s_nop 4\n" OB_ASM :: "{v[0:31]}"(cat4(t[0], t[1], t[2], t[3])), "{v[32:63]}"(cat4(t[4], t[5], t[6], t[7])), "{v[64:95]}"(cat4(t[8], t[9], t[10], t[11])),
                 "{v[96:103]}"(t[12]), "{v[104:111]}"(t[13]), "{v[112:119]}"(t[14]), "{v[120:123]}"(lc), "{s[16:23]}"(sc), "{s[8:11]}"(rd), [brn] "n"(BRN), [rmask] "n"(RMASK), [hs] "n"(HSEG), [hn] "n"(GH / 2) : ACLOB);
}

__device__ __forceinline__ void r_role(const Params& p, char* smem, const int w, const int c, const int r, const unsigned ns) {
    const rsrc_t rs = __builtin_amdgcn_make_buffer_rsrc((void*)wsp(p), 0, 0x7fffffff, 0x00020000);
    const unsigned hu = hot_page(ns, c) ^ RFX, hm = hot_page(ns, 2), hp = hm ^ RFX;
    RAWRD()
    const unsigned lo = lane_id() * 16;
    const unsigned mb = hm + HB_MEM + (unsigned)(c * 8 + (r >> 4)) * 16384u + (r & 15) * 512;
    if (w == 0) {
        const int l = lo >> 4;
        const unsigned* src = (const unsigned*)(p.mem0 + (((l >> 1) & 6) + (l & 1)) * 640 + r * 32 + ((l >> 4) & 1) * 8 + (l & 2));
        st128(rs, lo < 512 ? lo : 0xC0000000u, mb, (u32x4){nop(src[0]), nop(src[8]), nop(src[2]), nop(src[10])});
    }
    SU(3)
    const unsigned pn = PL == 4 || (PL == 9 && r >= 14) ? 43 : PL == 7 ? 52 : PL == 9 ? 19 : 13, pl = ((PL == 4 ? r / 3 : PL == 7 ? r >> 2 : PL == 9 && r >= 14 ? r - 14 : r) * 2 + (w >> 2)) * pn + (lo >> 4);
    const unsigned fw = (lo & 64) ? (2 * r + 1) / 5 : (2 * r) / 5;
    const unsigned xl = (unsigned)(w & 1) * 20480u + r * 1024 + lo;
    const u32x16 lc = {lo, mb + (lo & 0x1f0) + (lo >> 9) * 8,
                       lo < 128 ? hp + HB_FB + c * 32768u + (fw >> 2) * 16384 + (fw & 3) * 2048 + w * 256 + (lo & 48) * 4 : 0xC0000000u,
                       lo < 128 ? hp + HB_TS + (unsigned)c * 32768u + w * 512 + (lo & 64) * 64 + (lo & 48) * 8 : 0xC0000000u,
                       (lo & 0x20) ? 0xC0000000u : ((lo >> 1) & 0x1e0) + (lo & 0x10) + 2048,
                       w * 1024 + (lo & 0x80) * 4 + (lo >> 8) * 64 + (lo & 0x20) + ((lo & 0x40) >> 3) + ((lo & 0x10) >> 2),
                       (w >> 1) == 1 ? xl : 0xC0000000u, 8192 + (w & 1) * 2048 + (lo >> 8) * 512 + (lo & 0xf0), 8192 + w * 4096 + lo,
                       w == 5 ? (lo < 512 ? mb + lo : 0xC0000000u) : w >= 6 ? xl + OFF_X1 + (c * 64 + PDIST) * 40960u : 0xC0000000u,
                       0x4444 + ((lo >> 5) & 1) * 0xAAAA, lo || w != 1 ? 0xC0000000u : r * 4u, r == 5 ? lo : 0xC0000000u,
                       pl < 512 && lo < pn * 16 ? hp + HB_PT + (c * 32 + (pl >> 6)) * 16384 + (pl & 63) * 128 : 0xC0000000u, lo < 128 ? lo >> 2 : 0xC0000000u, 0u};
    const int dru = DRU - DRL * ((0x102ac >> (4 * (r % 5))) & 15);
    const u32x16 sc = {UNI(w), UNI(hp + HB_PM + (unsigned)(c * 16 + (w >> 1)) * 16384u + (w & 1) * 2560), UNI(hu + (unsigned)(2 * r) * 16384u + w * 1024),
                       UNI(OFF_BT + (unsigned)(c * BRN) * 2621440u + r * 131072u + (w >> 1) * 16384 + (w & 1) * 4096), UNI(hp + HB_PT + (unsigned)(c * 32 + w) * 16384u),
                       UNI(c * 64 * 40960), UNI(dru), UNI(w == 5 ? 3 : 63), UNI(w == 5 ? 32768 : 40960), UNI(c * 64), UNI(r * 8 + w), UNI(ns ^ (w < BNH / 4 ? 0 : 0x2000)), UNI(hp + HB_TS + c * 32768 + w * 512), 0u, 0u, 0u};
    asm volatile("s_nop 4\n" R_ASM :: "{v[0:15]}"(lc), "{s[16:31]}"(sc), "{s[8:11]}"(rd), [brn] "n"(BRN), [dr] "n"(DR), [dh] "n"(DH), [x0] "n"(OFF_X0), [x1] "n"(OFF_X1), [pdist] "n"(PDIST) : ACLOB);
}

#undef RB
#define RB 5
__device__ __forceinline__ void up_role(const Params& p, char* smem, const int w, const int j) {
    const int l = lane_id(), tid = w * 64 + l;
    const rsrc_t rs = __builtin_amdgcn_make_buffer_rsrc((void*)wsp(p), 0, 0x7fffffff, 0x00020000);
    const unsigned lo_ = l * 16;
    const int fg = j >> 1, mt = j & 1;
    bf16x8 wt[2][20];
    {
        auto src = [&](int rnd) { return [&p, fg, rnd](unsigned r, unsigned c) { return (c < 16 ? p.wg : p.wu) + (size_t)(rnd * 64 + r) * 1536 + fg * 128 + (c & 15) * 8; }; };
        const unsigned cb = (unsigned)((w >> 1) * 32 + (l & 1) * 16 + 4 * (2 * (w & 1) + ((l & 3) >> 1))) * 2;
#pragma unroll
        for (int r = 0; r < 3; ++r) stage_issue<32, 64>(smem + r * 32768, w, l, src(r));
#pragma unroll
        for (int r = 0; r < 10; ++r) {
            if (r < 8) WAITVN(8) else if (r == 8) WAITVN(4) else WAITV0();
            RAWBAR()
            if (r + 3 < 10) stage_issue<32, 64>(smem + ((r + 3) & 3) * 32768, w, l, src(r + 3));
#pragma unroll
            for (int kk = 0; kk < 2; ++kk)
#pragma unroll
                for (int i = 0; i < 2; ++i) wt[i][r * 2 + kk] = tr_frag<32, false>(smem + (r & 3) * 32768, kk * 32, cb + i * 256, l);
        }
        RAWBAR()
    }
    f32x4 cs0 = Z4, cs1 = Z4;
#pragma unroll
    for (int ks = 0; ks < 20; ++ks) { cs0 = MFMA(wt[0][ks], ONES8, cs0); cs1 = MFMA(wt[1][ks], ONES8, cs1); }
    SU(3)
    RAWRD()
    const unsigned lo = lane_id() * 16;
    const u32x8 lc = {lo, lo + w * 1024, j == 5 ? lo : 0xC0000000u, (lo >> 8) == ((lo >> 6) & 3) ? 40960 + w * 128 + ((lo >> 1) & 0x78) : 43008 + (lo >> 1), 40960 + (lo & 0x300) + ((lo >> 1) & 0x78),
                      OFF_HG + (unsigned)(mt * 48 + fg * 4 + (w >> 1)) * 1024u + (w & 1) * 512 + ((lo & 512) >> 1) + (lo & 0xf0) + ((lo >> 5) & 8), lo || w != 7 ? 0xC0000000u : j * 4u, 0u};
    const u32x4 sc = {UNI(w), UNI(OFF_X1 + mt * 20480 + w * 1024), UNI(FL_T + 128), 0u};
    bf16x16 t[20];
#pragma unroll
    for (int k = 0; k < 20; ++k) t[k] = cat16(wt[k / 10][(k % 10) * 2], wt[k / 10][(k % 10) * 2 + 1]);
    asm volatile("s_nop 7\n" UP_ASM :: "{v[0:31]}"(cat4(t[0], t[1], t[2], t[3])), "{v[32:63]}"(cat4(t[4], t[5], t[6], t[7])), "{v[64:95]}"(cat4(t[8], t[9], t[10], t[11])),
                 "{v[96:127]}"(cat4(t[12], t[13], t[14], t[15])), "{v[128:159]}"(cat4(t[16], t[17], t[18], t[19])), "{v[160:163]}"(cs0), "{v[164:167]}"(cs1), "{v[168:175]}"(lc),
                 "{s[16:19]}"(sc), "{s[8:11]}"(rd), [dx] "n"(DX), [dlast] "n"(DLAST), [eup] "n"(EUP), [pdist] "n"(PDIST) : ACLOB);
}

#undef RB
#define RB 6
__device__ __forceinline__ void dn_role(const Params& p, char* smem, const int w, const int j) {
    const int l = lane_id(), tid = w * 64 + l;
    const rsrc_t rs = __builtin_amdgcn_make_buffer_rsrc((void*)wsp(p), 0, 0x7fffffff, 0x00020000);
    const int cg = j >> 1, mt = j & 1, c0 = cg * 96;
    bf16x8 wt[6][6];
    {
        auto src = [&](int rnd) { return [&p, c0, rnd](unsigned r, unsigned c) { return p.wd + (size_t)((r >> 5) * 192 + rnd * 32 + (r & 31)) * 640 + min(c0 + (int)c * 8, 632); }; };
        stage_issue<16, 256>(smem, w, l, src(0));
#pragma unroll
        for (int r = 0; r < 6; ++r) {
            if (r + 1 < 6) { stage_issue<16, 256>(smem + ((r + 1) & 1) * 65536, w, l, src(r + 1)); WAITVN(8) } else WAITV0();
            RAWBAR()
#pragma unroll
            for (int i = 0; i < 6; ++i) {
                const int pp = l & 3;
                const unsigned cb = (unsigned)((i >> 1) * 32 + (pp & 1) * 16 + 4 * (2 * (i & 1) + (pp >> 1))) * 2;
                wt[i][r] = tr_frag<16, false>(smem + (r & 1) * 65536, w * 32, cb, l);
            }
            RAWBAR()
        }
    }
    SU(3)
    RAWRD()
    const u32x4 ro = (u32x4){UNI((size_t)p.out), UNI(((size_t)p.out >> 32) & 0xffffu), 2048u * 640 * 2, 0x00020000u};
    const unsigned lo = lane_id() * 16;
    const int pc = cg * 3 + (w >> 1);
    const bool fv = w < 6 && pc < 20;
    const u32x8 lc = {lo, w * 6144 + lo, w * 1024 + lo, fv ? mt * 20480 + pc * 1024 + (w & 1) * 512 + ((lo & 512) >> 1) + (lo & 0xf0) + ((lo >> 5) & 8) : 0xC0000000u,
                      fv ? (unsigned)(mt * 16 + ((lo >> 4) & 15)) * 1280u + pc * 64 + ((lo >> 3) & 32) + (w & 1) * 16 + ((lo >> 6) & 8) : 0xC0000000u,
                      lo || w ? 0xC0000000u : j * 4u, j == 5 ? lo : 0xC0000000u, 0u};
    const u32x4 sc = {UNI(w), UNI(OFF_HG + (unsigned)(mt * 48 + w * 6) * 1024u), UNI(FL_T + 256), 0u};
    bf16x16 t[18];
#pragma unroll
    for (int k = 0; k < 18; ++k) t[k] = cat16(wt[k / 3][(k % 3) * 2], wt[k / 3][(k % 3) * 2 + 1]);
    asm volatile("s_nop 4\n" DN_ASM :: "{v[0:31]}"(cat4(t[0], t[1], t[2], t[3])), "{v[32:63]}"(cat4(t[4], t[5], t[6], t[7])), "{v[64:95]}"(cat4(t[8], t[9], t[10], t[11])),
                 "{v[96:127]}"(cat4(t[12], t[13], t[14], t[15])), "{v[128:135]}"(t[16]), "{v[136:143]}"(t[17]), "{v[144:151]}"(lc), "{s[16:19]}"(sc), "{s[8:11]}"(rd), "{s[12:15]}"(ro),
                 [dxd] "n"(DXD), [dlast] "n"(DLAST), [eup] "n"(EUP), [pdist] "n"(PDIST), [x1] "n"(OFF_X1), [x2] "n"(OFF_X2) : ACLOB);
}

__device__ __forceinline__ void ln_role(const Params& p, char* smem, const int w, const int mt) {
    RAWRD()
    const unsigned lo = lane_id() * 16;
    const u32x8 lc = {lo, w < 4 ? lo : 0xC0000000u, (lo >> 8) == ((lo >> 6) & 3) ? w * 128 + ((lo >> 1) & 0x78) : 2048 + (lo >> 1), (lo & 0x300) + ((lo >> 1) & 0x78),
                      lo < 64 ? lo : 0xC0000000u, lo || w ? 0xC0000000u : mt * 4u, 0u, 0u};
    const u32x8 sc = {UNI(w), UNI(OFF_X2 + mt * 20480 + w * 1024), UNI(OFF_XN + 64 * 40960 + mt * 20480 + w * 1024), UNI(FL_N + mt * 512 + w * 64), UNI(FL_T + 384), 0u, 0u, 0u};
    asm volatile("s_nop 4\n" LN_ASM :: "{v[0:7]}"(lc), "{s[16:23]}"(sc), "{s[8:11]}"(rd), [dxl] "n"(DXL), [dlast] "n"(DLAST) : ACLOB);
}

#define MEGA_LDS 163840
__device__ __forceinline__ int role_of(const int b) {
    const int x = b & 7, r = b >> 3, i = r * 4 + (x & 3);
    if (x >= 4) return i < 32 ? 2048 + i : i < 56 ? 1280 + i - 32 : i < 104 ? 768 + i - 32 : i < 118 ? 1536 + i - 104 : i < 120 ? 1792 + i - 118 : 768 + i - 48;
    if (x == 0) return r;
    const int j = r * 3 + x - 1;
    return j < 40 ? 256 + (j & 1) * 20 + (j >> 1) : j < 72 ? 512 + j - 40 : 768 + j - 72;
}
__device__ __forceinline__ int init_of(const int ra) {
    const int role = ra >> 8, a = ra & 255;
    return role == 1 ? a : role == 5 ? 40 + a : role == 6 ? 64 + a : role == 3 && a >= 72 ? 6 + a : -1;
}
__global__ __launch_bounds__(512, 2) void mega_kernel(Params p) {
    extern __shared__ __attribute__((aligned(16))) char smem[];
    const int b = blockIdx.x, w = __builtin_amdgcn_readfirstlane(threadIdx.x >> 6);
    const unsigned long long t64 = __builtin_amdgcn_s_memrealtime();
    const u32x2 nc = {(unsigned)t64, (unsigned)(t64 >> 32)};
    if (threadIdx.x == 0) __builtin_amdgcn_raw_buffer_store_b64(nc, __builtin_amdgcn_make_buffer_rsrc((void*)wsp(p), 0, 0x7fffffff, 0x00020000), 0, HS_N + b * 8, AUX_SC1);
    const int ra = role_of(b), role = ra >> 8, a = ra & 255, ik = init_of(ra);
    SU(2)
    unsigned ns = 0u;
    if (ik >= 0 || role == 7) {
        unsigned dv = 1u;
        if (ik >= 0) init_work(p, smem, w, ik);
        else if (a) { if (w == 0) dv = 2u | calibrate(p); }
        else if (w == 1) start_master(p, [](int q) { const int z = role_of(q); return z == 1793 ? 2 : init_of(z) >= 0 ? 1 : 0; });
        SU(0)
        ns = start_wait(p, smem, w, nc, dv);
        SU(1)
        if (role != 1 && role != 7) wait_until(nc[0] + (role == 3 ? GHD : GLD));
    }
    if (ik < 0 && role != 7) wait_until(nc[0] + (role == 0 ? SDQ : role == 2 ? SDU : role == 8 ? SDO : a < 24 ? SDA : a < 48 ? SDB : SDV));
    if (role == 3 || (role == 1 && a >= 20)) {
        const bool hp = ik >= 0;
        if (role == 1) wait_until(nc[0] + RHD);
        int j = role == 1 ? a + 4 : hp ? 143 - a : a;
#pragma unroll 1
        do { g_role(p, smem, w, j, hp ? 2 * HSEG : 0, hp ? 2 * HSEG + GH : 128, hp ? 128 : 0, nc, ns); j -= 8; } while (hp && j >= 44);
        if (role == 3) return;
    }
    if (role == 0) qkm_role(p, smem, w, a, nc);
    else if (role == 1) r_role(p, smem, w, a >= 20, a >= 20 ? a - 20 : a, ns);
    else if (role == 2) ua_role(p, smem, w, a, nc);
    else if (role == 8) ob_role(p, smem, w, a, nc);
    else if (role == 5) up_role(p, smem, w, a);
    else if (role == 6) dn_role(p, smem, w, a);
    else ln_role(p, smem, w, a);
}

extern "C" void kernel_launch(void* const* d_in, const int* in_sizes, int n_in,
                              void* d_out, int out_size, void* d_ws, size_t ws_size,
                              hipStream_t stream) {
    Params p;
    p.x = (const u16*)d_in[0]; p.mem0 = (const u16*)d_in[1];
    p.Wq = (const u16*)d_in[2]; p.Wk = (const u16*)d_in[3]; p.Wv = (const u16*)d_in[4]; p.Wo = (const u16*)d_in[5];
    p.wg = (const u16*)d_in[8]; p.wu = (const u16*)d_in[9]; p.wd = (const u16*)d_in[10];
    p.ws = (char*)d_ws; p.out = (u16*)d_out;
    (void)hipFuncSetAttribute((const void*)mega_kernel, hipFuncAttributeMaxDynamicSharedMemorySize, MEGA_LDS);
    mega_kernel<<<256, 512, MEGA_LDS, stream>>>(p);
}
```

```cpp
#include <hip/hip_runtime.h>
#include <stdint.h>

typedef __attribute__((ext_vector_type(8))) short bf16x8;
typedef __attribute__((ext_vector_type(16))) short bf16x16;
typedef __attribute__((ext_vector_type(4))) float f32x4;
typedef __attribute__((ext_vector_type(2))) float f32x2;
typedef __attribute__((ext_vector_type(4))) unsigned u32x4;
typedef __attribute__((ext_vector_type(2))) unsigned u32x2;
typedef __attribute__((ext_vector_type(2))) __bf16 bf16v2;
typedef unsigned short u16;
typedef __attribute__((address_space(3))) void* lds_ptr_t;
typedef __attribute__((address_space(1))) const void* gptr_t;
typedef __amdgpu_buffer_rsrc_t rsrc_t;

#define NSEG 64
#define LAG 8
#define EUP (LAG + 2)
#define VAH 5
#define QAH 4
#define VHD 20
#define OBD (LAG + 1)
#define HM 0
#define HELPS 0
#define BRN 16
#define C1D 600
#define GLD 0
#define DW 200
#define UPK 8
#define DRL 1
#define DX 95
#define UDW 0
#define USL 3
#define DXD 325
#define DXL 510
#define DCK 250
#define DQ 218
#define DV 218
#define DR 118
#define DRU 108
#define BE 2
#define NCAL 6
#define PFL 0
#define R0N 40
static_assert(LAG <= 13, "rings too small");
#define QS0 4
#define QS1 4
#define WFS 2
#define WAUX 2
#define DQ3 27
#define PL 1
#define GH 20
#define GHD 1200
#define DLAST 230
#define GSW 1
#define DH 95
#define BNH 32
#define RFX (PL == 7 ? 0x2000 : 0)
#define QSX 0
#define KSX 0x2000
#define SDQ 0
#define SDU 0
#define SDO 0
#define SDA 0
#define SDB 0
#define SDV 0
#define GAUX 0
#define PFD 0
#define AXQ 0
#define AXG 0
#define AXV 0
#define AXO 2
#define AXUO 2
#define AXUV 0
#define UVF 4
#define GHN 6
#define HSEG 28
#define RHD 1200
#define QH 10
#define QSEG 54
#define VSEG 23
#define VH 20
#define HS0(m) (2 * ((m) == 0 ? QSEG : (m) == 1 ? HSEG : VSEG))
#define HS1(m) (HS0(m) + ((m) == 0 ? QH : (m) == 1 ? GH : VH))
#define DEND 18
#define AUX_SC1 16
#define POISON 0xFFFFFFFFu
#define SPIN_CAP (1u << 17)

constexpr unsigned FL_T = 2048;
constexpr unsigned FL_V = 4096;
constexpr unsigned FL_N = FL_V + 3u * 16384;
constexpr unsigned CTRL_BYTES = FL_N + 66u * 1024;
constexpr unsigned HS_D = CTRL_BYTES, HS_N = HS_D + 1024, HS_G = HS_N + 3072;
constexpr unsigned OFF_X1 = HS_G + 4096;
constexpr unsigned OFF_X3 = OFF_X1 + 64u * 40960;
constexpr unsigned OFF_X0 = OFF_X3 + 64u * 40960;
constexpr unsigned OFF_X2 = OFF_X0 + 64u * 40960;
constexpr unsigned OFF_XN = OFF_X2 + 64u * 40960;
constexpr unsigned OFF_HG = OFF_XN + 128u * 40960;
constexpr unsigned OFF_HOT = (OFF_HG + 2u * 64 * 98304 + 0x1FFFFFu) & ~0x1FFFFFu;
constexpr unsigned OFF_BT = OFF_HOT + 3u * 2097152;
constexpr unsigned OFF_QK = OFF_BT + 2u * BRN * 2621440;
constexpr unsigned OFF_END = OFF_QK + (R0N + 16u) * 1572864;
static_assert(HSEG + GH / 2 <= R0N && R0N >= 32, "helped segments");
__host__ __device__ constexpr unsigned blk(unsigned piece) { return (piece >> 3) * 16384u + (piece & 7) * 1024u; }
constexpr unsigned HB_MEM = 0;
constexpr unsigned HB_PM = 16u * 16384;
constexpr unsigned HB_PT = 48u * 16384;
constexpr unsigned HB_TS = 112u * 16384;
constexpr unsigned HB_TU = 116u * 16384;
constexpr unsigned HB_END = 120u * 16384;
constexpr unsigned HB_FB = 120u * 16384;
constexpr unsigned HB_FK = 124u * 16384;
static_assert(OFF_END + 2097152u <= 262000000u, "workspace too large");

__device__ __forceinline__ unsigned pack2(float a, float b) {
    f32x2 v = {a, b};
    bf16v2 r = __builtin_convertvector(v, bf16v2);
    return __builtin_bit_cast(unsigned, r);
}
__device__ __forceinline__ float bflo(unsigned u) { return __uint_as_float(u << 16); }
__device__ __forceinline__ float bfhi(unsigned u) { return __uint_as_float(u & 0xFFFF0000u); }
__device__ __forceinline__ float bfr(float f) { return bflo(pack2(f, 0.f)); }
__device__ __forceinline__ unsigned nop(unsigned d) { return min(d, 0xFFFFFFFEu); }
__device__ __forceinline__ u32x4 ld128(rsrc_t rs, unsigned voff, unsigned soff) { return __builtin_amdgcn_raw_buffer_load_b128(rs, (int)voff, (int)soff, AUX_SC1); }
__device__ __forceinline__ unsigned ld32(rsrc_t rs, unsigned voff, unsigned soff) { return __builtin_amdgcn_raw_buffer_load_b32(rs, (int)voff, (int)soff, AUX_SC1); }
__device__ __forceinline__ void st128(rsrc_t rs, unsigned voff, unsigned soff, u32x4 v) { __builtin_amdgcn_raw_buffer_store_b128(v, rs, (int)(voff + soff), 0, AUX_SC1); }
__device__ __forceinline__ void st32(rsrc_t rs, unsigned voff, unsigned soff, unsigned v) { __builtin_amdgcn_raw_buffer_store_b32(v, rs, (int)voff, (int)soff, AUX_SC1); }
__device__ __forceinline__ bf16x8 asbf(u32x4 v) { return __builtin_bit_cast(bf16x8, v); }
__device__ __forceinline__ unsigned mx4(u32x4 v) { return max(max(v[0], v[1]), max(v[2], v[3])); }
__device__ __forceinline__ unsigned mn4(u32x4 v) { return min(min(v[0], v[1]), min(v[2], v[3])); }
__device__ __forceinline__ u32x4 poison4() { unsigned z = POISON; asm volatile("" : "+v"(z)); return (u32x4){z, z, z, z}; }
__device__ __forceinline__ u32x4 ones4() { unsigned z = 1u; asm volatile("" : "+v"(z)); return (u32x4){z, z, z, z}; }
#define MFMA(a, b, c) __builtin_amdgcn_mfma_f32_16x16x32_bf16((a), (b), (c), 0, 0, 0)
#define MFMAP(d, a, b) { d = MFMA(a, b, d); asm volatile("" : "+v"(d)); }
#define Z4 ((f32x4){0.f, 0.f, 0.f, 0.f})
#define RAWBAR() { asm volatile("s_waitcnt lgkmcnt(0)" ::: "memory"); __builtin_amdgcn_s_barrier(); asm volatile("" ::: "memory"); }
#define WAITV0() { __builtin_amdgcn_s_waitcnt(0x0F70); asm volatile("" ::: "memory"); }
#define WAITVN(n) { __builtin_amdgcn_s_waitcnt(0x0F70 | (n)); asm volatile("" ::: "memory"); }
#define WAITL0() asm volatile("s_waitcnt lgkmcnt(0)" ::: "memory")
#define SB0() __builtin_amdgcn_sched_barrier(0)
#define MEMB() asm volatile("" ::: "memory")
#define ROR8U(x) ((unsigned)__builtin_amdgcn_update_dpp(0, (int)(x), 0x128, 0xf, 0xf, false))
#define ROR8(x) __uint_as_float(ROR8U(__float_as_uint(x)))
#define QX1U(x) ((unsigned)__builtin_amdgcn_update_dpp(0, (int)(x), 0xB1, 0xf, 0xf, false))
#define QEV(x) ((unsigned)__builtin_amdgcn_update_dpp(0, (int)(x), 0xA0, 0xf, 0xf, false))
#define QOD(x) ((unsigned)__builtin_amdgcn_update_dpp(0, (int)(x), 0xF5, 0xf, 0xf, false))
#define DMA(dst, voff, soff) __builtin_amdgcn_raw_ptr_buffer_load_lds(rs, (lds_ptr_t)(dst), 16, (int)(voff), (int)(soff), 0, AUX_SC1)
#define DMAI(dst, voff, soff, imm) __builtin_amdgcn_raw_ptr_buffer_load_lds(rs, (lds_ptr_t)(dst), 16, (int)(voff), (int)(soff), imm, AUX_SC1)
#define OPAQUE_LO() unsigned lo; asm volatile("v_mbcnt_lo_u32_b32 %0, -1, 0\n\tv_mbcnt_hi_u32_b32 %0, -1, %0\n\tv_lshlrev_b32 %0, 4, %0" : "=v"(lo));
__device__ __forceinline__ int lane_id() { int l; asm volatile("v_mbcnt_lo_u32_b32 %0, -1, 0\n\tv_mbcnt_hi_u32_b32 %0, -1, %0" : "=v"(l)); return l; }
__device__ __forceinline__ unsigned now32() { return (unsigned)__builtin_amdgcn_s_memrealtime(); }
#define FTS(slot)
#define FLV ones4()
__device__ __forceinline__ void wait_until(unsigned t) { while ((int)(t - now32()) > 0) __builtin_amdgcn_s_sleep(1); }
__device__ __forceinline__ float xmax(float v) {
    float t;
    asm("v_mov_b32 %1, %0\n\ts_nop 1\n\tv_permlane16_swap_b32 %0, %1\n\tv_max_f32 %0, %0, %1\n\t"
        "v_mov_b32 %1, %0\n\ts_nop 1\n\tv_permlane32_swap_b32 %0, %1\n\tv_max_f32 %0, %0, %1" : "+v"(v), "=&v"(t));
    return v;
}
__device__ __forceinline__ float max3(float a, float b, float c) { return fmaxf(fmaxf(a, b), c); }
__device__ __forceinline__ float xsum(float v) {
    unsigned a = __float_as_uint(v);
    auto r = __builtin_amdgcn_permlane16_swap(a, a, false, false);
    v = __uint_as_float(r[0]) + __uint_as_float(r[1]);
    a = __float_as_uint(v);
    auto s = __builtin_amdgcn_permlane32_swap(a, a, false, false);
    return __uint_as_float(s[0]) + __uint_as_float(s[1]);
}

struct Params {
    const u16* x; const u16* mem0; const u16* Wq; const u16* Wk; const u16* Wv; const u16* Wo;
    const u16* wg; const u16* wu; const u16* wd;
    char* ws; u16* out;
};
__device__ __forceinline__ char* wsp(const Params& p) { return (char*)(((size_t)p.ws + 0x1FFFFF) & ~(size_t)0x1FFFFF); }

__device__ __forceinline__ bool spin_fail(const Params& p, unsigned& spins) {
    ++spins;
    if ((spins & 255u) == 0) {
        unsigned* a = (unsigned*)wsp(p);
        if (spins > SPIN_CAP) { __hip_atomic_store(a, 1u, __ATOMIC_RELAXED, __HIP_MEMORY_SCOPE_AGENT); return true; }
        if (__hip_atomic_load(a, __ATOMIC_RELAXED, __HIP_MEMORY_SCOPE_AGENT)) return true;
    }
    return false;
}
#define POLL(BODY) { unsigned sp_ = 0; bool bad; \
    do { int z_ = 0; do { MEMB(); BODY } while (bad && ++z_ < 16); } while (__builtin_expect(bad, 0) && !spin_fail16(p, sp_)); }
__device__ __forceinline__ bool spin_fail16(const Params& p, unsigned& spins) { spins += 15; return spin_fail(p, spins); }
__device__ __forceinline__ void wait_flags(const Params& p, rsrc_t rs, unsigned voff, unsigned soff, bool use) {
    unsigned spins = 0;
    for (;;) {
        const unsigned f = use ? ld32(rs, voff, soff) : 1u;
        if (__all(f != 0u)) break;
        MEMB();
        __builtin_amdgcn_s_sleep(WFS);
        if (spin_fail(p, spins)) break;
    }
    MEMB();
}
template <int N> __device__ __forceinline__ bool bad_any(const u32x4 (&v)[N]) {
    unsigned m = mx4(v[0]);
#pragma unroll
    for (int i = 1; i < N; ++i) m = max(m, mx4(v[i]));
    return __any(m == POISON);
}
template <int N>
__device__ __forceinline__ void regather(const Params& p, rsrc_t rs, unsigned lo, unsigned soff, u32x4 (&v)[N], bool slp) {
    unsigned spins = 0;
    do {
        if (slp) __builtin_amdgcn_s_sleep(4);
#pragma unroll
        for (int i = 0; i < N; ++i) if (mx4(v[i]) == POISON) v[i] = ld128(rs, lo + i * 1024, soff);
        WAITV0();
    } while (bad_any(v) && !spin_fail(p, spins));
}
template <int N>
__device__ __forceinline__ void gather(const Params& p, rsrc_t rs, unsigned lo, unsigned soff, u32x4 (&v)[N], bool slp) {
#pragma unroll
    for (int i = 0; i < N; ++i) v[i] = ld128(rs, lo + i * 1024, soff);
    if (__builtin_expect(bad_any(v), 0)) regather(p, rs, lo, soff, v, slp);
}

template <int CPR, int NROW, typename SrcF>
__device__ __forceinline__ void stage_issue(char* buf, int w, int l, SrcF src  ) {
    constexpr int PPW = CPR * NROW / 512;
    static_assert(CPR * NROW % 512 == 0 && CPR % 8 == 0, "chunk size");
#pragma unroll
    for (int i = 0; i < PPW; ++i) {
        const unsigned idx = (unsigned)((w * PPW + i) * 64 + l), r = idx / CPR, c = (idx % CPR) ^ ((r & 3) << 1);
        __builtin_amdgcn_global_load_lds((gptr_t)src(r, c), (lds_ptr_t)(buf + (w * PPW + i) * 1024), 16, 0, GAUX);
    }
}
template <int CPR, bool NAT>
__device__ __forceinline__ bf16x8 tr_frag(const char* buf, int r0, unsigned cb, int l) {
    const int g = l >> 4, q = (l & 15) >> 2;
    const unsigned ra = r0 + (NAT ? 8 * g : 4 * g) + q;
    const unsigned co = (((cb >> 4) ^ (q << 1)) << 4) + (cb & 15);
    const unsigned ad = (unsigned)(size_t)(lds_ptr_t)buf + ra * (CPR * 16) + co;
    u32x2 a, b;
    asm volatile("ds_read_b64_tr_b16 %0, %2\n\tds_read_b64_tr_b16 %1, %2 offset:%3\n\ts_waitcnt lgkmcnt(0)"
                 : "=&v"(a), "=&v"(b) : "v"(ad), "i"((NAT ? 4 : 16) * CPR * 16) : "memory");
    return asbf((u32x4){a[0], a[1], b[0], b[1]});
}
template <int CPR>
__device__ __forceinline__ void tr_frag6(bf16x8 (&f)[6], const char* buf, unsigned cb, int l) {
    const int g = l >> 4, q = (l & 15) >> 2;
    const unsigned ad = (unsigned)(size_t)(lds_ptr_t)buf + (8 * g + q) * (CPR * 16) + (((cb >> 4) ^ (q << 1)) << 4) + (cb & 15);
    u32x2 a[12];
    static_assert(CPR == 16, "offsets below");
    asm volatile("ds_read_b64_tr_b16 %0, %12\n\tds_read_b64_tr_b16 %1, %12 offset:1024\n\t"
                 "ds_read_b64_tr_b16 %2, %12 offset:8192\n\tds_read_b64_tr_b16 %3, %12 offset:9216\n\t"
                 "ds_read_b64_tr_b16 %4, %12 offset:16384\n\tds_read_b64_tr_b16 %5, %12 offset:17408\n\t"
                 "ds_read_b64_tr_b16 %6, %12 offset:24576\n\tds_read_b64_tr_b16 %7, %12 offset:25600\n\t"
                 "ds_read_b64_tr_b16 %8, %12 offset:32768\n\tds_read_b64_tr_b16 %9, %12 offset:33792\n\t"
                 "ds_read_b64_tr_b16 %10, %12 offset:40960\n\tds_read_b64_tr_b16 %11, %12 offset:41984\n\ts_waitcnt lgkmcnt(0)"
                 : "=&v"(a[0]), "=&v"(a[1]), "=&v"(a[2]), "=&v"(a[3]), "=&v"(a[4]), "=&v"(a[5]), "=&v"(a[6]), "=&v"(a[7]), "=&v"(a[8]), "=&v"(a[9]), "=&v"(a[10]), "=&v"(a[11])
                 : "v"(ad) : "memory");
#pragma unroll
    for (int i = 0; i < 6; ++i) f[i] = asbf((u32x4){a[2 * i][0], a[2 * i][1], a[2 * i + 1][0], a[2 * i + 1][1]});
}
#define ONES8 ((bf16x8){0x3f80, 0x3f80, 0x3f80, 0x3f80, 0x3f80, 0x3f80, 0x3f80, 0x3f80})

#define SMFMA(a, b, c, ix) __builtin_amdgcn_smfmac_f32_16x16x64_bf16((a), (b), (c), (ix), 0, 0)
__device__ __forceinline__ bf16x16 cat16(bf16x8 a, bf16x8 b) { return __builtin_shufflevector(a, b, 0, 1, 2, 3, 4, 5, 6, 7, 8, 9, 10, 11, 12, 13, 14, 15); }
__device__ __forceinline__ bf16x8 half16(bf16x16 a, int hi) {
    return hi ? __builtin_shufflevector(a, a, 8, 9, 10, 11, 12, 13, 14, 15) : __builtin_shufflevector(a, a, 0, 1, 2, 3, 4, 5, 6, 7);
}
__device__ __forceinline__ u32x2 fold2(f32x4 a, f32x4 b) {
    float s[4];
#pragma unroll
    for (int i = 0; i < 4; ++i) {
        auto r = __builtin_amdgcn_permlane32_swap(__float_as_uint(a[i]), __float_as_uint(b[i]), false, false);
        s[i] = __uint_as_float(r[0]) + __uint_as_float(r[1]);
    }
    return (u32x2){pack2(s[0], s[1]), pack2(s[2], s[3])};
}
typedef __attribute__((ext_vector_type(4))) short s16x4;
__device__ __forceinline__ unsigned troff(unsigned lo) { return ((lo >> 6) << 4) + ((lo >> 1) & 8); }
__device__ __forceinline__ bf16x8 trf(const char* base, int i) {
    typedef __attribute__((address_space(3))) s16x4* tp_t;
    const s16x4 a = __builtin_amdgcn_ds_read_tr16_b64_v4i16((tp_t)(base + i * 512)), b = __builtin_amdgcn_ds_read_tr16_b64_v4i16((tp_t)(base + i * 512 + 256));
    return __builtin_shufflevector(a, b, 0, 1, 2, 3, 4, 5, 6, 7);
}
#define LDS(T, a) (*(__attribute__((address_space(3))) T*)(unsigned)(a))
__device__ __forceinline__ bf16x8 trfa(unsigned a) {
    typedef __attribute__((address_space(3))) s16x4* tp_t;
    const s16x4 x = __builtin_amdgcn_ds_read_tr16_b64_v4i16((tp_t)a), y = __builtin_amdgcn_ds_read_tr16_b64_v4i16((tp_t)(a + 256));
    return __builtin_shufflevector(x, y, 0, 1, 2, 3, 4, 5, 6, 7);
}
#define LSPIN(off, val) { for (;;) { MEMB(); if (__builtin_amdgcn_readfirstlane(mn4(*(const u32x4*)(smem + (off)))) >= (unsigned)(val)) break; } MEMB(); }
#define LADD(off) { unsigned o_; \
    asm volatile("v_mov_b32 %0, 1\n\ts_mov_b64 exec, 1\n\tds_add_u32 %1, %0 offset:%2\n\ts_mov_b64 exec, -1" : "=&v"(o_) : "v"(lo), "i"(off) : "memory"); }
#define LADDV(off, sv) { unsigned o_; \
    asm volatile("v_mov_b32 %0, %3\n\ts_mov_b64 exec, 1\n\tds_add_u32 %1, %0 offset:%2\n\ts_mov_b64 exec, -1" : "=&v"(o_) : "v"(lo), "i"(off), "s"(sv) : "memory"); }
#define LWORD(off) (*(unsigned*)(smem + (off) + (lo >> 10)))
template <int IMM>
__device__ __forceinline__ void dma_raw(u32x4 rd, unsigned dst, unsigned voff, unsigned soff) {
    unsigned keep;
    asm volatile("s_mov_b32 %0, m0\n\ts_mov_b32 m0, %1\n\ts_nop 0\n\tbuffer_load_dwordx4 %2, %3, %4 offen offset:%5 sc1 lds\n\ts_mov_b32 m0, %0"
                 : "=&s"(keep) : "s"(__builtin_amdgcn_readfirstlane(dst)), "v"(voff), "s"(rd), "s"(__builtin_amdgcn_readfirstlane(soff)), "i"(IMM) : "memory");
}
#define UNI(x) ((unsigned)__builtin_amdgcn_readfirstlane((unsigned)(x)))
#define RAWRD() const u32x4 rd = (u32x4){UNI((size_t)wsp(p)), UNI(((size_t)wsp(p) >> 32) & 0xffffu), 0x7fffffffu, 0x00020000u}; \
    const unsigned lb = (unsigned)(size_t)(lds_ptr_t)smem;
#define LWAITX(off, val, slp) { unsigned a_, d_, s_, n_; \
    asm volatile("v_mov_b32 %0, %4\n\ts_mov_b32 %3, 0x400000\n1:\n\tds_read_b32 %1, %0\n\ts_waitcnt lgkmcnt(0)\n\tv_readfirstlane_b32 %2, %1\n\t" \
                 "s_cmp_ge_u32 %2, %5\n\ts_cbranch_scc1 2f\n\t" slp "s_sub_u32 %3, %3, 1\n\ts_cbranch_scc0 1b\n2:" \
                 : "=&v"(a_), "=&v"(d_), "=&s"(s_), "=&s"(n_) : "s"(__builtin_amdgcn_readfirstlane((unsigned)(off))), "s"(__builtin_amdgcn_readfirstlane((unsigned)(val))) : "memory", "scc"); }
#define LWAIT(off, val) LWAITX(off, val, "")
#define LWAITS(off, val) LWAITX(off, val, "s_sleep 1\n\t")
#define FLRD(off) { MEMB(); fl = LWORD(off); }
#define FLOK(val) (__builtin_amdgcn_readfirstlane(fl) >= (unsigned)(val))
#define LSET(off, val) { MEMB(); LWORD(off) = (unsigned)(val); MEMB(); }
__device__ __forceinline__ f32x2 ln_stat(f32x2 a) {
    const float mean = a[0] * (1.f / 640.f);
    const float var = bfr(fmaxf(__builtin_fmaf(-mean, mean, a[1] * (1.f / 640.f)), 0.f));
    const float den = bfr(__builtin_amdgcn_sqrtf(bfr(var + 1e-5f)));
    return (f32x2){bfr(mean), __builtin_amdgcn_rcpf(den)};
}
#define KSO(i) ((i) % 5 == 4 ? 16 + (i) / 5 : (i) - (i) / 5)
#define XPC(dst, src, gq, i) { if ((i) < 4) DMAI((dst) + (gq) * 5120, lo, (src) + (unsigned)(gq) * 4096u, ((i) & 3) * 1024); else DMA((dst) + (gq) * 5120 + 4096, lo, (src) + (unsigned)(16 + (gq)) * 1024u); }
#define XGRP(dst, src, gq) { XPC(dst, src, gq, 0) XPC(dst, src, gq, 1) XPC(dst, src, gq, 2) XPC(dst, src, gq, 3) XPC(dst, src, gq, 4) }
template <int I> struct IC { static constexpr int v = I; };
template <int I, int N, typename F> __device__ __forceinline__ void static_for(F f) { if constexpr (I < N) { f(IC<I>{}); static_for<I + 1, N>(f); } }
template <typename F, typename E>
__device__ __forceinline__ void proj2(const bf16x16 (&wt)[3][8], const char* xf, const char* xg, const char* wl, F side, E epi) {
    bf16x8 B[40], W[8][3];
    f32x4 a[2][3] = {{Z4, Z4, Z4}, {Z4, Z4, Z4}};
#pragma unroll
    for (int i = 0; i < 3; ++i) B[i] = *(const bf16x8*)(xf + i * 1024);
    static_for<0, 40>([&](auto ic) __attribute__((always_inline)) {
        constexpr int i = decltype(ic)::v, k = i % 20, tt = i / 20;
        side(ic);
        SB0();
#pragma unroll
        for (int j = 0; j < 3; ++j) {
            a[tt][j] = MFMA(i % 5 == 4 ? W[i / 5][j] : half16(wt[j][(KSO(k) & 15) >> 1], KSO(k) & 1), B[i], a[tt][j]);
            asm volatile("" : "+v"(a[tt][j]));
            SB0();
            if (j == 0) { if constexpr (i + 3 < 40) B[i + 3] = *(const bf16x8*)((i + 3 < 20 ? xf : xg) + (i + 3) * 1024); }
            if (j == 1) { if constexpr (i % 5 < 3 && i != 20) W[i / 5][i % 5] = *(const bf16x8*)(wl + ((k / 5) * 3 + i % 5) * 1024); }
            if (j == 2) { if constexpr (i == 21) W[4][0] = *(const bf16x8*)wl; }
            SB0();
        }
        if constexpr (i == 20) { epi(0, 0, a[0][0]); epi(0, 1, a[0][1]); }
        if constexpr (i == 21) epi(0, 2, a[0][2]);
        SB0();
    });
#pragma unroll
    for (int j = 0; j < 3; ++j) epi(1, j, a[1][j]);
}

template <typename F, typename E>
__device__ __forceinline__ void proj1(const bf16x16 (&wt)[3][8], const unsigned xf, const unsigned wl, F side, E epi) {
    bf16x8 B[20], W[4][3];
    f32x4 a[3] = {Z4, Z4, Z4};
#pragma unroll
    for (int i = 0; i < 3; ++i) B[i] = LDS(bf16x8, xf + i * 1024);
    static_for<0, 20>([&](auto ic) __attribute__((always_inline)) {
        constexpr int i = decltype(ic)::v;
        side(ic);
        SB0();
#pragma unroll
        for (int j = 0; j < 3; ++j) {
            a[j] = MFMA(i % 5 == 4 ? W[i / 5][j] : half16(wt[j][(KSO(i) & 15) >> 1], KSO(i) & 1), B[i], a[j]);
            asm volatile("" : "+v"(a[j]));
            SB0();
            if (j == 0) { if constexpr (i + 3 < 20) B[i + 3] = LDS(bf16x8, xf + (i + 3) * 1024); }
            if (j == 1) { if constexpr (i % 5 < 3) W[i / 5][i % 5] = LDS(bf16x8, wl + ((i / 5) * 3 + i % 5) * 1024); }
            SB0();
        }
    });
    epi((u32x4){pack2(a[0][0], a[0][1]), pack2(a[0][2], a[0][3]), pack2(a[1][0], a[1][1]), pack2(a[1][2], a[1][3])}, (u32x2){pack2(a[2][0], a[2][1]), pack2(a[2][2], a[2][3])});
}

template <typename F, typename E>
__device__ __forceinline__ void proj1s(const bf16x16 (&wt)[3][8], bf16x8 (&hd)[3], const unsigned xf, const unsigned& xn, const unsigned wl, F side, E epi) {
    bf16x8 B[23], W[4][3];
    f32x4 a[3];
#pragma unroll
    for (int i = 0; i < 3; ++i) B[i] = hd[i];
    static_for<0, 20>([&](auto ic) __attribute__((always_inline)) {
        constexpr int i = decltype(ic)::v;
        side(ic);
        SB0();
#pragma unroll
        for (int j = 0; j < 3; ++j) {
            a[j] = MFMA(i % 5 == 4 ? W[i / 5][j] : half16(wt[j][(KSO(i) & 15) >> 1], KSO(i) & 1), B[i], i ? a[j] : Z4);
            asm volatile("" : "+v"(a[j]));
            SB0();
            if (j == 0) B[i + 3] = LDS(bf16x8, i + 3 < 20 ? xf + (i + 3) * 1024 : xn + (i - 17) * 1024);
            if (j == 1) { if constexpr (i % 5 < 3) W[i / 5][i % 5] = LDS(bf16x8, wl + ((i / 5) * 3 + i % 5) * 1024); }
            SB0();
        }
    });
#pragma unroll
    for (int i = 0; i < 3; ++i) hd[i] = B[20 + i];
    epi((u32x4){pack2(a[0][0], a[0][1]), pack2(a[0][2], a[0][3]), pack2(a[1][0], a[1][1]), pack2(a[1][2], a[1][3])}, (u32x2){pack2(a[2][0], a[2][1]), pack2(a[2][2], a[2][3])});
}

template <int I0, typename S>
__device__ __forceinline__ void projh(const bf16x16 (&wt)[3][8], const unsigned xf, const unsigned wl, f32x4 (&a)[3], S sync) {
    bf16x8 B[20], W[4][3];
#pragma unroll
    for (int i = I0; i < I0 + 3; ++i) B[i] = LDS(bf16x8, xf + i * 1024);
    sync();
    static_for<I0, I0 + 10>([&](auto ic) __attribute__((always_inline)) {
        constexpr int i = decltype(ic)::v;
        SB0();
#pragma unroll
        for (int j = 0; j < 3; ++j) {
            a[j] = MFMA(i % 5 == 4 ? W[i / 5][j] : half16(wt[j][(KSO(i) & 15) >> 1], KSO(i) & 1), B[i], i ? a[j] : Z4);
            asm volatile("" : "+v"(a[j]));
            SB0();
            if (j == 0) { if constexpr (i + 3 < I0 + 10) B[i + 3] = LDS(bf16x8, xf + (i + 3) * 1024); }
            if (j == 1) { if constexpr (i % 5 < 3) W[i / 5][i % 5] = LDS(bf16x8, wl + ((i / 5) * 3 + i % 5) * 1024); }
            SB0();
        }
    });
}

#define PDIST 4
#define NI 86
__device__ __forceinline__ void fill16(rsrc_t rs, unsigned dst, unsigned bytes, unsigned gt, unsigned val) {
    if (gt >= NI * 512) gt -= NI * 512;
    for (unsigned i = gt; i < bytes / 16; i += NI * 512) st128(rs, dst + i * 16, 0, (u32x4){val, val, val, val});
}
#define RORF(x, n) __uint_as_float((unsigned)__builtin_amdgcn_update_dpp(0, (int)__float_as_uint(x), 0x120 + (n), 0xf, 0xf, false))
__device__ __forceinline__ float wsum(float v) { v += RORF(v, 8); v += RORF(v, 4); v += RORF(v, 2); v += RORF(v, 1); return xsum(v); }
__device__ __forceinline__ void init_work(const Params& p, char* smem, const int w, const int k) {
    const rsrc_t rs = __builtin_amdgcn_make_buffer_rsrc((void*)wsp(p), 0, 0x7fffffff, 0x00020000);
    const int l = lane_id(), tid = w * 64 + l;
    const unsigned gt = k * 512 + tid;
    unsigned xd[3][5];
#pragma unroll
    for (int j = 0; j < 3; ++j) {
        const unsigned* xr = (const unsigned*)(p.x + (size_t)(min(k + j * NI, 255) * 8 + w) * 640);
#pragma unroll
        for (int i = 0; i < 5; ++i) xd[j][i] = __builtin_nontemporal_load(xr + i * 64 + l);
    }
    fill16(rs, 0, CTRL_BYTES, gt, 0u);
    fill16(rs, OFF_X1, PDIST * 40960, gt + 30208, POISON);
    fill16(rs, OFF_X2, PDIST * 40960, gt + 19968, POISON);
    fill16(rs, OFF_X3, PDIST * 40960, gt + 9728, POISON);
    fill16(rs, OFF_HG, PDIST * 98304, gt, POISON);
    fill16(rs, OFF_HG + 64u * 98304, PDIST * 98304, gt + 19456, POISON);
    fill16(rs, OFF_HOT + 4194304 + HB_MEM, 16u * 16384, gt + 38912, POISON);
    fill16(rs, OFF_HOT + 4194304 + HB_TS, 8u * 16384, gt + 22528, POISON);
    fill16(rs, OFF_HOT + HB_FB, 7u * 16384, gt + 33280, 0u);
#pragma unroll
    for (int j = 0; j < 3; ++j) {
        float v[10], sm = 0.f;
#pragma unroll
        for (int i = 0; i < 5; ++i) { v[2 * i] = bflo(xd[j][i]); v[2 * i + 1] = bfhi(xd[j][i]); sm += v[2 * i] + v[2 * i + 1]; }
        sm = wsum(sm);
        const float mean = sm * (1.f / 640.f);
        float q = 0.f;
#pragma unroll
        for (int i = 0; i < 10; ++i) { const float d = v[i] - mean; q += d * d; }
        q = wsum(q);
        const float rd = __builtin_amdgcn_rcpf(bfr(__builtin_amdgcn_sqrtf(bfr(bfr(q * (1.f / 640.f)) + 1e-5f)))), mb = bfr(mean);
        unsigned* xn = (unsigned*)(smem + j * 20480 + w * 1280);
#pragma unroll
        for (int i = 0; i < 5; ++i) { xn[i * 64 + l] = pack2(bfr(v[2 * i] - mb) * rd, bfr(v[2 * i + 1] - mb) * rd); xn[2560 + i * 64 + l] = xd[j][i]; }
    }
    RAWBAR()
    for (int i = tid; i < 3840; i += 512) {
        const int j = i / 1280, b = k + j * NI, e = i - j * 1280, raw = e >= 640, m = e - raw * 640, r = m & 7, u = m >> 3;
        const char* src = smem + j * 20480 + raw * 10240 + r * 1280 + ((u >> 2) * 32 + (u & 3) * 4) * 2;
        const u32x2 y0 = *(const u32x2*)src, y1 = *(const u32x2*)(src + 32);
        st128(rs, b < 256 ? (raw ? OFF_X0 : OFF_XN) + (b >> 1) * 20480 + u * 256 + ((b & 1) * 8 + r) * 16 : 0xC0000000u, 0, (u32x4){y0[0], y0[1], y1[0], y1[1]});
    }
    WAITV0();
    RAWBAR()
}
#define HS_KEY 0x5A5A5A5Au
template <bool GO = true>
__device__ __forceinline__ unsigned start_wait(const Params& p, char* smem, const int w, const u32x2 n, const unsigned dv = 1u) {
    const rsrc_t rs = __builtin_amdgcn_make_buffer_rsrc((void*)wsp(p), 0, 0x7fffffff, 0x00020000);
    if (w == 0) {
        bool sent = false;
        unsigned par = 0u;
        for (unsigned sp = 0; sp < SPIN_CAP; ++sp) {
            MEMB();
            const u32x4 g = ld128(rs, 0, HS_G + blockIdx.x * 16);
            if (__all(g[0] == n[0] && g[1] == n[1])) {
                if (!sent) { if (lane_id() == 0) st32(rs, 0, HS_D + blockIdx.x * 4, dv); sent = true; }
                par = g[2] ^ n[0] ^ HS_KEY;
                if (!GO || __all(par < 2u && g[3] == ~n[1])) break;
            }
            __builtin_amdgcn_s_sleep(2);
        }
        if (GO) *(unsigned*)smem = par & 1u;
    }
    if (!GO) return 0u;
    RAWBAR()
    const unsigned par = __builtin_amdgcn_readfirstlane(*(const unsigned*)smem);
    RAWBAR()
    return par * 0x2000u;
}
template <typename F>
__device__ __forceinline__ void start_master(const Params& p, F needed  ) {
    const rsrc_t rs = __builtin_amdgcn_make_buffer_rsrc((void*)wsp(p), 0, 0x7fffffff, 0x00020000);
    const int l = lane_id();
    st128(rs, l * 16, HS_D, (u32x4){0u, 0u, 0u, 0u});
    WAITV0();
    u32x2 last[4] = {{0u, 0u}, {0u, 0u}, {0u, 0u}, {0u, 0u}};
    int go = 0;
    unsigned par = 0u;
    for (unsigned sp = 0; sp < SPIN_CAP; ++sp) {
        MEMB();
        unsigned d[4];
        u32x2 n[4];
#pragma unroll
        for (int i = 0; i < 4; ++i) d[i] = ld32(rs, l * 4 + i * 256, HS_D);
#pragma unroll
        for (int i = 0; i < 4; ++i) n[i] = __builtin_amdgcn_raw_buffer_load_b64(rs, l * 8 + i * 512, HS_N, AUX_SC1);
        bool ini = true, all = true, chg = sp == 0, odd = false;
#pragma unroll
        for (int i = 0; i < 4; ++i) {
            const int q = needed(l + i * 64);
            ini = ini && (!q || d[i]); all = all && d[i]; odd = odd || (q == 2 && (d[i] & 1u));
            chg = chg || n[i][0] != last[i][0] || n[i][1] != last[i][1];
            last[i] = n[i];
        }
        if (!go && __all(ini)) { go = 1; par = __any(odd) ? 1u : 0u; chg = true; }
        if (__any(chg)) {
#pragma unroll
            for (int i = 0; i < 4; ++i) st128(rs, l * 16 + i * 1024, HS_G, (u32x4){n[i][0], n[i][1], go ? n[i][0] ^ HS_KEY ^ par : ~n[i][0], go ? ~n[i][1] : n[i][1]});
        }
        if (__all(all)) break;
        if (go) __builtin_amdgcn_s_sleep(8); else __builtin_amdgcn_s_sleep(2);
    }
    WAITV0();
}

#define SU(k)
#define TDECL(c)
#define TDECLL(c, off)
#define STAMP(k)
#define STFLUSH(id, it)
#define DBGDECL(bit, c, off)

__device__ __forceinline__ unsigned hot_page(const unsigned ns, const int pg) { const unsigned a = OFF_HOT + pg * 2097152u; return a + (((a >> 8) ^ ns) & 0x2000u); }
__device__ __forceinline__ unsigned calibrate(const Params& p) {
    const int tid = lane_id();
    const rsrc_t rs = __builtin_amdgcn_make_buffer_rsrc((void*)wsp(p), 0, 0x7fffffff, 0x00020000);
    const unsigned base = OFF_HOT + 4194304 + HB_END;
#pragma unroll
    for (int i = 0; i < 16; ++i) st128(rs, tid * 16, base + i * 2048, (u32x4){0u, 0u, 0u, 0u});
    WAITV0();
    unsigned best[2] = {~0u, ~0u};
#pragma unroll 1
    for (int i = 0; i < 32 && (i < NCAL || abs((int)(best[0] - best[1])) < 150 || max(best[0], best[1]) > 1000u); ++i) {
#pragma unroll
        for (int k = 0; k < 2; ++k) {
            const unsigned t0 = (unsigned)__builtin_amdgcn_s_memtime();
            unsigned v = ld32(rs, 0, base + k * 8192 + (i & 3) * 2048 + ((i >> 2) & 1) * 16384 + (i >> 3) * 128);
            WAITV0();
            asm volatile("" : "+v"(v));
            best[k] = min(best[k], (unsigned)__builtin_amdgcn_s_memtime() - t0);
        }
    }
    return ((best[1] < best[0] ? 0u : 1u) ^ (base >> 21)) & 1u;
}

#define QKM_ASM \
    "s_mov_b32 s32, 0\n" \
    "s_mov_b32 s34, 0\n" \
    "s_mov_b32 s35, 1\n" \
    "s_mov_b32 s47, 0x40000\n" \
    "s_mov_b32 s45, 0\n" \
    "s_memrealtime s[50:51]\n" \
    "s_waitcnt lgkmcnt(0)\n" \
    "s_sub_u32 s48, s50, 1000000\n" \
    "s_mov_b32 s49, s48\n" \
    "s_cmp_gt_u32 s16, 3\n" \
    "s_cbranch_scc1 .Lqkm_w4_%=\n" \
    "s_cmp_eq_u32 s16, 3\n" \
    "s_cbranch_scc1 .Lqkm_w3_%=\n" \
    "s_cmp_eq_u32 s16, 2\n" \
    "s_cbranch_scc1 .Lqkm_w2_%=\n" \
    "s_cmp_eq_u32 s16, 1\n" \
    "s_cbranch_scc1 .Lqkm_w1_%=\n" \
    ".Lqkm_w0_%=:\n" \
    "s_mov_b32 s39, 139264\n" \
    "s_mov_b32 s52, 0xff00ff00\n" \
    "s_mov_b32 s53, 0xff00ff00\n" \
    ".Lqkm_l0_%=:\n" \
    "ds_read_b128 v[240:243], v192 offset:16384\n" \
    "ds_read_b128 v[244:247], v192 offset:19456\n" \
    "ds_read_b128 v[248:251], v192 offset:17408\n" \
    "ds_read_b128 v[252:255], v192 offset:20480\n" \
    "v_mov_b64 v[204:205], 0\n" \
    "v_mov_b64 v[206:207], 0\n" \
    "v_mov_b64 v[208:209], 0\n" \
    "v_mov_b64 v[210:211], 0\n" \
    "v_mov_b64 v[212:213], 0\n" \
    "v_mov_b64 v[214:215], 0\n" \
    "s_barrier\n" \
    "ds_read_b128 v[216:219], v192 offset:0\n" \
    "ds_read_b128 v[220:223], v192 offset:1024\n" \
    "ds_read_b128 v[232:235], v192 offset:8192\n" \
    "ds_read_b128 v[224:227], v192 offset:2048\n" \
    "s_xor_b32 s39, s39, 253952\n" \
    "v_add_u32 v200, s39, v192\n" \
    "s_mov_b32 s33, s34\n" \
    "s_add_u32 s37, s32, 1\n" \
    "s_sub_i32 s36, s37, %[lag]\n" \
    "s_bitcmp1_b32 s36, 0\n" \
    "s_cselect_b32 s34, 128, 2*%[lag]\n" \
    "s_add_i32 s34, s34, s36\n" \
    "s_lshr_b32 s34, s34, 1\n" \
    "s_cmp_gt_u32 s36, 127-2*%[lag]\n" \
    "s_cselect_b32 s34, s37, s34\n" \
    "s_lshr_b32 s36, s33, 6\n" \
    "s_lshl_b32 s36, s36, 2\n" \
    "s_and_b32 s37, s33, 3\n" \
    "s_or_b32 s36, s36, s37\n" \
    "s_waitcnt lgkmcnt(3)\n" \
    "v_smfmac_f32_16x16x64_bf16 v[204:207], v[216:219], v[0:7], v193\n" \
    "ds_read_b128 v[228:231], v192 offset:3072\n" \
    "v_smfmac_f32_16x16x64_bf16 v[208:211], v[216:219], v[64:71], v193\n" \
    "v_smfmac_f32_16x16x64_bf16 v[212:215], v[216:219], v[128:135], v193\n" \
    "s_waitcnt lgkmcnt(3)\n" \
    "v_smfmac_f32_16x16x64_bf16 v[204:207], v[220:223], v[8:15], v193\n" \
    "ds_read_b128 v[216:219], v192 offset:4096\n" \
    "v_smfmac_f32_16x16x64_bf16 v[208:211], v[220:223], v[72:79], v193\n" \
    "ds_read_b128 v[236:239], v192 offset:9216\n" \
    "v_smfmac_f32_16x16x64_bf16 v[212:215], v[220:223], v[136:143], v193\n" \
    "s_waitcnt lgkmcnt(4)\n" \
    "v_smfmac_f32_16x16x64_bf16 v[204:207], v[232:235], v[240:247], v193\n" \
    "s_waitcnt lgkmcnt(3)\n" \
    "v_smfmac_f32_16x16x64_bf16 v[204:207], v[224:227], v[16:23], v193\n" \
    "ds_read_b128 v[220:223], v192 offset:5120\n" \
    "v_smfmac_f32_16x16x64_bf16 v[208:211], v[224:227], v[80:87], v193\n" \
    "ds_read_b128 v[240:243], v192 offset:18432\n" \
    "v_smfmac_f32_16x16x64_bf16 v[212:215], v[224:227], v[144:151], v193\n" \
    "ds_read_b128 v[244:247], v192 offset:21504\n" \
    "v_smfmac_f32_16x16x64_bf16 v[208:211], v[232:235], v[248:255], v193\n" \
    "s_waitcnt lgkmcnt(5)\n" \
    "v_smfmac_f32_16x16x64_bf16 v[204:207], v[228:231], v[24:31], v193\n" \
    "ds_read_b128 v[224:227], v192 offset:6144\n" \
    "v_smfmac_f32_16x16x64_bf16 v[208:211], v[228:231], v[88:95], v193\n" \
    "ds_read_b128 v[248:251], v192 offset:22528\n" \
    "v_smfmac_f32_16x16x64_bf16 v[212:215], v[228:231], v[152:159], v193\n" \
    "ds_read_b128 v[252:255], v192 offset:25600\n" \
    "s_waitcnt lgkmcnt(7)\n" \
    "v_smfmac_f32_16x16x64_bf16 v[204:207], v[216:219], v[32:39], v193\n" \
    "ds_read_b128 v[228:231], v192 offset:7168\n" \
    "v_smfmac_f32_16x16x64_bf16 v[208:211], v[216:219], v[96:103], v193\n" \
    "v_smfmac_f32_16x16x64_bf16 v[212:215], v[216:219], v[160:167], v193\n" \
    "s_waitcnt lgkmcnt(4)\n" \
    "v_smfmac_f32_16x16x64_bf16 v[212:215], v[232:235], v[240:247], v193\n" \
    "s_waitcnt lgkmcnt(1)\n" \
    "v_smfmac_f32_16x16x64_bf16 v[204:207], v[236:239], v[248:255], v193\n" \
    "ds_read_b128 v[240:243], v192 offset:23552\n" \
    "v_smfmac_f32_16x16x64_bf16 v[204:207], v[220:223], v[40:47], v193\n" \
    "ds_read_b128 v[244:247], v192 offset:26624\n" \
    "v_smfmac_f32_16x16x64_bf16 v[208:211], v[220:223], v[104:111], v193\n" \
    "ds_read_b128 v[248:251], v192 offset:24576\n" \
    "v_smfmac_f32_16x16x64_bf16 v[212:215], v[220:223], v[168:175], v193\n" \
    "ds_read_b128 v[252:255], v192 offset:27648\n" \
    "v_smfmac_f32_16x16x64_bf16 v[204:207], v[224:227], v[48:55], v193\n" \
    "v_smfmac_f32_16x16x64_bf16 v[208:211], v[224:227], v[112:119], v193\n" \
    "v_smfmac_f32_16x16x64_bf16 v[212:215], v[224:227], v[176:183], v193\n" \
    "s_waitcnt lgkmcnt(4)\n" \
    "v_smfmac_f32_16x16x64_bf16 v[204:207], v[228:231], v[56:63], v193\n" \
    "s_waitcnt lgkmcnt(2)\n" \
    "v_smfmac_f32_16x16x64_bf16 v[208:211], v[236:239], v[240:247], v193\n" \
    "v_smfmac_f32_16x16x64_bf16 v[208:211], v[228:231], v[120:127], v193\n" \
    "s_waitcnt lgkmcnt(0)\n" \
    "v_smfmac_f32_16x16x64_bf16 v[212:215], v[236:239], v[248:255], v193\n" \
    "v_smfmac_f32_16x16x64_bf16 v[212:215], v[228:231], v[184:191], v193\n" \
    "ds_read_b128 v[216:219], v200 offset:13312\n" \
    "ds_read_b128 v[220:223], v200 offset:14336\n" \
    "ds_read_b128 v[224:227], v200 offset:15360\n" \
    "ds_read_b128 v[228:231], v200 offset:16384\n" \
    "s_lshl_b32 s36, s36, s18\n" \
    "s_add_u32 s38, s17, s36\n" \
    "s_add_u32 s32, s32, 1\n" \
    "v_pk_add_f32 v[204:205], v[204:205], v[206:207]\n" \
    "v_pk_add_f32 v[208:209], v[208:209], v[210:211]\n" \
    "v_pk_add_f32 v[212:213], v[212:213], v[214:215]\n" \
    "v_cvt_pk_bf16_f32 v204, v204, v205\n" \
    "v_cvt_pk_bf16_f32 v208, v208, v209\n" \
    "ds_write2st64_b32 v194, v204, v208 offset1:1\n" \
    "v_cvt_pk_bf16_f32 v212, v212, v213\n" \
    "ds_write_b32 v195, v212\n" \
    "ds_read_b128 v[212:215], v200 offset:12288\n" \
    "s_waitcnt lgkmcnt(1)\n" \
    "s_barrier\n" \
    "ds_read_b64_tr_b16 v[232:233], v196 offset:10240\n" \
    "ds_read_b64_tr_b16 v[234:235], v196 offset:10496\n" \
    "ds_read_b64_tr_b16 v[236:237], v196 offset:10752\n" \
    "ds_read_b64_tr_b16 v[238:239], v196 offset:11008\n" \
    "ds_read_b64_tr_b16 v[240:241], v196 offset:11264\n" \
    "ds_read_b64_tr_b16 v[242:243], v196 offset:11520\n" \
    "ds_read_b64_tr_b16 v[244:245], v196 offset:11776\n" \
    "ds_read_b64_tr_b16 v[246:247], v196 offset:12032\n" \
    "ds_read_b64_tr_b16 v[248:249], v196 offset:12288\n" \
    "ds_read_b64_tr_b16 v[250:251], v196 offset:12544\n" \
    "ds_read_b64_tr_b16 v[252:253], v196 offset:12800\n" \
    "ds_read_b64_tr_b16 v[254:255], v196 offset:13056\n" \
    "s_waitcnt lgkmcnt(10)\n" \
    "v_mfma_f32_16x16x32_bf16 v[204:207], v[212:215], v[232:235], 0\n" \
    "ds_read_b128 v[212:215], v200 offset:17408\n" \
    "s_waitcnt lgkmcnt(9)\n" \
    "v_mfma_f32_16x16x32_bf16 v[204:207], v[216:219], v[236:239], v[204:207]\n" \
    "ds_read_b128 v[216:219], v200 offset:18432\n" \
    "s_waitcnt lgkmcnt(8)\n" \
    "v_mfma_f32_16x16x32_bf16 v[204:207], v[220:223], v[240:243], v[204:207]\n" \
    "ds_read_b128 v[220:223], v200 offset:19456\n" \
    "s_waitcnt lgkmcnt(7)\n" \
    "v_mfma_f32_16x16x32_bf16 v[204:207], v[224:227], v[244:247], v[204:207]\n" \
    "ds_read_b128 v[224:227], v200 offset:20480\n" \
    "s_waitcnt lgkmcnt(6)\n" \
    "v_mfma_f32_16x16x32_bf16 v[204:207], v[228:231], v[248:251], v[204:207]\n" \
    "ds_read_b128 v[228:231], v200 offset:21504\n" \
    "s_waitcnt lgkmcnt(4)\n" \
    "v_mfma_f32_16x16x32_bf16 v[204:207], v[212:215], v[252:255], v[204:207]\n" \
    "ds_read_b128 v[212:215], v200 offset:22528\n" \
    "s_waitcnt lgkmcnt(4)\n" \
    "v_mfma_f32_16x16x32_bf16 v[208:211], v[216:219], v[232:235], 0\n" \
    "ds_read_b128 v[216:219], v200 offset:23552\n" \
    "s_waitcnt lgkmcnt(4)\n" \
    "v_mfma_f32_16x16x32_bf16 v[208:211], v[220:223], v[236:239], v[208:211]\n" \
    "ds_read_b64_tr_b16 v[220:221], v196 offset:13312\n" \
    "ds_read_b64_tr_b16 v[222:223], v196 offset:13568\n" \
    "s_waitcnt lgkmcnt(5)\n" \
    "v_mfma_f32_16x16x32_bf16 v[208:211], v[224:227], v[240:243], v[208:211]\n" \
    "ds_read_b64_tr_b16 v[224:225], v196 offset:13824\n" \
    "ds_read_b64_tr_b16 v[226:227], v196 offset:14080\n" \
    "s_waitcnt lgkmcnt(6)\n" \
    "v_mfma_f32_16x16x32_bf16 v[208:211], v[228:231], v[244:247], v[208:211]\n" \
    "ds_read_b64_tr_b16 v[228:229], v196 offset:14336\n" \
    "ds_read_b64_tr_b16 v[230:231], v196 offset:14592\n" \
    "s_waitcnt lgkmcnt(7)\n" \
    "v_mfma_f32_16x16x32_bf16 v[208:211], v[212:215], v[248:251], v[208:211]\n" \
    "ds_read_b64_tr_b16 v[212:213], v196 offset:14848\n" \
    "ds_read_b64_tr_b16 v[214:215], v196 offset:15104\n" \
    "s_waitcnt lgkmcnt(8)\n" \
    "v_mfma_f32_16x16x32_bf16 v[208:211], v[216:219], v[252:255], v[208:211]\n" \
    "ds_read_b64_tr_b16 v[216:217], v196 offset:15360\n" \
    "ds_read_b64_tr_b16 v[218:219], v196 offset:15616\n" \
    "s_waitcnt lgkmcnt(8)\n" \
    "v_mfma_f32_16x16x32_bf16 v[232:235], v[220:223], v[232:235], 0\n" \
    "ds_read_b64_tr_b16 v[220:221], v196 offset:15872\n" \
    "ds_read_b64_tr_b16 v[222:223], v196 offset:16128\n" \
    "s_nop 1\n" \
    "s_waitcnt lgkmcnt(8)\n" \
    "v_mfma_f32_16x16x32_bf16 v[232:235], v[224:227], v[236:239], v[232:235]\n" \
    "s_nop 1\n" \
    "s_waitcnt lgkmcnt(6)\n" \
    "v_mfma_f32_16x16x32_bf16 v[232:235], v[228:231], v[240:243], v[232:235]\n" \
    "v_cndmask_b32 v208, v204, v208, s[52:53]\n" \
    "v_cndmask_b32 v209, v205, v209, s[52:53]\n" \
    "v_cndmask_b32 v210, v206, v210, s[52:53]\n" \
    "s_waitcnt lgkmcnt(4)\n" \
    "v_mfma_f32_16x16x32_bf16 v[232:235], v[212:215], v[244:247], v[232:235]\n" \
    "v_cndmask_b32 v211, v207, v211, s[52:53]\n" \
    "v_max3_f32 v200, v208, v209, v210\n" \
    "v_max_f32 v200, v200, v211\n" \
    "s_waitcnt lgkmcnt(2)\n" \
    "v_mfma_f32_16x16x32_bf16 v[232:235], v[216:219], v[248:251], v[232:235]\n" \
    "v_max_f32_dpp v201, v200, v200 row_ror:8 row_mask:0xf bank_mask:0xf bound_ctrl:1\n" \
    "v_max_f32_dpp v203, v200, v200 row_ror:8 row_mask:0xf bank_mask:0xf bound_ctrl:1\n" \
    "s_nop 1\n" \
    "v_permlane16_swap_b32 v201, v203\n" \
    "s_waitcnt lgkmcnt(0)\n" \
    "v_mfma_f32_16x16x32_bf16 v[232:235], v[220:223], v[252:255], v[232:235]\n" \
    "v_max_f32 v200, v201, v203\n" \
    "v_max_f32 v201, v201, v203\n" \
    "s_nop 1\n" \
    "v_permlane32_swap_b32 v201, v200\n" \
    "v_max_f32 v201, v201, v200\n" \
    "s_nop 1\n" \
    "v_max3_f32 v236, v232, v233, v201\n" \
    "v_max3_f32 v237, v236, v234, v235\n" \
    "v_max3_f32 v236, v236, v234, v235\n" \
    "s_nop 1\n" \
    "v_permlane16_swap_b32 v236, v237\n" \
    "v_max_f32 v236, v236, v237\n" \
    "v_mul_f32 v236, 0xbdd53b94, v236\n" \
    "v_fmamk_f32 v208, v208, 0x3dd53b94, v236\n" \
    "v_fmamk_f32 v209, v209, 0x3dd53b94, v236\n" \
    "v_fmamk_f32 v210, v210, 0x3dd53b94, v236\n" \
    "v_fmamk_f32 v211, v211, 0x3dd53b94, v236\n" \
    "v_fmamk_f32 v232, v232, 0x3dd53b94, v236\n" \
    "v_fmamk_f32 v233, v233, 0x3dd53b94, v236\n" \
    "v_fmamk_f32 v234, v234, 0x3dd53b94, v236\n" \
    "v_fmamk_f32 v235, v235, 0x3dd53b94, v236\n" \
    "v_exp_f32 v208, v208\n" \
    "v_exp_f32 v209, v209\n" \
    "v_exp_f32 v210, v210\n" \
    "v_exp_f32 v211, v211\n" \
    "v_exp_f32 v232, v232\n" \
    "v_add_f32 v200, v208, v209\n" \
    "v_add_f32 v203, v210, v211\n" \
    "v_exp_f32 v233, v233\n" \
    "v_add_f32 v200, v200, v203\n" \
    "v_exp_f32 v234, v234\n" \
    "v_exp_f32 v235, v235\n" \
    "v_add_f32_dpp v202, v200, v200 row_ror:8 row_mask:0xf bank_mask:0xf bound_ctrl:1\n" \
    "v_add_f32_dpp v203, v200, v200 row_ror:8 row_mask:0xf bank_mask:0xf bound_ctrl:1\n" \
    "v_add_f32 v236, v232, v233\n" \
    "v_add_f32 v237, v234, v235\n" \
    "v_permlane32_swap_b32 v202, v203\n" \
    "v_add_f32 v236, v236, v202\n" \
    "v_add_f32 v237, v237, v203\n" \
    "v_add_f32 v238, v236, v237\n" \
    "v_add_f32 v236, v236, v237\n" \
    "s_nop 1\n" \
    "v_permlane16_swap_b32 v236, v238\n" \
    "v_add_f32 v236, v236, v238\n" \
    "v_rcp_f32 v236, v236\n" \
    "s_nop 0\n" \
    "v_pk_mul_f32 v[208:209], v[208:209], v[236:237] op_sel_hi:[1,0]\n" \
    "v_pk_mul_f32 v[210:211], v[210:211], v[236:237] op_sel_hi:[1,0]\n" \
    "v_pk_mul_f32 v[232:233], v[232:233], v[236:237] op_sel_hi:[1,0]\n" \
    "v_pk_mul_f32 v[234:235], v[234:235], v[236:237] op_sel_hi:[1,0]\n" \
    "v_cvt_pk_bf16_f32 v216, v208, v209\n" \
    "v_cvt_pk_bf16_f32 v217, v210, v211\n" \
    "buffer_store_dwordx2 v[216:217], v197, s[8:11], s38 offen sc1\n" \
    "v_cvt_pk_bf16_f32 v220, v232, v233\n" \
    "v_cvt_pk_bf16_f32 v221, v234, v235\n" \
    "buffer_store_dwordx2 v[220:221], v198, s[8:11], s38 offen sc1\n" \
    "s_cmp_lt_u32 s32, 128\n" \
    "s_cbranch_scc1 .Lqkm_l0_%=\n" \
    "s_branch .Lqkm_end_%=\n" \
    ".Lqkm_w1_%=:\n" \
    "s_mov_b32 s39, 139264\n" \
    "v_bfe_u32 v201, v192, 4, 4\n" \
    "v_lshrrev_b32 v202, 6, v192\n" \
    "v_and_b32 v202, 12, v202\n" \
    "v_sub_u32 v201, v201, v202\n" \
    "v_cmp_gt_i32 s[54:55], 0, v201\n" \
    "v_cmp_gt_i32 s[56:57], 1, v201\n" \
    "v_cmp_gt_i32 s[58:59], 2, v201\n" \
    "v_cmp_gt_i32 s[60:61], 3, v201\n" \
    ".Lqkm_l1_%=:\n" \
    "ds_read_b128 v[240:243], v192 offset:28672\n" \
    "ds_read_b128 v[244:247], v192 offset:31744\n" \
    "ds_read_b128 v[248:251], v192 offset:29696\n" \
    "ds_read_b128 v[252:255], v192 offset:32768\n" \
    "v_mov_b64 v[204:205], 0\n" \
    "v_mov_b64 v[206:207], 0\n" \
    "v_mov_b64 v[208:209], 0\n" \
    "v_mov_b64 v[210:211], 0\n" \
    "v_mov_b64 v[212:213], 0\n" \
    "v_mov_b64 v[214:215], 0\n" \
    "s_barrier\n" \
    "ds_read_b128 v[216:219], v192 offset:0\n" \
    "ds_read_b128 v[220:223], v192 offset:1024\n" \
    "ds_read_b128 v[232:235], v192 offset:8192\n" \
    "ds_read_b128 v[224:227], v192 offset:2048\n" \
    "s_xor_b32 s39, s39, 253952\n" \
    "v_add_u32 v200, s39, v192\n" \
    "v_mov_b32 v203, 0xf149f2ca\n" \
    "s_mov_b32 s33, s34\n" \
    "s_add_u32 s37, s32, 1\n" \
    "s_sub_i32 s36, s37, %[lag]\n" \
    "s_bitcmp1_b32 s36, 0\n" \
    "s_cselect_b32 s34, 128, 2*%[lag]\n" \
    "s_add_i32 s34, s34, s36\n" \
    "s_lshr_b32 s34, s34, 1\n" \
    "s_cmp_gt_u32 s36, 127-2*%[lag]\n" \
    "s_cselect_b32 s34, s37, s34\n" \
    "s_lshr_b32 s36, s33, 6\n" \
    "s_lshl_b32 s36, s36, 2\n" \
    "s_and_b32 s37, s33, 3\n" \
    "s_or_b32 s36, s36, s37\n" \
    "s_waitcnt lgkmcnt(3)\n" \
    "v_smfmac_f32_16x16x64_bf16 v[204:207], v[216:219], v[0:7], v193\n" \
    "ds_read_b128 v[228:231], v192 offset:3072\n" \
    "v_smfmac_f32_16x16x64_bf16 v[208:211], v[216:219], v[64:71], v193\n" \
    "v_smfmac_f32_16x16x64_bf16 v[212:215], v[216:219], v[128:135], v193\n" \
    "s_waitcnt lgkmcnt(3)\n" \
    "v_smfmac_f32_16x16x64_bf16 v[204:207], v[220:223], v[8:15], v193\n" \
    "ds_read_b128 v[216:219], v192 offset:4096\n" \
    "v_smfmac_f32_16x16x64_bf16 v[208:211], v[220:223], v[72:79], v193\n" \
    "ds_read_b128 v[236:239], v192 offset:9216\n" \
    "v_smfmac_f32_16x16x64_bf16 v[212:215], v[220:223], v[136:143], v193\n" \
    "s_waitcnt lgkmcnt(4)\n" \
    "v_smfmac_f32_16x16x64_bf16 v[204:207], v[232:235], v[240:247], v193\n" \
    "s_waitcnt lgkmcnt(3)\n" \
    "v_smfmac_f32_16x16x64_bf16 v[204:207], v[224:227], v[16:23], v193\n" \
    "ds_read_b128 v[220:223], v192 offset:5120\n" \
    "v_smfmac_f32_16x16x64_bf16 v[208:211], v[224:227], v[80:87], v193\n" \
    "ds_read_b128 v[240:243], v192 offset:30720\n" \
    "v_smfmac_f32_16x16x64_bf16 v[212:215], v[224:227], v[144:151], v193\n" \
    "ds_read_b128 v[244:247], v192 offset:33792\n" \
    "v_smfmac_f32_16x16x64_bf16 v[208:211], v[232:235], v[248:255], v193\n" \
    "s_waitcnt lgkmcnt(5)\n" \
    "v_smfmac_f32_16x16x64_bf16 v[204:207], v[228:231], v[24:31], v193\n" \
    "ds_read_b128 v[224:227], v192 offset:6144\n" \
    "v_smfmac_f32_16x16x64_bf16 v[208:211], v[228:231], v[88:95], v193\n" \
    "ds_read_b128 v[248:251], v192 offset:34816\n" \
    "v_smfmac_f32_16x16x64_bf16 v[212:215], v[228:231], v[152:159], v193\n" \
    "ds_read_b128 v[252:255], v192 offset:37888\n" \
    "s_waitcnt lgkmcnt(7)\n" \
    "v_smfmac_f32_16x16x64_bf16 v[204:207], v[216:219], v[32:39], v193\n" \
    "ds_read_b128 v[228:231], v192 offset:7168\n" \
    "v_smfmac_f32_16x16x64_bf16 v[208:211], v[216:219], v[96:103], v193\n" \
    "v_smfmac_f32_16x16x64_bf16 v[212:215], v[216:219], v[160:167], v193\n" \
    "s_waitcnt lgkmcnt(4)\n" \
    "v_smfmac_f32_16x16x64_bf16 v[212:215], v[232:235], v[240:247], v193\n" \
    "s_waitcnt lgkmcnt(1)\n" \
    "v_smfmac_f32_16x16x64_bf16 v[204:207], v[236:239], v[248:255], v193\n" \
    "ds_read_b128 v[240:243], v192 offset:35840\n" \
    "v_smfmac_f32_16x16x64_bf16 v[204:207], v[220:223], v[40:47], v193\n" \
    "ds_read_b128 v[244:247], v192 offset:38912\n" \
    "v_smfmac_f32_16x16x64_bf16 v[208:211], v[220:223], v[104:111], v193\n" \
    "ds_read_b128 v[248:251], v192 offset:36864\n" \
    "v_smfmac_f32_16x16x64_bf16 v[212:215], v[220:223], v[168:175], v193\n" \
    "ds_read_b128 v[252:255], v192 offset:39936\n" \
    "v_smfmac_f32_16x16x64_bf16 v[204:207], v[224:227], v[48:55], v193\n" \
    "v_smfmac_f32_16x16x64_bf16 v[208:211], v[224:227], v[112:119], v193\n" \
    "v_smfmac_f32_16x16x64_bf16 v[212:215], v[224:227], v[176:183], v193\n" \
    "s_waitcnt lgkmcnt(4)\n" \
    "v_smfmac_f32_16x16x64_bf16 v[204:207], v[228:231], v[56:63], v193\n" \
    "s_waitcnt lgkmcnt(2)\n" \
    "v_smfmac_f32_16x16x64_bf16 v[208:211], v[236:239], v[240:247], v193\n" \
    "v_smfmac_f32_16x16x64_bf16 v[208:211], v[228:231], v[120:127], v193\n" \
    "s_waitcnt lgkmcnt(0)\n" \
    "v_smfmac_f32_16x16x64_bf16 v[212:215], v[236:239], v[248:255], v193\n" \
    "v_smfmac_f32_16x16x64_bf16 v[212:215], v[228:231], v[184:191], v193\n" \
    "ds_read_b128 v[216:219], v200 offset:13312\n" \
    "ds_read_b128 v[220:223], v200 offset:14336\n" \
    "ds_read_b128 v[224:227], v200 offset:15360\n" \
    "ds_read_b128 v[228:231], v200 offset:16384\n" \
    "ds_read_b128 v[232:235], v200 offset:0\n" \
    "ds_read_b128 v[236:239], v200 offset:1024\n" \
    "ds_read_b128 v[240:243], v200 offset:2048\n" \
    "ds_read_b128 v[244:247], v200 offset:3072\n" \
    "ds_read_b128 v[248:251], v200 offset:4096\n" \
    "ds_read_b128 v[252:255], v200 offset:5120\n" \
    "s_lshl_b32 s36, s36, s18\n" \
    "s_add_u32 s38, s17, s36\n" \
    "s_add_u32 s32, s32, 1\n" \
    "v_pk_add_f32 v[204:205], v[204:205], v[206:207]\n" \
    "v_pk_add_f32 v[208:209], v[208:209], v[210:211]\n" \
    "v_pk_add_f32 v[212:213], v[212:213], v[214:215]\n" \
    "v_cvt_pk_bf16_f32 v204, v204, v205\n" \
    "v_cvt_pk_bf16_f32 v208, v208, v209\n" \
    "ds_write2st64_b32 v194, v204, v208 offset1:1\n" \
    "v_cvt_pk_bf16_f32 v212, v212, v213\n" \
    "ds_write_b32 v195, v212\n" \
    "ds_read_b128 v[212:215], v200 offset:12288\n" \
    "s_waitcnt lgkmcnt(1)\n" \
    "s_barrier\n" \
    "v_cndmask_b32 v208, 0, v203, s[54:55]\n" \
    "v_cndmask_b32 v209, 0, v203, s[56:57]\n" \
    "v_cndmask_b32 v210, 0, v203, s[58:59]\n" \
    "v_cndmask_b32 v211, 0, v203, s[60:61]\n" \
    "s_waitcnt lgkmcnt(0)\n" \
    "s_nop 0\n" \
    "v_mfma_f32_16x16x32_bf16 v[208:211], v[212:215], v[232:235], v[208:211]\n" \
    "ds_read_b128 v[212:215], v200 offset:17408\n" \
    "v_mfma_f32_16x16x32_bf16 v[208:211], v[216:219], v[236:239], v[208:211]\n" \
    "ds_read_b64_tr_b16 v[216:217], v196 offset:13312\n" \
    "ds_read_b64_tr_b16 v[218:219], v196 offset:13568\n" \
    "v_mfma_f32_16x16x32_bf16 v[208:211], v[220:223], v[240:243], v[208:211]\n" \
    "ds_read_b64_tr_b16 v[220:221], v196 offset:13824\n" \
    "ds_read_b64_tr_b16 v[222:223], v196 offset:14080\n" \
    "v_mfma_f32_16x16x32_bf16 v[208:211], v[224:227], v[244:247], v[208:211]\n" \
    "ds_read_b64_tr_b16 v[224:225], v196 offset:14336\n" \
    "ds_read_b64_tr_b16 v[226:227], v196 offset:14592\n" \
    "v_mfma_f32_16x16x32_bf16 v[208:211], v[228:231], v[248:251], v[208:211]\n" \
    "ds_read_b64_tr_b16 v[228:229], v196 offset:14848\n" \
    "ds_read_b64_tr_b16 v[230:231], v196 offset:15104\n" \
    "s_waitcnt lgkmcnt(8)\n" \
    "v_mfma_f32_16x16x32_bf16 v[208:211], v[212:215], v[252:255], v[208:211]\n" \
    "ds_read_b64_tr_b16 v[212:213], v196 offset:15360\n" \
    "ds_read_b64_tr_b16 v[214:215], v196 offset:15616\n" \
    "s_waitcnt lgkmcnt(8)\n" \
    "v_mfma_f32_16x16x32_bf16 v[204:207], v[216:219], v[232:235], 0\n" \
    "ds_read_b64_tr_b16 v[216:217], v196 offset:15872\n" \
    "ds_read_b64_tr_b16 v[218:219], v196 offset:16128\n" \
    "s_nop 1\n" \
    "s_waitcnt lgkmcnt(8)\n" \
    "v_mfma_f32_16x16x32_bf16 v[204:207], v[220:223], v[236:239], v[204:207]\n" \
    "s_nop 1\n" \
    "s_waitcnt lgkmcnt(6)\n" \
    "v_mfma_f32_16x16x32_bf16 v[204:207], v[224:227], v[240:243], v[204:207]\n" \
    "v_max3_f32 v200, v208, v209, v210\n" \
    "v_max_f32 v201, v200, v211\n" \
    "v_max_f32 v203, v200, v211\n" \
    "s_waitcnt lgkmcnt(4)\n" \
    "v_mfma_f32_16x16x32_bf16 v[204:207], v[228:231], v[244:247], v[204:207]\n" \
    "v_permlane16_swap_b32 v201, v203\n" \
    "v_max_f32 v200, v201, v203\n" \
    "v_max_f32 v201, v201, v203\n" \
    "s_waitcnt lgkmcnt(2)\n" \
    "v_mfma_f32_16x16x32_bf16 v[204:207], v[212:215], v[248:251], v[204:207]\n" \
    "v_permlane32_swap_b32 v201, v200\n" \
    "v_max_f32 v201, v201, v200\n" \
    "s_waitcnt lgkmcnt(0)\n" \
    "v_mfma_f32_16x16x32_bf16 v[204:207], v[216:219], v[252:255], v[204:207]\n" \
    "s_nop 7\n" \
    "v_max3_f32 v236, v204, v205, v201\n" \
    "v_max3_f32 v237, v236, v206, v207\n" \
    "v_max3_f32 v236, v236, v206, v207\n" \
    "s_nop 1\n" \
    "v_permlane16_swap_b32 v236, v237\n" \
    "v_max_f32 v236, v236, v237\n" \
    "v_mul_f32 v236, 0xbdd53b94, v236\n" \
    "v_fmamk_f32 v208, v208, 0x3dd53b94, v236\n" \
    "v_fmamk_f32 v209, v209, 0x3dd53b94, v236\n" \
    "v_fmamk_f32 v210, v210, 0x3dd53b94, v236\n" \
    "v_fmamk_f32 v211, v211, 0x3dd53b94, v236\n" \
    "v_fmamk_f32 v204, v204, 0x3dd53b94, v236\n" \
    "v_fmamk_f32 v205, v205, 0x3dd53b94, v236\n" \
    "v_fmamk_f32 v206, v206, 0x3dd53b94, v236\n" \
    "v_fmamk_f32 v207, v207, 0x3dd53b94, v236\n" \
    "v_exp_f32 v208, v208\n" \
    "v_exp_f32 v209, v209\n" \
    "v_exp_f32 v210, v210\n" \
    "v_exp_f32 v211, v211\n" \
    "v_exp_f32 v204, v204\n" \
    "v_add_f32 v200, v208, v209\n" \
    "v_add_f32 v203, v210, v211\n" \
    "v_exp_f32 v205, v205\n" \
    "v_add_f32 v202, v200, v203\n" \
    "v_add_f32 v203, v200, v203\n" \
    "v_exp_f32 v206, v206\n" \
    "v_exp_f32 v207, v207\n" \
    "v_add_f32 v236, v204, v205\n" \
    "v_add_f32 v237, v206, v207\n" \
    "v_permlane32_swap_b32 v202, v203\n" \
    "v_add_f32 v236, v236, v202\n" \
    "v_add_f32 v237, v237, v203\n" \
    "v_add_f32 v238, v236, v237\n" \
    "v_add_f32 v236, v236, v237\n" \
    "s_nop 1\n" \
    "v_permlane16_swap_b32 v236, v238\n" \
    "v_add_f32 v236, v236, v238\n" \
    "v_rcp_f32 v236, v236\n" \
    "s_nop 0\n" \
    "v_pk_mul_f32 v[208:209], v[208:209], v[236:237] op_sel_hi:[1,0]\n" \
    "v_pk_mul_f32 v[210:211], v[210:211], v[236:237] op_sel_hi:[1,0]\n" \
    "v_pk_mul_f32 v[204:205], v[204:205], v[236:237] op_sel_hi:[1,0]\n" \
    "v_pk_mul_f32 v[206:207], v[206:207], v[236:237] op_sel_hi:[1,0]\n" \
    "v_cvt_pk_bf16_f32 v216, v208, v209\n" \
    "v_cvt_pk_bf16_f32 v217, v210, v211\n" \
    "buffer_store_dwordx2 v[216:217], v197, s[8:11], s38 offen sc1\n" \
    "v_cvt_pk_bf16_f32 v220, v204, v205\n" \
    "v_cvt_pk_bf16_f32 v221, v206, v207\n" \
    "buffer_store_dwordx2 v[220:221], v198, s[8:11], s38 offen sc1\n" \
    "s_cmp_lt_u32 s32, 128\n" \
    "s_cbranch_scc1 .Lqkm_l1_%=\n" \
    "s_branch .Lqkm_end_%=\n" \
    ".Lqkm_w2_%=:\n" \
    "s_mov_b32 s39, 139264\n" \
    "v_bfe_u32 v201, v192, 4, 4\n" \
    "v_lshrrev_b32 v202, 6, v192\n" \
    "v_and_b32 v202, 12, v202\n" \
    "v_sub_u32 v201, v201, v202\n" \
    "v_cmp_gt_i32 s[54:55], 0, v201\n" \
    "v_cmp_gt_i32 s[56:57], 1, v201\n" \
    "v_cmp_gt_i32 s[58:59], 2, v201\n" \
    "v_cmp_gt_i32 s[60:61], 3, v201\n" \
    ".Lqkm_l2_%=:\n" \
    "ds_read_b128 v[240:243], v192 offset:40960\n" \
    "ds_read_b128 v[244:247], v192 offset:44032\n" \
    "ds_read_b128 v[248:251], v192 offset:41984\n" \
    "ds_read_b128 v[252:255], v192 offset:45056\n" \
    "v_mov_b64 v[204:205], 0\n" \
    "v_mov_b64 v[206:207], 0\n" \
    "v_mov_b64 v[208:209], 0\n" \
    "v_mov_b64 v[210:211], 0\n" \
    "v_mov_b64 v[212:213], 0\n" \
    "v_mov_b64 v[214:215], 0\n" \
    "s_barrier\n" \
    "ds_read_b128 v[216:219], v192 offset:0\n" \
    "ds_read_b128 v[220:223], v192 offset:1024\n" \
    "ds_read_b128 v[232:235], v192 offset:8192\n" \
    "ds_read_b128 v[224:227], v192 offset:2048\n" \
    "s_xor_b32 s39, s39, 253952\n" \
    "v_add_u32 v200, s39, v192\n" \
    "v_mov_b32 v203, 0xf149f2ca\n" \
    "s_mov_b32 s33, s34\n" \
    "s_add_u32 s37, s32, 1\n" \
    "s_sub_i32 s36, s37, %[lag]\n" \
    "s_bitcmp1_b32 s36, 0\n" \
    "s_cselect_b32 s34, 128, 2*%[lag]\n" \
    "s_add_i32 s34, s34, s36\n" \
    "s_lshr_b32 s34, s34, 1\n" \
    "s_cmp_gt_u32 s36, 127-2*%[lag]\n" \
    "s_cselect_b32 s34, s37, s34\n" \
    "s_lshr_b32 s36, s33, 6\n" \
    "s_lshl_b32 s36, s36, 2\n" \
    "s_and_b32 s37, s33, 3\n" \
    "s_or_b32 s36, s36, s37\n" \
    "s_waitcnt lgkmcnt(3)\n" \
    "v_smfmac_f32_16x16x64_bf16 v[204:207], v[216:219], v[0:7], v193\n" \
    "ds_read_b128 v[228:231], v192 offset:3072\n" \
    "v_smfmac_f32_16x16x64_bf16 v[208:211], v[216:219], v[64:71], v193\n" \
    "v_smfmac_f32_16x16x64_bf16 v[212:215], v[216:219], v[128:135], v193\n" \
    "s_waitcnt lgkmcnt(3)\n" \
    "v_smfmac_f32_16x16x64_bf16 v[204:207], v[220:223], v[8:15], v193\n" \
    "ds_read_b128 v[216:219], v192 offset:4096\n" \
    "v_smfmac_f32_16x16x64_bf16 v[208:211], v[220:223], v[72:79], v193\n" \
    "ds_read_b128 v[236:239], v192 offset:9216\n" \
    "v_smfmac_f32_16x16x64_bf16 v[212:215], v[220:223], v[136:143], v193\n" \
    "s_waitcnt lgkmcnt(4)\n" \
    "v_smfmac_f32_16x16x64_bf16 v[204:207], v[232:235], v[240:247], v193\n" \
    "s_waitcnt lgkmcnt(3)\n" \
    "v_smfmac_f32_16x16x64_bf16 v[204:207], v[224:227], v[16:23], v193\n" \
    "ds_read_b128 v[220:223], v192 offset:5120\n" \
    "v_smfmac_f32_16x16x64_bf16 v[208:211], v[224:227], v[80:87], v193\n" \
    "ds_read_b128 v[240:243], v192 offset:43008\n" \
    "v_smfmac_f32_16x16x64_bf16 v[212:215], v[224:227], v[144:151], v193\n" \
    "ds_read_b128 v[244:247], v192 offset:46080\n" \
    "v_smfmac_f32_16x16x64_bf16 v[208:211], v[232:235], v[248:255], v193\n" \
    "s_waitcnt lgkmcnt(5)\n" \
    "v_smfmac_f32_16x16x64_bf16 v[204:207], v[228:231], v[24:31], v193\n" \
    "ds_read_b128 v[224:227], v192 offset:6144\n" \
    "v_smfmac_f32_16x16x64_bf16 v[208:211], v[228:231], v[88:95], v193\n" \
    "ds_read_b128 v[248:251], v192 offset:47104\n" \
    "v_smfmac_f32_16x16x64_bf16 v[212:215], v[228:231], v[152:159], v193\n" \
    "ds_read_b128 v[252:255], v192 offset:50176\n" \
    "s_waitcnt lgkmcnt(7)\n" \
    "v_smfmac_f32_16x16x64_bf16 v[204:207], v[216:219], v[32:39], v193\n" \
    "ds_read_b128 v[228:231], v192 offset:7168\n" \
    "v_smfmac_f32_16x16x64_bf16 v[208:211], v[216:219], v[96:103], v193\n" \
    "v_smfmac_f32_16x16x64_bf16 v[212:215], v[216:219], v[160:167], v193\n" \
    "s_waitcnt lgkmcnt(4)\n" \
    "v_smfmac_f32_16x16x64_bf16 v[212:215], v[232:235], v[240:247], v193\n" \
    "s_waitcnt lgkmcnt(1)\n" \
    "v_smfmac_f32_16x16x64_bf16 v[204:207], v[236:239], v[248:255], v193\n" \
    "ds_read_b128 v[240:243], v192 offset:48128\n" \
    "v_smfmac_f32_16x16x64_bf16 v[204:207], v[220:223], v[40:47], v193\n" \
    "ds_read_b128 v[244:247], v192 offset:51200\n" \
    "v_smfmac_f32_16x16x64_bf16 v[208:211], v[220:223], v[104:111], v193\n" \
    "ds_read_b128 v[248:251], v192 offset:49152\n" \
    "v_smfmac_f32_16x16x64_bf16 v[212:215], v[220:223], v[168:175], v193\n" \
    "ds_read_b128 v[252:255], v192 offset:52224\n" \
    "v_smfmac_f32_16x16x64_bf16 v[204:207], v[224:227], v[48:55], v193\n" \
    "v_smfmac_f32_16x16x64_bf16 v[208:211], v[224:227], v[112:119], v193\n" \
    "v_smfmac_f32_16x16x64_bf16 v[212:215], v[224:227], v[176:183], v193\n" \
    "s_waitcnt lgkmcnt(4)\n" \
    "v_smfmac_f32_16x16x64_bf16 v[204:207], v[228:231], v[56:63], v193\n" \
    "s_waitcnt lgkmcnt(2)\n" \
    "v_smfmac_f32_16x16x64_bf16 v[208:211], v[236:239], v[240:247], v193\n" \
    "v_smfmac_f32_16x16x64_bf16 v[208:211], v[228:231], v[120:127], v193\n" \
    "s_waitcnt lgkmcnt(0)\n" \
    "v_smfmac_f32_16x16x64_bf16 v[212:215], v[236:239], v[248:255], v193\n" \
    "v_smfmac_f32_16x16x64_bf16 v[212:215], v[228:231], v[184:191], v193\n" \
    "ds_read_b128 v[216:219], v200 offset:13312\n" \
    "ds_read_b128 v[220:223], v200 offset:14336\n" \
    "ds_read_b128 v[224:227], v200 offset:15360\n" \
    "ds_read_b128 v[228:231], v200 offset:16384\n" \
    "ds_read_b128 v[232:235], v200 offset:6144\n" \
    "ds_read_b128 v[236:239], v200 offset:7168\n" \
    "ds_read_b128 v[240:243], v200 offset:8192\n" \
    "ds_read_b128 v[244:247], v200 offset:9216\n" \
    "ds_read_b128 v[248:251], v200 offset:10240\n" \
    "ds_read_b128 v[252:255], v200 offset:11264\n" \
    "s_lshl_b32 s36, s36, s18\n" \
    "s_add_u32 s38, s17, s36\n" \
    "s_add_u32 s32, s32, 1\n" \
    "v_pk_add_f32 v[204:205], v[204:205], v[206:207]\n" \
    "v_pk_add_f32 v[208:209], v[208:209], v[210:211]\n" \
    "v_pk_add_f32 v[212:213], v[212:213], v[214:215]\n" \
    "v_cvt_pk_bf16_f32 v204, v204, v205\n" \
    "v_cvt_pk_bf16_f32 v208, v208, v209\n" \
    "ds_write2st64_b32 v194, v204, v208 offset1:1\n" \
    "v_cvt_pk_bf16_f32 v212, v212, v213\n" \
    "ds_write_b32 v195, v212\n" \
    "ds_read_b128 v[212:215], v200 offset:12288\n" \
    "s_waitcnt lgkmcnt(1)\n" \
    "s_barrier\n" \
    "s_waitcnt lgkmcnt(0)\n" \
    "v_mfma_f32_16x16x32_bf16 v[204:207], v[212:215], v[232:235], 0\n" \
    "ds_read_b128 v[212:215], v200 offset:17408\n" \
    "v_cndmask_b32 v208, 0, v203, s[54:55]\n" \
    "v_mfma_f32_16x16x32_bf16 v[204:207], v[216:219], v[236:239], v[204:207]\n" \
    "ds_read_b128 v[216:219], v200 offset:18432\n" \
    "v_cndmask_b32 v209, 0, v203, s[56:57]\n" \
    "v_mfma_f32_16x16x32_bf16 v[204:207], v[220:223], v[240:243], v[204:207]\n" \
    "ds_read_b128 v[220:223], v200 offset:19456\n" \
    "v_cndmask_b32 v210, 0, v203, s[58:59]\n" \
    "v_mfma_f32_16x16x32_bf16 v[204:207], v[224:227], v[244:247], v[204:207]\n" \
    "ds_read_b128 v[224:227], v200 offset:20480\n" \
    "v_cndmask_b32 v211, 0, v203, s[60:61]\n" \
    "v_mfma_f32_16x16x32_bf16 v[204:207], v[228:231], v[248:251], v[204:207]\n" \
    "ds_read_b128 v[228:231], v200 offset:21504\n" \
    "s_waitcnt lgkmcnt(4)\n" \
    "v_mfma_f32_16x16x32_bf16 v[204:207], v[212:215], v[252:255], v[204:207]\n" \
    "ds_read_b128 v[212:215], v200 offset:22528\n" \
    "s_waitcnt lgkmcnt(4)\n" \
    "v_mfma_f32_16x16x32_bf16 v[208:211], v[216:219], v[232:235], v[208:211]\n" \
    "ds_read_b128 v[216:219], v200 offset:23552\n" \
    "s_waitcnt lgkmcnt(4)\n" \
    "v_mfma_f32_16x16x32_bf16 v[208:211], v[220:223], v[236:239], v[208:211]\n" \
    "ds_read_b64_tr_b16 v[220:221], v196 offset:13312\n" \
    "ds_read_b64_tr_b16 v[222:223], v196 offset:13568\n" \
    "s_waitcnt lgkmcnt(5)\n" \
    "v_mfma_f32_16x16x32_bf16 v[208:211], v[224:227], v[240:243], v[208:211]\n" \
    "ds_read_b64_tr_b16 v[224:225], v196 offset:13824\n" \
    "ds_read_b64_tr_b16 v[226:227], v196 offset:14080\n" \
    "s_waitcnt lgkmcnt(6)\n" \
    "v_mfma_f32_16x16x32_bf16 v[208:211], v[228:231], v[244:247], v[208:211]\n" \
    "ds_read_b64_tr_b16 v[228:229], v196 offset:14336\n" \
    "ds_read_b64_tr_b16 v[230:231], v196 offset:14592\n" \
    "v_max3_f32 v203, v204, v205, v206\n" \
    "s_waitcnt lgkmcnt(7)\n" \
    "v_mfma_f32_16x16x32_bf16 v[208:211], v[212:215], v[248:251], v[208:211]\n" \
    "ds_read_b64_tr_b16 v[212:213], v196 offset:14848\n" \
    "ds_read_b64_tr_b16 v[214:215], v196 offset:15104\n" \
    "v_max_f32 v203, v203, v207\n" \
    "s_waitcnt lgkmcnt(8)\n" \
    "v_mfma_f32_16x16x32_bf16 v[208:211], v[216:219], v[252:255], v[208:211]\n" \
    "ds_read_b64_tr_b16 v[216:217], v196 offset:15360\n" \
    "ds_read_b64_tr_b16 v[218:219], v196 offset:15616\n" \
    "s_waitcnt lgkmcnt(8)\n" \
    "v_mfma_f32_16x16x32_bf16 v[232:235], v[220:223], v[232:235], 0\n" \
    "ds_read_b64_tr_b16 v[220:221], v196 offset:15872\n" \
    "ds_read_b64_tr_b16 v[222:223], v196 offset:16128\n" \
    "s_nop 1\n" \
    "s_waitcnt lgkmcnt(8)\n" \
    "v_mfma_f32_16x16x32_bf16 v[232:235], v[224:227], v[236:239], v[232:235]\n" \
    "s_nop 1\n" \
    "s_waitcnt lgkmcnt(6)\n" \
    "v_mfma_f32_16x16x32_bf16 v[232:235], v[228:231], v[240:243], v[232:235]\n" \
    "v_max3_f32 v200, v203, v208, v209\n" \
    "v_max3_f32 v201, v200, v210, v211\n" \
    "v_max3_f32 v203, v200, v210, v211\n" \
    "s_waitcnt lgkmcnt(4)\n" \
    "v_mfma_f32_16x16x32_bf16 v[232:235], v[212:215], v[244:247], v[232:235]\n" \
    "v_permlane16_swap_b32 v201, v203\n" \
    "v_max_f32 v200, v201, v203\n" \
    "v_max_f32 v201, v201, v203\n" \
    "s_waitcnt lgkmcnt(2)\n" \
    "v_mfma_f32_16x16x32_bf16 v[232:235], v[216:219], v[248:251], v[232:235]\n" \
    "v_permlane32_swap_b32 v201, v200\n" \
    "v_max_f32 v201, v201, v200\n" \
    "s_waitcnt lgkmcnt(0)\n" \
    "v_mfma_f32_16x16x32_bf16 v[232:235], v[220:223], v[252:255], v[232:235]\n" \
    "s_nop 7\n" \
    "v_max3_f32 v236, v232, v233, v201\n" \
    "v_max3_f32 v237, v236, v234, v235\n" \
    "v_max3_f32 v236, v236, v234, v235\n" \
    "s_nop 1\n" \
    "v_permlane16_swap_b32 v236, v237\n" \
    "v_max_f32 v236, v236, v237\n" \
    "v_mul_f32 v236, 0xbdd53b94, v236\n" \
    "v_fmamk_f32 v208, v208, 0x3dd53b94, v236\n" \
    "v_fmamk_f32 v209, v209, 0x3dd53b94, v236\n" \
    "v_fmamk_f32 v210, v210, 0x3dd53b94, v236\n" \
    "v_fmamk_f32 v211, v211, 0x3dd53b94, v236\n" \
    "v_fmamk_f32 v204, v204, 0x3dd53b94, v236\n" \
    "v_fmamk_f32 v205, v205, 0x3dd53b94, v236\n" \
    "v_fmamk_f32 v206, v206, 0x3dd53b94, v236\n" \
    "v_fmamk_f32 v207, v207, 0x3dd53b94, v236\n" \
    "v_fmamk_f32 v232, v232, 0x3dd53b94, v236\n" \
    "v_fmamk_f32 v233, v233, 0x3dd53b94, v236\n" \
    "v_fmamk_f32 v234, v234, 0x3dd53b94, v236\n" \
    "v_fmamk_f32 v235, v235, 0x3dd53b94, v236\n" \
    "v_exp_f32 v208, v208\n" \
    "v_exp_f32 v209, v209\n" \
    "v_exp_f32 v210, v210\n" \
    "v_exp_f32 v211, v211\n" \
    "v_exp_f32 v204, v204\n" \
    "v_exp_f32 v205, v205\n" \
    "v_exp_f32 v206, v206\n" \
    "v_exp_f32 v207, v207\n" \
    "v_exp_f32 v232, v232\n" \
    "v_add_f32 v200, v208, v209\n" \
    "v_add_f32 v203, v210, v211\n" \
    "v_exp_f32 v233, v233\n" \
    "v_add_f32 v200, v200, v204\n" \
    "v_add_f32 v203, v203, v205\n" \
    "v_add_f32 v200, v200, v206\n" \
    "v_add_f32 v203, v203, v207\n" \
    "v_add_f32 v202, v200, v203\n" \
    "v_add_f32 v203, v200, v203\n" \
    "v_exp_f32 v234, v234\n" \
    "v_exp_f32 v235, v235\n" \
    "v_add_f32 v236, v232, v233\n" \
    "v_add_f32 v237, v234, v235\n" \
    "v_permlane32_swap_b32 v202, v203\n" \
    "v_add_f32 v236, v236, v202\n" \
    "v_add_f32 v237, v237, v203\n" \
    "v_add_f32 v238, v236, v237\n" \
    "v_add_f32 v236, v236, v237\n" \
    "s_nop 1\n" \
    "v_permlane16_swap_b32 v236, v238\n" \
    "v_add_f32 v236, v236, v238\n" \
    "v_rcp_f32 v236, v236\n" \
    "s_nop 0\n" \
    "v_pk_mul_f32 v[204:205], v[204:205], v[236:237] op_sel_hi:[1,0]\n" \
    "v_pk_mul_f32 v[208:209], v[208:209], v[236:237] op_sel_hi:[1,0]\n" \
    "v_pk_mul_f32 v[206:207], v[206:207], v[236:237] op_sel_hi:[1,0]\n" \
    "v_pk_mul_f32 v[210:211], v[210:211], v[236:237] op_sel_hi:[1,0]\n" \
    "v_pk_mul_f32 v[232:233], v[232:233], v[236:237] op_sel_hi:[1,0]\n" \
    "v_pk_mul_f32 v[234:235], v[234:235], v[236:237] op_sel_hi:[1,0]\n" \
    "v_cvt_pk_bf16_f32 v216, v204, v205\n" \
    "v_cvt_pk_bf16_f32 v217, v208, v209\n" \
    "v_cvt_pk_bf16_f32 v218, v206, v207\n" \
    "v_cvt_pk_bf16_f32 v219, v210, v211\n" \
    "buffer_store_dwordx4 v[216:219], v197, s[8:11], s38 offen sc1\n" \
    "v_cvt_pk_bf16_f32 v220, v232, v233\n" \
    "v_cvt_pk_bf16_f32 v221, v234, v235\n" \
    "buffer_store_dwordx2 v[220:221], v198, s[8:11], s38 offen sc1\n" \
    "s_cmp_lt_u32 s32, 128\n" \
    "s_cbranch_scc1 .Lqkm_l2_%=\n" \
    "s_branch .Lqkm_end_%=\n" \
    ".Lqkm_w3_%=:\n" \
    "s_sub_u32 s36, s16, 3\n" \
    "s_lshl_b32 s36, s36, 11\n" \
    "v_add_u32 v200, s36, v192\n" \
    "s_mov_b32 s33, s34\n" \
    "s_lshr_b32 s63, s33, 6\n" \
    "s_lshl_b32 s63, s63, 2\n" \
    "s_and_b32 s37, s33, 3\n" \
    "s_or_b32 s63, s63, s37\n" \
    "s_lshl_b32 s40, s63, 15\n" \
    "s_add_u32 s40, s40, s19\n" \
    "s_cmp_gt_u32 s33, 63\n" \
    "s_cselect_b32 s41, s49, s48\n" \
    "s_add_u32 s41, s41, %[dq]\n" \
    "s_cmp_gt_u32 s33, 128-%[lag]\n" \
    "s_cselect_b32 s37, %[dend], 0\n" \
    "s_sub_u32 s41, s41, s37\n" \
    ".Lqkm_l3_%=:\n" \
    "s_branch .Lqkm_t3b_%=\n" \
    ".Lqkm_t3a_%=:\n" \
    "s_sleep 1\n" \
    ".Lqkm_t3b_%=:\n" \
    "s_memrealtime vcc\n" \
    "s_waitcnt lgkmcnt(0)\n" \
    "s_sub_i32 vcc_lo, s41, vcc_lo\n" \
    "s_cmp_gt_i32 vcc_lo, 8\n" \
    "s_cbranch_scc1 .Lqkm_t3a_%=\n" \
    "s_cmp_gt_i32 vcc_lo, 0\n" \
    "s_cbranch_scc1 .Lqkm_t3b_%=\n" \
    "buffer_load_dwordx4 v[216:219], v192, s[8:11], s40 offen sc1\n" \
    "buffer_load_dwordx4 v[220:223], v192, s[8:11], s40 offen offset:1024 sc1\n" \
    "ds_read_b128 v[240:243], v192 offset:53248\n" \
    "ds_read_b128 v[244:247], v192 offset:56320\n" \
    "ds_read_b128 v[248:251], v192 offset:54272\n" \
    "ds_read_b128 v[252:255], v192 offset:57344\n" \
    "v_mov_b64 v[204:205], 0\n" \
    "v_mov_b64 v[206:207], 0\n" \
    "v_mov_b64 v[208:209], 0\n" \
    "v_mov_b64 v[210:211], 0\n" \
    "v_mov_b64 v[212:213], 0\n" \
    "v_mov_b64 v[214:215], 0\n" \
    "s_add_u32 s37, s32, 1\n" \
    "s_sub_i32 s36, s37, %[lag]\n" \
    "s_bitcmp1_b32 s36, 0\n" \
    "s_cselect_b32 s34, 128, 2*%[lag]\n" \
    "s_add_i32 s34, s34, s36\n" \
    "s_lshr_b32 s34, s34, 1\n" \
    "s_cmp_gt_u32 s36, 127-2*%[lag]\n" \
    "s_cselect_b32 s34, s37, s34\n" \
    "s_waitcnt vmcnt(1)\n" \
    "ds_write_b128 v200, v[216:219]\n" \
    "v_max3_u32 v224, v216, v217, v218\n" \
    "s_waitcnt vmcnt(0)\n" \
    "ds_write_b128 v200, v[220:223] offset:1024\n" \
    "v_max3_u32 v224, v224, v219, v220\n" \
    "v_max3_u32 v225, v221, v222, v223\n" \
    "v_max_u32 v224, v224, v225\n" \
    "v_cmp_eq_u32 vcc, -1, v224\n" \
    "s_nop 0\n" \
    "s_cbranch_vccnz .Lqkm_cg3_%=\n" \
    ".Lqkm_cgr3_%=:\n" \
    "s_waitcnt lgkmcnt(0)\n" \
    "s_barrier\n" \
    "ds_read_b128 v[216:219], v192 offset:0\n" \
    "ds_read_b128 v[220:223], v192 offset:1024\n" \
    "ds_read_b128 v[232:235], v192 offset:8192\n" \
    "ds_read_b128 v[224:227], v192 offset:2048\n" \
    "s_waitcnt lgkmcnt(3)\n" \
    "v_smfmac_f32_16x16x64_bf16 v[204:207], v[216:219], v[0:7], v193\n" \
    "ds_read_b128 v[228:231], v192 offset:3072\n" \
    "v_smfmac_f32_16x16x64_bf16 v[208:211], v[216:219], v[64:71], v193\n" \
    "v_smfmac_f32_16x16x64_bf16 v[212:215], v[216:219], v[128:135], v193\n" \
    "s_waitcnt lgkmcnt(3)\n" \
    "v_smfmac_f32_16x16x64_bf16 v[204:207], v[220:223], v[8:15], v193\n" \
    "ds_read_b128 v[216:219], v192 offset:4096\n" \
    "v_smfmac_f32_16x16x64_bf16 v[208:211], v[220:223], v[72:79], v193\n" \
    "ds_read_b128 v[236:239], v192 offset:9216\n" \
    "v_smfmac_f32_16x16x64_bf16 v[212:215], v[220:223], v[136:143], v193\n" \
    "s_waitcnt lgkmcnt(4)\n" \
    "v_smfmac_f32_16x16x64_bf16 v[204:207], v[232:235], v[240:247], v193\n" \
    "s_waitcnt lgkmcnt(3)\n" \
    "v_smfmac_f32_16x16x64_bf16 v[204:207], v[224:227], v[16:23], v193\n" \
    "ds_read_b128 v[220:223], v192 offset:5120\n" \
    "v_smfmac_f32_16x16x64_bf16 v[208:211], v[224:227], v[80:87], v193\n" \
    "ds_read_b128 v[240:243], v192 offset:55296\n" \
    "v_smfmac_f32_16x16x64_bf16 v[212:215], v[224:227], v[144:151], v193\n" \
    "ds_read_b128 v[244:247], v192 offset:58368\n" \
    "v_smfmac_f32_16x16x64_bf16 v[208:211], v[232:235], v[248:255], v193\n" \
    "s_waitcnt lgkmcnt(5)\n" \
    "v_smfmac_f32_16x16x64_bf16 v[204:207], v[228:231], v[24:31], v193\n" \
    "ds_read_b128 v[224:227], v192 offset:6144\n" \
    "v_smfmac_f32_16x16x64_bf16 v[208:211], v[228:231], v[88:95], v193\n" \
    "ds_read_b128 v[248:251], v192 offset:59392\n" \
    "v_smfmac_f32_16x16x64_bf16 v[212:215], v[228:231], v[152:159], v193\n" \
    "ds_read_b128 v[252:255], v192 offset:62464\n" \
    "s_waitcnt lgkmcnt(7)\n" \
    "v_smfmac_f32_16x16x64_bf16 v[204:207], v[216:219], v[32:39], v193\n" \
    "ds_read_b128 v[228:231], v192 offset:7168\n" \
    "v_smfmac_f32_16x16x64_bf16 v[208:211], v[216:219], v[96:103], v193\n" \
    "v_smfmac_f32_16x16x64_bf16 v[212:215], v[216:219], v[160:167], v193\n" \
    "s_waitcnt lgkmcnt(4)\n" \
    "v_smfmac_f32_16x16x64_bf16 v[212:215], v[232:235], v[240:247], v193\n" \
    "s_waitcnt lgkmcnt(1)\n" \
    "v_smfmac_f32_16x16x64_bf16 v[204:207], v[236:239], v[248:255], v193\n" \
    "ds_read_b128 v[240:243], v192 offset:60416\n" \
    "v_smfmac_f32_16x16x64_bf16 v[204:207], v[220:223], v[40:47], v193\n" \
    "ds_read_b128 v[244:247], v192 offset:63488\n" \
    "v_smfmac_f32_16x16x64_bf16 v[208:211], v[220:223], v[104:111], v193\n" \
    "ds_read_b128 v[248:251], v192 offset:61440\n" \
    "v_smfmac_f32_16x16x64_bf16 v[212:215], v[220:223], v[168:175], v193\n" \
    "ds_read_b128 v[252:255], v192 offset:64512\n" \
    "v_smfmac_f32_16x16x64_bf16 v[204:207], v[224:227], v[48:55], v193\n" \
    "v_smfmac_f32_16x16x64_bf16 v[208:211], v[224:227], v[112:119], v193\n" \
    "v_smfmac_f32_16x16x64_bf16 v[212:215], v[224:227], v[176:183], v193\n" \
    "s_waitcnt lgkmcnt(4)\n" \
    "v_smfmac_f32_16x16x64_bf16 v[204:207], v[228:231], v[56:63], v193\n" \
    "s_waitcnt lgkmcnt(2)\n" \
    "v_smfmac_f32_16x16x64_bf16 v[208:211], v[236:239], v[240:247], v193\n" \
    "v_smfmac_f32_16x16x64_bf16 v[208:211], v[228:231], v[120:127], v193\n" \
    "s_waitcnt lgkmcnt(0)\n" \
    "v_smfmac_f32_16x16x64_bf16 v[212:215], v[236:239], v[248:255], v193\n" \
    "v_smfmac_f32_16x16x64_bf16 v[212:215], v[228:231], v[184:191], v193\n" \
    "s_memrealtime s[50:51]\n" \
    "s_waitcnt lgkmcnt(0)\n" \
    "s_sub_u32 s50, s50, %[dq3]\n" \
    "s_cmp_gt_u32 s33, 63\n" \
    "s_cselect_b32 s49, s50, s49\n" \
    "s_cselect_b32 s48, s48, s50\n" \
    "s_add_u32 s32, s32, 1\n" \
    "s_mov_b32 s65, s33\n" \
    "s_mov_b32 s33, s34\n" \
    "s_lshr_b32 s63, s33, 6\n" \
    "s_lshl_b32 s63, s63, 2\n" \
    "s_and_b32 s37, s33, 3\n" \
    "s_or_b32 s63, s63, s37\n" \
    "s_lshl_b32 s40, s63, 15\n" \
    "s_add_u32 s40, s40, s19\n" \
    "s_cmp_gt_u32 s33, 63\n" \
    "s_cselect_b32 s41, s49, s48\n" \
    "s_add_u32 s41, s41, %[dq]\n" \
    "s_cmp_gt_u32 s33, 128-%[lag]\n" \
    "s_cselect_b32 s37, %[dend], 0\n" \
    "s_sub_u32 s41, s41, s37\n" \
    "v_pk_add_f32 v[204:205], v[204:205], v[206:207]\n" \
    "v_pk_add_f32 v[208:209], v[208:209], v[210:211]\n" \
    "v_pk_add_f32 v[212:213], v[212:213], v[214:215]\n" \
    "v_cvt_pk_bf16_f32 v204, v204, v205\n" \
    "v_cvt_pk_bf16_f32 v208, v208, v209\n" \
    "ds_write2st64_b32 v194, v204, v208 offset1:1\n" \
    "v_cvt_pk_bf16_f32 v212, v212, v213\n" \
    "ds_write_b32 v195, v212\n" \
    "s_waitcnt lgkmcnt(0)\n" \
    "s_barrier\n" \
    "s_cmp_lt_u32 s32, 128\n" \
    "s_cbranch_scc1 .Lqkm_l3_%=\n" \
    "s_branch .Lqkm_end_%=\n" \
    ".Lqkm_cg3_%=:\n" \
    "s_mov_b32 s46, 0\n" \
    ".Lqkm_cg23_%=:\n" \
    "buffer_load_dwordx4 v[216:219], v192, s[8:11], s40 offen sc1\n" \
    "buffer_load_dwordx4 v[220:223], v192, s[8:11], s40 offen offset:1024 sc1\n" \
    "s_waitcnt vmcnt(1)\n" \
    "ds_write_b128 v200, v[216:219]\n" \
    "v_max3_u32 v224, v216, v217, v218\n" \
    "s_waitcnt vmcnt(0)\n" \
    "ds_write_b128 v200, v[220:223] offset:1024\n" \
    "v_max3_u32 v224, v224, v219, v220\n" \
    "v_max3_u32 v225, v221, v222, v223\n" \
    "v_max_u32 v224, v224, v225\n" \
    "v_cmp_eq_u32 vcc, -1, v224\n" \
    "s_nop 0\n" \
    "s_cbranch_vccz .Lqkm_cgr3_%=\n" \
    "s_add_u32 s46, s46, 1\n" \
    "s_cmp_lt_u32 s46, s47\n" \
    "s_cbranch_scc1 .Lqkm_cg23_%=\n" \
    "s_mov_b32 s47, 0\n" \
    "s_branch .Lqkm_cgr3_%=\n" \
    ".Lqkm_w4_%=:\n" \
    "s_sub_u32 s36, s16, 3\n" \
    "s_lshl_b32 s36, s36, 11\n" \
    "v_add_u32 v200, s36, v192\n" \
    "v_mov_b32 v201, 0\n" \
    "v_min_u32 v202, 80, v192\n" \
    "s_mov_b32 s33, s34\n" \
    "s_lshr_b32 s63, s33, 6\n" \
    "s_lshl_b32 s63, s63, 2\n" \
    "s_and_b32 s37, s33, 3\n" \
    "s_or_b32 s63, s63, s37\n" \
    "s_lshl_b32 s40, s63, 15\n" \
    "s_add_u32 s40, s40, s19\n" \
    "s_cmp_gt_u32 s33, 63\n" \
    "s_cselect_b32 s41, s49, s48\n" \
    "s_add_u32 s41, s41, %[dq]\n" \
    "s_cmp_gt_u32 s33, 128-%[lag]\n" \
    "s_cselect_b32 s37, %[dend], 0\n" \
    "s_sub_u32 s41, s41, s37\n" \
    ".Lqkm_l4_%=:\n" \
    "s_cmp_eq_u32 s45, 0\n" \
    "s_cbranch_scc1 .Lqkm_cq_%=\n" \
    ".Lqkm_cqr_%=:\n" \
    "s_branch .Lqkm_t4b_%=\n" \
    ".Lqkm_t4a_%=:\n" \
    "s_sleep 1\n" \
    ".Lqkm_t4b_%=:\n" \
    "s_memrealtime vcc\n" \
    "s_waitcnt lgkmcnt(0)\n" \
    "s_sub_i32 vcc_lo, s41, vcc_lo\n" \
    "s_cmp_gt_i32 vcc_lo, 8\n" \
    "s_cbranch_scc1 .Lqkm_t4a_%=\n" \
    "s_cmp_gt_i32 vcc_lo, 0\n" \
    "s_cbranch_scc1 .Lqkm_t4b_%=\n" \
    "buffer_load_dwordx4 v[216:219], v192, s[8:11], s40 offen sc1\n" \
    "buffer_load_dwordx4 v[220:223], v192, s[8:11], s40 offen offset:1024 sc1\n" \
    "ds_read_b128 v[240:243], v196 offset:0\n" \
    "ds_read_b128 v[244:247], v196 offset:3072\n" \
    "ds_read_b128 v[248:251], v196 offset:1024\n" \
    "ds_read_b128 v[252:255], v196 offset:4096\n" \
    "v_mov_b64 v[204:205], 0\n" \
    "v_mov_b64 v[206:207], 0\n" \
    "v_mov_b64 v[208:209], 0\n" \
    "v_mov_b64 v[210:211], 0\n" \
    "v_mov_b64 v[212:213], 0\n" \
    "v_mov_b64 v[214:215], 0\n" \
    "s_and_b32 s36, s35, 63\n" \
    "s_mov_b32 s34, s35\n" \
    "s_waitcnt vmcnt(2)\n" \
    "v_cmp_lt_u32 vcc, s36, v201\n" \
    "s_add_u32 s37, s32, 2\n" \
    "s_sub_i32 s36, s37, %[lag]\n" \
    "s_bitcmp1_b32 s36, 0\n" \
    "s_cselect_b32 s35, 128, 2*%[lag]\n" \
    "s_add_i32 s35, s35, s36\n" \
    "s_lshr_b32 s35, s35, 1\n" \
    "s_cmp_gt_u32 s36, 127-2*%[lag]\n" \
    "s_cselect_b32 s35, s37, s35\n" \
    "s_xor_b32 s36, s63, 2\n" \
    "s_lshl_b32 s36, s36, s18\n" \
    "s_add_u32 s38, s17, s36\n" \
    "s_lshr_b32 s36, s33, 6\n" \
    "s_lshl_b32 s36, s36, 1\n" \
    "s_and_b32 s37, s33, 1\n" \
    "s_or_b32 s36, s36, s37\n" \
    "s_lshl_b32 s36, s36, 14\n" \
    "s_add_u32 s36, s36, s23\n" \
    "s_and_b32 s64, s33, 63\n" \
    "s_add_u32 s64, s64, 1\n" \
    "s_cmp_eq_u32 s16, 7\n" \
    "s_cselect_b32 s38, s36, s38\n" \
    "s_cselect_b32 s64, s64, -1\n" \
    "v_mov_b32 v229, s64\n" \
    "v_mov_b32 v230, -1\n" \
    "v_mov_b32 v231, -1\n" \
    "s_sub_u32 s42, s34, %[r0]\n" \
    "s_cselect_b32 s42, s34, s42\n" \
    "s_and_b32 s36, s34, 15\n" \
    "s_add_u32 s36, s36, %[r0]\n" \
    "s_cmp_gt_u32 s34, 63\n" \
    "s_cselect_b32 s42, s36, s42\n" \
    "s_mul_i32 s42, s42, 1572864\n" \
    "s_add_u32 s68, s42, s24\n" \
    "s_add_u32 s42, s42, s20\n" \
    "s_lshr_b32 s36, s42, 8\n" \
    "s_xor_b32 s36, s36, s26\n" \
    "s_and_b32 s36, s36, 0x2000\n" \
    "s_or_b32 s42, s42, s36\n" \
    "s_lshr_b32 s36, s68, 8\n" \
    "s_xor_b32 s36, s36, s26\n" \
    "s_and_b32 s36, s36, 0x2000\n" \
    "s_or_b32 s68, s68, s36\n" \
    "s_bitcmp0_b32 s32, 0\n" \
    "s_cselect_b32 s43, 24576, 0\n" \
    "s_add_u32 s43, s43, s21\n" \
    "s_sub_u32 s36, s35, s27\n" \
    "s_cmp_lt_u32 s36, s28\n" \
    "s_cselect_b32 s44, 32768, 0\n" \
    "s_cmp_gt_u32 s35, 63\n" \
    "s_cselect_b32 s44, 16384, s44\n" \
    "s_add_u32 s44, s44, s22\n" \
    "s_cmp_eq_u64 vcc, -1\n" \
    "s_cselect_b32 s45, 1, 0\n" \
    "s_cmp_lt_u32 s34, 128\n" \
    "s_cselect_b32 s45, s45, 0\n" \
    "s_cmp_eq_u32 s45, 0\n" \
    "s_cselect_b32 s42, 0x80000000, s42\n" \
    "s_cselect_b32 s68, 0x80000000, s68\n" \
    "s_waitcnt vmcnt(1)\n" \
    "ds_write_b128 v200, v[216:219]\n" \
    "v_max3_u32 v224, v216, v217, v218\n" \
    "s_waitcnt vmcnt(0)\n" \
    "ds_write_b128 v200, v[220:223] offset:1024\n" \
    "v_max3_u32 v224, v224, v219, v220\n" \
    "v_max3_u32 v225, v221, v222, v223\n" \
    "v_max_u32 v224, v224, v225\n" \
    "v_cmp_eq_u32 vcc, -1, v224\n" \
    "s_nop 0\n" \
    "s_cbranch_vccnz .Lqkm_cg4_%=\n" \
    ".Lqkm_cgr4_%=:\n" \
    "s_waitcnt lgkmcnt(0)\n" \
    "s_barrier\n" \
    "s_memrealtime s[50:51]\n" \
    "s_cmp_eq_u32 s16, 7\n" \
    "s_waitcnt lgkmcnt(0)\n" \
    "s_cselect_b32 s36, s50, -1\n" \
    "v_mov_b32 v228, s36\n" \
    "buffer_store_dwordx4 v[228:231], v197, s[8:11], s38 offen sc1\n" \
    "ds_read_b128 v[216:219], v192 offset:0\n" \
    "ds_read_b128 v[220:223], v192 offset:1024\n" \
    "ds_read_b128 v[232:235], v192 offset:8192\n" \
    "ds_read_b128 v[224:227], v192 offset:2048\n" \
    "buffer_store_dwordx4 v[228:231], v198, s[8:11], s38 offen sc1\n" \
    "s_mov_b32 m0, s43\n" \
    "s_add_u32 s43, s43, s25\n" \
    "buffer_load_dwordx4 v192, s[8:11], s42 offen offset:0 sc1 lds\n" \
    "buffer_load_dwordx4 v192, s[8:11], s42 offen offset:1024 sc1 lds\n" \
    "buffer_load_dwordx4 v192, s[8:11], s42 offen offset:2048 sc1 lds\n" \
    "buffer_load_dwordx4 v192, s[8:11], s42 offen offset:3072 sc1 lds\n" \
    "s_mov_b32 m0, s43\n" \
    "s_nop 0\n" \
    "buffer_load_dwordx4 v192, s[8:11], s68 offen offset:0 sc1 lds\n" \
    "buffer_load_dwordx4 v192, s[8:11], s68 offen offset:1024 sc1 lds\n" \
    "buffer_load_dword v201, v202, s[8:11], s44 offen sc1\n" \
    "s_cmp_gt_u32 s33, 63\n" \
    "s_cselect_b32 s49, s50, s49\n" \
    "s_cselect_b32 s48, s48, s50\n" \
    "s_add_u32 s32, s32, 1\n" \
    "s_mov_b32 s65, s33\n" \
    "s_mov_b32 s33, s34\n" \
    "s_lshr_b32 s63, s33, 6\n" \
    "s_lshl_b32 s63, s63, 2\n" \
    "s_and_b32 s37, s33, 3\n" \
    "s_or_b32 s63, s63, s37\n" \
    "s_lshl_b32 s40, s63, 15\n" \
    "s_add_u32 s40, s40, s19\n" \
    "s_cmp_gt_u32 s33, 63\n" \
    "s_cselect_b32 s41, s49, s48\n" \
    "s_add_u32 s41, s41, %[dq]\n" \
    "s_cmp_gt_u32 s33, 128-%[lag]\n" \
    "s_cselect_b32 s37, %[dend], 0\n" \
    "s_sub_u32 s41, s41, s37\n" \
    "s_waitcnt lgkmcnt(3)\n" \
    "v_smfmac_f32_16x16x64_bf16 v[204:207], v[216:219], v[0:7], v193\n" \
    "ds_read_b128 v[228:231], v192 offset:3072\n" \
    "v_smfmac_f32_16x16x64_bf16 v[208:211], v[216:219], v[64:71], v193\n" \
    "v_smfmac_f32_16x16x64_bf16 v[212:215], v[216:219], v[128:135], v193\n" \
    "s_waitcnt lgkmcnt(3)\n" \
    "v_smfmac_f32_16x16x64_bf16 v[204:207], v[220:223], v[8:15], v193\n" \
    "ds_read_b128 v[216:219], v192 offset:4096\n" \
    "v_smfmac_f32_16x16x64_bf16 v[208:211], v[220:223], v[72:79], v193\n" \
    "ds_read_b128 v[236:239], v192 offset:9216\n" \
    "v_smfmac_f32_16x16x64_bf16 v[212:215], v[220:223], v[136:143], v193\n" \
    "s_waitcnt lgkmcnt(4)\n" \
    "v_smfmac_f32_16x16x64_bf16 v[204:207], v[232:235], v[240:247], v193\n" \
    "s_waitcnt lgkmcnt(3)\n" \
    "v_smfmac_f32_16x16x64_bf16 v[204:207], v[224:227], v[16:23], v193\n" \
    "ds_read_b128 v[220:223], v192 offset:5120\n" \
    "v_smfmac_f32_16x16x64_bf16 v[208:211], v[224:227], v[80:87], v193\n" \
    "ds_read_b128 v[240:243], v196 offset:2048\n" \
    "v_smfmac_f32_16x16x64_bf16 v[212:215], v[224:227], v[144:151], v193\n" \
    "ds_read_b128 v[244:247], v196 offset:5120\n" \
    "v_smfmac_f32_16x16x64_bf16 v[208:211], v[232:235], v[248:255], v193\n" \
    "s_waitcnt lgkmcnt(5)\n" \
    "v_smfmac_f32_16x16x64_bf16 v[204:207], v[228:231], v[24:31], v193\n" \
    "ds_read_b128 v[224:227], v192 offset:6144\n" \
    "v_smfmac_f32_16x16x64_bf16 v[208:211], v[228:231], v[88:95], v193\n" \
    "ds_read_b128 v[248:251], v196 offset:6144\n" \
    "v_smfmac_f32_16x16x64_bf16 v[212:215], v[228:231], v[152:159], v193\n" \
    "ds_read_b128 v[252:255], v196 offset:9216\n" \
    "s_waitcnt lgkmcnt(7)\n" \
    "v_smfmac_f32_16x16x64_bf16 v[204:207], v[216:219], v[32:39], v193\n" \
    "ds_read_b128 v[228:231], v192 offset:7168\n" \
    "v_smfmac_f32_16x16x64_bf16 v[208:211], v[216:219], v[96:103], v193\n" \
    "v_smfmac_f32_16x16x64_bf16 v[212:215], v[216:219], v[160:167], v193\n" \
    "s_waitcnt lgkmcnt(4)\n" \
    "v_smfmac_f32_16x16x64_bf16 v[212:215], v[232:235], v[240:247], v193\n" \
    "s_waitcnt lgkmcnt(1)\n" \
    "v_smfmac_f32_16x16x64_bf16 v[204:207], v[236:239], v[248:255], v193\n" \
    "ds_read_b128 v[240:243], v196 offset:7168\n" \
    "v_smfmac_f32_16x16x64_bf16 v[204:207], v[220:223], v[40:47], v193\n" \
    "ds_read_b128 v[244:247], v196 offset:10240\n" \
    "v_smfmac_f32_16x16x64_bf16 v[208:211], v[220:223], v[104:111], v193\n" \
    "ds_read_b128 v[248:251], v196 offset:8192\n" \
    "v_smfmac_f32_16x16x64_bf16 v[212:215], v[220:223], v[168:175], v193\n" \
    "ds_read_b128 v[252:255], v196 offset:11264\n" \
    "v_smfmac_f32_16x16x64_bf16 v[204:207], v[224:227], v[48:55], v193\n" \
    "v_smfmac_f32_16x16x64_bf16 v[208:211], v[224:227], v[112:119], v193\n" \
    "v_smfmac_f32_16x16x64_bf16 v[212:215], v[224:227], v[176:183], v193\n" \
    "s_waitcnt lgkmcnt(4)\n" \
    "v_smfmac_f32_16x16x64_bf16 v[204:207], v[228:231], v[56:63], v193\n" \
    "s_waitcnt lgkmcnt(2)\n" \
    "v_smfmac_f32_16x16x64_bf16 v[208:211], v[236:239], v[240:247], v193\n" \
    "v_smfmac_f32_16x16x64_bf16 v[208:211], v[228:231], v[120:127], v193\n" \
    "s_waitcnt lgkmcnt(0)\n" \
    "v_smfmac_f32_16x16x64_bf16 v[212:215], v[236:239], v[248:255], v193\n" \
    "v_smfmac_f32_16x16x64_bf16 v[212:215], v[228:231], v[184:191], v193\n" \
    "s_nop 1\n" \
    "v_pk_add_f32 v[204:205], v[204:205], v[206:207]\n" \
    "s_nop 1\n" \
    "v_pk_add_f32 v[208:209], v[208:209], v[210:211]\n" \
    "s_nop 1\n" \
    "v_pk_add_f32 v[212:213], v[212:213], v[214:215]\n" \
    "v_cvt_pk_bf16_f32 v204, v204, v205\n" \
    "v_cvt_pk_bf16_f32 v208, v208, v209\n" \
    "ds_write2st64_b32 v194, v204, v208 offset1:1\n" \
    "v_cvt_pk_bf16_f32 v212, v212, v213\n" \
    "ds_write_b32 v195, v212\n" \
    "s_waitcnt lgkmcnt(0)\n" \
    "s_barrier\n" \
    "s_cmp_lt_u32 s32, 128\n" \
    "s_cbranch_scc1 .Lqkm_l4_%=\n" \
    "s_branch .Lqkm_end_%=\n" \
    ".Lqkm_cg4_%=:\n" \
    "s_mov_b32 s46, 0\n" \
    ".Lqkm_cg24_%=:\n" \
    "buffer_load_dwordx4 v[216:219], v192, s[8:11], s40 offen sc1\n" \
    "buffer_load_dwordx4 v[220:223], v192, s[8:11], s40 offen offset:1024 sc1\n" \
    "s_waitcnt vmcnt(1)\n" \
    "ds_write_b128 v200, v[216:219]\n" \
    "v_max3_u32 v224, v216, v217, v218\n" \
    "s_waitcnt vmcnt(0)\n" \
    "ds_write_b128 v200, v[220:223] offset:1024\n" \
    "v_max3_u32 v224, v224, v219, v220\n" \
    "v_max3_u32 v225, v221, v222, v223\n" \
    "v_max_u32 v224, v224, v225\n" \
    "v_cmp_eq_u32 vcc, -1, v224\n" \
    "s_nop 0\n" \
    "s_cbranch_vccz .Lqkm_cgr4_%=\n" \
    "s_add_u32 s46, s46, 1\n" \
    "s_cmp_lt_u32 s46, s47\n" \
    "s_cbranch_scc1 .Lqkm_cg24_%=\n" \
    "s_mov_b32 s47, 0\n" \
    "s_branch .Lqkm_cgr4_%=\n" \
    ".Lqkm_cq_%=:\n" \
    "s_sub_u32 s36, s33, s27\n" \
    "s_cmp_lt_u32 s36, s28\n" \
    "s_cselect_b32 s37, 32768, 0\n" \
    "s_cmp_gt_u32 s33, 63\n" \
    "s_cselect_b32 s37, 16384, s37\n" \
    "s_add_u32 s37, s37, s22\n" \
    "s_and_b32 s36, s33, 63\n" \
    "s_mov_b32 s46, 0\n" \
    ".Lqkm_cq2_%=:\n" \
    "buffer_load_dword v216, v202, s[8:11], s37 offen sc1\n" \
    "s_waitcnt vmcnt(0)\n" \
    "v_cmp_lt_u32 s[66:67], s36, v216\n" \
    "s_nop 0\n" \
    "s_cmp_eq_u64 s[66:67], -1\n" \
    "s_cbranch_scc1 .Lqkm_cq3_%=\n" \
    "s_sleep 2\n" \
    "s_add_u32 s46, s46, 1\n" \
    "s_cmp_lt_u32 s46, s47\n" \
    "s_cbranch_scc1 .Lqkm_cq2_%=\n" \
    "s_mov_b32 s47, 0\n" \
    "s_branch .Lqkm_cq3_%=\n" \
    ".Lqkm_cq3_%=:\n" \
    "s_sub_u32 s36, s33, %[r0]\n" \
    "s_cselect_b32 s36, s33, s36\n" \
    "s_and_b32 s37, s33, 15\n" \
    "s_add_u32 s37, s37, %[r0]\n" \
    "s_cmp_gt_u32 s33, 63\n" \
    "s_cselect_b32 s36, s37, s36\n" \
    "s_mul_i32 s36, s36, 1572864\n" \
    "s_add_u32 s69, s36, s24\n" \
    "s_add_u32 s36, s36, s20\n" \
    "s_lshr_b32 s37, s36, 8\n" \
    "s_xor_b32 s37, s37, s26\n" \
    "s_and_b32 s37, s37, 0x2000\n" \
    "s_or_b32 s36, s36, s37\n" \
    "s_lshr_b32 s37, s69, 8\n" \
    "s_xor_b32 s37, s37, s26\n" \
    "s_and_b32 s37, s37, 0x2000\n" \
    "s_or_b32 s69, s69, s37\n" \
    "s_bitcmp1_b32 s32, 0\n" \
    "s_cselect_b32 s37, 24576, 0\n" \
    "s_add_u32 s37, s37, s21\n" \
    "s_mov_b32 m0, s37\n" \
    "s_add_u32 s37, s37, s25\n" \
    "buffer_load_dwordx4 v192, s[8:11], s36 offen offset:0 sc1 lds\n" \
    "buffer_load_dwordx4 v192, s[8:11], s36 offen offset:1024 sc1 lds\n" \
    "buffer_load_dwordx4 v192, s[8:11], s36 offen offset:2048 sc1 lds\n" \
    "buffer_load_dwordx4 v192, s[8:11], s36 offen offset:3072 sc1 lds\n" \
    "s_mov_b32 m0, s37\n" \
    "s_nop 0\n" \
    "buffer_load_dwordx4 v192, s[8:11], s69 offen offset:0 sc1 lds\n" \
    "buffer_load_dwordx4 v192, s[8:11], s69 offen offset:1024 sc1 lds\n" \
    "s_sub_u32 s36, s35, s27\n" \
    "s_cmp_lt_u32 s36, s28\n" \
    "s_cselect_b32 s37, 32768, 0\n" \
    "s_cmp_gt_u32 s35, 63\n" \
    "s_cselect_b32 s37, 16384, s37\n" \
    "s_add_u32 s37, s37, s22\n" \
    "buffer_load_dword v201, v202, s[8:11], s37 offen sc1\n" \
    "s_branch .Lqkm_cqr_%=\n" \
    ".Lqkm_end_%=:\n" \
    "s_waitcnt vmcnt(0)\n" \
    "s_endpgm\n" \
    ""
#define UAV_ASM \
    "s_mov_b32 s32, 0\n" \
    "s_mov_b32 s42, 0x40000\n" \
    "s_memrealtime s[44:45]\n" \
    "s_waitcnt lgkmcnt(0)\n" \
    "s_sub_u32 s44, s44, 100000\n" \
    "s_mov_b32 s45, s44\n" \
    ".Luav_l_%=:\n" \
    "ds_read_b128 v[240:243], v196 offset:0\n" \
    "ds_read_b128 v[244:247], v196 offset:3072\n" \
    "ds_read_b128 v[248:251], v196 offset:1024\n" \
    "ds_read_b128 v[252:255], v196 offset:4096\n" \
    "ds_read_b128 v[204:207], v192 offset:13312\n" \
    "ds_read_b128 v[208:211], v192 offset:13312\n" \
    "ds_read_b128 v[212:215], v192 offset:13312\n" \
    "s_sub_i32 s34, s32, %[lag]\n" \
    "s_bitcmp1_b32 s34, 0\n" \
    "s_cselect_b32 s33, 128, 2*%[lag]\n" \
    "s_add_i32 s33, s33, s34\n" \
    "s_lshr_b32 s33, s33, 1\n" \
    "s_cmp_gt_u32 s34, 127-2*%[lag]\n" \
    "s_cselect_b32 s33, s32, s33\n" \
    "s_add_u32 s32, s32, 1\n" \
    "s_lshr_b32 s34, s33, 6\n" \
    "s_lshl_b32 s36, s34, 2\n" \
    "s_and_b32 s35, s33, 3\n" \
    "s_or_b32 s36, s36, s35\n" \
    "s_lshl_b32 s36, s36, 15\n" \
    "s_add_u32 s37, s36, s18\n" \
    "s_add_u32 s36, s36, s17\n" \
    "s_lshl_b32 s39, s34, 1\n" \
    "s_and_b32 s35, s33, 1\n" \
    "s_xor_b32 s35, s35, 1\n" \
    "s_or_b32 s39, s39, s35\n" \
    "s_lshl_b32 s39, s39, 14\n" \
    "s_add_u32 s39, s39, s19\n" \
    "s_mov_b32 s41, 0\n" \
    "s_and_b32 s40, s33, 63\n" \
    "s_cbranch_scc0 .Luav_go_%=\n" \
    ".Luav_an_%=:\n" \
    "s_buffer_load_dwordx2 s[50:51], s[8:11], s39 glc\n" \
    "s_waitcnt lgkmcnt(0)\n" \
    "s_waitcnt lgkmcnt(0)\n" \
    "s_cmp_lg_u32 s51, s40\n" \
    "s_cbranch_scc1 .Luav_can_%=\n" \
    "s_waitcnt lgkmcnt(0)\n" \
    "s_add_u32 s38, s50, %[dv]\n" \
    "s_cmp_gt_u32 s33, 128-%[lag]\n" \
    "s_cselect_b32 s35, %[dend], 0\n" \
    "s_sub_u32 s38, s38, s35\n" \
    "s_branch .Luav_tb_%=\n" \
    ".Luav_ta_%=:\n" \
    "s_sleep 1\n" \
    ".Luav_tb_%=:\n" \
    "s_memrealtime vcc\n" \
    "s_waitcnt lgkmcnt(0)\n" \
    "s_waitcnt lgkmcnt(0)\n" \
    "s_sub_i32 vcc_lo, s38, vcc_lo\n" \
    "s_waitcnt lgkmcnt(0)\n" \
    "s_cmp_gt_i32 vcc_lo, 8\n" \
    "s_cbranch_scc1 .Luav_ta_%=\n" \
    "s_waitcnt lgkmcnt(0)\n" \
    "s_cmp_gt_i32 vcc_lo, 0\n" \
    "s_cbranch_scc1 .Luav_tb_%=\n" \
    ".Luav_go_%=:\n" \
    "buffer_load_dwordx4 v[216:219], v192, s[8:11], s36 offen sc1\n" \
    "buffer_load_dwordx4 v[220:223], v192, s[8:11], s36 offen offset:1024 sc1\n" \
    "buffer_load_dwordx4 v[224:227], v192, s[8:11], s37 offen sc1\n" \
    "s_waitcnt vmcnt(2)\n" \
    "ds_write_b128 v197, v[216:219]\n" \
    "v_max3_u32 v228, v216, v217, v218\n" \
    "s_waitcnt vmcnt(1)\n" \
    "ds_write_b128 v197, v[220:223] offset:1024\n" \
    "v_max3_u32 v228, v228, v219, v220\n" \
    "v_max3_u32 v229, v221, v222, v223\n" \
    "s_waitcnt vmcnt(0)\n" \
    "ds_write_b128 v198, v[224:227]\n" \
    "v_max3_u32 v228, v228, v224, v225\n" \
    "v_max3_u32 v229, v229, v226, v227\n" \
    "v_max_u32 v228, v228, v229\n" \
    "s_waitcnt lgkmcnt(0)\n" \
    "v_cmp_eq_u32 vcc, -1, v228\n" \
    "s_nop 0\n" \
    "s_cbranch_vccnz .Luav_cg_%=\n" \
    ".Luav_cgr_%=:\n" \
    "s_waitcnt lgkmcnt(0)\n" \
    "s_barrier\n" \
    "ds_read_b128 v[216:219], v192 offset:0\n" \
    "ds_read_b128 v[220:223], v192 offset:1024\n" \
    "ds_read_b128 v[232:235], v192 offset:8192\n" \
    "ds_read_b128 v[224:227], v192 offset:2048\n" \
    "s_waitcnt lgkmcnt(3)\n" \
    "v_smfmac_f32_16x16x64_bf16 v[204:207], v[216:219], v[0:7], v193\n" \
    "ds_read_b128 v[228:231], v192 offset:3072\n" \
    "v_smfmac_f32_16x16x64_bf16 v[208:211], v[216:219], v[64:71], v193\n" \
    "v_smfmac_f32_16x16x64_bf16 v[212:215], v[216:219], v[128:135], v193\n" \
    "s_waitcnt lgkmcnt(3)\n" \
    "v_smfmac_f32_16x16x64_bf16 v[204:207], v[220:223], v[8:15], v193\n" \
    "ds_read_b128 v[216:219], v192 offset:4096\n" \
    "v_smfmac_f32_16x16x64_bf16 v[208:211], v[220:223], v[72:79], v193\n" \
    "ds_read_b128 v[236:239], v192 offset:9216\n" \
    "v_smfmac_f32_16x16x64_bf16 v[212:215], v[220:223], v[136:143], v193\n" \
    "s_waitcnt lgkmcnt(4)\n" \
    "v_smfmac_f32_16x16x64_bf16 v[204:207], v[232:235], v[240:247], v193\n" \
    "s_waitcnt lgkmcnt(3)\n" \
    "v_smfmac_f32_16x16x64_bf16 v[204:207], v[224:227], v[16:23], v193\n" \
    "ds_read_b128 v[220:223], v192 offset:5120\n" \
    "v_smfmac_f32_16x16x64_bf16 v[208:211], v[224:227], v[80:87], v193\n" \
    "ds_read_b128 v[240:243], v196 offset:2048\n" \
    "v_smfmac_f32_16x16x64_bf16 v[212:215], v[224:227], v[144:151], v193\n" \
    "ds_read_b128 v[244:247], v196 offset:5120\n" \
    "v_smfmac_f32_16x16x64_bf16 v[208:211], v[232:235], v[248:255], v193\n" \
    "s_waitcnt lgkmcnt(5)\n" \
    "v_smfmac_f32_16x16x64_bf16 v[204:207], v[228:231], v[24:31], v193\n" \
    "ds_read_b128 v[224:227], v192 offset:6144\n" \
    "v_smfmac_f32_16x16x64_bf16 v[208:211], v[228:231], v[88:95], v193\n" \
    "ds_read_b128 v[248:251], v196 offset:6144\n" \
    "v_smfmac_f32_16x16x64_bf16 v[212:215], v[228:231], v[152:159], v193\n" \
    "ds_read_b128 v[252:255], v196 offset:9216\n" \
    "s_waitcnt lgkmcnt(7)\n" \
    "v_smfmac_f32_16x16x64_bf16 v[204:207], v[216:219], v[32:39], v193\n" \
    "ds_read_b128 v[228:231], v192 offset:7168\n" \
    "v_smfmac_f32_16x16x64_bf16 v[208:211], v[216:219], v[96:103], v193\n" \
    "v_smfmac_f32_16x16x64_bf16 v[212:215], v[216:219], v[160:167], v193\n" \
    "s_waitcnt lgkmcnt(4)\n" \
    "v_smfmac_f32_16x16x64_bf16 v[212:215], v[232:235], v[240:247], v193\n" \
    "s_waitcnt lgkmcnt(1)\n" \
    "v_smfmac_f32_16x16x64_bf16 v[204:207], v[236:239], v[248:255], v193\n" \
    "ds_read_b128 v[240:243], v196 offset:7168\n" \
    "v_smfmac_f32_16x16x64_bf16 v[204:207], v[220:223], v[40:47], v193\n" \
    "ds_read_b128 v[244:247], v196 offset:10240\n" \
    "v_smfmac_f32_16x16x64_bf16 v[208:211], v[220:223], v[104:111], v193\n" \
    "ds_read_b128 v[248:251], v196 offset:8192\n" \
    "v_smfmac_f32_16x16x64_bf16 v[212:215], v[220:223], v[168:175], v193\n" \
    "ds_read_b128 v[252:255], v196 offset:11264\n" \
    "v_smfmac_f32_16x16x64_bf16 v[204:207], v[224:227], v[48:55], v193\n" \
    "v_smfmac_f32_16x16x64_bf16 v[208:211], v[224:227], v[112:119], v193\n" \
    "v_smfmac_f32_16x16x64_bf16 v[212:215], v[224:227], v[176:183], v193\n" \
    "s_waitcnt lgkmcnt(4)\n" \
    "v_smfmac_f32_16x16x64_bf16 v[204:207], v[228:231], v[56:63], v193\n" \
    "s_waitcnt lgkmcnt(2)\n" \
    "v_smfmac_f32_16x16x64_bf16 v[208:211], v[236:239], v[240:247], v193\n" \
    "v_smfmac_f32_16x16x64_bf16 v[208:211], v[228:231], v[120:127], v193\n" \
    "s_waitcnt lgkmcnt(0)\n" \
    "v_smfmac_f32_16x16x64_bf16 v[212:215], v[236:239], v[248:255], v193\n" \
    "v_smfmac_f32_16x16x64_bf16 v[212:215], v[228:231], v[184:191], v193\n" \
    "s_nop 1\n" \
    "v_pk_add_f32 v[204:205], v[204:205], v[206:207]\n" \
    "s_nop 1\n" \
    "v_pk_add_f32 v[208:209], v[208:209], v[210:211]\n" \
    "s_nop 1\n" \
    "v_pk_add_f32 v[212:213], v[212:213], v[214:215]\n" \
    "v_cvt_pk_bf16_f32 v204, v204, v205\n" \
    "v_cvt_pk_bf16_f32 v208, v208, v209\n" \
    "ds_write2st64_b32 v194, v204, v208 offset1:1\n" \
    "v_cvt_pk_bf16_f32 v212, v212, v213\n" \
    "ds_write_b32 v195, v212\n" \
    "s_waitcnt lgkmcnt(0)\n" \
    "s_barrier\n" \
    "s_cmp_lt_u32 s32, 128\n" \
    "s_cbranch_scc1 .Luav_l_%=\n" \
    "s_endpgm\n" \
    ".Luav_cg_%=:\n" \
    "buffer_load_dwordx4 v[216:219], v192, s[8:11], s36 offen sc1\n" \
    "buffer_load_dwordx4 v[220:223], v192, s[8:11], s36 offen offset:1024 sc1\n" \
    "buffer_load_dwordx4 v[224:227], v192, s[8:11], s37 offen sc1\n" \
    "s_waitcnt vmcnt(2)\n" \
    "ds_write_b128 v197, v[216:219]\n" \
    "v_max3_u32 v228, v216, v217, v218\n" \
    "s_waitcnt vmcnt(1)\n" \
    "ds_write_b128 v197, v[220:223] offset:1024\n" \
    "v_max3_u32 v228, v228, v219, v220\n" \
    "v_max3_u32 v229, v221, v222, v223\n" \
    "s_waitcnt vmcnt(0)\n" \
    "ds_write_b128 v198, v[224:227]\n" \
    "v_max3_u32 v228, v228, v224, v225\n" \
    "v_max3_u32 v229, v229, v226, v227\n" \
    "v_max_u32 v228, v228, v229\n" \
    "v_cmp_eq_u32 vcc, -1, v228\n" \
    "s_nop 0\n" \
    "s_cbranch_vccz .Luav_cgr_%=\n" \
    "s_add_u32 s41, s41, 1\n" \
    "s_cmp_lt_u32 s41, s42\n" \
    "s_cbranch_scc1 .Luav_cg_%=\n" \
    "s_mov_b32 s42, 0\n" \
    "s_branch .Luav_cgr_%=\n" \
    ".Luav_can_%=:\n" \
    "s_sleep 2\n" \
    "s_add_u32 s41, s41, 1\n" \
    "s_cmp_lt_u32 s41, s42\n" \
    "s_cbranch_scc1 .Luav_an_%=\n" \
    "s_mov_b32 s42, 0\n" \
    "s_branch .Luav_go_%=\n" \
    ""
#define UAO_ASM \
    "s_mov_b32 s32, 0\n" \
    "s_mov_b32 s39, 0\n" \
    "s_mov_b32 s40, 0\n" \
    "s_setprio 2\n" \
    ".Luao_l_%=:\n" \
    "s_sub_i32 s34, s32, %[lag]\n" \
    "s_bitcmp1_b32 s34, 0\n" \
    "s_cselect_b32 s33, 128, 2*%[lag]\n" \
    "s_add_i32 s33, s33, s34\n" \
    "s_lshr_b32 s33, s33, 1\n" \
    "s_cmp_gt_u32 s34, 127-2*%[lag]\n" \
    "s_cselect_b32 s33, s32, s33\n" \
    "ds_read_b128 v[236:239], v194 offset:0\n" \
    "ds_read_b128 v[240:243], v194 offset:1024\n" \
    "ds_read_b128 v[244:247], v194 offset:2048\n" \
    "ds_read_b128 v[248:251], v194 offset:3072\n" \
    "ds_read_b128 v[228:231], v192 offset:13312\n" \
    "ds_read_b128 v[212:215], v192 offset:13312\n" \
    "ds_read_b128 v[216:219], v192 offset:13312\n" \
    "ds_read_b128 v[232:235], v192 offset:13312\n" \
    "ds_read_b128 v[220:223], v192 offset:13312\n" \
    "ds_read_b128 v[224:227], v192 offset:13312\n" \
    "ds_read_b128 v[252:255], v192 offset:13312\n" \
    "s_add_u32 s32, s32, 1\n" \
    "s_cmp_gt_u32 s33, 63\n" \
    "s_cselect_b32 s34, s40, s39\n" \
    "s_cselect_b32 s35, s18, s17\n" \
    "s_add_u32 s36, s34, s35\n" \
    "s_sub_u32 s37, s34, 655360\n" \
    "s_cmp_eq_u32 s34, 0\n" \
    "s_cselect_b32 s37, 1310720, s37\n" \
    "s_add_u32 s37, s37, s35\n" \
    "s_add_u32 s34, s34, 655360\n" \
    "s_cmp_eq_u32 s34, 1966080\n" \
    "s_cselect_b32 s34, 0, s34\n" \
    "s_cmp_gt_u32 s33, 63\n" \
    "s_cselect_b32 s40, s34, s40\n" \
    "s_cselect_b32 s39, s39, s34\n" \
    "s_lshr_b32 s34, s33, 6\n" \
    "s_lshl_b32 s34, s34, 1\n" \
    "s_and_b32 s35, s33, 1\n" \
    "s_or_b32 s34, s34, s35\n" \
    "s_lshl_b32 s38, s34, s20\n" \
    "s_add_u32 s38, s38, s19\n" \
    "s_and_b32 s35, s33, 63\n" \
    "s_add_u32 s35, s35, 1\n" \
    "v_mov_b32 v201, s35\n" \
    "v_mov_b32 v204, -1\n" \
    "v_mov_b32 v205, -1\n" \
    "v_mov_b32 v206, -1\n" \
    "v_mov_b32 v207, -1\n" \
    "s_waitcnt vmcnt(5) lgkmcnt(0)\n" \
    "s_barrier\n" \
    "s_memrealtime s[50:51]\n" \
    "s_waitcnt lgkmcnt(0)\n" \
    "v_mov_b32 v200, s50\n" \
    "buffer_store_dwordx4 v[200:203], v198, s[8:11], s38 offen sc1\n" \
    "buffer_store_dwordx4 v[204:207], v197, s[8:11], s37 offen sc1\n" \
    "s_add_u32 s37, s37, 65536\n" \
    "buffer_store_dwordx4 v[204:207], v197, s[8:11], s37 offen sc1\n" \
    "s_add_u32 s37, s37, 32768\n" \
    "buffer_store_dwordx4 v[204:207], v197, s[8:11], s37 offen sc1\n" \
    "s_add_u32 s41, s36, 32768\n" \
    "s_add_u32 s42, s36, 65536\n" \
    "s_add_u32 s43, s36, 98304\n" \
    "s_add_u32 s44, s36, 131072\n" \
    "s_barrier\n" \
    "ds_read_b64_tr_b16 v[200:201], v195 offset:0\n" \
    "ds_read_b64_tr_b16 v[202:203], v195 offset:64\n" \
    "ds_read_b64_tr_b16 v[204:205], v195 offset:1024\n" \
    "ds_read_b64_tr_b16 v[206:207], v195 offset:1088\n" \
    "ds_read_b64_tr_b16 v[208:209], v195 offset:2048\n" \
    "ds_read_b64_tr_b16 v[210:211], v195 offset:2112\n" \
    "s_waitcnt lgkmcnt(4)\n" \
    "v_smfmac_f32_16x16x64_bf16 v[212:215], v[200:203], v[0:7], v193\n" \
    "s_waitcnt lgkmcnt(2)\n" \
    "v_smfmac_f32_16x16x64_bf16 v[212:215], v[204:207], v[8:15], v193\n" \
    "s_waitcnt lgkmcnt(0)\n" \
    "v_smfmac_f32_16x16x64_bf16 v[212:215], v[208:211], v[16:23], v193\n" \
    "v_smfmac_f32_16x16x64_bf16 v[216:219], v[200:203], v[24:31], v193\n" \
    "v_smfmac_f32_16x16x64_bf16 v[216:219], v[204:207], v[32:39], v193\n" \
    "v_smfmac_f32_16x16x64_bf16 v[216:219], v[208:211], v[40:47], v193\n" \
    "v_smfmac_f32_16x16x64_bf16 v[228:231], v[200:203], v[236:243], v193\n" \
    "v_smfmac_f32_16x16x64_bf16 v[228:231], v[204:207], v[244:251], v193\n" \
    "ds_read_b128 v[236:239], v194 offset:4096\n" \
    "ds_read_b128 v[240:243], v194 offset:5120\n" \
    "s_nop 0\n" \
    "v_add_f32 v212, v212, v214\n" \
    "v_add_f32 v213, v213, v215\n" \
    "v_smfmac_f32_16x16x64_bf16 v[220:223], v[200:203], v[48:55], v193\n" \
    "ds_read_b128 v[244:247], v194 offset:6144\n" \
    "ds_read_b128 v[248:251], v194 offset:7168\n" \
    "v_smfmac_f32_16x16x64_bf16 v[220:223], v[204:207], v[56:63], v193\n" \
    "v_cvt_pk_bf16_f32 v212, v212, v213\n" \
    "v_add_f32 v216, v216, v218\n" \
    "v_add_f32 v217, v217, v219\n" \
    "v_smfmac_f32_16x16x64_bf16 v[220:223], v[208:211], v[64:71], v193\n" \
    "v_cvt_pk_bf16_f32 v213, v216, v217\n" \
    "v_smfmac_f32_16x16x64_bf16 v[224:227], v[200:203], v[72:79], v193\n" \
    "ds_read_b128 v[216:219], v192 offset:13312\n" \
    "v_smfmac_f32_16x16x64_bf16 v[224:227], v[204:207], v[80:87], v193\n" \
    "v_permlane16_swap_b32 v212, v213\n" \
    "v_smfmac_f32_16x16x64_bf16 v[224:227], v[208:211], v[88:95], v193\n" \
    "buffer_store_dwordx2 v[212:213], v196, s[8:11], s36 offen sc1\n" \
    "s_waitcnt lgkmcnt(3)\n" \
    "v_smfmac_f32_16x16x64_bf16 v[228:231], v[208:211], v[236:243], v193\n" \
    "ds_read_b128 v[212:215], v192 offset:13312\n" \
    "s_waitcnt lgkmcnt(2)\n" \
    "v_smfmac_f32_16x16x64_bf16 v[232:235], v[200:203], v[244:251], v193\n" \
    "ds_read_b128 v[236:239], v194 offset:8192\n" \
    "ds_read_b128 v[240:243], v194 offset:9216\n" \
    "v_add_f32 v220, v220, v222\n" \
    "v_add_f32 v221, v221, v223\n" \
    "v_smfmac_f32_16x16x64_bf16 v[252:255], v[200:203], v[96:103], v193\n" \
    "ds_read_b128 v[244:247], v194 offset:10240\n" \
    "ds_read_b128 v[248:251], v194 offset:11264\n" \
    "v_smfmac_f32_16x16x64_bf16 v[252:255], v[204:207], v[104:111], v193\n" \
    "v_cvt_pk_bf16_f32 v220, v220, v221\n" \
    "v_add_f32 v224, v224, v226\n" \
    "v_add_f32 v225, v225, v227\n" \
    "v_smfmac_f32_16x16x64_bf16 v[252:255], v[208:211], v[112:119], v193\n" \
    "v_cvt_pk_bf16_f32 v221, v224, v225\n" \
    "s_waitcnt lgkmcnt(5)\n" \
    "v_smfmac_f32_16x16x64_bf16 v[216:219], v[200:203], v[120:127], v193\n" \
    "ds_read_b128 v[224:227], v192 offset:13312\n" \
    "v_permlane16_swap_b32 v220, v221\n" \
    "v_smfmac_f32_16x16x64_bf16 v[216:219], v[204:207], v[128:135], v193\n" \
    "buffer_store_dwordx2 v[220:221], v196, s[8:11], s41 offen sc1\n" \
    "v_add_f32 v228, v228, v230\n" \
    "v_add_f32 v229, v229, v231\n" \
    "v_smfmac_f32_16x16x64_bf16 v[216:219], v[208:211], v[136:143], v193\n" \
    "v_cvt_pk_bf16_f32 v228, v228, v229\n" \
    "s_waitcnt lgkmcnt(3)\n" \
    "v_smfmac_f32_16x16x64_bf16 v[232:235], v[204:207], v[236:243], v193\n" \
    "s_waitcnt lgkmcnt(1)\n" \
    "v_smfmac_f32_16x16x64_bf16 v[232:235], v[208:211], v[244:251], v193\n" \
    "v_add_f32 v252, v252, v254\n" \
    "v_add_f32 v253, v253, v255\n" \
    "v_smfmac_f32_16x16x64_bf16 v[212:215], v[200:203], v[144:151], v193\n" \
    "v_cvt_pk_bf16_f32 v252, v252, v253\n" \
    "v_smfmac_f32_16x16x64_bf16 v[212:215], v[204:207], v[152:159], v193\n" \
    "v_add_f32 v216, v216, v218\n" \
    "v_add_f32 v217, v217, v219\n" \
    "v_smfmac_f32_16x16x64_bf16 v[212:215], v[208:211], v[160:167], v193\n" \
    "v_cvt_pk_bf16_f32 v253, v216, v217\n" \
    "s_waitcnt lgkmcnt(0)\n" \
    "v_smfmac_f32_16x16x64_bf16 v[224:227], v[200:203], v[168:175], v193\n" \
    "v_add_f32 v232, v232, v234\n" \
    "v_add_f32 v233, v233, v235\n" \
    "v_permlane16_swap_b32 v252, v253\n" \
    "v_smfmac_f32_16x16x64_bf16 v[224:227], v[204:207], v[176:183], v193\n" \
    "buffer_store_dwordx2 v[252:253], v196, s[8:11], s42 offen sc1\n" \
    "v_smfmac_f32_16x16x64_bf16 v[224:227], v[208:211], v[184:191], v193\n" \
    "v_cvt_pk_bf16_f32 v229, v232, v233\n" \
    "v_add_f32 v212, v212, v214\n" \
    "v_add_f32 v213, v213, v215\n" \
    "v_permlane16_swap_b32 v228, v229\n" \
    "buffer_store_dwordx2 v[228:229], v196, s[8:11], s44 offen sc1\n" \
    "v_cvt_pk_bf16_f32 v212, v212, v213\n" \
    "s_nop 1\n" \
    "v_add_f32 v224, v224, v226\n" \
    "v_add_f32 v225, v225, v227\n" \
    "v_cvt_pk_bf16_f32 v213, v224, v225\n" \
    "s_nop 1\n" \
    "v_permlane16_swap_b32 v212, v213\n" \
    "buffer_store_dwordx2 v[212:213], v196, s[8:11], s43 offen sc1\n" \
    "s_cmp_lt_u32 s32, 128\n" \
    "s_cbranch_scc1 .Luao_l_%=\n" \
    "s_waitcnt vmcnt(0)\n" \
    "s_endpgm\n" \
    ""
#define R_ASM \
    "s_mov_b32 s32, 0\n" \
    "s_mov_b32 s46, 0\n" \
    "s_mov_b32 s53, 0x40000\n" \
    "s_mov_b64 s[56:57], 0xff\n" \
    "s_mov_b32 s50, 0\n" \
    "s_mov_b32 s51, 0\n" \
    "s_mov_b32 s45, 0x80000000\n" \
    "s_lshr_b32 s33, s19, 8\n" \
    "s_xor_b32 s33, s33, s27\n" \
    "s_and_b32 s33, s33, 0x2000\n" \
    "s_or_b32 s39, s19, s33\n" \
    "s_add_u32 s40, s39, 65536\n" \
    "v_mov_b32 v160, -1\n" \
    "v_mov_b32 v161, -1\n" \
    "v_mov_b32 v162, -1\n" \
    "v_mov_b32 v163, -1\n" \
    "s_mov_b32 s52, 0\n" \
    ".Lr_b0_%=:\n" \
    "buffer_load_dword v18, v2, s[8:11], 0 offen sc1\n" \
    "s_waitcnt vmcnt(0)\n" \
    "v_cmp_ge_u32 vcc, s50, v18\n" \
    "s_nop 0\n" \
    "s_and_b64 vcc, vcc, s[56:57]\n" \
    "s_cbranch_scc0 .Lr_b1_%=\n" \
    "s_sleep 2\n" \
    "s_add_u32 s52, s52, 1\n" \
    "s_cmp_lt_u32 s52, s53\n" \
    "s_cbranch_scc1 .Lr_b0_%=\n" \
    "s_mov_b32 s53, 0\n" \
    "s_branch .Lr_b1_%=\n" \
    ".Lr_b1_%=:\n" \
    "buffer_load_dwordx4 v[192:195], v0, s[8:11], s39 offen offset:0 sc1\n" \
    "buffer_load_dwordx4 v[196:199], v0, s[8:11], s39 offen offset:1024 sc1\n" \
    "buffer_load_dwordx4 v[200:203], v0, s[8:11], s39 offen offset:2048 sc1\n" \
    "buffer_load_dwordx4 v[204:207], v0, s[8:11], s39 offen offset:3072 sc1\n" \
    "buffer_load_dwordx4 v[208:211], v0, s[8:11], s40 offen offset:0 sc1\n" \
    "buffer_load_dwordx4 v[212:215], v0, s[8:11], s40 offen offset:1024 sc1\n" \
    "buffer_load_dwordx4 v[216:219], v0, s[8:11], s40 offen offset:2048 sc1\n" \
    "buffer_load_dwordx4 v[220:223], v0, s[8:11], s40 offen offset:3072 sc1\n" \
    "s_mov_b32 s52, 0\n" \
    "buffer_load_dwordx2 v[16:17], v3, s[8:11], s51 offen sc1\n" \
    "s_memrealtime s[62:63]\n" \
    "s_waitcnt vmcnt(0) lgkmcnt(0)\n" \
    "s_mov_b32 s55, s62\n" \
    "s_cmp_eq_u32 s16, 0\n" \
    "s_cbranch_scc1 .Lr_lA_%=\n" \
    "s_cmp_gt_u32 s16, 4\n" \
    "s_cbranch_scc1 .Lr_lC_%=\n" \
    "s_cmp_eq_u32 s16, 4\n" \
    "s_cbranch_scc1 .Lr_lE_%=\n" \
    "s_bitcmp1_b32 s16, 1\n" \
    "s_cbranch_scc1 .Lr_lB_%=\n" \
    ".Lr_lD_%=:\n" \
    "s_add_u32 s49, s32, 1\n" \
    "s_add_u32 s36, s18, s46\n" \
    "s_add_u32 s37, s36, 16384\n" \
    "s_and_b32 s33, s32, 3\n" \
    "s_lshl_b32 s35, s33, 16\n" \
    "s_add_u32 s35, s35, s17\n" \
    ".Lr_tvD0_%=:\n" \
    "s_waitcnt vmcnt(0)\n" \
    "v_cmp_ne_u32 vcc, s49, v17\n" \
    "v_subrev_u32 v19, s55, v16\n" \
    "s_and_b64 vcc, vcc, s[56:57]\n" \
    "s_cbranch_scc1 .Lr_ctvD0_%=\n" \
    "v_max_i32_dpp v19, v19, v19 quad_perm:[1,0,3,2] row_mask:0xf bank_mask:0xf bound_ctrl:1\n" \
    "s_nop 1\n" \
    "v_max_i32_dpp v19, v19, v19 quad_perm:[2,3,0,1] row_mask:0xf bank_mask:0xf bound_ctrl:1\n" \
    "s_nop 0\n" \
    "v_readlane_b32 s48, v19, 4\n" \
    "v_readlane_b32 s47, v19, 0\n" \
    "s_add_u32 s48, s48, s55\n" \
    "s_add_u32 s48, s48, s22\n" \
    ".Lr_goD0_%=:\n" \
    "s_branch .Lr_uD0b_%=\n" \
    ".Lr_uD0a_%=:\n" \
    "s_sleep 1\n" \
    ".Lr_uD0b_%=:\n" \
    "s_memrealtime vcc\n" \
    "s_waitcnt lgkmcnt(0)\n" \
    "s_sub_i32 vcc_lo, s48, vcc_lo\n" \
    "s_cmp_gt_i32 vcc_lo, 8\n" \
    "s_cbranch_scc1 .Lr_uD0a_%=\n" \
    "s_cmp_gt_i32 vcc_lo, 0\n" \
    "s_cbranch_scc1 .Lr_uD0b_%=\n" \
    "buffer_load_dwordx4 v[36:39], v0, s[8:11], s36 offen sc1\n" \
    "buffer_load_dwordx4 v[40:43], v0, s[8:11], s37 offen sc1\n" \
    "s_add_u32 s47, s47, s55\n" \
    "s_add_u32 s47, s47, %[dr]\n" \
    "s_branch .Lr_pD0b_%=\n" \
    ".Lr_pD0a_%=:\n" \
    "s_sleep 1\n" \
    ".Lr_pD0b_%=:\n" \
    "s_memrealtime vcc\n" \
    "s_waitcnt lgkmcnt(0)\n" \
    "s_sub_i32 vcc_lo, s47, vcc_lo\n" \
    "s_cmp_gt_i32 vcc_lo, 8\n" \
    "s_cbranch_scc1 .Lr_pD0a_%=\n" \
    "s_cmp_gt_i32 vcc_lo, 0\n" \
    "s_cbranch_scc1 .Lr_pD0b_%=\n" \
    "buffer_load_dwordx4 v[32:35], v4, s[8:11], s35 offen sc1\n" \
    "buffer_load_dwordx4 v[24:27], v0, s[8:11], s35 offen sc1\n" \
    "buffer_load_dwordx4 v[28:31], v0, s[8:11], s35 offen offset:1024 sc1\n" \
    "s_and_b32 s33, s32, 3\n" \
    "s_lshl_b32 s61, s33, 17\n" \
    "s_add_u32 s41, s61, s20\n" \
    "s_add_u32 s42, s41, 4096\n" \
    "s_and_b32 s33, s49, %[brn]-1\n" \
    "s_mul_i32 s39, s33, 2621440\n" \
    "s_add_u32 s39, s39, s19\n" \
    "s_lshr_b32 s33, s39, 8\n" \
    "s_xor_b32 s33, s33, s27\n" \
    "s_and_b32 s33, s33, 0x2000\n" \
    "s_or_b32 s39, s39, s33\n" \
    "s_barrier\n" \
    "buffer_load_dword v18, v2, s[8:11], 0 offen sc1\n" \
    "buffer_load_dwordx4 v[52:55], v0, s[8:11], s41 offen offset:2048 sc1\n" \
    "buffer_load_dwordx4 v[44:47], v0, s[8:11], s41 offen offset:0 sc1\n" \
    "buffer_load_dwordx4 v[48:51], v0, s[8:11], s41 offen offset:1024 sc1\n" \
    "buffer_load_dwordx4 v[56:59], v0, s[8:11], s41 offen offset:3072 sc1\n" \
    "buffer_load_dword v21, v13, s[8:11], s61 offen sc1\n" \
    "s_add_u32 s46, s46, 655360\n" \
    "s_cmp_eq_u32 s46, 1966080\n" \
    "s_cselect_b32 s46, 0, s46\n" \
    "s_and_b32 s33, s49, 3\n" \
    "s_lshl_b32 s38, s33, 15\n" \
    "s_mul_i32 s43, s32, 40960\n" \
    "s_add_u32 s43, s43, s21\n" \
    "s_cmp_eq_u32 s32, 63\n" \
    "s_cselect_b32 s38, 0x80000000, s38\n" \
    "s_waitcnt vmcnt(10)\n" \
    "v_max3_u32 v19, v36, v37, v38\n" \
    "s_waitcnt vmcnt(9)\n" \
    "v_max3_u32 v19, v19, v39, v40\n" \
    "v_max3_u32 v19, v19, v41, v42\n" \
    "v_max_u32 v19, v19, v43\n" \
    "s_waitcnt vmcnt(8)\n" \
    "v_mfma_f32_16x16x32_bf16 v[80:83], v[32:35], v[36:39], 0\n" \
    "v_max3_u32 v20, v32, v33, v34\n" \
    "v_mfma_f32_16x16x32_bf16 v[84:87], v[32:35], v[40:43], 0\n" \
    "v_max_u32 v20, v20, v35\n" \
    "s_waitcnt vmcnt(7)\n" \
    "v_smfmac_f32_16x16x64_bf16 v[80:83], v[24:27], v[192:199], v10\n" \
    "v_smfmac_f32_16x16x64_bf16 v[84:87], v[24:27], v[208:215], v10\n" \
    "v_max3_u32 v19, v19, v24, v25\n" \
    "v_max3_u32 v19, v19, v26, v27\n" \
    "s_waitcnt vmcnt(6)\n" \
    "v_smfmac_f32_16x16x64_bf16 v[80:83], v[28:31], v[200:207], v10\n" \
    "v_smfmac_f32_16x16x64_bf16 v[84:87], v[28:31], v[216:223], v10\n" \
    "v_max3_u32 v20, v20, v28, v29\n" \
    "v_max3_u32 v20, v20, v30, v31\n" \
    "v_max_u32 v19, v19, v20\n" \
    "v_cmp_eq_u32 vcc, -1, v19\n" \
    "s_nop 0\n" \
    "s_cbranch_vccnz .Lr_cpD0_%=\n" \
    ".Lr_cprD0_%=:\n" \
    "s_nop 0\n" \
    "v_pk_add_f32 v[80:81], v[80:81], v[82:83]\n" \
    "v_pk_add_f32 v[84:85], v[84:85], v[86:87]\n" \
    "ds_write2_b32 v5, v80, v81 offset0:0 offset1:4\n" \
    "ds_write2_b32 v5, v84, v85 offset0:64 offset1:68\n" \
    "s_waitcnt lgkmcnt(0)\n" \
    "s_barrier\n" \
    "s_barrier\n" \
    "s_memrealtime s[62:63]\n" \
    "buffer_load_dwordx4 v[60:63], v0, s[8:11], s42 offen offset:0 sc1\n" \
    "buffer_load_dwordx4 v[64:67], v0, s[8:11], s42 offen offset:1024 sc1\n" \
    "buffer_load_dwordx4 v[68:71], v0, s[8:11], s42 offen offset:2048 sc1\n" \
    "buffer_load_dwordx4 v[72:75], v0, s[8:11], s42 offen offset:3072 sc1\n" \
    "s_waitcnt vmcnt(9)\n" \
    "v_cmp_ge_u32 vcc, s49, v18\n" \
    "s_nop 0\n" \
    "s_and_b64 vcc, vcc, s[56:57]\n" \
    "s_cselect_b32 s54, 0, 1\n" \
    "s_cmp_eq_u32 s32, 63\n" \
    "s_cselect_b32 s54, 2, s54\n" \
    "s_cmp_eq_u32 s54, 1\n" \
    "s_cselect_b32 s39, s39, 0x80000000\n" \
    "s_add_u32 s40, s39, 65536\n" \
    "s_barrier\n" \
    "buffer_load_dwordx4 v[224:227], v0, s[8:11], s39 offen offset:0 sc1\n" \
    "buffer_load_dwordx4 v[228:231], v0, s[8:11], s39 offen offset:1024 sc1\n" \
    "buffer_load_dwordx4 v[232:235], v0, s[8:11], s39 offen offset:2048 sc1\n" \
    "buffer_load_dwordx4 v[236:239], v0, s[8:11], s39 offen offset:3072 sc1\n" \
    "buffer_load_dwordx4 v[240:243], v0, s[8:11], s40 offen offset:0 sc1\n" \
    "buffer_load_dwordx4 v[244:247], v0, s[8:11], s40 offen offset:1024 sc1\n" \
    "buffer_load_dwordx4 v[248:251], v0, s[8:11], s40 offen offset:2048 sc1\n" \
    "buffer_load_dwordx4 v[252:255], v0, s[8:11], s40 offen offset:3072 sc1\n" \
    "s_and_b32 s33, s32, s23\n" \
    "s_mul_i32 s45, s33, s24\n" \
    "s_cmp_lt_u32 s33, 64-%[pdist]\n" \
    "s_cselect_b32 s45, s45, 0x80000000\n" \
    "s_add_u32 s44, s43, %[x1]\n" \
    "s_xor_b32 s51, s51, 16384\n" \
    "s_waitcnt vmcnt(15)\n" \
    "v_max3_u32 v19, v44, v45, v46\n" \
    "s_waitcnt vmcnt(14)\n" \
    "v_max3_u32 v19, v19, v47, v48\n" \
    "v_max3_u32 v19, v19, v49, v50\n" \
    "v_max3_u32 v19, v19, v51, v52\n" \
    "v_max3_u32 v19, v19, v53, v54\n" \
    "s_waitcnt vmcnt(13)\n" \
    "v_max3_u32 v19, v19, v55, v56\n" \
    "v_max3_u32 v19, v19, v57, v58\n" \
    "s_waitcnt vmcnt(11)\n" \
    "v_max3_u32 v19, v19, v59, v60\n" \
    "v_max3_u32 v19, v19, v61, v62\n" \
    "s_waitcnt vmcnt(10)\n" \
    "v_max3_u32 v19, v19, v63, v64\n" \
    "v_max3_u32 v19, v19, v65, v66\n" \
    "s_waitcnt vmcnt(9)\n" \
    "v_max3_u32 v19, v19, v67, v68\n" \
    "v_max3_u32 v19, v19, v69, v70\n" \
    "s_waitcnt vmcnt(8)\n" \
    "v_max3_u32 v19, v19, v71, v72\n" \
    "v_max3_u32 v19, v19, v73, v74\n" \
    "v_max_u32 v19, v19, v75\n" \
    "v_cmp_eq_u32 vcc, -1, v19\n" \
    "s_nop 0\n" \
    "s_cbranch_vccnz .Lr_ctD0_%=\n" \
    ".Lr_ctrD0_%=:\n" \
    "v_mfma_f32_16x16x32_bf16 v[88:91], v[36:39], v[72:75], 0\n" \
    "v_mfma_f32_16x16x32_bf16 v[88:91], v[192:195], v[56:59], v[88:91]\n" \
    "v_mfma_f32_16x16x32_bf16 v[88:91], v[196:199], v[60:63], v[88:91]\n" \
    "v_mfma_f32_16x16x32_bf16 v[88:91], v[200:203], v[64:67], v[88:91]\n" \
    "v_mfma_f32_16x16x32_bf16 v[88:91], v[204:207], v[68:71], v[88:91]\n" \
    "v_mfma_f32_16x16x32_bf16 v[92:95], v[40:43], v[72:75], 0\n" \
    "v_mfma_f32_16x16x32_bf16 v[92:95], v[208:211], v[56:59], v[92:95]\n" \
    "v_mfma_f32_16x16x32_bf16 v[92:95], v[212:215], v[60:63], v[92:95]\n" \
    "v_mfma_f32_16x16x32_bf16 v[92:95], v[216:219], v[64:67], v[92:95]\n" \
    "v_mfma_f32_16x16x32_bf16 v[92:95], v[220:223], v[68:71], v[92:95]\n" \
    "v_mfma_f32_16x16x32_bf16 v[80:83], v[36:39], v[52:55], 0\n" \
    "v_mfma_f32_16x16x32_bf16 v[84:87], v[40:43], v[52:55], 0\n" \
    "v_mov_b32 v193, v194\n" \
    "v_mov_b32 v194, v196\n" \
    "v_mov_b32 v195, v198\n" \
    "v_mov_b32 v201, v202\n" \
    "v_mov_b32 v202, v204\n" \
    "v_mov_b32 v203, v206\n" \
    "v_mov_b32 v209, v210\n" \
    "v_mov_b32 v210, v212\n" \
    "v_mov_b32 v211, v214\n" \
    "v_mov_b32 v217, v218\n" \
    "v_mov_b32 v218, v220\n" \
    "v_mov_b32 v219, v222\n" \
    "ds_write_b128 v8, v[88:91] offset:2048\n" \
    "ds_write_b128 v8, v[92:95] offset:3072\n" \
    "v_permlane16_swap_b32 v192, v194\n" \
    "v_permlane16_swap_b32 v193, v195\n" \
    "v_permlane16_swap_b32 v200, v202\n" \
    "v_permlane16_swap_b32 v201, v203\n" \
    "v_permlane16_swap_b32 v208, v210\n" \
    "v_permlane16_swap_b32 v209, v211\n" \
    "v_permlane16_swap_b32 v216, v218\n" \
    "v_permlane16_swap_b32 v217, v219\n" \
    "v_mfma_f32_16x16x32_bf16 v[80:83], v[192:195], v[44:47], v[80:83]\n" \
    "v_mfma_f32_16x16x32_bf16 v[84:87], v[208:211], v[44:47], v[84:87]\n" \
    "v_mfma_f32_16x16x32_bf16 v[80:83], v[200:203], v[48:51], v[80:83]\n" \
    "v_mfma_f32_16x16x32_bf16 v[84:87], v[216:219], v[48:51], v[84:87]\n" \
    "s_nop 6\n" \
    "ds_write_b128 v8, v[80:83] offset:0\n" \
    "ds_write_b128 v8, v[84:87] offset:1024\n" \
    "s_waitcnt lgkmcnt(0)\n" \
    "s_barrier\n" \
    "s_mov_b32 s55, s62\n" \
    "s_add_u32 s64, s62, %[dh]\n" \
    "s_branch .Lr_hD0b_%=\n" \
    ".Lr_hD0a_%=:\n" \
    "s_sleep 1\n" \
    ".Lr_hD0b_%=:\n" \
    "s_memrealtime vcc\n" \
    "s_waitcnt lgkmcnt(0)\n" \
    "s_sub_i32 vcc_lo, s64, vcc_lo\n" \
    "s_cmp_gt_i32 vcc_lo, 8\n" \
    "s_cbranch_scc1 .Lr_hD0a_%=\n" \
    "s_cmp_gt_i32 vcc_lo, 0\n" \
    "s_cbranch_scc1 .Lr_hD0b_%=\n" \
    "buffer_load_dwordx2 v[16:17], v3, s[8:11], s51 offen sc1\n" \
    "s_cmp_eq_u32 s54, 0\n" \
    "s_cbranch_scc1 .Lr_cbD0_%=\n" \
    ".Lr_cbrD0_%=:\n" \
    "s_add_u32 s32, s32, 1\n" \
    "s_add_u32 s49, s32, 1\n" \
    "s_add_u32 s36, s18, s46\n" \
    "s_add_u32 s37, s36, 16384\n" \
    "s_and_b32 s33, s32, 3\n" \
    "s_lshl_b32 s35, s33, 16\n" \
    "s_add_u32 s35, s35, s17\n" \
    ".Lr_tvD1_%=:\n" \
    "s_waitcnt vmcnt(0)\n" \
    "v_cmp_ne_u32 vcc, s49, v17\n" \
    "v_subrev_u32 v19, s55, v16\n" \
    "s_and_b64 vcc, vcc, s[56:57]\n" \
    "s_cbranch_scc1 .Lr_ctvD1_%=\n" \
    "v_max_i32_dpp v19, v19, v19 quad_perm:[1,0,3,2] row_mask:0xf bank_mask:0xf bound_ctrl:1\n" \
    "s_nop 1\n" \
    "v_max_i32_dpp v19, v19, v19 quad_perm:[2,3,0,1] row_mask:0xf bank_mask:0xf bound_ctrl:1\n" \
    "s_nop 0\n" \
    "v_readlane_b32 s48, v19, 4\n" \
    "v_readlane_b32 s47, v19, 0\n" \
    "s_add_u32 s48, s48, s55\n" \
    "s_add_u32 s48, s48, s22\n" \
    ".Lr_goD1_%=:\n" \
    "s_branch .Lr_uD1b_%=\n" \
    ".Lr_uD1a_%=:\n" \
    "s_sleep 1\n" \
    ".Lr_uD1b_%=:\n" \
    "s_memrealtime vcc\n" \
    "s_waitcnt lgkmcnt(0)\n" \
    "s_sub_i32 vcc_lo, s48, vcc_lo\n" \
    "s_cmp_gt_i32 vcc_lo, 8\n" \
    "s_cbranch_scc1 .Lr_uD1a_%=\n" \
    "s_cmp_gt_i32 vcc_lo, 0\n" \
    "s_cbranch_scc1 .Lr_uD1b_%=\n" \
    "buffer_load_dwordx4 v[36:39], v0, s[8:11], s36 offen sc1\n" \
    "buffer_load_dwordx4 v[40:43], v0, s[8:11], s37 offen sc1\n" \
    "s_add_u32 s47, s47, s55\n" \
    "s_add_u32 s47, s47, %[dr]\n" \
    "s_branch .Lr_pD1b_%=\n" \
    ".Lr_pD1a_%=:\n" \
    "s_sleep 1\n" \
    ".Lr_pD1b_%=:\n" \
    "s_memrealtime vcc\n" \
    "s_waitcnt lgkmcnt(0)\n" \
    "s_sub_i32 vcc_lo, s47, vcc_lo\n" \
    "s_cmp_gt_i32 vcc_lo, 8\n" \
    "s_cbranch_scc1 .Lr_pD1a_%=\n" \
    "s_cmp_gt_i32 vcc_lo, 0\n" \
    "s_cbranch_scc1 .Lr_pD1b_%=\n" \
    "buffer_load_dwordx4 v[32:35], v4, s[8:11], s35 offen sc1\n" \
    "buffer_load_dwordx4 v[24:27], v0, s[8:11], s35 offen sc1\n" \
    "buffer_load_dwordx4 v[28:31], v0, s[8:11], s35 offen offset:1024 sc1\n" \
    "s_and_b32 s33, s32, 3\n" \
    "s_lshl_b32 s61, s33, 17\n" \
    "s_add_u32 s41, s61, s20\n" \
    "s_add_u32 s42, s41, 4096\n" \
    "s_and_b32 s33, s49, %[brn]-1\n" \
    "s_mul_i32 s39, s33, 2621440\n" \
    "s_add_u32 s39, s39, s19\n" \
    "s_lshr_b32 s33, s39, 8\n" \
    "s_xor_b32 s33, s33, s27\n" \
    "s_and_b32 s33, s33, 0x2000\n" \
    "s_or_b32 s39, s39, s33\n" \
    "s_barrier\n" \
    "buffer_load_dword v18, v2, s[8:11], 0 offen sc1\n" \
    "buffer_load_dwordx4 v[52:55], v0, s[8:11], s41 offen offset:2048 sc1\n" \
    "buffer_load_dwordx4 v[44:47], v0, s[8:11], s41 offen offset:0 sc1\n" \
    "buffer_load_dwordx4 v[48:51], v0, s[8:11], s41 offen offset:1024 sc1\n" \
    "buffer_load_dwordx4 v[56:59], v0, s[8:11], s41 offen offset:3072 sc1\n" \
    "buffer_load_dword v21, v13, s[8:11], s61 offen sc1\n" \
    "s_add_u32 s46, s46, 655360\n" \
    "s_cmp_eq_u32 s46, 1966080\n" \
    "s_cselect_b32 s46, 0, s46\n" \
    "s_and_b32 s33, s49, 3\n" \
    "s_lshl_b32 s38, s33, 15\n" \
    "s_mul_i32 s43, s32, 40960\n" \
    "s_add_u32 s43, s43, s21\n" \
    "s_cmp_eq_u32 s32, 63\n" \
    "s_cselect_b32 s38, 0x80000000, s38\n" \
    "s_waitcnt vmcnt(10)\n" \
    "v_max3_u32 v19, v36, v37, v38\n" \
    "s_waitcnt vmcnt(9)\n" \
    "v_max3_u32 v19, v19, v39, v40\n" \
    "v_max3_u32 v19, v19, v41, v42\n" \
    "v_max_u32 v19, v19, v43\n" \
    "s_waitcnt vmcnt(8)\n" \
    "v_mfma_f32_16x16x32_bf16 v[80:83], v[32:35], v[36:39], 0\n" \
    "v_max3_u32 v20, v32, v33, v34\n" \
    "v_mfma_f32_16x16x32_bf16 v[84:87], v[32:35], v[40:43], 0\n" \
    "v_max_u32 v20, v20, v35\n" \
    "s_waitcnt vmcnt(7)\n" \
    "v_smfmac_f32_16x16x64_bf16 v[80:83], v[24:27], v[224:231], v10\n" \
    "v_smfmac_f32_16x16x64_bf16 v[84:87], v[24:27], v[240:247], v10\n" \
    "v_max3_u32 v19, v19, v24, v25\n" \
    "v_max3_u32 v19, v19, v26, v27\n" \
    "s_waitcnt vmcnt(6)\n" \
    "v_smfmac_f32_16x16x64_bf16 v[80:83], v[28:31], v[232:239], v10\n" \
    "v_smfmac_f32_16x16x64_bf16 v[84:87], v[28:31], v[248:255], v10\n" \
    "v_max3_u32 v20, v20, v28, v29\n" \
    "v_max3_u32 v20, v20, v30, v31\n" \
    "v_max_u32 v19, v19, v20\n" \
    "v_cmp_eq_u32 vcc, -1, v19\n" \
    "s_nop 0\n" \
    "s_cbranch_vccnz .Lr_cpD1_%=\n" \
    ".Lr_cprD1_%=:\n" \
    "s_nop 0\n" \
    "v_pk_add_f32 v[80:81], v[80:81], v[82:83]\n" \
    "v_pk_add_f32 v[84:85], v[84:85], v[86:87]\n" \
    "ds_write2_b32 v5, v80, v81 offset0:0 offset1:4\n" \
    "ds_write2_b32 v5, v84, v85 offset0:64 offset1:68\n" \
    "s_waitcnt lgkmcnt(0)\n" \
    "s_barrier\n" \
    "s_barrier\n" \
    "s_memrealtime s[62:63]\n" \
    "buffer_load_dwordx4 v[60:63], v0, s[8:11], s42 offen offset:0 sc1\n" \
    "buffer_load_dwordx4 v[64:67], v0, s[8:11], s42 offen offset:1024 sc1\n" \
    "buffer_load_dwordx4 v[68:71], v0, s[8:11], s42 offen offset:2048 sc1\n" \
    "buffer_load_dwordx4 v[72:75], v0, s[8:11], s42 offen offset:3072 sc1\n" \
    "s_waitcnt vmcnt(9)\n" \
    "v_cmp_ge_u32 vcc, s49, v18\n" \
    "s_nop 0\n" \
    "s_and_b64 vcc, vcc, s[56:57]\n" \
    "s_cselect_b32 s54, 0, 1\n" \
    "s_cmp_eq_u32 s32, 63\n" \
    "s_cselect_b32 s54, 2, s54\n" \
    "s_cmp_eq_u32 s54, 1\n" \
    "s_cselect_b32 s39, s39, 0x80000000\n" \
    "s_add_u32 s40, s39, 65536\n" \
    "s_barrier\n" \
    "buffer_load_dwordx4 v[192:195], v0, s[8:11], s39 offen offset:0 sc1\n" \
    "buffer_load_dwordx4 v[196:199], v0, s[8:11], s39 offen offset:1024 sc1\n" \
    "buffer_load_dwordx4 v[200:203], v0, s[8:11], s39 offen offset:2048 sc1\n" \
    "buffer_load_dwordx4 v[204:207], v0, s[8:11], s39 offen offset:3072 sc1\n" \
    "buffer_load_dwordx4 v[208:211], v0, s[8:11], s40 offen offset:0 sc1\n" \
    "buffer_load_dwordx4 v[212:215], v0, s[8:11], s40 offen offset:1024 sc1\n" \
    "buffer_load_dwordx4 v[216:219], v0, s[8:11], s40 offen offset:2048 sc1\n" \
    "buffer_load_dwordx4 v[220:223], v0, s[8:11], s40 offen offset:3072 sc1\n" \
    "s_and_b32 s33, s32, s23\n" \
    "s_mul_i32 s45, s33, s24\n" \
    "s_cmp_lt_u32 s33, 64-%[pdist]\n" \
    "s_cselect_b32 s45, s45, 0x80000000\n" \
    "s_add_u32 s44, s43, %[x1]\n" \
    "s_xor_b32 s51, s51, 16384\n" \
    "s_waitcnt vmcnt(15)\n" \
    "v_max3_u32 v19, v44, v45, v46\n" \
    "s_waitcnt vmcnt(14)\n" \
    "v_max3_u32 v19, v19, v47, v48\n" \
    "v_max3_u32 v19, v19, v49, v50\n" \
    "v_max3_u32 v19, v19, v51, v52\n" \
    "v_max3_u32 v19, v19, v53, v54\n" \
    "s_waitcnt vmcnt(13)\n" \
    "v_max3_u32 v19, v19, v55, v56\n" \
    "v_max3_u32 v19, v19, v57, v58\n" \
    "s_waitcnt vmcnt(11)\n" \
    "v_max3_u32 v19, v19, v59, v60\n" \
    "v_max3_u32 v19, v19, v61, v62\n" \
    "s_waitcnt vmcnt(10)\n" \
    "v_max3_u32 v19, v19, v63, v64\n" \
    "v_max3_u32 v19, v19, v65, v66\n" \
    "s_waitcnt vmcnt(9)\n" \
    "v_max3_u32 v19, v19, v67, v68\n" \
    "v_max3_u32 v19, v19, v69, v70\n" \
    "s_waitcnt vmcnt(8)\n" \
    "v_max3_u32 v19, v19, v71, v72\n" \
    "v_max3_u32 v19, v19, v73, v74\n" \
    "v_max_u32 v19, v19, v75\n" \
    "v_cmp_eq_u32 vcc, -1, v19\n" \
    "s_nop 0\n" \
    "s_cbranch_vccnz .Lr_ctD1_%=\n" \
    ".Lr_ctrD1_%=:\n" \
    "v_mfma_f32_16x16x32_bf16 v[88:91], v[36:39], v[72:75], 0\n" \
    "v_mfma_f32_16x16x32_bf16 v[88:91], v[224:227], v[56:59], v[88:91]\n" \
    "v_mfma_f32_16x16x32_bf16 v[88:91], v[228:231], v[60:63], v[88:91]\n" \
    "v_mfma_f32_16x16x32_bf16 v[88:91], v[232:235], v[64:67], v[88:91]\n" \
    "v_mfma_f32_16x16x32_bf16 v[88:91], v[236:239], v[68:71], v[88:91]\n" \
    "v_mfma_f32_16x16x32_bf16 v[92:95], v[40:43], v[72:75], 0\n" \
    "v_mfma_f32_16x16x32_bf16 v[92:95], v[240:243], v[56:59], v[92:95]\n" \
    "v_mfma_f32_16x16x32_bf16 v[92:95], v[244:247], v[60:63], v[92:95]\n" \
    "v_mfma_f32_16x16x32_bf16 v[92:95], v[248:251], v[64:67], v[92:95]\n" \
    "v_mfma_f32_16x16x32_bf16 v[92:95], v[252:255], v[68:71], v[92:95]\n" \
    "v_mfma_f32_16x16x32_bf16 v[80:83], v[36:39], v[52:55], 0\n" \
    "v_mfma_f32_16x16x32_bf16 v[84:87], v[40:43], v[52:55], 0\n" \
    "v_mov_b32 v225, v226\n" \
    "v_mov_b32 v226, v228\n" \
    "v_mov_b32 v227, v230\n" \
    "v_mov_b32 v233, v234\n" \
    "v_mov_b32 v234, v236\n" \
    "v_mov_b32 v235, v238\n" \
    "v_mov_b32 v241, v242\n" \
    "v_mov_b32 v242, v244\n" \
    "v_mov_b32 v243, v246\n" \
    "v_mov_b32 v249, v250\n" \
    "v_mov_b32 v250, v252\n" \
    "v_mov_b32 v251, v254\n" \
    "ds_write_b128 v8, v[88:91] offset:2048\n" \
    "ds_write_b128 v8, v[92:95] offset:3072\n" \
    "v_permlane16_swap_b32 v224, v226\n" \
    "v_permlane16_swap_b32 v225, v227\n" \
    "v_permlane16_swap_b32 v232, v234\n" \
    "v_permlane16_swap_b32 v233, v235\n" \
    "v_permlane16_swap_b32 v240, v242\n" \
    "v_permlane16_swap_b32 v241, v243\n" \
    "v_permlane16_swap_b32 v248, v250\n" \
    "v_permlane16_swap_b32 v249, v251\n" \
    "v_mfma_f32_16x16x32_bf16 v[80:83], v[224:227], v[44:47], v[80:83]\n" \
    "v_mfma_f32_16x16x32_bf16 v[84:87], v[240:243], v[44:47], v[84:87]\n" \
    "v_mfma_f32_16x16x32_bf16 v[80:83], v[232:235], v[48:51], v[80:83]\n" \
    "v_mfma_f32_16x16x32_bf16 v[84:87], v[248:251], v[48:51], v[84:87]\n" \
    "s_nop 6\n" \
    "ds_write_b128 v8, v[80:83] offset:0\n" \
    "ds_write_b128 v8, v[84:87] offset:1024\n" \
    "s_waitcnt lgkmcnt(0)\n" \
    "s_barrier\n" \
    "s_mov_b32 s55, s62\n" \
    "s_add_u32 s64, s62, %[dh]\n" \
    "s_branch .Lr_hD1b_%=\n" \
    ".Lr_hD1a_%=:\n" \
    "s_sleep 1\n" \
    ".Lr_hD1b_%=:\n" \
    "s_memrealtime vcc\n" \
    "s_waitcnt lgkmcnt(0)\n" \
    "s_sub_i32 vcc_lo, s64, vcc_lo\n" \
    "s_cmp_gt_i32 vcc_lo, 8\n" \
    "s_cbranch_scc1 .Lr_hD1a_%=\n" \
    "s_cmp_gt_i32 vcc_lo, 0\n" \
    "s_cbranch_scc1 .Lr_hD1b_%=\n" \
    "buffer_load_dwordx2 v[16:17], v3, s[8:11], s51 offen sc1\n" \
    "s_cmp_eq_u32 s54, 0\n" \
    "s_cbranch_scc1 .Lr_cbD1_%=\n" \
    ".Lr_cbrD1_%=:\n" \
    "s_add_u32 s32, s32, 1\n" \
    "s_cmp_lt_u32 s32, 64\n" \
    "s_cbranch_scc1 .Lr_lD_%=\n" \
    "s_waitcnt vmcnt(0)\n" \
    "s_endpgm\n" \
    ".Lr_lA_%=:\n" \
    "s_add_u32 s49, s32, 1\n" \
    "s_add_u32 s36, s18, s46\n" \
    "s_add_u32 s37, s36, 16384\n" \
    "s_and_b32 s33, s32, 3\n" \
    "s_lshl_b32 s35, s33, 16\n" \
    "s_add_u32 s35, s35, s17\n" \
    ".Lr_tvA0_%=:\n" \
    "s_waitcnt vmcnt(0)\n" \
    "v_cmp_ne_u32 vcc, s49, v17\n" \
    "v_subrev_u32 v19, s55, v16\n" \
    "s_and_b64 vcc, vcc, s[56:57]\n" \
    "s_cbranch_scc1 .Lr_ctvA0_%=\n" \
    "v_max_i32_dpp v19, v19, v19 quad_perm:[1,0,3,2] row_mask:0xf bank_mask:0xf bound_ctrl:1\n" \
    "s_nop 1\n" \
    "v_max_i32_dpp v19, v19, v19 quad_perm:[2,3,0,1] row_mask:0xf bank_mask:0xf bound_ctrl:1\n" \
    "s_nop 0\n" \
    "v_readlane_b32 s48, v19, 4\n" \
    "v_readlane_b32 s47, v19, 0\n" \
    "s_add_u32 s48, s48, s55\n" \
    "s_add_u32 s48, s48, s22\n" \
    ".Lr_goA0_%=:\n" \
    "s_branch .Lr_uA0b_%=\n" \
    ".Lr_uA0a_%=:\n" \
    "s_sleep 1\n" \
    ".Lr_uA0b_%=:\n" \
    "s_memrealtime vcc\n" \
    "s_waitcnt lgkmcnt(0)\n" \
    "s_sub_i32 vcc_lo, s48, vcc_lo\n" \
    "s_cmp_gt_i32 vcc_lo, 8\n" \
    "s_cbranch_scc1 .Lr_uA0a_%=\n" \
    "s_cmp_gt_i32 vcc_lo, 0\n" \
    "s_cbranch_scc1 .Lr_uA0b_%=\n" \
    "buffer_load_dwordx4 v[36:39], v0, s[8:11], s36 offen sc1\n" \
    "buffer_load_dwordx4 v[40:43], v0, s[8:11], s37 offen sc1\n" \
    "s_add_u32 s47, s47, s55\n" \
    "s_add_u32 s47, s47, %[dr]\n" \
    "s_branch .Lr_pA0b_%=\n" \
    ".Lr_pA0a_%=:\n" \
    "s_sleep 1\n" \
    ".Lr_pA0b_%=:\n" \
    "s_memrealtime vcc\n" \
    "s_waitcnt lgkmcnt(0)\n" \
    "s_sub_i32 vcc_lo, s47, vcc_lo\n" \
    "s_cmp_gt_i32 vcc_lo, 8\n" \
    "s_cbranch_scc1 .Lr_pA0a_%=\n" \
    "s_cmp_gt_i32 vcc_lo, 0\n" \
    "s_cbranch_scc1 .Lr_pA0b_%=\n" \
    "buffer_load_dwordx4 v[32:35], v4, s[8:11], s35 offen sc1\n" \
    "buffer_load_dwordx4 v[24:27], v0, s[8:11], s35 offen sc1\n" \
    "buffer_load_dwordx4 v[28:31], v0, s[8:11], s35 offen offset:1024 sc1\n" \
    "s_and_b32 s33, s32, 3\n" \
    "s_lshl_b32 s61, s33, 17\n" \
    "s_add_u32 s41, s61, s20\n" \
    "s_add_u32 s42, s41, 4096\n" \
    "s_and_b32 s33, s49, %[brn]-1\n" \
    "s_mul_i32 s39, s33, 2621440\n" \
    "s_add_u32 s39, s39, s19\n" \
    "s_lshr_b32 s33, s39, 8\n" \
    "s_xor_b32 s33, s33, s27\n" \
    "s_and_b32 s33, s33, 0x2000\n" \
    "s_or_b32 s39, s39, s33\n" \
    "s_barrier\n" \
    "buffer_load_dword v18, v2, s[8:11], 0 offen sc1\n" \
    "buffer_load_dwordx4 v[52:55], v0, s[8:11], s41 offen offset:2048 sc1\n" \
    "buffer_load_dwordx4 v[44:47], v0, s[8:11], s41 offen offset:0 sc1\n" \
    "buffer_load_dwordx4 v[48:51], v0, s[8:11], s41 offen offset:1024 sc1\n" \
    "buffer_load_dwordx4 v[56:59], v0, s[8:11], s41 offen offset:3072 sc1\n" \
    "s_add_u32 s46, s46, 655360\n" \
    "s_cmp_eq_u32 s46, 1966080\n" \
    "s_cselect_b32 s46, 0, s46\n" \
    "s_and_b32 s33, s49, 3\n" \
    "s_lshl_b32 s38, s33, 15\n" \
    "s_mul_i32 s43, s32, 40960\n" \
    "s_add_u32 s43, s43, s21\n" \
    "s_cmp_eq_u32 s32, 63\n" \
    "s_cselect_b32 s38, 0x80000000, s38\n" \
    "s_waitcnt vmcnt(9)\n" \
    "v_max3_u32 v19, v36, v37, v38\n" \
    "s_waitcnt vmcnt(8)\n" \
    "v_max3_u32 v19, v19, v39, v40\n" \
    "v_max3_u32 v19, v19, v41, v42\n" \
    "v_max_u32 v19, v19, v43\n" \
    "s_waitcnt vmcnt(7)\n" \
    "v_mfma_f32_16x16x32_bf16 v[80:83], v[32:35], v[36:39], 0\n" \
    "v_max3_u32 v20, v32, v33, v34\n" \
    "v_mfma_f32_16x16x32_bf16 v[84:87], v[32:35], v[40:43], 0\n" \
    "v_max_u32 v20, v20, v35\n" \
    "s_waitcnt vmcnt(6)\n" \
    "v_smfmac_f32_16x16x64_bf16 v[80:83], v[24:27], v[192:199], v10\n" \
    "v_smfmac_f32_16x16x64_bf16 v[84:87], v[24:27], v[208:215], v10\n" \
    "v_max3_u32 v19, v19, v24, v25\n" \
    "v_max3_u32 v19, v19, v26, v27\n" \
    "s_waitcnt vmcnt(5)\n" \
    "v_smfmac_f32_16x16x64_bf16 v[80:83], v[28:31], v[200:207], v10\n" \
    "v_smfmac_f32_16x16x64_bf16 v[84:87], v[28:31], v[216:223], v10\n" \
    "v_max3_u32 v20, v20, v28, v29\n" \
    "v_max3_u32 v20, v20, v30, v31\n" \
    "v_max_u32 v19, v19, v20\n" \
    "v_cmp_eq_u32 vcc, -1, v19\n" \
    "s_nop 0\n" \
    "s_cbranch_vccnz .Lr_cpA0_%=\n" \
    ".Lr_cprA0_%=:\n" \
    "s_nop 0\n" \
    "v_pk_add_f32 v[80:81], v[80:81], v[82:83]\n" \
    "v_pk_add_f32 v[84:85], v[84:85], v[86:87]\n" \
    "ds_write2_b32 v5, v80, v81 offset0:0 offset1:4\n" \
    "ds_write2_b32 v5, v84, v85 offset0:64 offset1:68\n" \
    "s_waitcnt lgkmcnt(0)\n" \
    "s_barrier\n" \
    "ds_read_b128 v[96:99], v0 offset:0\n" \
    "ds_read_b128 v[100:103], v0 offset:1024\n" \
    "ds_read_b128 v[104:107], v0 offset:2048\n" \
    "ds_read_b128 v[108:111], v0 offset:3072\n" \
    "ds_read_b128 v[112:115], v0 offset:4096\n" \
    "ds_read_b128 v[116:119], v0 offset:5120\n" \
    "ds_read_b128 v[120:123], v0 offset:6144\n" \
    "ds_read_b128 v[124:127], v0 offset:7168\n" \
    "s_waitcnt lgkmcnt(6)\n" \
    "v_pk_add_f32 v[96:97], v[96:97], v[100:101]\n" \
    "v_pk_add_f32 v[98:99], v[98:99], v[102:103]\n" \
    "s_waitcnt lgkmcnt(4)\n" \
    "v_pk_add_f32 v[104:105], v[104:105], v[108:109]\n" \
    "v_pk_add_f32 v[106:107], v[106:107], v[110:111]\n" \
    "s_waitcnt lgkmcnt(2)\n" \
    "v_pk_add_f32 v[112:113], v[112:113], v[116:117]\n" \
    "v_pk_add_f32 v[114:115], v[114:115], v[118:119]\n" \
    "v_pk_add_f32 v[96:97], v[96:97], v[104:105]\n" \
    "v_pk_add_f32 v[98:99], v[98:99], v[106:107]\n" \
    "s_waitcnt lgkmcnt(0)\n" \
    "v_pk_add_f32 v[120:121], v[120:121], v[124:125]\n" \
    "v_pk_add_f32 v[122:123], v[122:123], v[126:127]\n" \
    "v_pk_add_f32 v[96:97], v[96:97], v[112:113]\n" \
    "v_pk_add_f32 v[98:99], v[98:99], v[114:115]\n" \
    "v_pk_add_f32 v[96:97], v[96:97], v[120:121]\n" \
    "v_pk_add_f32 v[98:99], v[98:99], v[122:123]\n" \
    "v_cvt_pk_bf16_f32 v96, v96, v97\n" \
    "v_cvt_pk_bf16_f32 v97, v98, v99\n" \
    "buffer_store_dwordx2 v[96:97], v1, s[8:11], s38 offen sc1\n" \
    "s_barrier\n" \
    "s_memrealtime s[62:63]\n" \
    "buffer_load_dwordx4 v[60:63], v0, s[8:11], s42 offen offset:0 sc1\n" \
    "buffer_load_dwordx4 v[64:67], v0, s[8:11], s42 offen offset:1024 sc1\n" \
    "buffer_load_dwordx4 v[68:71], v0, s[8:11], s42 offen offset:2048 sc1\n" \
    "buffer_load_dwordx4 v[72:75], v0, s[8:11], s42 offen offset:3072 sc1\n" \
    "s_waitcnt vmcnt(9)\n" \
    "v_cmp_ge_u32 vcc, s49, v18\n" \
    "s_nop 0\n" \
    "s_and_b64 vcc, vcc, s[56:57]\n" \
    "s_cselect_b32 s54, 0, 1\n" \
    "s_cmp_eq_u32 s32, 63\n" \
    "s_cselect_b32 s54, 2, s54\n" \
    "s_cmp_eq_u32 s54, 1\n" \
    "s_cselect_b32 s39, s39, 0x80000000\n" \
    "s_add_u32 s40, s39, 65536\n" \
    "s_barrier\n" \
    "buffer_load_dwordx4 v[224:227], v0, s[8:11], s39 offen offset:0 sc1\n" \
    "buffer_load_dwordx4 v[228:231], v0, s[8:11], s39 offen offset:1024 sc1\n" \
    "buffer_load_dwordx4 v[232:235], v0, s[8:11], s39 offen offset:2048 sc1\n" \
    "buffer_load_dwordx4 v[236:239], v0, s[8:11], s39 offen offset:3072 sc1\n" \
    "buffer_load_dwordx4 v[240:243], v0, s[8:11], s40 offen offset:0 sc1\n" \
    "buffer_load_dwordx4 v[244:247], v0, s[8:11], s40 offen offset:1024 sc1\n" \
    "buffer_load_dwordx4 v[248:251], v0, s[8:11], s40 offen offset:2048 sc1\n" \
    "buffer_load_dwordx4 v[252:255], v0, s[8:11], s40 offen offset:3072 sc1\n" \
    "s_and_b32 s33, s32, s23\n" \
    "s_mul_i32 s45, s33, s24\n" \
    "s_cmp_lt_u32 s33, 64-%[pdist]\n" \
    "s_cselect_b32 s45, s45, 0x80000000\n" \
    "s_add_u32 s44, s43, %[x1]\n" \
    "s_xor_b32 s51, s51, 16384\n" \
    "s_waitcnt vmcnt(15)\n" \
    "v_max3_u32 v19, v44, v45, v46\n" \
    "s_waitcnt vmcnt(14)\n" \
    "v_max3_u32 v19, v19, v47, v48\n" \
    "v_max3_u32 v19, v19, v49, v50\n" \
    "v_max3_u32 v19, v19, v51, v52\n" \
    "v_max3_u32 v19, v19, v53, v54\n" \
    "s_waitcnt vmcnt(13)\n" \
    "v_max3_u32 v19, v19, v55, v56\n" \
    "v_max3_u32 v19, v19, v57, v58\n" \
    "s_waitcnt vmcnt(11)\n" \
    "v_max3_u32 v19, v19, v59, v60\n" \
    "v_max3_u32 v19, v19, v61, v62\n" \
    "s_waitcnt vmcnt(10)\n" \
    "v_max3_u32 v19, v19, v63, v64\n" \
    "v_max3_u32 v19, v19, v65, v66\n" \
    "s_waitcnt vmcnt(9)\n" \
    "v_max3_u32 v19, v19, v67, v68\n" \
    "v_max3_u32 v19, v19, v69, v70\n" \
    "s_waitcnt vmcnt(8)\n" \
    "v_max3_u32 v19, v19, v71, v72\n" \
    "v_max3_u32 v19, v19, v73, v74\n" \
    "v_max_u32 v19, v19, v75\n" \
    "v_cmp_eq_u32 vcc, -1, v19\n" \
    "s_nop 0\n" \
    "s_cbranch_vccnz .Lr_ctA0_%=\n" \
    ".Lr_ctrA0_%=:\n" \
    "v_mfma_f32_16x16x32_bf16 v[88:91], v[36:39], v[72:75], 0\n" \
    "v_mfma_f32_16x16x32_bf16 v[88:91], v[192:195], v[56:59], v[88:91]\n" \
    "v_mfma_f32_16x16x32_bf16 v[88:91], v[196:199], v[60:63], v[88:91]\n" \
    "v_mfma_f32_16x16x32_bf16 v[88:91], v[200:203], v[64:67], v[88:91]\n" \
    "v_mfma_f32_16x16x32_bf16 v[88:91], v[204:207], v[68:71], v[88:91]\n" \
    "v_mfma_f32_16x16x32_bf16 v[92:95], v[40:43], v[72:75], 0\n" \
    "v_mfma_f32_16x16x32_bf16 v[92:95], v[208:211], v[56:59], v[92:95]\n" \
    "v_mfma_f32_16x16x32_bf16 v[92:95], v[212:215], v[60:63], v[92:95]\n" \
    "v_mfma_f32_16x16x32_bf16 v[92:95], v[216:219], v[64:67], v[92:95]\n" \
    "v_mfma_f32_16x16x32_bf16 v[92:95], v[220:223], v[68:71], v[92:95]\n" \
    "v_mfma_f32_16x16x32_bf16 v[80:83], v[36:39], v[52:55], 0\n" \
    "v_mfma_f32_16x16x32_bf16 v[84:87], v[40:43], v[52:55], 0\n" \
    "v_mov_b32 v193, v194\n" \
    "v_mov_b32 v194, v196\n" \
    "v_mov_b32 v195, v198\n" \
    "v_mov_b32 v201, v202\n" \
    "v_mov_b32 v202, v204\n" \
    "v_mov_b32 v203, v206\n" \
    "v_mov_b32 v209, v210\n" \
    "v_mov_b32 v210, v212\n" \
    "v_mov_b32 v211, v214\n" \
    "v_mov_b32 v217, v218\n" \
    "v_mov_b32 v218, v220\n" \
    "v_mov_b32 v219, v222\n" \
    "ds_write_b128 v8, v[88:91] offset:2048\n" \
    "ds_write_b128 v8, v[92:95] offset:3072\n" \
    "v_permlane16_swap_b32 v192, v194\n" \
    "v_permlane16_swap_b32 v193, v195\n" \
    "v_permlane16_swap_b32 v200, v202\n" \
    "v_permlane16_swap_b32 v201, v203\n" \
    "v_permlane16_swap_b32 v208, v210\n" \
    "v_permlane16_swap_b32 v209, v211\n" \
    "v_permlane16_swap_b32 v216, v218\n" \
    "v_permlane16_swap_b32 v217, v219\n" \
    "v_mfma_f32_16x16x32_bf16 v[80:83], v[192:195], v[44:47], v[80:83]\n" \
    "v_mfma_f32_16x16x32_bf16 v[84:87], v[208:211], v[44:47], v[84:87]\n" \
    "v_mfma_f32_16x16x32_bf16 v[80:83], v[200:203], v[48:51], v[80:83]\n" \
    "v_mfma_f32_16x16x32_bf16 v[84:87], v[216:219], v[48:51], v[84:87]\n" \
    "s_nop 6\n" \
    "ds_write_b128 v8, v[80:83] offset:0\n" \
    "ds_write_b128 v8, v[84:87] offset:1024\n" \
    "s_waitcnt lgkmcnt(0)\n" \
    "s_barrier\n" \
    "s_mov_b32 s55, s62\n" \
    "s_add_u32 s64, s62, %[dh]\n" \
    "s_branch .Lr_hA0b_%=\n" \
    ".Lr_hA0a_%=:\n" \
    "s_sleep 1\n" \
    ".Lr_hA0b_%=:\n" \
    "s_memrealtime vcc\n" \
    "s_waitcnt lgkmcnt(0)\n" \
    "s_sub_i32 vcc_lo, s64, vcc_lo\n" \
    "s_cmp_gt_i32 vcc_lo, 8\n" \
    "s_cbranch_scc1 .Lr_hA0a_%=\n" \
    "s_cmp_gt_i32 vcc_lo, 0\n" \
    "s_cbranch_scc1 .Lr_hA0b_%=\n" \
    "buffer_load_dwordx2 v[16:17], v3, s[8:11], s51 offen sc1\n" \
    "s_cmp_eq_u32 s54, 0\n" \
    "s_cbranch_scc1 .Lr_cbA0_%=\n" \
    ".Lr_cbrA0_%=:\n" \
    "s_add_u32 s32, s32, 1\n" \
    "s_add_u32 s49, s32, 1\n" \
    "s_add_u32 s36, s18, s46\n" \
    "s_add_u32 s37, s36, 16384\n" \
    "s_and_b32 s33, s32, 3\n" \
    "s_lshl_b32 s35, s33, 16\n" \
    "s_add_u32 s35, s35, s17\n" \
    ".Lr_tvA1_%=:\n" \
    "s_waitcnt vmcnt(0)\n" \
    "v_cmp_ne_u32 vcc, s49, v17\n" \
    "v_subrev_u32 v19, s55, v16\n" \
    "s_and_b64 vcc, vcc, s[56:57]\n" \
    "s_cbranch_scc1 .Lr_ctvA1_%=\n" \
    "v_max_i32_dpp v19, v19, v19 quad_perm:[1,0,3,2] row_mask:0xf bank_mask:0xf bound_ctrl:1\n" \
    "s_nop 1\n" \
    "v_max_i32_dpp v19, v19, v19 quad_perm:[2,3,0,1] row_mask:0xf bank_mask:0xf bound_ctrl:1\n" \
    "s_nop 0\n" \
    "v_readlane_b32 s48, v19, 4\n" \
    "v_readlane_b32 s47, v19, 0\n" \
    "s_add_u32 s48, s48, s55\n" \
    "s_add_u32 s48, s48, s22\n" \
    ".Lr_goA1_%=:\n" \
    "s_branch .Lr_uA1b_%=\n" \
    ".Lr_uA1a_%=:\n" \
    "s_sleep 1\n" \
    ".Lr_uA1b_%=:\n" \
    "s_memrealtime vcc\n" \
    "s_waitcnt lgkmcnt(0)\n" \
    "s_sub_i32 vcc_lo, s48, vcc_lo\n" \
    "s_cmp_gt_i32 vcc_lo, 8\n" \
    "s_cbranch_scc1 .Lr_uA1a_%=\n" \
    "s_cmp_gt_i32 vcc_lo, 0\n" \
    "s_cbranch_scc1 .Lr_uA1b_%=\n" \
    "buffer_load_dwordx4 v[36:39], v0, s[8:11], s36 offen sc1\n" \
    "buffer_load_dwordx4 v[40:43], v0, s[8:11], s37 offen sc1\n" \
    "s_add_u32 s47, s47, s55\n" \
    "s_add_u32 s47, s47, %[dr]\n" \
    "s_branch .Lr_pA1b_%=\n" \
    ".Lr_pA1a_%=:\n" \
    "s_sleep 1\n" \
    ".Lr_pA1b_%=:\n" \
    "s_memrealtime vcc\n" \
    "s_waitcnt lgkmcnt(0)\n" \
    "s_sub_i32 vcc_lo, s47, vcc_lo\n" \
    "s_cmp_gt_i32 vcc_lo, 8\n" \
    "s_cbranch_scc1 .Lr_pA1a_%=\n" \
    "s_cmp_gt_i32 vcc_lo, 0\n" \
    "s_cbranch_scc1 .Lr_pA1b_%=\n" \
    "buffer_load_dwordx4 v[32:35], v4, s[8:11], s35 offen sc1\n" \
    "buffer_load_dwordx4 v[24:27], v0, s[8:11], s35 offen sc1\n" \
    "buffer_load_dwordx4 v[28:31], v0, s[8:11], s35 offen offset:1024 sc1\n" \
    "s_and_b32 s33, s32, 3\n" \
    "s_lshl_b32 s61, s33, 17\n" \
    "s_add_u32 s41, s61, s20\n" \
    "s_add_u32 s42, s41, 4096\n" \
    "s_and_b32 s33, s49, %[brn]-1\n" \
    "s_mul_i32 s39, s33, 2621440\n" \
    "s_add_u32 s39, s39, s19\n" \
    "s_lshr_b32 s33, s39, 8\n" \
    "s_xor_b32 s33, s33, s27\n" \
    "s_and_b32 s33, s33, 0x2000\n" \
    "s_or_b32 s39, s39, s33\n" \
    "s_barrier\n" \
    "buffer_load_dword v18, v2, s[8:11], 0 offen sc1\n" \
    "buffer_load_dwordx4 v[52:55], v0, s[8:11], s41 offen offset:2048 sc1\n" \
    "buffer_load_dwordx4 v[44:47], v0, s[8:11], s41 offen offset:0 sc1\n" \
    "buffer_load_dwordx4 v[48:51], v0, s[8:11], s41 offen offset:1024 sc1\n" \
    "buffer_load_dwordx4 v[56:59], v0, s[8:11], s41 offen offset:3072 sc1\n" \
    "s_add_u32 s46, s46, 655360\n" \
    "s_cmp_eq_u32 s46, 1966080\n" \
    "s_cselect_b32 s46, 0, s46\n" \
    "s_and_b32 s33, s49, 3\n" \
    "s_lshl_b32 s38, s33, 15\n" \
    "s_mul_i32 s43, s32, 40960\n" \
    "s_add_u32 s43, s43, s21\n" \
    "s_cmp_eq_u32 s32, 63\n" \
    "s_cselect_b32 s38, 0x80000000, s38\n" \
    "s_waitcnt vmcnt(9)\n" \
    "v_max3_u32 v19, v36, v37, v38\n" \
    "s_waitcnt vmcnt(8)\n" \
    "v_max3_u32 v19, v19, v39, v40\n" \
    "v_max3_u32 v19, v19, v41, v42\n" \
    "v_max_u32 v19, v19, v43\n" \
    "s_waitcnt vmcnt(7)\n" \
    "v_mfma_f32_16x16x32_bf16 v[80:83], v[32:35], v[36:39], 0\n" \
    "v_max3_u32 v20, v32, v33, v34\n" \
    "v_mfma_f32_16x16x32_bf16 v[84:87], v[32:35], v[40:43], 0\n" \
    "v_max_u32 v20, v20, v35\n" \
    "s_waitcnt vmcnt(6)\n" \
    "v_smfmac_f32_16x16x64_bf16 v[80:83], v[24:27], v[224:231], v10\n" \
    "v_smfmac_f32_16x16x64_bf16 v[84:87], v[24:27], v[240:247], v10\n" \
    "v_max3_u32 v19, v19, v24, v25\n" \
    "v_max3_u32 v19, v19, v26, v27\n" \
    "s_waitcnt vmcnt(5)\n" \
    "v_smfmac_f32_16x16x64_bf16 v[80:83], v[28:31], v[232:239], v10\n" \
    "v_smfmac_f32_16x16x64_bf16 v[84:87], v[28:31], v[248:255], v10\n" \
    "v_max3_u32 v20, v20, v28, v29\n" \
    "v_max3_u32 v20, v20, v30, v31\n" \
    "v_max_u32 v19, v19, v20\n" \
    "v_cmp_eq_u32 vcc, -1, v19\n" \
    "s_nop 0\n" \
    "s_cbranch_vccnz .Lr_cpA1_%=\n" \
    ".Lr_cprA1_%=:\n" \
    "s_nop 0\n" \
    "v_pk_add_f32 v[80:81], v[80:81], v[82:83]\n" \
    "v_pk_add_f32 v[84:85], v[84:85], v[86:87]\n" \
    "ds_write2_b32 v5, v80, v81 offset0:0 offset1:4\n" \
    "ds_write2_b32 v5, v84, v85 offset0:64 offset1:68\n" \
    "s_waitcnt lgkmcnt(0)\n" \
    "s_barrier\n" \
    "ds_read_b128 v[96:99], v0 offset:0\n" \
    "ds_read_b128 v[100:103], v0 offset:1024\n" \
    "ds_read_b128 v[104:107], v0 offset:2048\n" \
    "ds_read_b128 v[108:111], v0 offset:3072\n" \
    "ds_read_b128 v[112:115], v0 offset:4096\n" \
    "ds_read_b128 v[116:119], v0 offset:5120\n" \
    "ds_read_b128 v[120:123], v0 offset:6144\n" \
    "ds_read_b128 v[124:127], v0 offset:7168\n" \
    "s_waitcnt lgkmcnt(6)\n" \
    "v_pk_add_f32 v[96:97], v[96:97], v[100:101]\n" \
    "v_pk_add_f32 v[98:99], v[98:99], v[102:103]\n" \
    "s_waitcnt lgkmcnt(4)\n" \
    "v_pk_add_f32 v[104:105], v[104:105], v[108:109]\n" \
    "v_pk_add_f32 v[106:107], v[106:107], v[110:111]\n" \
    "s_waitcnt lgkmcnt(2)\n" \
    "v_pk_add_f32 v[112:113], v[112:113], v[116:117]\n" \
    "v_pk_add_f32 v[114:115], v[114:115], v[118:119]\n" \
    "v_pk_add_f32 v[96:97], v[96:97], v[104:105]\n" \
    "v_pk_add_f32 v[98:99], v[98:99], v[106:107]\n" \
    "s_waitcnt lgkmcnt(0)\n" \
    "v_pk_add_f32 v[120:121], v[120:121], v[124:125]\n" \
    "v_pk_add_f32 v[122:123], v[122:123], v[126:127]\n" \
    "v_pk_add_f32 v[96:97], v[96:97], v[112:113]\n" \
    "v_pk_add_f32 v[98:99], v[98:99], v[114:115]\n" \
    "v_pk_add_f32 v[96:97], v[96:97], v[120:121]\n" \
    "v_pk_add_f32 v[98:99], v[98:99], v[122:123]\n" \
    "v_cvt_pk_bf16_f32 v96, v96, v97\n" \
    "v_cvt_pk_bf16_f32 v97, v98, v99\n" \
    "buffer_store_dwordx2 v[96:97], v1, s[8:11], s38 offen sc1\n" \
    "s_barrier\n" \
    "s_memrealtime s[62:63]\n" \
    "buffer_load_dwordx4 v[60:63], v0, s[8:11], s42 offen offset:0 sc1\n" \
    "buffer_load_dwordx4 v[64:67], v0, s[8:11], s42 offen offset:1024 sc1\n" \
    "buffer_load_dwordx4 v[68:71], v0, s[8:11], s42 offen offset:2048 sc1\n" \
    "buffer_load_dwordx4 v[72:75], v0, s[8:11], s42 offen offset:3072 sc1\n" \
    "s_waitcnt vmcnt(9)\n" \
    "v_cmp_ge_u32 vcc, s49, v18\n" \
    "s_nop 0\n" \
    "s_and_b64 vcc, vcc, s[56:57]\n" \
    "s_cselect_b32 s54, 0, 1\n" \
    "s_cmp_eq_u32 s32, 63\n" \
    "s_cselect_b32 s54, 2, s54\n" \
    "s_cmp_eq_u32 s54, 1\n" \
    "s_cselect_b32 s39, s39, 0x80000000\n" \
    "s_add_u32 s40, s39, 65536\n" \
    "s_barrier\n" \
    "buffer_load_dwordx4 v[192:195], v0, s[8:11], s39 offen offset:0 sc1\n" \
    "buffer_load_dwordx4 v[196:199], v0, s[8:11], s39 offen offset:1024 sc1\n" \
    "buffer_load_dwordx4 v[200:203], v0, s[8:11], s39 offen offset:2048 sc1\n" \
    "buffer_load_dwordx4 v[204:207], v0, s[8:11], s39 offen offset:3072 sc1\n" \
    "buffer_load_dwordx4 v[208:211], v0, s[8:11], s40 offen offset:0 sc1\n" \
    "buffer_load_dwordx4 v[212:215], v0, s[8:11], s40 offen offset:1024 sc1\n" \
    "buffer_load_dwordx4 v[216:219], v0, s[8:11], s40 offen offset:2048 sc1\n" \
    "buffer_load_dwordx4 v[220:223], v0, s[8:11], s40 offen offset:3072 sc1\n" \
    "s_and_b32 s33, s32, s23\n" \
    "s_mul_i32 s45, s33, s24\n" \
    "s_cmp_lt_u32 s33, 64-%[pdist]\n" \
    "s_cselect_b32 s45, s45, 0x80000000\n" \
    "s_add_u32 s44, s43, %[x1]\n" \
    "s_xor_b32 s51, s51, 16384\n" \
    "s_waitcnt vmcnt(15)\n" \
    "v_max3_u32 v19, v44, v45, v46\n" \
    "s_waitcnt vmcnt(14)\n" \
    "v_max3_u32 v19, v19, v47, v48\n" \
    "v_max3_u32 v19, v19, v49, v50\n" \
    "v_max3_u32 v19, v19, v51, v52\n" \
    "v_max3_u32 v19, v19, v53, v54\n" \
    "s_waitcnt vmcnt(13)\n" \
    "v_max3_u32 v19, v19, v55, v56\n" \
    "v_max3_u32 v19, v19, v57, v58\n" \
    "s_waitcnt vmcnt(11)\n" \
    "v_max3_u32 v19, v19, v59, v60\n" \
    "v_max3_u32 v19, v19, v61, v62\n" \
    "s_waitcnt vmcnt(10)\n" \
    "v_max3_u32 v19, v19, v63, v64\n" \
    "v_max3_u32 v19, v19, v65, v66\n" \
    "s_waitcnt vmcnt(9)\n" \
    "v_max3_u32 v19, v19, v67, v68\n" \
    "v_max3_u32 v19, v19, v69, v70\n" \
    "s_waitcnt vmcnt(8)\n" \
    "v_max3_u32 v19, v19, v71, v72\n" \
    "v_max3_u32 v19, v19, v73, v74\n" \
    "v_max_u32 v19, v19, v75\n" \
    "v_cmp_eq_u32 vcc, -1, v19\n" \
    "s_nop 0\n" \
    "s_cbranch_vccnz .Lr_ctA1_%=\n" \
    ".Lr_ctrA1_%=:\n" \
    "v_mfma_f32_16x16x32_bf16 v[88:91], v[36:39], v[72:75], 0\n" \
    "v_mfma_f32_16x16x32_bf16 v[88:91], v[224:227], v[56:59], v[88:91]\n" \
    "v_mfma_f32_16x16x32_bf16 v[88:91], v[228:231], v[60:63], v[88:91]\n" \
    "v_mfma_f32_16x16x32_bf16 v[88:91], v[232:235], v[64:67], v[88:91]\n" \
    "v_mfma_f32_16x16x32_bf16 v[88:91], v[236:239], v[68:71], v[88:91]\n" \
    "v_mfma_f32_16x16x32_bf16 v[92:95], v[40:43], v[72:75], 0\n" \
    "v_mfma_f32_16x16x32_bf16 v[92:95], v[240:243], v[56:59], v[92:95]\n" \
    "v_mfma_f32_16x16x32_bf16 v[92:95], v[244:247], v[60:63], v[92:95]\n" \
    "v_mfma_f32_16x16x32_bf16 v[92:95], v[248:251], v[64:67], v[92:95]\n" \
    "v_mfma_f32_16x16x32_bf16 v[92:95], v[252:255], v[68:71], v[92:95]\n" \
    "v_mfma_f32_16x16x32_bf16 v[80:83], v[36:39], v[52:55], 0\n" \
    "v_mfma_f32_16x16x32_bf16 v[84:87], v[40:43], v[52:55], 0\n" \
    "v_mov_b32 v225, v226\n" \
    "v_mov_b32 v226, v228\n" \
    "v_mov_b32 v227, v230\n" \
    "v_mov_b32 v233, v234\n" \
    "v_mov_b32 v234, v236\n" \
    "v_mov_b32 v235, v238\n" \
    "v_mov_b32 v241, v242\n" \
    "v_mov_b32 v242, v244\n" \
    "v_mov_b32 v243, v246\n" \
    "v_mov_b32 v249, v250\n" \
    "v_mov_b32 v250, v252\n" \
    "v_mov_b32 v251, v254\n" \
    "ds_write_b128 v8, v[88:91] offset:2048\n" \
    "ds_write_b128 v8, v[92:95] offset:3072\n" \
    "v_permlane16_swap_b32 v224, v226\n" \
    "v_permlane16_swap_b32 v225, v227\n" \
    "v_permlane16_swap_b32 v232, v234\n" \
    "v_permlane16_swap_b32 v233, v235\n" \
    "v_permlane16_swap_b32 v240, v242\n" \
    "v_permlane16_swap_b32 v241, v243\n" \
    "v_permlane16_swap_b32 v248, v250\n" \
    "v_permlane16_swap_b32 v249, v251\n" \
    "v_mfma_f32_16x16x32_bf16 v[80:83], v[224:227], v[44:47], v[80:83]\n" \
    "v_mfma_f32_16x16x32_bf16 v[84:87], v[240:243], v[44:47], v[84:87]\n" \
    "v_mfma_f32_16x16x32_bf16 v[80:83], v[232:235], v[48:51], v[80:83]\n" \
    "v_mfma_f32_16x16x32_bf16 v[84:87], v[248:251], v[48:51], v[84:87]\n" \
    "s_nop 6\n" \
    "ds_write_b128 v8, v[80:83] offset:0\n" \
    "ds_write_b128 v8, v[84:87] offset:1024\n" \
    "s_waitcnt lgkmcnt(0)\n" \
    "s_barrier\n" \
    "s_mov_b32 s55, s62\n" \
    "s_add_u32 s64, s62, %[dh]\n" \
    "s_branch .Lr_hA1b_%=\n" \
    ".Lr_hA1a_%=:\n" \
    "s_sleep 1\n" \
    ".Lr_hA1b_%=:\n" \
    "s_memrealtime vcc\n" \
    "s_waitcnt lgkmcnt(0)\n" \
    "s_sub_i32 vcc_lo, s64, vcc_lo\n" \
    "s_cmp_gt_i32 vcc_lo, 8\n" \
    "s_cbranch_scc1 .Lr_hA1a_%=\n" \
    "s_cmp_gt_i32 vcc_lo, 0\n" \
    "s_cbranch_scc1 .Lr_hA1b_%=\n" \
    "buffer_load_dwordx2 v[16:17], v3, s[8:11], s51 offen sc1\n" \
    "s_cmp_eq_u32 s54, 0\n" \
    "s_cbranch_scc1 .Lr_cbA1_%=\n" \
    ".Lr_cbrA1_%=:\n" \
    "s_add_u32 s32, s32, 1\n" \
    "s_cmp_lt_u32 s32, 64\n" \
    "s_cbranch_scc1 .Lr_lA_%=\n" \
    "s_waitcnt vmcnt(0)\n" \
    "s_endpgm\n" \
    ".Lr_lB_%=:\n" \
    "s_add_u32 s49, s32, 1\n" \
    "s_add_u32 s36, s18, s46\n" \
    "s_add_u32 s37, s36, 16384\n" \
    "s_and_b32 s33, s32, 3\n" \
    "s_lshl_b32 s35, s33, 16\n" \
    "s_add_u32 s35, s35, s17\n" \
    ".Lr_tvB0_%=:\n" \
    "s_waitcnt vmcnt(0)\n" \
    "v_cmp_ne_u32 vcc, s49, v17\n" \
    "v_subrev_u32 v19, s55, v16\n" \
    "s_and_b64 vcc, vcc, s[56:57]\n" \
    "s_cbranch_scc1 .Lr_ctvB0_%=\n" \
    "v_max_i32_dpp v19, v19, v19 quad_perm:[1,0,3,2] row_mask:0xf bank_mask:0xf bound_ctrl:1\n" \
    "s_nop 1\n" \
    "v_max_i32_dpp v19, v19, v19 quad_perm:[2,3,0,1] row_mask:0xf bank_mask:0xf bound_ctrl:1\n" \
    "s_nop 0\n" \
    "v_readlane_b32 s48, v19, 4\n" \
    "v_readlane_b32 s47, v19, 0\n" \
    "s_add_u32 s48, s48, s55\n" \
    "s_add_u32 s48, s48, s22\n" \
    ".Lr_goB0_%=:\n" \
    "s_branch .Lr_uB0b_%=\n" \
    ".Lr_uB0a_%=:\n" \
    "s_sleep 1\n" \
    ".Lr_uB0b_%=:\n" \
    "s_memrealtime vcc\n" \
    "s_waitcnt lgkmcnt(0)\n" \
    "s_sub_i32 vcc_lo, s48, vcc_lo\n" \
    "s_cmp_gt_i32 vcc_lo, 8\n" \
    "s_cbranch_scc1 .Lr_uB0a_%=\n" \
    "s_cmp_gt_i32 vcc_lo, 0\n" \
    "s_cbranch_scc1 .Lr_uB0b_%=\n" \
    "buffer_load_dwordx4 v[36:39], v0, s[8:11], s36 offen sc1\n" \
    "buffer_load_dwordx4 v[40:43], v0, s[8:11], s37 offen sc1\n" \
    "s_add_u32 s47, s47, s55\n" \
    "s_add_u32 s47, s47, %[dr]\n" \
    "s_branch .Lr_pB0b_%=\n" \
    ".Lr_pB0a_%=:\n" \
    "s_sleep 1\n" \
    ".Lr_pB0b_%=:\n" \
    "s_memrealtime vcc\n" \
    "s_waitcnt lgkmcnt(0)\n" \
    "s_sub_i32 vcc_lo, s47, vcc_lo\n" \
    "s_cmp_gt_i32 vcc_lo, 8\n" \
    "s_cbranch_scc1 .Lr_pB0a_%=\n" \
    "s_cmp_gt_i32 vcc_lo, 0\n" \
    "s_cbranch_scc1 .Lr_pB0b_%=\n" \
    "buffer_load_dwordx4 v[32:35], v4, s[8:11], s35 offen sc1\n" \
    "buffer_load_dwordx4 v[24:27], v0, s[8:11], s35 offen sc1\n" \
    "buffer_load_dwordx4 v[28:31], v0, s[8:11], s35 offen offset:1024 sc1\n" \
    "s_and_b32 s33, s32, 3\n" \
    "s_lshl_b32 s61, s33, 17\n" \
    "s_add_u32 s41, s61, s20\n" \
    "s_add_u32 s42, s41, 4096\n" \
    "s_and_b32 s33, s49, %[brn]-1\n" \
    "s_mul_i32 s39, s33, 2621440\n" \
    "s_add_u32 s39, s39, s19\n" \
    "s_lshr_b32 s33, s39, 8\n" \
    "s_xor_b32 s33, s33, s27\n" \
    "s_and_b32 s33, s33, 0x2000\n" \
    "s_or_b32 s39, s39, s33\n" \
    "s_barrier\n" \
    "buffer_load_dword v18, v2, s[8:11], 0 offen sc1\n" \
    "buffer_load_dwordx4 v[52:55], v0, s[8:11], s41 offen offset:2048 sc1\n" \
    "buffer_load_dwordx4 v[44:47], v0, s[8:11], s41 offen offset:0 sc1\n" \
    "buffer_load_dwordx4 v[48:51], v0, s[8:11], s41 offen offset:1024 sc1\n" \
    "buffer_load_dwordx4 v[56:59], v0, s[8:11], s41 offen offset:3072 sc1\n" \
    "s_add_u32 s46, s46, 655360\n" \
    "s_cmp_eq_u32 s46, 1966080\n" \
    "s_cselect_b32 s46, 0, s46\n" \
    "s_and_b32 s33, s49, 3\n" \
    "s_lshl_b32 s38, s33, 15\n" \
    "s_mul_i32 s43, s32, 40960\n" \
    "s_add_u32 s43, s43, s21\n" \
    "s_cmp_eq_u32 s32, 63\n" \
    "s_cselect_b32 s38, 0x80000000, s38\n" \
    "s_waitcnt vmcnt(9)\n" \
    "v_max3_u32 v19, v36, v37, v38\n" \
    "s_waitcnt vmcnt(8)\n" \
    "v_max3_u32 v19, v19, v39, v40\n" \
    "v_max3_u32 v19, v19, v41, v42\n" \
    "v_max_u32 v19, v19, v43\n" \
    "s_waitcnt vmcnt(7)\n" \
    "v_mfma_f32_16x16x32_bf16 v[80:83], v[32:35], v[36:39], 0\n" \
    "v_max3_u32 v20, v32, v33, v34\n" \
    "v_mfma_f32_16x16x32_bf16 v[84:87], v[32:35], v[40:43], 0\n" \
    "v_max_u32 v20, v20, v35\n" \
    "s_waitcnt vmcnt(6)\n" \
    "v_smfmac_f32_16x16x64_bf16 v[80:83], v[24:27], v[192:199], v10\n" \
    "v_smfmac_f32_16x16x64_bf16 v[84:87], v[24:27], v[208:215], v10\n" \
    "v_max3_u32 v19, v19, v24, v25\n" \
    "v_max3_u32 v19, v19, v26, v27\n" \
    "s_waitcnt vmcnt(5)\n" \
    "v_smfmac_f32_16x16x64_bf16 v[80:83], v[28:31], v[200:207], v10\n" \
    "v_smfmac_f32_16x16x64_bf16 v[84:87], v[28:31], v[216:223], v10\n" \
    "v_max3_u32 v20, v20, v28, v29\n" \
    "v_max3_u32 v20, v20, v30, v31\n" \
    "v_max_u32 v19, v19, v20\n" \
    "v_cmp_eq_u32 vcc, -1, v19\n" \
    "s_nop 0\n" \
    "s_cbranch_vccnz .Lr_cpB0_%=\n" \
    ".Lr_cprB0_%=:\n" \
    "s_nop 0\n" \
    "v_pk_add_f32 v[80:81], v[80:81], v[82:83]\n" \
    "v_pk_add_f32 v[84:85], v[84:85], v[86:87]\n" \
    "ds_write2_b32 v5, v80, v81 offset0:0 offset1:4\n" \
    "ds_write2_b32 v5, v84, v85 offset0:64 offset1:68\n" \
    "s_waitcnt lgkmcnt(0)\n" \
    "s_barrier\n" \
    "s_barrier\n" \
    "s_memrealtime s[62:63]\n" \
    "buffer_load_dwordx4 v[60:63], v0, s[8:11], s42 offen offset:0 sc1\n" \
    "buffer_load_dwordx4 v[64:67], v0, s[8:11], s42 offen offset:1024 sc1\n" \
    "buffer_load_dwordx4 v[68:71], v0, s[8:11], s42 offen offset:2048 sc1\n" \
    "buffer_load_dwordx4 v[72:75], v0, s[8:11], s42 offen offset:3072 sc1\n" \
    "s_waitcnt vmcnt(8)\n" \
    "v_cmp_ge_u32 vcc, s49, v18\n" \
    "s_nop 0\n" \
    "s_and_b64 vcc, vcc, s[56:57]\n" \
    "s_cselect_b32 s54, 0, 1\n" \
    "s_cmp_eq_u32 s32, 63\n" \
    "s_cselect_b32 s54, 2, s54\n" \
    "s_cmp_eq_u32 s54, 1\n" \
    "s_cselect_b32 s39, s39, 0x80000000\n" \
    "s_add_u32 s40, s39, 65536\n" \
    "s_barrier\n" \
    "s_add_u32 s33, s43, %[x0]\n" \
    "buffer_load_dwordx4 v[76:79], v6, s[8:11], s33 offen sc1\n" \
    "buffer_load_dwordx4 v[224:227], v0, s[8:11], s39 offen offset:0 sc1\n" \
    "buffer_load_dwordx4 v[228:231], v0, s[8:11], s39 offen offset:1024 sc1\n" \
    "buffer_load_dwordx4 v[232:235], v0, s[8:11], s39 offen offset:2048 sc1\n" \
    "buffer_load_dwordx4 v[236:239], v0, s[8:11], s39 offen offset:3072 sc1\n" \
    "buffer_load_dwordx4 v[240:243], v0, s[8:11], s40 offen offset:0 sc1\n" \
    "buffer_load_dwordx4 v[244:247], v0, s[8:11], s40 offen offset:1024 sc1\n" \
    "buffer_load_dwordx4 v[248:251], v0, s[8:11], s40 offen offset:2048 sc1\n" \
    "buffer_load_dwordx4 v[252:255], v0, s[8:11], s40 offen offset:3072 sc1\n" \
    "s_and_b32 s33, s32, s23\n" \
    "s_mul_i32 s45, s33, s24\n" \
    "s_cmp_lt_u32 s33, 64-%[pdist]\n" \
    "s_cselect_b32 s45, s45, 0x80000000\n" \
    "s_add_u32 s44, s43, %[x1]\n" \
    "s_xor_b32 s51, s51, 16384\n" \
    "s_waitcnt vmcnt(15)\n" \
    "v_max3_u32 v19, v44, v45, v46\n" \
    "s_waitcnt vmcnt(14)\n" \
    "v_max3_u32 v19, v19, v47, v48\n" \
    "v_max3_u32 v19, v19, v49, v50\n" \
    "v_max3_u32 v19, v19, v51, v52\n" \
    "v_max3_u32 v19, v19, v53, v54\n" \
    "s_waitcnt vmcnt(13)\n" \
    "v_max3_u32 v19, v19, v55, v56\n" \
    "v_max3_u32 v19, v19, v57, v58\n" \
    "s_waitcnt vmcnt(12)\n" \
    "v_max3_u32 v19, v19, v59, v60\n" \
    "v_max3_u32 v19, v19, v61, v62\n" \
    "s_waitcnt vmcnt(11)\n" \
    "v_max3_u32 v19, v19, v63, v64\n" \
    "v_max3_u32 v19, v19, v65, v66\n" \
    "s_waitcnt vmcnt(10)\n" \
    "v_max3_u32 v19, v19, v67, v68\n" \
    "v_max3_u32 v19, v19, v69, v70\n" \
    "s_waitcnt vmcnt(9)\n" \
    "v_max3_u32 v19, v19, v71, v72\n" \
    "v_max3_u32 v19, v19, v73, v74\n" \
    "v_max_u32 v19, v19, v75\n" \
    "v_cmp_eq_u32 vcc, -1, v19\n" \
    "s_nop 0\n" \
    "s_cbranch_vccnz .Lr_ctB0_%=\n" \
    ".Lr_ctrB0_%=:\n" \
    "v_mfma_f32_16x16x32_bf16 v[88:91], v[36:39], v[72:75], 0\n" \
    "v_mfma_f32_16x16x32_bf16 v[88:91], v[192:195], v[56:59], v[88:91]\n" \
    "v_mfma_f32_16x16x32_bf16 v[88:91], v[196:199], v[60:63], v[88:91]\n" \
    "v_mfma_f32_16x16x32_bf16 v[88:91], v[200:203], v[64:67], v[88:91]\n" \
    "v_mfma_f32_16x16x32_bf16 v[88:91], v[204:207], v[68:71], v[88:91]\n" \
    "v_mfma_f32_16x16x32_bf16 v[92:95], v[40:43], v[72:75], 0\n" \
    "v_mfma_f32_16x16x32_bf16 v[92:95], v[208:211], v[56:59], v[92:95]\n" \
    "v_mfma_f32_16x16x32_bf16 v[92:95], v[212:215], v[60:63], v[92:95]\n" \
    "v_mfma_f32_16x16x32_bf16 v[92:95], v[216:219], v[64:67], v[92:95]\n" \
    "v_mfma_f32_16x16x32_bf16 v[92:95], v[220:223], v[68:71], v[92:95]\n" \
    "v_mfma_f32_16x16x32_bf16 v[80:83], v[36:39], v[52:55], 0\n" \
    "v_mfma_f32_16x16x32_bf16 v[84:87], v[40:43], v[52:55], 0\n" \
    "v_mov_b32 v193, v194\n" \
    "v_mov_b32 v194, v196\n" \
    "v_mov_b32 v195, v198\n" \
    "v_mov_b32 v201, v202\n" \
    "v_mov_b32 v202, v204\n" \
    "v_mov_b32 v203, v206\n" \
    "v_mov_b32 v209, v210\n" \
    "v_mov_b32 v210, v212\n" \
    "v_mov_b32 v211, v214\n" \
    "v_mov_b32 v217, v218\n" \
    "v_mov_b32 v218, v220\n" \
    "v_mov_b32 v219, v222\n" \
    "ds_write_b128 v8, v[88:91] offset:2048\n" \
    "ds_write_b128 v8, v[92:95] offset:3072\n" \
    "v_permlane16_swap_b32 v192, v194\n" \
    "v_permlane16_swap_b32 v193, v195\n" \
    "v_permlane16_swap_b32 v200, v202\n" \
    "v_permlane16_swap_b32 v201, v203\n" \
    "v_permlane16_swap_b32 v208, v210\n" \
    "v_permlane16_swap_b32 v209, v211\n" \
    "v_permlane16_swap_b32 v216, v218\n" \
    "v_permlane16_swap_b32 v217, v219\n" \
    "v_mfma_f32_16x16x32_bf16 v[80:83], v[192:195], v[44:47], v[80:83]\n" \
    "v_mfma_f32_16x16x32_bf16 v[84:87], v[208:211], v[44:47], v[84:87]\n" \
    "v_mfma_f32_16x16x32_bf16 v[80:83], v[200:203], v[48:51], v[80:83]\n" \
    "v_mfma_f32_16x16x32_bf16 v[84:87], v[216:219], v[48:51], v[84:87]\n" \
    "s_nop 6\n" \
    "ds_write_b128 v8, v[80:83] offset:0\n" \
    "ds_write_b128 v8, v[84:87] offset:1024\n" \
    "s_waitcnt lgkmcnt(0)\n" \
    "s_barrier\n" \
    "s_mov_b32 s55, s62\n" \
    "ds_read_b128 v[96:99], v7 offset:0\n" \
    "ds_read_b128 v[100:103], v7 offset:256\n" \
    "ds_read_b128 v[104:107], v7 offset:4096\n" \
    "ds_read_b128 v[108:111], v7 offset:4352\n" \
    "ds_read_b128 v[112:115], v7 offset:8192\n" \
    "ds_read_b128 v[116:119], v7 offset:8448\n" \
    "ds_read_b128 v[120:123], v7 offset:12288\n" \
    "ds_read_b128 v[124:127], v7 offset:12544\n" \
    "ds_read_b128 v[128:131], v7 offset:16384\n" \
    "ds_read_b128 v[132:135], v7 offset:16640\n" \
    "ds_read_b128 v[136:139], v7 offset:20480\n" \
    "ds_read_b128 v[140:143], v7 offset:20736\n" \
    "ds_read_b128 v[144:147], v7 offset:24576\n" \
    "ds_read_b128 v[148:151], v7 offset:24832\n" \
    "ds_read_b128 v[152:155], v7 offset:28672\n" \
    "ds_read_b128 v[156:159], v7 offset:28928\n" \
    "s_waitcnt lgkmcnt(13)\n" \
    "v_pk_add_f32 v[96:97], v[96:97], v[104:105]\n" \
    "v_pk_add_f32 v[98:99], v[98:99], v[106:107]\n" \
    "s_waitcnt lgkmcnt(12)\n" \
    "v_pk_add_f32 v[100:101], v[100:101], v[108:109]\n" \
    "v_pk_add_f32 v[102:103], v[102:103], v[110:111]\n" \
    "s_waitcnt lgkmcnt(9)\n" \
    "v_pk_add_f32 v[112:113], v[112:113], v[120:121]\n" \
    "v_pk_add_f32 v[114:115], v[114:115], v[122:123]\n" \
    "s_waitcnt lgkmcnt(8)\n" \
    "v_pk_add_f32 v[116:117], v[116:117], v[124:125]\n" \
    "v_pk_add_f32 v[118:119], v[118:119], v[126:127]\n" \
    "v_pk_add_f32 v[96:97], v[96:97], v[112:113]\n" \
    "v_pk_add_f32 v[98:99], v[98:99], v[114:115]\n" \
    "v_pk_add_f32 v[100:101], v[100:101], v[116:117]\n" \
    "v_pk_add_f32 v[102:103], v[102:103], v[118:119]\n" \
    "s_waitcnt lgkmcnt(5)\n" \
    "v_pk_add_f32 v[128:129], v[128:129], v[136:137]\n" \
    "v_pk_add_f32 v[130:131], v[130:131], v[138:139]\n" \
    "s_waitcnt lgkmcnt(4)\n" \
    "v_pk_add_f32 v[132:133], v[132:133], v[140:141]\n" \
    "v_pk_add_f32 v[134:135], v[134:135], v[142:143]\n" \
    "s_waitcnt lgkmcnt(1)\n" \
    "v_pk_add_f32 v[144:145], v[144:145], v[152:153]\n" \
    "v_pk_add_f32 v[146:147], v[146:147], v[154:155]\n" \
    "s_waitcnt lgkmcnt(0)\n" \
    "v_pk_add_f32 v[148:149], v[148:149], v[156:157]\n" \
    "v_pk_add_f32 v[150:151], v[150:151], v[158:159]\n" \
    "v_pk_add_f32 v[128:129], v[128:129], v[144:145]\n" \
    "v_pk_add_f32 v[130:131], v[130:131], v[146:147]\n" \
    "v_pk_add_f32 v[132:133], v[132:133], v[148:149]\n" \
    "v_pk_add_f32 v[134:135], v[134:135], v[150:151]\n" \
    "v_pk_add_f32 v[96:97], v[96:97], v[128:129]\n" \
    "v_pk_add_f32 v[98:99], v[98:99], v[130:131]\n" \
    "v_pk_add_f32 v[100:101], v[100:101], v[132:133]\n" \
    "v_pk_add_f32 v[102:103], v[102:103], v[134:135]\n" \
    "v_cvt_pk_bf16_f32 v104, v96, v97\n" \
    "v_cvt_pk_bf16_f32 v105, v98, v99\n" \
    "v_cvt_pk_bf16_f32 v106, v100, v101\n" \
    "v_cvt_pk_bf16_f32 v107, v102, v103\n" \
    "v_lshlrev_b32 v96, 16, v104\n" \
    "v_and_b32 v97, 0xffff0000, v104\n" \
    "s_waitcnt vmcnt(8)\n" \
    "v_lshlrev_b32 v112, 16, v76\n" \
    "v_and_b32 v113, 0xffff0000, v76\n" \
    "v_lshlrev_b32 v98, 16, v105\n" \
    "v_and_b32 v99, 0xffff0000, v105\n" \
    "v_lshlrev_b32 v114, 16, v77\n" \
    "v_and_b32 v115, 0xffff0000, v77\n" \
    "v_lshlrev_b32 v100, 16, v106\n" \
    "v_and_b32 v101, 0xffff0000, v106\n" \
    "v_lshlrev_b32 v116, 16, v78\n" \
    "v_and_b32 v117, 0xffff0000, v78\n" \
    "v_lshlrev_b32 v102, 16, v107\n" \
    "v_and_b32 v103, 0xffff0000, v107\n" \
    "v_lshlrev_b32 v118, 16, v79\n" \
    "v_and_b32 v119, 0xffff0000, v79\n" \
    "v_pk_add_f32 v[96:97], v[96:97], v[112:113]\n" \
    "v_pk_add_f32 v[98:99], v[98:99], v[114:115]\n" \
    "v_pk_add_f32 v[100:101], v[100:101], v[116:117]\n" \
    "v_pk_add_f32 v[102:103], v[102:103], v[118:119]\n" \
    "v_cvt_pk_bf16_f32 v76, v96, v97\n" \
    "v_cvt_pk_bf16_f32 v77, v98, v99\n" \
    "v_cvt_pk_bf16_f32 v78, v100, v101\n" \
    "v_cvt_pk_bf16_f32 v79, v102, v103\n" \
    "v_min_u32 v76, -2, v76\n" \
    "v_min_u32 v77, -2, v77\n" \
    "v_min_u32 v78, -2, v78\n" \
    "v_min_u32 v79, -2, v79\n" \
    "buffer_store_dwordx4 v[76:79], v6, s[8:11], s44 offen sc1\n" \
    "s_add_u32 s64, s62, %[dh]\n" \
    "s_branch .Lr_hB0b_%=\n" \
    ".Lr_hB0a_%=:\n" \
    "s_sleep 1\n" \
    ".Lr_hB0b_%=:\n" \
    "s_memrealtime vcc\n" \
    "s_waitcnt lgkmcnt(0)\n" \
    "s_sub_i32 vcc_lo, s64, vcc_lo\n" \
    "s_cmp_gt_i32 vcc_lo, 8\n" \
    "s_cbranch_scc1 .Lr_hB0a_%=\n" \
    "s_cmp_gt_i32 vcc_lo, 0\n" \
    "s_cbranch_scc1 .Lr_hB0b_%=\n" \
    "buffer_load_dwordx2 v[16:17], v3, s[8:11], s51 offen sc1\n" \
    "s_cmp_eq_u32 s54, 0\n" \
    "s_cbranch_scc1 .Lr_cbB0_%=\n" \
    ".Lr_cbrB0_%=:\n" \
    "s_add_u32 s32, s32, 1\n" \
    "s_add_u32 s49, s32, 1\n" \
    "s_add_u32 s36, s18, s46\n" \
    "s_add_u32 s37, s36, 16384\n" \
    "s_and_b32 s33, s32, 3\n" \
    "s_lshl_b32 s35, s33, 16\n" \
    "s_add_u32 s35, s35, s17\n" \
    ".Lr_tvB1_%=:\n" \
    "s_waitcnt vmcnt(0)\n" \
    "v_cmp_ne_u32 vcc, s49, v17\n" \
    "v_subrev_u32 v19, s55, v16\n" \
    "s_and_b64 vcc, vcc, s[56:57]\n" \
    "s_cbranch_scc1 .Lr_ctvB1_%=\n" \
    "v_max_i32_dpp v19, v19, v19 quad_perm:[1,0,3,2] row_mask:0xf bank_mask:0xf bound_ctrl:1\n" \
    "s_nop 1\n" \
    "v_max_i32_dpp v19, v19, v19 quad_perm:[2,3,0,1] row_mask:0xf bank_mask:0xf bound_ctrl:1\n" \
    "s_nop 0\n" \
    "v_readlane_b32 s48, v19, 4\n" \
    "v_readlane_b32 s47, v19, 0\n" \
    "s_add_u32 s48, s48, s55\n" \
    "s_add_u32 s48, s48, s22\n" \
    ".Lr_goB1_%=:\n" \
    "s_branch .Lr_uB1b_%=\n" \
    ".Lr_uB1a_%=:\n" \
    "s_sleep 1\n" \
    ".Lr_uB1b_%=:\n" \
    "s_memrealtime vcc\n" \
    "s_waitcnt lgkmcnt(0)\n" \
    "s_sub_i32 vcc_lo, s48, vcc_lo\n" \
    "s_cmp_gt_i32 vcc_lo, 8\n" \
    "s_cbranch_scc1 .Lr_uB1a_%=\n" \
    "s_cmp_gt_i32 vcc_lo, 0\n" \
    "s_cbranch_scc1 .Lr_uB1b_%=\n" \
    "buffer_load_dwordx4 v[36:39], v0, s[8:11], s36 offen sc1\n" \
    "buffer_load_dwordx4 v[40:43], v0, s[8:11], s37 offen sc1\n" \
    "s_add_u32 s47, s47, s55\n" \
    "s_add_u32 s47, s47, %[dr]\n" \
    "s_branch .Lr_pB1b_%=\n" \
    ".Lr_pB1a_%=:\n" \
    "s_sleep 1\n" \
    ".Lr_pB1b_%=:\n" \
    "s_memrealtime vcc\n" \
    "s_waitcnt lgkmcnt(0)\n" \
    "s_sub_i32 vcc_lo, s47, vcc_lo\n" \
    "s_cmp_gt_i32 vcc_lo, 8\n" \
    "s_cbranch_scc1 .Lr_pB1a_%=\n" \
    "s_cmp_gt_i32 vcc_lo, 0\n" \
    "s_cbranch_scc1 .Lr_pB1b_%=\n" \
    "buffer_load_dwordx4 v[32:35], v4, s[8:11], s35 offen sc1\n" \
    "buffer_load_dwordx4 v[24:27], v0, s[8:11], s35 offen sc1\n" \
    "buffer_load_dwordx4 v[28:31], v0, s[8:11], s35 offen offset:1024 sc1\n" \
    "s_and_b32 s33, s32, 3\n" \
    "s_lshl_b32 s61, s33, 17\n" \
    "s_add_u32 s41, s61, s20\n" \
    "s_add_u32 s42, s41, 4096\n" \
    "s_and_b32 s33, s49, %[brn]-1\n" \
    "s_mul_i32 s39, s33, 2621440\n" \
    "s_add_u32 s39, s39, s19\n" \
    "s_lshr_b32 s33, s39, 8\n" \
    "s_xor_b32 s33, s33, s27\n" \
    "s_and_b32 s33, s33, 0x2000\n" \
    "s_or_b32 s39, s39, s33\n" \
    "s_barrier\n" \
    "buffer_load_dword v18, v2, s[8:11], 0 offen sc1\n" \
    "buffer_load_dwordx4 v[52:55], v0, s[8:11], s41 offen offset:2048 sc1\n" \
    "buffer_load_dwordx4 v[44:47], v0, s[8:11], s41 offen offset:0 sc1\n" \
    "buffer_load_dwordx4 v[48:51], v0, s[8:11], s41 offen offset:1024 sc1\n" \
    "buffer_load_dwordx4 v[56:59], v0, s[8:11], s41 offen offset:3072 sc1\n" \
    "s_add_u32 s46, s46, 655360\n" \
    "s_cmp_eq_u32 s46, 1966080\n" \
    "s_cselect_b32 s46, 0, s46\n" \
    "s_and_b32 s33, s49, 3\n" \
    "s_lshl_b32 s38, s33, 15\n" \
    "s_mul_i32 s43, s32, 40960\n" \
    "s_add_u32 s43, s43, s21\n" \
    "s_cmp_eq_u32 s32, 63\n" \
    "s_cselect_b32 s38, 0x80000000, s38\n" \
    "s_waitcnt vmcnt(9)\n" \
    "v_max3_u32 v19, v36, v37, v38\n" \
    "s_waitcnt vmcnt(8)\n" \
    "v_max3_u32 v19, v19, v39, v40\n" \
    "v_max3_u32 v19, v19, v41, v42\n" \
    "v_max_u32 v19, v19, v43\n" \
    "s_waitcnt vmcnt(7)\n" \
    "v_mfma_f32_16x16x32_bf16 v[80:83], v[32:35], v[36:39], 0\n" \
    "v_max3_u32 v20, v32, v33, v34\n" \
    "v_mfma_f32_16x16x32_bf16 v[84:87], v[32:35], v[40:43], 0\n" \
    "v_max_u32 v20, v20, v35\n" \
    "s_waitcnt vmcnt(6)\n" \
    "v_smfmac_f32_16x16x64_bf16 v[80:83], v[24:27], v[224:231], v10\n" \
    "v_smfmac_f32_16x16x64_bf16 v[84:87], v[24:27], v[240:247], v10\n" \
    "v_max3_u32 v19, v19, v24, v25\n" \
    "v_max3_u32 v19, v19, v26, v27\n" \
    "s_waitcnt vmcnt(5)\n" \
    "v_smfmac_f32_16x16x64_bf16 v[80:83], v[28:31], v[232:239], v10\n" \
    "v_smfmac_f32_16x16x64_bf16 v[84:87], v[28:31], v[248:255], v10\n" \
    "v_max3_u32 v20, v20, v28, v29\n" \
    "v_max3_u32 v20, v20, v30, v31\n" \
    "v_max_u32 v19, v19, v20\n" \
    "v_cmp_eq_u32 vcc, -1, v19\n" \
    "s_nop 0\n" \
    "s_cbranch_vccnz .Lr_cpB1_%=\n" \
    ".Lr_cprB1_%=:\n" \
    "s_nop 0\n" \
    "v_pk_add_f32 v[80:81], v[80:81], v[82:83]\n" \
    "v_pk_add_f32 v[84:85], v[84:85], v[86:87]\n" \
    "ds_write2_b32 v5, v80, v81 offset0:0 offset1:4\n" \
    "ds_write2_b32 v5, v84, v85 offset0:64 offset1:68\n" \
    "s_waitcnt lgkmcnt(0)\n" \
    "s_barrier\n" \
    "s_barrier\n" \
    "s_memrealtime s[62:63]\n" \
    "buffer_load_dwordx4 v[60:63], v0, s[8:11], s42 offen offset:0 sc1\n" \
    "buffer_load_dwordx4 v[64:67], v0, s[8:11], s42 offen offset:1024 sc1\n" \
    "buffer_load_dwordx4 v[68:71], v0, s[8:11], s42 offen offset:2048 sc1\n" \
    "buffer_load_dwordx4 v[72:75], v0, s[8:11], s42 offen offset:3072 sc1\n" \
    "s_waitcnt vmcnt(8)\n" \
    "v_cmp_ge_u32 vcc, s49, v18\n" \
    "s_nop 0\n" \
    "s_and_b64 vcc, vcc, s[56:57]\n" \
    "s_cselect_b32 s54, 0, 1\n" \
    "s_cmp_eq_u32 s32, 63\n" \
    "s_cselect_b32 s54, 2, s54\n" \
    "s_cmp_eq_u32 s54, 1\n" \
    "s_cselect_b32 s39, s39, 0x80000000\n" \
    "s_add_u32 s40, s39, 65536\n" \
    "s_barrier\n" \
    "s_add_u32 s33, s43, %[x0]\n" \
    "buffer_load_dwordx4 v[76:79], v6, s[8:11], s33 offen sc1\n" \
    "buffer_load_dwordx4 v[192:195], v0, s[8:11], s39 offen offset:0 sc1\n" \
    "buffer_load_dwordx4 v[196:199], v0, s[8:11], s39 offen offset:1024 sc1\n" \
    "buffer_load_dwordx4 v[200:203], v0, s[8:11], s39 offen offset:2048 sc1\n" \
    "buffer_load_dwordx4 v[204:207], v0, s[8:11], s39 offen offset:3072 sc1\n" \
    "buffer_load_dwordx4 v[208:211], v0, s[8:11], s40 offen offset:0 sc1\n" \
    "buffer_load_dwordx4 v[212:215], v0, s[8:11], s40 offen offset:1024 sc1\n" \
    "buffer_load_dwordx4 v[216:219], v0, s[8:11], s40 offen offset:2048 sc1\n" \
    "buffer_load_dwordx4 v[220:223], v0, s[8:11], s40 offen offset:3072 sc1\n" \
    "s_and_b32 s33, s32, s23\n" \
    "s_mul_i32 s45, s33, s24\n" \
    "s_cmp_lt_u32 s33, 64-%[pdist]\n" \
    "s_cselect_b32 s45, s45, 0x80000000\n" \
    "s_add_u32 s44, s43, %[x1]\n" \
    "s_xor_b32 s51, s51, 16384\n" \
    "s_waitcnt vmcnt(15)\n" \
    "v_max3_u32 v19, v44, v45, v46\n" \
    "s_waitcnt vmcnt(14)\n" \
    "v_max3_u32 v19, v19, v47, v48\n" \
    "v_max3_u32 v19, v19, v49, v50\n" \
    "v_max3_u32 v19, v19, v51, v52\n" \
    "v_max3_u32 v19, v19, v53, v54\n" \
    "s_waitcnt vmcnt(13)\n" \
    "v_max3_u32 v19, v19, v55, v56\n" \
    "v_max3_u32 v19, v19, v57, v58\n" \
    "s_waitcnt vmcnt(12)\n" \
    "v_max3_u32 v19, v19, v59, v60\n" \
    "v_max3_u32 v19, v19, v61, v62\n" \
    "s_waitcnt vmcnt(11)\n" \
    "v_max3_u32 v19, v19, v63, v64\n" \
    "v_max3_u32 v19, v19, v65, v66\n" \
    "s_waitcnt vmcnt(10)\n" \
    "v_max3_u32 v19, v19, v67, v68\n" \
    "v_max3_u32 v19, v19, v69, v70\n" \
    "s_waitcnt vmcnt(9)\n" \
    "v_max3_u32 v19, v19, v71, v72\n" \
    "v_max3_u32 v19, v19, v73, v74\n" \
    "v_max_u32 v19, v19, v75\n" \
    "v_cmp_eq_u32 vcc, -1, v19\n" \
    "s_nop 0\n" \
    "s_cbranch_vccnz .Lr_ctB1_%=\n" \
    ".Lr_ctrB1_%=:\n" \
    "v_mfma_f32_16x16x32_bf16 v[88:91], v[36:39], v[72:75], 0\n" \
    "v_mfma_f32_16x16x32_bf16 v[88:91], v[224:227], v[56:59], v[88:91]\n" \
    "v_mfma_f32_16x16x32_bf16 v[88:91], v[228:231], v[60:63], v[88:91]\n" \
    "v_mfma_f32_16x16x32_bf16 v[88:91], v[232:235], v[64:67], v[88:91]\n" \
    "v_mfma_f32_16x16x32_bf16 v[88:91], v[236:239], v[68:71], v[88:91]\n" \
    "v_mfma_f32_16x16x32_bf16 v[92:95], v[40:43], v[72:75], 0\n" \
    "v_mfma_f32_16x16x32_bf16 v[92:95], v[240:243], v[56:59], v[92:95]\n" \
    "v_mfma_f32_16x16x32_bf16 v[92:95], v[244:247], v[60:63], v[92:95]\n" \
    "v_mfma_f32_16x16x32_bf16 v[92:95], v[248:251], v[64:67], v[92:95]\n" \
    "v_mfma_f32_16x16x32_bf16 v[92:95], v[252:255], v[68:71], v[92:95]\n" \
    "v_mfma_f32_16x16x32_bf16 v[80:83], v[36:39], v[52:55], 0\n" \
    "v_mfma_f32_16x16x32_bf16 v[84:87], v[40:43], v[52:55], 0\n" \
    "v_mov_b32 v225, v226\n" \
    "v_mov_b32 v226, v228\n" \
    "v_mov_b32 v227, v230\n" \
    "v_mov_b32 v233, v234\n" \
    "v_mov_b32 v234, v236\n" \
    "v_mov_b32 v235, v238\n" \
    "v_mov_b32 v241, v242\n" \
    "v_mov_b32 v242, v244\n" \
    "v_mov_b32 v243, v246\n" \
    "v_mov_b32 v249, v250\n" \
    "v_mov_b32 v250, v252\n" \
    "v_mov_b32 v251, v254\n" \
    "ds_write_b128 v8, v[88:91] offset:2048\n" \
    "ds_write_b128 v8, v[92:95] offset:3072\n" \
    "v_permlane16_swap_b32 v224, v226\n" \
    "v_permlane16_swap_b32 v225, v227\n" \
    "v_permlane16_swap_b32 v232, v234\n" \
    "v_permlane16_swap_b32 v233, v235\n" \
    "v_permlane16_swap_b32 v240, v242\n" \
    "v_permlane16_swap_b32 v241, v243\n" \
    "v_permlane16_swap_b32 v248, v250\n" \
    "v_permlane16_swap_b32 v249, v251\n" \
    "v_mfma_f32_16x16x32_bf16 v[80:83], v[224:227], v[44:47], v[80:83]\n" \
    "v_mfma_f32_16x16x32_bf16 v[84:87], v[240:243], v[44:47], v[84:87]\n" \
    "v_mfma_f32_16x16x32_bf16 v[80:83], v[232:235], v[48:51], v[80:83]\n" \
    "v_mfma_f32_16x16x32_bf16 v[84:87], v[248:251], v[48:51], v[84:87]\n" \
    "s_nop 6\n" \
    "ds_write_b128 v8, v[80:83] offset:0\n" \
    "ds_write_b128 v8, v[84:87] offset:1024\n" \
    "s_waitcnt lgkmcnt(0)\n" \
    "s_barrier\n" \
    "s_mov_b32 s55, s62\n" \
    "ds_read_b128 v[96:99], v7 offset:0\n" \
    "ds_read_b128 v[100:103], v7 offset:256\n" \
    "ds_read_b128 v[104:107], v7 offset:4096\n" \
    "ds_read_b128 v[108:111], v7 offset:4352\n" \
    "ds_read_b128 v[112:115], v7 offset:8192\n" \
    "ds_read_b128 v[116:119], v7 offset:8448\n" \
    "ds_read_b128 v[120:123], v7 offset:12288\n" \
    "ds_read_b128 v[124:127], v7 offset:12544\n" \
    "ds_read_b128 v[128:131], v7 offset:16384\n" \
    "ds_read_b128 v[132:135], v7 offset:16640\n" \
    "ds_read_b128 v[136:139], v7 offset:20480\n" \
    "ds_read_b128 v[140:143], v7 offset:20736\n" \
    "ds_read_b128 v[144:147], v7 offset:24576\n" \
    "ds_read_b128 v[148:151], v7 offset:24832\n" \
    "ds_read_b128 v[152:155], v7 offset:28672\n" \
    "ds_read_b128 v[156:159], v7 offset:28928\n" \
    "s_waitcnt lgkmcnt(13)\n" \
    "v_pk_add_f32 v[96:97], v[96:97], v[104:105]\n" \
    "v_pk_add_f32 v[98:99], v[98:99], v[106:107]\n" \
    "s_waitcnt lgkmcnt(12)\n" \
    "v_pk_add_f32 v[100:101], v[100:101], v[108:109]\n" \
    "v_pk_add_f32 v[102:103], v[102:103], v[110:111]\n" \
    "s_waitcnt lgkmcnt(9)\n" \
    "v_pk_add_f32 v[112:113], v[112:113], v[120:121]\n" \
    "v_pk_add_f32 v[114:115], v[114:115], v[122:123]\n" \
    "s_waitcnt lgkmcnt(8)\n" \
    "v_pk_add_f32 v[116:117], v[116:117], v[124:125]\n" \
    "v_pk_add_f32 v[118:119], v[118:119], v[126:127]\n" \
    "v_pk_add_f32 v[96:97], v[96:97], v[112:113]\n" \
    "v_pk_add_f32 v[98:99], v[98:99], v[114:115]\n" \
    "v_pk_add_f32 v[100:101], v[100:101], v[116:117]\n" \
    "v_pk_add_f32 v[102:103], v[102:103], v[118:119]\n" \
    "s_waitcnt lgkmcnt(5)\n" \
    "v_pk_add_f32 v[128:129], v[128:129], v[136:137]\n" \
    "v_pk_add_f32 v[130:131], v[130:131], v[138:139]\n" \
    "s_waitcnt lgkmcnt(4)\n" \
    "v_pk_add_f32 v[132:133], v[132:133], v[140:141]\n" \
    "v_pk_add_f32 v[134:135], v[134:135], v[142:143]\n" \
    "s_waitcnt lgkmcnt(1)\n" \
    "v_pk_add_f32 v[144:145], v[144:145], v[152:153]\n" \
    "v_pk_add_f32 v[146:147], v[146:147], v[154:155]\n" \
    "s_waitcnt lgkmcnt(0)\n" \
    "v_pk_add_f32 v[148:149], v[148:149], v[156:157]\n" \
    "v_pk_add_f32 v[150:151], v[150:151], v[158:159]\n" \
    "v_pk_add_f32 v[128:129], v[128:129], v[144:145]\n" \
    "v_pk_add_f32 v[130:131], v[130:131], v[146:147]\n" \
    "v_pk_add_f32 v[132:133], v[132:133], v[148:149]\n" \
    "v_pk_add_f32 v[134:135], v[134:135], v[150:151]\n" \
    "v_pk_add_f32 v[96:97], v[96:97], v[128:129]\n" \
    "v_pk_add_f32 v[98:99], v[98:99], v[130:131]\n" \
    "v_pk_add_f32 v[100:101], v[100:101], v[132:133]\n" \
    "v_pk_add_f32 v[102:103], v[102:103], v[134:135]\n" \
    "v_cvt_pk_bf16_f32 v104, v96, v97\n" \
    "v_cvt_pk_bf16_f32 v105, v98, v99\n" \
    "v_cvt_pk_bf16_f32 v106, v100, v101\n" \
    "v_cvt_pk_bf16_f32 v107, v102, v103\n" \
    "v_lshlrev_b32 v96, 16, v104\n" \
    "v_and_b32 v97, 0xffff0000, v104\n" \
    "s_waitcnt vmcnt(8)\n" \
    "v_lshlrev_b32 v112, 16, v76\n" \
    "v_and_b32 v113, 0xffff0000, v76\n" \
    "v_lshlrev_b32 v98, 16, v105\n" \
    "v_and_b32 v99, 0xffff0000, v105\n" \
    "v_lshlrev_b32 v114, 16, v77\n" \
    "v_and_b32 v115, 0xffff0000, v77\n" \
    "v_lshlrev_b32 v100, 16, v106\n" \
    "v_and_b32 v101, 0xffff0000, v106\n" \
    "v_lshlrev_b32 v116, 16, v78\n" \
    "v_and_b32 v117, 0xffff0000, v78\n" \
    "v_lshlrev_b32 v102, 16, v107\n" \
    "v_and_b32 v103, 0xffff0000, v107\n" \
    "v_lshlrev_b32 v118, 16, v79\n" \
    "v_and_b32 v119, 0xffff0000, v79\n" \
    "v_pk_add_f32 v[96:97], v[96:97], v[112:113]\n" \
    "v_pk_add_f32 v[98:99], v[98:99], v[114:115]\n" \
    "v_pk_add_f32 v[100:101], v[100:101], v[116:117]\n" \
    "v_pk_add_f32 v[102:103], v[102:103], v[118:119]\n" \
    "v_cvt_pk_bf16_f32 v76, v96, v97\n" \
    "v_cvt_pk_bf16_f32 v77, v98, v99\n" \
    "v_cvt_pk_bf16_f32 v78, v100, v101\n" \
    "v_cvt_pk_bf16_f32 v79, v102, v103\n" \
    "v_min_u32 v76, -2, v76\n" \
    "v_min_u32 v77, -2, v77\n" \
    "v_min_u32 v78, -2, v78\n" \
    "v_min_u32 v79, -2, v79\n" \
    "buffer_store_dwordx4 v[76:79], v6, s[8:11], s44 offen sc1\n" \
    "s_add_u32 s64, s62, %[dh]\n" \
    "s_branch .Lr_hB1b_%=\n" \
    ".Lr_hB1a_%=:\n" \
    "s_sleep 1\n" \
    ".Lr_hB1b_%=:\n" \
    "s_memrealtime vcc\n" \
    "s_waitcnt lgkmcnt(0)\n" \
    "s_sub_i32 vcc_lo, s64, vcc_lo\n" \
    "s_cmp_gt_i32 vcc_lo, 8\n" \
    "s_cbranch_scc1 .Lr_hB1a_%=\n" \
    "s_cmp_gt_i32 vcc_lo, 0\n" \
    "s_cbranch_scc1 .Lr_hB1b_%=\n" \
    "buffer_load_dwordx2 v[16:17], v3, s[8:11], s51 offen sc1\n" \
    "s_cmp_eq_u32 s54, 0\n" \
    "s_cbranch_scc1 .Lr_cbB1_%=\n" \
    ".Lr_cbrB1_%=:\n" \
    "s_add_u32 s32, s32, 1\n" \
    "s_cmp_lt_u32 s32, 64\n" \
    "s_cbranch_scc1 .Lr_lB_%=\n" \
    "s_waitcnt vmcnt(0)\n" \
    "s_endpgm\n" \
    ".Lr_lC_%=:\n" \
    "s_add_u32 s49, s32, 1\n" \
    "s_add_u32 s36, s18, s46\n" \
    "s_add_u32 s37, s36, 16384\n" \
    "s_and_b32 s33, s32, 3\n" \
    "s_lshl_b32 s35, s33, 16\n" \
    "s_add_u32 s35, s35, s17\n" \
    ".Lr_tvC0_%=:\n" \
    "s_waitcnt vmcnt(0)\n" \
    "v_cmp_ne_u32 vcc, s49, v17\n" \
    "v_subrev_u32 v19, s55, v16\n" \
    "s_and_b64 vcc, vcc, s[56:57]\n" \
    "s_cbranch_scc1 .Lr_ctvC0_%=\n" \
    "v_max_i32_dpp v19, v19, v19 quad_perm:[1,0,3,2] row_mask:0xf bank_mask:0xf bound_ctrl:1\n" \
    "s_nop 1\n" \
    "v_max_i32_dpp v19, v19, v19 quad_perm:[2,3,0,1] row_mask:0xf bank_mask:0xf bound_ctrl:1\n" \
    "s_nop 0\n" \
    "v_readlane_b32 s48, v19, 4\n" \
    "v_readlane_b32 s47, v19, 0\n" \
    "s_add_u32 s48, s48, s55\n" \
    "s_add_u32 s48, s48, s22\n" \
    ".Lr_goC0_%=:\n" \
    "s_branch .Lr_uC0b_%=\n" \
    ".Lr_uC0a_%=:\n" \
    "s_sleep 1\n" \
    ".Lr_uC0b_%=:\n" \
    "s_memrealtime vcc\n" \
    "s_waitcnt lgkmcnt(0)\n" \
    "s_sub_i32 vcc_lo, s48, vcc_lo\n" \
    "s_cmp_gt_i32 vcc_lo, 8\n" \
    "s_cbranch_scc1 .Lr_uC0a_%=\n" \
    "s_cmp_gt_i32 vcc_lo, 0\n" \
    "s_cbranch_scc1 .Lr_uC0b_%=\n" \
    "buffer_load_dwordx4 v[36:39], v0, s[8:11], s36 offen sc1\n" \
    "buffer_load_dwordx4 v[40:43], v0, s[8:11], s37 offen sc1\n" \
    "s_add_u32 s47, s47, s55\n" \
    "s_add_u32 s47, s47, %[dr]\n" \
    "s_branch .Lr_pC0b_%=\n" \
    ".Lr_pC0a_%=:\n" \
    "s_sleep 1\n" \
    ".Lr_pC0b_%=:\n" \
    "s_memrealtime vcc\n" \
    "s_waitcnt lgkmcnt(0)\n" \
    "s_sub_i32 vcc_lo, s47, vcc_lo\n" \
    "s_cmp_gt_i32 vcc_lo, 8\n" \
    "s_cbranch_scc1 .Lr_pC0a_%=\n" \
    "s_cmp_gt_i32 vcc_lo, 0\n" \
    "s_cbranch_scc1 .Lr_pC0b_%=\n" \
    "buffer_load_dwordx4 v[32:35], v4, s[8:11], s35 offen sc1\n" \
    "buffer_load_dwordx4 v[24:27], v0, s[8:11], s35 offen sc1\n" \
    "buffer_load_dwordx4 v[28:31], v0, s[8:11], s35 offen offset:1024 sc1\n" \
    "s_and_b32 s33, s32, 3\n" \
    "s_lshl_b32 s61, s33, 17\n" \
    "s_add_u32 s41, s61, s20\n" \
    "s_add_u32 s42, s41, 4096\n" \
    "s_and_b32 s33, s49, %[brn]-1\n" \
    "s_mul_i32 s39, s33, 2621440\n" \
    "s_add_u32 s39, s39, s19\n" \
    "s_lshr_b32 s33, s39, 8\n" \
    "s_xor_b32 s33, s33, s27\n" \
    "s_and_b32 s33, s33, 0x2000\n" \
    "s_or_b32 s39, s39, s33\n" \
    "s_barrier\n" \
    "buffer_load_dword v18, v2, s[8:11], 0 offen sc1\n" \
    "buffer_load_dwordx4 v[52:55], v0, s[8:11], s41 offen offset:2048 sc1\n" \
    "buffer_load_dwordx4 v[44:47], v0, s[8:11], s41 offen offset:0 sc1\n" \
    "buffer_load_dwordx4 v[48:51], v0, s[8:11], s41 offen offset:1024 sc1\n" \
    "buffer_load_dwordx4 v[56:59], v0, s[8:11], s41 offen offset:3072 sc1\n" \
    "s_add_u32 s46, s46, 655360\n" \
    "s_cmp_eq_u32 s46, 1966080\n" \
    "s_cselect_b32 s46, 0, s46\n" \
    "s_and_b32 s33, s49, 3\n" \
    "s_lshl_b32 s38, s33, 15\n" \
    "s_mul_i32 s43, s32, 40960\n" \
    "s_add_u32 s43, s43, s21\n" \
    "s_cmp_eq_u32 s32, 63\n" \
    "s_cselect_b32 s38, 0x80000000, s38\n" \
    "s_waitcnt vmcnt(9)\n" \
    "v_max3_u32 v19, v36, v37, v38\n" \
    "s_waitcnt vmcnt(8)\n" \
    "v_max3_u32 v19, v19, v39, v40\n" \
    "v_max3_u32 v19, v19, v41, v42\n" \
    "v_max_u32 v19, v19, v43\n" \
    "s_waitcnt vmcnt(7)\n" \
    "v_mfma_f32_16x16x32_bf16 v[80:83], v[32:35], v[36:39], 0\n" \
    "v_max3_u32 v20, v32, v33, v34\n" \
    "v_mfma_f32_16x16x32_bf16 v[84:87], v[32:35], v[40:43], 0\n" \
    "v_max_u32 v20, v20, v35\n" \
    "s_waitcnt vmcnt(6)\n" \
    "v_smfmac_f32_16x16x64_bf16 v[80:83], v[24:27], v[192:199], v10\n" \
    "v_smfmac_f32_16x16x64_bf16 v[84:87], v[24:27], v[208:215], v10\n" \
    "v_max3_u32 v19, v19, v24, v25\n" \
    "v_max3_u32 v19, v19, v26, v27\n" \
    "s_waitcnt vmcnt(5)\n" \
    "v_smfmac_f32_16x16x64_bf16 v[80:83], v[28:31], v[200:207], v10\n" \
    "v_smfmac_f32_16x16x64_bf16 v[84:87], v[28:31], v[216:223], v10\n" \
    "v_max3_u32 v20, v20, v28, v29\n" \
    "v_max3_u32 v20, v20, v30, v31\n" \
    "v_max_u32 v19, v19, v20\n" \
    "v_cmp_eq_u32 vcc, -1, v19\n" \
    "s_nop 0\n" \
    "s_cbranch_vccnz .Lr_cpC0_%=\n" \
    ".Lr_cprC0_%=:\n" \
    "s_nop 0\n" \
    "v_pk_add_f32 v[80:81], v[80:81], v[82:83]\n" \
    "v_pk_add_f32 v[84:85], v[84:85], v[86:87]\n" \
    "ds_write2_b32 v5, v80, v81 offset0:0 offset1:4\n" \
    "ds_write2_b32 v5, v84, v85 offset0:64 offset1:68\n" \
    "s_waitcnt lgkmcnt(0)\n" \
    "s_barrier\n" \
    "s_barrier\n" \
    "s_memrealtime s[62:63]\n" \
    "buffer_load_dwordx4 v[60:63], v0, s[8:11], s42 offen offset:0 sc1\n" \
    "buffer_load_dwordx4 v[64:67], v0, s[8:11], s42 offen offset:1024 sc1\n" \
    "buffer_load_dwordx4 v[68:71], v0, s[8:11], s42 offen offset:2048 sc1\n" \
    "buffer_load_dwordx4 v[72:75], v0, s[8:11], s42 offen offset:3072 sc1\n" \
    "s_waitcnt vmcnt(8)\n" \
    "v_cmp_ge_u32 vcc, s49, v18\n" \
    "s_nop 0\n" \
    "s_and_b64 vcc, vcc, s[56:57]\n" \
    "s_cselect_b32 s54, 0, 1\n" \
    "s_cmp_eq_u32 s32, 63\n" \
    "s_cselect_b32 s54, 2, s54\n" \
    "s_cmp_eq_u32 s54, 1\n" \
    "s_cselect_b32 s39, s39, 0x80000000\n" \
    "s_add_u32 s40, s39, 65536\n" \
    "s_barrier\n" \
    "buffer_store_dwordx4 v[160:163], v9, s[8:11], s45 offen sc1\n" \
    "s_and_b32 s33, s32, s23\n" \
    "s_mul_i32 s45, s33, s24\n" \
    "s_cmp_lt_u32 s33, 64-%[pdist]\n" \
    "s_cselect_b32 s45, s45, 0x80000000\n" \
    "s_add_u32 s44, s43, %[x1]\n" \
    "s_xor_b32 s51, s51, 16384\n" \
    "s_waitcnt vmcnt(7)\n" \
    "v_max3_u32 v19, v44, v45, v46\n" \
    "s_waitcnt vmcnt(6)\n" \
    "v_max3_u32 v19, v19, v47, v48\n" \
    "v_max3_u32 v19, v19, v49, v50\n" \
    "v_max3_u32 v19, v19, v51, v52\n" \
    "v_max3_u32 v19, v19, v53, v54\n" \
    "s_waitcnt vmcnt(5)\n" \
    "v_max3_u32 v19, v19, v55, v56\n" \
    "v_max3_u32 v19, v19, v57, v58\n" \
    "s_waitcnt vmcnt(4)\n" \
    "v_max3_u32 v19, v19, v59, v60\n" \
    "v_max3_u32 v19, v19, v61, v62\n" \
    "s_waitcnt vmcnt(3)\n" \
    "v_max3_u32 v19, v19, v63, v64\n" \
    "v_max3_u32 v19, v19, v65, v66\n" \
    "s_waitcnt vmcnt(2)\n" \
    "v_max3_u32 v19, v19, v67, v68\n" \
    "v_max3_u32 v19, v19, v69, v70\n" \
    "s_waitcnt vmcnt(1)\n" \
    "v_max3_u32 v19, v19, v71, v72\n" \
    "v_max3_u32 v19, v19, v73, v74\n" \
    "v_max_u32 v19, v19, v75\n" \
    "v_cmp_eq_u32 vcc, -1, v19\n" \
    "s_nop 0\n" \
    "s_cbranch_vccnz .Lr_ctC0_%=\n" \
    ".Lr_ctrC0_%=:\n" \
    "v_mfma_f32_16x16x32_bf16 v[88:91], v[36:39], v[72:75], 0\n" \
    "v_mfma_f32_16x16x32_bf16 v[88:91], v[192:195], v[56:59], v[88:91]\n" \
    "v_mfma_f32_16x16x32_bf16 v[88:91], v[196:199], v[60:63], v[88:91]\n" \
    "v_mfma_f32_16x16x32_bf16 v[88:91], v[200:203], v[64:67], v[88:91]\n" \
    "v_mfma_f32_16x16x32_bf16 v[88:91], v[204:207], v[68:71], v[88:91]\n" \
    "v_mfma_f32_16x16x32_bf16 v[92:95], v[40:43], v[72:75], 0\n" \
    "v_mfma_f32_16x16x32_bf16 v[92:95], v[208:211], v[56:59], v[92:95]\n" \
    "v_mfma_f32_16x16x32_bf16 v[92:95], v[212:215], v[60:63], v[92:95]\n" \
    "v_mfma_f32_16x16x32_bf16 v[92:95], v[216:219], v[64:67], v[92:95]\n" \
    "v_mfma_f32_16x16x32_bf16 v[92:95], v[220:223], v[68:71], v[92:95]\n" \
    "v_mfma_f32_16x16x32_bf16 v[80:83], v[36:39], v[52:55], 0\n" \
    "v_mfma_f32_16x16x32_bf16 v[84:87], v[40:43], v[52:55], 0\n" \
    "v_mov_b32 v193, v194\n" \
    "v_mov_b32 v194, v196\n" \
    "v_mov_b32 v195, v198\n" \
    "v_mov_b32 v201, v202\n" \
    "v_mov_b32 v202, v204\n" \
    "v_mov_b32 v203, v206\n" \
    "v_mov_b32 v209, v210\n" \
    "v_mov_b32 v210, v212\n" \
    "v_mov_b32 v211, v214\n" \
    "v_mov_b32 v217, v218\n" \
    "v_mov_b32 v218, v220\n" \
    "v_mov_b32 v219, v222\n" \
    "ds_write_b128 v8, v[88:91] offset:2048\n" \
    "ds_write_b128 v8, v[92:95] offset:3072\n" \
    "v_permlane16_swap_b32 v192, v194\n" \
    "v_permlane16_swap_b32 v193, v195\n" \
    "v_permlane16_swap_b32 v200, v202\n" \
    "v_permlane16_swap_b32 v201, v203\n" \
    "v_permlane16_swap_b32 v208, v210\n" \
    "v_permlane16_swap_b32 v209, v211\n" \
    "v_permlane16_swap_b32 v216, v218\n" \
    "v_permlane16_swap_b32 v217, v219\n" \
    "v_mfma_f32_16x16x32_bf16 v[80:83], v[192:195], v[44:47], v[80:83]\n" \
    "v_mfma_f32_16x16x32_bf16 v[84:87], v[208:211], v[44:47], v[84:87]\n" \
    "v_mfma_f32_16x16x32_bf16 v[80:83], v[200:203], v[48:51], v[80:83]\n" \
    "v_mfma_f32_16x16x32_bf16 v[84:87], v[216:219], v[48:51], v[84:87]\n" \
    "s_nop 6\n" \
    "ds_write_b128 v8, v[80:83] offset:0\n" \
    "ds_write_b128 v8, v[84:87] offset:1024\n" \
    "s_waitcnt lgkmcnt(0)\n" \
    "s_barrier\n" \
    "s_mov_b32 s55, s62\n" \
    "buffer_load_dwordx4 v[224:227], v0, s[8:11], s39 offen offset:0 sc1\n" \
    "buffer_load_dwordx4 v[228:231], v0, s[8:11], s39 offen offset:1024 sc1\n" \
    "buffer_load_dwordx4 v[232:235], v0, s[8:11], s39 offen offset:2048 sc1\n" \
    "buffer_load_dwordx4 v[236:239], v0, s[8:11], s39 offen offset:3072 sc1\n" \
    "buffer_load_dwordx4 v[240:243], v0, s[8:11], s40 offen offset:0 sc1\n" \
    "buffer_load_dwordx4 v[244:247], v0, s[8:11], s40 offen offset:1024 sc1\n" \
    "buffer_load_dwordx4 v[248:251], v0, s[8:11], s40 offen offset:2048 sc1\n" \
    "buffer_load_dwordx4 v[252:255], v0, s[8:11], s40 offen offset:3072 sc1\n" \
    "s_add_u32 s64, s62, %[dh]\n" \
    "s_branch .Lr_hC0b_%=\n" \
    ".Lr_hC0a_%=:\n" \
    "s_sleep 1\n" \
    ".Lr_hC0b_%=:\n" \
    "s_memrealtime vcc\n" \
    "s_waitcnt lgkmcnt(0)\n" \
    "s_sub_i32 vcc_lo, s64, vcc_lo\n" \
    "s_cmp_gt_i32 vcc_lo, 8\n" \
    "s_cbranch_scc1 .Lr_hC0a_%=\n" \
    "s_cmp_gt_i32 vcc_lo, 0\n" \
    "s_cbranch_scc1 .Lr_hC0b_%=\n" \
    "buffer_load_dwordx2 v[16:17], v3, s[8:11], s51 offen sc1\n" \
    "s_cmp_eq_u32 s54, 0\n" \
    "s_cbranch_scc1 .Lr_cbC0_%=\n" \
    ".Lr_cbrC0_%=:\n" \
    "s_add_u32 s32, s32, 1\n" \
    "s_add_u32 s49, s32, 1\n" \
    "s_add_u32 s36, s18, s46\n" \
    "s_add_u32 s37, s36, 16384\n" \
    "s_and_b32 s33, s32, 3\n" \
    "s_lshl_b32 s35, s33, 16\n" \
    "s_add_u32 s35, s35, s17\n" \
    ".Lr_tvC1_%=:\n" \
    "s_waitcnt vmcnt(0)\n" \
    "v_cmp_ne_u32 vcc, s49, v17\n" \
    "v_subrev_u32 v19, s55, v16\n" \
    "s_and_b64 vcc, vcc, s[56:57]\n" \
    "s_cbranch_scc1 .Lr_ctvC1_%=\n" \
    "v_max_i32_dpp v19, v19, v19 quad_perm:[1,0,3,2] row_mask:0xf bank_mask:0xf bound_ctrl:1\n" \
    "s_nop 1\n" \
    "v_max_i32_dpp v19, v19, v19 quad_perm:[2,3,0,1] row_mask:0xf bank_mask:0xf bound_ctrl:1\n" \
    "s_nop 0\n" \
    "v_readlane_b32 s48, v19, 4\n" \
    "v_readlane_b32 s47, v19, 0\n" \
    "s_add_u32 s48, s48, s55\n" \
    "s_add_u32 s48, s48, s22\n" \
    ".Lr_goC1_%=:\n" \
    "s_branch .Lr_uC1b_%=\n" \
    ".Lr_uC1a_%=:\n" \
    "s_sleep 1\n" \
    ".Lr_uC1b_%=:\n" \
    "s_memrealtime vcc\n" \
    "s_waitcnt lgkmcnt(0)\n" \
    "s_sub_i32 vcc_lo, s48, vcc_lo\n" \
    "s_cmp_gt_i32 vcc_lo, 8\n" \
    "s_cbranch_scc1 .Lr_uC1a_%=\n" \
    "s_cmp_gt_i32 vcc_lo, 0\n" \
    "s_cbranch_scc1 .Lr_uC1b_%=\n" \
    "buffer_load_dwordx4 v[36:39], v0, s[8:11], s36 offen sc1\n" \
    "buffer_load_dwordx4 v[40:43], v0, s[8:11], s37 offen sc1\n" \
    "s_add_u32 s47, s47, s55\n" \
    "s_add_u32 s47, s47, %[dr]\n" \
    "s_branch .Lr_pC1b_%=\n" \
    ".Lr_pC1a_%=:\n" \
    "s_sleep 1\n" \
    ".Lr_pC1b_%=:\n" \
    "s_memrealtime vcc\n" \
    "s_waitcnt lgkmcnt(0)\n" \
    "s_sub_i32 vcc_lo, s47, vcc_lo\n" \
    "s_cmp_gt_i32 vcc_lo, 8\n" \
    "s_cbranch_scc1 .Lr_pC1a_%=\n" \
    "s_cmp_gt_i32 vcc_lo, 0\n" \
    "s_cbranch_scc1 .Lr_pC1b_%=\n" \
    "buffer_load_dwordx4 v[32:35], v4, s[8:11], s35 offen sc1\n" \
    "buffer_load_dwordx4 v[24:27], v0, s[8:11], s35 offen sc1\n" \
    "buffer_load_dwordx4 v[28:31], v0, s[8:11], s35 offen offset:1024 sc1\n" \
    "s_and_b32 s33, s32, 3\n" \
    "s_lshl_b32 s61, s33, 17\n" \
    "s_add_u32 s41, s61, s20\n" \
    "s_add_u32 s42, s41, 4096\n" \
    "s_and_b32 s33, s49, %[brn]-1\n" \
    "s_mul_i32 s39, s33, 2621440\n" \
    "s_add_u32 s39, s39, s19\n" \
    "s_lshr_b32 s33, s39, 8\n" \
    "s_xor_b32 s33, s33, s27\n" \
    "s_and_b32 s33, s33, 0x2000\n" \
    "s_or_b32 s39, s39, s33\n" \
    "s_barrier\n" \
    "buffer_load_dword v18, v2, s[8:11], 0 offen sc1\n" \
    "buffer_load_dwordx4 v[52:55], v0, s[8:11], s41 offen offset:2048 sc1\n" \
    "buffer_load_dwordx4 v[44:47], v0, s[8:11], s41 offen offset:0 sc1\n" \
    "buffer_load_dwordx4 v[48:51], v0, s[8:11], s41 offen offset:1024 sc1\n" \
    "buffer_load_dwordx4 v[56:59], v0, s[8:11], s41 offen offset:3072 sc1\n" \
    "s_add_u32 s46, s46, 655360\n" \
    "s_cmp_eq_u32 s46, 1966080\n" \
    "s_cselect_b32 s46, 0, s46\n" \
    "s_and_b32 s33, s49, 3\n" \
    "s_lshl_b32 s38, s33, 15\n" \
    "s_mul_i32 s43, s32, 40960\n" \
    "s_add_u32 s43, s43, s21\n" \
    "s_cmp_eq_u32 s32, 63\n" \
    "s_cselect_b32 s38, 0x80000000, s38\n" \
    "s_waitcnt vmcnt(9)\n" \
    "v_max3_u32 v19, v36, v37, v38\n" \
    "s_waitcnt vmcnt(8)\n" \
    "v_max3_u32 v19, v19, v39, v40\n" \
    "v_max3_u32 v19, v19, v41, v42\n" \
    "v_max_u32 v19, v19, v43\n" \
    "s_waitcnt vmcnt(7)\n" \
    "v_mfma_f32_16x16x32_bf16 v[80:83], v[32:35], v[36:39], 0\n" \
    "v_max3_u32 v20, v32, v33, v34\n" \
    "v_mfma_f32_16x16x32_bf16 v[84:87], v[32:35], v[40:43], 0\n" \
    "v_max_u32 v20, v20, v35\n" \
    "s_waitcnt vmcnt(6)\n" \
    "v_smfmac_f32_16x16x64_bf16 v[80:83], v[24:27], v[224:231], v10\n" \
    "v_smfmac_f32_16x16x64_bf16 v[84:87], v[24:27], v[240:247], v10\n" \
    "v_max3_u32 v19, v19, v24, v25\n" \
    "v_max3_u32 v19, v19, v26, v27\n" \
    "s_waitcnt vmcnt(5)\n" \
    "v_smfmac_f32_16x16x64_bf16 v[80:83], v[28:31], v[232:239], v10\n" \
    "v_smfmac_f32_16x16x64_bf16 v[84:87], v[28:31], v[248:255], v10\n" \
    "v_max3_u32 v20, v20, v28, v29\n" \
    "v_max3_u32 v20, v20, v30, v31\n" \
    "v_max_u32 v19, v19, v20\n" \
    "v_cmp_eq_u32 vcc, -1, v19\n" \
    "s_nop 0\n" \
    "s_cbranch_vccnz .Lr_cpC1_%=\n" \
    ".Lr_cprC1_%=:\n" \
    "s_nop 0\n" \
    "v_pk_add_f32 v[80:81], v[80:81], v[82:83]\n" \
    "v_pk_add_f32 v[84:85], v[84:85], v[86:87]\n" \
    "ds_write2_b32 v5, v80, v81 offset0:0 offset1:4\n" \
    "ds_write2_b32 v5, v84, v85 offset0:64 offset1:68\n" \
    "s_waitcnt lgkmcnt(0)\n" \
    "s_barrier\n" \
    "s_barrier\n" \
    "s_memrealtime s[62:63]\n" \
    "buffer_load_dwordx4 v[60:63], v0, s[8:11], s42 offen offset:0 sc1\n" \
    "buffer_load_dwordx4 v[64:67], v0, s[8:11], s42 offen offset:1024 sc1\n" \
    "buffer_load_dwordx4 v[68:71], v0, s[8:11], s42 offen offset:2048 sc1\n" \
    "buffer_load_dwordx4 v[72:75], v0, s[8:11], s42 offen offset:3072 sc1\n" \
    "s_waitcnt vmcnt(8)\n" \
    "v_cmp_ge_u32 vcc, s49, v18\n" \
    "s_nop 0\n" \
    "s_and_b64 vcc, vcc, s[56:57]\n" \
    "s_cselect_b32 s54, 0, 1\n" \
    "s_cmp_eq_u32 s32, 63\n" \
    "s_cselect_b32 s54, 2, s54\n" \
    "s_cmp_eq_u32 s54, 1\n" \
    "s_cselect_b32 s39, s39, 0x80000000\n" \
    "s_add_u32 s40, s39, 65536\n" \
    "s_barrier\n" \
    "buffer_store_dwordx4 v[160:163], v9, s[8:11], s45 offen sc1\n" \
    "s_and_b32 s33, s32, s23\n" \
    "s_mul_i32 s45, s33, s24\n" \
    "s_cmp_lt_u32 s33, 64-%[pdist]\n" \
    "s_cselect_b32 s45, s45, 0x80000000\n" \
    "s_add_u32 s44, s43, %[x1]\n" \
    "s_xor_b32 s51, s51, 16384\n" \
    "s_waitcnt vmcnt(7)\n" \
    "v_max3_u32 v19, v44, v45, v46\n" \
    "s_waitcnt vmcnt(6)\n" \
    "v_max3_u32 v19, v19, v47, v48\n" \
    "v_max3_u32 v19, v19, v49, v50\n" \
    "v_max3_u32 v19, v19, v51, v52\n" \
    "v_max3_u32 v19, v19, v53, v54\n" \
    "s_waitcnt vmcnt(5)\n" \
    "v_max3_u32 v19, v19, v55, v56\n" \
    "v_max3_u32 v19, v19, v57, v58\n" \
    "s_waitcnt vmcnt(4)\n" \
    "v_max3_u32 v19, v19, v59, v60\n" \
    "v_max3_u32 v19, v19, v61, v62\n" \
    "s_waitcnt vmcnt(3)\n" \
    "v_max3_u32 v19, v19, v63, v64\n" \
    "v_max3_u32 v19, v19, v65, v66\n" \
    "s_waitcnt vmcnt(2)\n" \
    "v_max3_u32 v19, v19, v67, v68\n" \
    "v_max3_u32 v19, v19, v69, v70\n" \
    "s_waitcnt vmcnt(1)\n" \
    "v_max3_u32 v19, v19, v71, v72\n" \
    "v_max3_u32 v19, v19, v73, v74\n" \
    "v_max_u32 v19, v19, v75\n" \
    "v_cmp_eq_u32 vcc, -1, v19\n" \
    "s_nop 0\n" \
    "s_cbranch_vccnz .Lr_ctC1_%=\n" \
    ".Lr_ctrC1_%=:\n" \
    "v_mfma_f32_16x16x32_bf16 v[88:91], v[36:39], v[72:75], 0\n" \
    "v_mfma_f32_16x16x32_bf16 v[88:91], v[224:227], v[56:59], v[88:91]\n" \
    "v_mfma_f32_16x16x32_bf16 v[88:91], v[228:231], v[60:63], v[88:91]\n" \
    "v_mfma_f32_16x16x32_bf16 v[88:91], v[232:235], v[64:67], v[88:91]\n" \
    "v_mfma_f32_16x16x32_bf16 v[88:91], v[236:239], v[68:71], v[88:91]\n" \
    "v_mfma_f32_16x16x32_bf16 v[92:95], v[40:43], v[72:75], 0\n" \
    "v_mfma_f32_16x16x32_bf16 v[92:95], v[240:243], v[56:59], v[92:95]\n" \
    "v_mfma_f32_16x16x32_bf16 v[92:95], v[244:247], v[60:63], v[92:95]\n" \
    "v_mfma_f32_16x16x32_bf16 v[92:95], v[248:251], v[64:67], v[92:95]\n" \
    "v_mfma_f32_16x16x32_bf16 v[92:95], v[252:255], v[68:71], v[92:95]\n" \
    "v_mfma_f32_16x16x32_bf16 v[80:83], v[36:39], v[52:55], 0\n" \
    "v_mfma_f32_16x16x32_bf16 v[84:87], v[40:43], v[52:55], 0\n" \
    "v_mov_b32 v225, v226\n" \
    "v_mov_b32 v226, v228\n" \
    "v_mov_b32 v227, v230\n" \
    "v_mov_b32 v233, v234\n" \
    "v_mov_b32 v234, v236\n" \
    "v_mov_b32 v235, v238\n" \
    "v_mov_b32 v241, v242\n" \
    "v_mov_b32 v242, v244\n" \
    "v_mov_b32 v243, v246\n" \
    "v_mov_b32 v249, v250\n" \
    "v_mov_b32 v250, v252\n" \
    "v_mov_b32 v251, v254\n" \
    "ds_write_b128 v8, v[88:91] offset:2048\n" \
    "ds_write_b128 v8, v[92:95] offset:3072\n" \
    "v_permlane16_swap_b32 v224, v226\n" \
    "v_permlane16_swap_b32 v225, v227\n" \
    "v_permlane16_swap_b32 v232, v234\n" \
    "v_permlane16_swap_b32 v233, v235\n" \
    "v_permlane16_swap_b32 v240, v242\n" \
    "v_permlane16_swap_b32 v241, v243\n" \
    "v_permlane16_swap_b32 v248, v250\n" \
    "v_permlane16_swap_b32 v249, v251\n" \
    "v_mfma_f32_16x16x32_bf16 v[80:83], v[224:227], v[44:47], v[80:83]\n" \
    "v_mfma_f32_16x16x32_bf16 v[84:87], v[240:243], v[44:47], v[84:87]\n" \
    "v_mfma_f32_16x16x32_bf16 v[80:83], v[232:235], v[48:51], v[80:83]\n" \
    "v_mfma_f32_16x16x32_bf16 v[84:87], v[248:251], v[48:51], v[84:87]\n" \
    "s_nop 6\n" \
    "ds_write_b128 v8, v[80:83] offset:0\n" \
    "ds_write_b128 v8, v[84:87] offset:1024\n" \
    "s_waitcnt lgkmcnt(0)\n" \
    "s_barrier\n" \
    "s_mov_b32 s55, s62\n" \
    "buffer_load_dwordx4 v[192:195], v0, s[8:11], s39 offen offset:0 sc1\n" \
    "buffer_load_dwordx4 v[196:199], v0, s[8:11], s39 offen offset:1024 sc1\n" \
    "buffer_load_dwordx4 v[200:203], v0, s[8:11], s39 offen offset:2048 sc1\n" \
    "buffer_load_dwordx4 v[204:207], v0, s[8:11], s39 offen offset:3072 sc1\n" \
    "buffer_load_dwordx4 v[208:211], v0, s[8:11], s40 offen offset:0 sc1\n" \
    "buffer_load_dwordx4 v[212:215], v0, s[8:11], s40 offen offset:1024 sc1\n" \
    "buffer_load_dwordx4 v[216:219], v0, s[8:11], s40 offen offset:2048 sc1\n" \
    "buffer_load_dwordx4 v[220:223], v0, s[8:11], s40 offen offset:3072 sc1\n" \
    "s_add_u32 s64, s62, %[dh]\n" \
    "s_branch .Lr_hC1b_%=\n" \
    ".Lr_hC1a_%=:\n" \
    "s_sleep 1\n" \
    ".Lr_hC1b_%=:\n" \
    "s_memrealtime vcc\n" \
    "s_waitcnt lgkmcnt(0)\n" \
    "s_sub_i32 vcc_lo, s64, vcc_lo\n" \
    "s_cmp_gt_i32 vcc_lo, 8\n" \
    "s_cbranch_scc1 .Lr_hC1a_%=\n" \
    "s_cmp_gt_i32 vcc_lo, 0\n" \
    "s_cbranch_scc1 .Lr_hC1b_%=\n" \
    "buffer_load_dwordx2 v[16:17], v3, s[8:11], s51 offen sc1\n" \
    "s_cmp_eq_u32 s54, 0\n" \
    "s_cbranch_scc1 .Lr_cbC1_%=\n" \
    ".Lr_cbrC1_%=:\n" \
    "s_add_u32 s32, s32, 1\n" \
    "s_cmp_lt_u32 s32, 64\n" \
    "s_cbranch_scc1 .Lr_lC_%=\n" \
    "s_waitcnt vmcnt(0)\n" \
    "s_endpgm\n" \
    ".Lr_lE_%=:\n" \
    "s_add_u32 s49, s32, 1\n" \
    "s_add_u32 s36, s18, s46\n" \
    "s_add_u32 s37, s36, 16384\n" \
    "s_and_b32 s33, s32, 3\n" \
    "s_lshl_b32 s35, s33, 16\n" \
    "s_add_u32 s35, s35, s17\n" \
    ".Lr_tvE0_%=:\n" \
    "s_waitcnt vmcnt(0)\n" \
    "v_cmp_ne_u32 vcc, s49, v17\n" \
    "v_subrev_u32 v19, s55, v16\n" \
    "s_and_b64 vcc, vcc, s[56:57]\n" \
    "s_cbranch_scc1 .Lr_ctvE0_%=\n" \
    "v_max_i32_dpp v19, v19, v19 quad_perm:[1,0,3,2] row_mask:0xf bank_mask:0xf bound_ctrl:1\n" \
    "s_nop 1\n" \
    "v_max_i32_dpp v19, v19, v19 quad_perm:[2,3,0,1] row_mask:0xf bank_mask:0xf bound_ctrl:1\n" \
    "s_nop 0\n" \
    "v_readlane_b32 s48, v19, 4\n" \
    "v_readlane_b32 s47, v19, 0\n" \
    "s_add_u32 s48, s48, s55\n" \
    "s_add_u32 s48, s48, s22\n" \
    ".Lr_goE0_%=:\n" \
    "s_branch .Lr_uE0b_%=\n" \
    ".Lr_uE0a_%=:\n" \
    "s_sleep 1\n" \
    ".Lr_uE0b_%=:\n" \
    "s_memrealtime vcc\n" \
    "s_waitcnt lgkmcnt(0)\n" \
    "s_sub_i32 vcc_lo, s48, vcc_lo\n" \
    "s_cmp_gt_i32 vcc_lo, 8\n" \
    "s_cbranch_scc1 .Lr_uE0a_%=\n" \
    "s_cmp_gt_i32 vcc_lo, 0\n" \
    "s_cbranch_scc1 .Lr_uE0b_%=\n" \
    "buffer_load_dwordx4 v[36:39], v0, s[8:11], s36 offen sc1\n" \
    "buffer_load_dwordx4 v[40:43], v0, s[8:11], s37 offen sc1\n" \
    "s_add_u32 s47, s47, s55\n" \
    "s_add_u32 s47, s47, %[dr]\n" \
    "s_branch .Lr_pE0b_%=\n" \
    ".Lr_pE0a_%=:\n" \
    "s_sleep 1\n" \
    ".Lr_pE0b_%=:\n" \
    "s_memrealtime vcc\n" \
    "s_waitcnt lgkmcnt(0)\n" \
    "s_sub_i32 vcc_lo, s47, vcc_lo\n" \
    "s_cmp_gt_i32 vcc_lo, 8\n" \
    "s_cbranch_scc1 .Lr_pE0a_%=\n" \
    "s_cmp_gt_i32 vcc_lo, 0\n" \
    "s_cbranch_scc1 .Lr_pE0b_%=\n" \
    "buffer_load_dwordx4 v[32:35], v4, s[8:11], s35 offen sc1\n" \
    "buffer_load_dwordx4 v[24:27], v0, s[8:11], s35 offen sc1\n" \
    "buffer_load_dwordx4 v[28:31], v0, s[8:11], s35 offen offset:1024 sc1\n" \
    "s_and_b32 s33, s32, 3\n" \
    "s_lshl_b32 s61, s33, 17\n" \
    "s_add_u32 s41, s61, s20\n" \
    "s_add_u32 s42, s41, 4096\n" \
    "s_and_b32 s33, s49, %[brn]-1\n" \
    "s_mul_i32 s39, s33, 2621440\n" \
    "s_add_u32 s39, s39, s19\n" \
    "s_lshr_b32 s33, s39, 8\n" \
    "s_xor_b32 s33, s33, s27\n" \
    "s_and_b32 s33, s33, 0x2000\n" \
    "s_or_b32 s39, s39, s33\n" \
    "s_barrier\n" \
    "buffer_load_dword v18, v2, s[8:11], 0 offen sc1\n" \
    "buffer_load_dwordx4 v[52:55], v0, s[8:11], s41 offen offset:2048 sc1\n" \
    "buffer_load_dwordx4 v[44:47], v0, s[8:11], s41 offen offset:0 sc1\n" \
    "buffer_load_dwordx4 v[48:51], v0, s[8:11], s41 offen offset:1024 sc1\n" \
    "buffer_load_dwordx4 v[56:59], v0, s[8:11], s41 offen offset:3072 sc1\n" \
    "buffer_load_dword v21, v13, s[8:11], s61 offen sc1\n" \
    "s_add_u32 s46, s46, 655360\n" \
    "s_cmp_eq_u32 s46, 1966080\n" \
    "s_cselect_b32 s46, 0, s46\n" \
    "s_and_b32 s33, s49, 3\n" \
    "s_lshl_b32 s38, s33, 15\n" \
    "s_mul_i32 s43, s32, 40960\n" \
    "s_add_u32 s43, s43, s21\n" \
    "s_cmp_eq_u32 s32, 63\n" \
    "s_cselect_b32 s38, 0x80000000, s38\n" \
    "s_waitcnt vmcnt(10)\n" \
    "v_max3_u32 v19, v36, v37, v38\n" \
    "s_waitcnt vmcnt(9)\n" \
    "v_max3_u32 v19, v19, v39, v40\n" \
    "v_max3_u32 v19, v19, v41, v42\n" \
    "v_max_u32 v19, v19, v43\n" \
    "s_waitcnt vmcnt(8)\n" \
    "v_mfma_f32_16x16x32_bf16 v[80:83], v[32:35], v[36:39], 0\n" \
    "v_max3_u32 v20, v32, v33, v34\n" \
    "v_mfma_f32_16x16x32_bf16 v[84:87], v[32:35], v[40:43], 0\n" \
    "v_max_u32 v20, v20, v35\n" \
    "s_waitcnt vmcnt(7)\n" \
    "v_smfmac_f32_16x16x64_bf16 v[80:83], v[24:27], v[192:199], v10\n" \
    "v_smfmac_f32_16x16x64_bf16 v[84:87], v[24:27], v[208:215], v10\n" \
    "v_max3_u32 v19, v19, v24, v25\n" \
    "v_max3_u32 v19, v19, v26, v27\n" \
    "s_waitcnt vmcnt(6)\n" \
    "v_smfmac_f32_16x16x64_bf16 v[80:83], v[28:31], v[200:207], v10\n" \
    "v_smfmac_f32_16x16x64_bf16 v[84:87], v[28:31], v[216:223], v10\n" \
    "v_max3_u32 v20, v20, v28, v29\n" \
    "v_max3_u32 v20, v20, v30, v31\n" \
    "v_max_u32 v19, v19, v20\n" \
    "v_cmp_eq_u32 vcc, -1, v19\n" \
    "s_nop 0\n" \
    "s_cbranch_vccnz .Lr_cpE0_%=\n" \
    ".Lr_cprE0_%=:\n" \
    "s_nop 0\n" \
    "v_pk_add_f32 v[80:81], v[80:81], v[82:83]\n" \
    "v_pk_add_f32 v[84:85], v[84:85], v[86:87]\n" \
    "ds_write2_b32 v5, v80, v81 offset0:0 offset1:4\n" \
    "ds_write2_b32 v5, v84, v85 offset0:64 offset1:68\n" \
    "s_waitcnt lgkmcnt(0)\n" \
    "s_barrier\n" \
    "s_barrier\n" \
    "s_memrealtime s[62:63]\n" \
    "buffer_load_dwordx4 v[60:63], v0, s[8:11], s42 offen offset:0 sc1\n" \
    "buffer_load_dwordx4 v[64:67], v0, s[8:11], s42 offen offset:1024 sc1\n" \
    "buffer_load_dwordx4 v[68:71], v0, s[8:11], s42 offen offset:2048 sc1\n" \
    "buffer_load_dwordx4 v[72:75], v0, s[8:11], s42 offen offset:3072 sc1\n" \
    "s_waitcnt vmcnt(9)\n" \
    "v_cmp_ge_u32 vcc, s49, v18\n" \
    "s_nop 0\n" \
    "s_and_b64 vcc, vcc, s[56:57]\n" \
    "s_cselect_b32 s54, 0, 1\n" \
    "s_cmp_eq_u32 s32, 63\n" \
    "s_cselect_b32 s54, 2, s54\n" \
    "s_cmp_eq_u32 s54, 1\n" \
    "s_cselect_b32 s39, s39, 0x80000000\n" \
    "s_add_u32 s40, s39, 65536\n" \
    "s_barrier\n" \
    "s_and_b32 s33, s32, s23\n" \
    "s_mul_i32 s45, s33, s24\n" \
    "s_cmp_lt_u32 s33, 64-%[pdist]\n" \
    "s_cselect_b32 s45, s45, 0x80000000\n" \
    "s_add_u32 s44, s43, %[x1]\n" \
    "s_xor_b32 s51, s51, 16384\n" \
    "s_waitcnt vmcnt(7)\n" \
    "v_max3_u32 v19, v44, v45, v46\n" \
    "s_waitcnt vmcnt(6)\n" \
    "v_max3_u32 v19, v19, v47, v48\n" \
    "v_max3_u32 v19, v19, v49, v50\n" \
    "v_max3_u32 v19, v19, v51, v52\n" \
    "v_max3_u32 v19, v19, v53, v54\n" \
    "s_waitcnt vmcnt(5)\n" \
    "v_max3_u32 v19, v19, v55, v56\n" \
    "v_max3_u32 v19, v19, v57, v58\n" \
    "s_waitcnt vmcnt(3)\n" \
    "v_max3_u32 v19, v19, v59, v60\n" \
    "v_max3_u32 v19, v19, v61, v62\n" \
    "s_waitcnt vmcnt(2)\n" \
    "v_max3_u32 v19, v19, v63, v64\n" \
    "v_max3_u32 v19, v19, v65, v66\n" \
    "s_waitcnt vmcnt(1)\n" \
    "v_max3_u32 v19, v19, v67, v68\n" \
    "v_max3_u32 v19, v19, v69, v70\n" \
    "s_waitcnt vmcnt(0)\n" \
    "v_max3_u32 v19, v19, v71, v72\n" \
    "v_max3_u32 v19, v19, v73, v74\n" \
    "v_max_u32 v19, v19, v75\n" \
    "v_cmp_eq_u32 vcc, -1, v19\n" \
    "s_nop 0\n" \
    "s_cbranch_vccnz .Lr_ctE0_%=\n" \
    ".Lr_ctrE0_%=:\n" \
    "v_mfma_f32_16x16x32_bf16 v[88:91], v[36:39], v[72:75], 0\n" \
    "v_mfma_f32_16x16x32_bf16 v[88:91], v[192:195], v[56:59], v[88:91]\n" \
    "v_mfma_f32_16x16x32_bf16 v[88:91], v[196:199], v[60:63], v[88:91]\n" \
    "v_mfma_f32_16x16x32_bf16 v[88:91], v[200:203], v[64:67], v[88:91]\n" \
    "v_mfma_f32_16x16x32_bf16 v[88:91], v[204:207], v[68:71], v[88:91]\n" \
    "v_mfma_f32_16x16x32_bf16 v[92:95], v[40:43], v[72:75], 0\n" \
    "v_mfma_f32_16x16x32_bf16 v[92:95], v[208:211], v[56:59], v[92:95]\n" \
    "v_mfma_f32_16x16x32_bf16 v[92:95], v[212:215], v[60:63], v[92:95]\n" \
    "v_mfma_f32_16x16x32_bf16 v[92:95], v[216:219], v[64:67], v[92:95]\n" \
    "v_mfma_f32_16x16x32_bf16 v[92:95], v[220:223], v[68:71], v[92:95]\n" \
    "v_mfma_f32_16x16x32_bf16 v[80:83], v[36:39], v[52:55], 0\n" \
    "v_mfma_f32_16x16x32_bf16 v[84:87], v[40:43], v[52:55], 0\n" \
    "v_mov_b32 v193, v194\n" \
    "v_mov_b32 v194, v196\n" \
    "v_mov_b32 v195, v198\n" \
    "v_mov_b32 v201, v202\n" \
    "v_mov_b32 v202, v204\n" \
    "v_mov_b32 v203, v206\n" \
    "v_mov_b32 v209, v210\n" \
    "v_mov_b32 v210, v212\n" \
    "v_mov_b32 v211, v214\n" \
    "v_mov_b32 v217, v218\n" \
    "v_mov_b32 v218, v220\n" \
    "v_mov_b32 v219, v222\n" \
    "ds_write_b128 v8, v[88:91] offset:2048\n" \
    "ds_write_b128 v8, v[92:95] offset:3072\n" \
    "v_permlane16_swap_b32 v192, v194\n" \
    "v_permlane16_swap_b32 v193, v195\n" \
    "v_permlane16_swap_b32 v200, v202\n" \
    "v_permlane16_swap_b32 v201, v203\n" \
    "v_permlane16_swap_b32 v208, v210\n" \
    "v_permlane16_swap_b32 v209, v211\n" \
    "v_permlane16_swap_b32 v216, v218\n" \
    "v_permlane16_swap_b32 v217, v219\n" \
    "v_mfma_f32_16x16x32_bf16 v[80:83], v[192:195], v[44:47], v[80:83]\n" \
    "v_mfma_f32_16x16x32_bf16 v[84:87], v[208:211], v[44:47], v[84:87]\n" \
    "v_mfma_f32_16x16x32_bf16 v[80:83], v[200:203], v[48:51], v[80:83]\n" \
    "v_mfma_f32_16x16x32_bf16 v[84:87], v[216:219], v[48:51], v[84:87]\n" \
    "s_nop 6\n" \
    "ds_write_b128 v8, v[80:83] offset:0\n" \
    "ds_write_b128 v8, v[84:87] offset:1024\n" \
    "s_waitcnt lgkmcnt(0)\n" \
    "s_barrier\n" \
    "s_mov_b32 s55, s62\n" \
    "buffer_load_dwordx4 v[224:227], v0, s[8:11], s39 offen offset:0 sc1\n" \
    "buffer_load_dwordx4 v[228:231], v0, s[8:11], s39 offen offset:1024 sc1\n" \
    "buffer_load_dwordx4 v[232:235], v0, s[8:11], s39 offen offset:2048 sc1\n" \
    "buffer_load_dwordx4 v[236:239], v0, s[8:11], s39 offen offset:3072 sc1\n" \
    "buffer_load_dwordx4 v[240:243], v0, s[8:11], s40 offen offset:0 sc1\n" \
    "buffer_load_dwordx4 v[244:247], v0, s[8:11], s40 offen offset:1024 sc1\n" \
    "buffer_load_dwordx4 v[248:251], v0, s[8:11], s40 offen offset:2048 sc1\n" \
    "buffer_load_dwordx4 v[252:255], v0, s[8:11], s40 offen offset:3072 sc1\n" \
    "s_add_u32 s64, s62, %[dh]\n" \
    "s_branch .Lr_hE0b_%=\n" \
    ".Lr_hE0a_%=:\n" \
    "s_sleep 1\n" \
    ".Lr_hE0b_%=:\n" \
    "s_memrealtime vcc\n" \
    "s_waitcnt lgkmcnt(0)\n" \
    "s_sub_i32 vcc_lo, s64, vcc_lo\n" \
    "s_cmp_gt_i32 vcc_lo, 8\n" \
    "s_cbranch_scc1 .Lr_hE0a_%=\n" \
    "s_cmp_gt_i32 vcc_lo, 0\n" \
    "s_cbranch_scc1 .Lr_hE0b_%=\n" \
    "buffer_load_dwordx2 v[16:17], v3, s[8:11], s51 offen sc1\n" \
    "s_cmp_eq_u32 s54, 0\n" \
    "s_cbranch_scc1 .Lr_cbE0_%=\n" \
    ".Lr_cbrE0_%=:\n" \
    "s_add_u32 s32, s32, 1\n" \
    "s_add_u32 s49, s32, 1\n" \
    "s_add_u32 s36, s18, s46\n" \
    "s_add_u32 s37, s36, 16384\n" \
    "s_and_b32 s33, s32, 3\n" \
    "s_lshl_b32 s35, s33, 16\n" \
    "s_add_u32 s35, s35, s17\n" \
    ".Lr_tvE1_%=:\n" \
    "s_waitcnt vmcnt(0)\n" \
    "v_cmp_ne_u32 vcc, s49, v17\n" \
    "v_subrev_u32 v19, s55, v16\n" \
    "s_and_b64 vcc, vcc, s[56:57]\n" \
    "s_cbranch_scc1 .Lr_ctvE1_%=\n" \
    "v_max_i32_dpp v19, v19, v19 quad_perm:[1,0,3,2] row_mask:0xf bank_mask:0xf bound_ctrl:1\n" \
    "s_nop 1\n" \
    "v_max_i32_dpp v19, v19, v19 quad_perm:[2,3,0,1] row_mask:0xf bank_mask:0xf bound_ctrl:1\n" \
    "s_nop 0\n" \
    "v_readlane_b32 s48, v19, 4\n" \
    "v_readlane_b32 s47, v19, 0\n" \
    "s_add_u32 s48, s48, s55\n" \
    "s_add_u32 s48, s48, s22\n" \
    ".Lr_goE1_%=:\n" \
    "s_branch .Lr_uE1b_%=\n" \
    ".Lr_uE1a_%=:\n" \
    "s_sleep 1\n" \
    ".Lr_uE1b_%=:\n" \
    "s_memrealtime vcc\n" \
    "s_waitcnt lgkmcnt(0)\n" \
    "s_sub_i32 vcc_lo, s48, vcc_lo\n" \
    "s_cmp_gt_i32 vcc_lo, 8\n" \
    "s_cbranch_scc1 .Lr_uE1a_%=\n" \
    "s_cmp_gt_i32 vcc_lo, 0\n" \
    "s_cbranch_scc1 .Lr_uE1b_%=\n" \
    "buffer_load_dwordx4 v[36:39], v0, s[8:11], s36 offen sc1\n" \
    "buffer_load_dwordx4 v[40:43], v0, s[8:11], s37 offen sc1\n" \
    "s_add_u32 s47, s47, s55\n" \
    "s_add_u32 s47, s47, %[dr]\n" \
    "s_branch .Lr_pE1b_%=\n" \
    ".Lr_pE1a_%=:\n" \
    "s_sleep 1\n" \
    ".Lr_pE1b_%=:\n" \
    "s_memrealtime vcc\n" \
    "s_waitcnt lgkmcnt(0)\n" \
    "s_sub_i32 vcc_lo, s47, vcc_lo\n" \
    "s_cmp_gt_i32 vcc_lo, 8\n" \
    "s_cbranch_scc1 .Lr_pE1a_%=\n" \
    "s_cmp_gt_i32 vcc_lo, 0\n" \
    "s_cbranch_scc1 .Lr_pE1b_%=\n" \
    "buffer_load_dwordx4 v[32:35], v4, s[8:11], s35 offen sc1\n" \
    "buffer_load_dwordx4 v[24:27], v0, s[8:11], s35 offen sc1\n" \
    "buffer_load_dwordx4 v[28:31], v0, s[8:11], s35 offen offset:1024 sc1\n" \
    "s_and_b32 s33, s32, 3\n" \
    "s_lshl_b32 s61, s33, 17\n" \
    "s_add_u32 s41, s61, s20\n" \
    "s_add_u32 s42, s41, 4096\n" \
    "s_and_b32 s33, s49, %[brn]-1\n" \
    "s_mul_i32 s39, s33, 2621440\n" \
    "s_add_u32 s39, s39, s19\n" \
    "s_lshr_b32 s33, s39, 8\n" \
    "s_xor_b32 s33, s33, s27\n" \
    "s_and_b32 s33, s33, 0x2000\n" \
    "s_or_b32 s39, s39, s33\n" \
    "s_barrier\n" \
    "buffer_load_dword v18, v2, s[8:11], 0 offen sc1\n" \
    "buffer_load_dwordx4 v[52:55], v0, s[8:11], s41 offen offset:2048 sc1\n" \
    "buffer_load_dwordx4 v[44:47], v0, s[8:11], s41 offen offset:0 sc1\n" \
    "buffer_load_dwordx4 v[48:51], v0, s[8:11], s41 offen offset:1024 sc1\n" \
    "buffer_load_dwordx4 v[56:59], v0, s[8:11], s41 offen offset:3072 sc1\n" \
    "buffer_load_dword v21, v13, s[8:11], s61 offen sc1\n" \
    "s_add_u32 s46, s46, 655360\n" \
    "s_cmp_eq_u32 s46, 1966080\n" \
    "s_cselect_b32 s46, 0, s46\n" \
    "s_and_b32 s33, s49, 3\n" \
    "s_lshl_b32 s38, s33, 15\n" \
    "s_mul_i32 s43, s32, 40960\n" \
    "s_add_u32 s43, s43, s21\n" \
    "s_cmp_eq_u32 s32, 63\n" \
    "s_cselect_b32 s38, 0x80000000, s38\n" \
    "s_waitcnt vmcnt(10)\n" \
    "v_max3_u32 v19, v36, v37, v38\n" \
    "s_waitcnt vmcnt(9)\n" \
    "v_max3_u32 v19, v19, v39, v40\n" \
    "v_max3_u32 v19, v19, v41, v42\n" \
    "v_max_u32 v19, v19, v43\n" \
    "s_waitcnt vmcnt(8)\n" \
    "v_mfma_f32_16x16x32_bf16 v[80:83], v[32:35], v[36:39], 0\n" \
    "v_max3_u32 v20, v32, v33, v34\n" \
    "v_mfma_f32_16x16x32_bf16 v[84:87], v[32:35], v[40:43], 0\n" \
    "v_max_u32 v20, v20, v35\n" \
    "s_waitcnt vmcnt(7)\n" \
    "v_smfmac_f32_16x16x64_bf16 v[80:83], v[24:27], v[224:231], v10\n" \
    "v_smfmac_f32_16x16x64_bf16 v[84:87], v[24:27], v[240:247], v10\n" \
    "v_max3_u32 v19, v19, v24, v25\n" \
    "v_max3_u32 v19, v19, v26, v27\n" \
    "s_waitcnt vmcnt(6)\n" \
    "v_smfmac_f32_16x16x64_bf16 v[80:83], v[28:31], v[232:239], v10\n" \
    "v_smfmac_f32_16x16x64_bf16 v[84:87], v[28:31], v[248:255], v10\n" \
    "v_max3_u32 v20, v20, v28, v29\n" \
    "v_max3_u32 v20, v20, v30, v31\n" \
    "v_max_u32 v19, v19, v20\n" \
    "v_cmp_eq_u32 vcc, -1, v19\n" \
    "s_nop 0\n" \
    "s_cbranch_vccnz .Lr_cpE1_%=\n" \
    ".Lr_cprE1_%=:\n" \
    "s_nop 0\n" \
    "v_pk_add_f32 v[80:81], v[80:81], v[82:83]\n" \
    "v_pk_add_f32 v[84:85], v[84:85], v[86:87]\n" \
    "ds_write2_b32 v5, v80, v81 offset0:0 offset1:4\n" \
    "ds_write2_b32 v5, v84, v85 offset0:64 offset1:68\n" \
    "s_waitcnt lgkmcnt(0)\n" \
    "s_barrier\n" \
    "s_barrier\n" \
    "s_memrealtime s[62:63]\n" \
    "buffer_load_dwordx4 v[60:63], v0, s[8:11], s42 offen offset:0 sc1\n" \
    "buffer_load_dwordx4 v[64:67], v0, s[8:11], s42 offen offset:1024 sc1\n" \
    "buffer_load_dwordx4 v[68:71], v0, s[8:11], s42 offen offset:2048 sc1\n" \
    "buffer_load_dwordx4 v[72:75], v0, s[8:11], s42 offen offset:3072 sc1\n" \
    "s_waitcnt vmcnt(9)\n" \
    "v_cmp_ge_u32 vcc, s49, v18\n" \
    "s_nop 0\n" \
    "s_and_b64 vcc, vcc, s[56:57]\n" \
    "s_cselect_b32 s54, 0, 1\n" \
    "s_cmp_eq_u32 s32, 63\n" \
    "s_cselect_b32 s54, 2, s54\n" \
    "s_cmp_eq_u32 s54, 1\n" \
    "s_cselect_b32 s39, s39, 0x80000000\n" \
    "s_add_u32 s40, s39, 65536\n" \
    "s_barrier\n" \
    "s_and_b32 s33, s32, s23\n" \
    "s_mul_i32 s45, s33, s24\n" \
    "s_cmp_lt_u32 s33, 64-%[pdist]\n" \
    "s_cselect_b32 s45, s45, 0x80000000\n" \
    "s_add_u32 s44, s43, %[x1]\n" \
    "s_xor_b32 s51, s51, 16384\n" \
    "s_waitcnt vmcnt(7)\n" \
    "v_max3_u32 v19, v44, v45, v46\n" \
    "s_waitcnt vmcnt(6)\n" \
    "v_max3_u32 v19, v19, v47, v48\n" \
    "v_max3_u32 v19, v19, v49, v50\n" \
    "v_max3_u32 v19, v19, v51, v52\n" \
    "v_max3_u32 v19, v19, v53, v54\n" \
    "s_waitcnt vmcnt(5)\n" \
    "v_max3_u32 v19, v19, v55, v56\n" \
    "v_max3_u32 v19, v19, v57, v58\n" \
    "s_waitcnt vmcnt(3)\n" \
    "v_max3_u32 v19, v19, v59, v60\n" \
    "v_max3_u32 v19, v19, v61, v62\n" \
    "s_waitcnt vmcnt(2)\n" \
    "v_max3_u32 v19, v19, v63, v64\n" \
    "v_max3_u32 v19, v19, v65, v66\n" \
    "s_waitcnt vmcnt(1)\n" \
    "v_max3_u32 v19, v19, v67, v68\n" \
    "v_max3_u32 v19, v19, v69, v70\n" \
    "s_waitcnt vmcnt(0)\n" \
    "v_max3_u32 v19, v19, v71, v72\n" \
    "v_max3_u32 v19, v19, v73, v74\n" \
    "v_max_u32 v19, v19, v75\n" \
    "v_cmp_eq_u32 vcc, -1, v19\n" \
    "s_nop 0\n" \
    "s_cbranch_vccnz .Lr_ctE1_%=\n" \
    ".Lr_ctrE1_%=:\n" \
    "v_mfma_f32_16x16x32_bf16 v[88:91], v[36:39], v[72:75], 0\n" \
    "v_mfma_f32_16x16x32_bf16 v[88:91], v[224:227], v[56:59], v[88:91]\n" \
    "v_mfma_f32_16x16x32_bf16 v[88:91], v[228:231], v[60:63], v[88:91]\n" \
    "v_mfma_f32_16x16x32_bf16 v[88:91], v[232:235], v[64:67], v[88:91]\n" \
    "v_mfma_f32_16x16x32_bf16 v[88:91], v[236:239], v[68:71], v[88:91]\n" \
    "v_mfma_f32_16x16x32_bf16 v[92:95], v[40:43], v[72:75], 0\n" \
    "v_mfma_f32_16x16x32_bf16 v[92:95], v[240:243], v[56:59], v[92:95]\n" \
    "v_mfma_f32_16x16x32_bf16 v[92:95], v[244:247], v[60:63], v[92:95]\n" \
    "v_mfma_f32_16x16x32_bf16 v[92:95], v[248:251], v[64:67], v[92:95]\n" \
    "v_mfma_f32_16x16x32_bf16 v[92:95], v[252:255], v[68:71], v[92:95]\n" \
    "v_mfma_f32_16x16x32_bf16 v[80:83], v[36:39], v[52:55], 0\n" \
    "v_mfma_f32_16x16x32_bf16 v[84:87], v[40:43], v[52:55], 0\n" \
    "v_mov_b32 v225, v226\n" \
    "v_mov_b32 v226, v228\n" \
    "v_mov_b32 v227, v230\n" \
    "v_mov_b32 v233, v234\n" \
    "v_mov_b32 v234, v236\n" \
    "v_mov_b32 v235, v238\n" \
    "v_mov_b32 v241, v242\n" \
    "v_mov_b32 v242, v244\n" \
    "v_mov_b32 v243, v246\n" \
    "v_mov_b32 v249, v250\n" \
    "v_mov_b32 v250, v252\n" \
    "v_mov_b32 v251, v254\n" \
    "ds_write_b128 v8, v[88:91] offset:2048\n" \
    "ds_write_b128 v8, v[92:95] offset:3072\n" \
    "v_permlane16_swap_b32 v224, v226\n" \
    "v_permlane16_swap_b32 v225, v227\n" \
    "v_permlane16_swap_b32 v232, v234\n" \
    "v_permlane16_swap_b32 v233, v235\n" \
    "v_permlane16_swap_b32 v240, v242\n" \
    "v_permlane16_swap_b32 v241, v243\n" \
    "v_permlane16_swap_b32 v248, v250\n" \
    "v_permlane16_swap_b32 v249, v251\n" \
    "v_mfma_f32_16x16x32_bf16 v[80:83], v[224:227], v[44:47], v[80:83]\n" \
    "v_mfma_f32_16x16x32_bf16 v[84:87], v[240:243], v[44:47], v[84:87]\n" \
    "v_mfma_f32_16x16x32_bf16 v[80:83], v[232:235], v[48:51], v[80:83]\n" \
    "v_mfma_f32_16x16x32_bf16 v[84:87], v[248:251], v[48:51], v[84:87]\n" \
    "s_nop 6\n" \
    "ds_write_b128 v8, v[80:83] offset:0\n" \
    "ds_write_b128 v8, v[84:87] offset:1024\n" \
    "s_waitcnt lgkmcnt(0)\n" \
    "s_barrier\n" \
    "s_mov_b32 s55, s62\n" \
    "buffer_load_dwordx4 v[192:195], v0, s[8:11], s39 offen offset:0 sc1\n" \
    "buffer_load_dwordx4 v[196:199], v0, s[8:11], s39 offen offset:1024 sc1\n" \
    "buffer_load_dwordx4 v[200:203], v0, s[8:11], s39 offen offset:2048 sc1\n" \
    "buffer_load_dwordx4 v[204:207], v0, s[8:11], s39 offen offset:3072 sc1\n" \
    "buffer_load_dwordx4 v[208:211], v0, s[8:11], s40 offen offset:0 sc1\n" \
    "buffer_load_dwordx4 v[212:215], v0, s[8:11], s40 offen offset:1024 sc1\n" \
    "buffer_load_dwordx4 v[216:219], v0, s[8:11], s40 offen offset:2048 sc1\n" \
    "buffer_load_dwordx4 v[220:223], v0, s[8:11], s40 offen offset:3072 sc1\n" \
    "s_add_u32 s64, s62, %[dh]\n" \
    "s_branch .Lr_hE1b_%=\n" \
    ".Lr_hE1a_%=:\n" \
    "s_sleep 1\n" \
    ".Lr_hE1b_%=:\n" \
    "s_memrealtime vcc\n" \
    "s_waitcnt lgkmcnt(0)\n" \
    "s_sub_i32 vcc_lo, s64, vcc_lo\n" \
    "s_cmp_gt_i32 vcc_lo, 8\n" \
    "s_cbranch_scc1 .Lr_hE1a_%=\n" \
    "s_cmp_gt_i32 vcc_lo, 0\n" \
    "s_cbranch_scc1 .Lr_hE1b_%=\n" \
    "buffer_load_dwordx2 v[16:17], v3, s[8:11], s51 offen sc1\n" \
    "s_cmp_eq_u32 s54, 0\n" \
    "s_cbranch_scc1 .Lr_cbE1_%=\n" \
    ".Lr_cbrE1_%=:\n" \
    "s_add_u32 s32, s32, 1\n" \
    "s_cmp_lt_u32 s32, 64\n" \
    "s_cbranch_scc1 .Lr_lE_%=\n" \
    "s_waitcnt vmcnt(0)\n" \
    "s_endpgm\n" \
    ".Lr_ctvD0_%=:\n" \
    "s_mov_b32 s52, 0\n" \
    "s_add_u32 s73, s32, s25\n" \
    ".Lr_ctwD0_%=:\n" \
    "s_sleep 3\n" \
    "buffer_load_dwordx2 v[16:17], v3, s[8:11], s51 offen sc1\n" \
    "s_waitcnt vmcnt(0)\n" \
    "v_cmp_ne_u32 vcc, s49, v17\n" \
    "s_nop 0\n" \
    "s_and_b64 vcc, vcc, s[56:57]\n" \
    "s_cbranch_scc0 .Lr_tvD0_%=\n" \
    "s_add_u32 s52, s52, 1\n" \
    "s_cmp_lt_u32 s52, s53\n" \
    "s_cbranch_scc1 .Lr_ctwD0_%=\n" \
    "s_mov_b32 s53, 0\n" \
    "v_mov_b32 v17, s49\n" \
    "v_mov_b32 v16, s55\n" \
    "s_branch .Lr_tvD0_%=\n" \
    ".Lr_cpD0_%=:\n" \
    "s_mov_b32 s52, 0\n" \
    "s_nop 7\n" \
    ".Lr_cqD0_%=:\n" \
    "buffer_load_dwordx4 v[36:39], v0, s[8:11], s36 offen sc1\n" \
    "buffer_load_dwordx4 v[40:43], v0, s[8:11], s37 offen sc1\n" \
    "buffer_load_dwordx4 v[32:35], v4, s[8:11], s35 offen sc1\n" \
    "buffer_load_dwordx4 v[24:27], v0, s[8:11], s35 offen sc1\n" \
    "buffer_load_dwordx4 v[28:31], v0, s[8:11], s35 offen offset:1024 sc1\n" \
    "s_waitcnt vmcnt(4)\n" \
    "v_max3_u32 v19, v36, v37, v38\n" \
    "s_waitcnt vmcnt(3)\n" \
    "v_max3_u32 v19, v19, v39, v40\n" \
    "v_max3_u32 v19, v19, v41, v42\n" \
    "v_max_u32 v19, v19, v43\n" \
    "s_waitcnt vmcnt(2)\n" \
    "v_mfma_f32_16x16x32_bf16 v[80:83], v[32:35], v[36:39], 0\n" \
    "v_max3_u32 v20, v32, v33, v34\n" \
    "v_mfma_f32_16x16x32_bf16 v[84:87], v[32:35], v[40:43], 0\n" \
    "v_max_u32 v20, v20, v35\n" \
    "s_waitcnt vmcnt(1)\n" \
    "v_smfmac_f32_16x16x64_bf16 v[80:83], v[24:27], v[192:199], v10\n" \
    "v_smfmac_f32_16x16x64_bf16 v[84:87], v[24:27], v[208:215], v10\n" \
    "v_max3_u32 v19, v19, v24, v25\n" \
    "v_max3_u32 v19, v19, v26, v27\n" \
    "s_waitcnt vmcnt(0)\n" \
    "v_smfmac_f32_16x16x64_bf16 v[80:83], v[28:31], v[200:207], v10\n" \
    "v_smfmac_f32_16x16x64_bf16 v[84:87], v[28:31], v[216:223], v10\n" \
    "v_max3_u32 v20, v20, v28, v29\n" \
    "v_max3_u32 v20, v20, v30, v31\n" \
    "v_max_u32 v19, v19, v20\n" \
    "v_cmp_eq_u32 vcc, -1, v19\n" \
    "s_nop 7\n" \
    "s_cbranch_vccz .Lr_cprD0_%=\n" \
    "s_add_u32 s52, s52, 1\n" \
    "s_cmp_lt_u32 s52, s53\n" \
    "s_cbranch_scc1 .Lr_cqD0_%=\n" \
    "s_mov_b32 s53, 0\n" \
    "s_branch .Lr_cprD0_%=\n" \
    ".Lr_ctD0_%=:\n" \
    "s_mov_b32 s52, 0\n" \
    "s_add_u32 s73, s32, s25\n" \
    ".Lr_cuD0_%=:\n" \
    "s_sleep 4\n" \
    "buffer_load_dwordx4 v[44:47], v0, s[8:11], s41 offen offset:0 sc1\n" \
    "buffer_load_dwordx4 v[48:51], v0, s[8:11], s41 offen offset:1024 sc1\n" \
    "buffer_load_dwordx4 v[52:55], v0, s[8:11], s41 offen offset:2048 sc1\n" \
    "buffer_load_dwordx4 v[56:59], v0, s[8:11], s41 offen offset:3072 sc1\n" \
    "buffer_load_dwordx4 v[60:63], v0, s[8:11], s42 offen offset:0 sc1\n" \
    "buffer_load_dwordx4 v[64:67], v0, s[8:11], s42 offen offset:1024 sc1\n" \
    "buffer_load_dwordx4 v[68:71], v0, s[8:11], s42 offen offset:2048 sc1\n" \
    "buffer_load_dwordx4 v[72:75], v0, s[8:11], s42 offen offset:3072 sc1\n" \
    "s_waitcnt vmcnt(7)\n" \
    "v_max3_u32 v19, v44, v45, v46\n" \
    "s_waitcnt vmcnt(6)\n" \
    "v_max3_u32 v19, v19, v47, v48\n" \
    "v_max3_u32 v19, v19, v49, v50\n" \
    "s_waitcnt vmcnt(5)\n" \
    "v_max3_u32 v19, v19, v51, v52\n" \
    "v_max3_u32 v19, v19, v53, v54\n" \
    "s_waitcnt vmcnt(4)\n" \
    "v_max3_u32 v19, v19, v55, v56\n" \
    "v_max3_u32 v19, v19, v57, v58\n" \
    "s_waitcnt vmcnt(3)\n" \
    "v_max3_u32 v19, v19, v59, v60\n" \
    "v_max3_u32 v19, v19, v61, v62\n" \
    "s_waitcnt vmcnt(2)\n" \
    "v_max3_u32 v19, v19, v63, v64\n" \
    "v_max3_u32 v19, v19, v65, v66\n" \
    "s_waitcnt vmcnt(1)\n" \
    "v_max3_u32 v19, v19, v67, v68\n" \
    "v_max3_u32 v19, v19, v69, v70\n" \
    "s_waitcnt vmcnt(0)\n" \
    "v_max3_u32 v19, v19, v71, v72\n" \
    "v_max3_u32 v19, v19, v73, v74\n" \
    "v_max_u32 v19, v19, v75\n" \
    "v_cmp_eq_u32 vcc, -1, v19\n" \
    "s_nop 0\n" \
    "s_cbranch_vccz .Lr_ctrD0_%=\n" \
    "s_add_u32 s52, s52, 1\n" \
    "s_cmp_lt_u32 s52, s53\n" \
    "s_cbranch_scc1 .Lr_cuD0_%=\n" \
    "s_mov_b32 s53, 0\n" \
    "s_branch .Lr_ctrD0_%=\n" \
    ".Lr_cbD0_%=:\n" \
    "s_add_u32 s73, s32, s25\n" \
    "s_mov_b32 s50, s49\n" \
    "s_mov_b32 s52, 0\n" \
    ".Lr_ccD0_%=:\n" \
    "buffer_load_dword v18, v2, s[8:11], 0 offen sc1\n" \
    "s_waitcnt vmcnt(0)\n" \
    "v_cmp_ge_u32 vcc, s50, v18\n" \
    "s_nop 0\n" \
    "s_and_b64 vcc, vcc, s[56:57]\n" \
    "s_cbranch_scc0 .Lr_cdD0_%=\n" \
    "s_sleep 2\n" \
    "s_add_u32 s52, s52, 1\n" \
    "s_cmp_lt_u32 s52, s53\n" \
    "s_cbranch_scc1 .Lr_ccD0_%=\n" \
    "s_mov_b32 s53, 0\n" \
    "s_branch .Lr_cdD0_%=\n" \
    ".Lr_cdD0_%=:\n" \
    "s_and_b32 s33, s49, %[brn]-1\n" \
    "s_mul_i32 s39, s33, 2621440\n" \
    "s_add_u32 s39, s39, s19\n" \
    "s_lshr_b32 s33, s39, 8\n" \
    "s_xor_b32 s33, s33, s27\n" \
    "s_and_b32 s33, s33, 0x2000\n" \
    "s_or_b32 s39, s39, s33\n" \
    "s_add_u32 s40, s39, 65536\n" \
    "s_waitcnt vmcnt(0)\n" \
    "buffer_load_dwordx4 v[224:227], v0, s[8:11], s39 offen offset:0 sc1\n" \
    "buffer_load_dwordx4 v[228:231], v0, s[8:11], s39 offen offset:1024 sc1\n" \
    "buffer_load_dwordx4 v[232:235], v0, s[8:11], s39 offen offset:2048 sc1\n" \
    "buffer_load_dwordx4 v[236:239], v0, s[8:11], s39 offen offset:3072 sc1\n" \
    "buffer_load_dwordx4 v[240:243], v0, s[8:11], s40 offen offset:0 sc1\n" \
    "buffer_load_dwordx4 v[244:247], v0, s[8:11], s40 offen offset:1024 sc1\n" \
    "buffer_load_dwordx4 v[248:251], v0, s[8:11], s40 offen offset:2048 sc1\n" \
    "buffer_load_dwordx4 v[252:255], v0, s[8:11], s40 offen offset:3072 sc1\n" \
    "s_waitcnt vmcnt(0)\n" \
    "s_branch .Lr_cbrD0_%=\n" \
    ".Lr_ctvD1_%=:\n" \
    "s_mov_b32 s52, 0\n" \
    "s_add_u32 s73, s32, s25\n" \
    ".Lr_ctwD1_%=:\n" \
    "s_sleep 3\n" \
    "buffer_load_dwordx2 v[16:17], v3, s[8:11], s51 offen sc1\n" \
    "s_waitcnt vmcnt(0)\n" \
    "v_cmp_ne_u32 vcc, s49, v17\n" \
    "s_nop 0\n" \
    "s_and_b64 vcc, vcc, s[56:57]\n" \
    "s_cbranch_scc0 .Lr_tvD1_%=\n" \
    "s_add_u32 s52, s52, 1\n" \
    "s_cmp_lt_u32 s52, s53\n" \
    "s_cbranch_scc1 .Lr_ctwD1_%=\n" \
    "s_mov_b32 s53, 0\n" \
    "v_mov_b32 v17, s49\n" \
    "v_mov_b32 v16, s55\n" \
    "s_branch .Lr_tvD1_%=\n" \
    ".Lr_cpD1_%=:\n" \
    "s_mov_b32 s52, 0\n" \
    "s_nop 7\n" \
    ".Lr_cqD1_%=:\n" \
    "buffer_load_dwordx4 v[36:39], v0, s[8:11], s36 offen sc1\n" \
    "buffer_load_dwordx4 v[40:43], v0, s[8:11], s37 offen sc1\n" \
    "buffer_load_dwordx4 v[32:35], v4, s[8:11], s35 offen sc1\n" \
    "buffer_load_dwordx4 v[24:27], v0, s[8:11], s35 offen sc1\n" \
    "buffer_load_dwordx4 v[28:31], v0, s[8:11], s35 offen offset:1024 sc1\n" \
    "s_waitcnt vmcnt(4)\n" \
    "v_max3_u32 v19, v36, v37, v38\n" \
    "s_waitcnt vmcnt(3)\n" \
    "v_max3_u32 v19, v19, v39, v40\n" \
    "v_max3_u32 v19, v19, v41, v42\n" \
    "v_max_u32 v19, v19, v43\n" \
    "s_waitcnt vmcnt(2)\n" \
    "v_mfma_f32_16x16x32_bf16 v[80:83], v[32:35], v[36:39], 0\n" \
    "v_max3_u32 v20, v32, v33, v34\n" \
    "v_mfma_f32_16x16x32_bf16 v[84:87], v[32:35], v[40:43], 0\n" \
    "v_max_u32 v20, v20, v35\n" \
    "s_waitcnt vmcnt(1)\n" \
    "v_smfmac_f32_16x16x64_bf16 v[80:83], v[24:27], v[224:231], v10\n" \
    "v_smfmac_f32_16x16x64_bf16 v[84:87], v[24:27], v[240:247], v10\n" \
    "v_max3_u32 v19, v19, v24, v25\n" \
    "v_max3_u32 v19, v19, v26, v27\n" \
    "s_waitcnt vmcnt(0)\n" \
    "v_smfmac_f32_16x16x64_bf16 v[80:83], v[28:31], v[232:239], v10\n" \
    "v_smfmac_f32_16x16x64_bf16 v[84:87], v[28:31], v[248:255], v10\n" \
    "v_max3_u32 v20, v20, v28, v29\n" \
    "v_max3_u32 v20, v20, v30, v31\n" \
    "v_max_u32 v19, v19, v20\n" \
    "v_cmp_eq_u32 vcc, -1, v19\n" \
    "s_nop 7\n" \
    "s_cbranch_vccz .Lr_cprD1_%=\n" \
    "s_add_u32 s52, s52, 1\n" \
    "s_cmp_lt_u32 s52, s53\n" \
    "s_cbranch_scc1 .Lr_cqD1_%=\n" \
    "s_mov_b32 s53, 0\n" \
    "s_branch .Lr_cprD1_%=\n" \
    ".Lr_ctD1_%=:\n" \
    "s_mov_b32 s52, 0\n" \
    "s_add_u32 s73, s32, s25\n" \
    ".Lr_cuD1_%=:\n" \
    "s_sleep 4\n" \
    "buffer_load_dwordx4 v[44:47], v0, s[8:11], s41 offen offset:0 sc1\n" \
    "buffer_load_dwordx4 v[48:51], v0, s[8:11], s41 offen offset:1024 sc1\n" \
    "buffer_load_dwordx4 v[52:55], v0, s[8:11], s41 offen offset:2048 sc1\n" \
    "buffer_load_dwordx4 v[56:59], v0, s[8:11], s41 offen offset:3072 sc1\n" \
    "buffer_load_dwordx4 v[60:63], v0, s[8:11], s42 offen offset:0 sc1\n" \
    "buffer_load_dwordx4 v[64:67], v0, s[8:11], s42 offen offset:1024 sc1\n" \
    "buffer_load_dwordx4 v[68:71], v0, s[8:11], s42 offen offset:2048 sc1\n" \
    "buffer_load_dwordx4 v[72:75], v0, s[8:11], s42 offen offset:3072 sc1\n" \
    "s_waitcnt vmcnt(7)\n" \
    "v_max3_u32 v19, v44, v45, v46\n" \
    "s_waitcnt vmcnt(6)\n" \
    "v_max3_u32 v19, v19, v47, v48\n" \
    "v_max3_u32 v19, v19, v49, v50\n" \
    "s_waitcnt vmcnt(5)\n" \
    "v_max3_u32 v19, v19, v51, v52\n" \
    "v_max3_u32 v19, v19, v53, v54\n" \
    "s_waitcnt vmcnt(4)\n" \
    "v_max3_u32 v19, v19, v55, v56\n" \
    "v_max3_u32 v19, v19, v57, v58\n" \
    "s_waitcnt vmcnt(3)\n" \
    "v_max3_u32 v19, v19, v59, v60\n" \
    "v_max3_u32 v19, v19, v61, v62\n" \
    "s_waitcnt vmcnt(2)\n" \
    "v_max3_u32 v19, v19, v63, v64\n" \
    "v_max3_u32 v19, v19, v65, v66\n" \
    "s_waitcnt vmcnt(1)\n" \
    "v_max3_u32 v19, v19, v67, v68\n" \
    "v_max3_u32 v19, v19, v69, v70\n" \
    "s_waitcnt vmcnt(0)\n" \
    "v_max3_u32 v19, v19, v71, v72\n" \
    "v_max3_u32 v19, v19, v73, v74\n" \
    "v_max_u32 v19, v19, v75\n" \
    "v_cmp_eq_u32 vcc, -1, v19\n" \
    "s_nop 0\n" \
    "s_cbranch_vccz .Lr_ctrD1_%=\n" \
    "s_add_u32 s52, s52, 1\n" \
    "s_cmp_lt_u32 s52, s53\n" \
    "s_cbranch_scc1 .Lr_cuD1_%=\n" \
    "s_mov_b32 s53, 0\n" \
    "s_branch .Lr_ctrD1_%=\n" \
    ".Lr_cbD1_%=:\n" \
    "s_add_u32 s73, s32, s25\n" \
    "s_mov_b32 s50, s49\n" \
    "s_mov_b32 s52, 0\n" \
    ".Lr_ccD1_%=:\n" \
    "buffer_load_dword v18, v2, s[8:11], 0 offen sc1\n" \
    "s_waitcnt vmcnt(0)\n" \
    "v_cmp_ge_u32 vcc, s50, v18\n" \
    "s_nop 0\n" \
    "s_and_b64 vcc, vcc, s[56:57]\n" \
    "s_cbranch_scc0 .Lr_cdD1_%=\n" \
    "s_sleep 2\n" \
    "s_add_u32 s52, s52, 1\n" \
    "s_cmp_lt_u32 s52, s53\n" \
    "s_cbranch_scc1 .Lr_ccD1_%=\n" \
    "s_mov_b32 s53, 0\n" \
    "s_branch .Lr_cdD1_%=\n" \
    ".Lr_cdD1_%=:\n" \
    "s_and_b32 s33, s49, %[brn]-1\n" \
    "s_mul_i32 s39, s33, 2621440\n" \
    "s_add_u32 s39, s39, s19\n" \
    "s_lshr_b32 s33, s39, 8\n" \
    "s_xor_b32 s33, s33, s27\n" \
    "s_and_b32 s33, s33, 0x2000\n" \
    "s_or_b32 s39, s39, s33\n" \
    "s_add_u32 s40, s39, 65536\n" \
    "s_waitcnt vmcnt(0)\n" \
    "buffer_load_dwordx4 v[192:195], v0, s[8:11], s39 offen offset:0 sc1\n" \
    "buffer_load_dwordx4 v[196:199], v0, s[8:11], s39 offen offset:1024 sc1\n" \
    "buffer_load_dwordx4 v[200:203], v0, s[8:11], s39 offen offset:2048 sc1\n" \
    "buffer_load_dwordx4 v[204:207], v0, s[8:11], s39 offen offset:3072 sc1\n" \
    "buffer_load_dwordx4 v[208:211], v0, s[8:11], s40 offen offset:0 sc1\n" \
    "buffer_load_dwordx4 v[212:215], v0, s[8:11], s40 offen offset:1024 sc1\n" \
    "buffer_load_dwordx4 v[216:219], v0, s[8:11], s40 offen offset:2048 sc1\n" \
    "buffer_load_dwordx4 v[220:223], v0, s[8:11], s40 offen offset:3072 sc1\n" \
    "s_waitcnt vmcnt(0)\n" \
    "s_branch .Lr_cbrD1_%=\n" \
    ".Lr_ctvA0_%=:\n" \
    "s_mov_b32 s52, 0\n" \
    "s_add_u32 s73, s32, s25\n" \
    ".Lr_ctwA0_%=:\n" \
    "s_sleep 3\n" \
    "buffer_load_dwordx2 v[16:17], v3, s[8:11], s51 offen sc1\n" \
    "s_waitcnt vmcnt(0)\n" \
    "v_cmp_ne_u32 vcc, s49, v17\n" \
    "s_nop 0\n" \
    "s_and_b64 vcc, vcc, s[56:57]\n" \
    "s_cbranch_scc0 .Lr_tvA0_%=\n" \
    "s_add_u32 s52, s52, 1\n" \
    "s_cmp_lt_u32 s52, s53\n" \
    "s_cbranch_scc1 .Lr_ctwA0_%=\n" \
    "s_mov_b32 s53, 0\n" \
    "v_mov_b32 v17, s49\n" \
    "v_mov_b32 v16, s55\n" \
    "s_branch .Lr_tvA0_%=\n" \
    ".Lr_cpA0_%=:\n" \
    "s_mov_b32 s52, 0\n" \
    "s_nop 7\n" \
    ".Lr_cqA0_%=:\n" \
    "buffer_load_dwordx4 v[36:39], v0, s[8:11], s36 offen sc1\n" \
    "buffer_load_dwordx4 v[40:43], v0, s[8:11], s37 offen sc1\n" \
    "buffer_load_dwordx4 v[32:35], v4, s[8:11], s35 offen sc1\n" \
    "buffer_load_dwordx4 v[24:27], v0, s[8:11], s35 offen sc1\n" \
    "buffer_load_dwordx4 v[28:31], v0, s[8:11], s35 offen offset:1024 sc1\n" \
    "s_waitcnt vmcnt(4)\n" \
    "v_max3_u32 v19, v36, v37, v38\n" \
    "s_waitcnt vmcnt(3)\n" \
    "v_max3_u32 v19, v19, v39, v40\n" \
    "v_max3_u32 v19, v19, v41, v42\n" \
    "v_max_u32 v19, v19, v43\n" \
    "s_waitcnt vmcnt(2)\n" \
    "v_mfma_f32_16x16x32_bf16 v[80:83], v[32:35], v[36:39], 0\n" \
    "v_max3_u32 v20, v32, v33, v34\n" \
    "v_mfma_f32_16x16x32_bf16 v[84:87], v[32:35], v[40:43], 0\n" \
    "v_max_u32 v20, v20, v35\n" \
    "s_waitcnt vmcnt(1)\n" \
    "v_smfmac_f32_16x16x64_bf16 v[80:83], v[24:27], v[192:199], v10\n" \
    "v_smfmac_f32_16x16x64_bf16 v[84:87], v[24:27], v[208:215], v10\n" \
    "v_max3_u32 v19, v19, v24, v25\n" \
    "v_max3_u32 v19, v19, v26, v27\n" \
    "s_waitcnt vmcnt(0)\n" \
    "v_smfmac_f32_16x16x64_bf16 v[80:83], v[28:31], v[200:207], v10\n" \
    "v_smfmac_f32_16x16x64_bf16 v[84:87], v[28:31], v[216:223], v10\n" \
    "v_max3_u32 v20, v20, v28, v29\n" \
    "v_max3_u32 v20, v20, v30, v31\n" \
    "v_max_u32 v19, v19, v20\n" \
    "v_cmp_eq_u32 vcc, -1, v19\n" \
    "s_nop 7\n" \
    "s_cbranch_vccz .Lr_cprA0_%=\n" \
    "s_add_u32 s52, s52, 1\n" \
    "s_cmp_lt_u32 s52, s53\n" \
    "s_cbranch_scc1 .Lr_cqA0_%=\n" \
    "s_mov_b32 s53, 0\n" \
    "s_branch .Lr_cprA0_%=\n" \
    ".Lr_ctA0_%=:\n" \
    "s_mov_b32 s52, 0\n" \
    "s_add_u32 s73, s32, s25\n" \
    ".Lr_cuA0_%=:\n" \
    "s_sleep 4\n" \
    "buffer_load_dwordx4 v[44:47], v0, s[8:11], s41 offen offset:0 sc1\n" \
    "buffer_load_dwordx4 v[48:51], v0, s[8:11], s41 offen offset:1024 sc1\n" \
    "buffer_load_dwordx4 v[52:55], v0, s[8:11], s41 offen offset:2048 sc1\n" \
    "buffer_load_dwordx4 v[56:59], v0, s[8:11], s41 offen offset:3072 sc1\n" \
    "buffer_load_dwordx4 v[60:63], v0, s[8:11], s42 offen offset:0 sc1\n" \
    "buffer_load_dwordx4 v[64:67], v0, s[8:11], s42 offen offset:1024 sc1\n" \
    "buffer_load_dwordx4 v[68:71], v0, s[8:11], s42 offen offset:2048 sc1\n" \
    "buffer_load_dwordx4 v[72:75], v0, s[8:11], s42 offen offset:3072 sc1\n" \
    "s_waitcnt vmcnt(7)\n" \
    "v_max3_u32 v19, v44, v45, v46\n" \
    "s_waitcnt vmcnt(6)\n" \
    "v_max3_u32 v19, v19, v47, v48\n" \
    "v_max3_u32 v19, v19, v49, v50\n" \
    "s_waitcnt vmcnt(5)\n" \
    "v_max3_u32 v19, v19, v51, v52\n" \
    "v_max3_u32 v19, v19, v53, v54\n" \
    "s_waitcnt vmcnt(4)\n" \
    "v_max3_u32 v19, v19, v55, v56\n" \
    "v_max3_u32 v19, v19, v57, v58\n" \
    "s_waitcnt vmcnt(3)\n" \
    "v_max3_u32 v19, v19, v59, v60\n" \
    "v_max3_u32 v19, v19, v61, v62\n" \
    "s_waitcnt vmcnt(2)\n" \
    "v_max3_u32 v19, v19, v63, v64\n" \
    "v_max3_u32 v19, v19, v65, v66\n" \
    "s_waitcnt vmcnt(1)\n" \
    "v_max3_u32 v19, v19, v67, v68\n" \
    "v_max3_u32 v19, v19, v69, v70\n" \
    "s_waitcnt vmcnt(0)\n" \
    "v_max3_u32 v19, v19, v71, v72\n" \
    "v_max3_u32 v19, v19, v73, v74\n" \
    "v_max_u32 v19, v19, v75\n" \
    "v_cmp_eq_u32 vcc, -1, v19\n" \
    "s_nop 0\n" \
    "s_cbranch_vccz .Lr_ctrA0_%=\n" \
    "s_add_u32 s52, s52, 1\n" \
    "s_cmp_lt_u32 s52, s53\n" \
    "s_cbranch_scc1 .Lr_cuA0_%=\n" \
    "s_mov_b32 s53, 0\n" \
    "s_branch .Lr_ctrA0_%=\n" \
    ".Lr_cbA0_%=:\n" \
    "s_add_u32 s73, s32, s25\n" \
    "s_mov_b32 s50, s49\n" \
    "s_mov_b32 s52, 0\n" \
    ".Lr_ccA0_%=:\n" \
    "buffer_load_dword v18, v2, s[8:11], 0 offen sc1\n" \
    "s_waitcnt vmcnt(0)\n" \
    "v_cmp_ge_u32 vcc, s50, v18\n" \
    "s_nop 0\n" \
    "s_and_b64 vcc, vcc, s[56:57]\n" \
    "s_cbranch_scc0 .Lr_cdA0_%=\n" \
    "s_sleep 2\n" \
    "s_add_u32 s52, s52, 1\n" \
    "s_cmp_lt_u32 s52, s53\n" \
    "s_cbranch_scc1 .Lr_ccA0_%=\n" \
    "s_mov_b32 s53, 0\n" \
    "s_branch .Lr_cdA0_%=\n" \
    ".Lr_cdA0_%=:\n" \
    "s_and_b32 s33, s49, %[brn]-1\n" \
    "s_mul_i32 s39, s33, 2621440\n" \
    "s_add_u32 s39, s39, s19\n" \
    "s_lshr_b32 s33, s39, 8\n" \
    "s_xor_b32 s33, s33, s27\n" \
    "s_and_b32 s33, s33, 0x2000\n" \
    "s_or_b32 s39, s39, s33\n" \
    "s_add_u32 s40, s39, 65536\n" \
    "s_waitcnt vmcnt(0)\n" \
    "buffer_load_dwordx4 v[224:227], v0, s[8:11], s39 offen offset:0 sc1\n" \
    "buffer_load_dwordx4 v[228:231], v0, s[8:11], s39 offen offset:1024 sc1\n" \
    "buffer_load_dwordx4 v[232:235], v0, s[8:11], s39 offen offset:2048 sc1\n" \
    "buffer_load_dwordx4 v[236:239], v0, s[8:11], s39 offen offset:3072 sc1\n" \
    "buffer_load_dwordx4 v[240:243], v0, s[8:11], s40 offen offset:0 sc1\n" \
    "buffer_load_dwordx4 v[244:247], v0, s[8:11], s40 offen offset:1024 sc1\n" \
    "buffer_load_dwordx4 v[248:251], v0, s[8:11], s40 offen offset:2048 sc1\n" \
    "buffer_load_dwordx4 v[252:255], v0, s[8:11], s40 offen offset:3072 sc1\n" \
    "s_waitcnt vmcnt(0)\n" \
    "s_branch .Lr_cbrA0_%=\n" \
    ".Lr_ctvA1_%=:\n" \
    "s_mov_b32 s52, 0\n" \
    "s_add_u32 s73, s32, s25\n" \
    ".Lr_ctwA1_%=:\n" \
    "s_sleep 3\n" \
    "buffer_load_dwordx2 v[16:17], v3, s[8:11], s51 offen sc1\n" \
    "s_waitcnt vmcnt(0)\n" \
    "v_cmp_ne_u32 vcc, s49, v17\n" \
    "s_nop 0\n" \
    "s_and_b64 vcc, vcc, s[56:57]\n" \
    "s_cbranch_scc0 .Lr_tvA1_%=\n" \
    "s_add_u32 s52, s52, 1\n" \
    "s_cmp_lt_u32 s52, s53\n" \
    "s_cbranch_scc1 .Lr_ctwA1_%=\n" \
    "s_mov_b32 s53, 0\n" \
    "v_mov_b32 v17, s49\n" \
    "v_mov_b32 v16, s55\n" \
    "s_branch .Lr_tvA1_%=\n" \
    ".Lr_cpA1_%=:\n" \
    "s_mov_b32 s52, 0\n" \
    "s_nop 7\n" \
    ".Lr_cqA1_%=:\n" \
    "buffer_load_dwordx4 v[36:39], v0, s[8:11], s36 offen sc1\n" \
    "buffer_load_dwordx4 v[40:43], v0, s[8:11], s37 offen sc1\n" \
    "buffer_load_dwordx4 v[32:35], v4, s[8:11], s35 offen sc1\n" \
    "buffer_load_dwordx4 v[24:27], v0, s[8:11], s35 offen sc1\n" \
    "buffer_load_dwordx4 v[28:31], v0, s[8:11], s35 offen offset:1024 sc1\n" \
    "s_waitcnt vmcnt(4)\n" \
    "v_max3_u32 v19, v36, v37, v38\n" \
    "s_waitcnt vmcnt(3)\n" \
    "v_max3_u32 v19, v19, v39, v40\n" \
    "v_max3_u32 v19, v19, v41, v42\n" \
    "v_max_u32 v19, v19, v43\n" \
    "s_waitcnt vmcnt(2)\n" \
    "v_mfma_f32_16x16x32_bf16 v[80:83], v[32:35], v[36:39], 0\n" \
    "v_max3_u32 v20, v32, v33, v34\n" \
    "v_mfma_f32_16x16x32_bf16 v[84:87], v[32:35], v[40:43], 0\n" \
    "v_max_u32 v20, v20, v35\n" \
    "s_waitcnt vmcnt(1)\n" \
    "v_smfmac_f32_16x16x64_bf16 v[80:83], v[24:27], v[224:231], v10\n" \
    "v_smfmac_f32_16x16x64_bf16 v[84:87], v[24:27], v[240:247], v10\n" \
    "v_max3_u32 v19, v19, v24, v25\n" \
    "v_max3_u32 v19, v19, v26, v27\n" \
    "s_waitcnt vmcnt(0)\n" \
    "v_smfmac_f32_16x16x64_bf16 v[80:83], v[28:31], v[232:239], v10\n" \
    "v_smfmac_f32_16x16x64_bf16 v[84:87], v[28:31], v[248:255], v10\n" \
    "v_max3_u32 v20, v20, v28, v29\n" \
    "v_max3_u32 v20, v20, v30, v31\n" \
    "v_max_u32 v19, v19, v20\n" \
    "v_cmp_eq_u32 vcc, -1, v19\n" \
    "s_nop 7\n" \
    "s_cbranch_vccz .Lr_cprA1_%=\n" \
    "s_add_u32 s52, s52, 1\n" \
    "s_cmp_lt_u32 s52, s53\n" \
    "s_cbranch_scc1 .Lr_cqA1_%=\n" \
    "s_mov_b32 s53, 0\n" \
    "s_branch .Lr_cprA1_%=\n" \
    ".Lr_ctA1_%=:\n" \
    "s_mov_b32 s52, 0\n" \
    "s_add_u32 s73, s32, s25\n" \
    ".Lr_cuA1_%=:\n" \
    "s_sleep 4\n" \
    "buffer_load_dwordx4 v[44:47], v0, s[8:11], s41 offen offset:0 sc1\n" \
    "buffer_load_dwordx4 v[48:51], v0, s[8:11], s41 offen offset:1024 sc1\n" \
    "buffer_load_dwordx4 v[52:55], v0, s[8:11], s41 offen offset:2048 sc1\n" \
    "buffer_load_dwordx4 v[56:59], v0, s[8:11], s41 offen offset:3072 sc1\n" \
    "buffer_load_dwordx4 v[60:63], v0, s[8:11], s42 offen offset:0 sc1\n" \
    "buffer_load_dwordx4 v[64:67], v0, s[8:11], s42 offen offset:1024 sc1\n" \
    "buffer_load_dwordx4 v[68:71], v0, s[8:11], s42 offen offset:2048 sc1\n" \
    "buffer_load_dwordx4 v[72:75], v0, s[8:11], s42 offen offset:3072 sc1\n" \
    "s_waitcnt vmcnt(7)\n" \
    "v_max3_u32 v19, v44, v45, v46\n" \
    "s_waitcnt vmcnt(6)\n" \
    "v_max3_u32 v19, v19, v47, v48\n" \
    "v_max3_u32 v19, v19, v49, v50\n" \
    "s_waitcnt vmcnt(5)\n" \
    "v_max3_u32 v19, v19, v51, v52\n" \
    "v_max3_u32 v19, v19, v53, v54\n" \
    "s_waitcnt vmcnt(4)\n" \
    "v_max3_u32 v19, v19, v55, v56\n" \
    "v_max3_u32 v19, v19, v57, v58\n" \
    "s_waitcnt vmcnt(3)\n" \
    "v_max3_u32 v19, v19, v59, v60\n" \
    "v_max3_u32 v19, v19, v61, v62\n" \
    "s_waitcnt vmcnt(2)\n" \
    "v_max3_u32 v19, v19, v63, v64\n" \
    "v_max3_u32 v19, v19, v65, v66\n" \
    "s_waitcnt vmcnt(1)\n" \
    "v_max3_u32 v19, v19, v67, v68\n" \
    "v_max3_u32 v19, v19, v69, v70\n" \
    "s_waitcnt vmcnt(0)\n" \
    "v_max3_u32 v19, v19, v71, v72\n" \
    "v_max3_u32 v19, v19, v73, v74\n" \
    "v_max_u32 v19, v19, v75\n" \
    "v_cmp_eq_u32 vcc, -1, v19\n" \
    "s_nop 0\n" \
    "s_cbranch_vccz .Lr_ctrA1_%=\n" \
    "s_add_u32 s52, s52, 1\n" \
    "s_cmp_lt_u32 s52, s53\n" \
    "s_cbranch_scc1 .Lr_cuA1_%=\n" \
    "s_mov_b32 s53, 0\n" \
    "s_branch .Lr_ctrA1_%=\n" \
    ".Lr_cbA1_%=:\n" \
    "s_add_u32 s73, s32, s25\n" \
    "s_mov_b32 s50, s49\n" \
    "s_mov_b32 s52, 0\n" \
    ".Lr_ccA1_%=:\n" \
    "buffer_load_dword v18, v2, s[8:11], 0 offen sc1\n" \
    "s_waitcnt vmcnt(0)\n" \
    "v_cmp_ge_u32 vcc, s50, v18\n" \
    "s_nop 0\n" \
    "s_and_b64 vcc, vcc, s[56:57]\n" \
    "s_cbranch_scc0 .Lr_cdA1_%=\n" \
    "s_sleep 2\n" \
    "s_add_u32 s52, s52, 1\n" \
    "s_cmp_lt_u32 s52, s53\n" \
    "s_cbranch_scc1 .Lr_ccA1_%=\n" \
    "s_mov_b32 s53, 0\n" \
    "s_branch .Lr_cdA1_%=\n" \
    ".Lr_cdA1_%=:\n" \
    "s_and_b32 s33, s49, %[brn]-1\n" \
    "s_mul_i32 s39, s33, 2621440\n" \
    "s_add_u32 s39, s39, s19\n" \
    "s_lshr_b32 s33, s39, 8\n" \
    "s_xor_b32 s33, s33, s27\n" \
    "s_and_b32 s33, s33, 0x2000\n" \
    "s_or_b32 s39, s39, s33\n" \
    "s_add_u32 s40, s39, 65536\n" \
    "s_waitcnt vmcnt(0)\n" \
    "buffer_load_dwordx4 v[192:195], v0, s[8:11], s39 offen offset:0 sc1\n" \
    "buffer_load_dwordx4 v[196:199], v0, s[8:11], s39 offen offset:1024 sc1\n" \
    "buffer_load_dwordx4 v[200:203], v0, s[8:11], s39 offen offset:2048 sc1\n" \
    "buffer_load_dwordx4 v[204:207], v0, s[8:11], s39 offen offset:3072 sc1\n" \
    "buffer_load_dwordx4 v[208:211], v0, s[8:11], s40 offen offset:0 sc1\n" \
    "buffer_load_dwordx4 v[212:215], v0, s[8:11], s40 offen offset:1024 sc1\n" \
    "buffer_load_dwordx4 v[216:219], v0, s[8:11], s40 offen offset:2048 sc1\n" \
    "buffer_load_dwordx4 v[220:223], v0, s[8:11], s40 offen offset:3072 sc1\n" \
    "s_waitcnt vmcnt(0)\n" \
    "s_branch .Lr_cbrA1_%=\n" \
    ".Lr_ctvB0_%=:\n" \
    "s_mov_b32 s52, 0\n" \
    "s_add_u32 s73, s32, s25\n" \
    ".Lr_ctwB0_%=:\n" \
    "s_sleep 3\n" \
    "buffer_load_dwordx2 v[16:17], v3, s[8:11], s51 offen sc1\n" \
    "s_waitcnt vmcnt(0)\n" \
    "v_cmp_ne_u32 vcc, s49, v17\n" \
    "s_nop 0\n" \
    "s_and_b64 vcc, vcc, s[56:57]\n" \
    "s_cbranch_scc0 .Lr_tvB0_%=\n" \
    "s_add_u32 s52, s52, 1\n" \
    "s_cmp_lt_u32 s52, s53\n" \
    "s_cbranch_scc1 .Lr_ctwB0_%=\n" \
    "s_mov_b32 s53, 0\n" \
    "v_mov_b32 v17, s49\n" \
    "v_mov_b32 v16, s55\n" \
    "s_branch .Lr_tvB0_%=\n" \
    ".Lr_cpB0_%=:\n" \
    "s_mov_b32 s52, 0\n" \
    "s_nop 7\n" \
    ".Lr_cqB0_%=:\n" \
    "buffer_load_dwordx4 v[36:39], v0, s[8:11], s36 offen sc1\n" \
    "buffer_load_dwordx4 v[40:43], v0, s[8:11], s37 offen sc1\n" \
    "buffer_load_dwordx4 v[32:35], v4, s[8:11], s35 offen sc1\n" \
    "buffer_load_dwordx4 v[24:27], v0, s[8:11], s35 offen sc1\n" \
    "buffer_load_dwordx4 v[28:31], v0, s[8:11], s35 offen offset:1024 sc1\n" \
    "s_waitcnt vmcnt(4)\n" \
    "v_max3_u32 v19, v36, v37, v38\n" \
    "s_waitcnt vmcnt(3)\n" \
    "v_max3_u32 v19, v19, v39, v40\n" \
    "v_max3_u32 v19, v19, v41, v42\n" \
    "v_max_u32 v19, v19, v43\n" \
    "s_waitcnt vmcnt(2)\n" \
    "v_mfma_f32_16x16x32_bf16 v[80:83], v[32:35], v[36:39], 0\n" \
    "v_max3_u32 v20, v32, v33, v34\n" \
    "v_mfma_f32_16x16x32_bf16 v[84:87], v[32:35], v[40:43], 0\n" \
    "v_max_u32 v20, v20, v35\n" \
    "s_waitcnt vmcnt(1)\n" \
    "v_smfmac_f32_16x16x64_bf16 v[80:83], v[24:27], v[192:199], v10\n" \
    "v_smfmac_f32_16x16x64_bf16 v[84:87], v[24:27], v[208:215], v10\n" \
    "v_max3_u32 v19, v19, v24, v25\n" \
    "v_max3_u32 v19, v19, v26, v27\n" \
    "s_waitcnt vmcnt(0)\n" \
    "v_smfmac_f32_16x16x64_bf16 v[80:83], v[28:31], v[200:207], v10\n" \
    "v_smfmac_f32_16x16x64_bf16 v[84:87], v[28:31], v[216:223], v10\n" \
    "v_max3_u32 v20, v20, v28, v29\n" \
    "v_max3_u32 v20, v20, v30, v31\n" \
    "v_max_u32 v19, v19, v20\n" \
    "v_cmp_eq_u32 vcc, -1, v19\n" \
    "s_nop 7\n" \
    "s_cbranch_vccz .Lr_cprB0_%=\n" \
    "s_add_u32 s52, s52, 1\n" \
    "s_cmp_lt_u32 s52, s53\n" \
    "s_cbranch_scc1 .Lr_cqB0_%=\n" \
    "s_mov_b32 s53, 0\n" \
    "s_branch .Lr_cprB0_%=\n" \
    ".Lr_ctB0_%=:\n" \
    "s_mov_b32 s52, 0\n" \
    "s_add_u32 s73, s32, s25\n" \
    ".Lr_cuB0_%=:\n" \
    "s_sleep 4\n" \
    "buffer_load_dwordx4 v[44:47], v0, s[8:11], s41 offen offset:0 sc1\n" \
    "buffer_load_dwordx4 v[48:51], v0, s[8:11], s41 offen offset:1024 sc1\n" \
    "buffer_load_dwordx4 v[52:55], v0, s[8:11], s41 offen offset:2048 sc1\n" \
    "buffer_load_dwordx4 v[56:59], v0, s[8:11], s41 offen offset:3072 sc1\n" \
    "buffer_load_dwordx4 v[60:63], v0, s[8:11], s42 offen offset:0 sc1\n" \
    "buffer_load_dwordx4 v[64:67], v0, s[8:11], s42 offen offset:1024 sc1\n" \
    "buffer_load_dwordx4 v[68:71], v0, s[8:11], s42 offen offset:2048 sc1\n" \
    "buffer_load_dwordx4 v[72:75], v0, s[8:11], s42 offen offset:3072 sc1\n" \
    "s_waitcnt vmcnt(7)\n" \
    "v_max3_u32 v19, v44, v45, v46\n" \
    "s_waitcnt vmcnt(6)\n" \
    "v_max3_u32 v19, v19, v47, v48\n" \
    "v_max3_u32 v19, v19, v49, v50\n" \
    "s_waitcnt vmcnt(5)\n" \
    "v_max3_u32 v19, v19, v51, v52\n" \
    "v_max3_u32 v19, v19, v53, v54\n" \
    "s_waitcnt vmcnt(4)\n" \
    "v_max3_u32 v19, v19, v55, v56\n" \
    "v_max3_u32 v19, v19, v57, v58\n" \
    "s_waitcnt vmcnt(3)\n" \
    "v_max3_u32 v19, v19, v59, v60\n" \
    "v_max3_u32 v19, v19, v61, v62\n" \
    "s_waitcnt vmcnt(2)\n" \
    "v_max3_u32 v19, v19, v63, v64\n" \
    "v_max3_u32 v19, v19, v65, v66\n" \
    "s_waitcnt vmcnt(1)\n" \
    "v_max3_u32 v19, v19, v67, v68\n" \
    "v_max3_u32 v19, v19, v69, v70\n" \
    "s_waitcnt vmcnt(0)\n" \
    "v_max3_u32 v19, v19, v71, v72\n" \
    "v_max3_u32 v19, v19, v73, v74\n" \
    "v_max_u32 v19, v19, v75\n" \
    "v_cmp_eq_u32 vcc, -1, v19\n" \
    "s_nop 0\n" \
    "s_cbranch_vccz .Lr_ctrB0_%=\n" \
    "s_add_u32 s52, s52, 1\n" \
    "s_cmp_lt_u32 s52, s53\n" \
    "s_cbranch_scc1 .Lr_cuB0_%=\n" \
    "s_mov_b32 s53, 0\n" \
    "s_branch .Lr_ctrB0_%=\n" \
    ".Lr_cbB0_%=:\n" \
    "s_add_u32 s73, s32, s25\n" \
    "s_mov_b32 s50, s49\n" \
    "s_mov_b32 s52, 0\n" \
    ".Lr_ccB0_%=:\n" \
    "buffer_load_dword v18, v2, s[8:11], 0 offen sc1\n" \
    "s_waitcnt vmcnt(0)\n" \
    "v_cmp_ge_u32 vcc, s50, v18\n" \
    "s_nop 0\n" \
    "s_and_b64 vcc, vcc, s[56:57]\n" \
    "s_cbranch_scc0 .Lr_cdB0_%=\n" \
    "s_sleep 2\n" \
    "s_add_u32 s52, s52, 1\n" \
    "s_cmp_lt_u32 s52, s53\n" \
    "s_cbranch_scc1 .Lr_ccB0_%=\n" \
    "s_mov_b32 s53, 0\n" \
    "s_branch .Lr_cdB0_%=\n" \
    ".Lr_cdB0_%=:\n" \
    "s_and_b32 s33, s49, %[brn]-1\n" \
    "s_mul_i32 s39, s33, 2621440\n" \
    "s_add_u32 s39, s39, s19\n" \
    "s_lshr_b32 s33, s39, 8\n" \
    "s_xor_b32 s33, s33, s27\n" \
    "s_and_b32 s33, s33, 0x2000\n" \
    "s_or_b32 s39, s39, s33\n" \
    "s_add_u32 s40, s39, 65536\n" \
    "s_waitcnt vmcnt(0)\n" \
    "buffer_load_dwordx4 v[224:227], v0, s[8:11], s39 offen offset:0 sc1\n" \
    "buffer_load_dwordx4 v[228:231], v0, s[8:11], s39 offen offset:1024 sc1\n" \
    "buffer_load_dwordx4 v[232:235], v0, s[8:11], s39 offen offset:2048 sc1\n" \
    "buffer_load_dwordx4 v[236:239], v0, s[8:11], s39 offen offset:3072 sc1\n" \
    "buffer_load_dwordx4 v[240:243], v0, s[8:11], s40 offen offset:0 sc1\n" \
    "buffer_load_dwordx4 v[244:247], v0, s[8:11], s40 offen offset:1024 sc1\n" \
    "buffer_load_dwordx4 v[248:251], v0, s[8:11], s40 offen offset:2048 sc1\n" \
    "buffer_load_dwordx4 v[252:255], v0, s[8:11], s40 offen offset:3072 sc1\n" \
    "s_waitcnt vmcnt(0)\n" \
    "s_branch .Lr_cbrB0_%=\n" \
    ".Lr_ctvB1_%=:\n" \
    "s_mov_b32 s52, 0\n" \
    "s_add_u32 s73, s32, s25\n" \
    ".Lr_ctwB1_%=:\n" \
    "s_sleep 3\n" \
    "buffer_load_dwordx2 v[16:17], v3, s[8:11], s51 offen sc1\n" \
    "s_waitcnt vmcnt(0)\n" \
    "v_cmp_ne_u32 vcc, s49, v17\n" \
    "s_nop 0\n" \
    "s_and_b64 vcc, vcc, s[56:57]\n" \
    "s_cbranch_scc0 .Lr_tvB1_%=\n" \
    "s_add_u32 s52, s52, 1\n" \
    "s_cmp_lt_u32 s52, s53\n" \
    "s_cbranch_scc1 .Lr_ctwB1_%=\n" \
    "s_mov_b32 s53, 0\n" \
    "v_mov_b32 v17, s49\n" \
    "v_mov_b32 v16, s55\n" \
    "s_branch .Lr_tvB1_%=\n" \
    ".Lr_cpB1_%=:\n" \
    "s_mov_b32 s52, 0\n" \
    "s_nop 7\n" \
    ".Lr_cqB1_%=:\n" \
    "buffer_load_dwordx4 v[36:39], v0, s[8:11], s36 offen sc1\n" \
    "buffer_load_dwordx4 v[40:43], v0, s[8:11], s37 offen sc1\n" \
    "buffer_load_dwordx4 v[32:35], v4, s[8:11], s35 offen sc1\n" \
    "buffer_load_dwordx4 v[24:27], v0, s[8:11], s35 offen sc1\n" \
    "buffer_load_dwordx4 v[28:31], v0, s[8:11], s35 offen offset:1024 sc1\n" \
    "s_waitcnt vmcnt(4)\n" \
    "v_max3_u32 v19, v36, v37, v38\n" \
    "s_waitcnt vmcnt(3)\n" \
    "v_max3_u32 v19, v19, v39, v40\n" \
    "v_max3_u32 v19, v19, v41, v42\n" \
    "v_max_u32 v19, v19, v43\n" \
    "s_waitcnt vmcnt(2)\n" \
    "v_mfma_f32_16x16x32_bf16 v[80:83], v[32:35], v[36:39], 0\n" \
    "v_max3_u32 v20, v32, v33, v34\n" \
    "v_mfma_f32_16x16x32_bf16 v[84:87], v[32:35], v[40:43], 0\n" \
    "v_max_u32 v20, v20, v35\n" \
    "s_waitcnt vmcnt(1)\n" \
    "v_smfmac_f32_16x16x64_bf16 v[80:83], v[24:27], v[224:231], v10\n" \
    "v_smfmac_f32_16x16x64_bf16 v[84:87], v[24:27], v[240:247], v10\n" \
    "v_max3_u32 v19, v19, v24, v25\n" \
    "v_max3_u32 v19, v19, v26, v27\n" \
    "s_waitcnt vmcnt(0)\n" \
    "v_smfmac_f32_16x16x64_bf16 v[80:83], v[28:31], v[232:239], v10\n" \
    "v_smfmac_f32_16x16x64_bf16 v[84:87], v[28:31], v[248:255], v10\n" \
    "v_max3_u32 v20, v20, v28, v29\n" \
    "v_max3_u32 v20, v20, v30, v31\n" \
    "v_max_u32 v19, v19, v20\n" \
    "v_cmp_eq_u32 vcc, -1, v19\n" \
    "s_nop 7\n" \
    "s_cbranch_vccz .Lr_cprB1_%=\n" \
    "s_add_u32 s52, s52, 1\n" \
    "s_cmp_lt_u32 s52, s53\n" \
    "s_cbranch_scc1 .Lr_cqB1_%=\n" \
    "s_mov_b32 s53, 0\n" \
    "s_branch .Lr_cprB1_%=\n" \
    ".Lr_ctB1_%=:\n" \
    "s_mov_b32 s52, 0\n" \
    "s_add_u32 s73, s32, s25\n" \
    ".Lr_cuB1_%=:\n" \
    "s_sleep 4\n" \
    "buffer_load_dwordx4 v[44:47], v0, s[8:11], s41 offen offset:0 sc1\n" \
    "buffer_load_dwordx4 v[48:51], v0, s[8:11], s41 offen offset:1024 sc1\n" \
    "buffer_load_dwordx4 v[52:55], v0, s[8:11], s41 offen offset:2048 sc1\n" \
    "buffer_load_dwordx4 v[56:59], v0, s[8:11], s41 offen offset:3072 sc1\n" \
    "buffer_load_dwordx4 v[60:63], v0, s[8:11], s42 offen offset:0 sc1\n" \
    "buffer_load_dwordx4 v[64:67], v0, s[8:11], s42 offen offset:1024 sc1\n" \
    "buffer_load_dwordx4 v[68:71], v0, s[8:11], s42 offen offset:2048 sc1\n" \
    "buffer_load_dwordx4 v[72:75], v0, s[8:11], s42 offen offset:3072 sc1\n" \
    "s_waitcnt vmcnt(7)\n" \
    "v_max3_u32 v19, v44, v45, v46\n" \
    "s_waitcnt vmcnt(6)\n" \
    "v_max3_u32 v19, v19, v47, v48\n" \
    "v_max3_u32 v19, v19, v49, v50\n" \
    "s_waitcnt vmcnt(5)\n" \
    "v_max3_u32 v19, v19, v51, v52\n" \
    "v_max3_u32 v19, v19, v53, v54\n" \
    "s_waitcnt vmcnt(4)\n" \
    "v_max3_u32 v19, v19, v55, v56\n" \
    "v_max3_u32 v19, v19, v57, v58\n" \
    "s_waitcnt vmcnt(3)\n" \
    "v_max3_u32 v19, v19, v59, v60\n" \
    "v_max3_u32 v19, v19, v61, v62\n" \
    "s_waitcnt vmcnt(2)\n" \
    "v_max3_u32 v19, v19, v63, v64\n" \
    "v_max3_u32 v19, v19, v65, v66\n" \
    "s_waitcnt vmcnt(1)\n" \
    "v_max3_u32 v19, v19, v67, v68\n" \
    "v_max3_u32 v19, v19, v69, v70\n" \
    "s_waitcnt vmcnt(0)\n" \
    "v_max3_u32 v19, v19, v71, v72\n" \
    "v_max3_u32 v19, v19, v73, v74\n" \
    "v_max_u32 v19, v19, v75\n" \
    "v_cmp_eq_u32 vcc, -1, v19\n" \
    "s_nop 0\n" \
    "s_cbranch_vccz .Lr_ctrB1_%=\n" \
    "s_add_u32 s52, s52, 1\n" \
    "s_cmp_lt_u32 s52, s53\n" \
    "s_cbranch_scc1 .Lr_cuB1_%=\n" \
    "s_mov_b32 s53, 0\n" \
    "s_branch .Lr_ctrB1_%=\n" \
    ".Lr_cbB1_%=:\n" \
    "s_add_u32 s73, s32, s25\n" \
    "s_mov_b32 s50, s49\n" \
    "s_mov_b32 s52, 0\n" \
    ".Lr_ccB1_%=:\n" \
    "buffer_load_dword v18, v2, s[8:11], 0 offen sc1\n" \
    "s_waitcnt vmcnt(0)\n" \
    "v_cmp_ge_u32 vcc, s50, v18\n" \
    "s_nop 0\n" \
    "s_and_b64 vcc, vcc, s[56:57]\n" \
    "s_cbranch_scc0 .Lr_cdB1_%=\n" \
    "s_sleep 2\n" \
    "s_add_u32 s52, s52, 1\n" \
    "s_cmp_lt_u32 s52, s53\n" \
    "s_cbranch_scc1 .Lr_ccB1_%=\n" \
    "s_mov_b32 s53, 0\n" \
    "s_branch .Lr_cdB1_%=\n" \
    ".Lr_cdB1_%=:\n" \
    "s_and_b32 s33, s49, %[brn]-1\n" \
    "s_mul_i32 s39, s33, 2621440\n" \
    "s_add_u32 s39, s39, s19\n" \
    "s_lshr_b32 s33, s39, 8\n" \
    "s_xor_b32 s33, s33, s27\n" \
    "s_and_b32 s33, s33, 0x2000\n" \
    "s_or_b32 s39, s39, s33\n" \
    "s_add_u32 s40, s39, 65536\n" \
    "s_waitcnt vmcnt(0)\n" \
    "buffer_load_dwordx4 v[192:195], v0, s[8:11], s39 offen offset:0 sc1\n" \
    "buffer_load_dwordx4 v[196:199], v0, s[8:11], s39 offen offset:1024 sc1\n" \
    "buffer_load_dwordx4 v[200:203], v0, s[8:11], s39 offen offset:2048 sc1\n" \
    "buffer_load_dwordx4 v[204:207], v0, s[8:11], s39 offen offset:3072 sc1\n" \
    "buffer_load_dwordx4 v[208:211], v0, s[8:11], s40 offen offset:0 sc1\n" \
    "buffer_load_dwordx4 v[212:215], v0, s[8:11], s40 offen offset:1024 sc1\n" \
    "buffer_load_dwordx4 v[216:219], v0, s[8:11], s40 offen offset:2048 sc1\n" \
    "buffer_load_dwordx4 v[220:223], v0, s[8:11], s40 offen offset:3072 sc1\n" \
    "s_waitcnt vmcnt(0)\n" \
    "s_branch .Lr_cbrB1_%=\n" \
    ".Lr_ctvC0_%=:\n" \
    "s_mov_b32 s52, 0\n" \
    "s_add_u32 s73, s32, s25\n" \
    ".Lr_ctwC0_%=:\n" \
    "s_sleep 3\n" \
    "buffer_load_dwordx2 v[16:17], v3, s[8:11], s51 offen sc1\n" \
    "s_waitcnt vmcnt(0)\n" \
    "v_cmp_ne_u32 vcc, s49, v17\n" \
    "s_nop 0\n" \
    "s_and_b64 vcc, vcc, s[56:57]\n" \
    "s_cbranch_scc0 .Lr_tvC0_%=\n" \
    "s_add_u32 s52, s52, 1\n" \
    "s_cmp_lt_u32 s52, s53\n" \
    "s_cbranch_scc1 .Lr_ctwC0_%=\n" \
    "s_mov_b32 s53, 0\n" \
    "v_mov_b32 v17, s49\n" \
    "v_mov_b32 v16, s55\n" \
    "s_branch .Lr_tvC0_%=\n" \
    ".Lr_cpC0_%=:\n" \
    "s_mov_b32 s52, 0\n" \
    "s_nop 7\n" \
    ".Lr_cqC0_%=:\n" \
    "buffer_load_dwordx4 v[36:39], v0, s[8:11], s36 offen sc1\n" \
    "buffer_load_dwordx4 v[40:43], v0, s[8:11], s37 offen sc1\n" \
    "buffer_load_dwordx4 v[32:35], v4, s[8:11], s35 offen sc1\n" \
    "buffer_load_dwordx4 v[24:27], v0, s[8:11], s35 offen sc1\n" \
    "buffer_load_dwordx4 v[28:31], v0, s[8:11], s35 offen offset:1024 sc1\n" \
    "s_waitcnt vmcnt(4)\n" \
    "v_max3_u32 v19, v36, v37, v38\n" \
    "s_waitcnt vmcnt(3)\n" \
    "v_max3_u32 v19, v19, v39, v40\n" \
    "v_max3_u32 v19, v19, v41, v42\n" \
    "v_max_u32 v19, v19, v43\n" \
    "s_waitcnt vmcnt(2)\n" \
    "v_mfma_f32_16x16x32_bf16 v[80:83], v[32:35], v[36:39], 0\n" \
    "v_max3_u32 v20, v32, v33, v34\n" \
    "v_mfma_f32_16x16x32_bf16 v[84:87], v[32:35], v[40:43], 0\n" \
    "v_max_u32 v20, v20, v35\n" \
    "s_waitcnt vmcnt(1)\n" \
    "v_smfmac_f32_16x16x64_bf16 v[80:83], v[24:27], v[192:199], v10\n" \
    "v_smfmac_f32_16x16x64_bf16 v[84:87], v[24:27], v[208:215], v10\n" \
    "v_max3_u32 v19, v19, v24, v25\n" \
    "v_max3_u32 v19, v19, v26, v27\n" \
    "s_waitcnt vmcnt(0)\n" \
    "v_smfmac_f32_16x16x64_bf16 v[80:83], v[28:31], v[200:207], v10\n" \
    "v_smfmac_f32_16x16x64_bf16 v[84:87], v[28:31], v[216:223], v10\n" \
    "v_max3_u32 v20, v20, v28, v29\n" \
    "v_max3_u32 v20, v20, v30, v31\n" \
    "v_max_u32 v19, v19, v20\n" \
    "v_cmp_eq_u32 vcc, -1, v19\n" \
    "s_nop 7\n" \
    "s_cbranch_vccz .Lr_cprC0_%=\n" \
    "s_add_u32 s52, s52, 1\n" \
    "s_cmp_lt_u32 s52, s53\n" \
    "s_cbranch_scc1 .Lr_cqC0_%=\n" \
    "s_mov_b32 s53, 0\n" \
    "s_branch .Lr_cprC0_%=\n" \
    ".Lr_ctC0_%=:\n" \
    "s_mov_b32 s52, 0\n" \
    "s_add_u32 s73, s32, s25\n" \
    ".Lr_cuC0_%=:\n" \
    "s_sleep 4\n" \
    "buffer_load_dwordx4 v[44:47], v0, s[8:11], s41 offen offset:0 sc1\n" \
    "buffer_load_dwordx4 v[48:51], v0, s[8:11], s41 offen offset:1024 sc1\n" \
    "buffer_load_dwordx4 v[52:55], v0, s[8:11], s41 offen offset:2048 sc1\n" \
    "buffer_load_dwordx4 v[56:59], v0, s[8:11], s41 offen offset:3072 sc1\n" \
    "buffer_load_dwordx4 v[60:63], v0, s[8:11], s42 offen offset:0 sc1\n" \
    "buffer_load_dwordx4 v[64:67], v0, s[8:11], s42 offen offset:1024 sc1\n" \
    "buffer_load_dwordx4 v[68:71], v0, s[8:11], s42 offen offset:2048 sc1\n" \
    "buffer_load_dwordx4 v[72:75], v0, s[8:11], s42 offen offset:3072 sc1\n" \
    "s_waitcnt vmcnt(7)\n" \
    "v_max3_u32 v19, v44, v45, v46\n" \
    "s_waitcnt vmcnt(6)\n" \
    "v_max3_u32 v19, v19, v47, v48\n" \
    "v_max3_u32 v19, v19, v49, v50\n" \
    "s_waitcnt vmcnt(5)\n" \
    "v_max3_u32 v19, v19, v51, v52\n" \
    "v_max3_u32 v19, v19, v53, v54\n" \
    "s_waitcnt vmcnt(4)\n" \
    "v_max3_u32 v19, v19, v55, v56\n" \
    "v_max3_u32 v19, v19, v57, v58\n" \
    "s_waitcnt vmcnt(3)\n" \
    "v_max3_u32 v19, v19, v59, v60\n" \
    "v_max3_u32 v19, v19, v61, v62\n" \
    "s_waitcnt vmcnt(2)\n" \
    "v_max3_u32 v19, v19, v63, v64\n" \
    "v_max3_u32 v19, v19, v65, v66\n" \
    "s_waitcnt vmcnt(1)\n" \
    "v_max3_u32 v19, v19, v67, v68\n" \
    "v_max3_u32 v19, v19, v69, v70\n" \
    "s_waitcnt vmcnt(0)\n" \
    "v_max3_u32 v19, v19, v71, v72\n" \
    "v_max3_u32 v19, v19, v73, v74\n" \
    "v_max_u32 v19, v19, v75\n" \
    "v_cmp_eq_u32 vcc, -1, v19\n" \
    "s_nop 0\n" \
    "s_cbranch_vccz .Lr_ctrC0_%=\n" \
    "s_add_u32 s52, s52, 1\n" \
    "s_cmp_lt_u32 s52, s53\n" \
    "s_cbranch_scc1 .Lr_cuC0_%=\n" \
    "s_mov_b32 s53, 0\n" \
    "s_branch .Lr_ctrC0_%=\n" \
    ".Lr_cbC0_%=:\n" \
    "s_add_u32 s73, s32, s25\n" \
    "s_mov_b32 s50, s49\n" \
    "s_mov_b32 s52, 0\n" \
    ".Lr_ccC0_%=:\n" \
    "buffer_load_dword v18, v2, s[8:11], 0 offen sc1\n" \
    "s_waitcnt vmcnt(0)\n" \
    "v_cmp_ge_u32 vcc, s50, v18\n" \
    "s_nop 0\n" \
    "s_and_b64 vcc, vcc, s[56:57]\n" \
    "s_cbranch_scc0 .Lr_cdC0_%=\n" \
    "s_sleep 2\n" \
    "s_add_u32 s52, s52, 1\n" \
    "s_cmp_lt_u32 s52, s53\n" \
    "s_cbranch_scc1 .Lr_ccC0_%=\n" \
    "s_mov_b32 s53, 0\n" \
    "s_branch .Lr_cdC0_%=\n" \
    ".Lr_cdC0_%=:\n" \
    "s_and_b32 s33, s49, %[brn]-1\n" \
    "s_mul_i32 s39, s33, 2621440\n" \
    "s_add_u32 s39, s39, s19\n" \
    "s_lshr_b32 s33, s39, 8\n" \
    "s_xor_b32 s33, s33, s27\n" \
    "s_and_b32 s33, s33, 0x2000\n" \
    "s_or_b32 s39, s39, s33\n" \
    "s_add_u32 s40, s39, 65536\n" \
    "s_waitcnt vmcnt(0)\n" \
    "buffer_load_dwordx4 v[224:227], v0, s[8:11], s39 offen offset:0 sc1\n" \
    "buffer_load_dwordx4 v[228:231], v0, s[8:11], s39 offen offset:1024 sc1\n" \
    "buffer_load_dwordx4 v[232:235], v0, s[8:11], s39 offen offset:2048 sc1\n" \
    "buffer_load_dwordx4 v[236:239], v0, s[8:11], s39 offen offset:3072 sc1\n" \
    "buffer_load_dwordx4 v[240:243], v0, s[8:11], s40 offen offset:0 sc1\n" \
    "buffer_load_dwordx4 v[244:247], v0, s[8:11], s40 offen offset:1024 sc1\n" \
    "buffer_load_dwordx4 v[248:251], v0, s[8:11], s40 offen offset:2048 sc1\n" \
    "buffer_load_dwordx4 v[252:255], v0, s[8:11], s40 offen offset:3072 sc1\n" \
    "s_waitcnt vmcnt(0)\n" \
    "s_branch .Lr_cbrC0_%=\n" \
    ".Lr_ctvC1_%=:\n" \
    "s_mov_b32 s52, 0\n" \
    "s_add_u32 s73, s32, s25\n" \
    ".Lr_ctwC1_%=:\n" \
    "s_sleep 3\n" \
    "buffer_load_dwordx2 v[16:17], v3, s[8:11], s51 offen sc1\n" \
    "s_waitcnt vmcnt(0)\n" \
    "v_cmp_ne_u32 vcc, s49, v17\n" \
    "s_nop 0\n" \
    "s_and_b64 vcc, vcc, s[56:57]\n" \
    "s_cbranch_scc0 .Lr_tvC1_%=\n" \
    "s_add_u32 s52, s52, 1\n" \
    "s_cmp_lt_u32 s52, s53\n" \
    "s_cbranch_scc1 .Lr_ctwC1_%=\n" \
    "s_mov_b32 s53, 0\n" \
    "v_mov_b32 v17, s49\n" \
    "v_mov_b32 v16, s55\n" \
    "s_branch .Lr_tvC1_%=\n" \
    ".Lr_cpC1_%=:\n" \
    "s_mov_b32 s52, 0\n" \
    "s_nop 7\n" \
    ".Lr_cqC1_%=:\n" \
    "buffer_load_dwordx4 v[36:39], v0, s[8:11], s36 offen sc1\n" \
    "buffer_load_dwordx4 v[40:43], v0, s[8:11], s37 offen sc1\n" \
    "buffer_load_dwordx4 v[32:35], v4, s[8:11], s35 offen sc1\n" \
    "buffer_load_dwordx4 v[24:27], v0, s[8:11], s35 offen sc1\n" \
    "buffer_load_dwordx4 v[28:31], v0, s[8:11], s35 offen offset:1024 sc1\n" \
    "s_waitcnt vmcnt(4)\n" \
    "v_max3_u32 v19, v36, v37, v38\n" \
    "s_waitcnt vmcnt(3)\n" \
    "v_max3_u32 v19, v19, v39, v40\n" \
    "v_max3_u32 v19, v19, v41, v42\n" \
    "v_max_u32 v19, v19, v43\n" \
    "s_waitcnt vmcnt(2)\n" \
    "v_mfma_f32_16x16x32_bf16 v[80:83], v[32:35], v[36:39], 0\n" \
    "v_max3_u32 v20, v32, v33, v34\n" \
    "v_mfma_f32_16x16x32_bf16 v[84:87], v[32:35], v[40:43], 0\n" \
    "v_max_u32 v20, v20, v35\n" \
    "s_waitcnt vmcnt(1)\n" \
    "v_smfmac_f32_16x16x64_bf16 v[80:83], v[24:27], v[224:231], v10\n" \
    "v_smfmac_f32_16x16x64_bf16 v[84:87], v[24:27], v[240:247], v10\n" \
    "v_max3_u32 v19, v19, v24, v25\n" \
    "v_max3_u32 v19, v19, v26, v27\n" \
    "s_waitcnt vmcnt(0)\n" \
    "v_smfmac_f32_16x16x64_bf16 v[80:83], v[28:31], v[232:239], v10\n" \
    "v_smfmac_f32_16x16x64_bf16 v[84:87], v[28:31], v[248:255], v10\n" \
    "v_max3_u32 v20, v20, v28, v29\n" \
    "v_max3_u32 v20, v20, v30, v31\n" \
    "v_max_u32 v19, v19, v20\n" \
    "v_cmp_eq_u32 vcc, -1, v19\n" \
    "s_nop 7\n" \
    "s_cbranch_vccz .Lr_cprC1_%=\n" \
    "s_add_u32 s52, s52, 1\n" \
    "s_cmp_lt_u32 s52, s53\n" \
    "s_cbranch_scc1 .Lr_cqC1_%=\n" \
    "s_mov_b32 s53, 0\n" \
    "s_branch .Lr_cprC1_%=\n" \
    ".Lr_ctC1_%=:\n" \
    "s_mov_b32 s52, 0\n" \
    "s_add_u32 s73, s32, s25\n" \
    ".Lr_cuC1_%=:\n" \
    "s_sleep 4\n" \
    "buffer_load_dwordx4 v[44:47], v0, s[8:11], s41 offen offset:0 sc1\n" \
    "buffer_load_dwordx4 v[48:51], v0, s[8:11], s41 offen offset:1024 sc1\n" \
    "buffer_load_dwordx4 v[52:55], v0, s[8:11], s41 offen offset:2048 sc1\n" \
    "buffer_load_dwordx4 v[56:59], v0, s[8:11], s41 offen offset:3072 sc1\n" \
    "buffer_load_dwordx4 v[60:63], v0, s[8:11], s42 offen offset:0 sc1\n" \
    "buffer_load_dwordx4 v[64:67], v0, s[8:11], s42 offen offset:1024 sc1\n" \
    "buffer_load_dwordx4 v[68:71], v0, s[8:11], s42 offen offset:2048 sc1\n" \
    "buffer_load_dwordx4 v[72:75], v0, s[8:11], s42 offen offset:3072 sc1\n" \
    "s_waitcnt vmcnt(7)\n" \
    "v_max3_u32 v19, v44, v45, v46\n" \
    "s_waitcnt vmcnt(6)\n" \
    "v_max3_u32 v19, v19, v47, v48\n" \
    "v_max3_u32 v19, v19, v49, v50\n" \
    "s_waitcnt vmcnt(5)\n" \
    "v_max3_u32 v19, v19, v51, v52\n" \
    "v_max3_u32 v19, v19, v53, v54\n" \
    "s_waitcnt vmcnt(4)\n" \
    "v_max3_u32 v19, v19, v55, v56\n" \
    "v_max3_u32 v19, v19, v57, v58\n" \
    "s_waitcnt vmcnt(3)\n" \
    "v_max3_u32 v19, v19, v59, v60\n" \
    "v_max3_u32 v19, v19, v61, v62\n" \
    "s_waitcnt vmcnt(2)\n" \
    "v_max3_u32 v19, v19, v63, v64\n" \
    "v_max3_u32 v19, v19, v65, v66\n" \
    "s_waitcnt vmcnt(1)\n" \
    "v_max3_u32 v19, v19, v67, v68\n" \
    "v_max3_u32 v19, v19, v69, v70\n" \
    "s_waitcnt vmcnt(0)\n" \
    "v_max3_u32 v19, v19, v71, v72\n" \
    "v_max3_u32 v19, v19, v73, v74\n" \
    "v_max_u32 v19, v19, v75\n" \
    "v_cmp_eq_u32 vcc, -1, v19\n" \
    "s_nop 0\n" \
    "s_cbranch_vccz .Lr_ctrC1_%=\n" \
    "s_add_u32 s52, s52, 1\n" \
    "s_cmp_lt_u32 s52, s53\n" \
    "s_cbranch_scc1 .Lr_cuC1_%=\n" \
    "s_mov_b32 s53, 0\n" \
    "s_branch .Lr_ctrC1_%=\n" \
    ".Lr_cbC1_%=:\n" \
    "s_add_u32 s73, s32, s25\n" \
    "s_mov_b32 s50, s49\n" \
    "s_mov_b32 s52, 0\n" \
    ".Lr_ccC1_%=:\n" \
    "buffer_load_dword v18, v2, s[8:11], 0 offen sc1\n" \
    "s_waitcnt vmcnt(0)\n" \
    "v_cmp_ge_u32 vcc, s50, v18\n" \
    "s_nop 0\n" \
    "s_and_b64 vcc, vcc, s[56:57]\n" \
    "s_cbranch_scc0 .Lr_cdC1_%=\n" \
    "s_sleep 2\n" \
    "s_add_u32 s52, s52, 1\n" \
    "s_cmp_lt_u32 s52, s53\n" \
    "s_cbranch_scc1 .Lr_ccC1_%=\n" \
    "s_mov_b32 s53, 0\n" \
    "s_branch .Lr_cdC1_%=\n" \
    ".Lr_cdC1_%=:\n" \
    "s_and_b32 s33, s49, %[brn]-1\n" \
    "s_mul_i32 s39, s33, 2621440\n" \
    "s_add_u32 s39, s39, s19\n" \
    "s_lshr_b32 s33, s39, 8\n" \
    "s_xor_b32 s33, s33, s27\n" \
    "s_and_b32 s33, s33, 0x2000\n" \
    "s_or_b32 s39, s39, s33\n" \
    "s_add_u32 s40, s39, 65536\n" \
    "s_waitcnt vmcnt(0)\n" \
    "buffer_load_dwordx4 v[192:195], v0, s[8:11], s39 offen offset:0 sc1\n" \
    "buffer_load_dwordx4 v[196:199], v0, s[8:11], s39 offen offset:1024 sc1\n" \
    "buffer_load_dwordx4 v[200:203], v0, s[8:11], s39 offen offset:2048 sc1\n" \
    "buffer_load_dwordx4 v[204:207], v0, s[8:11], s39 offen offset:3072 sc1\n" \
    "buffer_load_dwordx4 v[208:211], v0, s[8:11], s40 offen offset:0 sc1\n" \
    "buffer_load_dwordx4 v[212:215], v0, s[8:11], s40 offen offset:1024 sc1\n" \
    "buffer_load_dwordx4 v[216:219], v0, s[8:11], s40 offen offset:2048 sc1\n" \
    "buffer_load_dwordx4 v[220:223], v0, s[8:11], s40 offen offset:3072 sc1\n" \
    "s_waitcnt vmcnt(0)\n" \
    "s_branch .Lr_cbrC1_%=\n" \
    ".Lr_ctvE0_%=:\n" \
    "s_mov_b32 s52, 0\n" \
    "s_add_u32 s73, s32, s25\n" \
    ".Lr_ctwE0_%=:\n" \
    "s_sleep 3\n" \
    "buffer_load_dwordx2 v[16:17], v3, s[8:11], s51 offen sc1\n" \
    "s_waitcnt vmcnt(0)\n" \
    "v_cmp_ne_u32 vcc, s49, v17\n" \
    "s_nop 0\n" \
    "s_and_b64 vcc, vcc, s[56:57]\n" \
    "s_cbranch_scc0 .Lr_tvE0_%=\n" \
    "s_add_u32 s52, s52, 1\n" \
    "s_cmp_lt_u32 s52, s53\n" \
    "s_cbranch_scc1 .Lr_ctwE0_%=\n" \
    "s_mov_b32 s53, 0\n" \
    "v_mov_b32 v17, s49\n" \
    "v_mov_b32 v16, s55\n" \
    "s_branch .Lr_tvE0_%=\n" \
    ".Lr_cpE0_%=:\n" \
    "s_mov_b32 s52, 0\n" \
    "s_nop 7\n" \
    ".Lr_cqE0_%=:\n" \
    "buffer_load_dwordx4 v[36:39], v0, s[8:11], s36 offen sc1\n" \
    "buffer_load_dwordx4 v[40:43], v0, s[8:11], s37 offen sc1\n" \
    "buffer_load_dwordx4 v[32:35], v4, s[8:11], s35 offen sc1\n" \
    "buffer_load_dwordx4 v[24:27], v0, s[8:11], s35 offen sc1\n" \
    "buffer_load_dwordx4 v[28:31], v0, s[8:11], s35 offen offset:1024 sc1\n" \
    "s_waitcnt vmcnt(4)\n" \
    "v_max3_u32 v19, v36, v37, v38\n" \
    "s_waitcnt vmcnt(3)\n" \
    "v_max3_u32 v19, v19, v39, v40\n" \
    "v_max3_u32 v19, v19, v41, v42\n" \
    "v_max_u32 v19, v19, v43\n" \
    "s_waitcnt vmcnt(2)\n" \
    "v_mfma_f32_16x16x32_bf16 v[80:83], v[32:35], v[36:39], 0\n" \
    "v_max3_u32 v20, v32, v33, v34\n" \
    "v_mfma_f32_16x16x32_bf16 v[84:87], v[32:35], v[40:43], 0\n" \
    "v_max_u32 v20, v20, v35\n" \
    "s_waitcnt vmcnt(1)\n" \
    "v_smfmac_f32_16x16x64_bf16 v[80:83], v[24:27], v[192:199], v10\n" \
    "v_smfmac_f32_16x16x64_bf16 v[84:87], v[24:27], v[208:215], v10\n" \
    "v_max3_u32 v19, v19, v24, v25\n" \
    "v_max3_u32 v19, v19, v26, v27\n" \
    "s_waitcnt vmcnt(0)\n" \
    "v_smfmac_f32_16x16x64_bf16 v[80:83], v[28:31], v[200:207], v10\n" \
    "v_smfmac_f32_16x16x64_bf16 v[84:87], v[28:31], v[216:223], v10\n" \
    "v_max3_u32 v20, v20, v28, v29\n" \
    "v_max3_u32 v20, v20, v30, v31\n" \
    "v_max_u32 v19, v19, v20\n" \
    "v_cmp_eq_u32 vcc, -1, v19\n" \
    "s_nop 7\n" \
    "s_cbranch_vccz .Lr_cprE0_%=\n" \
    "s_add_u32 s52, s52, 1\n" \
    "s_cmp_lt_u32 s52, s53\n" \
    "s_cbranch_scc1 .Lr_cqE0_%=\n" \
    "s_mov_b32 s53, 0\n" \
    "s_branch .Lr_cprE0_%=\n" \
    ".Lr_ctE0_%=:\n" \
    "s_mov_b32 s52, 0\n" \
    "s_add_u32 s73, s32, s25\n" \
    ".Lr_cuE0_%=:\n" \
    "s_sleep 4\n" \
    "buffer_load_dwordx4 v[44:47], v0, s[8:11], s41 offen offset:0 sc1\n" \
    "buffer_load_dwordx4 v[48:51], v0, s[8:11], s41 offen offset:1024 sc1\n" \
    "buffer_load_dwordx4 v[52:55], v0, s[8:11], s41 offen offset:2048 sc1\n" \
    "buffer_load_dwordx4 v[56:59], v0, s[8:11], s41 offen offset:3072 sc1\n" \
    "buffer_load_dwordx4 v[60:63], v0, s[8:11], s42 offen offset:0 sc1\n" \
    "buffer_load_dwordx4 v[64:67], v0, s[8:11], s42 offen offset:1024 sc1\n" \
    "buffer_load_dwordx4 v[68:71], v0, s[8:11], s42 offen offset:2048 sc1\n" \
    "buffer_load_dwordx4 v[72:75], v0, s[8:11], s42 offen offset:3072 sc1\n" \
    "s_waitcnt vmcnt(7)\n" \
    "v_max3_u32 v19, v44, v45, v46\n" \
    "s_waitcnt vmcnt(6)\n" \
    "v_max3_u32 v19, v19, v47, v48\n" \
    "v_max3_u32 v19, v19, v49, v50\n" \
    "s_waitcnt vmcnt(5)\n" \
    "v_max3_u32 v19, v19, v51, v52\n" \
    "v_max3_u32 v19, v19, v53, v54\n" \
    "s_waitcnt vmcnt(4)\n" \
    "v_max3_u32 v19, v19, v55, v56\n" \
    "v_max3_u32 v19, v19, v57, v58\n" \
    "s_waitcnt vmcnt(3)\n" \
    "v_max3_u32 v19, v19, v59, v60\n" \
    "v_max3_u32 v19, v19, v61, v62\n" \
    "s_waitcnt vmcnt(2)\n" \
    "v_max3_u32 v19, v19, v63, v64\n" \
    "v_max3_u32 v19, v19, v65, v66\n" \
    "s_waitcnt vmcnt(1)\n" \
    "v_max3_u32 v19, v19, v67, v68\n" \
    "v_max3_u32 v19, v19, v69, v70\n" \
    "s_waitcnt vmcnt(0)\n" \
    "v_max3_u32 v19, v19, v71, v72\n" \
    "v_max3_u32 v19, v19, v73, v74\n" \
    "v_max_u32 v19, v19, v75\n" \
    "v_cmp_eq_u32 vcc, -1, v19\n" \
    "s_nop 0\n" \
    "s_cbranch_vccz .Lr_ctrE0_%=\n" \
    "s_add_u32 s52, s52, 1\n" \
    "s_cmp_lt_u32 s52, s53\n" \
    "s_cbranch_scc1 .Lr_cuE0_%=\n" \
    "s_mov_b32 s53, 0\n" \
    "s_branch .Lr_ctrE0_%=\n" \
    ".Lr_cbE0_%=:\n" \
    "s_add_u32 s73, s32, s25\n" \
    "s_mov_b32 s50, s49\n" \
    "s_mov_b32 s52, 0\n" \
    ".Lr_ccE0_%=:\n" \
    "buffer_load_dword v18, v2, s[8:11], 0 offen sc1\n" \
    "s_waitcnt vmcnt(0)\n" \
    "v_cmp_ge_u32 vcc, s50, v18\n" \
    "s_nop 0\n" \
    "s_and_b64 vcc, vcc, s[56:57]\n" \
    "s_cbranch_scc0 .Lr_cdE0_%=\n" \
    "s_sleep 2\n" \
    "s_add_u32 s52, s52, 1\n" \
    "s_cmp_lt_u32 s52, s53\n" \
    "s_cbranch_scc1 .Lr_ccE0_%=\n" \
    "s_mov_b32 s53, 0\n" \
    "s_branch .Lr_cdE0_%=\n" \
    ".Lr_cdE0_%=:\n" \
    "s_and_b32 s33, s49, %[brn]-1\n" \
    "s_mul_i32 s39, s33, 2621440\n" \
    "s_add_u32 s39, s39, s19\n" \
    "s_lshr_b32 s33, s39, 8\n" \
    "s_xor_b32 s33, s33, s27\n" \
    "s_and_b32 s33, s33, 0x2000\n" \
    "s_or_b32 s39, s39, s33\n" \
    "s_add_u32 s40, s39, 65536\n" \
    "s_waitcnt vmcnt(0)\n" \
    "buffer_load_dwordx4 v[224:227], v0, s[8:11], s39 offen offset:0 sc1\n" \
    "buffer_load_dwordx4 v[228:231], v0, s[8:11], s39 offen offset:1024 sc1\n" \
    "buffer_load_dwordx4 v[232:235], v0, s[8:11], s39 offen offset:2048 sc1\n" \
    "buffer_load_dwordx4 v[236:239], v0, s[8:11], s39 offen offset:3072 sc1\n" \
    "buffer_load_dwordx4 v[240:243], v0, s[8:11], s40 offen offset:0 sc1\n" \
    "buffer_load_dwordx4 v[244:247], v0, s[8:11], s40 offen offset:1024 sc1\n" \
    "buffer_load_dwordx4 v[248:251], v0, s[8:11], s40 offen offset:2048 sc1\n" \
    "buffer_load_dwordx4 v[252:255], v0, s[8:11], s40 offen offset:3072 sc1\n" \
    "s_waitcnt vmcnt(0)\n" \
    "s_branch .Lr_cbrE0_%=\n" \
    ".Lr_ctvE1_%=:\n" \
    "s_mov_b32 s52, 0\n" \
    "s_add_u32 s73, s32, s25\n" \
    ".Lr_ctwE1_%=:\n" \
    "s_sleep 3\n" \
    "buffer_load_dwordx2 v[16:17], v3, s[8:11], s51 offen sc1\n" \
    "s_waitcnt vmcnt(0)\n" \
    "v_cmp_ne_u32 vcc, s49, v17\n" \
    "s_nop 0\n" \
    "s_and_b64 vcc, vcc, s[56:57]\n" \
    "s_cbranch_scc0 .Lr_tvE1_%=\n" \
    "s_add_u32 s52, s52, 1\n" \
    "s_cmp_lt_u32 s52, s53\n" \
    "s_cbranch_scc1 .Lr_ctwE1_%=\n" \
    "s_mov_b32 s53, 0\n" \
    "v_mov_b32 v17, s49\n" \
    "v_mov_b32 v16, s55\n" \
    "s_branch .Lr_tvE1_%=\n" \
    ".Lr_cpE1_%=:\n" \
    "s_mov_b32 s52, 0\n" \
    "s_nop 7\n" \
    ".Lr_cqE1_%=:\n" \
    "buffer_load_dwordx4 v[36:39], v0, s[8:11], s36 offen sc1\n" \
    "buffer_load_dwordx4 v[40:43], v0, s[8:11], s37 offen sc1\n" \
    "buffer_load_dwordx4 v[32:35], v4, s[8:11], s35 offen sc1\n" \
    "buffer_load_dwordx4 v[24:27], v0, s[8:11], s35 offen sc1\n" \
    "buffer_load_dwordx4 v[28:31], v0, s[8:11], s35 offen offset:1024 sc1\n" \
    "s_waitcnt vmcnt(4)\n" \
    "v_max3_u32 v19, v36, v37, v38\n" \
    "s_waitcnt vmcnt(3)\n" \
    "v_max3_u32 v19, v19, v39, v40\n" \
    "v_max3_u32 v19, v19, v41, v42\n" \
    "v_max_u32 v19, v19, v43\n" \
    "s_waitcnt vmcnt(2)\n" \
    "v_mfma_f32_16x16x32_bf16 v[80:83], v[32:35], v[36:39], 0\n" \
    "v_max3_u32 v20, v32, v33, v34\n" \
    "v_mfma_f32_16x16x32_bf16 v[84:87], v[32:35], v[40:43], 0\n" \
    "v_max_u32 v20, v20, v35\n" \
    "s_waitcnt vmcnt(1)\n" \
    "v_smfmac_f32_16x16x64_bf16 v[80:83], v[24:27], v[224:231], v10\n" \
    "v_smfmac_f32_16x16x64_bf16 v[84:87], v[24:27], v[240:247], v10\n" \
    "v_max3_u32 v19, v19, v24, v25\n" \
    "v_max3_u32 v19, v19, v26, v27\n" \
    "s_waitcnt vmcnt(0)\n" \
    "v_smfmac_f32_16x16x64_bf16 v[80:83], v[28:31], v[232:239], v10\n" \
    "v_smfmac_f32_16x16x64_bf16 v[84:87], v[28:31], v[248:255], v10\n" \
    "v_max3_u32 v20, v20, v28, v29\n" \
    "v_max3_u32 v20, v20, v30, v31\n" \
    "v_max_u32 v19, v19, v20\n" \
    "v_cmp_eq_u32 vcc, -1, v19\n" \
    "s_nop 7\n" \
    "s_cbranch_vccz .Lr_cprE1_%=\n" \
    "s_add_u32 s52, s52, 1\n" \
    "s_cmp_lt_u32 s52, s53\n" \
    "s_cbranch_scc1 .Lr_cqE1_%=\n" \
    "s_mov_b32 s53, 0\n" \
    "s_branch .Lr_cprE1_%=\n" \
    ".Lr_ctE1_%=:\n" \
    "s_mov_b32 s52, 0\n" \
    "s_add_u32 s73, s32, s25\n" \
    ".Lr_cuE1_%=:\n" \
    "s_sleep 4\n" \
    "buffer_load_dwordx4 v[44:47], v0, s[8:11], s41 offen offset:0 sc1\n" \
    "buffer_load_dwordx4 v[48:51], v0, s[8:11], s41 offen offset:1024 sc1\n" \
    "buffer_load_dwordx4 v[52:55], v0, s[8:11], s41 offen offset:2048 sc1\n" \
    "buffer_load_dwordx4 v[56:59], v0, s[8:11], s41 offen offset:3072 sc1\n" \
    "buffer_load_dwordx4 v[60:63], v0, s[8:11], s42 offen offset:0 sc1\n" \
    "buffer_load_dwordx4 v[64:67], v0, s[8:11], s42 offen offset:1024 sc1\n" \
    "buffer_load_dwordx4 v[68:71], v0, s[8:11], s42 offen offset:2048 sc1\n" \
    "buffer_load_dwordx4 v[72:75], v0, s[8:11], s42 offen offset:3072 sc1\n" \
    "s_waitcnt vmcnt(7)\n" \
    "v_max3_u32 v19, v44, v45, v46\n" \
    "s_waitcnt vmcnt(6)\n" \
    "v_max3_u32 v19, v19, v47, v48\n" \
    "v_max3_u32 v19, v19, v49, v50\n" \
    "s_waitcnt vmcnt(5)\n" \
    "v_max3_u32 v19, v19, v51, v52\n" \
    "v_max3_u32 v19, v19, v53, v54\n" \
    "s_waitcnt vmcnt(4)\n" \
    "v_max3_u32 v19, v19, v55, v56\n" \
    "v_max3_u32 v19, v19, v57, v58\n" \
    "s_waitcnt vmcnt(3)\n" \
    "v_max3_u32 v19, v19, v59, v60\n" \
    "v_max3_u32 v19, v19, v61, v62\n" \
    "s_waitcnt vmcnt(2)\n" \
    "v_max3_u32 v19, v19, v63, v64\n" \
    "v_max3_u32 v19, v19, v65, v66\n" \
    "s_waitcnt vmcnt(1)\n" \
    "v_max3_u32 v19, v19, v67, v68\n" \
    "v_max3_u32 v19, v19, v69, v70\n" \
    "s_waitcnt vmcnt(0)\n" \
    "v_max3_u32 v19, v19, v71, v72\n" \
    "v_max3_u32 v19, v19, v73, v74\n" \
    "v_max_u32 v19, v19, v75\n" \
    "v_cmp_eq_u32 vcc, -1, v19\n" \
    "s_nop 0\n" \
    "s_cbranch_vccz .Lr_ctrE1_%=\n" \
    "s_add_u32 s52, s52, 1\n" \
    "s_cmp_lt_u32 s52, s53\n" \
    "s_cbranch_scc1 .Lr_cuE1_%=\n" \
    "s_mov_b32 s53, 0\n" \
    "s_branch .Lr_ctrE1_%=\n" \
    ".Lr_cbE1_%=:\n" \
    "s_add_u32 s73, s32, s25\n" \
    "s_mov_b32 s50, s49\n" \
    "s_mov_b32 s52, 0\n" \
    ".Lr_ccE1_%=:\n" \
    "buffer_load_dword v18, v2, s[8:11], 0 offen sc1\n" \
    "s_waitcnt vmcnt(0)\n" \
    "v_cmp_ge_u32 vcc, s50, v18\n" \
    "s_nop 0\n" \
    "s_and_b64 vcc, vcc, s[56:57]\n" \
    "s_cbranch_scc0 .Lr_cdE1_%=\n" \
    "s_sleep 2\n" \
    "s_add_u32 s52, s52, 1\n" \
    "s_cmp_lt_u32 s52, s53\n" \
    "s_cbranch_scc1 .Lr_ccE1_%=\n" \
    "s_mov_b32 s53, 0\n" \
    "s_branch .Lr_cdE1_%=\n" \
    ".Lr_cdE1_%=:\n" \
    "s_and_b32 s33, s49, %[brn]-1\n" \
    "s_mul_i32 s39, s33, 2621440\n" \
    "s_add_u32 s39, s39, s19\n" \
    "s_lshr_b32 s33, s39, 8\n" \
    "s_xor_b32 s33, s33, s27\n" \
    "s_and_b32 s33, s33, 0x2000\n" \
    "s_or_b32 s39, s39, s33\n" \
    "s_add_u32 s40, s39, 65536\n" \
    "s_waitcnt vmcnt(0)\n" \
    "buffer_load_dwordx4 v[192:195], v0, s[8:11], s39 offen offset:0 sc1\n" \
    "buffer_load_dwordx4 v[196:199], v0, s[8:11], s39 offen offset:1024 sc1\n" \
    "buffer_load_dwordx4 v[200:203], v0, s[8:11], s39 offen offset:2048 sc1\n" \
    "buffer_load_dwordx4 v[204:207], v0, s[8:11], s39 offen offset:3072 sc1\n" \
    "buffer_load_dwordx4 v[208:211], v0, s[8:11], s40 offen offset:0 sc1\n" \
    "buffer_load_dwordx4 v[212:215], v0, s[8:11], s40 offen offset:1024 sc1\n" \
    "buffer_load_dwordx4 v[216:219], v0, s[8:11], s40 offen offset:2048 sc1\n" \
    "buffer_load_dwordx4 v[220:223], v0, s[8:11], s40 offen offset:3072 sc1\n" \
    "s_waitcnt vmcnt(0)\n" \
    "s_branch .Lr_cbrE1_%=\n" \
    ""
#define G_ASM \
    "s_mov_b32 s32, s24\n" \
    "s_mov_b32 s33, s27\n" \
    "s_mov_b32 s34, -1\n" \
    "s_mov_b32 s35, -1\n" \
    "s_mov_b32 s37, 0\n" \
    "s_mov_b32 s45, 0x80000000\n" \
    "s_mov_b32 s38, 0x80000000\n" \
    "s_mov_b32 s41, 0\n" \
    "s_mov_b32 s49, 256\n" \
    "s_mov_b32 s42, 0\n" \
    "s_mov_b32 s43, 0\n" \
    "s_mov_b32 s46, 0\n" \
    "s_mov_b32 s69, 0x80000000\n" \
    "v_mov_b32 v165, 81920\n" \
    "v_mov_b32 v168, 1\n" \
    "v_mov_b32 v169, 1\n" \
    "v_mov_b32 v170, 1\n" \
    "v_mov_b32 v171, 1\n" \
    "s_cmp_eq_u32 s16, 0\n" \
    "s_cbranch_scc1 .Lg_lS_%=\n" \
    "s_cmp_lt_u32 s16, 4\n" \
    "s_cbranch_scc1 .Lg_lO_%=\n" \
    "s_barrier\n" \
    "s_add_u32 s41, s41, 20480\n" \
    ".Lg_lY_%=:\n" \
    "s_barrier\n" \
    "ds_read_b32 v167, v165\n" \
    ".Lg_nt_%=:\n" \
    "v_add_u32 v164, s41, v160\n" \
    "s_setprio 0\n" \
    "s_waitcnt vmcnt(63)\n" \
    "buffer_store_dwordx4 v[168:171], v161, s[8:11], s38 offen sc1\n" \
    "s_mov_b32 s38, 0x80000000\n" \
    "s_waitcnt lgkmcnt(0)\n" \
    "v_readfirstlane_b32 s44, v167\n" \
    "s_nop 0\n" \
    "s_and_b32 s44, s44, 255\n" \
    "s_add_u32 s39, s33, 2*%[r0]-2\n" \
    "s_min_u32 s39, s39, s25\n" \
    "s_cmp_lt_u32 s32, s39\n" \
    "s_cselect_b32 s36, s32, -1\n" \
    "s_add_u32 s39, s33, 128\n" \
    "s_cmp_lt_u32 s33, s44\n" \
    "s_cselect_b32 s36, s39, s36\n" \
    "s_addc_u32 s33, s33, 0\n" \
    "s_cmp_lt_u32 s36, 128\n" \
    "s_addc_u32 s32, s32, 0\n" \
    "s_cmp_eq_u32 s32, s29\n" \
    "s_cselect_b32 s32, s30, s32\n" \
    "s_add_u32 s48, s41, 20480\n" \
    "s_cmp_ge_u32 s48, 81920\n" \
    "s_cselect_b32 s40, 81920, 0\n" \
    "s_sub_u32 s48, s48, s40\n" \
    "s_add_u32 m0, s48, s23\n" \
    "s_mul_i32 s40, s36, 20480\n" \
    "s_add_u32 s40, s40, s22\n" \
    "s_cmp_eq_u32 s36, -1\n" \
    "s_cselect_b32 s40, 0x80000000, s40\n" \
    "buffer_load_dwordx4 v160, s[8:11], s40 offen offset:0 sc1 lds\n" \
    "v_cvt_pk_bf16_f32 v172, v208, v209\n" \
    "v_cvt_pk_bf16_f32 v173, v210, v211\n" \
    "v_cvt_pk_bf16_f32 v174, v212, v213\n" \
    "v_cvt_pk_bf16_f32 v175, v214, v215\n" \
    "buffer_store_dwordx4 v[172:175], v160, s[8:11], s45 offen sc1\n" \
    "s_cmp_gt_u32 s37, 127\n" \
    "s_cselect_b32 s38, s21, s20\n" \
    "s_bitcmp1_b32 s37, 0\n" \
    "s_cselect_b32 s38, s38, 0x80000000\n" \
    "s_bfe_u32 s39, s37, 0x60001\n" \
    "s_add_u32 s39, s39, 1\n" \
    "v_mov_b32 v168, s39\n" \
    "s_add_u32 s41, s41, 20480\n" \
    "s_cmp_eq_u32 s41, 81920\n" \
    "s_cselect_b32 s41, 0, s41\n" \
    "s_add_u32 s49, s49, 256\n" \
    "v_xor_b32 v165, 4, v165\n" \
    "s_mov_b32 s37, s34\n" \
    "s_mov_b32 s34, s36\n" \
    "s_cmp_eq_u32 s37, -1\n" \
    "s_cbranch_scc1 .Lg_eY_%=\n" \
    "ds_read_b128 v[176:179], v164 offset:0\n" \
    "ds_read_b128 v[180:183], v164 offset:1024\n" \
    "ds_read_b128 v[184:187], v164 offset:2048\n" \
    "ds_read_b128 v[188:191], v164 offset:3072\n" \
    "ds_read_b128 v[192:195], v164 offset:4096\n" \
    "s_lshr_b32 s39, s37, 1\n" \
    "s_sub_u32 s45, s39, %[r0]\n" \
    "s_cselect_b32 s45, s39, s45\n" \
    "s_and_b32 s40, s39, 15\n" \
    "s_add_u32 s40, s40, %[r0]\n" \
    "s_cmp_gt_u32 s39, 63\n" \
    "s_cselect_b32 s45, s40, s45\n" \
    "s_mul_i32 s45, s45, 1572864\n" \
    "s_bitcmp1_b32 s37, 0\n" \
    "s_cselect_b32 s40, s18, s17\n" \
    "s_add_u32 s45, s45, s40\n" \
    "s_lshr_b32 s40, s45, 8\n" \
    "s_xor_b32 s40, s40, s19\n" \
    "s_and_b32 s40, s40, 0x2000\n" \
    "s_or_b32 s45, s45, s40\n" \
    "s_mov_b32 s46, 0\n" \
    "s_waitcnt lgkmcnt(4)\n" \
    "v_mfma_f32_16x16x32_bf16 v[208:211], v[0:3], v[176:179], 0\n" \
    "ds_read_b128 v[196:199], v164 offset:5120\n" \
    "v_mfma_f32_16x16x32_bf16 v[212:215], v[80:83], v[176:179], 0\n" \
    "s_waitcnt lgkmcnt(4)\n" \
    "v_mfma_f32_16x16x32_bf16 v[208:211], v[4:7], v[180:183], v[208:211]\n" \
    "ds_read_b128 v[200:203], v164 offset:6144\n" \
    "v_mfma_f32_16x16x32_bf16 v[212:215], v[84:87], v[180:183], v[212:215]\n" \
    "s_waitcnt lgkmcnt(4)\n" \
    "v_mfma_f32_16x16x32_bf16 v[208:211], v[8:11], v[184:187], v[208:211]\n" \
    "ds_read_b128 v[204:207], v164 offset:7168\n" \
    "v_mfma_f32_16x16x32_bf16 v[212:215], v[88:91], v[184:187], v[212:215]\n" \
    "s_waitcnt lgkmcnt(4)\n" \
    "v_mfma_f32_16x16x32_bf16 v[208:211], v[12:15], v[188:191], v[208:211]\n" \
    "ds_read_b128 v[176:179], v164 offset:8192\n" \
    "v_mfma_f32_16x16x32_bf16 v[212:215], v[92:95], v[188:191], v[212:215]\n" \
    "s_waitcnt lgkmcnt(4)\n" \
    "v_mfma_f32_16x16x32_bf16 v[208:211], v[16:19], v[192:195], v[208:211]\n" \
    "ds_read_b128 v[180:183], v164 offset:9216\n" \
    "v_mfma_f32_16x16x32_bf16 v[212:215], v[96:99], v[192:195], v[212:215]\n" \
    "s_waitcnt lgkmcnt(4)\n" \
    "v_mfma_f32_16x16x32_bf16 v[208:211], v[20:23], v[196:199], v[208:211]\n" \
    "ds_read_b128 v[184:187], v164 offset:10240\n" \
    "v_mfma_f32_16x16x32_bf16 v[212:215], v[100:103], v[196:199], v[212:215]\n" \
    "s_waitcnt lgkmcnt(4)\n" \
    "v_mfma_f32_16x16x32_bf16 v[208:211], v[24:27], v[200:203], v[208:211]\n" \
    "ds_read_b128 v[188:191], v164 offset:11264\n" \
    "v_mfma_f32_16x16x32_bf16 v[212:215], v[104:107], v[200:203], v[212:215]\n" \
    "s_waitcnt lgkmcnt(4)\n" \
    "v_mfma_f32_16x16x32_bf16 v[208:211], v[28:31], v[204:207], v[208:211]\n" \
    "ds_read_b128 v[192:195], v164 offset:12288\n" \
    "v_mfma_f32_16x16x32_bf16 v[212:215], v[108:111], v[204:207], v[212:215]\n" \
    "s_waitcnt lgkmcnt(4)\n" \
    "v_mfma_f32_16x16x32_bf16 v[208:211], v[32:35], v[176:179], v[208:211]\n" \
    "ds_read_b128 v[196:199], v164 offset:13312\n" \
    "v_mfma_f32_16x16x32_bf16 v[212:215], v[112:115], v[176:179], v[212:215]\n" \
    "s_waitcnt lgkmcnt(4)\n" \
    "v_mfma_f32_16x16x32_bf16 v[208:211], v[36:39], v[180:183], v[208:211]\n" \
    "ds_read_b128 v[200:203], v164 offset:14336\n" \
    "v_mfma_f32_16x16x32_bf16 v[212:215], v[116:119], v[180:183], v[212:215]\n" \
    "s_waitcnt lgkmcnt(4)\n" \
    "v_mfma_f32_16x16x32_bf16 v[208:211], v[40:43], v[184:187], v[208:211]\n" \
    "ds_read_b128 v[204:207], v164 offset:15360\n" \
    "v_mfma_f32_16x16x32_bf16 v[212:215], v[120:123], v[184:187], v[212:215]\n" \
    "s_waitcnt lgkmcnt(4)\n" \
    "v_mfma_f32_16x16x32_bf16 v[208:211], v[44:47], v[188:191], v[208:211]\n" \
    "ds_read_b128 v[176:179], v164 offset:16384\n" \
    "v_mfma_f32_16x16x32_bf16 v[212:215], v[124:127], v[188:191], v[212:215]\n" \
    "s_waitcnt lgkmcnt(4)\n" \
    "v_mfma_f32_16x16x32_bf16 v[208:211], v[48:51], v[192:195], v[208:211]\n" \
    "ds_read_b128 v[180:183], v164 offset:17408\n" \
    "v_mfma_f32_16x16x32_bf16 v[212:215], v[128:131], v[192:195], v[212:215]\n" \
    "s_waitcnt lgkmcnt(4)\n" \
    "v_mfma_f32_16x16x32_bf16 v[208:211], v[52:55], v[196:199], v[208:211]\n" \
    "ds_read_b128 v[184:187], v164 offset:18432\n" \
    "v_mfma_f32_16x16x32_bf16 v[212:215], v[132:135], v[196:199], v[212:215]\n" \
    "s_waitcnt lgkmcnt(4)\n" \
    "v_mfma_f32_16x16x32_bf16 v[208:211], v[56:59], v[200:203], v[208:211]\n" \
    "ds_read_b128 v[188:191], v164 offset:19456\n" \
    "v_mfma_f32_16x16x32_bf16 v[212:215], v[136:139], v[200:203], v[212:215]\n" \
    "s_setprio 1\n" \
    "s_waitcnt vmcnt(1)\n" \
    "s_waitcnt lgkmcnt(4)\n" \
    "v_mfma_f32_16x16x32_bf16 v[208:211], v[60:63], v[204:207], v[208:211]\n" \
    "v_mfma_f32_16x16x32_bf16 v[212:215], v[140:143], v[204:207], v[212:215]\n" \
    "s_barrier\n" \
    "s_waitcnt lgkmcnt(3)\n" \
    "v_mfma_f32_16x16x32_bf16 v[208:211], v[64:67], v[176:179], v[208:211]\n" \
    "v_mfma_f32_16x16x32_bf16 v[212:215], v[144:147], v[176:179], v[212:215]\n" \
    "ds_read_b32 v167, v165\n" \
    "s_waitcnt lgkmcnt(3)\n" \
    "v_mfma_f32_16x16x32_bf16 v[208:211], v[68:71], v[180:183], v[208:211]\n" \
    "v_mfma_f32_16x16x32_bf16 v[212:215], v[148:151], v[180:183], v[212:215]\n" \
    "s_waitcnt lgkmcnt(2)\n" \
    "v_mfma_f32_16x16x32_bf16 v[208:211], v[72:75], v[184:187], v[208:211]\n" \
    "v_mfma_f32_16x16x32_bf16 v[212:215], v[152:155], v[184:187], v[212:215]\n" \
    "s_waitcnt lgkmcnt(1)\n" \
    "v_mfma_f32_16x16x32_bf16 v[208:211], v[76:79], v[188:191], v[208:211]\n" \
    "v_mfma_f32_16x16x32_bf16 v[212:215], v[156:159], v[188:191], v[212:215]\n" \
    "s_waitcnt vmcnt(0)\n" \
    "s_branch .Lg_nt_%=\n" \
    ".Lg_eY_%=:\n" \
    "s_waitcnt vmcnt(0)\n" \
    "buffer_store_dwordx4 v[168:171], v161, s[8:11], s38 offen sc1\n" \
    "s_mov_b32 s38, 0x80000000\n" \
    "s_mov_b32 s45, 0x80000000\n" \
    "s_mov_b32 s37, 0\n" \
    "s_add_u32 s40, s32, s33\n" \
    "s_cmp_eq_u32 s34, -1\n" \
    "s_cselect_b32 s40, s40, 0\n" \
    "s_cmp_eq_u32 s40, s26\n" \
    "s_cbranch_scc1 .Lg_end_%=\n" \
    "s_add_u32 s46, s46, 1\n" \
    "s_cmp_lt_u32 s46, 0x20000\n" \
    "s_cbranch_scc1 .Lg_lY_%=\n" \
    "s_branch .Lg_end_%=\n" \
    ".Lg_lO_%=:\n" \
    "v_add_u32 v164, s41, v160\n" \
    "s_cmp_eq_u32 s34, -1\n" \
    "s_barrier\n" \
    "s_cbranch_scc1 .Lg_eO_%=\n" \
    ".Lg_mO_%=:\n" \
    "ds_read_b128 v[176:179], v164 offset:0\n" \
    "ds_read_b128 v[180:183], v164 offset:1024\n" \
    "ds_read_b128 v[184:187], v164 offset:2048\n" \
    "ds_read_b128 v[188:191], v164 offset:3072\n" \
    "ds_read_b128 v[192:195], v164 offset:4096\n" \
    "s_add_u32 s48, s41, 40960\n" \
    "s_cmp_ge_u32 s48, 81920\n" \
    "s_cselect_b32 s40, 81920, 0\n" \
    "s_sub_u32 s48, s48, s40\n" \
    "s_add_u32 m0, s48, s23\n" \
    "s_waitcnt lgkmcnt(4)\n" \
    "v_mfma_f32_16x16x32_bf16 v[208:211], v[0:3], v[176:179], 0\n" \
    "ds_read_b128 v[196:199], v164 offset:5120\n" \
    "v_mfma_f32_16x16x32_bf16 v[212:215], v[80:83], v[176:179], 0\n" \
    "s_waitcnt lgkmcnt(4)\n" \
    "v_mfma_f32_16x16x32_bf16 v[208:211], v[4:7], v[180:183], v[208:211]\n" \
    "ds_read_b128 v[200:203], v164 offset:6144\n" \
    "v_mfma_f32_16x16x32_bf16 v[212:215], v[84:87], v[180:183], v[212:215]\n" \
    "s_lshr_b32 s39, s34, 1\n" \
    "s_waitcnt lgkmcnt(4)\n" \
    "v_mfma_f32_16x16x32_bf16 v[208:211], v[8:11], v[184:187], v[208:211]\n" \
    "ds_read_b128 v[204:207], v164 offset:7168\n" \
    "v_mfma_f32_16x16x32_bf16 v[212:215], v[88:91], v[184:187], v[212:215]\n" \
    "s_sub_u32 s45, s39, %[r0]\n" \
    "s_cselect_b32 s45, s39, s45\n" \
    "s_waitcnt lgkmcnt(4)\n" \
    "v_mfma_f32_16x16x32_bf16 v[208:211], v[12:15], v[188:191], v[208:211]\n" \
    "ds_read_b128 v[176:179], v164 offset:8192\n" \
    "v_mfma_f32_16x16x32_bf16 v[212:215], v[92:95], v[188:191], v[212:215]\n" \
    "s_and_b32 s40, s39, 15\n" \
    "s_add_u32 s40, s40, %[r0]\n" \
    "s_waitcnt lgkmcnt(4)\n" \
    "v_mfma_f32_16x16x32_bf16 v[208:211], v[16:19], v[192:195], v[208:211]\n" \
    "ds_read_b128 v[180:183], v164 offset:9216\n" \
    "v_mfma_f32_16x16x32_bf16 v[212:215], v[96:99], v[192:195], v[212:215]\n" \
    "s_cmp_gt_u32 s39, 63\n" \
    "s_cselect_b32 s45, s40, s45\n" \
    "s_waitcnt lgkmcnt(4)\n" \
    "v_mfma_f32_16x16x32_bf16 v[208:211], v[20:23], v[196:199], v[208:211]\n" \
    "ds_read_b128 v[184:187], v164 offset:10240\n" \
    "v_mfma_f32_16x16x32_bf16 v[212:215], v[100:103], v[196:199], v[212:215]\n" \
    "s_mul_i32 s45, s45, 1572864\n" \
    "s_waitcnt lgkmcnt(4)\n" \
    "v_mfma_f32_16x16x32_bf16 v[208:211], v[24:27], v[200:203], v[208:211]\n" \
    "ds_read_b128 v[188:191], v164 offset:11264\n" \
    "v_mfma_f32_16x16x32_bf16 v[212:215], v[104:107], v[200:203], v[212:215]\n" \
    "s_bitcmp1_b32 s34, 0\n" \
    "s_cselect_b32 s40, s18, s17\n" \
    "s_waitcnt lgkmcnt(4)\n" \
    "v_mfma_f32_16x16x32_bf16 v[208:211], v[28:31], v[204:207], v[208:211]\n" \
    "ds_read_b128 v[192:195], v164 offset:12288\n" \
    "v_mfma_f32_16x16x32_bf16 v[212:215], v[108:111], v[204:207], v[212:215]\n" \
    "s_add_u32 s45, s45, s40\n" \
    "s_waitcnt lgkmcnt(4)\n" \
    "v_mfma_f32_16x16x32_bf16 v[208:211], v[32:35], v[176:179], v[208:211]\n" \
    "ds_read_b128 v[196:199], v164 offset:13312\n" \
    "v_mfma_f32_16x16x32_bf16 v[212:215], v[112:115], v[176:179], v[212:215]\n" \
    "s_lshr_b32 s40, s45, 8\n" \
    "s_waitcnt lgkmcnt(4)\n" \
    "v_mfma_f32_16x16x32_bf16 v[208:211], v[36:39], v[180:183], v[208:211]\n" \
    "ds_read_b128 v[200:203], v164 offset:14336\n" \
    "v_mfma_f32_16x16x32_bf16 v[212:215], v[116:119], v[180:183], v[212:215]\n" \
    "s_xor_b32 s40, s40, s19\n" \
    "s_waitcnt lgkmcnt(4)\n" \
    "v_mfma_f32_16x16x32_bf16 v[208:211], v[40:43], v[184:187], v[208:211]\n" \
    "ds_read_b128 v[204:207], v164 offset:15360\n" \
    "v_mfma_f32_16x16x32_bf16 v[212:215], v[120:123], v[184:187], v[212:215]\n" \
    "s_and_b32 s40, s40, 0x2000\n" \
    "s_or_b32 s45, s45, s40\n" \
    "s_waitcnt lgkmcnt(4)\n" \
    "v_mfma_f32_16x16x32_bf16 v[208:211], v[44:47], v[188:191], v[208:211]\n" \
    "ds_read_b128 v[176:179], v164 offset:16384\n" \
    "v_mfma_f32_16x16x32_bf16 v[212:215], v[124:127], v[188:191], v[212:215]\n" \
    "s_cmp_gt_u32 s34, 127\n" \
    "s_cselect_b32 s50, s21, s20\n" \
    "s_waitcnt lgkmcnt(4)\n" \
    "v_mfma_f32_16x16x32_bf16 v[208:211], v[48:51], v[192:195], v[208:211]\n" \
    "ds_read_b128 v[180:183], v164 offset:17408\n" \
    "v_mfma_f32_16x16x32_bf16 v[212:215], v[128:131], v[192:195], v[212:215]\n" \
    "s_bitcmp1_b32 s34, 0\n" \
    "s_cselect_b32 s50, s50, 0x80000000\n" \
    "s_waitcnt lgkmcnt(4)\n" \
    "v_mfma_f32_16x16x32_bf16 v[208:211], v[52:55], v[196:199], v[208:211]\n" \
    "ds_read_b128 v[184:187], v164 offset:18432\n" \
    "v_mfma_f32_16x16x32_bf16 v[212:215], v[132:135], v[196:199], v[212:215]\n" \
    "s_bfe_u32 s51, s34, 0x60001\n" \
    "s_add_u32 s51, s51, 1\n" \
    "s_waitcnt lgkmcnt(4)\n" \
    "v_mfma_f32_16x16x32_bf16 v[208:211], v[56:59], v[200:203], v[208:211]\n" \
    "ds_read_b128 v[188:191], v164 offset:19456\n" \
    "v_mfma_f32_16x16x32_bf16 v[212:215], v[136:139], v[200:203], v[212:215]\n" \
    "s_add_u32 s41, s41, 20480\n" \
    "s_waitcnt lgkmcnt(4)\n" \
    "v_mfma_f32_16x16x32_bf16 v[208:211], v[60:63], v[204:207], v[208:211]\n" \
    "v_mfma_f32_16x16x32_bf16 v[212:215], v[140:143], v[204:207], v[212:215]\n" \
    "s_cmp_eq_u32 s41, 81920\n" \
    "s_cselect_b32 s41, 0, s41\n" \
    "s_waitcnt lgkmcnt(3)\n" \
    "v_mfma_f32_16x16x32_bf16 v[208:211], v[64:67], v[176:179], v[208:211]\n" \
    "v_mfma_f32_16x16x32_bf16 v[212:215], v[144:147], v[176:179], v[212:215]\n" \
    "s_mov_b32 s46, 0\n" \
    "s_waitcnt lgkmcnt(2)\n" \
    "v_mfma_f32_16x16x32_bf16 v[208:211], v[68:71], v[180:183], v[208:211]\n" \
    "v_mfma_f32_16x16x32_bf16 v[212:215], v[148:151], v[180:183], v[212:215]\n" \
    "s_waitcnt lgkmcnt(1)\n" \
    "v_mfma_f32_16x16x32_bf16 v[208:211], v[72:75], v[184:187], v[208:211]\n" \
    "v_mfma_f32_16x16x32_bf16 v[212:215], v[152:155], v[184:187], v[212:215]\n" \
    "s_waitcnt lgkmcnt(0)\n" \
    "v_mfma_f32_16x16x32_bf16 v[208:211], v[76:79], v[188:191], v[208:211]\n" \
    "v_mfma_f32_16x16x32_bf16 v[212:215], v[156:159], v[188:191], v[212:215]\n" \
    "ds_read_b32 v167, v165\n" \
    "s_waitcnt vmcnt(4)\n" \
    "buffer_store_dwordx4 v[168:171], v161, s[8:11], s38 offen sc1\n" \
    "s_mov_b32 s38, 0x80000000\n" \
    "s_nop 2\n" \
    "v_cvt_pk_bf16_f32 v172, v208, v209\n" \
    "v_cvt_pk_bf16_f32 v173, v210, v211\n" \
    "v_cvt_pk_bf16_f32 v174, v212, v213\n" \
    "v_cvt_pk_bf16_f32 v175, v214, v215\n" \
    "buffer_store_dwordx4 v[172:175], v160, s[8:11], s45 offen sc1\n" \
    "s_mov_b32 s38, s50\n" \
    "v_mov_b32 v168, s51\n" \
    "v_add_u32 v164, s41, v160\n" \
    "s_waitcnt lgkmcnt(0)\n" \
    "v_readfirstlane_b32 s44, v167\n" \
    "s_nop 0\n" \
    "s_cmp_ge_u32 s44, s49\n" \
    "s_cbranch_scc1 .Lg_vO_%=\n" \
    ".Lg_wO_%=:\n" \
    "ds_read_b32 v167, v165\n" \
    "s_waitcnt lgkmcnt(0)\n" \
    "v_readfirstlane_b32 s44, v167\n" \
    "s_nop 0\n" \
    "s_cmp_lt_u32 s44, s49\n" \
    "s_cbranch_scc1 .Lg_wO_%=\n" \
    ".Lg_vO_%=:\n" \
    "s_add_u32 s49, s49, 256\n" \
    "v_xor_b32 v165, 4, v165\n" \
    "s_and_b32 s44, s44, 255\n" \
    "s_add_u32 s39, s33, 2*%[r0]-2\n" \
    "s_min_u32 s39, s39, s25\n" \
    "s_cmp_lt_u32 s32, s39\n" \
    "s_cselect_b32 s36, s32, -1\n" \
    "s_add_u32 s39, s33, 128\n" \
    "s_cmp_lt_u32 s33, s44\n" \
    "s_cselect_b32 s36, s39, s36\n" \
    "s_addc_u32 s33, s33, 0\n" \
    "s_cmp_lt_u32 s36, 128\n" \
    "s_addc_u32 s32, s32, 0\n" \
    "s_cmp_eq_u32 s32, s29\n" \
    "s_cselect_b32 s32, s30, s32\n" \
    "s_mul_i32 s40, s36, 20480\n" \
    "s_add_u32 s40, s40, s22\n" \
    "s_cmp_eq_u32 s36, -1\n" \
    "s_cselect_b32 s40, 0x80000000, s40\n" \
    "buffer_load_dwordx4 v160, s[8:11], s40 offen offset:0 sc1 lds\n" \
    "buffer_load_dwordx4 v160, s[8:11], s40 offen offset:1024 sc1 lds\n" \
    "buffer_load_dwordx4 v160, s[8:11], s40 offen offset:2048 sc1 lds\n" \
    "buffer_load_dwordx4 v160, s[8:11], s40 offen offset:3072 sc1 lds\n" \
    "s_mov_b32 s34, s35\n" \
    "s_mov_b32 s35, s36\n" \
    "s_cmp_eq_u32 s34, -1\n" \
    "s_waitcnt vmcnt(6) lgkmcnt(0)\n" \
    "s_barrier\n" \
    "s_cbranch_scc0 .Lg_mO_%=\n" \
    ".Lg_eO_%=:\n" \
    "s_waitcnt vmcnt(0)\n" \
    "buffer_store_dwordx4 v[168:171], v161, s[8:11], s38 offen sc1\n" \
    "s_mov_b32 s38, 0x80000000\n" \
    ".Lg_xO_%=:\n" \
    "ds_read_b32 v167, v165\n" \
    "s_waitcnt lgkmcnt(0)\n" \
    "v_readfirstlane_b32 s44, v167\n" \
    "s_nop 0\n" \
    "s_cmp_lt_u32 s44, s49\n" \
    "s_cbranch_scc1 .Lg_xO_%=\n" \
    "s_and_b32 s44, s44, 255\n" \
    "s_add_u32 s39, s33, 2*%[r0]-2\n" \
    "s_min_u32 s39, s39, s25\n" \
    "s_cmp_lt_u32 s32, s39\n" \
    "s_cselect_b32 s36, s32, -1\n" \
    "s_add_u32 s39, s33, 128\n" \
    "s_cmp_lt_u32 s33, s44\n" \
    "s_cselect_b32 s36, s39, s36\n" \
    "s_addc_u32 s33, s33, 0\n" \
    "s_cmp_lt_u32 s36, 128\n" \
    "s_addc_u32 s32, s32, 0\n" \
    "s_cmp_eq_u32 s32, s29\n" \
    "s_cselect_b32 s32, s30, s32\n" \
    "s_add_u32 s48, s41, 40960\n" \
    "s_cmp_ge_u32 s48, 81920\n" \
    "s_cselect_b32 s40, 81920, 0\n" \
    "s_sub_u32 s48, s48, s40\n" \
    "s_add_u32 m0, s48, s23\n" \
    "s_mul_i32 s40, s36, 20480\n" \
    "s_add_u32 s40, s40, s22\n" \
    "s_cmp_eq_u32 s36, -1\n" \
    "s_cselect_b32 s40, 0x80000000, s40\n" \
    "buffer_load_dwordx4 v160, s[8:11], s40 offen offset:0 sc1 lds\n" \
    "buffer_load_dwordx4 v160, s[8:11], s40 offen offset:1024 sc1 lds\n" \
    "buffer_load_dwordx4 v160, s[8:11], s40 offen offset:2048 sc1 lds\n" \
    "buffer_load_dwordx4 v160, s[8:11], s40 offen offset:3072 sc1 lds\n" \
    "s_mov_b32 s34, s35\n" \
    "s_mov_b32 s35, s36\n" \
    "s_add_u32 s41, s41, 20480\n" \
    "s_cmp_eq_u32 s41, 81920\n" \
    "s_cselect_b32 s41, 0, s41\n" \
    "s_add_u32 s49, s49, 256\n" \
    "v_xor_b32 v165, 4, v165\n" \
    "s_and_b32 s39, s34, s35\n" \
    "s_waitcnt lgkmcnt(0)\n" \
    "s_add_u32 s40, s32, s33\n" \
    "s_cmp_eq_u32 s39, -1\n" \
    "s_cselect_b32 s40, s40, 0\n" \
    "s_cmp_eq_u32 s40, s26\n" \
    "s_cbranch_scc1 .Lg_end_%=\n" \
    "s_add_u32 s46, s46, 1\n" \
    "s_cmp_lt_u32 s46, 0x20000\n" \
    "s_cbranch_scc1 .Lg_lO_%=\n" \
    "s_branch .Lg_end_%=\n" \
    ".Lg_lS_%=:\n" \
    "v_add_u32 v164, s41, v160\n" \
    "s_cmp_eq_u32 s34, -1\n" \
    "s_barrier\n" \
    "s_cbranch_scc1 .Lg_eS_%=\n" \
    ".Lg_mS_%=:\n" \
    "ds_read_b128 v[176:179], v164 offset:0\n" \
    "ds_read_b128 v[180:183], v164 offset:1024\n" \
    "ds_read_b128 v[184:187], v164 offset:2048\n" \
    "ds_read_b128 v[188:191], v164 offset:3072\n" \
    "ds_read_b128 v[192:195], v164 offset:4096\n" \
    "s_add_u32 s48, s41, 40960\n" \
    "s_cmp_ge_u32 s48, 81920\n" \
    "s_cselect_b32 s40, 81920, 0\n" \
    "s_sub_u32 s48, s48, s40\n" \
    "s_add_u32 m0, s48, s23\n" \
    "s_waitcnt lgkmcnt(4)\n" \
    "v_mfma_f32_16x16x32_bf16 v[208:211], v[0:3], v[176:179], 0\n" \
    "ds_read_b128 v[196:199], v164 offset:5120\n" \
    "v_mfma_f32_16x16x32_bf16 v[212:215], v[80:83], v[176:179], 0\n" \
    "s_waitcnt lgkmcnt(4)\n" \
    "v_mfma_f32_16x16x32_bf16 v[208:211], v[4:7], v[180:183], v[208:211]\n" \
    "ds_read_b128 v[200:203], v164 offset:6144\n" \
    "v_mfma_f32_16x16x32_bf16 v[212:215], v[84:87], v[180:183], v[212:215]\n" \
    "s_lshr_b32 s39, s34, 1\n" \
    "s_waitcnt lgkmcnt(4)\n" \
    "v_mfma_f32_16x16x32_bf16 v[208:211], v[8:11], v[184:187], v[208:211]\n" \
    "ds_read_b128 v[204:207], v164 offset:7168\n" \
    "v_mfma_f32_16x16x32_bf16 v[212:215], v[88:91], v[184:187], v[212:215]\n" \
    "s_sub_u32 s45, s39, %[r0]\n" \
    "s_cselect_b32 s45, s39, s45\n" \
    "s_waitcnt lgkmcnt(4)\n" \
    "v_mfma_f32_16x16x32_bf16 v[208:211], v[12:15], v[188:191], v[208:211]\n" \
    "ds_read_b128 v[176:179], v164 offset:8192\n" \
    "v_mfma_f32_16x16x32_bf16 v[212:215], v[92:95], v[188:191], v[212:215]\n" \
    "s_and_b32 s40, s39, 15\n" \
    "s_add_u32 s40, s40, %[r0]\n" \
    "s_waitcnt lgkmcnt(4)\n" \
    "v_mfma_f32_16x16x32_bf16 v[208:211], v[16:19], v[192:195], v[208:211]\n" \
    "ds_read_b128 v[180:183], v164 offset:9216\n" \
    "v_mfma_f32_16x16x32_bf16 v[212:215], v[96:99], v[192:195], v[212:215]\n" \
    "s_cmp_gt_u32 s39, 63\n" \
    "s_cselect_b32 s45, s40, s45\n" \
    "s_waitcnt lgkmcnt(4)\n" \
    "v_mfma_f32_16x16x32_bf16 v[208:211], v[20:23], v[196:199], v[208:211]\n" \
    "ds_read_b128 v[184:187], v164 offset:10240\n" \
    "v_mfma_f32_16x16x32_bf16 v[212:215], v[100:103], v[196:199], v[212:215]\n" \
    "s_mul_i32 s45, s45, 1572864\n" \
    "s_waitcnt lgkmcnt(4)\n" \
    "v_mfma_f32_16x16x32_bf16 v[208:211], v[24:27], v[200:203], v[208:211]\n" \
    "ds_read_b128 v[188:191], v164 offset:11264\n" \
    "v_mfma_f32_16x16x32_bf16 v[212:215], v[104:107], v[200:203], v[212:215]\n" \
    "s_bitcmp1_b32 s34, 0\n" \
    "s_cselect_b32 s40, s18, s17\n" \
    "s_waitcnt lgkmcnt(4)\n" \
    "v_mfma_f32_16x16x32_bf16 v[208:211], v[28:31], v[204:207], v[208:211]\n" \
    "ds_read_b128 v[192:195], v164 offset:12288\n" \
    "v_mfma_f32_16x16x32_bf16 v[212:215], v[108:111], v[204:207], v[212:215]\n" \
    "s_add_u32 s45, s45, s40\n" \
    "s_waitcnt lgkmcnt(4)\n" \
    "v_mfma_f32_16x16x32_bf16 v[208:211], v[32:35], v[176:179], v[208:211]\n" \
    "ds_read_b128 v[196:199], v164 offset:13312\n" \
    "v_mfma_f32_16x16x32_bf16 v[212:215], v[112:115], v[176:179], v[212:215]\n" \
    "s_lshr_b32 s40, s45, 8\n" \
    "s_waitcnt lgkmcnt(4)\n" \
    "v_mfma_f32_16x16x32_bf16 v[208:211], v[36:39], v[180:183], v[208:211]\n" \
    "ds_read_b128 v[200:203], v164 offset:14336\n" \
    "v_mfma_f32_16x16x32_bf16 v[212:215], v[116:119], v[180:183], v[212:215]\n" \
    "s_xor_b32 s40, s40, s19\n" \
    "s_waitcnt lgkmcnt(4)\n" \
    "v_mfma_f32_16x16x32_bf16 v[208:211], v[40:43], v[184:187], v[208:211]\n" \
    "ds_read_b128 v[204:207], v164 offset:15360\n" \
    "v_mfma_f32_16x16x32_bf16 v[212:215], v[120:123], v[184:187], v[212:215]\n" \
    "s_and_b32 s40, s40, 0x2000\n" \
    "s_or_b32 s45, s45, s40\n" \
    "s_waitcnt lgkmcnt(4)\n" \
    "v_mfma_f32_16x16x32_bf16 v[208:211], v[44:47], v[188:191], v[208:211]\n" \
    "ds_read_b128 v[176:179], v164 offset:16384\n" \
    "v_mfma_f32_16x16x32_bf16 v[212:215], v[124:127], v[188:191], v[212:215]\n" \
    "s_cmp_gt_u32 s34, 127\n" \
    "s_cselect_b32 s50, s21, s20\n" \
    "s_waitcnt lgkmcnt(4)\n" \
    "v_mfma_f32_16x16x32_bf16 v[208:211], v[48:51], v[192:195], v[208:211]\n" \
    "ds_read_b128 v[180:183], v164 offset:17408\n" \
    "v_mfma_f32_16x16x32_bf16 v[212:215], v[128:131], v[192:195], v[212:215]\n" \
    "s_bitcmp1_b32 s34, 0\n" \
    "s_cselect_b32 s50, s50, 0x80000000\n" \
    "s_waitcnt vmcnt(4)\n" \
    "v_cmp_ne_u32 vcc, 0, v166\n" \
    "s_waitcnt lgkmcnt(4)\n" \
    "v_mfma_f32_16x16x32_bf16 v[208:211], v[52:55], v[196:199], v[208:211]\n" \
    "ds_read_b128 v[184:187], v164 offset:18432\n" \
    "v_mfma_f32_16x16x32_bf16 v[212:215], v[132:135], v[196:199], v[212:215]\n" \
    "s_bfe_u32 s51, s34, 0x60001\n" \
    "s_add_u32 s51, s51, 1\n" \
    "s_not_b32 s39, vcc_lo\n" \
    "s_bitset1_b32 s39, 24\n" \
    "s_ff1_i32_b32 s39, s39\n" \
    "s_waitcnt lgkmcnt(4)\n" \
    "v_mfma_f32_16x16x32_bf16 v[208:211], v[56:59], v[200:203], v[208:211]\n" \
    "ds_read_b128 v[188:191], v164 offset:19456\n" \
    "v_mfma_f32_16x16x32_bf16 v[212:215], v[136:139], v[200:203], v[212:215]\n" \
    "s_add_u32 s41, s41, 20480\n" \
    "s_lshr_b32 s39, s39, 3\n" \
    "s_add_u32 s43, s43, s39\n" \
    "s_min_u32 s43, s43, 128\n" \
    "s_waitcnt lgkmcnt(4)\n" \
    "v_mfma_f32_16x16x32_bf16 v[208:211], v[60:63], v[204:207], v[208:211]\n" \
    "v_mfma_f32_16x16x32_bf16 v[212:215], v[140:143], v[204:207], v[212:215]\n" \
    "s_cmp_eq_u32 s41, 81920\n" \
    "s_cselect_b32 s41, 0, s41\n" \
    "s_or_b32 s44, s43, s49\n" \
    "v_mov_b32 v167, s44\n" \
    "ds_write_b32 v165, v167\n" \
    "s_waitcnt lgkmcnt(4)\n" \
    "v_mfma_f32_16x16x32_bf16 v[208:211], v[64:67], v[176:179], v[208:211]\n" \
    "v_mfma_f32_16x16x32_bf16 v[212:215], v[144:147], v[176:179], v[212:215]\n" \
    "s_mov_b32 s46, 0\n" \
    "s_waitcnt lgkmcnt(3)\n" \
    "v_mfma_f32_16x16x32_bf16 v[208:211], v[68:71], v[180:183], v[208:211]\n" \
    "v_mfma_f32_16x16x32_bf16 v[212:215], v[148:151], v[180:183], v[212:215]\n" \
    "s_waitcnt lgkmcnt(2)\n" \
    "v_mfma_f32_16x16x32_bf16 v[208:211], v[72:75], v[184:187], v[208:211]\n" \
    "v_mfma_f32_16x16x32_bf16 v[212:215], v[152:155], v[184:187], v[212:215]\n" \
    "s_waitcnt lgkmcnt(1)\n" \
    "v_mfma_f32_16x16x32_bf16 v[208:211], v[76:79], v[188:191], v[208:211]\n" \
    "v_mfma_f32_16x16x32_bf16 v[212:215], v[156:159], v[188:191], v[212:215]\n" \
    "s_waitcnt vmcnt(4)\n" \
    "buffer_store_dwordx4 v[168:171], v161, s[8:11], s38 offen sc1\n" \
    "s_mov_b32 s38, 0x80000000\n" \
    "s_nop 3\n" \
    "v_cvt_pk_bf16_f32 v172, v208, v209\n" \
    "v_cvt_pk_bf16_f32 v173, v210, v211\n" \
    "v_cvt_pk_bf16_f32 v174, v212, v213\n" \
    "v_cvt_pk_bf16_f32 v175, v214, v215\n" \
    "buffer_store_dwordx4 v[172:175], v160, s[8:11], s45 offen sc1\n" \
    "s_mov_b32 s38, s50\n" \
    "v_mov_b32 v168, s51\n" \
    "v_add_u32 v164, s41, v160\n" \
    "s_add_u32 s49, s49, 256\n" \
    "v_xor_b32 v165, 4, v165\n" \
    "s_and_b32 s44, s44, 255\n" \
    "s_add_u32 s39, s33, 2*%[r0]-2\n" \
    "s_min_u32 s39, s39, s25\n" \
    "s_cmp_lt_u32 s32, s39\n" \
    "s_cselect_b32 s36, s32, -1\n" \
    "s_add_u32 s39, s33, 128\n" \
    "s_cmp_lt_u32 s33, s44\n" \
    "s_cselect_b32 s36, s39, s36\n" \
    "s_addc_u32 s33, s33, 0\n" \
    "s_cmp_lt_u32 s36, 128\n" \
    "s_addc_u32 s32, s32, 0\n" \
    "s_cmp_eq_u32 s32, s29\n" \
    "s_cselect_b32 s32, s30, s32\n" \
    "s_lshl_b32 s42, s43, 9\n" \
    "buffer_load_dword v166, v162, s[8:11], s42 offen sc1\n" \
    "s_mul_i32 s40, s36, 20480\n" \
    "s_add_u32 s40, s40, s22\n" \
    "s_cmp_eq_u32 s36, -1\n" \
    "s_cselect_b32 s40, 0x80000000, s40\n" \
    "buffer_load_dwordx4 v160, s[8:11], s40 offen offset:0 sc1 lds\n" \
    "buffer_load_dwordx4 v160, s[8:11], s40 offen offset:1024 sc1 lds\n" \
    "buffer_load_dwordx4 v160, s[8:11], s40 offen offset:2048 sc1 lds\n" \
    "buffer_load_dwordx4 v160, s[8:11], s40 offen offset:3072 sc1 lds\n" \
    "s_mov_b32 s34, s35\n" \
    "s_mov_b32 s35, s36\n" \
    "s_cmp_eq_u32 s34, -1\n" \
    "s_waitcnt vmcnt(7) lgkmcnt(0)\n" \
    "s_barrier\n" \
    "s_cbranch_scc0 .Lg_mS_%=\n" \
    ".Lg_eS_%=:\n" \
    "s_and_b32 s39, s35, s46\n" \
    "s_cmp_lt_u32 s39, 2\n" \
    "s_cbranch_scc1 .Lg_ns_%=\n" \
    "s_sleep 2\n" \
    ".Lg_ns_%=:\n" \
    "s_lshl_b32 s42, s43, 9\n" \
    "buffer_load_dword v166, v162, s[8:11], s42 offen sc1\n" \
    "s_waitcnt vmcnt(0)\n" \
    "v_cmp_ne_u32 vcc, 0, v166\n" \
    "s_nop 0\n" \
    "s_not_b32 s39, vcc_lo\n" \
    "s_bitset1_b32 s39, 24\n" \
    "s_ff1_i32_b32 s39, s39\n" \
    "s_lshr_b32 s39, s39, 3\n" \
    "s_add_u32 s43, s43, s39\n" \
    "s_min_u32 s43, s43, 128\n" \
    "s_or_b32 s44, s43, s49\n" \
    "v_mov_b32 v167, s44\n" \
    "ds_write_b32 v165, v167\n" \
    "s_waitcnt vmcnt(0)\n" \
    "buffer_store_dwordx4 v[168:171], v161, s[8:11], s38 offen sc1\n" \
    "s_mov_b32 s38, 0x80000000\n" \
    "s_and_b32 s44, s44, 255\n" \
    "s_add_u32 s39, s33, 2*%[r0]-2\n" \
    "s_min_u32 s39, s39, s25\n" \
    "s_cmp_lt_u32 s32, s39\n" \
    "s_cselect_b32 s36, s32, -1\n" \
    "s_add_u32 s39, s33, 128\n" \
    "s_cmp_lt_u32 s33, s44\n" \
    "s_cselect_b32 s36, s39, s36\n" \
    "s_addc_u32 s33, s33, 0\n" \
    "s_cmp_lt_u32 s36, 128\n" \
    "s_addc_u32 s32, s32, 0\n" \
    "s_cmp_eq_u32 s32, s29\n" \
    "s_cselect_b32 s32, s30, s32\n" \
    "s_add_u32 s48, s41, 40960\n" \
    "s_cmp_ge_u32 s48, 81920\n" \
    "s_cselect_b32 s40, 81920, 0\n" \
    "s_sub_u32 s48, s48, s40\n" \
    "s_add_u32 m0, s48, s23\n" \
    "s_lshl_b32 s42, s43, 9\n" \
    "buffer_load_dword v166, v162, s[8:11], s42 offen sc1\n" \
    "s_mul_i32 s40, s36, 20480\n" \
    "s_add_u32 s40, s40, s22\n" \
    "s_cmp_eq_u32 s36, -1\n" \
    "s_cselect_b32 s40, 0x80000000, s40\n" \
    "buffer_load_dwordx4 v160, s[8:11], s40 offen offset:0 sc1 lds\n" \
    "buffer_load_dwordx4 v160, s[8:11], s40 offen offset:1024 sc1 lds\n" \
    "buffer_load_dwordx4 v160, s[8:11], s40 offen offset:2048 sc1 lds\n" \
    "buffer_load_dwordx4 v160, s[8:11], s40 offen offset:3072 sc1 lds\n" \
    "s_mov_b32 s34, s35\n" \
    "s_mov_b32 s35, s36\n" \
    "s_add_u32 s41, s41, 20480\n" \
    "s_cmp_eq_u32 s41, 81920\n" \
    "s_cselect_b32 s41, 0, s41\n" \
    "s_add_u32 s49, s49, 256\n" \
    "v_xor_b32 v165, 4, v165\n" \
    "s_and_b32 s39, s34, s35\n" \
    "s_waitcnt lgkmcnt(0)\n" \
    "s_add_u32 s40, s32, s33\n" \
    "s_cmp_eq_u32 s39, -1\n" \
    "s_cselect_b32 s40, s40, 0\n" \
    "s_cmp_eq_u32 s40, s26\n" \
    "s_cbranch_scc1 .Lg_end_%=\n" \
    "s_add_u32 s46, s46, 1\n" \
    "s_cmp_lt_u32 s46, 0x20000\n" \
    "s_cbranch_scc1 .Lg_lS_%=\n" \
    "s_branch .Lg_end_%=\n" \
    ".Lg_end_%=:\n" \
    "s_setprio 0\n" \
    "s_waitcnt vmcnt(0)\n" \
    ""
#define OB_ASM \
    "s_mov_b32 s32, 0\n" \
    "s_mov_b32 s33, 0\n" \
    "s_mov_b32 s34, -1\n" \
    "s_mov_b32 s36, 0x80000000\n" \
    "s_mov_b32 s39, 0\n" \
    "s_mov_b32 s69, 0x80000000\n" \
    "s_mov_b32 s45, 0\n" \
    "s_mov_b32 s44, 0x10000\n" \
    "v_mov_b32 v125, 24576\n" \
    "v_mov_b32 v132, 1\n" \
    "v_mov_b32 v133, 1\n" \
    "v_mov_b32 v134, 1\n" \
    "v_mov_b32 v135, 1\n" \
    "s_cmp_eq_u32 s16, 0\n" \
    "s_cbranch_scc1 .Lo_lS_%=\n" \
    "s_cmp_lt_u32 s16, 4\n" \
    "s_cbranch_scc1 .Lo_lO_%=\n" \
    ".Lo_lY_%=:\n" \
    "s_barrier\n" \
    "ds_read_b32 v129, v125\n" \
    ".Lo_nt_%=:\n" \
    "s_waitcnt lgkmcnt(0)\n" \
    "v_readfirstlane_b32 s42, v129\n" \
    "s_nop 0\n" \
    "s_bfe_u32 s43, s42, 0x80008\n" \
    "s_and_b32 s42, s42, 255\n" \
    "s_add_u32 s37, s33, %[brn]\n" \
    "s_min_u32 s42, s42, s37\n" \
    "s_cmp_lt_u32 s32, s42\n" \
    "s_cselect_b32 s35, s32, -1\n" \
    "s_add_u32 s37, s33, 64\n" \
    "s_cmp_lt_u32 s33, s43\n" \
    "s_cselect_b32 s35, s37, s35\n" \
    "s_addc_u32 s33, s33, 0\n" \
    "s_cmp_lt_u32 s35, 64\n" \
    "s_addc_u32 s32, s32, 0\n" \
    "s_xor_b32 s37, s39, 12288\n" \
    "s_add_u32 m0, s37, s20\n" \
    "s_sub_u32 s38, s35, %[r0]\n" \
    "s_cselect_b32 s38, s35, s38\n" \
    "s_and_b32 s37, s35, 15\n" \
    "s_add_u32 s37, s37, %[r0]\n" \
    "s_cmp_gt_u32 s35, 63\n" \
    "s_cselect_b32 s38, s37, s38\n" \
    "s_mul_i32 s38, s38, 1572864\n" \
    "s_add_u32 s38, s38, s19\n" \
    "s_lshr_b32 s37, s38, 8\n" \
    "s_xor_b32 s37, s37, s22\n" \
    "s_and_b32 s37, s37, 0x2000\n" \
    "s_or_b32 s38, s38, s37\n" \
    "s_cmp_eq_u32 s35, -1\n" \
    "s_cselect_b32 s38, 0x80000000, s38\n" \
    "buffer_load_dwordx4 v120, s[8:11], s38 offen offset:0 sc1 lds\n" \
    "buffer_load_dwordx4 v120, s[8:11], s38 offen offset:1024 sc1 lds\n" \
    "buffer_load_dwordx4 v120, s[8:11], s38 offen offset:2048 sc1 lds\n" \
    "buffer_load_dwordx4 v120, s[8:11], s38 offen offset:3072 sc1 lds\n" \
    "v_add_u32 v124, s39, v120\n" \
    "s_xor_b32 s39, s39, 12288\n" \
    "s_add_u32 s44, s44, 0x10000\n" \
    "v_xor_b32 v125, 4, v125\n" \
    "s_cmp_eq_u32 s34, -1\n" \
    "s_cbranch_scc1 .Lo_eY_%=\n" \
    "ds_read_b128 v[160:163], v124 offset:0\n" \
    "ds_read_b128 v[184:187], v124 offset:6144\n" \
    "ds_read_b128 v[164:167], v124 offset:1024\n" \
    "ds_read_b128 v[188:191], v124 offset:7168\n" \
    "ds_read_b128 v[168:171], v124 offset:2048\n" \
    "ds_read_b128 v[192:195], v124 offset:8192\n" \
    "ds_read_b128 v[172:175], v124 offset:3072\n" \
    "ds_read_b128 v[196:199], v124 offset:9216\n" \
    "ds_read_b128 v[176:179], v124 offset:4096\n" \
    "ds_read_b128 v[200:203], v124 offset:10240\n" \
    "ds_read_b128 v[180:183], v124 offset:5120\n" \
    "ds_read_b128 v[204:207], v124 offset:11264\n" \
    "s_lshr_b32 s37, s34, 6\n" \
    "s_mul_i32 s37, s37, %[brn]\n" \
    "s_and_b32 s38, s34, %[brn]-1\n" \
    "s_add_u32 s37, s37, s38\n" \
    "s_mul_i32 s37, s37, 2621440\n" \
    "s_add_u32 s37, s37, s17\n" \
    "s_lshr_b32 s38, s37, 8\n" \
    "s_xor_b32 s38, s38, s21\n" \
    "s_and_b32 s38, s38, 0x2000\n" \
    "s_or_b32 s46, s37, s38\n" \
    "s_add_u32 s37, s37, 65536\n" \
    "s_lshr_b32 s38, s37, 8\n" \
    "s_xor_b32 s38, s38, s21\n" \
    "s_and_b32 s38, s38, 0x2000\n" \
    "s_or_b32 s47, s37, s38\n" \
    "s_add_u32 s37, s37, 65536\n" \
    "s_lshr_b32 s38, s37, 8\n" \
    "s_xor_b32 s38, s38, s21\n" \
    "s_and_b32 s38, s38, 0x2000\n" \
    "s_or_b32 s48, s37, s38\n" \
    "s_add_u32 s37, s37, 65536\n" \
    "s_lshr_b32 s38, s37, 8\n" \
    "s_xor_b32 s38, s38, s21\n" \
    "s_and_b32 s38, s38, 0x2000\n" \
    "s_or_b32 s49, s37, s38\n" \
    "s_add_u32 s37, s37, 65536\n" \
    "s_lshr_b32 s38, s37, 8\n" \
    "s_xor_b32 s38, s38, s21\n" \
    "s_and_b32 s38, s38, 0x2000\n" \
    "s_or_b32 s50, s37, s38\n" \
    "s_waitcnt vmcnt(4)\n" \
    "buffer_store_dwordx4 v[132:135], v121, s[8:11], s36 offen sc1\n" \
    "s_mov_b32 s36, 0x80000000\n" \
    "s_mov_b32 s45, 0\n" \
    "s_waitcnt lgkmcnt(11)\n" \
    "v_mfma_f32_16x16x32_bf16 v[144:147], v[160:163], v[0:3], 0\n" \
    "s_waitcnt lgkmcnt(10)\n" \
    "v_mfma_f32_16x16x32_bf16 v[148:151], v[184:187], v[0:3], 0\n" \
    "s_waitcnt lgkmcnt(9)\n" \
    "v_mfma_f32_16x16x32_bf16 v[144:147], v[164:167], v[4:7], v[144:147]\n" \
    "s_waitcnt lgkmcnt(8)\n" \
    "v_mfma_f32_16x16x32_bf16 v[148:151], v[188:191], v[4:7], v[148:151]\n" \
    "s_waitcnt lgkmcnt(7)\n" \
    "v_mfma_f32_16x16x32_bf16 v[144:147], v[168:171], v[8:11], v[144:147]\n" \
    "s_waitcnt lgkmcnt(6)\n" \
    "v_mfma_f32_16x16x32_bf16 v[148:151], v[192:195], v[8:11], v[148:151]\n" \
    "s_waitcnt lgkmcnt(5)\n" \
    "v_mfma_f32_16x16x32_bf16 v[144:147], v[172:175], v[12:15], v[144:147]\n" \
    "s_waitcnt lgkmcnt(4)\n" \
    "v_mfma_f32_16x16x32_bf16 v[148:151], v[196:199], v[12:15], v[148:151]\n" \
    "s_waitcnt lgkmcnt(3)\n" \
    "v_mfma_f32_16x16x32_bf16 v[144:147], v[176:179], v[16:19], v[144:147]\n" \
    "s_waitcnt lgkmcnt(2)\n" \
    "v_mfma_f32_16x16x32_bf16 v[148:151], v[200:203], v[16:19], v[148:151]\n" \
    "s_waitcnt lgkmcnt(1)\n" \
    "v_mfma_f32_16x16x32_bf16 v[144:147], v[180:183], v[20:23], v[144:147]\n" \
    "s_waitcnt lgkmcnt(0)\n" \
    "v_mfma_f32_16x16x32_bf16 v[148:151], v[204:207], v[20:23], v[148:151]\n" \
    "v_mfma_f32_16x16x32_bf16 v[152:155], v[160:163], v[24:27], 0\n" \
    "v_mfma_f32_16x16x32_bf16 v[156:159], v[184:187], v[24:27], 0\n" \
    "v_mfma_f32_16x16x32_bf16 v[152:155], v[164:167], v[28:31], v[152:155]\n" \
    "v_mfma_f32_16x16x32_bf16 v[156:159], v[188:191], v[28:31], v[156:159]\n" \
    "v_mfma_f32_16x16x32_bf16 v[152:155], v[168:171], v[32:35], v[152:155]\n" \
    "v_mfma_f32_16x16x32_bf16 v[156:159], v[192:195], v[32:35], v[156:159]\n" \
    "v_mfma_f32_16x16x32_bf16 v[152:155], v[172:175], v[36:39], v[152:155]\n" \
    "v_mfma_f32_16x16x32_bf16 v[156:159], v[196:199], v[36:39], v[156:159]\n" \
    "v_cvt_pk_bf16_f32 v136, v144, v145\n" \
    "v_mfma_f32_16x16x32_bf16 v[152:155], v[176:179], v[40:43], v[152:155]\n" \
    "v_cvt_pk_bf16_f32 v137, v148, v149\n" \
    "v_mfma_f32_16x16x32_bf16 v[156:159], v[200:203], v[40:43], v[156:159]\n" \
    "v_cvt_pk_bf16_f32 v138, v146, v147\n" \
    "v_mfma_f32_16x16x32_bf16 v[152:155], v[180:183], v[44:47], v[152:155]\n" \
    "v_cvt_pk_bf16_f32 v139, v150, v151\n" \
    "v_mfma_f32_16x16x32_bf16 v[156:159], v[204:207], v[44:47], v[156:159]\n" \
    "buffer_store_dwordx4 v[136:139], v120, s[8:11], s46 offen sc1\n" \
    "v_mfma_f32_16x16x32_bf16 v[144:147], v[160:163], v[48:51], 0\n" \
    "v_mfma_f32_16x16x32_bf16 v[148:151], v[184:187], v[48:51], 0\n" \
    "v_mfma_f32_16x16x32_bf16 v[144:147], v[164:167], v[52:55], v[144:147]\n" \
    "v_mfma_f32_16x16x32_bf16 v[148:151], v[188:191], v[52:55], v[148:151]\n" \
    "v_mfma_f32_16x16x32_bf16 v[144:147], v[168:171], v[56:59], v[144:147]\n" \
    "v_mfma_f32_16x16x32_bf16 v[148:151], v[192:195], v[56:59], v[148:151]\n" \
    "v_mfma_f32_16x16x32_bf16 v[144:147], v[172:175], v[60:63], v[144:147]\n" \
    "v_mfma_f32_16x16x32_bf16 v[148:151], v[196:199], v[60:63], v[148:151]\n" \
    "v_cvt_pk_bf16_f32 v140, v152, v153\n" \
    "v_mfma_f32_16x16x32_bf16 v[144:147], v[176:179], v[64:67], v[144:147]\n" \
    "v_cvt_pk_bf16_f32 v141, v156, v157\n" \
    "v_mfma_f32_16x16x32_bf16 v[148:151], v[200:203], v[64:67], v[148:151]\n" \
    "v_cvt_pk_bf16_f32 v142, v154, v155\n" \
    "v_mfma_f32_16x16x32_bf16 v[144:147], v[180:183], v[68:71], v[144:147]\n" \
    "v_cvt_pk_bf16_f32 v143, v158, v159\n" \
    "v_mfma_f32_16x16x32_bf16 v[148:151], v[204:207], v[68:71], v[148:151]\n" \
    "buffer_store_dwordx4 v[140:143], v120, s[8:11], s47 offen sc1\n" \
    "v_mfma_f32_16x16x32_bf16 v[152:155], v[160:163], v[72:75], 0\n" \
    "v_mfma_f32_16x16x32_bf16 v[156:159], v[184:187], v[72:75], 0\n" \
    "v_mfma_f32_16x16x32_bf16 v[152:155], v[164:167], v[76:79], v[152:155]\n" \
    "v_mfma_f32_16x16x32_bf16 v[156:159], v[188:191], v[76:79], v[156:159]\n" \
    "v_mfma_f32_16x16x32_bf16 v[152:155], v[168:171], v[80:83], v[152:155]\n" \
    "v_mfma_f32_16x16x32_bf16 v[156:159], v[192:195], v[80:83], v[156:159]\n" \
    "v_mfma_f32_16x16x32_bf16 v[152:155], v[172:175], v[84:87], v[152:155]\n" \
    "v_mfma_f32_16x16x32_bf16 v[156:159], v[196:199], v[84:87], v[156:159]\n" \
    "v_cvt_pk_bf16_f32 v136, v144, v145\n" \
    "v_mfma_f32_16x16x32_bf16 v[152:155], v[176:179], v[88:91], v[152:155]\n" \
    "v_cvt_pk_bf16_f32 v137, v148, v149\n" \
    "v_mfma_f32_16x16x32_bf16 v[156:159], v[200:203], v[88:91], v[156:159]\n" \
    "v_cvt_pk_bf16_f32 v138, v146, v147\n" \
    "v_mfma_f32_16x16x32_bf16 v[152:155], v[180:183], v[92:95], v[152:155]\n" \
    "v_cvt_pk_bf16_f32 v139, v150, v151\n" \
    "s_setprio 1\n" \
    "s_waitcnt vmcnt(3)\n" \
    "v_mfma_f32_16x16x32_bf16 v[156:159], v[204:207], v[92:95], v[156:159]\n" \
    "buffer_store_dwordx4 v[136:139], v120, s[8:11], s48 offen sc1\n" \
    "s_barrier\n" \
    "v_mfma_f32_16x16x32_bf16 v[144:147], v[160:163], v[96:99], 0\n" \
    "ds_read_b32 v129, v125\n" \
    "v_mfma_f32_16x16x32_bf16 v[148:151], v[184:187], v[96:99], 0\n" \
    "v_mfma_f32_16x16x32_bf16 v[144:147], v[164:167], v[100:103], v[144:147]\n" \
    "v_mfma_f32_16x16x32_bf16 v[148:151], v[188:191], v[100:103], v[148:151]\n" \
    "v_mfma_f32_16x16x32_bf16 v[144:147], v[168:171], v[104:107], v[144:147]\n" \
    "v_mfma_f32_16x16x32_bf16 v[148:151], v[192:195], v[104:107], v[148:151]\n" \
    "v_mfma_f32_16x16x32_bf16 v[144:147], v[172:175], v[108:111], v[144:147]\n" \
    "v_mfma_f32_16x16x32_bf16 v[148:151], v[196:199], v[108:111], v[148:151]\n" \
    "v_cvt_pk_bf16_f32 v140, v152, v153\n" \
    "v_mfma_f32_16x16x32_bf16 v[144:147], v[176:179], v[112:115], v[144:147]\n" \
    "v_cvt_pk_bf16_f32 v141, v156, v157\n" \
    "v_mfma_f32_16x16x32_bf16 v[148:151], v[200:203], v[112:115], v[148:151]\n" \
    "v_cvt_pk_bf16_f32 v142, v154, v155\n" \
    "v_mfma_f32_16x16x32_bf16 v[144:147], v[180:183], v[116:119], v[144:147]\n" \
    "v_cvt_pk_bf16_f32 v143, v158, v159\n" \
    "v_mfma_f32_16x16x32_bf16 v[148:151], v[204:207], v[116:119], v[148:151]\n" \
    "buffer_store_dwordx4 v[140:143], v120, s[8:11], s49 offen sc1\n" \
    "s_setprio 0\n" \
    "s_lshr_b32 s36, s34, 6\n" \
    "s_lshl_b32 s36, s36, 15\n" \
    "s_add_u32 s36, s36, s18\n" \
    "s_and_b32 s37, s34, 63\n" \
    "s_add_u32 s37, s37, 1\n" \
    "v_mov_b32 v132, s37\n" \
    "v_cvt_pk_bf16_f32 v136, v144, v145\n" \
    "v_cvt_pk_bf16_f32 v137, v148, v149\n" \
    "v_cvt_pk_bf16_f32 v138, v146, v147\n" \
    "v_cvt_pk_bf16_f32 v139, v150, v151\n" \
    "buffer_store_dwordx4 v[136:139], v120, s[8:11], s50 offen sc1\n" \
    "s_mov_b32 s34, s35\n" \
    "s_branch .Lo_nt_%=\n" \
    ".Lo_eY_%=:\n" \
    "s_waitcnt vmcnt(4)\n" \
    "buffer_store_dwordx4 v[132:135], v121, s[8:11], s36 offen sc1\n" \
    "s_mov_b32 s36, 0x80000000\n" \
    "s_mov_b32 s34, s35\n" \
    "s_waitcnt vmcnt(1)\n" \
    "s_add_u32 s38, s32, s33\n" \
    "s_cmp_eq_u32 s34, -1\n" \
    "s_cselect_b32 s38, s38, 0\n" \
    "s_cmp_eq_u32 s38, 128\n" \
    "s_cbranch_scc1 .Lo_end_%=\n" \
    "s_add_u32 s45, s45, 1\n" \
    "s_cmp_lt_u32 s45, 0x8000\n" \
    "s_cbranch_scc1 .Lo_lY_%=\n" \
    "s_branch .Lo_end_%=\n" \
    ".Lo_lO_%=:\n" \
    "v_add_u32 v124, s39, v120\n" \
    "s_cmp_eq_u32 s34, -1\n" \
    "s_barrier\n" \
    "s_cbranch_scc1 .Lo_eO_%=\n" \
    "ds_read_b128 v[160:163], v124 offset:0\n" \
    "ds_read_b128 v[184:187], v124 offset:6144\n" \
    "ds_read_b128 v[164:167], v124 offset:1024\n" \
    "ds_read_b128 v[188:191], v124 offset:7168\n" \
    "ds_read_b128 v[168:171], v124 offset:2048\n" \
    "ds_read_b128 v[192:195], v124 offset:8192\n" \
    "ds_read_b128 v[172:175], v124 offset:3072\n" \
    "ds_read_b128 v[196:199], v124 offset:9216\n" \
    "ds_read_b128 v[176:179], v124 offset:4096\n" \
    "ds_read_b128 v[200:203], v124 offset:10240\n" \
    "ds_read_b128 v[180:183], v124 offset:5120\n" \
    "ds_read_b128 v[204:207], v124 offset:11264\n" \
    "ds_read_b32 v129, v125\n" \
    "s_waitcnt lgkmcnt(12)\n" \
    "v_mfma_f32_16x16x32_bf16 v[144:147], v[160:163], v[0:3], 0\n" \
    "s_waitcnt lgkmcnt(11)\n" \
    "v_mfma_f32_16x16x32_bf16 v[148:151], v[184:187], v[0:3], 0\n" \
    "s_waitcnt lgkmcnt(10)\n" \
    "v_mfma_f32_16x16x32_bf16 v[144:147], v[164:167], v[4:7], v[144:147]\n" \
    "s_waitcnt lgkmcnt(9)\n" \
    "v_mfma_f32_16x16x32_bf16 v[148:151], v[188:191], v[4:7], v[148:151]\n" \
    "s_waitcnt lgkmcnt(8)\n" \
    "v_mfma_f32_16x16x32_bf16 v[144:147], v[168:171], v[8:11], v[144:147]\n" \
    "s_waitcnt vmcnt(0)\n" \
    "buffer_store_dwordx4 v[132:135], v121, s[8:11], s36 offen sc1\n" \
    "s_waitcnt lgkmcnt(7)\n" \
    "v_mfma_f32_16x16x32_bf16 v[148:151], v[192:195], v[8:11], v[148:151]\n" \
    "s_waitcnt lgkmcnt(6)\n" \
    "v_mfma_f32_16x16x32_bf16 v[144:147], v[172:175], v[12:15], v[144:147]\n" \
    "s_waitcnt lgkmcnt(5)\n" \
    "v_mfma_f32_16x16x32_bf16 v[148:151], v[196:199], v[12:15], v[148:151]\n" \
    "s_waitcnt lgkmcnt(4)\n" \
    "v_mfma_f32_16x16x32_bf16 v[144:147], v[176:179], v[16:19], v[144:147]\n" \
    "s_waitcnt lgkmcnt(3)\n" \
    "v_mfma_f32_16x16x32_bf16 v[148:151], v[200:203], v[16:19], v[148:151]\n" \
    "s_waitcnt lgkmcnt(2)\n" \
    "v_mfma_f32_16x16x32_bf16 v[144:147], v[180:183], v[20:23], v[144:147]\n" \
    "s_waitcnt lgkmcnt(1)\n" \
    "v_mfma_f32_16x16x32_bf16 v[148:151], v[204:207], v[20:23], v[148:151]\n" \
    "v_mfma_f32_16x16x32_bf16 v[152:155], v[160:163], v[24:27], 0\n" \
    "v_mfma_f32_16x16x32_bf16 v[156:159], v[184:187], v[24:27], 0\n" \
    "v_mfma_f32_16x16x32_bf16 v[152:155], v[164:167], v[28:31], v[152:155]\n" \
    "v_mfma_f32_16x16x32_bf16 v[156:159], v[188:191], v[28:31], v[156:159]\n" \
    "v_mfma_f32_16x16x32_bf16 v[152:155], v[168:171], v[32:35], v[152:155]\n" \
    "v_mfma_f32_16x16x32_bf16 v[156:159], v[192:195], v[32:35], v[156:159]\n" \
    "v_mfma_f32_16x16x32_bf16 v[152:155], v[172:175], v[36:39], v[152:155]\n" \
    "v_mfma_f32_16x16x32_bf16 v[156:159], v[196:199], v[36:39], v[156:159]\n" \
    "v_cvt_pk_bf16_f32 v136, v144, v145\n" \
    "v_mfma_f32_16x16x32_bf16 v[152:155], v[176:179], v[40:43], v[152:155]\n" \
    "v_cvt_pk_bf16_f32 v137, v148, v149\n" \
    "v_mfma_f32_16x16x32_bf16 v[156:159], v[200:203], v[40:43], v[156:159]\n" \
    "v_cvt_pk_bf16_f32 v138, v146, v147\n" \
    "v_mfma_f32_16x16x32_bf16 v[152:155], v[180:183], v[44:47], v[152:155]\n" \
    "v_cvt_pk_bf16_f32 v139, v150, v151\n" \
    "v_mfma_f32_16x16x32_bf16 v[156:159], v[204:207], v[44:47], v[156:159]\n" \
    "buffer_store_dwordx4 v[136:139], v120, s[8:11], s46 offen sc1\n" \
    "v_mfma_f32_16x16x32_bf16 v[144:147], v[160:163], v[48:51], 0\n" \
    "v_mfma_f32_16x16x32_bf16 v[148:151], v[184:187], v[48:51], 0\n" \
    "v_mfma_f32_16x16x32_bf16 v[144:147], v[164:167], v[52:55], v[144:147]\n" \
    "v_mfma_f32_16x16x32_bf16 v[148:151], v[188:191], v[52:55], v[148:151]\n" \
    "v_mfma_f32_16x16x32_bf16 v[144:147], v[168:171], v[56:59], v[144:147]\n" \
    "v_mfma_f32_16x16x32_bf16 v[148:151], v[192:195], v[56:59], v[148:151]\n" \
    "v_mfma_f32_16x16x32_bf16 v[144:147], v[172:175], v[60:63], v[144:147]\n" \
    "v_mfma_f32_16x16x32_bf16 v[148:151], v[196:199], v[60:63], v[148:151]\n" \
    "v_cvt_pk_bf16_f32 v140, v152, v153\n" \
    "v_mfma_f32_16x16x32_bf16 v[144:147], v[176:179], v[64:67], v[144:147]\n" \
    "v_cvt_pk_bf16_f32 v141, v156, v157\n" \
    "v_mfma_f32_16x16x32_bf16 v[148:151], v[200:203], v[64:67], v[148:151]\n" \
    "v_cvt_pk_bf16_f32 v142, v154, v155\n" \
    "v_mfma_f32_16x16x32_bf16 v[144:147], v[180:183], v[68:71], v[144:147]\n" \
    "v_cvt_pk_bf16_f32 v143, v158, v159\n" \
    "v_mfma_f32_16x16x32_bf16 v[148:151], v[204:207], v[68:71], v[148:151]\n" \
    "buffer_store_dwordx4 v[140:143], v120, s[8:11], s47 offen sc1\n" \
    "v_mfma_f32_16x16x32_bf16 v[152:155], v[160:163], v[72:75], 0\n" \
    "v_mfma_f32_16x16x32_bf16 v[156:159], v[184:187], v[72:75], 0\n" \
    "v_mfma_f32_16x16x32_bf16 v[152:155], v[164:167], v[76:79], v[152:155]\n" \
    "v_mfma_f32_16x16x32_bf16 v[156:159], v[188:191], v[76:79], v[156:159]\n" \
    "v_mfma_f32_16x16x32_bf16 v[152:155], v[168:171], v[80:83], v[152:155]\n" \
    "v_mfma_f32_16x16x32_bf16 v[156:159], v[192:195], v[80:83], v[156:159]\n" \
    "v_mfma_f32_16x16x32_bf16 v[152:155], v[172:175], v[84:87], v[152:155]\n" \
    "v_mfma_f32_16x16x32_bf16 v[156:159], v[196:199], v[84:87], v[156:159]\n" \
    "v_cvt_pk_bf16_f32 v136, v144, v145\n" \
    "v_mfma_f32_16x16x32_bf16 v[152:155], v[176:179], v[88:91], v[152:155]\n" \
    "v_cvt_pk_bf16_f32 v137, v148, v149\n" \
    "v_mfma_f32_16x16x32_bf16 v[156:159], v[200:203], v[88:91], v[156:159]\n" \
    "v_cvt_pk_bf16_f32 v138, v146, v147\n" \
    "v_mfma_f32_16x16x32_bf16 v[152:155], v[180:183], v[92:95], v[152:155]\n" \
    "v_cvt_pk_bf16_f32 v139, v150, v151\n" \
    "v_mfma_f32_16x16x32_bf16 v[156:159], v[204:207], v[92:95], v[156:159]\n" \
    "buffer_store_dwordx4 v[136:139], v120, s[8:11], s48 offen sc1\n" \
    "v_mfma_f32_16x16x32_bf16 v[144:147], v[160:163], v[96:99], 0\n" \
    "v_mfma_f32_16x16x32_bf16 v[148:151], v[184:187], v[96:99], 0\n" \
    "v_mfma_f32_16x16x32_bf16 v[144:147], v[164:167], v[100:103], v[144:147]\n" \
    "v_mfma_f32_16x16x32_bf16 v[148:151], v[188:191], v[100:103], v[148:151]\n" \
    "v_mfma_f32_16x16x32_bf16 v[144:147], v[168:171], v[104:107], v[144:147]\n" \
    "v_mfma_f32_16x16x32_bf16 v[148:151], v[192:195], v[104:107], v[148:151]\n" \
    "v_mfma_f32_16x16x32_bf16 v[144:147], v[172:175], v[108:111], v[144:147]\n" \
    "v_mfma_f32_16x16x32_bf16 v[148:151], v[196:199], v[108:111], v[148:151]\n" \
    "v_cvt_pk_bf16_f32 v140, v152, v153\n" \
    "v_mfma_f32_16x16x32_bf16 v[144:147], v[176:179], v[112:115], v[144:147]\n" \
    "v_cvt_pk_bf16_f32 v141, v156, v157\n" \
    "v_mfma_f32_16x16x32_bf16 v[148:151], v[200:203], v[112:115], v[148:151]\n" \
    "v_cvt_pk_bf16_f32 v142, v154, v155\n" \
    "v_mfma_f32_16x16x32_bf16 v[144:147], v[180:183], v[116:119], v[144:147]\n" \
    "v_cvt_pk_bf16_f32 v143, v158, v159\n" \
    "v_mfma_f32_16x16x32_bf16 v[148:151], v[204:207], v[116:119], v[148:151]\n" \
    "buffer_store_dwordx4 v[140:143], v120, s[8:11], s49 offen sc1\n" \
    "s_lshr_b32 s36, s34, 6\n" \
    "s_lshl_b32 s36, s36, 15\n" \
    "s_add_u32 s36, s36, s18\n" \
    "s_and_b32 s37, s34, 63\n" \
    "s_add_u32 s37, s37, 1\n" \
    "v_mov_b32 v132, s37\n" \
    "v_cvt_pk_bf16_f32 v136, v144, v145\n" \
    "v_cvt_pk_bf16_f32 v137, v148, v149\n" \
    "v_cvt_pk_bf16_f32 v138, v146, v147\n" \
    "v_cvt_pk_bf16_f32 v139, v150, v151\n" \
    "buffer_store_dwordx4 v[136:139], v120, s[8:11], s50 offen sc1\n" \
    "s_waitcnt lgkmcnt(0)\n" \
    "v_readfirstlane_b32 s42, v129\n" \
    "s_nop 0\n" \
    "s_bfe_u32 s43, s42, 0x80008\n" \
    "s_and_b32 s42, s42, 255\n" \
    "s_add_u32 s37, s33, %[brn]\n" \
    "s_min_u32 s42, s42, s37\n" \
    "s_cmp_lt_u32 s32, s42\n" \
    "s_cselect_b32 s34, s32, -1\n" \
    "s_add_u32 s37, s33, 64\n" \
    "s_cmp_lt_u32 s33, s43\n" \
    "s_cselect_b32 s34, s37, s34\n" \
    "s_addc_u32 s33, s33, 0\n" \
    "s_cmp_lt_u32 s34, 64\n" \
    "s_addc_u32 s32, s32, 0\n" \
    "s_mov_b32 s45, 0\n" \
    "s_xor_b32 s39, s39, 12288\n" \
    "s_add_u32 s44, s44, 0x10000\n" \
    "v_xor_b32 v125, 4, v125\n" \
    "s_lshr_b32 s37, s34, 6\n" \
    "s_mul_i32 s37, s37, %[brn]\n" \
    "s_and_b32 s38, s34, %[brn]-1\n" \
    "s_add_u32 s37, s37, s38\n" \
    "s_mul_i32 s37, s37, 2621440\n" \
    "s_add_u32 s37, s37, s17\n" \
    "s_lshr_b32 s38, s37, 8\n" \
    "s_xor_b32 s38, s38, s21\n" \
    "s_and_b32 s38, s38, 0x2000\n" \
    "s_or_b32 s46, s37, s38\n" \
    "s_add_u32 s37, s37, 65536\n" \
    "s_lshr_b32 s38, s37, 8\n" \
    "s_xor_b32 s38, s38, s21\n" \
    "s_and_b32 s38, s38, 0x2000\n" \
    "s_or_b32 s47, s37, s38\n" \
    "s_add_u32 s37, s37, 65536\n" \
    "s_lshr_b32 s38, s37, 8\n" \
    "s_xor_b32 s38, s38, s21\n" \
    "s_and_b32 s38, s38, 0x2000\n" \
    "s_or_b32 s48, s37, s38\n" \
    "s_add_u32 s37, s37, 65536\n" \
    "s_lshr_b32 s38, s37, 8\n" \
    "s_xor_b32 s38, s38, s21\n" \
    "s_and_b32 s38, s38, 0x2000\n" \
    "s_or_b32 s49, s37, s38\n" \
    "s_add_u32 s37, s37, 65536\n" \
    "s_lshr_b32 s38, s37, 8\n" \
    "s_xor_b32 s38, s38, s21\n" \
    "s_and_b32 s38, s38, 0x2000\n" \
    "s_or_b32 s50, s37, s38\n" \
    "s_waitcnt lgkmcnt(0)\n" \
    "s_branch .Lo_lO_%=\n" \
    ".Lo_eO_%=:\n" \
    "ds_read_b32 v129, v125\n" \
    "s_waitcnt lgkmcnt(0)\n" \
    "v_readfirstlane_b32 s42, v129\n" \
    "s_nop 0\n" \
    "s_bfe_u32 s43, s42, 0x80008\n" \
    "s_and_b32 s42, s42, 255\n" \
    "s_add_u32 s37, s33, %[brn]\n" \
    "s_min_u32 s42, s42, s37\n" \
    "s_cmp_lt_u32 s32, s42\n" \
    "s_cselect_b32 s34, s32, -1\n" \
    "s_add_u32 s37, s33, 64\n" \
    "s_cmp_lt_u32 s33, s43\n" \
    "s_cselect_b32 s34, s37, s34\n" \
    "s_addc_u32 s33, s33, 0\n" \
    "s_cmp_lt_u32 s34, 64\n" \
    "s_addc_u32 s32, s32, 0\n" \
    "s_waitcnt vmcnt(0)\n" \
    "buffer_store_dwordx4 v[132:135], v121, s[8:11], s36 offen sc1\n" \
    "s_mov_b32 s36, 0x80000000\n" \
    "s_xor_b32 s39, s39, 12288\n" \
    "s_add_u32 s44, s44, 0x10000\n" \
    "v_xor_b32 v125, 4, v125\n" \
    "s_lshr_b32 s37, s34, 6\n" \
    "s_mul_i32 s37, s37, %[brn]\n" \
    "s_and_b32 s38, s34, %[brn]-1\n" \
    "s_add_u32 s37, s37, s38\n" \
    "s_mul_i32 s37, s37, 2621440\n" \
    "s_add_u32 s37, s37, s17\n" \
    "s_lshr_b32 s38, s37, 8\n" \
    "s_xor_b32 s38, s38, s21\n" \
    "s_and_b32 s38, s38, 0x2000\n" \
    "s_or_b32 s46, s37, s38\n" \
    "s_add_u32 s37, s37, 65536\n" \
    "s_lshr_b32 s38, s37, 8\n" \
    "s_xor_b32 s38, s38, s21\n" \
    "s_and_b32 s38, s38, 0x2000\n" \
    "s_or_b32 s47, s37, s38\n" \
    "s_add_u32 s37, s37, 65536\n" \
    "s_lshr_b32 s38, s37, 8\n" \
    "s_xor_b32 s38, s38, s21\n" \
    "s_and_b32 s38, s38, 0x2000\n" \
    "s_or_b32 s48, s37, s38\n" \
    "s_add_u32 s37, s37, 65536\n" \
    "s_lshr_b32 s38, s37, 8\n" \
    "s_xor_b32 s38, s38, s21\n" \
    "s_and_b32 s38, s38, 0x2000\n" \
    "s_or_b32 s49, s37, s38\n" \
    "s_add_u32 s37, s37, 65536\n" \
    "s_lshr_b32 s38, s37, 8\n" \
    "s_xor_b32 s38, s38, s21\n" \
    "s_and_b32 s38, s38, 0x2000\n" \
    "s_or_b32 s50, s37, s38\n" \
    "s_waitcnt lgkmcnt(0)\n" \
    "s_add_u32 s38, s32, s33\n" \
    "s_cmp_eq_u32 s34, -1\n" \
    "s_cselect_b32 s38, s38, 0\n" \
    "s_cmp_eq_u32 s38, 128\n" \
    "s_cbranch_scc1 .Lo_end_%=\n" \
    "s_add_u32 s45, s45, 1\n" \
    "s_cmp_lt_u32 s45, 0x8000\n" \
    "s_cbranch_scc1 .Lo_lO_%=\n" \
    "s_branch .Lo_end_%=\n" \
    ".Lo_lS_%=:\n" \
    "v_add_u32 v124, s39, v120\n" \
    "s_cmp_eq_u32 s34, -1\n" \
    "s_barrier\n" \
    "s_cbranch_scc1 .Lo_eS_%=\n" \
    "ds_read_b128 v[160:163], v124 offset:0\n" \
    "ds_read_b128 v[184:187], v124 offset:6144\n" \
    "ds_read_b128 v[164:167], v124 offset:1024\n" \
    "ds_read_b128 v[188:191], v124 offset:7168\n" \
    "ds_read_b128 v[168:171], v124 offset:2048\n" \
    "ds_read_b128 v[192:195], v124 offset:8192\n" \
    "ds_read_b128 v[172:175], v124 offset:3072\n" \
    "ds_read_b128 v[196:199], v124 offset:9216\n" \
    "ds_read_b128 v[176:179], v124 offset:4096\n" \
    "ds_read_b128 v[200:203], v124 offset:10240\n" \
    "ds_read_b128 v[180:183], v124 offset:5120\n" \
    "ds_read_b128 v[204:207], v124 offset:11264\n" \
    "ds_read_b32 v129, v125\n" \
    "s_waitcnt lgkmcnt(12)\n" \
    "v_mfma_f32_16x16x32_bf16 v[144:147], v[160:163], v[0:3], 0\n" \
    "s_waitcnt lgkmcnt(11)\n" \
    "v_mfma_f32_16x16x32_bf16 v[148:151], v[184:187], v[0:3], 0\n" \
    "s_waitcnt lgkmcnt(10)\n" \
    "v_mfma_f32_16x16x32_bf16 v[144:147], v[164:167], v[4:7], v[144:147]\n" \
    "s_waitcnt lgkmcnt(9)\n" \
    "v_mfma_f32_16x16x32_bf16 v[148:151], v[188:191], v[4:7], v[148:151]\n" \
    "s_waitcnt lgkmcnt(8)\n" \
    "v_mfma_f32_16x16x32_bf16 v[144:147], v[168:171], v[8:11], v[144:147]\n" \
    "s_waitcnt vmcnt(0)\n" \
    "buffer_store_dwordx4 v[132:135], v121, s[8:11], s36 offen sc1\n" \
    "s_waitcnt lgkmcnt(7)\n" \
    "v_mfma_f32_16x16x32_bf16 v[148:151], v[192:195], v[8:11], v[148:151]\n" \
    "s_waitcnt lgkmcnt(6)\n" \
    "v_mfma_f32_16x16x32_bf16 v[144:147], v[172:175], v[12:15], v[144:147]\n" \
    "s_waitcnt lgkmcnt(5)\n" \
    "v_mfma_f32_16x16x32_bf16 v[148:151], v[196:199], v[12:15], v[148:151]\n" \
    "s_waitcnt lgkmcnt(4)\n" \
    "v_mfma_f32_16x16x32_bf16 v[144:147], v[176:179], v[16:19], v[144:147]\n" \
    "s_waitcnt lgkmcnt(3)\n" \
    "v_mfma_f32_16x16x32_bf16 v[148:151], v[200:203], v[16:19], v[148:151]\n" \
    "s_waitcnt lgkmcnt(2)\n" \
    "v_mfma_f32_16x16x32_bf16 v[144:147], v[180:183], v[20:23], v[144:147]\n" \
    "s_waitcnt lgkmcnt(1)\n" \
    "v_mfma_f32_16x16x32_bf16 v[148:151], v[204:207], v[20:23], v[148:151]\n" \
    "v_mfma_f32_16x16x32_bf16 v[152:155], v[160:163], v[24:27], 0\n" \
    "v_mfma_f32_16x16x32_bf16 v[156:159], v[184:187], v[24:27], 0\n" \
    "v_mfma_f32_16x16x32_bf16 v[152:155], v[164:167], v[28:31], v[152:155]\n" \
    "v_mfma_f32_16x16x32_bf16 v[156:159], v[188:191], v[28:31], v[156:159]\n" \
    "v_mfma_f32_16x16x32_bf16 v[152:155], v[168:171], v[32:35], v[152:155]\n" \
    "v_mfma_f32_16x16x32_bf16 v[156:159], v[192:195], v[32:35], v[156:159]\n" \
    "v_mfma_f32_16x16x32_bf16 v[152:155], v[172:175], v[36:39], v[152:155]\n" \
    "v_mfma_f32_16x16x32_bf16 v[156:159], v[196:199], v[36:39], v[156:159]\n" \
    "v_cvt_pk_bf16_f32 v136, v144, v145\n" \
    "v_mfma_f32_16x16x32_bf16 v[152:155], v[176:179], v[40:43], v[152:155]\n" \
    "v_cvt_pk_bf16_f32 v137, v148, v149\n" \
    "v_mfma_f32_16x16x32_bf16 v[156:159], v[200:203], v[40:43], v[156:159]\n" \
    "v_cvt_pk_bf16_f32 v138, v146, v147\n" \
    "v_mfma_f32_16x16x32_bf16 v[152:155], v[180:183], v[44:47], v[152:155]\n" \
    "v_cvt_pk_bf16_f32 v139, v150, v151\n" \
    "v_mfma_f32_16x16x32_bf16 v[156:159], v[204:207], v[44:47], v[156:159]\n" \
    "buffer_store_dwordx4 v[136:139], v120, s[8:11], s46 offen sc1\n" \
    "v_mfma_f32_16x16x32_bf16 v[144:147], v[160:163], v[48:51], 0\n" \
    "v_mfma_f32_16x16x32_bf16 v[148:151], v[184:187], v[48:51], 0\n" \
    "v_mfma_f32_16x16x32_bf16 v[144:147], v[164:167], v[52:55], v[144:147]\n" \
    "v_mfma_f32_16x16x32_bf16 v[148:151], v[188:191], v[52:55], v[148:151]\n" \
    "v_mfma_f32_16x16x32_bf16 v[144:147], v[168:171], v[56:59], v[144:147]\n" \
    "v_mfma_f32_16x16x32_bf16 v[148:151], v[192:195], v[56:59], v[148:151]\n" \
    "v_mfma_f32_16x16x32_bf16 v[144:147], v[172:175], v[60:63], v[144:147]\n" \
    "v_mfma_f32_16x16x32_bf16 v[148:151], v[196:199], v[60:63], v[148:151]\n" \
    "v_cvt_pk_bf16_f32 v140, v152, v153\n" \
    "v_mfma_f32_16x16x32_bf16 v[144:147], v[176:179], v[64:67], v[144:147]\n" \
    "v_cvt_pk_bf16_f32 v141, v156, v157\n" \
    "v_mfma_f32_16x16x32_bf16 v[148:151], v[200:203], v[64:67], v[148:151]\n" \
    "v_cvt_pk_bf16_f32 v142, v154, v155\n" \
    "v_mfma_f32_16x16x32_bf16 v[144:147], v[180:183], v[68:71], v[144:147]\n" \
    "v_cvt_pk_bf16_f32 v143, v158, v159\n" \
    "v_mfma_f32_16x16x32_bf16 v[148:151], v[204:207], v[68:71], v[148:151]\n" \
    "buffer_store_dwordx4 v[140:143], v120, s[8:11], s47 offen sc1\n" \
    "v_mfma_f32_16x16x32_bf16 v[152:155], v[160:163], v[72:75], 0\n" \
    "v_mfma_f32_16x16x32_bf16 v[156:159], v[184:187], v[72:75], 0\n" \
    "v_mfma_f32_16x16x32_bf16 v[152:155], v[164:167], v[76:79], v[152:155]\n" \
    "v_mfma_f32_16x16x32_bf16 v[156:159], v[188:191], v[76:79], v[156:159]\n" \
    "v_mfma_f32_16x16x32_bf16 v[152:155], v[168:171], v[80:83], v[152:155]\n" \
    "v_mfma_f32_16x16x32_bf16 v[156:159], v[192:195], v[80:83], v[156:159]\n" \
    "v_mfma_f32_16x16x32_bf16 v[152:155], v[172:175], v[84:87], v[152:155]\n" \
    "v_mfma_f32_16x16x32_bf16 v[156:159], v[196:199], v[84:87], v[156:159]\n" \
    "v_cvt_pk_bf16_f32 v136, v144, v145\n" \
    "v_mfma_f32_16x16x32_bf16 v[152:155], v[176:179], v[88:91], v[152:155]\n" \
    "v_cvt_pk_bf16_f32 v137, v148, v149\n" \
    "v_mfma_f32_16x16x32_bf16 v[156:159], v[200:203], v[88:91], v[156:159]\n" \
    "v_cvt_pk_bf16_f32 v138, v146, v147\n" \
    "v_mfma_f32_16x16x32_bf16 v[152:155], v[180:183], v[92:95], v[152:155]\n" \
    "v_cvt_pk_bf16_f32 v139, v150, v151\n" \
    "v_mfma_f32_16x16x32_bf16 v[156:159], v[204:207], v[92:95], v[156:159]\n" \
    "buffer_store_dwordx4 v[136:139], v120, s[8:11], s48 offen sc1\n" \
    "v_mfma_f32_16x16x32_bf16 v[144:147], v[160:163], v[96:99], 0\n" \
    "v_mfma_f32_16x16x32_bf16 v[148:151], v[184:187], v[96:99], 0\n" \
    "v_mfma_f32_16x16x32_bf16 v[144:147], v[164:167], v[100:103], v[144:147]\n" \
    "buffer_load_dword v126, v122, s[8:11], 0 offen sc1\n" \
    "v_mfma_f32_16x16x32_bf16 v[148:151], v[188:191], v[100:103], v[148:151]\n" \
    "v_mfma_f32_16x16x32_bf16 v[144:147], v[168:171], v[104:107], v[144:147]\n" \
    "v_mfma_f32_16x16x32_bf16 v[148:151], v[192:195], v[104:107], v[148:151]\n" \
    "v_mfma_f32_16x16x32_bf16 v[144:147], v[172:175], v[108:111], v[144:147]\n" \
    "v_mfma_f32_16x16x32_bf16 v[148:151], v[196:199], v[108:111], v[148:151]\n" \
    "v_cvt_pk_bf16_f32 v140, v152, v153\n" \
    "v_mfma_f32_16x16x32_bf16 v[144:147], v[176:179], v[112:115], v[144:147]\n" \
    "v_cvt_pk_bf16_f32 v141, v156, v157\n" \
    "v_mfma_f32_16x16x32_bf16 v[148:151], v[200:203], v[112:115], v[148:151]\n" \
    "v_cvt_pk_bf16_f32 v142, v154, v155\n" \
    "v_mfma_f32_16x16x32_bf16 v[144:147], v[180:183], v[116:119], v[144:147]\n" \
    "v_cvt_pk_bf16_f32 v143, v158, v159\n" \
    "v_mfma_f32_16x16x32_bf16 v[148:151], v[204:207], v[116:119], v[148:151]\n" \
    "buffer_store_dwordx4 v[140:143], v120, s[8:11], s49 offen sc1\n" \
    "s_lshr_b32 s36, s34, 6\n" \
    "s_lshl_b32 s36, s36, 15\n" \
    "s_add_u32 s36, s36, s18\n" \
    "s_and_b32 s37, s34, 63\n" \
    "s_add_u32 s37, s37, 1\n" \
    "v_mov_b32 v132, s37\n" \
    "v_cvt_pk_bf16_f32 v136, v144, v145\n" \
    "v_cvt_pk_bf16_f32 v137, v148, v149\n" \
    "v_cvt_pk_bf16_f32 v138, v146, v147\n" \
    "v_cvt_pk_bf16_f32 v139, v150, v151\n" \
    "buffer_store_dwordx4 v[136:139], v120, s[8:11], s50 offen sc1\n" \
    "s_waitcnt lgkmcnt(0)\n" \
    "v_readfirstlane_b32 s42, v129\n" \
    "s_nop 0\n" \
    "s_bfe_u32 s43, s42, 0x80008\n" \
    "s_and_b32 s42, s42, 255\n" \
    "s_add_u32 s37, s33, %[brn]\n" \
    "s_min_u32 s42, s42, s37\n" \
    "s_cmp_lt_u32 s32, s42\n" \
    "s_cselect_b32 s34, s32, -1\n" \
    "s_add_u32 s37, s33, 64\n" \
    "s_cmp_lt_u32 s33, s43\n" \
    "s_cselect_b32 s34, s37, s34\n" \
    "s_addc_u32 s33, s33, 0\n" \
    "s_cmp_lt_u32 s34, 64\n" \
    "s_addc_u32 s32, s32, 0\n" \
    "s_mov_b32 s45, 0\n" \
    "s_xor_b32 s39, s39, 12288\n" \
    "s_add_u32 s44, s44, 0x10000\n" \
    "v_xor_b32 v125, 4, v125\n" \
    "s_lshr_b32 s37, s34, 6\n" \
    "s_mul_i32 s37, s37, %[brn]\n" \
    "s_and_b32 s38, s34, %[brn]-1\n" \
    "s_add_u32 s37, s37, s38\n" \
    "s_mul_i32 s37, s37, 2621440\n" \
    "s_add_u32 s37, s37, s17\n" \
    "s_lshr_b32 s38, s37, 8\n" \
    "s_xor_b32 s38, s38, s21\n" \
    "s_and_b32 s38, s38, 0x2000\n" \
    "s_or_b32 s46, s37, s38\n" \
    "s_add_u32 s37, s37, 65536\n" \
    "s_lshr_b32 s38, s37, 8\n" \
    "s_xor_b32 s38, s38, s21\n" \
    "s_and_b32 s38, s38, 0x2000\n" \
    "s_or_b32 s47, s37, s38\n" \
    "s_add_u32 s37, s37, 65536\n" \
    "s_lshr_b32 s38, s37, 8\n" \
    "s_xor_b32 s38, s38, s21\n" \
    "s_and_b32 s38, s38, 0x2000\n" \
    "s_or_b32 s48, s37, s38\n" \
    "s_add_u32 s37, s37, 65536\n" \
    "s_lshr_b32 s38, s37, 8\n" \
    "s_xor_b32 s38, s38, s21\n" \
    "s_and_b32 s38, s38, 0x2000\n" \
    "s_or_b32 s49, s37, s38\n" \
    "s_add_u32 s37, s37, 65536\n" \
    "s_lshr_b32 s38, s37, 8\n" \
    "s_xor_b32 s38, s38, s21\n" \
    "s_and_b32 s38, s38, 0x2000\n" \
    "s_or_b32 s50, s37, s38\n" \
    "s_waitcnt vmcnt(2)\n" \
    "v_min_u32_dpp v126, v126, v126 quad_perm:[1,0,3,2] row_mask:0xf bank_mask:0xf bound_ctrl:1\n" \
    "s_nop 1\n" \
    "v_min_u32_dpp v126, v126, v126 quad_perm:[2,3,0,1] row_mask:0xf bank_mask:0xf bound_ctrl:1\n" \
    "s_nop 1\n" \
    "v_min_u32_dpp v126, v126, v126 row_half_mirror row_mask:0xf bank_mask:0xf bound_ctrl:1\n" \
    "s_nop 0\n" \
    "v_readlane_b32 s40, v126, 0\n" \
    "v_readlane_b32 s37, v126, 16\n" \
    "v_readlane_b32 s41, v126, 8\n" \
    "s_max_u32 s38, s40, %[hs]+%[hn]\n" \
    "s_cmp_lt_u32 s37, %[hs]+%[hn]\n" \
    "s_cselect_b32 s37, s37, s38\n" \
    "s_max_u32 s37, s37, %[hs]\n" \
    "s_cmp_ge_u32 s40, %[hs]\n" \
    "s_cselect_b32 s40, s37, s40\n" \
    "s_lshl_b32 s41, s41, 8\n" \
    "s_or_b32 s42, s40, s41\n" \
    "s_or_b32 s42, s42, s44\n" \
    "v_mov_b32 v128, s42\n" \
    "ds_write_b32 v125, v128\n" \
    "s_waitcnt lgkmcnt(0)\n" \
    "s_branch .Lo_lS_%=\n" \
    ".Lo_eS_%=:\n" \
    "ds_read_b32 v129, v125\n" \
    "s_waitcnt lgkmcnt(0)\n" \
    "v_readfirstlane_b32 s42, v129\n" \
    "s_nop 0\n" \
    "s_bfe_u32 s43, s42, 0x80008\n" \
    "s_and_b32 s42, s42, 255\n" \
    "s_add_u32 s37, s33, %[brn]\n" \
    "s_min_u32 s42, s42, s37\n" \
    "s_cmp_lt_u32 s32, s42\n" \
    "s_cselect_b32 s34, s32, -1\n" \
    "s_add_u32 s37, s33, 64\n" \
    "s_cmp_lt_u32 s33, s43\n" \
    "s_cselect_b32 s34, s37, s34\n" \
    "s_addc_u32 s33, s33, 0\n" \
    "s_cmp_lt_u32 s34, 64\n" \
    "s_addc_u32 s32, s32, 0\n" \
    "s_cmp_lg_u32 s34, -1\n" \
    "s_cbranch_scc1 .Lo_ns_%=\n" \
    "s_sleep 2\n" \
    ".Lo_ns_%=:\n" \
    "buffer_load_dword v126, v122, s[8:11], 0 offen sc1\n" \
    "s_waitcnt vmcnt(1)\n" \
    "buffer_store_dwordx4 v[132:135], v121, s[8:11], s36 offen sc1\n" \
    "s_mov_b32 s36, 0x80000000\n" \
    "s_xor_b32 s39, s39, 12288\n" \
    "s_add_u32 s44, s44, 0x10000\n" \
    "v_xor_b32 v125, 4, v125\n" \
    "s_lshr_b32 s37, s34, 6\n" \
    "s_mul_i32 s37, s37, %[brn]\n" \
    "s_and_b32 s38, s34, %[brn]-1\n" \
    "s_add_u32 s37, s37, s38\n" \
    "s_mul_i32 s37, s37, 2621440\n" \
    "s_add_u32 s37, s37, s17\n" \
    "s_lshr_b32 s38, s37, 8\n" \
    "s_xor_b32 s38, s38, s21\n" \
    "s_and_b32 s38, s38, 0x2000\n" \
    "s_or_b32 s46, s37, s38\n" \
    "s_add_u32 s37, s37, 65536\n" \
    "s_lshr_b32 s38, s37, 8\n" \
    "s_xor_b32 s38, s38, s21\n" \
    "s_and_b32 s38, s38, 0x2000\n" \
    "s_or_b32 s47, s37, s38\n" \
    "s_add_u32 s37, s37, 65536\n" \
    "s_lshr_b32 s38, s37, 8\n" \
    "s_xor_b32 s38, s38, s21\n" \
    "s_and_b32 s38, s38, 0x2000\n" \
    "s_or_b32 s48, s37, s38\n" \
    "s_add_u32 s37, s37, 65536\n" \
    "s_lshr_b32 s38, s37, 8\n" \
    "s_xor_b32 s38, s38, s21\n" \
    "s_and_b32 s38, s38, 0x2000\n" \
    "s_or_b32 s49, s37, s38\n" \
    "s_add_u32 s37, s37, 65536\n" \
    "s_lshr_b32 s38, s37, 8\n" \
    "s_xor_b32 s38, s38, s21\n" \
    "s_and_b32 s38, s38, 0x2000\n" \
    "s_or_b32 s50, s37, s38\n" \
    "s_waitcnt vmcnt(1)\n" \
    "v_min_u32_dpp v126, v126, v126 quad_perm:[1,0,3,2] row_mask:0xf bank_mask:0xf bound_ctrl:1\n" \
    "s_nop 1\n" \
    "v_min_u32_dpp v126, v126, v126 quad_perm:[2,3,0,1] row_mask:0xf bank_mask:0xf bound_ctrl:1\n" \
    "s_nop 1\n" \
    "v_min_u32_dpp v126, v126, v126 row_half_mirror row_mask:0xf bank_mask:0xf bound_ctrl:1\n" \
    "s_nop 0\n" \
    "v_readlane_b32 s40, v126, 0\n" \
    "v_readlane_b32 s37, v126, 16\n" \
    "v_readlane_b32 s41, v126, 8\n" \
    "s_max_u32 s38, s40, %[hs]+%[hn]\n" \
    "s_cmp_lt_u32 s37, %[hs]+%[hn]\n" \
    "s_cselect_b32 s37, s37, s38\n" \
    "s_max_u32 s37, s37, %[hs]\n" \
    "s_cmp_ge_u32 s40, %[hs]\n" \
    "s_cselect_b32 s40, s37, s40\n" \
    "s_lshl_b32 s41, s41, 8\n" \
    "s_or_b32 s42, s40, s41\n" \
    "s_or_b32 s42, s42, s44\n" \
    "v_mov_b32 v128, s42\n" \
    "ds_write_b32 v125, v128\n" \
    "s_waitcnt lgkmcnt(0)\n" \
    "s_add_u32 s38, s32, s33\n" \
    "s_cmp_eq_u32 s34, -1\n" \
    "s_cselect_b32 s38, s38, 0\n" \
    "s_cmp_eq_u32 s38, 128\n" \
    "s_cbranch_scc1 .Lo_end_%=\n" \
    "s_add_u32 s45, s45, 1\n" \
    "s_cmp_lt_u32 s45, 0x8000\n" \
    "s_cbranch_scc1 .Lo_lS_%=\n" \
    "s_branch .Lo_end_%=\n" \
    ".Lo_end_%=:\n" \
    "s_waitcnt vmcnt(0)\n" \
    "s_endpgm\n" \
    ""
#define UP_ASM \
    "s_mov_b32 s32, 0\n" \
    "s_mov_b32 s38, 0x40000\n" \
    "s_mov_b32 s44, 0xaaaaaaaa\n" \
    "s_mov_b32 s45, 0xaaaaaaaa\n" \
    "s_mov_b32 s46, 0xcccccccc\n" \
    "s_mov_b32 s47, 0xcccccccc\n" \
    "v_mov_b32 v182, -1\n" \
    "v_mov_b32 v183, -1\n" \
    "v_mov_b32 v178, 0x3f803f80\n" \
    "v_mov_b32 v179, 0x3f803f80\n" \
    "v_mov_b32 v180, 0x3f803f80\n" \
    "v_mov_b32 v181, 0x3f803f80\n" \
    "s_cmp_lt_u32 s16, 4\n" \
    "s_cbranch_scc1 .Lup_lO_%=\n" \
    ".Lup_lY_%=:\n" \
    "s_mov_b32 s33, 0\n" \
    "s_mov_b32 s42, 0\n" \
    ".Lup_kY_%=:\n" \
    "s_waitcnt lgkmcnt(0)\n" \
    "s_barrier\n" \
    "ds_read_b128 v[184:187], v168 offset:0\n" \
    "ds_read_b128 v[188:191], v168 offset:1024\n" \
    "ds_read_b128 v[192:195], v168 offset:2048\n" \
    "ds_read_b128 v[196:199], v168 offset:3072\n" \
    "ds_read_b128 v[200:203], v168 offset:4096\n" \
    "ds_read_b64 v[248:249], v172 offset:0\n" \
    "s_waitcnt lgkmcnt(5)\n" \
    "v_mfma_f32_16x16x32_bf16 v[208:211], v[0:3], v[184:187], 0\n" \
    "ds_read_b128 v[204:207], v168 offset:5120\n" \
    "v_mfma_f32_16x16x32_bf16 v[212:215], v[80:83], v[184:187], 0\n" \
    "s_waitcnt lgkmcnt(5)\n" \
    "v_mfma_f32_16x16x32_bf16 v[208:211], v[4:7], v[188:191], v[208:211]\n" \
    "ds_read_b128 v[184:187], v168 offset:6144\n" \
    "v_mfma_f32_16x16x32_bf16 v[212:215], v[84:87], v[188:191], v[212:215]\n" \
    "s_mul_i32 s39, s32, 98304\n" \
    "s_add_u32 s39, s39, 0\n" \
    "s_waitcnt lgkmcnt(5)\n" \
    "v_mfma_f32_16x16x32_bf16 v[208:211], v[8:11], v[192:195], v[208:211]\n" \
    "ds_read_b128 v[188:191], v168 offset:7168\n" \
    "v_mfma_f32_16x16x32_bf16 v[212:215], v[88:91], v[192:195], v[212:215]\n" \
    "s_add_u32 s40, s39, %[pdist]*98304\n" \
    "s_cmp_lt_u32 s32, 64-%[pdist]\n" \
    "s_cselect_b32 s40, s40, 0x80000000\n" \
    "s_waitcnt lgkmcnt(5)\n" \
    "v_mfma_f32_16x16x32_bf16 v[208:211], v[12:15], v[196:199], v[208:211]\n" \
    "ds_read_b128 v[192:195], v168 offset:8192\n" \
    "v_mfma_f32_16x16x32_bf16 v[212:215], v[92:95], v[196:199], v[212:215]\n" \
    "s_waitcnt lgkmcnt(5)\n" \
    "v_mfma_f32_16x16x32_bf16 v[208:211], v[16:19], v[200:203], v[208:211]\n" \
    "ds_read_b128 v[196:199], v168 offset:9216\n" \
    "v_mfma_f32_16x16x32_bf16 v[212:215], v[96:99], v[200:203], v[212:215]\n" \
    "s_waitcnt lgkmcnt(4)\n" \
    "v_mfma_f32_16x16x32_bf16 v[208:211], v[20:23], v[204:207], v[208:211]\n" \
    "ds_read_b128 v[200:203], v168 offset:10240\n" \
    "v_mfma_f32_16x16x32_bf16 v[212:215], v[100:103], v[204:207], v[212:215]\n" \
    "v_permlane16_swap_b32 v248, v249\n" \
    "s_waitcnt lgkmcnt(4)\n" \
    "v_mfma_f32_16x16x32_bf16 v[208:211], v[24:27], v[184:187], v[208:211]\n" \
    "ds_read_b128 v[204:207], v168 offset:11264\n" \
    "v_mfma_f32_16x16x32_bf16 v[212:215], v[104:107], v[184:187], v[212:215]\n" \
    "v_add_f32 v250, v248, v249\n" \
    "s_waitcnt lgkmcnt(4)\n" \
    "v_mfma_f32_16x16x32_bf16 v[208:211], v[28:31], v[188:191], v[208:211]\n" \
    "ds_read_b128 v[184:187], v168 offset:12288\n" \
    "v_mfma_f32_16x16x32_bf16 v[212:215], v[108:111], v[188:191], v[212:215]\n" \
    "v_add_f32 v248, v248, v249\n" \
    "s_waitcnt lgkmcnt(4)\n" \
    "v_mfma_f32_16x16x32_bf16 v[208:211], v[32:35], v[192:195], v[208:211]\n" \
    "ds_read_b128 v[188:191], v168 offset:13312\n" \
    "v_mfma_f32_16x16x32_bf16 v[212:215], v[112:115], v[192:195], v[212:215]\n" \
    "v_permlane32_swap_b32 v248, v250\n" \
    "s_waitcnt lgkmcnt(4)\n" \
    "v_mfma_f32_16x16x32_bf16 v[208:211], v[36:39], v[196:199], v[208:211]\n" \
    "ds_read_b128 v[192:195], v168 offset:14336\n" \
    "v_mfma_f32_16x16x32_bf16 v[212:215], v[116:119], v[196:199], v[212:215]\n" \
    "v_add_f32 v249, v248, v250\n" \
    "s_waitcnt lgkmcnt(4)\n" \
    "v_mfma_f32_16x16x32_bf16 v[208:211], v[40:43], v[200:203], v[208:211]\n" \
    "ds_read_b128 v[196:199], v168 offset:15360\n" \
    "v_mfma_f32_16x16x32_bf16 v[212:215], v[120:123], v[200:203], v[212:215]\n" \
    "v_add_f32 v248, v248, v250\n" \
    "s_waitcnt lgkmcnt(4)\n" \
    "v_mfma_f32_16x16x32_bf16 v[208:211], v[44:47], v[204:207], v[208:211]\n" \
    "ds_read_b128 v[200:203], v168 offset:16384\n" \
    "v_mfma_f32_16x16x32_bf16 v[212:215], v[124:127], v[204:207], v[212:215]\n" \
    "v_permlane16_swap_b32 v248, v249\n" \
    "s_waitcnt lgkmcnt(4)\n" \
    "v_mfma_f32_16x16x32_bf16 v[208:211], v[48:51], v[184:187], v[208:211]\n" \
    "ds_read_b128 v[204:207], v168 offset:17408\n" \
    "v_mfma_f32_16x16x32_bf16 v[212:215], v[128:131], v[184:187], v[212:215]\n" \
    "v_mul_f32 v252, 0x3acccccd, v248\n" \
    "s_waitcnt lgkmcnt(4)\n" \
    "v_mfma_f32_16x16x32_bf16 v[208:211], v[52:55], v[188:191], v[208:211]\n" \
    "ds_read_b128 v[184:187], v168 offset:18432\n" \
    "v_mfma_f32_16x16x32_bf16 v[212:215], v[132:135], v[188:191], v[212:215]\n" \
    "v_mul_f32 v249, 0x3acccccd, v249\n" \
    "s_waitcnt lgkmcnt(4)\n" \
    "v_mfma_f32_16x16x32_bf16 v[208:211], v[56:59], v[192:195], v[208:211]\n" \
    "ds_read_b128 v[188:191], v168 offset:19456\n" \
    "v_mfma_f32_16x16x32_bf16 v[212:215], v[136:139], v[192:195], v[212:215]\n" \
    "v_fma_f32 v249, -v252, v252, v249\n" \
    "s_waitcnt lgkmcnt(4)\n" \
    "v_mfma_f32_16x16x32_bf16 v[208:211], v[60:63], v[196:199], v[208:211]\n" \
    "v_mfma_f32_16x16x32_bf16 v[212:215], v[140:143], v[196:199], v[212:215]\n" \
    "v_max_f32 v249, 0, v249\n" \
    "s_waitcnt lgkmcnt(3)\n" \
    "v_mfma_f32_16x16x32_bf16 v[208:211], v[64:67], v[200:203], v[208:211]\n" \
    "v_mfma_f32_16x16x32_bf16 v[212:215], v[144:147], v[200:203], v[212:215]\n" \
    "v_add_f32 v249, 0x3727c5ac, v249\n" \
    "s_waitcnt lgkmcnt(2)\n" \
    "v_mfma_f32_16x16x32_bf16 v[208:211], v[68:71], v[204:207], v[208:211]\n" \
    "v_mfma_f32_16x16x32_bf16 v[212:215], v[148:151], v[204:207], v[212:215]\n" \
    "v_rsq_f32 v253, v249\n" \
    "s_waitcnt lgkmcnt(1)\n" \
    "v_mfma_f32_16x16x32_bf16 v[208:211], v[72:75], v[184:187], v[208:211]\n" \
    "v_mfma_f32_16x16x32_bf16 v[212:215], v[152:155], v[184:187], v[212:215]\n" \
    "v_mul_f32 v251, 0xbfb8aa3b, v253\n" \
    "s_waitcnt lgkmcnt(0)\n" \
    "v_mfma_f32_16x16x32_bf16 v[208:211], v[76:79], v[188:191], v[208:211]\n" \
    "v_mfma_f32_16x16x32_bf16 v[212:215], v[156:159], v[188:191], v[212:215]\n" \
    "s_barrier\n" \
    "s_nop 5\n" \
    "v_fma_f32 v208, -v252, v160, v208\n" \
    "v_fma_f32 v209, -v252, v161, v209\n" \
    "v_fma_f32 v210, -v252, v162, v210\n" \
    "v_fma_f32 v211, -v252, v163, v211\n" \
    "v_mul_f32 v208, v251, v208\n" \
    "v_mul_f32 v209, v251, v209\n" \
    "v_mul_f32 v210, v251, v210\n" \
    "v_mul_f32 v211, v251, v211\n" \
    "v_exp_f32 v208, v208\n" \
    "v_fma_f32 v212, -v252, v164, v212\n" \
    "v_exp_f32 v209, v209\n" \
    "v_fma_f32 v213, -v252, v165, v213\n" \
    "v_exp_f32 v210, v210\n" \
    "v_fma_f32 v214, -v252, v166, v214\n" \
    "v_exp_f32 v211, v211\n" \
    "v_fma_f32 v215, -v252, v167, v215\n" \
    "v_add_f32 v208, 1.0, v208\n" \
    "v_add_f32 v209, 1.0, v209\n" \
    "v_add_f32 v210, 1.0, v210\n" \
    "v_add_f32 v211, 1.0, v211\n" \
    "v_rcp_f32 v208, v208\n" \
    "v_mul_f32 v212, v253, v212\n" \
    "v_rcp_f32 v209, v209\n" \
    "v_mul_f32 v213, v253, v213\n" \
    "v_rcp_f32 v210, v210\n" \
    "v_mul_f32 v214, v253, v214\n" \
    "v_rcp_f32 v211, v211\n" \
    "v_mul_f32 v215, v253, v215\n" \
    "v_mul_f32 v208, v208, v212\n" \
    "v_mul_f32 v209, v209, v213\n" \
    "v_mul_f32 v210, v210, v214\n" \
    "v_mul_f32 v211, v211, v215\n" \
    "v_cvt_pk_bf16_f32 v236, v208, v209\n" \
    "v_cvt_pk_bf16_f32 v237, v210, v211\n" \
    "v_min_u32 v236, -2, v236\n" \
    "v_min_u32 v237, -2, v237\n" \
    "buffer_store_dwordx2 v[236:237], v173, s[8:11], s39 offen sc1\n" \
    "buffer_store_dwordx2 v[182:183], v173, s[8:11], s40 offen sc1\n" \
    "s_add_u32 s32, s32, 1\n" \
    "s_cmp_lt_u32 s32, %[eup]\n" \
    "s_cbranch_scc1 .Lup_kY_%=\n" \
    "s_mul_i32 s41, s33, 40960\n" \
    "s_add_u32 s41, s41, s17\n" \
    "s_add_u32 s41, s41, 2621440\n" \
    "buffer_load_dwordx4 v[216:219], v168, s[8:11], s41 offen sc1\n" \
    "s_add_u32 s41, s41, 4096\n" \
    "buffer_load_dwordx4 v[220:223], v168, s[8:11], s41 offen sc1\n" \
    "s_add_u32 s41, s41, 4096\n" \
    "buffer_load_dwordx4 v[224:227], v168, s[8:11], s41 offen sc1\n" \
    "s_add_u32 s41, s41, 4096\n" \
    "buffer_load_dwordx4 v[228:231], v168, s[8:11], s41 offen sc1\n" \
    "s_add_u32 s41, s41, 4096\n" \
    "buffer_load_dwordx4 v[232:235], v168, s[8:11], s41 offen sc1\n" \
    "s_waitcnt vmcnt(0)\n" \
    ".Lup_mY_%=:\n" \
    "s_waitcnt lgkmcnt(0)\n" \
    "s_barrier\n" \
    "s_waitcnt vmcnt(4)\n" \
    "s_mov_b32 s37, 0\n" \
    "s_setprio 1\n" \
    ".Lup_srYc_%=:\n" \
    "v_mfma_f32_16x16x32_bf16 v[244:247], v[216:219], v[216:219], 0\n" \
    "ds_write_b128 v169, v[216:219] offset:20480\n" \
    "v_max3_u32 v254, v216, v217, v218\n" \
    "v_mfma_f32_16x16x32_bf16 v[240:243], v[178:181], v[216:219], 0\n" \
    "v_max_u32 v254, v254, v219\n" \
    "v_mfma_f32_16x16x32_bf16 v[244:247], v[220:223], v[220:223], v[244:247]\n" \
    "ds_write_b128 v169, v[220:223] offset:24576\n" \
    "v_max3_u32 v254, v254, v220, v221\n" \
    "v_mfma_f32_16x16x32_bf16 v[240:243], v[178:181], v[220:223], v[240:243]\n" \
    "v_max3_u32 v254, v254, v222, v223\n" \
    "v_mfma_f32_16x16x32_bf16 v[244:247], v[224:227], v[224:227], v[244:247]\n" \
    "ds_write_b128 v169, v[224:227] offset:28672\n" \
    "v_max3_u32 v254, v254, v224, v225\n" \
    "v_mfma_f32_16x16x32_bf16 v[240:243], v[178:181], v[224:227], v[240:243]\n" \
    "v_max3_u32 v254, v254, v226, v227\n" \
    "v_mfma_f32_16x16x32_bf16 v[244:247], v[228:231], v[228:231], v[244:247]\n" \
    "ds_write_b128 v169, v[228:231] offset:32768\n" \
    "v_max3_u32 v254, v254, v228, v229\n" \
    "v_mfma_f32_16x16x32_bf16 v[240:243], v[178:181], v[228:231], v[240:243]\n" \
    "v_max3_u32 v254, v254, v230, v231\n" \
    "v_mfma_f32_16x16x32_bf16 v[244:247], v[232:235], v[232:235], v[244:247]\n" \
    "ds_write_b128 v169, v[232:235] offset:36864\n" \
    "v_max3_u32 v254, v254, v232, v233\n" \
    "v_mfma_f32_16x16x32_bf16 v[240:243], v[178:181], v[232:235], v[240:243]\n" \
    "v_max3_u32 v254, v254, v234, v235\n" \
    "v_cmp_eq_u32 vcc, -1, v254\n" \
    "s_nop 0\n" \
    "s_cbranch_vccnz .Lup_scYc_%=\n" \
    ".Lup_sxYc_%=:\n" \
    "s_nop 0\n" \
    "v_cndmask_b32 v254, v244, v245, s[44:45]\n" \
    "v_cndmask_b32 v255, v246, v247, s[44:45]\n" \
    "s_nop 0\n" \
    "v_cndmask_b32 v241, v254, v255, s[46:47]\n" \
    "ds_write_b64 v171, v[240:241] offset:1024\n" \
    "s_setprio 0\n" \
    "s_add_u32 s36, s33, 1\n" \
    "s_waitcnt lgkmcnt(0)\n" \
    "s_mul_i32 s41, s36, 40960\n" \
    "s_add_u32 s41, s41, s17\n" \
    "s_add_u32 s41, s41, 2621440\n" \
    "buffer_load_dwordx4 v[216:219], v168, s[8:11], s41 offen sc1\n" \
    "s_add_u32 s41, s41, 4096\n" \
    "buffer_load_dwordx4 v[220:223], v168, s[8:11], s41 offen sc1\n" \
    "s_add_u32 s41, s41, 4096\n" \
    "buffer_load_dwordx4 v[224:227], v168, s[8:11], s41 offen sc1\n" \
    "s_add_u32 s41, s41, 4096\n" \
    "buffer_load_dwordx4 v[228:231], v168, s[8:11], s41 offen sc1\n" \
    "s_add_u32 s41, s41, 4096\n" \
    "buffer_load_dwordx4 v[232:235], v168, s[8:11], s41 offen sc1\n" \
    "ds_read_b128 v[184:187], v168 offset:0\n" \
    "ds_read_b128 v[188:191], v168 offset:1024\n" \
    "ds_read_b128 v[192:195], v168 offset:2048\n" \
    "ds_read_b128 v[196:199], v168 offset:3072\n" \
    "ds_read_b128 v[200:203], v168 offset:4096\n" \
    "ds_read_b64 v[248:249], v172 offset:0\n" \
    "s_waitcnt lgkmcnt(5)\n" \
    "v_mfma_f32_16x16x32_bf16 v[208:211], v[0:3], v[184:187], 0\n" \
    "ds_read_b128 v[204:207], v168 offset:5120\n" \
    "v_mfma_f32_16x16x32_bf16 v[212:215], v[80:83], v[184:187], 0\n" \
    "s_waitcnt lgkmcnt(5)\n" \
    "v_mfma_f32_16x16x32_bf16 v[208:211], v[4:7], v[188:191], v[208:211]\n" \
    "ds_read_b128 v[184:187], v168 offset:6144\n" \
    "v_mfma_f32_16x16x32_bf16 v[212:215], v[84:87], v[188:191], v[212:215]\n" \
    "s_mul_i32 s39, s32, 98304\n" \
    "s_add_u32 s39, s39, 0\n" \
    "s_waitcnt lgkmcnt(5)\n" \
    "v_mfma_f32_16x16x32_bf16 v[208:211], v[8:11], v[192:195], v[208:211]\n" \
    "ds_read_b128 v[188:191], v168 offset:7168\n" \
    "v_mfma_f32_16x16x32_bf16 v[212:215], v[88:91], v[192:195], v[212:215]\n" \
    "s_add_u32 s40, s39, %[pdist]*98304\n" \
    "s_cmp_lt_u32 s32, 64-%[pdist]\n" \
    "s_cselect_b32 s40, s40, 0x80000000\n" \
    "s_waitcnt lgkmcnt(5)\n" \
    "v_mfma_f32_16x16x32_bf16 v[208:211], v[12:15], v[196:199], v[208:211]\n" \
    "ds_read_b128 v[192:195], v168 offset:8192\n" \
    "v_mfma_f32_16x16x32_bf16 v[212:215], v[92:95], v[196:199], v[212:215]\n" \
    "s_waitcnt lgkmcnt(5)\n" \
    "v_mfma_f32_16x16x32_bf16 v[208:211], v[16:19], v[200:203], v[208:211]\n" \
    "ds_read_b128 v[196:199], v168 offset:9216\n" \
    "v_mfma_f32_16x16x32_bf16 v[212:215], v[96:99], v[200:203], v[212:215]\n" \
    "s_waitcnt lgkmcnt(4)\n" \
    "v_mfma_f32_16x16x32_bf16 v[208:211], v[20:23], v[204:207], v[208:211]\n" \
    "ds_read_b128 v[200:203], v168 offset:10240\n" \
    "v_mfma_f32_16x16x32_bf16 v[212:215], v[100:103], v[204:207], v[212:215]\n" \
    "v_permlane16_swap_b32 v248, v249\n" \
    "s_waitcnt lgkmcnt(4)\n" \
    "v_mfma_f32_16x16x32_bf16 v[208:211], v[24:27], v[184:187], v[208:211]\n" \
    "ds_read_b128 v[204:207], v168 offset:11264\n" \
    "v_mfma_f32_16x16x32_bf16 v[212:215], v[104:107], v[184:187], v[212:215]\n" \
    "v_add_f32 v250, v248, v249\n" \
    "s_waitcnt lgkmcnt(4)\n" \
    "v_mfma_f32_16x16x32_bf16 v[208:211], v[28:31], v[188:191], v[208:211]\n" \
    "ds_read_b128 v[184:187], v168 offset:12288\n" \
    "v_mfma_f32_16x16x32_bf16 v[212:215], v[108:111], v[188:191], v[212:215]\n" \
    "v_add_f32 v248, v248, v249\n" \
    "s_waitcnt lgkmcnt(4)\n" \
    "v_mfma_f32_16x16x32_bf16 v[208:211], v[32:35], v[192:195], v[208:211]\n" \
    "ds_read_b128 v[188:191], v168 offset:13312\n" \
    "v_mfma_f32_16x16x32_bf16 v[212:215], v[112:115], v[192:195], v[212:215]\n" \
    "v_permlane32_swap_b32 v248, v250\n" \
    "s_waitcnt lgkmcnt(4)\n" \
    "v_mfma_f32_16x16x32_bf16 v[208:211], v[36:39], v[196:199], v[208:211]\n" \
    "ds_read_b128 v[192:195], v168 offset:14336\n" \
    "v_mfma_f32_16x16x32_bf16 v[212:215], v[116:119], v[196:199], v[212:215]\n" \
    "v_add_f32 v249, v248, v250\n" \
    "s_waitcnt lgkmcnt(4)\n" \
    "v_mfma_f32_16x16x32_bf16 v[208:211], v[40:43], v[200:203], v[208:211]\n" \
    "ds_read_b128 v[196:199], v168 offset:15360\n" \
    "v_mfma_f32_16x16x32_bf16 v[212:215], v[120:123], v[200:203], v[212:215]\n" \
    "v_add_f32 v248, v248, v250\n" \
    "s_waitcnt lgkmcnt(4)\n" \
    "v_mfma_f32_16x16x32_bf16 v[208:211], v[44:47], v[204:207], v[208:211]\n" \
    "ds_read_b128 v[200:203], v168 offset:16384\n" \
    "v_mfma_f32_16x16x32_bf16 v[212:215], v[124:127], v[204:207], v[212:215]\n" \
    "v_permlane16_swap_b32 v248, v249\n" \
    "s_waitcnt lgkmcnt(4)\n" \
    "v_mfma_f32_16x16x32_bf16 v[208:211], v[48:51], v[184:187], v[208:211]\n" \
    "ds_read_b128 v[204:207], v168 offset:17408\n" \
    "v_mfma_f32_16x16x32_bf16 v[212:215], v[128:131], v[184:187], v[212:215]\n" \
    "v_mul_f32 v252, 0x3acccccd, v248\n" \
    "s_waitcnt lgkmcnt(4)\n" \
    "v_mfma_f32_16x16x32_bf16 v[208:211], v[52:55], v[188:191], v[208:211]\n" \
    "ds_read_b128 v[184:187], v168 offset:18432\n" \
    "v_mfma_f32_16x16x32_bf16 v[212:215], v[132:135], v[188:191], v[212:215]\n" \
    "v_mul_f32 v249, 0x3acccccd, v249\n" \
    "s_waitcnt lgkmcnt(4)\n" \
    "v_mfma_f32_16x16x32_bf16 v[208:211], v[56:59], v[192:195], v[208:211]\n" \
    "ds_read_b128 v[188:191], v168 offset:19456\n" \
    "v_mfma_f32_16x16x32_bf16 v[212:215], v[136:139], v[192:195], v[212:215]\n" \
    "v_fma_f32 v249, -v252, v252, v249\n" \
    "s_waitcnt lgkmcnt(4)\n" \
    "v_mfma_f32_16x16x32_bf16 v[208:211], v[60:63], v[196:199], v[208:211]\n" \
    "v_mfma_f32_16x16x32_bf16 v[212:215], v[140:143], v[196:199], v[212:215]\n" \
    "v_max_f32 v249, 0, v249\n" \
    "s_waitcnt lgkmcnt(3)\n" \
    "v_mfma_f32_16x16x32_bf16 v[208:211], v[64:67], v[200:203], v[208:211]\n" \
    "v_mfma_f32_16x16x32_bf16 v[212:215], v[144:147], v[200:203], v[212:215]\n" \
    "v_add_f32 v249, 0x3727c5ac, v249\n" \
    "s_waitcnt lgkmcnt(2)\n" \
    "v_mfma_f32_16x16x32_bf16 v[208:211], v[68:71], v[204:207], v[208:211]\n" \
    "v_mfma_f32_16x16x32_bf16 v[212:215], v[148:151], v[204:207], v[212:215]\n" \
    "v_rsq_f32 v253, v249\n" \
    "s_waitcnt lgkmcnt(1)\n" \
    "v_mfma_f32_16x16x32_bf16 v[208:211], v[72:75], v[184:187], v[208:211]\n" \
    "v_mfma_f32_16x16x32_bf16 v[212:215], v[152:155], v[184:187], v[212:215]\n" \
    "v_mul_f32 v251, 0xbfb8aa3b, v253\n" \
    "s_waitcnt lgkmcnt(0)\n" \
    "v_mfma_f32_16x16x32_bf16 v[208:211], v[76:79], v[188:191], v[208:211]\n" \
    "v_mfma_f32_16x16x32_bf16 v[212:215], v[156:159], v[188:191], v[212:215]\n" \
    "s_waitcnt lgkmcnt(0)\n" \
    "s_barrier\n" \
    "s_nop 4\n" \
    "v_fma_f32 v208, -v252, v160, v208\n" \
    "v_fma_f32 v209, -v252, v161, v209\n" \
    "v_fma_f32 v210, -v252, v162, v210\n" \
    "v_fma_f32 v211, -v252, v163, v211\n" \
    "v_mul_f32 v208, v251, v208\n" \
    "v_mul_f32 v209, v251, v209\n" \
    "v_mul_f32 v210, v251, v210\n" \
    "v_mul_f32 v211, v251, v211\n" \
    "v_exp_f32 v208, v208\n" \
    "v_fma_f32 v212, -v252, v164, v212\n" \
    "v_exp_f32 v209, v209\n" \
    "v_fma_f32 v213, -v252, v165, v213\n" \
    "v_exp_f32 v210, v210\n" \
    "v_fma_f32 v214, -v252, v166, v214\n" \
    "v_exp_f32 v211, v211\n" \
    "v_fma_f32 v215, -v252, v167, v215\n" \
    "v_add_f32 v208, 1.0, v208\n" \
    "v_add_f32 v209, 1.0, v209\n" \
    "v_add_f32 v210, 1.0, v210\n" \
    "v_add_f32 v211, 1.0, v211\n" \
    "v_rcp_f32 v208, v208\n" \
    "v_mul_f32 v212, v253, v212\n" \
    "v_rcp_f32 v209, v209\n" \
    "v_mul_f32 v213, v253, v213\n" \
    "v_rcp_f32 v210, v210\n" \
    "v_mul_f32 v214, v253, v214\n" \
    "v_rcp_f32 v211, v211\n" \
    "v_mul_f32 v215, v253, v215\n" \
    "v_mul_f32 v208, v208, v212\n" \
    "v_mul_f32 v209, v209, v213\n" \
    "v_mul_f32 v210, v210, v214\n" \
    "v_mul_f32 v211, v211, v215\n" \
    "v_cvt_pk_bf16_f32 v236, v208, v209\n" \
    "v_cvt_pk_bf16_f32 v237, v210, v211\n" \
    "v_min_u32 v236, -2, v236\n" \
    "v_min_u32 v237, -2, v237\n" \
    "buffer_store_dwordx2 v[236:237], v173, s[8:11], s39 offen sc1\n" \
    "buffer_store_dwordx2 v[182:183], v173, s[8:11], s40 offen sc1\n" \
    "ds_read_b128 v[184:187], v168 offset:20480\n" \
    "ds_read_b128 v[188:191], v168 offset:21504\n" \
    "ds_read_b128 v[192:195], v168 offset:22528\n" \
    "ds_read_b128 v[196:199], v168 offset:23552\n" \
    "ds_read_b128 v[200:203], v168 offset:24576\n" \
    "ds_read_b64 v[248:249], v172 offset:1024\n" \
    "s_waitcnt lgkmcnt(5)\n" \
    "v_mfma_f32_16x16x32_bf16 v[208:211], v[0:3], v[184:187], 0\n" \
    "ds_read_b128 v[204:207], v168 offset:25600\n" \
    "v_mfma_f32_16x16x32_bf16 v[212:215], v[80:83], v[184:187], 0\n" \
    "s_waitcnt lgkmcnt(5)\n" \
    "v_mfma_f32_16x16x32_bf16 v[208:211], v[4:7], v[188:191], v[208:211]\n" \
    "ds_read_b128 v[184:187], v168 offset:26624\n" \
    "v_mfma_f32_16x16x32_bf16 v[212:215], v[84:87], v[188:191], v[212:215]\n" \
    "s_mul_i32 s39, s33, 98304\n" \
    "s_add_u32 s39, s39, 6291456\n" \
    "s_waitcnt lgkmcnt(5)\n" \
    "v_mfma_f32_16x16x32_bf16 v[208:211], v[8:11], v[192:195], v[208:211]\n" \
    "ds_read_b128 v[188:191], v168 offset:27648\n" \
    "v_mfma_f32_16x16x32_bf16 v[212:215], v[88:91], v[192:195], v[212:215]\n" \
    "s_add_u32 s40, s39, %[pdist]*98304\n" \
    "s_cmp_lt_u32 s33, 64-%[pdist]\n" \
    "s_cselect_b32 s40, s40, 0x80000000\n" \
    "s_waitcnt lgkmcnt(5)\n" \
    "v_mfma_f32_16x16x32_bf16 v[208:211], v[12:15], v[196:199], v[208:211]\n" \
    "ds_read_b128 v[192:195], v168 offset:28672\n" \
    "v_mfma_f32_16x16x32_bf16 v[212:215], v[92:95], v[196:199], v[212:215]\n" \
    "s_waitcnt lgkmcnt(5)\n" \
    "v_mfma_f32_16x16x32_bf16 v[208:211], v[16:19], v[200:203], v[208:211]\n" \
    "ds_read_b128 v[196:199], v168 offset:29696\n" \
    "v_mfma_f32_16x16x32_bf16 v[212:215], v[96:99], v[200:203], v[212:215]\n" \
    "s_waitcnt lgkmcnt(4)\n" \
    "v_mfma_f32_16x16x32_bf16 v[208:211], v[20:23], v[204:207], v[208:211]\n" \
    "ds_read_b128 v[200:203], v168 offset:30720\n" \
    "v_mfma_f32_16x16x32_bf16 v[212:215], v[100:103], v[204:207], v[212:215]\n" \
    "v_permlane16_swap_b32 v248, v249\n" \
    "s_waitcnt lgkmcnt(4)\n" \
    "v_mfma_f32_16x16x32_bf16 v[208:211], v[24:27], v[184:187], v[208:211]\n" \
    "ds_read_b128 v[204:207], v168 offset:31744\n" \
    "v_mfma_f32_16x16x32_bf16 v[212:215], v[104:107], v[184:187], v[212:215]\n" \
    "v_add_f32 v250, v248, v249\n" \
    "s_waitcnt lgkmcnt(4)\n" \
    "v_mfma_f32_16x16x32_bf16 v[208:211], v[28:31], v[188:191], v[208:211]\n" \
    "ds_read_b128 v[184:187], v168 offset:32768\n" \
    "v_mfma_f32_16x16x32_bf16 v[212:215], v[108:111], v[188:191], v[212:215]\n" \
    "v_add_f32 v248, v248, v249\n" \
    "s_waitcnt lgkmcnt(4)\n" \
    "v_mfma_f32_16x16x32_bf16 v[208:211], v[32:35], v[192:195], v[208:211]\n" \
    "ds_read_b128 v[188:191], v168 offset:33792\n" \
    "v_mfma_f32_16x16x32_bf16 v[212:215], v[112:115], v[192:195], v[212:215]\n" \
    "v_permlane32_swap_b32 v248, v250\n" \
    "s_waitcnt lgkmcnt(4)\n" \
    "v_mfma_f32_16x16x32_bf16 v[208:211], v[36:39], v[196:199], v[208:211]\n" \
    "ds_read_b128 v[192:195], v168 offset:34816\n" \
    "v_mfma_f32_16x16x32_bf16 v[212:215], v[116:119], v[196:199], v[212:215]\n" \
    "v_add_f32 v249, v248, v250\n" \
    "s_waitcnt lgkmcnt(4)\n" \
    "v_mfma_f32_16x16x32_bf16 v[208:211], v[40:43], v[200:203], v[208:211]\n" \
    "ds_read_b128 v[196:199], v168 offset:35840\n" \
    "v_mfma_f32_16x16x32_bf16 v[212:215], v[120:123], v[200:203], v[212:215]\n" \
    "v_add_f32 v248, v248, v250\n" \
    "s_waitcnt lgkmcnt(4)\n" \
    "v_mfma_f32_16x16x32_bf16 v[208:211], v[44:47], v[204:207], v[208:211]\n" \
    "ds_read_b128 v[200:203], v168 offset:36864\n" \
    "v_mfma_f32_16x16x32_bf16 v[212:215], v[124:127], v[204:207], v[212:215]\n" \
    "v_permlane16_swap_b32 v248, v249\n" \
    "s_waitcnt lgkmcnt(4)\n" \
    "v_mfma_f32_16x16x32_bf16 v[208:211], v[48:51], v[184:187], v[208:211]\n" \
    "ds_read_b128 v[204:207], v168 offset:37888\n" \
    "v_mfma_f32_16x16x32_bf16 v[212:215], v[128:131], v[184:187], v[212:215]\n" \
    "v_mul_f32 v252, 0x3acccccd, v248\n" \
    "s_waitcnt lgkmcnt(4)\n" \
    "v_mfma_f32_16x16x32_bf16 v[208:211], v[52:55], v[188:191], v[208:211]\n" \
    "ds_read_b128 v[184:187], v168 offset:38912\n" \
    "v_mfma_f32_16x16x32_bf16 v[212:215], v[132:135], v[188:191], v[212:215]\n" \
    "v_mul_f32 v249, 0x3acccccd, v249\n" \
    "s_waitcnt lgkmcnt(4)\n" \
    "v_mfma_f32_16x16x32_bf16 v[208:211], v[56:59], v[192:195], v[208:211]\n" \
    "ds_read_b128 v[188:191], v168 offset:39936\n" \
    "v_mfma_f32_16x16x32_bf16 v[212:215], v[136:139], v[192:195], v[212:215]\n" \
    "v_fma_f32 v249, -v252, v252, v249\n" \
    "s_waitcnt lgkmcnt(4)\n" \
    "v_mfma_f32_16x16x32_bf16 v[208:211], v[60:63], v[196:199], v[208:211]\n" \
    "v_mfma_f32_16x16x32_bf16 v[212:215], v[140:143], v[196:199], v[212:215]\n" \
    "v_max_f32 v249, 0, v249\n" \
    "s_waitcnt lgkmcnt(3)\n" \
    "v_mfma_f32_16x16x32_bf16 v[208:211], v[64:67], v[200:203], v[208:211]\n" \
    "v_mfma_f32_16x16x32_bf16 v[212:215], v[144:147], v[200:203], v[212:215]\n" \
    "v_add_f32 v249, 0x3727c5ac, v249\n" \
    "s_waitcnt lgkmcnt(2)\n" \
    "v_mfma_f32_16x16x32_bf16 v[208:211], v[68:71], v[204:207], v[208:211]\n" \
    "v_mfma_f32_16x16x32_bf16 v[212:215], v[148:151], v[204:207], v[212:215]\n" \
    "v_rsq_f32 v253, v249\n" \
    "s_waitcnt lgkmcnt(1)\n" \
    "v_mfma_f32_16x16x32_bf16 v[208:211], v[72:75], v[184:187], v[208:211]\n" \
    "v_mfma_f32_16x16x32_bf16 v[212:215], v[152:155], v[184:187], v[212:215]\n" \
    "v_mul_f32 v251, 0xbfb8aa3b, v253\n" \
    "s_waitcnt lgkmcnt(0)\n" \
    "v_mfma_f32_16x16x32_bf16 v[208:211], v[76:79], v[188:191], v[208:211]\n" \
    "v_mfma_f32_16x16x32_bf16 v[212:215], v[156:159], v[188:191], v[212:215]\n" \
    "s_nop 6\n" \
    "v_fma_f32 v208, -v252, v160, v208\n" \
    "v_fma_f32 v209, -v252, v161, v209\n" \
    "v_fma_f32 v210, -v252, v162, v210\n" \
    "v_fma_f32 v211, -v252, v163, v211\n" \
    "v_mul_f32 v208, v251, v208\n" \
    "v_mul_f32 v209, v251, v209\n" \
    "v_mul_f32 v210, v251, v210\n" \
    "v_mul_f32 v211, v251, v211\n" \
    "v_exp_f32 v208, v208\n" \
    "v_fma_f32 v212, -v252, v164, v212\n" \
    "v_exp_f32 v209, v209\n" \
    "v_fma_f32 v213, -v252, v165, v213\n" \
    "v_exp_f32 v210, v210\n" \
    "v_fma_f32 v214, -v252, v166, v214\n" \
    "v_exp_f32 v211, v211\n" \
    "v_fma_f32 v215, -v252, v167, v215\n" \
    "v_add_f32 v208, 1.0, v208\n" \
    "v_add_f32 v209, 1.0, v209\n" \
    "v_add_f32 v210, 1.0, v210\n" \
    "v_add_f32 v211, 1.0, v211\n" \
    "v_rcp_f32 v208, v208\n" \
    "v_mul_f32 v212, v253, v212\n" \
    "v_rcp_f32 v209, v209\n" \
    "v_mul_f32 v213, v253, v213\n" \
    "v_rcp_f32 v210, v210\n" \
    "v_mul_f32 v214, v253, v214\n" \
    "v_rcp_f32 v211, v211\n" \
    "v_mul_f32 v215, v253, v215\n" \
    "v_mul_f32 v208, v208, v212\n" \
    "v_mul_f32 v209, v209, v213\n" \
    "v_mul_f32 v210, v210, v214\n" \
    "v_mul_f32 v211, v211, v215\n" \
    "v_cvt_pk_bf16_f32 v236, v208, v209\n" \
    "v_cvt_pk_bf16_f32 v237, v210, v211\n" \
    "v_min_u32 v236, -2, v236\n" \
    "v_min_u32 v237, -2, v237\n" \
    "buffer_store_dwordx2 v[236:237], v173, s[8:11], s39 offen sc1\n" \
    "buffer_store_dwordx2 v[182:183], v173, s[8:11], s40 offen sc1\n" \
    "s_add_u32 s33, s33, 1\n" \
    "s_add_u32 s32, s32, 1\n" \
    "s_cmp_lt_u32 s32, 64\n" \
    "s_cbranch_scc1 .Lup_mY_%=\n" \
    "s_add_u32 s18, s18, 1024\n" \
    "s_waitcnt vmcnt(0)\n" \
    "s_barrier\n" \
    ".Lup_nY_%=:\n" \
    "s_waitcnt lgkmcnt(0)\n" \
    "s_barrier\n" \
    "ds_read_b128 v[184:187], v168 offset:20480\n" \
    "ds_read_b128 v[188:191], v168 offset:21504\n" \
    "ds_read_b128 v[192:195], v168 offset:22528\n" \
    "ds_read_b128 v[196:199], v168 offset:23552\n" \
    "ds_read_b128 v[200:203], v168 offset:24576\n" \
    "ds_read_b64 v[248:249], v172 offset:1024\n" \
    "s_waitcnt lgkmcnt(5)\n" \
    "v_mfma_f32_16x16x32_bf16 v[208:211], v[0:3], v[184:187], 0\n" \
    "ds_read_b128 v[204:207], v168 offset:25600\n" \
    "v_mfma_f32_16x16x32_bf16 v[212:215], v[80:83], v[184:187], 0\n" \
    "s_waitcnt lgkmcnt(5)\n" \
    "v_mfma_f32_16x16x32_bf16 v[208:211], v[4:7], v[188:191], v[208:211]\n" \
    "ds_read_b128 v[184:187], v168 offset:26624\n" \
    "v_mfma_f32_16x16x32_bf16 v[212:215], v[84:87], v[188:191], v[212:215]\n" \
    "s_mul_i32 s39, s33, 98304\n" \
    "s_add_u32 s39, s39, 6291456\n" \
    "s_waitcnt lgkmcnt(5)\n" \
    "v_mfma_f32_16x16x32_bf16 v[208:211], v[8:11], v[192:195], v[208:211]\n" \
    "ds_read_b128 v[188:191], v168 offset:27648\n" \
    "v_mfma_f32_16x16x32_bf16 v[212:215], v[88:91], v[192:195], v[212:215]\n" \
    "s_add_u32 s40, s39, %[pdist]*98304\n" \
    "s_cmp_lt_u32 s33, 64-%[pdist]\n" \
    "s_cselect_b32 s40, s40, 0x80000000\n" \
    "s_waitcnt lgkmcnt(5)\n" \
    "v_mfma_f32_16x16x32_bf16 v[208:211], v[12:15], v[196:199], v[208:211]\n" \
    "ds_read_b128 v[192:195], v168 offset:28672\n" \
    "v_mfma_f32_16x16x32_bf16 v[212:215], v[92:95], v[196:199], v[212:215]\n" \
    "s_waitcnt lgkmcnt(5)\n" \
    "v_mfma_f32_16x16x32_bf16 v[208:211], v[16:19], v[200:203], v[208:211]\n" \
    "ds_read_b128 v[196:199], v168 offset:29696\n" \
    "v_mfma_f32_16x16x32_bf16 v[212:215], v[96:99], v[200:203], v[212:215]\n" \
    "s_waitcnt lgkmcnt(4)\n" \
    "v_mfma_f32_16x16x32_bf16 v[208:211], v[20:23], v[204:207], v[208:211]\n" \
    "ds_read_b128 v[200:203], v168 offset:30720\n" \
    "v_mfma_f32_16x16x32_bf16 v[212:215], v[100:103], v[204:207], v[212:215]\n" \
    "v_permlane16_swap_b32 v248, v249\n" \
    "s_waitcnt lgkmcnt(4)\n" \
    "v_mfma_f32_16x16x32_bf16 v[208:211], v[24:27], v[184:187], v[208:211]\n" \
    "ds_read_b128 v[204:207], v168 offset:31744\n" \
    "v_mfma_f32_16x16x32_bf16 v[212:215], v[104:107], v[184:187], v[212:215]\n" \
    "v_add_f32 v250, v248, v249\n" \
    "s_waitcnt lgkmcnt(4)\n" \
    "v_mfma_f32_16x16x32_bf16 v[208:211], v[28:31], v[188:191], v[208:211]\n" \
    "ds_read_b128 v[184:187], v168 offset:32768\n" \
    "v_mfma_f32_16x16x32_bf16 v[212:215], v[108:111], v[188:191], v[212:215]\n" \
    "v_add_f32 v248, v248, v249\n" \
    "s_waitcnt lgkmcnt(4)\n" \
    "v_mfma_f32_16x16x32_bf16 v[208:211], v[32:35], v[192:195], v[208:211]\n" \
    "ds_read_b128 v[188:191], v168 offset:33792\n" \
    "v_mfma_f32_16x16x32_bf16 v[212:215], v[112:115], v[192:195], v[212:215]\n" \
    "v_permlane32_swap_b32 v248, v250\n" \
    "s_waitcnt lgkmcnt(4)\n" \
    "v_mfma_f32_16x16x32_bf16 v[208:211], v[36:39], v[196:199], v[208:211]\n" \
    "ds_read_b128 v[192:195], v168 offset:34816\n" \
    "v_mfma_f32_16x16x32_bf16 v[212:215], v[116:119], v[196:199], v[212:215]\n" \
    "v_add_f32 v249, v248, v250\n" \
    "s_waitcnt lgkmcnt(4)\n" \
    "v_mfma_f32_16x16x32_bf16 v[208:211], v[40:43], v[200:203], v[208:211]\n" \
    "ds_read_b128 v[196:199], v168 offset:35840\n" \
    "v_mfma_f32_16x16x32_bf16 v[212:215], v[120:123], v[200:203], v[212:215]\n" \
    "v_add_f32 v248, v248, v250\n" \
    "s_waitcnt lgkmcnt(4)\n" \
    "v_mfma_f32_16x16x32_bf16 v[208:211], v[44:47], v[204:207], v[208:211]\n" \
    "ds_read_b128 v[200:203], v168 offset:36864\n" \
    "v_mfma_f32_16x16x32_bf16 v[212:215], v[124:127], v[204:207], v[212:215]\n" \
    "v_permlane16_swap_b32 v248, v249\n" \
    "s_waitcnt lgkmcnt(4)\n" \
    "v_mfma_f32_16x16x32_bf16 v[208:211], v[48:51], v[184:187], v[208:211]\n" \
    "ds_read_b128 v[204:207], v168 offset:37888\n" \
    "v_mfma_f32_16x16x32_bf16 v[212:215], v[128:131], v[184:187], v[212:215]\n" \
    "v_mul_f32 v252, 0x3acccccd, v248\n" \
    "s_waitcnt lgkmcnt(4)\n" \
    "v_mfma_f32_16x16x32_bf16 v[208:211], v[52:55], v[188:191], v[208:211]\n" \
    "ds_read_b128 v[184:187], v168 offset:38912\n" \
    "v_mfma_f32_16x16x32_bf16 v[212:215], v[132:135], v[188:191], v[212:215]\n" \
    "v_mul_f32 v249, 0x3acccccd, v249\n" \
    "s_waitcnt lgkmcnt(4)\n" \
    "v_mfma_f32_16x16x32_bf16 v[208:211], v[56:59], v[192:195], v[208:211]\n" \
    "ds_read_b128 v[188:191], v168 offset:39936\n" \
    "v_mfma_f32_16x16x32_bf16 v[212:215], v[136:139], v[192:195], v[212:215]\n" \
    "v_fma_f32 v249, -v252, v252, v249\n" \
    "s_waitcnt lgkmcnt(4)\n" \
    "v_mfma_f32_16x16x32_bf16 v[208:211], v[60:63], v[196:199], v[208:211]\n" \
    "v_mfma_f32_16x16x32_bf16 v[212:215], v[140:143], v[196:199], v[212:215]\n" \
    "v_max_f32 v249, 0, v249\n" \
    "s_waitcnt lgkmcnt(3)\n" \
    "v_mfma_f32_16x16x32_bf16 v[208:211], v[64:67], v[200:203], v[208:211]\n" \
    "v_mfma_f32_16x16x32_bf16 v[212:215], v[144:147], v[200:203], v[212:215]\n" \
    "v_add_f32 v249, 0x3727c5ac, v249\n" \
    "s_waitcnt lgkmcnt(2)\n" \
    "v_mfma_f32_16x16x32_bf16 v[208:211], v[68:71], v[204:207], v[208:211]\n" \
    "v_mfma_f32_16x16x32_bf16 v[212:215], v[148:151], v[204:207], v[212:215]\n" \
    "v_rsq_f32 v253, v249\n" \
    "s_waitcnt lgkmcnt(1)\n" \
    "v_mfma_f32_16x16x32_bf16 v[208:211], v[72:75], v[184:187], v[208:211]\n" \
    "v_mfma_f32_16x16x32_bf16 v[212:215], v[152:155], v[184:187], v[212:215]\n" \
    "v_mul_f32 v251, 0xbfb8aa3b, v253\n" \
    "s_waitcnt lgkmcnt(0)\n" \
    "v_mfma_f32_16x16x32_bf16 v[208:211], v[76:79], v[188:191], v[208:211]\n" \
    "v_mfma_f32_16x16x32_bf16 v[212:215], v[156:159], v[188:191], v[212:215]\n" \
    "s_barrier\n" \
    "s_nop 5\n" \
    "v_fma_f32 v208, -v252, v160, v208\n" \
    "v_fma_f32 v209, -v252, v161, v209\n" \
    "v_fma_f32 v210, -v252, v162, v210\n" \
    "v_fma_f32 v211, -v252, v163, v211\n" \
    "v_mul_f32 v208, v251, v208\n" \
    "v_mul_f32 v209, v251, v209\n" \
    "v_mul_f32 v210, v251, v210\n" \
    "v_mul_f32 v211, v251, v211\n" \
    "v_exp_f32 v208, v208\n" \
    "v_fma_f32 v212, -v252, v164, v212\n" \
    "v_exp_f32 v209, v209\n" \
    "v_fma_f32 v213, -v252, v165, v213\n" \
    "v_exp_f32 v210, v210\n" \
    "v_fma_f32 v214, -v252, v166, v214\n" \
    "v_exp_f32 v211, v211\n" \
    "v_fma_f32 v215, -v252, v167, v215\n" \
    "v_add_f32 v208, 1.0, v208\n" \
    "v_add_f32 v209, 1.0, v209\n" \
    "v_add_f32 v210, 1.0, v210\n" \
    "v_add_f32 v211, 1.0, v211\n" \
    "v_rcp_f32 v208, v208\n" \
    "v_mul_f32 v212, v253, v212\n" \
    "v_rcp_f32 v209, v209\n" \
    "v_mul_f32 v213, v253, v213\n" \
    "v_rcp_f32 v210, v210\n" \
    "v_mul_f32 v214, v253, v214\n" \
    "v_rcp_f32 v211, v211\n" \
    "v_mul_f32 v215, v253, v215\n" \
    "v_mul_f32 v208, v208, v212\n" \
    "v_mul_f32 v209, v209, v213\n" \
    "v_mul_f32 v210, v210, v214\n" \
    "v_mul_f32 v211, v211, v215\n" \
    "v_cvt_pk_bf16_f32 v236, v208, v209\n" \
    "v_cvt_pk_bf16_f32 v237, v210, v211\n" \
    "v_min_u32 v236, -2, v236\n" \
    "v_min_u32 v237, -2, v237\n" \
    "buffer_store_dwordx2 v[236:237], v173, s[8:11], s39 offen sc1\n" \
    "buffer_store_dwordx2 v[182:183], v173, s[8:11], s40 offen sc1\n" \
    "s_add_u32 s33, s33, 1\n" \
    "s_cmp_lt_u32 s33, 64\n" \
    "s_cbranch_scc1 .Lup_nY_%=\n" \
    "s_waitcnt vmcnt(0)\n" \
    "s_endpgm\n" \
    ".Lup_lO_%=:\n" \
    "s_mov_b32 s33, 0\n" \
    "s_mov_b32 s42, 0\n" \
    "s_add_u32 s48, s32, 2\n" \
    "s_min_u32 s48, s48, 64\n" \
    "s_and_b32 s49, s48, 1\n" \
    "s_xor_b32 s49, s49, 1\n" \
    "s_lshl_b32 s49, s49, 9\n" \
    "s_add_u32 s49, s49, s18\n" \
    "buffer_load_dwordx2 v[176:177], off, s[8:11], s49 sc1\n" \
    "s_waitcnt vmcnt(0)\n" \
    ".Lup_kO_%=:\n" \
    "s_mov_b32 s37, 0\n" \
    "s_waitcnt vmcnt(2)\n" \
    ".Lup_Oap_%=:\n" \
    "v_readfirstlane_b32 s36, v177\n" \
    "s_nop 0\n" \
    "s_cmp_gt_i32 s36, s48\n" \
    "s_cbranch_scc1 .Lup_Oax_%=\n" \
    "s_cmp_eq_u32 s36, s48\n" \
    "s_cbranch_scc1 .Lup_Oaw_%=\n" \
    "s_sleep 3\n" \
    "buffer_load_dwordx2 v[176:177], off, s[8:11], s49 sc1\n" \
    "s_waitcnt vmcnt(0)\n" \
    "s_add_u32 s37, s37, 1\n" \
    "s_cmp_lt_u32 s37, s38\n" \
    "s_cbranch_scc1 .Lup_Oap_%=\n" \
    "s_mov_b32 s38, 0\n" \
    "s_branch .Lup_Oax_%=\n" \
    ".Lup_Oaw_%=:\n" \
    "v_readfirstlane_b32 s36, v176\n" \
    "s_cmp_eq_u32 s32, 63\n" \
    "s_cselect_b32 s34, %[dlast], 0\n" \
    "s_add_u32 s36, s36, s34\n" \
    "s_add_u32 s36, s36, %[dx]\n" \
    "s_branch .Lup_Oatb_%=\n" \
    ".Lup_Oata_%=:\n" \
    "s_sleep 1\n" \
    ".Lup_Oatb_%=:\n" \
    "s_memrealtime vcc\n" \
    "s_waitcnt lgkmcnt(0)\n" \
    "s_sub_i32 vcc_lo, s36, vcc_lo\n" \
    "s_cmp_gt_i32 vcc_lo, 8\n" \
    "s_cbranch_scc1 .Lup_Oata_%=\n" \
    "s_cmp_gt_i32 vcc_lo, 0\n" \
    "s_cbranch_scc1 .Lup_Oatb_%=\n" \
    ".Lup_Oax_%=:\n" \
    "s_mul_i32 s41, s32, 40960\n" \
    "s_add_u32 s41, s41, s17\n" \
    "buffer_load_dwordx4 v[216:219], v168, s[8:11], s41 offen sc1\n" \
    "s_add_u32 s41, s41, 4096\n" \
    "buffer_load_dwordx4 v[220:223], v168, s[8:11], s41 offen sc1\n" \
    "s_add_u32 s41, s41, 4096\n" \
    "buffer_load_dwordx4 v[224:227], v168, s[8:11], s41 offen sc1\n" \
    "s_add_u32 s41, s41, 4096\n" \
    "buffer_load_dwordx4 v[228:231], v168, s[8:11], s41 offen sc1\n" \
    "s_add_u32 s41, s41, 4096\n" \
    "buffer_load_dwordx4 v[232:235], v168, s[8:11], s41 offen sc1\n" \
    "s_mov_b32 s37, 0\n" \
    ".Lup_srOa_%=:\n" \
    "s_waitcnt vmcnt(4)\n" \
    "v_mfma_f32_16x16x32_bf16 v[244:247], v[216:219], v[216:219], 0\n" \
    "ds_write_b128 v169, v[216:219] offset:0\n" \
    "v_max3_u32 v254, v216, v217, v218\n" \
    "v_mfma_f32_16x16x32_bf16 v[240:243], v[178:181], v[216:219], 0\n" \
    "v_max_u32 v254, v254, v219\n" \
    "s_waitcnt vmcnt(3)\n" \
    "v_mfma_f32_16x16x32_bf16 v[244:247], v[220:223], v[220:223], v[244:247]\n" \
    "ds_write_b128 v169, v[220:223] offset:4096\n" \
    "v_max3_u32 v254, v254, v220, v221\n" \
    "v_mfma_f32_16x16x32_bf16 v[240:243], v[178:181], v[220:223], v[240:243]\n" \
    "v_max3_u32 v254, v254, v222, v223\n" \
    "s_waitcnt vmcnt(2)\n" \
    "v_mfma_f32_16x16x32_bf16 v[244:247], v[224:227], v[224:227], v[244:247]\n" \
    "ds_write_b128 v169, v[224:227] offset:8192\n" \
    "v_max3_u32 v254, v254, v224, v225\n" \
    "v_mfma_f32_16x16x32_bf16 v[240:243], v[178:181], v[224:227], v[240:243]\n" \
    "v_max3_u32 v254, v254, v226, v227\n" \
    "s_waitcnt vmcnt(1)\n" \
    "v_mfma_f32_16x16x32_bf16 v[244:247], v[228:231], v[228:231], v[244:247]\n" \
    "ds_write_b128 v169, v[228:231] offset:12288\n" \
    "v_max3_u32 v254, v254, v228, v229\n" \
    "v_mfma_f32_16x16x32_bf16 v[240:243], v[178:181], v[228:231], v[240:243]\n" \
    "v_max3_u32 v254, v254, v230, v231\n" \
    "s_waitcnt vmcnt(0)\n" \
    "v_mfma_f32_16x16x32_bf16 v[244:247], v[232:235], v[232:235], v[244:247]\n" \
    "ds_write_b128 v169, v[232:235] offset:16384\n" \
    "v_max3_u32 v254, v254, v232, v233\n" \
    "v_mfma_f32_16x16x32_bf16 v[240:243], v[178:181], v[232:235], v[240:243]\n" \
    "v_max3_u32 v254, v254, v234, v235\n" \
    "v_cmp_eq_u32 vcc, -1, v254\n" \
    "s_nop 0\n" \
    "s_cbranch_vccnz .Lup_scOa_%=\n" \
    ".Lup_sxOa_%=:\n" \
    "s_nop 0\n" \
    "v_cndmask_b32 v254, v244, v245, s[44:45]\n" \
    "v_cndmask_b32 v255, v246, v247, s[44:45]\n" \
    "s_nop 0\n" \
    "v_cndmask_b32 v241, v254, v255, s[46:47]\n" \
    "ds_write_b64 v171, v[240:241] offset:0\n" \
    "s_waitcnt lgkmcnt(0)\n" \
    "s_barrier\n" \
    "ds_read_b128 v[184:187], v168 offset:0\n" \
    "ds_read_b128 v[188:191], v168 offset:1024\n" \
    "ds_read_b128 v[192:195], v168 offset:2048\n" \
    "ds_read_b128 v[196:199], v168 offset:3072\n" \
    "ds_read_b128 v[200:203], v168 offset:4096\n" \
    "ds_read_b64 v[248:249], v172 offset:0\n" \
    "s_waitcnt lgkmcnt(5)\n" \
    "v_mfma_f32_16x16x32_bf16 v[208:211], v[0:3], v[184:187], 0\n" \
    "ds_read_b128 v[204:207], v168 offset:5120\n" \
    "v_mfma_f32_16x16x32_bf16 v[212:215], v[80:83], v[184:187], 0\n" \
    "s_waitcnt lgkmcnt(5)\n" \
    "v_mfma_f32_16x16x32_bf16 v[208:211], v[4:7], v[188:191], v[208:211]\n" \
    "ds_read_b128 v[184:187], v168 offset:6144\n" \
    "v_mfma_f32_16x16x32_bf16 v[212:215], v[84:87], v[188:191], v[212:215]\n" \
    "s_mul_i32 s39, s32, 98304\n" \
    "s_add_u32 s39, s39, 0\n" \
    "s_waitcnt lgkmcnt(5)\n" \
    "v_mfma_f32_16x16x32_bf16 v[208:211], v[8:11], v[192:195], v[208:211]\n" \
    "ds_read_b128 v[188:191], v168 offset:7168\n" \
    "v_mfma_f32_16x16x32_bf16 v[212:215], v[88:91], v[192:195], v[212:215]\n" \
    "s_add_u32 s40, s39, %[pdist]*98304\n" \
    "s_cmp_lt_u32 s32, 64-%[pdist]\n" \
    "s_cselect_b32 s40, s40, 0x80000000\n" \
    "s_waitcnt lgkmcnt(5)\n" \
    "v_mfma_f32_16x16x32_bf16 v[208:211], v[12:15], v[196:199], v[208:211]\n" \
    "ds_read_b128 v[192:195], v168 offset:8192\n" \
    "v_mfma_f32_16x16x32_bf16 v[212:215], v[92:95], v[196:199], v[212:215]\n" \
    "s_waitcnt lgkmcnt(5)\n" \
    "v_mfma_f32_16x16x32_bf16 v[208:211], v[16:19], v[200:203], v[208:211]\n" \
    "ds_read_b128 v[196:199], v168 offset:9216\n" \
    "v_mfma_f32_16x16x32_bf16 v[212:215], v[96:99], v[200:203], v[212:215]\n" \
    "s_waitcnt lgkmcnt(4)\n" \
    "v_mfma_f32_16x16x32_bf16 v[208:211], v[20:23], v[204:207], v[208:211]\n" \
    "ds_read_b128 v[200:203], v168 offset:10240\n" \
    "v_mfma_f32_16x16x32_bf16 v[212:215], v[100:103], v[204:207], v[212:215]\n" \
    "v_permlane16_swap_b32 v248, v249\n" \
    "s_waitcnt lgkmcnt(4)\n" \
    "v_mfma_f32_16x16x32_bf16 v[208:211], v[24:27], v[184:187], v[208:211]\n" \
    "ds_read_b128 v[204:207], v168 offset:11264\n" \
    "v_mfma_f32_16x16x32_bf16 v[212:215], v[104:107], v[184:187], v[212:215]\n" \
    "v_add_f32 v250, v248, v249\n" \
    "s_waitcnt lgkmcnt(4)\n" \
    "v_mfma_f32_16x16x32_bf16 v[208:211], v[28:31], v[188:191], v[208:211]\n" \
    "ds_read_b128 v[184:187], v168 offset:12288\n" \
    "v_mfma_f32_16x16x32_bf16 v[212:215], v[108:111], v[188:191], v[212:215]\n" \
    "v_add_f32 v248, v248, v249\n" \
    "s_waitcnt lgkmcnt(4)\n" \
    "v_mfma_f32_16x16x32_bf16 v[208:211], v[32:35], v[192:195], v[208:211]\n" \
    "ds_read_b128 v[188:191], v168 offset:13312\n" \
    "v_mfma_f32_16x16x32_bf16 v[212:215], v[112:115], v[192:195], v[212:215]\n" \
    "v_permlane32_swap_b32 v248, v250\n" \
    "s_waitcnt lgkmcnt(4)\n" \
    "v_mfma_f32_16x16x32_bf16 v[208:211], v[36:39], v[196:199], v[208:211]\n" \
    "ds_read_b128 v[192:195], v168 offset:14336\n" \
    "v_mfma_f32_16x16x32_bf16 v[212:215], v[116:119], v[196:199], v[212:215]\n" \
    "v_add_f32 v249, v248, v250\n" \
    "s_waitcnt lgkmcnt(4)\n" \
    "v_mfma_f32_16x16x32_bf16 v[208:211], v[40:43], v[200:203], v[208:211]\n" \
    "ds_read_b128 v[196:199], v168 offset:15360\n" \
    "v_mfma_f32_16x16x32_bf16 v[212:215], v[120:123], v[200:203], v[212:215]\n" \
    "v_add_f32 v248, v248, v250\n" \
    "s_add_u32 s48, s32, 3\n" \
    "s_min_u32 s48, s48, 64\n" \
    "s_and_b32 s49, s48, 1\n" \
    "s_xor_b32 s49, s49, 1\n" \
    "s_lshl_b32 s49, s49, 9\n" \
    "s_add_u32 s49, s49, s18\n" \
    "buffer_load_dwordx2 v[176:177], off, s[8:11], s49 sc1\n" \
    "s_waitcnt lgkmcnt(4)\n" \
    "v_mfma_f32_16x16x32_bf16 v[208:211], v[44:47], v[204:207], v[208:211]\n" \
    "ds_read_b128 v[200:203], v168 offset:16384\n" \
    "v_mfma_f32_16x16x32_bf16 v[212:215], v[124:127], v[204:207], v[212:215]\n" \
    "v_permlane16_swap_b32 v248, v249\n" \
    "s_waitcnt lgkmcnt(4)\n" \
    "v_mfma_f32_16x16x32_bf16 v[208:211], v[48:51], v[184:187], v[208:211]\n" \
    "ds_read_b128 v[204:207], v168 offset:17408\n" \
    "v_mfma_f32_16x16x32_bf16 v[212:215], v[128:131], v[184:187], v[212:215]\n" \
    "v_mul_f32 v252, 0x3acccccd, v248\n" \
    "s_waitcnt lgkmcnt(4)\n" \
    "v_mfma_f32_16x16x32_bf16 v[208:211], v[52:55], v[188:191], v[208:211]\n" \
    "ds_read_b128 v[184:187], v168 offset:18432\n" \
    "v_mfma_f32_16x16x32_bf16 v[212:215], v[132:135], v[188:191], v[212:215]\n" \
    "v_mul_f32 v249, 0x3acccccd, v249\n" \
    "s_waitcnt lgkmcnt(4)\n" \
    "v_mfma_f32_16x16x32_bf16 v[208:211], v[56:59], v[192:195], v[208:211]\n" \
    "ds_read_b128 v[188:191], v168 offset:19456\n" \
    "v_mfma_f32_16x16x32_bf16 v[212:215], v[136:139], v[192:195], v[212:215]\n" \
    "v_fma_f32 v249, -v252, v252, v249\n" \
    "s_waitcnt lgkmcnt(4)\n" \
    "v_mfma_f32_16x16x32_bf16 v[208:211], v[60:63], v[196:199], v[208:211]\n" \
    "v_mfma_f32_16x16x32_bf16 v[212:215], v[140:143], v[196:199], v[212:215]\n" \
    "v_max_f32 v249, 0, v249\n" \
    "s_waitcnt lgkmcnt(3)\n" \
    "v_mfma_f32_16x16x32_bf16 v[208:211], v[64:67], v[200:203], v[208:211]\n" \
    "v_mfma_f32_16x16x32_bf16 v[212:215], v[144:147], v[200:203], v[212:215]\n" \
    "v_add_f32 v249, 0x3727c5ac, v249\n" \
    "s_waitcnt lgkmcnt(2)\n" \
    "v_mfma_f32_16x16x32_bf16 v[208:211], v[68:71], v[204:207], v[208:211]\n" \
    "v_mfma_f32_16x16x32_bf16 v[212:215], v[148:151], v[204:207], v[212:215]\n" \
    "v_rsq_f32 v253, v249\n" \
    "s_waitcnt lgkmcnt(1)\n" \
    "v_mfma_f32_16x16x32_bf16 v[208:211], v[72:75], v[184:187], v[208:211]\n" \
    "v_mfma_f32_16x16x32_bf16 v[212:215], v[152:155], v[184:187], v[212:215]\n" \
    "v_mul_f32 v251, 0xbfb8aa3b, v253\n" \
    "s_waitcnt lgkmcnt(0)\n" \
    "v_mfma_f32_16x16x32_bf16 v[208:211], v[76:79], v[188:191], v[208:211]\n" \
    "v_mfma_f32_16x16x32_bf16 v[212:215], v[156:159], v[188:191], v[212:215]\n" \
    "s_nop 6\n" \
    "v_fma_f32 v208, -v252, v160, v208\n" \
    "v_fma_f32 v209, -v252, v161, v209\n" \
    "v_fma_f32 v210, -v252, v162, v210\n" \
    "v_fma_f32 v211, -v252, v163, v211\n" \
    "v_mul_f32 v208, v251, v208\n" \
    "v_mul_f32 v209, v251, v209\n" \
    "v_mul_f32 v210, v251, v210\n" \
    "v_mul_f32 v211, v251, v211\n" \
    "v_exp_f32 v208, v208\n" \
    "v_fma_f32 v212, -v252, v164, v212\n" \
    "v_exp_f32 v209, v209\n" \
    "v_fma_f32 v213, -v252, v165, v213\n" \
    "v_exp_f32 v210, v210\n" \
    "v_fma_f32 v214, -v252, v166, v214\n" \
    "v_exp_f32 v211, v211\n" \
    "v_fma_f32 v215, -v252, v167, v215\n" \
    "v_add_f32 v208, 1.0, v208\n" \
    "v_add_f32 v209, 1.0, v209\n" \
    "v_add_f32 v210, 1.0, v210\n" \
    "v_add_f32 v211, 1.0, v211\n" \
    "v_rcp_f32 v208, v208\n" \
    "v_mul_f32 v212, v253, v212\n" \
    "v_rcp_f32 v209, v209\n" \
    "v_mul_f32 v213, v253, v213\n" \
    "v_rcp_f32 v210, v210\n" \
    "v_mul_f32 v214, v253, v214\n" \
    "v_rcp_f32 v211, v211\n" \
    "v_mul_f32 v215, v253, v215\n" \
    "v_mul_f32 v208, v208, v212\n" \
    "v_mul_f32 v209, v209, v213\n" \
    "v_mul_f32 v210, v210, v214\n" \
    "v_mul_f32 v211, v211, v215\n" \
    "v_cvt_pk_bf16_f32 v236, v208, v209\n" \
    "v_cvt_pk_bf16_f32 v237, v210, v211\n" \
    "v_min_u32 v236, -2, v236\n" \
    "v_min_u32 v237, -2, v237\n" \
    "buffer_store_dwordx2 v[236:237], v173, s[8:11], s39 offen sc1\n" \
    "buffer_store_dwordx2 v[182:183], v173, s[8:11], s40 offen sc1\n" \
    "s_barrier\n" \
    "s_add_u32 s32, s32, 1\n" \
    "s_cmp_lt_u32 s32, %[eup]\n" \
    "s_cbranch_scc1 .Lup_kO_%=\n" \
    ".Lup_mO_%=:\n" \
    "s_cmp_eq_u32 s42, 0\n" \
    "s_waitcnt vmcnt(2)\n" \
    "s_cbranch_scc1 .Lup_qb_%=\n" \
    "s_mov_b32 s37, 0\n" \
    ".Lup_srObq_%=:\n" \
    "v_mfma_f32_16x16x32_bf16 v[244:247], v[216:219], v[216:219], 0\n" \
    "ds_write_b128 v169, v[216:219] offset:0\n" \
    "v_max3_u32 v254, v216, v217, v218\n" \
    "v_mfma_f32_16x16x32_bf16 v[240:243], v[178:181], v[216:219], 0\n" \
    "v_max_u32 v254, v254, v219\n" \
    "v_mfma_f32_16x16x32_bf16 v[244:247], v[220:223], v[220:223], v[244:247]\n" \
    "ds_write_b128 v169, v[220:223] offset:4096\n" \
    "v_max3_u32 v254, v254, v220, v221\n" \
    "v_mfma_f32_16x16x32_bf16 v[240:243], v[178:181], v[220:223], v[240:243]\n" \
    "v_max3_u32 v254, v254, v222, v223\n" \
    "v_mfma_f32_16x16x32_bf16 v[244:247], v[224:227], v[224:227], v[244:247]\n" \
    "ds_write_b128 v169, v[224:227] offset:8192\n" \
    "v_max3_u32 v254, v254, v224, v225\n" \
    "v_mfma_f32_16x16x32_bf16 v[240:243], v[178:181], v[224:227], v[240:243]\n" \
    "v_max3_u32 v254, v254, v226, v227\n" \
    "v_mfma_f32_16x16x32_bf16 v[244:247], v[228:231], v[228:231], v[244:247]\n" \
    "ds_write_b128 v169, v[228:231] offset:12288\n" \
    "v_max3_u32 v254, v254, v228, v229\n" \
    "v_mfma_f32_16x16x32_bf16 v[240:243], v[178:181], v[228:231], v[240:243]\n" \
    "v_max3_u32 v254, v254, v230, v231\n" \
    "v_mfma_f32_16x16x32_bf16 v[244:247], v[232:235], v[232:235], v[244:247]\n" \
    "ds_write_b128 v169, v[232:235] offset:16384\n" \
    "v_max3_u32 v254, v254, v232, v233\n" \
    "v_mfma_f32_16x16x32_bf16 v[240:243], v[178:181], v[232:235], v[240:243]\n" \
    "v_max3_u32 v254, v254, v234, v235\n" \
    "v_cmp_eq_u32 vcc, -1, v254\n" \
    "s_nop 0\n" \
    "s_cbranch_vccnz .Lup_scObq_%=\n" \
    ".Lup_sxObq_%=:\n" \
    "s_nop 0\n" \
    "v_cndmask_b32 v254, v244, v245, s[44:45]\n" \
    "v_cndmask_b32 v255, v246, v247, s[44:45]\n" \
    "s_nop 0\n" \
    "v_cndmask_b32 v241, v254, v255, s[46:47]\n" \
    "ds_write_b64 v171, v[240:241] offset:0\n" \
    "s_branch .Lup_jb_%=\n" \
    ".Lup_qb_%=:\n" \
    "s_mov_b32 s37, 0\n" \
    "s_waitcnt vmcnt(2)\n" \
    ".Lup_Obp_%=:\n" \
    "v_readfirstlane_b32 s36, v177\n" \
    "s_nop 0\n" \
    "s_cmp_gt_i32 s36, s48\n" \
    "s_cbranch_scc1 .Lup_Obx_%=\n" \
    "s_cmp_eq_u32 s36, s48\n" \
    "s_cbranch_scc1 .Lup_Obw_%=\n" \
    "s_sleep 3\n" \
    "buffer_load_dwordx2 v[176:177], off, s[8:11], s49 sc1\n" \
    "s_waitcnt vmcnt(0)\n" \
    "s_add_u32 s37, s37, 1\n" \
    "s_cmp_lt_u32 s37, s38\n" \
    "s_cbranch_scc1 .Lup_Obp_%=\n" \
    "s_mov_b32 s38, 0\n" \
    "s_branch .Lup_Obx_%=\n" \
    ".Lup_Obw_%=:\n" \
    "v_readfirstlane_b32 s36, v176\n" \
    "s_cmp_eq_u32 s32, 63\n" \
    "s_cselect_b32 s34, %[dlast], 0\n" \
    "s_add_u32 s36, s36, s34\n" \
    "s_add_u32 s36, s36, %[dx]\n" \
    "s_branch .Lup_Obtb_%=\n" \
    ".Lup_Obta_%=:\n" \
    "s_sleep 1\n" \
    ".Lup_Obtb_%=:\n" \
    "s_memrealtime vcc\n" \
    "s_waitcnt lgkmcnt(0)\n" \
    "s_sub_i32 vcc_lo, s36, vcc_lo\n" \
    "s_cmp_gt_i32 vcc_lo, 8\n" \
    "s_cbranch_scc1 .Lup_Obta_%=\n" \
    "s_cmp_gt_i32 vcc_lo, 0\n" \
    "s_cbranch_scc1 .Lup_Obtb_%=\n" \
    ".Lup_Obx_%=:\n" \
    "s_mul_i32 s41, s32, 40960\n" \
    "s_add_u32 s41, s41, s17\n" \
    "buffer_load_dwordx4 v[216:219], v168, s[8:11], s41 offen sc1\n" \
    "s_add_u32 s41, s41, 4096\n" \
    "buffer_load_dwordx4 v[220:223], v168, s[8:11], s41 offen sc1\n" \
    "s_add_u32 s41, s41, 4096\n" \
    "buffer_load_dwordx4 v[224:227], v168, s[8:11], s41 offen sc1\n" \
    "s_add_u32 s41, s41, 4096\n" \
    "buffer_load_dwordx4 v[228:231], v168, s[8:11], s41 offen sc1\n" \
    "s_add_u32 s41, s41, 4096\n" \
    "buffer_load_dwordx4 v[232:235], v168, s[8:11], s41 offen sc1\n" \
    "s_mov_b32 s37, 0\n" \
    ".Lup_srOb_%=:\n" \
    "s_waitcnt vmcnt(4)\n" \
    "v_mfma_f32_16x16x32_bf16 v[244:247], v[216:219], v[216:219], 0\n" \
    "ds_write_b128 v169, v[216:219] offset:0\n" \
    "v_max3_u32 v254, v216, v217, v218\n" \
    "v_mfma_f32_16x16x32_bf16 v[240:243], v[178:181], v[216:219], 0\n" \
    "v_max_u32 v254, v254, v219\n" \
    "s_waitcnt vmcnt(3)\n" \
    "v_mfma_f32_16x16x32_bf16 v[244:247], v[220:223], v[220:223], v[244:247]\n" \
    "ds_write_b128 v169, v[220:223] offset:4096\n" \
    "v_max3_u32 v254, v254, v220, v221\n" \
    "v_mfma_f32_16x16x32_bf16 v[240:243], v[178:181], v[220:223], v[240:243]\n" \
    "v_max3_u32 v254, v254, v222, v223\n" \
    "s_waitcnt vmcnt(2)\n" \
    "v_mfma_f32_16x16x32_bf16 v[244:247], v[224:227], v[224:227], v[244:247]\n" \
    "ds_write_b128 v169, v[224:227] offset:8192\n" \
    "v_max3_u32 v254, v254, v224, v225\n" \
    "v_mfma_f32_16x16x32_bf16 v[240:243], v[178:181], v[224:227], v[240:243]\n" \
    "v_max3_u32 v254, v254, v226, v227\n" \
    "s_waitcnt vmcnt(1)\n" \
    "v_mfma_f32_16x16x32_bf16 v[244:247], v[228:231], v[228:231], v[244:247]\n" \
    "ds_write_b128 v169, v[228:231] offset:12288\n" \
    "v_max3_u32 v254, v254, v228, v229\n" \
    "v_mfma_f32_16x16x32_bf16 v[240:243], v[178:181], v[228:231], v[240:243]\n" \
    "v_max3_u32 v254, v254, v230, v231\n" \
    "s_waitcnt vmcnt(0)\n" \
    "v_mfma_f32_16x16x32_bf16 v[244:247], v[232:235], v[232:235], v[244:247]\n" \
    "ds_write_b128 v169, v[232:235] offset:16384\n" \
    "v_max3_u32 v254, v254, v232, v233\n" \
    "v_mfma_f32_16x16x32_bf16 v[240:243], v[178:181], v[232:235], v[240:243]\n" \
    "v_max3_u32 v254, v254, v234, v235\n" \
    "v_cmp_eq_u32 vcc, -1, v254\n" \
    "s_nop 0\n" \
    "s_cbranch_vccnz .Lup_scOb_%=\n" \
    ".Lup_sxOb_%=:\n" \
    "s_nop 0\n" \
    "v_cndmask_b32 v254, v244, v245, s[44:45]\n" \
    "v_cndmask_b32 v255, v246, v247, s[44:45]\n" \
    "s_nop 0\n" \
    "v_cndmask_b32 v241, v254, v255, s[46:47]\n" \
    "ds_write_b64 v171, v[240:241] offset:0\n" \
    ".Lup_jb_%=:\n" \
    "s_waitcnt lgkmcnt(0)\n" \
    "s_barrier\n" \
    "ds_read_b128 v[184:187], v168 offset:0\n" \
    "ds_read_b128 v[188:191], v168 offset:1024\n" \
    "ds_read_b128 v[192:195], v168 offset:2048\n" \
    "ds_read_b128 v[196:199], v168 offset:3072\n" \
    "ds_read_b128 v[200:203], v168 offset:4096\n" \
    "ds_read_b64 v[248:249], v172 offset:0\n" \
    "s_waitcnt lgkmcnt(5)\n" \
    "v_mfma_f32_16x16x32_bf16 v[208:211], v[0:3], v[184:187], 0\n" \
    "ds_read_b128 v[204:207], v168 offset:5120\n" \
    "v_mfma_f32_16x16x32_bf16 v[212:215], v[80:83], v[184:187], 0\n" \
    "s_waitcnt lgkmcnt(5)\n" \
    "v_mfma_f32_16x16x32_bf16 v[208:211], v[4:7], v[188:191], v[208:211]\n" \
    "ds_read_b128 v[184:187], v168 offset:6144\n" \
    "v_mfma_f32_16x16x32_bf16 v[212:215], v[84:87], v[188:191], v[212:215]\n" \
    "s_mul_i32 s39, s32, 98304\n" \
    "s_add_u32 s39, s39, 0\n" \
    "s_waitcnt lgkmcnt(5)\n" \
    "v_mfma_f32_16x16x32_bf16 v[208:211], v[8:11], v[192:195], v[208:211]\n" \
    "ds_read_b128 v[188:191], v168 offset:7168\n" \
    "v_mfma_f32_16x16x32_bf16 v[212:215], v[88:91], v[192:195], v[212:215]\n" \
    "s_add_u32 s40, s39, %[pdist]*98304\n" \
    "s_cmp_lt_u32 s32, 64-%[pdist]\n" \
    "s_cselect_b32 s40, s40, 0x80000000\n" \
    "s_waitcnt lgkmcnt(5)\n" \
    "v_mfma_f32_16x16x32_bf16 v[208:211], v[12:15], v[196:199], v[208:211]\n" \
    "ds_read_b128 v[192:195], v168 offset:8192\n" \
    "v_mfma_f32_16x16x32_bf16 v[212:215], v[92:95], v[196:199], v[212:215]\n" \
    "s_waitcnt lgkmcnt(5)\n" \
    "v_mfma_f32_16x16x32_bf16 v[208:211], v[16:19], v[200:203], v[208:211]\n" \
    "ds_read_b128 v[196:199], v168 offset:9216\n" \
    "v_mfma_f32_16x16x32_bf16 v[212:215], v[96:99], v[200:203], v[212:215]\n" \
    "s_waitcnt lgkmcnt(4)\n" \
    "v_mfma_f32_16x16x32_bf16 v[208:211], v[20:23], v[204:207], v[208:211]\n" \
    "ds_read_b128 v[200:203], v168 offset:10240\n" \
    "v_mfma_f32_16x16x32_bf16 v[212:215], v[100:103], v[204:207], v[212:215]\n" \
    "v_permlane16_swap_b32 v248, v249\n" \
    "s_waitcnt lgkmcnt(4)\n" \
    "v_mfma_f32_16x16x32_bf16 v[208:211], v[24:27], v[184:187], v[208:211]\n" \
    "ds_read_b128 v[204:207], v168 offset:11264\n" \
    "v_mfma_f32_16x16x32_bf16 v[212:215], v[104:107], v[184:187], v[212:215]\n" \
    "v_add_f32 v250, v248, v249\n" \
    "s_waitcnt lgkmcnt(4)\n" \
    "v_mfma_f32_16x16x32_bf16 v[208:211], v[28:31], v[188:191], v[208:211]\n" \
    "ds_read_b128 v[184:187], v168 offset:12288\n" \
    "v_mfma_f32_16x16x32_bf16 v[212:215], v[108:111], v[188:191], v[212:215]\n" \
    "v_add_f32 v248, v248, v249\n" \
    "s_waitcnt lgkmcnt(4)\n" \
    "v_mfma_f32_16x16x32_bf16 v[208:211], v[32:35], v[192:195], v[208:211]\n" \
    "ds_read_b128 v[188:191], v168 offset:13312\n" \
    "v_mfma_f32_16x16x32_bf16 v[212:215], v[112:115], v[192:195], v[212:215]\n" \
    "v_permlane32_swap_b32 v248, v250\n" \
    "s_waitcnt lgkmcnt(4)\n" \
    "v_mfma_f32_16x16x32_bf16 v[208:211], v[36:39], v[196:199], v[208:211]\n" \
    "ds_read_b128 v[192:195], v168 offset:14336\n" \
    "v_mfma_f32_16x16x32_bf16 v[212:215], v[116:119], v[196:199], v[212:215]\n" \
    "v_add_f32 v249, v248, v250\n" \
    "s_waitcnt lgkmcnt(4)\n" \
    "v_mfma_f32_16x16x32_bf16 v[208:211], v[40:43], v[200:203], v[208:211]\n" \
    "ds_read_b128 v[196:199], v168 offset:15360\n" \
    "v_mfma_f32_16x16x32_bf16 v[212:215], v[120:123], v[200:203], v[212:215]\n" \
    "v_add_f32 v248, v248, v250\n" \
    "s_add_u32 s48, s32, 3\n" \
    "s_min_u32 s48, s48, 64\n" \
    "s_and_b32 s49, s48, 1\n" \
    "s_xor_b32 s49, s49, 1\n" \
    "s_lshl_b32 s49, s49, 9\n" \
    "s_add_u32 s49, s49, s18\n" \
    "buffer_load_dwordx2 v[176:177], off, s[8:11], s49 sc1\n" \
    "s_waitcnt lgkmcnt(4)\n" \
    "v_mfma_f32_16x16x32_bf16 v[208:211], v[44:47], v[204:207], v[208:211]\n" \
    "ds_read_b128 v[200:203], v168 offset:16384\n" \
    "v_mfma_f32_16x16x32_bf16 v[212:215], v[124:127], v[204:207], v[212:215]\n" \
    "v_permlane16_swap_b32 v248, v249\n" \
    "s_waitcnt lgkmcnt(4)\n" \
    "v_mfma_f32_16x16x32_bf16 v[208:211], v[48:51], v[184:187], v[208:211]\n" \
    "ds_read_b128 v[204:207], v168 offset:17408\n" \
    "v_mfma_f32_16x16x32_bf16 v[212:215], v[128:131], v[184:187], v[212:215]\n" \
    "v_mul_f32 v252, 0x3acccccd, v248\n" \
    "s_waitcnt lgkmcnt(4)\n" \
    "v_mfma_f32_16x16x32_bf16 v[208:211], v[52:55], v[188:191], v[208:211]\n" \
    "ds_read_b128 v[184:187], v168 offset:18432\n" \
    "v_mfma_f32_16x16x32_bf16 v[212:215], v[132:135], v[188:191], v[212:215]\n" \
    "v_mul_f32 v249, 0x3acccccd, v249\n" \
    "s_waitcnt lgkmcnt(4)\n" \
    "v_mfma_f32_16x16x32_bf16 v[208:211], v[56:59], v[192:195], v[208:211]\n" \
    "ds_read_b128 v[188:191], v168 offset:19456\n" \
    "v_mfma_f32_16x16x32_bf16 v[212:215], v[136:139], v[192:195], v[212:215]\n" \
    "v_fma_f32 v249, -v252, v252, v249\n" \
    "s_waitcnt lgkmcnt(4)\n" \
    "v_mfma_f32_16x16x32_bf16 v[208:211], v[60:63], v[196:199], v[208:211]\n" \
    "v_mfma_f32_16x16x32_bf16 v[212:215], v[140:143], v[196:199], v[212:215]\n" \
    "v_max_f32 v249, 0, v249\n" \
    "s_waitcnt lgkmcnt(3)\n" \
    "v_mfma_f32_16x16x32_bf16 v[208:211], v[64:67], v[200:203], v[208:211]\n" \
    "v_mfma_f32_16x16x32_bf16 v[212:215], v[144:147], v[200:203], v[212:215]\n" \
    "v_add_f32 v249, 0x3727c5ac, v249\n" \
    "s_waitcnt lgkmcnt(2)\n" \
    "v_mfma_f32_16x16x32_bf16 v[208:211], v[68:71], v[204:207], v[208:211]\n" \
    "v_mfma_f32_16x16x32_bf16 v[212:215], v[148:151], v[204:207], v[212:215]\n" \
    "v_rsq_f32 v253, v249\n" \
    "s_waitcnt lgkmcnt(1)\n" \
    "v_mfma_f32_16x16x32_bf16 v[208:211], v[72:75], v[184:187], v[208:211]\n" \
    "v_mfma_f32_16x16x32_bf16 v[212:215], v[152:155], v[184:187], v[212:215]\n" \
    "v_mul_f32 v251, 0xbfb8aa3b, v253\n" \
    "s_waitcnt lgkmcnt(0)\n" \
    "v_mfma_f32_16x16x32_bf16 v[208:211], v[76:79], v[188:191], v[208:211]\n" \
    "v_mfma_f32_16x16x32_bf16 v[212:215], v[156:159], v[188:191], v[212:215]\n" \
    "s_nop 6\n" \
    "v_fma_f32 v208, -v252, v160, v208\n" \
    "v_fma_f32 v209, -v252, v161, v209\n" \
    "v_fma_f32 v210, -v252, v162, v210\n" \
    "v_fma_f32 v211, -v252, v163, v211\n" \
    "v_mul_f32 v208, v251, v208\n" \
    "v_mul_f32 v209, v251, v209\n" \
    "v_mul_f32 v210, v251, v210\n" \
    "v_mul_f32 v211, v251, v211\n" \
    "v_exp_f32 v208, v208\n" \
    "v_fma_f32 v212, -v252, v164, v212\n" \
    "v_exp_f32 v209, v209\n" \
    "v_fma_f32 v213, -v252, v165, v213\n" \
    "v_exp_f32 v210, v210\n" \
    "v_fma_f32 v214, -v252, v166, v214\n" \
    "v_exp_f32 v211, v211\n" \
    "v_fma_f32 v215, -v252, v167, v215\n" \
    "v_add_f32 v208, 1.0, v208\n" \
    "v_add_f32 v209, 1.0, v209\n" \
    "v_add_f32 v210, 1.0, v210\n" \
    "v_add_f32 v211, 1.0, v211\n" \
    "v_rcp_f32 v208, v208\n" \
    "v_mul_f32 v212, v253, v212\n" \
    "v_rcp_f32 v209, v209\n" \
    "v_mul_f32 v213, v253, v213\n" \
    "v_rcp_f32 v210, v210\n" \
    "v_mul_f32 v214, v253, v214\n" \
    "v_rcp_f32 v211, v211\n" \
    "v_mul_f32 v215, v253, v215\n" \
    "v_mul_f32 v208, v208, v212\n" \
    "v_mul_f32 v209, v209, v213\n" \
    "v_mul_f32 v210, v210, v214\n" \
    "v_mul_f32 v211, v211, v215\n" \
    "v_cvt_pk_bf16_f32 v236, v208, v209\n" \
    "v_cvt_pk_bf16_f32 v237, v210, v211\n" \
    "v_min_u32 v236, -2, v236\n" \
    "v_min_u32 v237, -2, v237\n" \
    "buffer_store_dwordx2 v[236:237], v173, s[8:11], s39 offen sc1\n" \
    "buffer_store_dwordx2 v[182:183], v173, s[8:11], s40 offen sc1\n" \
    "s_memrealtime s[50:51]\n" \
    "s_waitcnt vmcnt(2)\n" \
    "v_readfirstlane_b32 s36, v177\n" \
    "v_readfirstlane_b32 s35, v176\n" \
    "s_waitcnt lgkmcnt(0)\n" \
    "s_sub_u32 s35, s50, s35\n" \
    "s_cmp_ge_i32 s35, %[dx]\n" \
    "s_addc_u32 s36, s36, 0\n" \
    "s_cmp_lt_u32 s32, 62\n" \
    "s_cselect_b32 s36, s36, 0\n" \
    "s_cmp_gt_i32 s36, s48\n" \
    "s_cselect_b32 s42, 1, 0\n" \
    "s_cbranch_scc0 .Lup_d1_%=\n" \
    "s_add_u32 s36, s32, 1\n" \
    "s_mul_i32 s41, s36, 40960\n" \
    "s_add_u32 s41, s41, s17\n" \
    "buffer_load_dwordx4 v[216:219], v168, s[8:11], s41 offen sc1\n" \
    "s_add_u32 s41, s41, 4096\n" \
    "buffer_load_dwordx4 v[220:223], v168, s[8:11], s41 offen sc1\n" \
    "s_add_u32 s41, s41, 4096\n" \
    "buffer_load_dwordx4 v[224:227], v168, s[8:11], s41 offen sc1\n" \
    "s_add_u32 s41, s41, 4096\n" \
    "buffer_load_dwordx4 v[228:231], v168, s[8:11], s41 offen sc1\n" \
    "s_add_u32 s41, s41, 4096\n" \
    "buffer_load_dwordx4 v[232:235], v168, s[8:11], s41 offen sc1\n" \
    ".Lup_d1_%=:\n" \
    "s_waitcnt lgkmcnt(0)\n" \
    "s_barrier\n" \
    "ds_read_b128 v[184:187], v168 offset:20480\n" \
    "ds_read_b128 v[188:191], v168 offset:21504\n" \
    "ds_read_b128 v[192:195], v168 offset:22528\n" \
    "ds_read_b128 v[196:199], v168 offset:23552\n" \
    "ds_read_b128 v[200:203], v168 offset:24576\n" \
    "ds_read_b64 v[248:249], v172 offset:1024\n" \
    "s_waitcnt lgkmcnt(5)\n" \
    "v_mfma_f32_16x16x32_bf16 v[208:211], v[0:3], v[184:187], 0\n" \
    "ds_read_b128 v[204:207], v168 offset:25600\n" \
    "v_mfma_f32_16x16x32_bf16 v[212:215], v[80:83], v[184:187], 0\n" \
    "s_waitcnt lgkmcnt(5)\n" \
    "v_mfma_f32_16x16x32_bf16 v[208:211], v[4:7], v[188:191], v[208:211]\n" \
    "ds_read_b128 v[184:187], v168 offset:26624\n" \
    "v_mfma_f32_16x16x32_bf16 v[212:215], v[84:87], v[188:191], v[212:215]\n" \
    "s_mul_i32 s39, s33, 98304\n" \
    "s_add_u32 s39, s39, 6291456\n" \
    "s_waitcnt lgkmcnt(5)\n" \
    "v_mfma_f32_16x16x32_bf16 v[208:211], v[8:11], v[192:195], v[208:211]\n" \
    "ds_read_b128 v[188:191], v168 offset:27648\n" \
    "v_mfma_f32_16x16x32_bf16 v[212:215], v[88:91], v[192:195], v[212:215]\n" \
    "s_add_u32 s40, s39, %[pdist]*98304\n" \
    "s_cmp_lt_u32 s33, 64-%[pdist]\n" \
    "s_cselect_b32 s40, s40, 0x80000000\n" \
    "s_waitcnt lgkmcnt(5)\n" \
    "v_mfma_f32_16x16x32_bf16 v[208:211], v[12:15], v[196:199], v[208:211]\n" \
    "ds_read_b128 v[192:195], v168 offset:28672\n" \
    "v_mfma_f32_16x16x32_bf16 v[212:215], v[92:95], v[196:199], v[212:215]\n" \
    "s_waitcnt lgkmcnt(5)\n" \
    "v_mfma_f32_16x16x32_bf16 v[208:211], v[16:19], v[200:203], v[208:211]\n" \
    "ds_read_b128 v[196:199], v168 offset:29696\n" \
    "v_mfma_f32_16x16x32_bf16 v[212:215], v[96:99], v[200:203], v[212:215]\n" \
    "s_waitcnt lgkmcnt(4)\n" \
    "v_mfma_f32_16x16x32_bf16 v[208:211], v[20:23], v[204:207], v[208:211]\n" \
    "ds_read_b128 v[200:203], v168 offset:30720\n" \
    "v_mfma_f32_16x16x32_bf16 v[212:215], v[100:103], v[204:207], v[212:215]\n" \
    "v_permlane16_swap_b32 v248, v249\n" \
    "s_waitcnt lgkmcnt(4)\n" \
    "v_mfma_f32_16x16x32_bf16 v[208:211], v[24:27], v[184:187], v[208:211]\n" \
    "ds_read_b128 v[204:207], v168 offset:31744\n" \
    "v_mfma_f32_16x16x32_bf16 v[212:215], v[104:107], v[184:187], v[212:215]\n" \
    "v_add_f32 v250, v248, v249\n" \
    "s_waitcnt lgkmcnt(4)\n" \
    "v_mfma_f32_16x16x32_bf16 v[208:211], v[28:31], v[188:191], v[208:211]\n" \
    "ds_read_b128 v[184:187], v168 offset:32768\n" \
    "v_mfma_f32_16x16x32_bf16 v[212:215], v[108:111], v[188:191], v[212:215]\n" \
    "v_add_f32 v248, v248, v249\n" \
    "s_waitcnt lgkmcnt(4)\n" \
    "v_mfma_f32_16x16x32_bf16 v[208:211], v[32:35], v[192:195], v[208:211]\n" \
    "ds_read_b128 v[188:191], v168 offset:33792\n" \
    "v_mfma_f32_16x16x32_bf16 v[212:215], v[112:115], v[192:195], v[212:215]\n" \
    "v_permlane32_swap_b32 v248, v250\n" \
    "s_waitcnt lgkmcnt(4)\n" \
    "v_mfma_f32_16x16x32_bf16 v[208:211], v[36:39], v[196:199], v[208:211]\n" \
    "ds_read_b128 v[192:195], v168 offset:34816\n" \
    "v_mfma_f32_16x16x32_bf16 v[212:215], v[116:119], v[196:199], v[212:215]\n" \
    "v_add_f32 v249, v248, v250\n" \
    "s_waitcnt lgkmcnt(4)\n" \
    "v_mfma_f32_16x16x32_bf16 v[208:211], v[40:43], v[200:203], v[208:211]\n" \
    "ds_read_b128 v[196:199], v168 offset:35840\n" \
    "v_mfma_f32_16x16x32_bf16 v[212:215], v[120:123], v[200:203], v[212:215]\n" \
    "v_add_f32 v248, v248, v250\n" \
    "s_add_u32 s48, s32, 3\n" \
    "s_min_u32 s48, s48, 64\n" \
    "s_and_b32 s49, s48, 1\n" \
    "s_xor_b32 s49, s49, 1\n" \
    "s_lshl_b32 s49, s49, 9\n" \
    "s_add_u32 s49, s49, s18\n" \
    "buffer_load_dwordx2 v[176:177], off, s[8:11], s49 sc1\n" \
    "s_waitcnt lgkmcnt(4)\n" \
    "v_mfma_f32_16x16x32_bf16 v[208:211], v[44:47], v[204:207], v[208:211]\n" \
    "ds_read_b128 v[200:203], v168 offset:36864\n" \
    "v_mfma_f32_16x16x32_bf16 v[212:215], v[124:127], v[204:207], v[212:215]\n" \
    "v_permlane16_swap_b32 v248, v249\n" \
    "s_waitcnt lgkmcnt(4)\n" \
    "v_mfma_f32_16x16x32_bf16 v[208:211], v[48:51], v[184:187], v[208:211]\n" \
    "ds_read_b128 v[204:207], v168 offset:37888\n" \
    "v_mfma_f32_16x16x32_bf16 v[212:215], v[128:131], v[184:187], v[212:215]\n" \
    "v_mul_f32 v252, 0x3acccccd, v248\n" \
    "s_waitcnt lgkmcnt(4)\n" \
    "v_mfma_f32_16x16x32_bf16 v[208:211], v[52:55], v[188:191], v[208:211]\n" \
    "ds_read_b128 v[184:187], v168 offset:38912\n" \
    "v_mfma_f32_16x16x32_bf16 v[212:215], v[132:135], v[188:191], v[212:215]\n" \
    "v_mul_f32 v249, 0x3acccccd, v249\n" \
    "s_waitcnt lgkmcnt(4)\n" \
    "v_mfma_f32_16x16x32_bf16 v[208:211], v[56:59], v[192:195], v[208:211]\n" \
    "ds_read_b128 v[188:191], v168 offset:39936\n" \
    "v_mfma_f32_16x16x32_bf16 v[212:215], v[136:139], v[192:195], v[212:215]\n" \
    "v_fma_f32 v249, -v252, v252, v249\n" \
    "s_waitcnt lgkmcnt(4)\n" \
    "v_mfma_f32_16x16x32_bf16 v[208:211], v[60:63], v[196:199], v[208:211]\n" \
    "v_mfma_f32_16x16x32_bf16 v[212:215], v[140:143], v[196:199], v[212:215]\n" \
    "v_max_f32 v249, 0, v249\n" \
    "s_waitcnt lgkmcnt(3)\n" \
    "v_mfma_f32_16x16x32_bf16 v[208:211], v[64:67], v[200:203], v[208:211]\n" \
    "v_mfma_f32_16x16x32_bf16 v[212:215], v[144:147], v[200:203], v[212:215]\n" \
    "v_add_f32 v249, 0x3727c5ac, v249\n" \
    "s_waitcnt lgkmcnt(2)\n" \
    "v_mfma_f32_16x16x32_bf16 v[208:211], v[68:71], v[204:207], v[208:211]\n" \
    "v_mfma_f32_16x16x32_bf16 v[212:215], v[148:151], v[204:207], v[212:215]\n" \
    "v_rsq_f32 v253, v249\n" \
    "s_waitcnt lgkmcnt(1)\n" \
    "v_mfma_f32_16x16x32_bf16 v[208:211], v[72:75], v[184:187], v[208:211]\n" \
    "v_mfma_f32_16x16x32_bf16 v[212:215], v[152:155], v[184:187], v[212:215]\n" \
    "v_mul_f32 v251, 0xbfb8aa3b, v253\n" \
    "s_waitcnt lgkmcnt(0)\n" \
    "v_mfma_f32_16x16x32_bf16 v[208:211], v[76:79], v[188:191], v[208:211]\n" \
    "v_mfma_f32_16x16x32_bf16 v[212:215], v[156:159], v[188:191], v[212:215]\n" \
    "s_nop 6\n" \
    "v_fma_f32 v208, -v252, v160, v208\n" \
    "v_fma_f32 v209, -v252, v161, v209\n" \
    "v_fma_f32 v210, -v252, v162, v210\n" \
    "v_fma_f32 v211, -v252, v163, v211\n" \
    "v_mul_f32 v208, v251, v208\n" \
    "v_mul_f32 v209, v251, v209\n" \
    "v_mul_f32 v210, v251, v210\n" \
    "v_mul_f32 v211, v251, v211\n" \
    "v_exp_f32 v208, v208\n" \
    "v_fma_f32 v212, -v252, v164, v212\n" \
    "v_exp_f32 v209, v209\n" \
    "v_fma_f32 v213, -v252, v165, v213\n" \
    "v_exp_f32 v210, v210\n" \
    "v_fma_f32 v214, -v252, v166, v214\n" \
    "v_exp_f32 v211, v211\n" \
    "v_fma_f32 v215, -v252, v167, v215\n" \
    "v_add_f32 v208, 1.0, v208\n" \
    "v_add_f32 v209, 1.0, v209\n" \
    "v_add_f32 v210, 1.0, v210\n" \
    "v_add_f32 v211, 1.0, v211\n" \
    "v_rcp_f32 v208, v208\n" \
    "v_mul_f32 v212, v253, v212\n" \
    "v_rcp_f32 v209, v209\n" \
    "v_mul_f32 v213, v253, v213\n" \
    "v_rcp_f32 v210, v210\n" \
    "v_mul_f32 v214, v253, v214\n" \
    "v_rcp_f32 v211, v211\n" \
    "v_mul_f32 v215, v253, v215\n" \
    "v_mul_f32 v208, v208, v212\n" \
    "v_mul_f32 v209, v209, v213\n" \
    "v_mul_f32 v210, v210, v214\n" \
    "v_mul_f32 v211, v211, v215\n" \
    "v_cvt_pk_bf16_f32 v236, v208, v209\n" \
    "v_cvt_pk_bf16_f32 v237, v210, v211\n" \
    "v_min_u32 v236, -2, v236\n" \
    "v_min_u32 v237, -2, v237\n" \
    "buffer_store_dwordx2 v[236:237], v173, s[8:11], s39 offen sc1\n" \
    "buffer_store_dwordx2 v[182:183], v173, s[8:11], s40 offen sc1\n" \
    "s_add_u32 s33, s33, 1\n" \
    "s_add_u32 s32, s32, 1\n" \
    "s_cmp_lt_u32 s32, 64\n" \
    "s_cbranch_scc1 .Lup_mO_%=\n" \
    "s_add_u32 s18, s18, 1024\n" \
    "s_waitcnt vmcnt(0)\n" \
    "s_barrier\n" \
    "s_add_u32 s48, s33, 2\n" \
    "s_min_u32 s48, s48, 64\n" \
    "s_and_b32 s49, s48, 1\n" \
    "s_xor_b32 s49, s49, 1\n" \
    "s_lshl_b32 s49, s49, 9\n" \
    "s_add_u32 s49, s49, s18\n" \
    "buffer_load_dwordx2 v[176:177], off, s[8:11], s49 sc1\n" \
    "s_waitcnt vmcnt(0)\n" \
    ".Lup_nO_%=:\n" \
    "s_mov_b32 s37, 0\n" \
    "s_waitcnt vmcnt(2)\n" \
    ".Lup_Odp_%=:\n" \
    "v_readfirstlane_b32 s36, v177\n" \
    "s_nop 0\n" \
    "s_cmp_gt_i32 s36, s48\n" \
    "s_cbranch_scc1 .Lup_Odx_%=\n" \
    "s_cmp_eq_u32 s36, s48\n" \
    "s_cbranch_scc1 .Lup_Odw_%=\n" \
    "s_sleep 3\n" \
    "buffer_load_dwordx2 v[176:177], off, s[8:11], s49 sc1\n" \
    "s_waitcnt vmcnt(0)\n" \
    "s_add_u32 s37, s37, 1\n" \
    "s_cmp_lt_u32 s37, s38\n" \
    "s_cbranch_scc1 .Lup_Odp_%=\n" \
    "s_mov_b32 s38, 0\n" \
    "s_branch .Lup_Odx_%=\n" \
    ".Lup_Odw_%=:\n" \
    "v_readfirstlane_b32 s36, v176\n" \
    "s_cmp_eq_u32 s33, 63\n" \
    "s_cselect_b32 s34, %[dlast], 0\n" \
    "s_add_u32 s36, s36, s34\n" \
    "s_add_u32 s36, s36, %[dx]\n" \
    "s_branch .Lup_Odtb_%=\n" \
    ".Lup_Odta_%=:\n" \
    "s_sleep 1\n" \
    ".Lup_Odtb_%=:\n" \
    "s_memrealtime vcc\n" \
    "s_waitcnt lgkmcnt(0)\n" \
    "s_sub_i32 vcc_lo, s36, vcc_lo\n" \
    "s_cmp_gt_i32 vcc_lo, 8\n" \
    "s_cbranch_scc1 .Lup_Odta_%=\n" \
    "s_cmp_gt_i32 vcc_lo, 0\n" \
    "s_cbranch_scc1 .Lup_Odtb_%=\n" \
    ".Lup_Odx_%=:\n" \
    "s_mul_i32 s41, s33, 40960\n" \
    "s_add_u32 s41, s41, s17\n" \
    "s_add_u32 s41, s41, 2621440\n" \
    "buffer_load_dwordx4 v[216:219], v168, s[8:11], s41 offen sc1\n" \
    "s_add_u32 s41, s41, 4096\n" \
    "buffer_load_dwordx4 v[220:223], v168, s[8:11], s41 offen sc1\n" \
    "s_add_u32 s41, s41, 4096\n" \
    "buffer_load_dwordx4 v[224:227], v168, s[8:11], s41 offen sc1\n" \
    "s_add_u32 s41, s41, 4096\n" \
    "buffer_load_dwordx4 v[228:231], v168, s[8:11], s41 offen sc1\n" \
    "s_add_u32 s41, s41, 4096\n" \
    "buffer_load_dwordx4 v[232:235], v168, s[8:11], s41 offen sc1\n" \
    "s_mov_b32 s37, 0\n" \
    ".Lup_srOd_%=:\n" \
    "s_waitcnt vmcnt(4)\n" \
    "v_mfma_f32_16x16x32_bf16 v[244:247], v[216:219], v[216:219], 0\n" \
    "ds_write_b128 v169, v[216:219] offset:20480\n" \
    "v_max3_u32 v254, v216, v217, v218\n" \
    "v_mfma_f32_16x16x32_bf16 v[240:243], v[178:181], v[216:219], 0\n" \
    "v_max_u32 v254, v254, v219\n" \
    "s_waitcnt vmcnt(3)\n" \
    "v_mfma_f32_16x16x32_bf16 v[244:247], v[220:223], v[220:223], v[244:247]\n" \
    "ds_write_b128 v169, v[220:223] offset:24576\n" \
    "v_max3_u32 v254, v254, v220, v221\n" \
    "v_mfma_f32_16x16x32_bf16 v[240:243], v[178:181], v[220:223], v[240:243]\n" \
    "v_max3_u32 v254, v254, v222, v223\n" \
    "s_waitcnt vmcnt(2)\n" \
    "v_mfma_f32_16x16x32_bf16 v[244:247], v[224:227], v[224:227], v[244:247]\n" \
    "ds_write_b128 v169, v[224:227] offset:28672\n" \
    "v_max3_u32 v254, v254, v224, v225\n" \
    "v_mfma_f32_16x16x32_bf16 v[240:243], v[178:181], v[224:227], v[240:243]\n" \
    "v_max3_u32 v254, v254, v226, v227\n" \
    "s_waitcnt vmcnt(1)\n" \
    "v_mfma_f32_16x16x32_bf16 v[244:247], v[228:231], v[228:231], v[244:247]\n" \
    "ds_write_b128 v169, v[228:231] offset:32768\n" \
    "v_max3_u32 v254, v254, v228, v229\n" \
    "v_mfma_f32_16x16x32_bf16 v[240:243], v[178:181], v[228:231], v[240:243]\n" \
    "v_max3_u32 v254, v254, v230, v231\n" \
    "s_waitcnt vmcnt(0)\n" \
    "v_mfma_f32_16x16x32_bf16 v[244:247], v[232:235], v[232:235], v[244:247]\n" \
    "ds_write_b128 v169, v[232:235] offset:36864\n" \
    "v_max3_u32 v254, v254, v232, v233\n" \
    "v_mfma_f32_16x16x32_bf16 v[240:243], v[178:181], v[232:235], v[240:243]\n" \
    "v_max3_u32 v254, v254, v234, v235\n" \
    "v_cmp_eq_u32 vcc, -1, v254\n" \
    "s_nop 0\n" \
    "s_cbranch_vccnz .Lup_scOd_%=\n" \
    ".Lup_sxOd_%=:\n" \
    "s_nop 0\n" \
    "v_cndmask_b32 v254, v244, v245, s[44:45]\n" \
    "v_cndmask_b32 v255, v246, v247, s[44:45]\n" \
    "s_nop 0\n" \
    "v_cndmask_b32 v241, v254, v255, s[46:47]\n" \
    "ds_write_b64 v171, v[240:241] offset:1024\n" \
    "s_waitcnt lgkmcnt(0)\n" \
    "s_barrier\n" \
    "ds_read_b128 v[184:187], v168 offset:20480\n" \
    "ds_read_b128 v[188:191], v168 offset:21504\n" \
    "ds_read_b128 v[192:195], v168 offset:22528\n" \
    "ds_read_b128 v[196:199], v168 offset:23552\n" \
    "ds_read_b128 v[200:203], v168 offset:24576\n" \
    "ds_read_b64 v[248:249], v172 offset:1024\n" \
    "s_waitcnt lgkmcnt(5)\n" \
    "v_mfma_f32_16x16x32_bf16 v[208:211], v[0:3], v[184:187], 0\n" \
    "ds_read_b128 v[204:207], v168 offset:25600\n" \
    "v_mfma_f32_16x16x32_bf16 v[212:215], v[80:83], v[184:187], 0\n" \
    "s_waitcnt lgkmcnt(5)\n" \
    "v_mfma_f32_16x16x32_bf16 v[208:211], v[4:7], v[188:191], v[208:211]\n" \
    "ds_read_b128 v[184:187], v168 offset:26624\n" \
    "v_mfma_f32_16x16x32_bf16 v[212:215], v[84:87], v[188:191], v[212:215]\n" \
    "s_mul_i32 s39, s33, 98304\n" \
    "s_add_u32 s39, s39, 6291456\n" \
    "s_waitcnt lgkmcnt(5)\n" \
    "v_mfma_f32_16x16x32_bf16 v[208:211], v[8:11], v[192:195], v[208:211]\n" \
    "ds_read_b128 v[188:191], v168 offset:27648\n" \
    "v_mfma_f32_16x16x32_bf16 v[212:215], v[88:91], v[192:195], v[212:215]\n" \
    "s_add_u32 s40, s39, %[pdist]*98304\n" \
    "s_cmp_lt_u32 s33, 64-%[pdist]\n" \
    "s_cselect_b32 s40, s40, 0x80000000\n" \
    "s_waitcnt lgkmcnt(5)\n" \
    "v_mfma_f32_16x16x32_bf16 v[208:211], v[12:15], v[196:199], v[208:211]\n" \
    "ds_read_b128 v[192:195], v168 offset:28672\n" \
    "v_mfma_f32_16x16x32_bf16 v[212:215], v[92:95], v[196:199], v[212:215]\n" \
    "s_waitcnt lgkmcnt(5)\n" \
    "v_mfma_f32_16x16x32_bf16 v[208:211], v[16:19], v[200:203], v[208:211]\n" \
    "ds_read_b128 v[196:199], v168 offset:29696\n" \
    "v_mfma_f32_16x16x32_bf16 v[212:215], v[96:99], v[200:203], v[212:215]\n" \
    "s_waitcnt lgkmcnt(4)\n" \
    "v_mfma_f32_16x16x32_bf16 v[208:211], v[20:23], v[204:207], v[208:211]\n" \
    "ds_read_b128 v[200:203], v168 offset:30720\n" \
    "v_mfma_f32_16x16x32_bf16 v[212:215], v[100:103], v[204:207], v[212:215]\n" \
    "v_permlane16_swap_b32 v248, v249\n" \
    "s_waitcnt lgkmcnt(4)\n" \
    "v_mfma_f32_16x16x32_bf16 v[208:211], v[24:27], v[184:187], v[208:211]\n" \
    "ds_read_b128 v[204:207], v168 offset:31744\n" \
    "v_mfma_f32_16x16x32_bf16 v[212:215], v[104:107], v[184:187], v[212:215]\n" \
    "v_add_f32 v250, v248, v249\n" \
    "s_waitcnt lgkmcnt(4)\n" \
    "v_mfma_f32_16x16x32_bf16 v[208:211], v[28:31], v[188:191], v[208:211]\n" \
    "ds_read_b128 v[184:187], v168 offset:32768\n" \
    "v_mfma_f32_16x16x32_bf16 v[212:215], v[108:111], v[188:191], v[212:215]\n" \
    "v_add_f32 v248, v248, v249\n" \
    "s_waitcnt lgkmcnt(4)\n" \
    "v_mfma_f32_16x16x32_bf16 v[208:211], v[32:35], v[192:195], v[208:211]\n" \
    "ds_read_b128 v[188:191], v168 offset:33792\n" \
    "v_mfma_f32_16x16x32_bf16 v[212:215], v[112:115], v[192:195], v[212:215]\n" \
    "v_permlane32_swap_b32 v248, v250\n" \
    "s_waitcnt lgkmcnt(4)\n" \
    "v_mfma_f32_16x16x32_bf16 v[208:211], v[36:39], v[196:199], v[208:211]\n" \
    "ds_read_b128 v[192:195], v168 offset:34816\n" \
    "v_mfma_f32_16x16x32_bf16 v[212:215], v[116:119], v[196:199], v[212:215]\n" \
    "v_add_f32 v249, v248, v250\n" \
    "s_waitcnt lgkmcnt(4)\n" \
    "v_mfma_f32_16x16x32_bf16 v[208:211], v[40:43], v[200:203], v[208:211]\n" \
    "ds_read_b128 v[196:199], v168 offset:35840\n" \
    "v_mfma_f32_16x16x32_bf16 v[212:215], v[120:123], v[200:203], v[212:215]\n" \
    "v_add_f32 v248, v248, v250\n" \
    "s_add_u32 s48, s33, 3\n" \
    "s_min_u32 s48, s48, 64\n" \
    "s_and_b32 s49, s48, 1\n" \
    "s_xor_b32 s49, s49, 1\n" \
    "s_lshl_b32 s49, s49, 9\n" \
    "s_add_u32 s49, s49, s18\n" \
    "buffer_load_dwordx2 v[176:177], off, s[8:11], s49 sc1\n" \
    "s_waitcnt lgkmcnt(4)\n" \
    "v_mfma_f32_16x16x32_bf16 v[208:211], v[44:47], v[204:207], v[208:211]\n" \
    "ds_read_b128 v[200:203], v168 offset:36864\n" \
    "v_mfma_f32_16x16x32_bf16 v[212:215], v[124:127], v[204:207], v[212:215]\n" \
    "v_permlane16_swap_b32 v248, v249\n" \
    "s_waitcnt lgkmcnt(4)\n" \
    "v_mfma_f32_16x16x32_bf16 v[208:211], v[48:51], v[184:187], v[208:211]\n" \
    "ds_read_b128 v[204:207], v168 offset:37888\n" \
    "v_mfma_f32_16x16x32_bf16 v[212:215], v[128:131], v[184:187], v[212:215]\n" \
    "v_mul_f32 v252, 0x3acccccd, v248\n" \
    "s_waitcnt lgkmcnt(4)\n" \
    "v_mfma_f32_16x16x32_bf16 v[208:211], v[52:55], v[188:191], v[208:211]\n" \
    "ds_read_b128 v[184:187], v168 offset:38912\n" \
    "v_mfma_f32_16x16x32_bf16 v[212:215], v[132:135], v[188:191], v[212:215]\n" \
    "v_mul_f32 v249, 0x3acccccd, v249\n" \
    "s_waitcnt lgkmcnt(4)\n" \
    "v_mfma_f32_16x16x32_bf16 v[208:211], v[56:59], v[192:195], v[208:211]\n" \
    "ds_read_b128 v[188:191], v168 offset:39936\n" \
    "v_mfma_f32_16x16x32_bf16 v[212:215], v[136:139], v[192:195], v[212:215]\n" \
    "v_fma_f32 v249, -v252, v252, v249\n" \
    "s_waitcnt lgkmcnt(4)\n" \
    "v_mfma_f32_16x16x32_bf16 v[208:211], v[60:63], v[196:199], v[208:211]\n" \
    "v_mfma_f32_16x16x32_bf16 v[212:215], v[140:143], v[196:199], v[212:215]\n" \
    "v_max_f32 v249, 0, v249\n" \
    "s_waitcnt lgkmcnt(3)\n" \
    "v_mfma_f32_16x16x32_bf16 v[208:211], v[64:67], v[200:203], v[208:211]\n" \
    "v_mfma_f32_16x16x32_bf16 v[212:215], v[144:147], v[200:203], v[212:215]\n" \
    "v_add_f32 v249, 0x3727c5ac, v249\n" \
    "s_waitcnt lgkmcnt(2)\n" \
    "v_mfma_f32_16x16x32_bf16 v[208:211], v[68:71], v[204:207], v[208:211]\n" \
    "v_mfma_f32_16x16x32_bf16 v[212:215], v[148:151], v[204:207], v[212:215]\n" \
    "v_rsq_f32 v253, v249\n" \
    "s_waitcnt lgkmcnt(1)\n" \
    "v_mfma_f32_16x16x32_bf16 v[208:211], v[72:75], v[184:187], v[208:211]\n" \
    "v_mfma_f32_16x16x32_bf16 v[212:215], v[152:155], v[184:187], v[212:215]\n" \
    "v_mul_f32 v251, 0xbfb8aa3b, v253\n" \
    "s_waitcnt lgkmcnt(0)\n" \
    "v_mfma_f32_16x16x32_bf16 v[208:211], v[76:79], v[188:191], v[208:211]\n" \
    "v_mfma_f32_16x16x32_bf16 v[212:215], v[156:159], v[188:191], v[212:215]\n" \
    "s_nop 6\n" \
    "v_fma_f32 v208, -v252, v160, v208\n" \
    "v_fma_f32 v209, -v252, v161, v209\n" \
    "v_fma_f32 v210, -v252, v162, v210\n" \
    "v_fma_f32 v211, -v252, v163, v211\n" \
    "v_mul_f32 v208, v251, v208\n" \
    "v_mul_f32 v209, v251, v209\n" \
    "v_mul_f32 v210, v251, v210\n" \
    "v_mul_f32 v211, v251, v211\n" \
    "v_exp_f32 v208, v208\n" \
    "v_fma_f32 v212, -v252, v164, v212\n" \
    "v_exp_f32 v209, v209\n" \
    "v_fma_f32 v213, -v252, v165, v213\n" \
    "v_exp_f32 v210, v210\n" \
    "v_fma_f32 v214, -v252, v166, v214\n" \
    "v_exp_f32 v211, v211\n" \
    "v_fma_f32 v215, -v252, v167, v215\n" \
    "v_add_f32 v208, 1.0, v208\n" \
    "v_add_f32 v209, 1.0, v209\n" \
    "v_add_f32 v210, 1.0, v210\n" \
    "v_add_f32 v211, 1.0, v211\n" \
    "v_rcp_f32 v208, v208\n" \
    "v_mul_f32 v212, v253, v212\n" \
    "v_rcp_f32 v209, v209\n" \
    "v_mul_f32 v213, v253, v213\n" \
    "v_rcp_f32 v210, v210\n" \
    "v_mul_f32 v214, v253, v214\n" \
    "v_rcp_f32 v211, v211\n" \
    "v_mul_f32 v215, v253, v215\n" \
    "v_mul_f32 v208, v208, v212\n" \
    "v_mul_f32 v209, v209, v213\n" \
    "v_mul_f32 v210, v210, v214\n" \
    "v_mul_f32 v211, v211, v215\n" \
    "v_cvt_pk_bf16_f32 v236, v208, v209\n" \
    "v_cvt_pk_bf16_f32 v237, v210, v211\n" \
    "v_min_u32 v236, -2, v236\n" \
    "v_min_u32 v237, -2, v237\n" \
    "buffer_store_dwordx2 v[236:237], v173, s[8:11], s39 offen sc1\n" \
    "buffer_store_dwordx2 v[182:183], v173, s[8:11], s40 offen sc1\n" \
    "s_barrier\n" \
    "s_add_u32 s33, s33, 1\n" \
    "s_cmp_lt_u32 s33, 64\n" \
    "s_cbranch_scc1 .Lup_nO_%=\n" \
    "s_waitcnt vmcnt(0)\n" \
    "s_endpgm\n" \
    ".Lup_scYc_%=:\n" \
    "s_add_u32 s37, s37, 1\n" \
    "s_cmp_lt_u32 s37, s38\n" \
    "s_cselect_b32 s38, s38, 0\n" \
    "s_cbranch_scc0 .Lup_sxYc_%=\n" \
    "s_sleep 3\n" \
    "s_waitcnt lgkmcnt(0)\n" \
    "s_mul_i32 s41, s33, 40960\n" \
    "s_add_u32 s41, s41, s17\n" \
    "s_add_u32 s41, s41, 2621440\n" \
    "buffer_load_dwordx4 v[216:219], v168, s[8:11], s41 offen sc1\n" \
    "s_add_u32 s41, s41, 4096\n" \
    "buffer_load_dwordx4 v[220:223], v168, s[8:11], s41 offen sc1\n" \
    "s_add_u32 s41, s41, 4096\n" \
    "buffer_load_dwordx4 v[224:227], v168, s[8:11], s41 offen sc1\n" \
    "s_add_u32 s41, s41, 4096\n" \
    "buffer_load_dwordx4 v[228:231], v168, s[8:11], s41 offen sc1\n" \
    "s_add_u32 s41, s41, 4096\n" \
    "buffer_load_dwordx4 v[232:235], v168, s[8:11], s41 offen sc1\n" \
    "s_waitcnt vmcnt(0)\n" \
    "s_branch .Lup_srYc_%=\n" \
    ".Lup_scOa_%=:\n" \
    "s_add_u32 s37, s37, 1\n" \
    "s_cmp_lt_u32 s37, s38\n" \
    "s_cselect_b32 s38, s38, 0\n" \
    "s_cbranch_scc0 .Lup_sxOa_%=\n" \
    "s_sleep 3\n" \
    "s_waitcnt lgkmcnt(0)\n" \
    "s_mul_i32 s41, s32, 40960\n" \
    "s_add_u32 s41, s41, s17\n" \
    "buffer_load_dwordx4 v[216:219], v168, s[8:11], s41 offen sc1\n" \
    "s_add_u32 s41, s41, 4096\n" \
    "buffer_load_dwordx4 v[220:223], v168, s[8:11], s41 offen sc1\n" \
    "s_add_u32 s41, s41, 4096\n" \
    "buffer_load_dwordx4 v[224:227], v168, s[8:11], s41 offen sc1\n" \
    "s_add_u32 s41, s41, 4096\n" \
    "buffer_load_dwordx4 v[228:231], v168, s[8:11], s41 offen sc1\n" \
    "s_add_u32 s41, s41, 4096\n" \
    "buffer_load_dwordx4 v[232:235], v168, s[8:11], s41 offen sc1\n" \
    "s_waitcnt vmcnt(0)\n" \
    "s_branch .Lup_srOa_%=\n" \
    ".Lup_scObq_%=:\n" \
    "s_add_u32 s37, s37, 1\n" \
    "s_cmp_lt_u32 s37, s38\n" \
    "s_cselect_b32 s38, s38, 0\n" \
    "s_cbranch_scc0 .Lup_sxObq_%=\n" \
    "s_sleep 3\n" \
    "s_waitcnt lgkmcnt(0)\n" \
    "s_mul_i32 s41, s32, 40960\n" \
    "s_add_u32 s41, s41, s17\n" \
    "buffer_load_dwordx4 v[216:219], v168, s[8:11], s41 offen sc1\n" \
    "s_add_u32 s41, s41, 4096\n" \
    "buffer_load_dwordx4 v[220:223], v168, s[8:11], s41 offen sc1\n" \
    "s_add_u32 s41, s41, 4096\n" \
    "buffer_load_dwordx4 v[224:227], v168, s[8:11], s41 offen sc1\n" \
    "s_add_u32 s41, s41, 4096\n" \
    "buffer_load_dwordx4 v[228:231], v168, s[8:11], s41 offen sc1\n" \
    "s_add_u32 s41, s41, 4096\n" \
    "buffer_load_dwordx4 v[232:235], v168, s[8:11], s41 offen sc1\n" \
    "s_waitcnt vmcnt(0)\n" \
    "s_branch .Lup_srObq_%=\n" \
    ".Lup_scOb_%=:\n" \
    "s_add_u32 s37, s37, 1\n" \
    "s_cmp_lt_u32 s37, s38\n" \
    "s_cselect_b32 s38, s38, 0\n" \
    "s_cbranch_scc0 .Lup_sxOb_%=\n" \
    "s_sleep 3\n" \
    "s_waitcnt lgkmcnt(0)\n" \
    "s_mul_i32 s41, s32, 40960\n" \
    "s_add_u32 s41, s41, s17\n" \
    "buffer_load_dwordx4 v[216:219], v168, s[8:11], s41 offen sc1\n" \
    "s_add_u32 s41, s41, 4096\n" \
    "buffer_load_dwordx4 v[220:223], v168, s[8:11], s41 offen sc1\n" \
    "s_add_u32 s41, s41, 4096\n" \
    "buffer_load_dwordx4 v[224:227], v168, s[8:11], s41 offen sc1\n" \
    "s_add_u32 s41, s41, 4096\n" \
    "buffer_load_dwordx4 v[228:231], v168, s[8:11], s41 offen sc1\n" \
    "s_add_u32 s41, s41, 4096\n" \
    "buffer_load_dwordx4 v[232:235], v168, s[8:11], s41 offen sc1\n" \
    "s_waitcnt vmcnt(0)\n" \
    "s_branch .Lup_srOb_%=\n" \
    ".Lup_scOd_%=:\n" \
    "s_add_u32 s37, s37, 1\n" \
    "s_cmp_lt_u32 s37, s38\n" \
    "s_cselect_b32 s38, s38, 0\n" \
    "s_cbranch_scc0 .Lup_sxOd_%=\n" \
    "s_sleep 3\n" \
    "s_waitcnt lgkmcnt(0)\n" \
    "s_mul_i32 s41, s33, 40960\n" \
    "s_add_u32 s41, s41, s17\n" \
    "s_add_u32 s41, s41, 2621440\n" \
    "buffer_load_dwordx4 v[216:219], v168, s[8:11], s41 offen sc1\n" \
    "s_add_u32 s41, s41, 4096\n" \
    "buffer_load_dwordx4 v[220:223], v168, s[8:11], s41 offen sc1\n" \
    "s_add_u32 s41, s41, 4096\n" \
    "buffer_load_dwordx4 v[224:227], v168, s[8:11], s41 offen sc1\n" \
    "s_add_u32 s41, s41, 4096\n" \
    "buffer_load_dwordx4 v[228:231], v168, s[8:11], s41 offen sc1\n" \
    "s_add_u32 s41, s41, 4096\n" \
    "buffer_load_dwordx4 v[232:235], v168, s[8:11], s41 offen sc1\n" \
    "s_waitcnt vmcnt(0)\n" \
    "s_branch .Lup_srOd_%=\n" \
    ""
#define DN_ASM \
    "s_mov_b32 s32, 0\n" \
    "s_mov_b32 s33, 0\n" \
    "s_mov_b32 s38, 0x40000\n" \
    "v_mov_b32 v236, -1\n" \
    "v_mov_b32 v237, -1\n" \
    "v_mov_b32 v252, v150\n" \
    "s_add_u32 s48, s32, 2\n" \
    "s_min_u32 s48, s48, 64\n" \
    "s_and_b32 s49, s48, 1\n" \
    "s_xor_b32 s49, s49, 1\n" \
    "s_lshl_b32 s49, s49, 9\n" \
    "s_add_u32 s49, s49, s18\n" \
    "buffer_load_dwordx2 v[150:151], off, s[8:11], s49 sc1\n" \
    "s_cmp_gt_u32 s16, 5\n" \
    "s_cbranch_scc1 .Ldn_lN_%=\n" \
    ".Ldn_lF_%=:\n" \
    "s_mov_b32 s37, 0\n" \
    "s_waitcnt vmcnt(0)\n" \
    ".Ldn_Fap_%=:\n" \
    "v_readfirstlane_b32 s36, v151\n" \
    "s_nop 0\n" \
    "s_cmp_gt_i32 s36, s48\n" \
    "s_cbranch_scc1 .Ldn_Fax_%=\n" \
    "s_cmp_eq_u32 s36, s48\n" \
    "s_cbranch_scc1 .Ldn_Faw_%=\n" \
    "s_sleep 3\n" \
    "buffer_load_dwordx2 v[150:151], off, s[8:11], s49 sc1\n" \
    "s_waitcnt vmcnt(0)\n" \
    "s_add_u32 s37, s37, 1\n" \
    "s_cmp_lt_u32 s37, s38\n" \
    "s_cbranch_scc1 .Ldn_Fap_%=\n" \
    "s_mov_b32 s38, 0\n" \
    "s_branch .Ldn_Fax_%=\n" \
    ".Ldn_Faw_%=:\n" \
    "v_readfirstlane_b32 s36, v150\n" \
    "s_cmp_eq_u32 s32, 63\n" \
    "s_cselect_b32 s34, %[dlast], 0\n" \
    "s_add_u32 s36, s36, s34\n" \
    "s_add_u32 s36, s36, %[dxd]\n" \
    "s_branch .Ldn_Fatb_%=\n" \
    ".Ldn_Fata_%=:\n" \
    "s_sleep 1\n" \
    ".Ldn_Fatb_%=:\n" \
    "s_memrealtime vcc\n" \
    "s_waitcnt lgkmcnt(0)\n" \
    "s_sub_i32 vcc_lo, s36, vcc_lo\n" \
    "s_cmp_gt_i32 vcc_lo, 8\n" \
    "s_cbranch_scc1 .Ldn_Fata_%=\n" \
    "s_cmp_gt_i32 vcc_lo, 0\n" \
    "s_cbranch_scc1 .Ldn_Fatb_%=\n" \
    ".Ldn_Fax_%=:\n" \
    "s_mul_i32 s39, s32, 98304\n" \
    "s_add_u32 s39, s39, s17\n" \
    "buffer_load_dwordx4 v[152:155], v144, s[8:11], s39 offen offset:0 sc1\n" \
    "buffer_load_dwordx4 v[156:159], v144, s[8:11], s39 offen offset:1024 sc1\n" \
    "buffer_load_dwordx4 v[160:163], v144, s[8:11], s39 offen offset:2048 sc1\n" \
    "buffer_load_dwordx4 v[164:167], v144, s[8:11], s39 offen offset:3072 sc1\n" \
    "s_add_u32 s39, s39, 4096\n" \
    "buffer_load_dwordx4 v[168:171], v144, s[8:11], s39 offen offset:0 sc1\n" \
    "buffer_load_dwordx4 v[172:175], v144, s[8:11], s39 offen offset:1024 sc1\n" \
    "s_mul_i32 s39, s32, 40960\n" \
    "s_add_u32 s39, s39, %[x1]+0\n" \
    "buffer_load_dwordx2 v[176:177], v147, s[8:11], s39 offen sc1\n" \
    "s_mov_b32 s37, 0\n" \
    ".Ldn_rFa_%=:\n" \
    "s_waitcnt vmcnt(6)\n" \
    "v_mfma_f32_16x16x32_bf16 v[204:207], v[0:3], v[152:155], 0\n" \
    "v_mfma_f32_16x16x32_bf16 v[208:211], v[24:27], v[152:155], 0\n" \
    "v_mfma_f32_16x16x32_bf16 v[212:215], v[48:51], v[152:155], 0\n" \
    "v_mfma_f32_16x16x32_bf16 v[216:219], v[72:75], v[152:155], 0\n" \
    "v_max3_u32 v238, v152, v153, v154\n" \
    "v_mfma_f32_16x16x32_bf16 v[220:223], v[96:99], v[152:155], 0\n" \
    "v_mfma_f32_16x16x32_bf16 v[224:227], v[120:123], v[152:155], 0\n" \
    "s_waitcnt vmcnt(5)\n" \
    "v_mfma_f32_16x16x32_bf16 v[204:207], v[4:7], v[156:159], v[204:207]\n" \
    "v_mfma_f32_16x16x32_bf16 v[208:211], v[28:31], v[156:159], v[208:211]\n" \
    "v_max3_u32 v238, v238, v155, v156\n" \
    "v_mfma_f32_16x16x32_bf16 v[212:215], v[52:55], v[156:159], v[212:215]\n" \
    "v_mfma_f32_16x16x32_bf16 v[216:219], v[76:79], v[156:159], v[216:219]\n" \
    "v_max3_u32 v238, v238, v157, v158\n" \
    "v_mfma_f32_16x16x32_bf16 v[220:223], v[100:103], v[156:159], v[220:223]\n" \
    "v_mfma_f32_16x16x32_bf16 v[224:227], v[124:127], v[156:159], v[224:227]\n" \
    "s_waitcnt vmcnt(4)\n" \
    "v_mfma_f32_16x16x32_bf16 v[204:207], v[8:11], v[160:163], v[204:207]\n" \
    "v_mfma_f32_16x16x32_bf16 v[208:211], v[32:35], v[160:163], v[208:211]\n" \
    "v_max3_u32 v238, v238, v159, v160\n" \
    "v_mfma_f32_16x16x32_bf16 v[212:215], v[56:59], v[160:163], v[212:215]\n" \
    "v_mfma_f32_16x16x32_bf16 v[216:219], v[80:83], v[160:163], v[216:219]\n" \
    "v_max3_u32 v238, v238, v161, v162\n" \
    "v_mfma_f32_16x16x32_bf16 v[220:223], v[104:107], v[160:163], v[220:223]\n" \
    "v_mfma_f32_16x16x32_bf16 v[224:227], v[128:131], v[160:163], v[224:227]\n" \
    "s_waitcnt vmcnt(3)\n" \
    "v_mfma_f32_16x16x32_bf16 v[204:207], v[12:15], v[164:167], v[204:207]\n" \
    "v_mfma_f32_16x16x32_bf16 v[208:211], v[36:39], v[164:167], v[208:211]\n" \
    "v_max3_u32 v238, v238, v163, v164\n" \
    "v_mfma_f32_16x16x32_bf16 v[212:215], v[60:63], v[164:167], v[212:215]\n" \
    "v_mfma_f32_16x16x32_bf16 v[216:219], v[84:87], v[164:167], v[216:219]\n" \
    "v_max3_u32 v238, v238, v165, v166\n" \
    "v_mfma_f32_16x16x32_bf16 v[220:223], v[108:111], v[164:167], v[220:223]\n" \
    "v_mfma_f32_16x16x32_bf16 v[224:227], v[132:135], v[164:167], v[224:227]\n" \
    "s_waitcnt vmcnt(2)\n" \
    "v_mfma_f32_16x16x32_bf16 v[204:207], v[16:19], v[168:171], v[204:207]\n" \
    "s_waitcnt vmcnt(1)\n" \
    "v_mfma_f32_16x16x32_bf16 v[204:207], v[20:23], v[172:175], v[204:207]\n" \
    "v_mfma_f32_16x16x32_bf16 v[208:211], v[40:43], v[168:171], v[208:211]\n" \
    "v_max3_u32 v238, v238, v167, v168\n" \
    "v_mfma_f32_16x16x32_bf16 v[208:211], v[44:47], v[172:175], v[208:211]\n" \
    "v_max3_u32 v238, v238, v169, v170\n" \
    "v_mfma_f32_16x16x32_bf16 v[212:215], v[64:67], v[168:171], v[212:215]\n" \
    "v_max3_u32 v238, v238, v171, v172\n" \
    "v_mfma_f32_16x16x32_bf16 v[212:215], v[68:71], v[172:175], v[212:215]\n" \
    "v_max3_u32 v238, v238, v173, v174\n" \
    "v_mfma_f32_16x16x32_bf16 v[216:219], v[88:91], v[168:171], v[216:219]\n" \
    "s_waitcnt vmcnt(0)\n" \
    "v_max3_u32 v238, v238, v175, v176\n" \
    "v_mfma_f32_16x16x32_bf16 v[216:219], v[92:95], v[172:175], v[216:219]\n" \
    "v_max_u32 v238, v238, v177\n" \
    "ds_write_b128 v145, v[204:207] offset:0\n" \
    "v_mfma_f32_16x16x32_bf16 v[220:223], v[112:115], v[168:171], v[220:223]\n" \
    "v_mfma_f32_16x16x32_bf16 v[220:223], v[116:119], v[172:175], v[220:223]\n" \
    "ds_write_b128 v145, v[208:211] offset:1024\n" \
    "v_mfma_f32_16x16x32_bf16 v[224:227], v[136:139], v[168:171], v[224:227]\n" \
    "v_mfma_f32_16x16x32_bf16 v[224:227], v[140:143], v[172:175], v[224:227]\n" \
    "ds_write_b128 v145, v[212:215] offset:2048\n" \
    "v_cmp_eq_u32 vcc, -1, v238\n" \
    "ds_write_b128 v145, v[216:219] offset:3072\n" \
    "s_nop 1\n" \
    "ds_write_b128 v145, v[220:223] offset:4096\n" \
    "s_nop 1\n" \
    "ds_write_b128 v145, v[224:227] offset:5120\n" \
    "s_cbranch_vccnz .Ldn_cFa_%=\n" \
    ".Ldn_xFa_%=:\n" \
    "v_lshlrev_b32 v240, 16, v176\n" \
    "v_and_b32 v241, 0xffff0000, v176\n" \
    "v_lshlrev_b32 v242, 16, v177\n" \
    "v_and_b32 v243, 0xffff0000, v177\n" \
    "s_mul_i32 s39, s33, 98304\n" \
    "s_add_u32 s39, s39, s17\n" \
    "s_add_u32 s39, s39, 6291456\n" \
    "buffer_load_dwordx4 v[178:181], v144, s[8:11], s39 offen offset:0 sc1\n" \
    "buffer_load_dwordx4 v[182:185], v144, s[8:11], s39 offen offset:1024 sc1\n" \
    "buffer_load_dwordx4 v[186:189], v144, s[8:11], s39 offen offset:2048 sc1\n" \
    "buffer_load_dwordx4 v[190:193], v144, s[8:11], s39 offen offset:3072 sc1\n" \
    "s_add_u32 s39, s39, 4096\n" \
    "buffer_load_dwordx4 v[194:197], v144, s[8:11], s39 offen offset:0 sc1\n" \
    "buffer_load_dwordx4 v[198:201], v144, s[8:11], s39 offen offset:1024 sc1\n" \
    "s_mul_i32 s39, s33, 40960\n" \
    "s_add_u32 s39, s39, %[x1]+2621440\n" \
    "buffer_load_dwordx2 v[202:203], v147, s[8:11], s39 offen sc1\n" \
    "s_add_u32 s48, s32, 3\n" \
    "s_min_u32 s48, s48, 64\n" \
    "s_and_b32 s49, s48, 1\n" \
    "s_xor_b32 s49, s49, 1\n" \
    "s_lshl_b32 s49, s49, 9\n" \
    "s_add_u32 s49, s49, s18\n" \
    "buffer_load_dwordx2 v[150:151], off, s[8:11], s49 sc1\n" \
    "s_waitcnt lgkmcnt(0)\n" \
    "s_barrier\n" \
    "ds_read_b128 v[204:207], v146 offset:0\n" \
    "ds_read_b128 v[208:211], v146 offset:6144\n" \
    "ds_read_b128 v[212:215], v146 offset:12288\n" \
    "ds_read_b128 v[216:219], v146 offset:18432\n" \
    "ds_read_b128 v[220:223], v146 offset:24576\n" \
    "ds_read_b128 v[224:227], v146 offset:30720\n" \
    "ds_read_b128 v[228:231], v146 offset:36864\n" \
    "ds_read_b128 v[232:235], v146 offset:43008\n" \
    "s_mul_i32 s34, s32, 40960\n" \
    "s_waitcnt lgkmcnt(6)\n" \
    "v_pk_add_f32 v[204:205], v[204:205], v[208:209]\n" \
    "v_pk_add_f32 v[206:207], v[206:207], v[210:211]\n" \
    "s_waitcnt lgkmcnt(4)\n" \
    "v_pk_add_f32 v[212:213], v[212:213], v[216:217]\n" \
    "v_pk_add_f32 v[214:215], v[214:215], v[218:219]\n" \
    "v_pk_add_f32 v[204:205], v[204:205], v[212:213]\n" \
    "v_pk_add_f32 v[206:207], v[206:207], v[214:215]\n" \
    "s_waitcnt lgkmcnt(2)\n" \
    "v_pk_add_f32 v[220:221], v[220:221], v[224:225]\n" \
    "v_pk_add_f32 v[222:223], v[222:223], v[226:227]\n" \
    "s_waitcnt lgkmcnt(0)\n" \
    "s_barrier\n" \
    "v_pk_add_f32 v[228:229], v[228:229], v[232:233]\n" \
    "v_pk_add_f32 v[230:231], v[230:231], v[234:235]\n" \
    "v_pk_add_f32 v[220:221], v[220:221], v[228:229]\n" \
    "v_pk_add_f32 v[222:223], v[222:223], v[230:231]\n" \
    "v_pk_add_f32 v[204:205], v[204:205], v[220:221]\n" \
    "v_pk_add_f32 v[206:207], v[206:207], v[222:223]\n" \
    "v_cvt_pk_bf16_f32 v208, v204, v205\n" \
    "v_cvt_pk_bf16_f32 v209, v206, v207\n" \
    "v_lshlrev_b32 v204, 16, v208\n" \
    "v_and_b32 v205, 0xffff0000, v208\n" \
    "v_lshlrev_b32 v206, 16, v209\n" \
    "v_and_b32 v207, 0xffff0000, v209\n" \
    "v_pk_add_f32 v[204:205], v[204:205], v[240:241]\n" \
    "v_pk_add_f32 v[206:207], v[206:207], v[242:243]\n" \
    "v_cvt_pk_bf16_f32 v248, v204, v205\n" \
    "v_cvt_pk_bf16_f32 v249, v206, v207\n" \
    "v_min_u32 v248, -2, v248\n" \
    "v_min_u32 v249, -2, v249\n" \
    "s_add_u32 s34, s34, %[x2]\n" \
    "buffer_store_dwordx2 v[248:249], v147, s[8:11], s34 offen sc1\n" \
    "s_add_u32 s34, s34, %[pdist]*40960\n" \
    "s_cmp_lt_u32 s32, 64-%[pdist]\n" \
    "s_cselect_b32 s34, s34, 0x80000000\n" \
    "buffer_store_dwordx2 v[236:237], v147, s[8:11], s34 offen sc1\n" \
    "s_add_u32 s32, s32, 1\n" \
    "s_cmp_lt_u32 s32, %[eup]+1\n" \
    "s_cbranch_scc1 .Ldn_lF_%=\n" \
    "s_waitcnt vmcnt(0)\n" \
    ".Ldn_mF_%=:\n" \
    "s_memrealtime s[50:51]\n" \
    "v_readfirstlane_b32 s36, v151\n" \
    "v_readfirstlane_b32 s35, v150\n" \
    "s_waitcnt lgkmcnt(0)\n" \
    "s_sub_u32 s35, s50, s35\n" \
    "s_cmp_ge_i32 s35, %[dxd]\n" \
    "s_addc_u32 s36, s36, 0\n" \
    "s_cmp_eq_u32 s32, 63\n" \
    "s_cselect_b32 s36, 0, s36\n" \
    "s_cmp_gt_i32 s36, s48\n" \
    "s_cbranch_scc0 .Ldn_ndF_%=\n" \
    "s_mul_i32 s39, s32, 98304\n" \
    "s_add_u32 s39, s39, s17\n" \
    "buffer_load_dwordx4 v[152:155], v144, s[8:11], s39 offen offset:0 sc1\n" \
    "buffer_load_dwordx4 v[156:159], v144, s[8:11], s39 offen offset:1024 sc1\n" \
    "buffer_load_dwordx4 v[160:163], v144, s[8:11], s39 offen offset:2048 sc1\n" \
    "buffer_load_dwordx4 v[164:167], v144, s[8:11], s39 offen offset:3072 sc1\n" \
    "s_add_u32 s39, s39, 4096\n" \
    "buffer_load_dwordx4 v[168:171], v144, s[8:11], s39 offen offset:0 sc1\n" \
    "buffer_load_dwordx4 v[172:175], v144, s[8:11], s39 offen offset:1024 sc1\n" \
    "s_mul_i32 s39, s32, 40960\n" \
    "s_add_u32 s39, s39, %[x1]+0\n" \
    "buffer_load_dwordx2 v[176:177], v147, s[8:11], s39 offen sc1\n" \
    "s_waitcnt vmcnt(10)\n" \
    "s_mov_b32 s37, 0\n" \
    ".Ldn_rFb_%=:\n" \
    "v_mfma_f32_16x16x32_bf16 v[204:207], v[0:3], v[178:181], 0\n" \
    "v_mfma_f32_16x16x32_bf16 v[204:207], v[4:7], v[182:185], v[204:207]\n" \
    "v_mfma_f32_16x16x32_bf16 v[204:207], v[8:11], v[186:189], v[204:207]\n" \
    "v_mfma_f32_16x16x32_bf16 v[204:207], v[12:15], v[190:193], v[204:207]\n" \
    "v_mfma_f32_16x16x32_bf16 v[204:207], v[16:19], v[194:197], v[204:207]\n" \
    "v_mfma_f32_16x16x32_bf16 v[204:207], v[20:23], v[198:201], v[204:207]\n" \
    "v_mfma_f32_16x16x32_bf16 v[208:211], v[24:27], v[178:181], 0\n" \
    "v_max3_u32 v238, v178, v179, v180\n" \
    "v_mfma_f32_16x16x32_bf16 v[208:211], v[28:31], v[182:185], v[208:211]\n" \
    "v_max3_u32 v238, v238, v181, v182\n" \
    "v_mfma_f32_16x16x32_bf16 v[208:211], v[32:35], v[186:189], v[208:211]\n" \
    "v_max3_u32 v238, v238, v183, v184\n" \
    "v_mfma_f32_16x16x32_bf16 v[208:211], v[36:39], v[190:193], v[208:211]\n" \
    "v_max3_u32 v238, v238, v185, v186\n" \
    "v_mfma_f32_16x16x32_bf16 v[208:211], v[40:43], v[194:197], v[208:211]\n" \
    "v_max3_u32 v238, v238, v187, v188\n" \
    "v_mfma_f32_16x16x32_bf16 v[208:211], v[44:47], v[198:201], v[208:211]\n" \
    "v_max3_u32 v238, v238, v189, v190\n" \
    "v_mfma_f32_16x16x32_bf16 v[212:215], v[48:51], v[178:181], 0\n" \
    "v_max3_u32 v238, v238, v191, v192\n" \
    "v_mfma_f32_16x16x32_bf16 v[212:215], v[52:55], v[182:185], v[212:215]\n" \
    "v_max3_u32 v238, v238, v193, v194\n" \
    "v_mfma_f32_16x16x32_bf16 v[212:215], v[56:59], v[186:189], v[212:215]\n" \
    "v_max3_u32 v238, v238, v195, v196\n" \
    "v_mfma_f32_16x16x32_bf16 v[212:215], v[60:63], v[190:193], v[212:215]\n" \
    "v_max3_u32 v238, v238, v197, v198\n" \
    "v_mfma_f32_16x16x32_bf16 v[212:215], v[64:67], v[194:197], v[212:215]\n" \
    "v_max3_u32 v238, v238, v199, v200\n" \
    "v_mfma_f32_16x16x32_bf16 v[212:215], v[68:71], v[198:201], v[212:215]\n" \
    "v_max3_u32 v238, v238, v201, v202\n" \
    "ds_write_b128 v145, v[204:207] offset:49152\n" \
    "v_mfma_f32_16x16x32_bf16 v[216:219], v[72:75], v[178:181], 0\n" \
    "v_max_u32 v238, v238, v203\n" \
    "v_mfma_f32_16x16x32_bf16 v[216:219], v[76:79], v[182:185], v[216:219]\n" \
    "v_mfma_f32_16x16x32_bf16 v[216:219], v[80:83], v[186:189], v[216:219]\n" \
    "v_mfma_f32_16x16x32_bf16 v[216:219], v[84:87], v[190:193], v[216:219]\n" \
    "v_mfma_f32_16x16x32_bf16 v[216:219], v[88:91], v[194:197], v[216:219]\n" \
    "v_mfma_f32_16x16x32_bf16 v[216:219], v[92:95], v[198:201], v[216:219]\n" \
    "ds_write_b128 v145, v[208:211] offset:50176\n" \
    "v_mfma_f32_16x16x32_bf16 v[220:223], v[96:99], v[178:181], 0\n" \
    "v_mfma_f32_16x16x32_bf16 v[220:223], v[100:103], v[182:185], v[220:223]\n" \
    "v_mfma_f32_16x16x32_bf16 v[220:223], v[104:107], v[186:189], v[220:223]\n" \
    "v_mfma_f32_16x16x32_bf16 v[220:223], v[108:111], v[190:193], v[220:223]\n" \
    "v_mfma_f32_16x16x32_bf16 v[220:223], v[112:115], v[194:197], v[220:223]\n" \
    "v_mfma_f32_16x16x32_bf16 v[220:223], v[116:119], v[198:201], v[220:223]\n" \
    "ds_write_b128 v145, v[212:215] offset:51200\n" \
    "v_mfma_f32_16x16x32_bf16 v[224:227], v[120:123], v[178:181], 0\n" \
    "v_mfma_f32_16x16x32_bf16 v[224:227], v[124:127], v[182:185], v[224:227]\n" \
    "v_mfma_f32_16x16x32_bf16 v[224:227], v[128:131], v[186:189], v[224:227]\n" \
    "v_mfma_f32_16x16x32_bf16 v[224:227], v[132:135], v[190:193], v[224:227]\n" \
    "v_mfma_f32_16x16x32_bf16 v[224:227], v[136:139], v[194:197], v[224:227]\n" \
    "v_mfma_f32_16x16x32_bf16 v[224:227], v[140:143], v[198:201], v[224:227]\n" \
    "ds_write_b128 v145, v[216:219] offset:52224\n" \
    "v_cmp_eq_u32 vcc, -1, v238\n" \
    "ds_write_b128 v145, v[220:223] offset:53248\n" \
    "s_nop 4\n" \
    "ds_write_b128 v145, v[224:227] offset:54272\n" \
    "s_cbranch_vccnz .Ldn_cFb_%=\n" \
    ".Ldn_xFb_%=:\n" \
    "s_branch .Ldn_jnF_%=\n" \
    ".Ldn_ndF_%=:\n" \
    "s_waitcnt vmcnt(3)\n" \
    "s_mov_b32 s37, 0\n" \
    ".Ldn_rFc_%=:\n" \
    "v_mfma_f32_16x16x32_bf16 v[204:207], v[0:3], v[178:181], 0\n" \
    "v_mfma_f32_16x16x32_bf16 v[204:207], v[4:7], v[182:185], v[204:207]\n" \
    "v_mfma_f32_16x16x32_bf16 v[204:207], v[8:11], v[186:189], v[204:207]\n" \
    "v_mfma_f32_16x16x32_bf16 v[204:207], v[12:15], v[190:193], v[204:207]\n" \
    "v_mfma_f32_16x16x32_bf16 v[204:207], v[16:19], v[194:197], v[204:207]\n" \
    "v_mfma_f32_16x16x32_bf16 v[204:207], v[20:23], v[198:201], v[204:207]\n" \
    "v_mfma_f32_16x16x32_bf16 v[208:211], v[24:27], v[178:181], 0\n" \
    "v_max3_u32 v238, v178, v179, v180\n" \
    "v_mfma_f32_16x16x32_bf16 v[208:211], v[28:31], v[182:185], v[208:211]\n" \
    "v_max3_u32 v238, v238, v181, v182\n" \
    "v_mfma_f32_16x16x32_bf16 v[208:211], v[32:35], v[186:189], v[208:211]\n" \
    "v_max3_u32 v238, v238, v183, v184\n" \
    "v_mfma_f32_16x16x32_bf16 v[208:211], v[36:39], v[190:193], v[208:211]\n" \
    "v_max3_u32 v238, v238, v185, v186\n" \
    "v_mfma_f32_16x16x32_bf16 v[208:211], v[40:43], v[194:197], v[208:211]\n" \
    "v_max3_u32 v238, v238, v187, v188\n" \
    "v_mfma_f32_16x16x32_bf16 v[208:211], v[44:47], v[198:201], v[208:211]\n" \
    "v_max3_u32 v238, v238, v189, v190\n" \
    "v_mfma_f32_16x16x32_bf16 v[212:215], v[48:51], v[178:181], 0\n" \
    "v_max3_u32 v238, v238, v191, v192\n" \
    "v_mfma_f32_16x16x32_bf16 v[212:215], v[52:55], v[182:185], v[212:215]\n" \
    "v_max3_u32 v238, v238, v193, v194\n" \
    "v_mfma_f32_16x16x32_bf16 v[212:215], v[56:59], v[186:189], v[212:215]\n" \
    "v_max3_u32 v238, v238, v195, v196\n" \
    "v_mfma_f32_16x16x32_bf16 v[212:215], v[60:63], v[190:193], v[212:215]\n" \
    "v_max3_u32 v238, v238, v197, v198\n" \
    "v_mfma_f32_16x16x32_bf16 v[212:215], v[64:67], v[194:197], v[212:215]\n" \
    "v_max3_u32 v238, v238, v199, v200\n" \
    "v_mfma_f32_16x16x32_bf16 v[212:215], v[68:71], v[198:201], v[212:215]\n" \
    "v_max3_u32 v238, v238, v201, v202\n" \
    "ds_write_b128 v145, v[204:207] offset:49152\n" \
    "v_mfma_f32_16x16x32_bf16 v[216:219], v[72:75], v[178:181], 0\n" \
    "v_max_u32 v238, v238, v203\n" \
    "v_mfma_f32_16x16x32_bf16 v[216:219], v[76:79], v[182:185], v[216:219]\n" \
    "v_mfma_f32_16x16x32_bf16 v[216:219], v[80:83], v[186:189], v[216:219]\n" \
    "v_mfma_f32_16x16x32_bf16 v[216:219], v[84:87], v[190:193], v[216:219]\n" \
    "v_mfma_f32_16x16x32_bf16 v[216:219], v[88:91], v[194:197], v[216:219]\n" \
    "v_mfma_f32_16x16x32_bf16 v[216:219], v[92:95], v[198:201], v[216:219]\n" \
    "ds_write_b128 v145, v[208:211] offset:50176\n" \
    "v_mfma_f32_16x16x32_bf16 v[220:223], v[96:99], v[178:181], 0\n" \
    "v_mfma_f32_16x16x32_bf16 v[220:223], v[100:103], v[182:185], v[220:223]\n" \
    "v_mfma_f32_16x16x32_bf16 v[220:223], v[104:107], v[186:189], v[220:223]\n" \
    "v_mfma_f32_16x16x32_bf16 v[220:223], v[108:111], v[190:193], v[220:223]\n" \
    "v_mfma_f32_16x16x32_bf16 v[220:223], v[112:115], v[194:197], v[220:223]\n" \
    "v_mfma_f32_16x16x32_bf16 v[220:223], v[116:119], v[198:201], v[220:223]\n" \
    "ds_write_b128 v145, v[212:215] offset:51200\n" \
    "v_mfma_f32_16x16x32_bf16 v[224:227], v[120:123], v[178:181], 0\n" \
    "v_mfma_f32_16x16x32_bf16 v[224:227], v[124:127], v[182:185], v[224:227]\n" \
    "v_mfma_f32_16x16x32_bf16 v[224:227], v[128:131], v[186:189], v[224:227]\n" \
    "v_mfma_f32_16x16x32_bf16 v[224:227], v[132:135], v[190:193], v[224:227]\n" \
    "v_mfma_f32_16x16x32_bf16 v[224:227], v[136:139], v[194:197], v[224:227]\n" \
    "v_mfma_f32_16x16x32_bf16 v[224:227], v[140:143], v[198:201], v[224:227]\n" \
    "ds_write_b128 v145, v[216:219] offset:52224\n" \
    "v_cmp_eq_u32 vcc, -1, v238\n" \
    "ds_write_b128 v145, v[220:223] offset:53248\n" \
    "s_nop 4\n" \
    "ds_write_b128 v145, v[224:227] offset:54272\n" \
    "s_cbranch_vccnz .Ldn_cFc_%=\n" \
    ".Ldn_xFc_%=:\n" \
    "s_mov_b32 s37, 0\n" \
    "s_waitcnt vmcnt(63)\n" \
    ".Ldn_Fep_%=:\n" \
    "v_readfirstlane_b32 s36, v151\n" \
    "s_nop 0\n" \
    "s_cmp_gt_i32 s36, s48\n" \
    "s_cbranch_scc1 .Ldn_Fex_%=\n" \
    "s_cmp_eq_u32 s36, s48\n" \
    "s_cbranch_scc1 .Ldn_Few_%=\n" \
    "s_sleep 3\n" \
    "buffer_load_dwordx2 v[150:151], off, s[8:11], s49 sc1\n" \
    "s_waitcnt vmcnt(0)\n" \
    "s_add_u32 s37, s37, 1\n" \
    "s_cmp_lt_u32 s37, s38\n" \
    "s_cbranch_scc1 .Ldn_Fep_%=\n" \
    "s_mov_b32 s38, 0\n" \
    "s_branch .Ldn_Fex_%=\n" \
    ".Ldn_Few_%=:\n" \
    "v_readfirstlane_b32 s36, v150\n" \
    "s_cmp_eq_u32 s32, 63\n" \
    "s_cselect_b32 s34, %[dlast], 0\n" \
    "s_add_u32 s36, s36, s34\n" \
    "s_add_u32 s36, s36, %[dxd]\n" \
    "s_branch .Ldn_Fetb_%=\n" \
    ".Ldn_Feta_%=:\n" \
    "s_sleep 1\n" \
    ".Ldn_Fetb_%=:\n" \
    "s_memrealtime vcc\n" \
    "s_waitcnt lgkmcnt(0)\n" \
    "s_sub_i32 vcc_lo, s36, vcc_lo\n" \
    "s_cmp_gt_i32 vcc_lo, 8\n" \
    "s_cbranch_scc1 .Ldn_Feta_%=\n" \
    "s_cmp_gt_i32 vcc_lo, 0\n" \
    "s_cbranch_scc1 .Ldn_Fetb_%=\n" \
    ".Ldn_Fex_%=:\n" \
    "s_mul_i32 s39, s32, 98304\n" \
    "s_add_u32 s39, s39, s17\n" \
    "buffer_load_dwordx4 v[152:155], v144, s[8:11], s39 offen offset:0 sc1\n" \
    "buffer_load_dwordx4 v[156:159], v144, s[8:11], s39 offen offset:1024 sc1\n" \
    "buffer_load_dwordx4 v[160:163], v144, s[8:11], s39 offen offset:2048 sc1\n" \
    "buffer_load_dwordx4 v[164:167], v144, s[8:11], s39 offen offset:3072 sc1\n" \
    "s_add_u32 s39, s39, 4096\n" \
    "buffer_load_dwordx4 v[168:171], v144, s[8:11], s39 offen offset:0 sc1\n" \
    "buffer_load_dwordx4 v[172:175], v144, s[8:11], s39 offen offset:1024 sc1\n" \
    "s_mul_i32 s39, s32, 40960\n" \
    "s_add_u32 s39, s39, %[x1]+0\n" \
    "buffer_load_dwordx2 v[176:177], v147, s[8:11], s39 offen sc1\n" \
    ".Ldn_jnF_%=:\n" \
    "v_lshlrev_b32 v244, 16, v202\n" \
    "v_and_b32 v245, 0xffff0000, v202\n" \
    "v_lshlrev_b32 v246, 16, v203\n" \
    "v_and_b32 v247, 0xffff0000, v203\n" \
    "s_add_u32 s48, s32, 3\n" \
    "s_min_u32 s48, s48, 64\n" \
    "s_and_b32 s49, s48, 1\n" \
    "s_xor_b32 s49, s49, 1\n" \
    "s_lshl_b32 s49, s49, 9\n" \
    "s_add_u32 s49, s49, s18\n" \
    "buffer_load_dwordx2 v[150:151], off, s[8:11], s49 sc1\n" \
    "s_add_u32 s36, s33, 1\n" \
    "s_mov_b32 s37, 0\n" \
    ".Ldn_rFd_%=:\n" \
    "s_waitcnt vmcnt(7)\n" \
    "v_mfma_f32_16x16x32_bf16 v[204:207], v[0:3], v[152:155], 0\n" \
    "v_mfma_f32_16x16x32_bf16 v[208:211], v[24:27], v[152:155], 0\n" \
    "v_mfma_f32_16x16x32_bf16 v[212:215], v[48:51], v[152:155], 0\n" \
    "v_mfma_f32_16x16x32_bf16 v[216:219], v[72:75], v[152:155], 0\n" \
    "v_max3_u32 v238, v152, v153, v154\n" \
    "v_mfma_f32_16x16x32_bf16 v[220:223], v[96:99], v[152:155], 0\n" \
    "v_mfma_f32_16x16x32_bf16 v[224:227], v[120:123], v[152:155], 0\n" \
    "s_waitcnt vmcnt(6)\n" \
    "v_mfma_f32_16x16x32_bf16 v[204:207], v[4:7], v[156:159], v[204:207]\n" \
    "v_mfma_f32_16x16x32_bf16 v[208:211], v[28:31], v[156:159], v[208:211]\n" \
    "v_max3_u32 v238, v238, v155, v156\n" \
    "v_mfma_f32_16x16x32_bf16 v[212:215], v[52:55], v[156:159], v[212:215]\n" \
    "v_mfma_f32_16x16x32_bf16 v[216:219], v[76:79], v[156:159], v[216:219]\n" \
    "v_max3_u32 v238, v238, v157, v158\n" \
    "v_mfma_f32_16x16x32_bf16 v[220:223], v[100:103], v[156:159], v[220:223]\n" \
    "v_mfma_f32_16x16x32_bf16 v[224:227], v[124:127], v[156:159], v[224:227]\n" \
    "s_waitcnt vmcnt(5)\n" \
    "v_mfma_f32_16x16x32_bf16 v[204:207], v[8:11], v[160:163], v[204:207]\n" \
    "v_mfma_f32_16x16x32_bf16 v[208:211], v[32:35], v[160:163], v[208:211]\n" \
    "v_max3_u32 v238, v238, v159, v160\n" \
    "v_mfma_f32_16x16x32_bf16 v[212:215], v[56:59], v[160:163], v[212:215]\n" \
    "v_mfma_f32_16x16x32_bf16 v[216:219], v[80:83], v[160:163], v[216:219]\n" \
    "v_max3_u32 v238, v238, v161, v162\n" \
    "v_mfma_f32_16x16x32_bf16 v[220:223], v[104:107], v[160:163], v[220:223]\n" \
    "v_mfma_f32_16x16x32_bf16 v[224:227], v[128:131], v[160:163], v[224:227]\n" \
    "s_waitcnt vmcnt(4)\n" \
    "v_mfma_f32_16x16x32_bf16 v[204:207], v[12:15], v[164:167], v[204:207]\n" \
    "v_mfma_f32_16x16x32_bf16 v[208:211], v[36:39], v[164:167], v[208:211]\n" \
    "v_max3_u32 v238, v238, v163, v164\n" \
    "v_mfma_f32_16x16x32_bf16 v[212:215], v[60:63], v[164:167], v[212:215]\n" \
    "v_mfma_f32_16x16x32_bf16 v[216:219], v[84:87], v[164:167], v[216:219]\n" \
    "v_max3_u32 v238, v238, v165, v166\n" \
    "v_mfma_f32_16x16x32_bf16 v[220:223], v[108:111], v[164:167], v[220:223]\n" \
    "v_mfma_f32_16x16x32_bf16 v[224:227], v[132:135], v[164:167], v[224:227]\n" \
    "s_waitcnt vmcnt(3)\n" \
    "v_mfma_f32_16x16x32_bf16 v[204:207], v[16:19], v[168:171], v[204:207]\n" \
    "s_waitcnt vmcnt(2)\n" \
    "v_mfma_f32_16x16x32_bf16 v[204:207], v[20:23], v[172:175], v[204:207]\n" \
    "v_mfma_f32_16x16x32_bf16 v[208:211], v[40:43], v[168:171], v[208:211]\n" \
    "v_max3_u32 v238, v238, v167, v168\n" \
    "v_mfma_f32_16x16x32_bf16 v[208:211], v[44:47], v[172:175], v[208:211]\n" \
    "v_max3_u32 v238, v238, v169, v170\n" \
    "v_mfma_f32_16x16x32_bf16 v[212:215], v[64:67], v[168:171], v[212:215]\n" \
    "v_max3_u32 v238, v238, v171, v172\n" \
    "v_mfma_f32_16x16x32_bf16 v[212:215], v[68:71], v[172:175], v[212:215]\n" \
    "v_max3_u32 v238, v238, v173, v174\n" \
    "v_mfma_f32_16x16x32_bf16 v[216:219], v[88:91], v[168:171], v[216:219]\n" \
    "s_waitcnt vmcnt(1)\n" \
    "v_max3_u32 v238, v238, v175, v176\n" \
    "v_mfma_f32_16x16x32_bf16 v[216:219], v[92:95], v[172:175], v[216:219]\n" \
    "v_max_u32 v238, v238, v177\n" \
    "ds_write_b128 v145, v[204:207] offset:0\n" \
    "v_mfma_f32_16x16x32_bf16 v[220:223], v[112:115], v[168:171], v[220:223]\n" \
    "v_mfma_f32_16x16x32_bf16 v[220:223], v[116:119], v[172:175], v[220:223]\n" \
    "ds_write_b128 v145, v[208:211] offset:1024\n" \
    "v_mfma_f32_16x16x32_bf16 v[224:227], v[136:139], v[168:171], v[224:227]\n" \
    "v_mfma_f32_16x16x32_bf16 v[224:227], v[140:143], v[172:175], v[224:227]\n" \
    "ds_write_b128 v145, v[212:215] offset:2048\n" \
    "v_cmp_eq_u32 vcc, -1, v238\n" \
    "ds_write_b128 v145, v[216:219] offset:3072\n" \
    "s_nop 1\n" \
    "ds_write_b128 v145, v[220:223] offset:4096\n" \
    "s_nop 1\n" \
    "ds_write_b128 v145, v[224:227] offset:5120\n" \
    "s_cbranch_vccnz .Ldn_cFd_%=\n" \
    ".Ldn_xFd_%=:\n" \
    "v_lshlrev_b32 v240, 16, v176\n" \
    "v_and_b32 v241, 0xffff0000, v176\n" \
    "v_lshlrev_b32 v242, 16, v177\n" \
    "v_and_b32 v243, 0xffff0000, v177\n" \
    "s_mul_i32 s39, s36, 98304\n" \
    "s_add_u32 s39, s39, s17\n" \
    "s_add_u32 s39, s39, 6291456\n" \
    "buffer_load_dwordx4 v[178:181], v144, s[8:11], s39 offen offset:0 sc1\n" \
    "buffer_load_dwordx4 v[182:185], v144, s[8:11], s39 offen offset:1024 sc1\n" \
    "buffer_load_dwordx4 v[186:189], v144, s[8:11], s39 offen offset:2048 sc1\n" \
    "buffer_load_dwordx4 v[190:193], v144, s[8:11], s39 offen offset:3072 sc1\n" \
    "s_add_u32 s39, s39, 4096\n" \
    "buffer_load_dwordx4 v[194:197], v144, s[8:11], s39 offen offset:0 sc1\n" \
    "buffer_load_dwordx4 v[198:201], v144, s[8:11], s39 offen offset:1024 sc1\n" \
    "s_mul_i32 s39, s36, 40960\n" \
    "s_add_u32 s39, s39, %[x1]+2621440\n" \
    "buffer_load_dwordx2 v[202:203], v147, s[8:11], s39 offen sc1\n" \
    "s_waitcnt vmcnt(7) lgkmcnt(0)\n" \
    "s_barrier\n" \
    "ds_read_b128 v[204:207], v146 offset:0\n" \
    "ds_read_b128 v[208:211], v146 offset:6144\n" \
    "ds_read_b128 v[212:215], v146 offset:12288\n" \
    "ds_read_b128 v[216:219], v146 offset:18432\n" \
    "ds_read_b128 v[220:223], v146 offset:24576\n" \
    "ds_read_b128 v[224:227], v146 offset:30720\n" \
    "ds_read_b128 v[228:231], v146 offset:36864\n" \
    "ds_read_b128 v[232:235], v146 offset:43008\n" \
    "s_mul_i32 s34, s32, 40960\n" \
    "s_waitcnt lgkmcnt(6)\n" \
    "v_pk_add_f32 v[204:205], v[204:205], v[208:209]\n" \
    "v_pk_add_f32 v[206:207], v[206:207], v[210:211]\n" \
    "s_waitcnt lgkmcnt(4)\n" \
    "v_pk_add_f32 v[212:213], v[212:213], v[216:217]\n" \
    "v_pk_add_f32 v[214:215], v[214:215], v[218:219]\n" \
    "v_pk_add_f32 v[204:205], v[204:205], v[212:213]\n" \
    "v_pk_add_f32 v[206:207], v[206:207], v[214:215]\n" \
    "s_waitcnt lgkmcnt(2)\n" \
    "v_pk_add_f32 v[220:221], v[220:221], v[224:225]\n" \
    "v_pk_add_f32 v[222:223], v[222:223], v[226:227]\n" \
    "s_waitcnt lgkmcnt(0)\n" \
    "v_pk_add_f32 v[228:229], v[228:229], v[232:233]\n" \
    "v_pk_add_f32 v[230:231], v[230:231], v[234:235]\n" \
    "v_pk_add_f32 v[220:221], v[220:221], v[228:229]\n" \
    "v_pk_add_f32 v[222:223], v[222:223], v[230:231]\n" \
    "v_pk_add_f32 v[204:205], v[204:205], v[220:221]\n" \
    "v_pk_add_f32 v[206:207], v[206:207], v[222:223]\n" \
    "v_cvt_pk_bf16_f32 v208, v204, v205\n" \
    "v_cvt_pk_bf16_f32 v209, v206, v207\n" \
    "v_lshlrev_b32 v204, 16, v208\n" \
    "v_and_b32 v205, 0xffff0000, v208\n" \
    "v_lshlrev_b32 v206, 16, v209\n" \
    "v_and_b32 v207, 0xffff0000, v209\n" \
    "v_pk_add_f32 v[204:205], v[204:205], v[240:241]\n" \
    "v_pk_add_f32 v[206:207], v[206:207], v[242:243]\n" \
    "v_cvt_pk_bf16_f32 v248, v204, v205\n" \
    "v_cvt_pk_bf16_f32 v249, v206, v207\n" \
    "v_min_u32 v248, -2, v248\n" \
    "v_min_u32 v249, -2, v249\n" \
    "s_add_u32 s34, s34, %[x2]\n" \
    "buffer_store_dwordx2 v[248:249], v147, s[8:11], s34 offen sc1\n" \
    "s_add_u32 s34, s34, %[pdist]*40960\n" \
    "s_cmp_lt_u32 s32, 64-%[pdist]\n" \
    "s_cselect_b32 s34, s34, 0x80000000\n" \
    "buffer_store_dwordx2 v[236:237], v147, s[8:11], s34 offen sc1\n" \
    "v_add_u32 v239, 49152, v146\n" \
    "ds_read_b128 v[204:207], v239 offset:0\n" \
    "ds_read_b128 v[208:211], v239 offset:6144\n" \
    "ds_read_b128 v[212:215], v239 offset:12288\n" \
    "ds_read_b128 v[216:219], v239 offset:18432\n" \
    "ds_read_b128 v[220:223], v239 offset:24576\n" \
    "ds_read_b128 v[224:227], v239 offset:30720\n" \
    "ds_read_b128 v[228:231], v239 offset:36864\n" \
    "ds_read_b128 v[232:235], v239 offset:43008\n" \
    "s_mul_i32 s34, s33, 40960\n" \
    "s_waitcnt lgkmcnt(6)\n" \
    "v_pk_add_f32 v[204:205], v[204:205], v[208:209]\n" \
    "v_pk_add_f32 v[206:207], v[206:207], v[210:211]\n" \
    "s_waitcnt lgkmcnt(4)\n" \
    "v_pk_add_f32 v[212:213], v[212:213], v[216:217]\n" \
    "v_pk_add_f32 v[214:215], v[214:215], v[218:219]\n" \
    "v_pk_add_f32 v[204:205], v[204:205], v[212:213]\n" \
    "v_pk_add_f32 v[206:207], v[206:207], v[214:215]\n" \
    "s_waitcnt lgkmcnt(2)\n" \
    "v_pk_add_f32 v[220:221], v[220:221], v[224:225]\n" \
    "v_pk_add_f32 v[222:223], v[222:223], v[226:227]\n" \
    "s_waitcnt lgkmcnt(0)\n" \
    "s_barrier\n" \
    "v_pk_add_f32 v[228:229], v[228:229], v[232:233]\n" \
    "v_pk_add_f32 v[230:231], v[230:231], v[234:235]\n" \
    "v_pk_add_f32 v[220:221], v[220:221], v[228:229]\n" \
    "v_pk_add_f32 v[222:223], v[222:223], v[230:231]\n" \
    "v_pk_add_f32 v[204:205], v[204:205], v[220:221]\n" \
    "v_pk_add_f32 v[206:207], v[206:207], v[222:223]\n" \
    "v_cvt_pk_bf16_f32 v208, v204, v205\n" \
    "v_cvt_pk_bf16_f32 v209, v206, v207\n" \
    "v_lshlrev_b32 v204, 16, v208\n" \
    "v_and_b32 v205, 0xffff0000, v208\n" \
    "v_lshlrev_b32 v206, 16, v209\n" \
    "v_and_b32 v207, 0xffff0000, v209\n" \
    "v_pk_add_f32 v[204:205], v[204:205], v[244:245]\n" \
    "v_pk_add_f32 v[206:207], v[206:207], v[246:247]\n" \
    "v_cvt_pk_bf16_f32 v250, v204, v205\n" \
    "v_cvt_pk_bf16_f32 v251, v206, v207\n" \
    "buffer_store_dwordx2 v[250:251], v148, s[12:15], s34 offen\n" \
    "s_add_u32 s33, s33, 1\n" \
    "s_add_u32 s32, s32, 1\n" \
    "s_cmp_lt_u32 s32, 64\n" \
    "s_cbranch_scc1 .Ldn_mF_%=\n" \
    "s_add_u32 s18, s18, 1024\n" \
    "s_waitcnt vmcnt(0)\n" \
    "s_add_u32 s48, s33, 2\n" \
    "s_min_u32 s48, s48, 64\n" \
    "s_and_b32 s49, s48, 1\n" \
    "s_xor_b32 s49, s49, 1\n" \
    "s_lshl_b32 s49, s49, 9\n" \
    "s_add_u32 s49, s49, s18\n" \
    "buffer_load_dwordx2 v[150:151], off, s[8:11], s49 sc1\n" \
    ".Ldn_nF_%=:\n" \
    "s_mov_b32 s37, 0\n" \
    "s_waitcnt vmcnt(0)\n" \
    ".Ldn_Ffp_%=:\n" \
    "v_readfirstlane_b32 s36, v151\n" \
    "s_nop 0\n" \
    "s_cmp_gt_i32 s36, s48\n" \
    "s_cbranch_scc1 .Ldn_Ffx_%=\n" \
    "s_cmp_eq_u32 s36, s48\n" \
    "s_cbranch_scc1 .Ldn_Ffw_%=\n" \
    "s_sleep 3\n" \
    "buffer_load_dwordx2 v[150:151], off, s[8:11], s49 sc1\n" \
    "s_waitcnt vmcnt(0)\n" \
    "s_add_u32 s37, s37, 1\n" \
    "s_cmp_lt_u32 s37, s38\n" \
    "s_cbranch_scc1 .Ldn_Ffp_%=\n" \
    "s_mov_b32 s38, 0\n" \
    "s_branch .Ldn_Ffx_%=\n" \
    ".Ldn_Ffw_%=:\n" \
    "v_readfirstlane_b32 s36, v150\n" \
    "s_cmp_eq_u32 s33, 63\n" \
    "s_cselect_b32 s34, %[dlast], 0\n" \
    "s_add_u32 s36, s36, s34\n" \
    "s_add_u32 s36, s36, %[dxd]\n" \
    "s_branch .Ldn_Fftb_%=\n" \
    ".Ldn_Ffta_%=:\n" \
    "s_sleep 1\n" \
    ".Ldn_Fftb_%=:\n" \
    "s_memrealtime vcc\n" \
    "s_waitcnt lgkmcnt(0)\n" \
    "s_sub_i32 vcc_lo, s36, vcc_lo\n" \
    "s_cmp_gt_i32 vcc_lo, 8\n" \
    "s_cbranch_scc1 .Ldn_Ffta_%=\n" \
    "s_cmp_gt_i32 vcc_lo, 0\n" \
    "s_cbranch_scc1 .Ldn_Fftb_%=\n" \
    ".Ldn_Ffx_%=:\n" \
    "s_mul_i32 s39, s33, 98304\n" \
    "s_add_u32 s39, s39, s17\n" \
    "s_add_u32 s39, s39, 6291456\n" \
    "buffer_load_dwordx4 v[178:181], v144, s[8:11], s39 offen offset:0 sc1\n" \
    "buffer_load_dwordx4 v[182:185], v144, s[8:11], s39 offen offset:1024 sc1\n" \
    "buffer_load_dwordx4 v[186:189], v144, s[8:11], s39 offen offset:2048 sc1\n" \
    "buffer_load_dwordx4 v[190:193], v144, s[8:11], s39 offen offset:3072 sc1\n" \
    "s_add_u32 s39, s39, 4096\n" \
    "buffer_load_dwordx4 v[194:197], v144, s[8:11], s39 offen offset:0 sc1\n" \
    "buffer_load_dwordx4 v[198:201], v144, s[8:11], s39 offen offset:1024 sc1\n" \
    "s_mul_i32 s39, s33, 40960\n" \
    "s_add_u32 s39, s39, %[x1]+2621440\n" \
    "buffer_load_dwordx2 v[202:203], v147, s[8:11], s39 offen sc1\n" \
    "s_mov_b32 s37, 0\n" \
    ".Ldn_rFf_%=:\n" \
    "s_waitcnt vmcnt(6)\n" \
    "v_mfma_f32_16x16x32_bf16 v[204:207], v[0:3], v[178:181], 0\n" \
    "v_mfma_f32_16x16x32_bf16 v[208:211], v[24:27], v[178:181], 0\n" \
    "v_mfma_f32_16x16x32_bf16 v[212:215], v[48:51], v[178:181], 0\n" \
    "v_mfma_f32_16x16x32_bf16 v[216:219], v[72:75], v[178:181], 0\n" \
    "v_max3_u32 v238, v178, v179, v180\n" \
    "v_mfma_f32_16x16x32_bf16 v[220:223], v[96:99], v[178:181], 0\n" \
    "v_mfma_f32_16x16x32_bf16 v[224:227], v[120:123], v[178:181], 0\n" \
    "s_waitcnt vmcnt(5)\n" \
    "v_mfma_f32_16x16x32_bf16 v[204:207], v[4:7], v[182:185], v[204:207]\n" \
    "v_mfma_f32_16x16x32_bf16 v[208:211], v[28:31], v[182:185], v[208:211]\n" \
    "v_max3_u32 v238, v238, v181, v182\n" \
    "v_mfma_f32_16x16x32_bf16 v[212:215], v[52:55], v[182:185], v[212:215]\n" \
    "v_mfma_f32_16x16x32_bf16 v[216:219], v[76:79], v[182:185], v[216:219]\n" \
    "v_max3_u32 v238, v238, v183, v184\n" \
    "v_mfma_f32_16x16x32_bf16 v[220:223], v[100:103], v[182:185], v[220:223]\n" \
    "v_mfma_f32_16x16x32_bf16 v[224:227], v[124:127], v[182:185], v[224:227]\n" \
    "s_waitcnt vmcnt(4)\n" \
    "v_mfma_f32_16x16x32_bf16 v[204:207], v[8:11], v[186:189], v[204:207]\n" \
    "v_mfma_f32_16x16x32_bf16 v[208:211], v[32:35], v[186:189], v[208:211]\n" \
    "v_max3_u32 v238, v238, v185, v186\n" \
    "v_mfma_f32_16x16x32_bf16 v[212:215], v[56:59], v[186:189], v[212:215]\n" \
    "v_mfma_f32_16x16x32_bf16 v[216:219], v[80:83], v[186:189], v[216:219]\n" \
    "v_max3_u32 v238, v238, v187, v188\n" \
    "v_mfma_f32_16x16x32_bf16 v[220:223], v[104:107], v[186:189], v[220:223]\n" \
    "v_mfma_f32_16x16x32_bf16 v[224:227], v[128:131], v[186:189], v[224:227]\n" \
    "s_waitcnt vmcnt(3)\n" \
    "v_mfma_f32_16x16x32_bf16 v[204:207], v[12:15], v[190:193], v[204:207]\n" \
    "v_mfma_f32_16x16x32_bf16 v[208:211], v[36:39], v[190:193], v[208:211]\n" \
    "v_max3_u32 v238, v238, v189, v190\n" \
    "v_mfma_f32_16x16x32_bf16 v[212:215], v[60:63], v[190:193], v[212:215]\n" \
    "v_mfma_f32_16x16x32_bf16 v[216:219], v[84:87], v[190:193], v[216:219]\n" \
    "v_max3_u32 v238, v238, v191, v192\n" \
    "v_mfma_f32_16x16x32_bf16 v[220:223], v[108:111], v[190:193], v[220:223]\n" \
    "v_mfma_f32_16x16x32_bf16 v[224:227], v[132:135], v[190:193], v[224:227]\n" \
    "s_waitcnt vmcnt(2)\n" \
    "v_mfma_f32_16x16x32_bf16 v[204:207], v[16:19], v[194:197], v[204:207]\n" \
    "s_waitcnt vmcnt(1)\n" \
    "v_mfma_f32_16x16x32_bf16 v[204:207], v[20:23], v[198:201], v[204:207]\n" \
    "v_mfma_f32_16x16x32_bf16 v[208:211], v[40:43], v[194:197], v[208:211]\n" \
    "v_max3_u32 v238, v238, v193, v194\n" \
    "v_mfma_f32_16x16x32_bf16 v[208:211], v[44:47], v[198:201], v[208:211]\n" \
    "v_max3_u32 v238, v238, v195, v196\n" \
    "v_mfma_f32_16x16x32_bf16 v[212:215], v[64:67], v[194:197], v[212:215]\n" \
    "v_max3_u32 v238, v238, v197, v198\n" \
    "v_mfma_f32_16x16x32_bf16 v[212:215], v[68:71], v[198:201], v[212:215]\n" \
    "v_max3_u32 v238, v238, v199, v200\n" \
    "v_mfma_f32_16x16x32_bf16 v[216:219], v[88:91], v[194:197], v[216:219]\n" \
    "s_waitcnt vmcnt(0)\n" \
    "v_max3_u32 v238, v238, v201, v202\n" \
    "v_mfma_f32_16x16x32_bf16 v[216:219], v[92:95], v[198:201], v[216:219]\n" \
    "v_max_u32 v238, v238, v203\n" \
    "ds_write_b128 v145, v[204:207] offset:49152\n" \
    "v_mfma_f32_16x16x32_bf16 v[220:223], v[112:115], v[194:197], v[220:223]\n" \
    "v_mfma_f32_16x16x32_bf16 v[220:223], v[116:119], v[198:201], v[220:223]\n" \
    "ds_write_b128 v145, v[208:211] offset:50176\n" \
    "v_mfma_f32_16x16x32_bf16 v[224:227], v[136:139], v[194:197], v[224:227]\n" \
    "v_mfma_f32_16x16x32_bf16 v[224:227], v[140:143], v[198:201], v[224:227]\n" \
    "ds_write_b128 v145, v[212:215] offset:51200\n" \
    "v_cmp_eq_u32 vcc, -1, v238\n" \
    "ds_write_b128 v145, v[216:219] offset:52224\n" \
    "s_nop 1\n" \
    "ds_write_b128 v145, v[220:223] offset:53248\n" \
    "s_nop 1\n" \
    "ds_write_b128 v145, v[224:227] offset:54272\n" \
    "s_cbranch_vccnz .Ldn_cFf_%=\n" \
    ".Ldn_xFf_%=:\n" \
    "v_lshlrev_b32 v244, 16, v202\n" \
    "v_and_b32 v245, 0xffff0000, v202\n" \
    "v_lshlrev_b32 v246, 16, v203\n" \
    "v_and_b32 v247, 0xffff0000, v203\n" \
    "s_add_u32 s48, s33, 3\n" \
    "s_min_u32 s48, s48, 64\n" \
    "s_and_b32 s49, s48, 1\n" \
    "s_xor_b32 s49, s49, 1\n" \
    "s_lshl_b32 s49, s49, 9\n" \
    "s_add_u32 s49, s49, s18\n" \
    "buffer_load_dwordx2 v[150:151], off, s[8:11], s49 sc1\n" \
    "s_waitcnt lgkmcnt(0)\n" \
    "s_barrier\n" \
    "v_add_u32 v239, 49152, v146\n" \
    "ds_read_b128 v[204:207], v239 offset:0\n" \
    "ds_read_b128 v[208:211], v239 offset:6144\n" \
    "ds_read_b128 v[212:215], v239 offset:12288\n" \
    "ds_read_b128 v[216:219], v239 offset:18432\n" \
    "ds_read_b128 v[220:223], v239 offset:24576\n" \
    "ds_read_b128 v[224:227], v239 offset:30720\n" \
    "ds_read_b128 v[228:231], v239 offset:36864\n" \
    "ds_read_b128 v[232:235], v239 offset:43008\n" \
    "s_mul_i32 s34, s33, 40960\n" \
    "s_waitcnt lgkmcnt(6)\n" \
    "v_pk_add_f32 v[204:205], v[204:205], v[208:209]\n" \
    "v_pk_add_f32 v[206:207], v[206:207], v[210:211]\n" \
    "s_waitcnt lgkmcnt(4)\n" \
    "v_pk_add_f32 v[212:213], v[212:213], v[216:217]\n" \
    "v_pk_add_f32 v[214:215], v[214:215], v[218:219]\n" \
    "v_pk_add_f32 v[204:205], v[204:205], v[212:213]\n" \
    "v_pk_add_f32 v[206:207], v[206:207], v[214:215]\n" \
    "s_waitcnt lgkmcnt(2)\n" \
    "v_pk_add_f32 v[220:221], v[220:221], v[224:225]\n" \
    "v_pk_add_f32 v[222:223], v[222:223], v[226:227]\n" \
    "s_waitcnt lgkmcnt(0)\n" \
    "s_barrier\n" \
    "v_pk_add_f32 v[228:229], v[228:229], v[232:233]\n" \
    "v_pk_add_f32 v[230:231], v[230:231], v[234:235]\n" \
    "v_pk_add_f32 v[220:221], v[220:221], v[228:229]\n" \
    "v_pk_add_f32 v[222:223], v[222:223], v[230:231]\n" \
    "v_pk_add_f32 v[204:205], v[204:205], v[220:221]\n" \
    "v_pk_add_f32 v[206:207], v[206:207], v[222:223]\n" \
    "v_cvt_pk_bf16_f32 v208, v204, v205\n" \
    "v_cvt_pk_bf16_f32 v209, v206, v207\n" \
    "v_lshlrev_b32 v204, 16, v208\n" \
    "v_and_b32 v205, 0xffff0000, v208\n" \
    "v_lshlrev_b32 v206, 16, v209\n" \
    "v_and_b32 v207, 0xffff0000, v209\n" \
    "v_pk_add_f32 v[204:205], v[204:205], v[244:245]\n" \
    "v_pk_add_f32 v[206:207], v[206:207], v[246:247]\n" \
    "v_cvt_pk_bf16_f32 v250, v204, v205\n" \
    "v_cvt_pk_bf16_f32 v251, v206, v207\n" \
    "buffer_store_dwordx2 v[250:251], v148, s[12:15], s34 offen\n" \
    "s_add_u32 s33, s33, 1\n" \
    "s_cmp_lt_u32 s33, 64\n" \
    "s_cbranch_scc1 .Ldn_nF_%=\n" \
    "s_waitcnt vmcnt(0)\n" \
    "s_endpgm\n" \
    ".Ldn_lN_%=:\n" \
    "s_mov_b32 s37, 0\n" \
    "s_waitcnt vmcnt(0)\n" \
    ".Ldn_Nap_%=:\n" \
    "v_readfirstlane_b32 s36, v151\n" \
    "s_nop 0\n" \
    "s_cmp_gt_i32 s36, s48\n" \
    "s_cbranch_scc1 .Ldn_Nax_%=\n" \
    "s_cmp_eq_u32 s36, s48\n" \
    "s_cbranch_scc1 .Ldn_Naw_%=\n" \
    "s_sleep 3\n" \
    "buffer_load_dwordx2 v[150:151], off, s[8:11], s49 sc1\n" \
    "s_waitcnt vmcnt(0)\n" \
    "s_add_u32 s37, s37, 1\n" \
    "s_cmp_lt_u32 s37, s38\n" \
    "s_cbranch_scc1 .Ldn_Nap_%=\n" \
    "s_mov_b32 s38, 0\n" \
    "s_branch .Ldn_Nax_%=\n" \
    ".Ldn_Naw_%=:\n" \
    "v_readfirstlane_b32 s36, v150\n" \
    "s_cmp_eq_u32 s32, 63\n" \
    "s_cselect_b32 s34, %[dlast], 0\n" \
    "s_add_u32 s36, s36, s34\n" \
    "s_add_u32 s36, s36, %[dxd]\n" \
    "s_branch .Ldn_Natb_%=\n" \
    ".Ldn_Nata_%=:\n" \
    "s_sleep 1\n" \
    ".Ldn_Natb_%=:\n" \
    "s_memrealtime vcc\n" \
    "s_waitcnt lgkmcnt(0)\n" \
    "s_sub_i32 vcc_lo, s36, vcc_lo\n" \
    "s_cmp_gt_i32 vcc_lo, 8\n" \
    "s_cbranch_scc1 .Ldn_Nata_%=\n" \
    "s_cmp_gt_i32 vcc_lo, 0\n" \
    "s_cbranch_scc1 .Ldn_Natb_%=\n" \
    ".Ldn_Nax_%=:\n" \
    "s_mul_i32 s39, s32, 98304\n" \
    "s_add_u32 s39, s39, s17\n" \
    "buffer_load_dwordx4 v[152:155], v144, s[8:11], s39 offen offset:0 sc1\n" \
    "buffer_load_dwordx4 v[156:159], v144, s[8:11], s39 offen offset:1024 sc1\n" \
    "buffer_load_dwordx4 v[160:163], v144, s[8:11], s39 offen offset:2048 sc1\n" \
    "buffer_load_dwordx4 v[164:167], v144, s[8:11], s39 offen offset:3072 sc1\n" \
    "s_add_u32 s39, s39, 4096\n" \
    "buffer_load_dwordx4 v[168:171], v144, s[8:11], s39 offen offset:0 sc1\n" \
    "buffer_load_dwordx4 v[172:175], v144, s[8:11], s39 offen offset:1024 sc1\n" \
    "s_mul_i32 s39, s32, 40960\n" \
    "s_add_u32 s39, s39, %[x1]+0\n" \
    "buffer_load_dwordx2 v[176:177], v147, s[8:11], s39 offen sc1\n" \
    "s_mov_b32 s37, 0\n" \
    ".Ldn_rNa_%=:\n" \
    "s_waitcnt vmcnt(6)\n" \
    "v_mfma_f32_16x16x32_bf16 v[204:207], v[0:3], v[152:155], 0\n" \
    "v_mfma_f32_16x16x32_bf16 v[208:211], v[24:27], v[152:155], 0\n" \
    "v_mfma_f32_16x16x32_bf16 v[212:215], v[48:51], v[152:155], 0\n" \
    "v_mfma_f32_16x16x32_bf16 v[216:219], v[72:75], v[152:155], 0\n" \
    "v_max3_u32 v238, v152, v153, v154\n" \
    "v_mfma_f32_16x16x32_bf16 v[220:223], v[96:99], v[152:155], 0\n" \
    "v_mfma_f32_16x16x32_bf16 v[224:227], v[120:123], v[152:155], 0\n" \
    "s_waitcnt vmcnt(5)\n" \
    "v_mfma_f32_16x16x32_bf16 v[204:207], v[4:7], v[156:159], v[204:207]\n" \
    "v_mfma_f32_16x16x32_bf16 v[208:211], v[28:31], v[156:159], v[208:211]\n" \
    "v_max3_u32 v238, v238, v155, v156\n" \
    "v_mfma_f32_16x16x32_bf16 v[212:215], v[52:55], v[156:159], v[212:215]\n" \
    "v_mfma_f32_16x16x32_bf16 v[216:219], v[76:79], v[156:159], v[216:219]\n" \
    "v_max3_u32 v238, v238, v157, v158\n" \
    "v_mfma_f32_16x16x32_bf16 v[220:223], v[100:103], v[156:159], v[220:223]\n" \
    "v_mfma_f32_16x16x32_bf16 v[224:227], v[124:127], v[156:159], v[224:227]\n" \
    "s_waitcnt vmcnt(4)\n" \
    "v_mfma_f32_16x16x32_bf16 v[204:207], v[8:11], v[160:163], v[204:207]\n" \
    "v_mfma_f32_16x16x32_bf16 v[208:211], v[32:35], v[160:163], v[208:211]\n" \
    "v_max3_u32 v238, v238, v159, v160\n" \
    "v_mfma_f32_16x16x32_bf16 v[212:215], v[56:59], v[160:163], v[212:215]\n" \
    "v_mfma_f32_16x16x32_bf16 v[216:219], v[80:83], v[160:163], v[216:219]\n" \
    "v_max3_u32 v238, v238, v161, v162\n" \
    "v_mfma_f32_16x16x32_bf16 v[220:223], v[104:107], v[160:163], v[220:223]\n" \
    "v_mfma_f32_16x16x32_bf16 v[224:227], v[128:131], v[160:163], v[224:227]\n" \
    "s_waitcnt vmcnt(3)\n" \
    "v_mfma_f32_16x16x32_bf16 v[204:207], v[12:15], v[164:167], v[204:207]\n" \
    "v_mfma_f32_16x16x32_bf16 v[208:211], v[36:39], v[164:167], v[208:211]\n" \
    "v_max3_u32 v238, v238, v163, v164\n" \
    "v_mfma_f32_16x16x32_bf16 v[212:215], v[60:63], v[164:167], v[212:215]\n" \
    "v_mfma_f32_16x16x32_bf16 v[216:219], v[84:87], v[164:167], v[216:219]\n" \
    "v_max3_u32 v238, v238, v165, v166\n" \
    "v_mfma_f32_16x16x32_bf16 v[220:223], v[108:111], v[164:167], v[220:223]\n" \
    "v_mfma_f32_16x16x32_bf16 v[224:227], v[132:135], v[164:167], v[224:227]\n" \
    "s_waitcnt vmcnt(2)\n" \
    "v_mfma_f32_16x16x32_bf16 v[204:207], v[16:19], v[168:171], v[204:207]\n" \
    "s_waitcnt vmcnt(1)\n" \
    "v_mfma_f32_16x16x32_bf16 v[204:207], v[20:23], v[172:175], v[204:207]\n" \
    "v_mfma_f32_16x16x32_bf16 v[208:211], v[40:43], v[168:171], v[208:211]\n" \
    "v_max3_u32 v238, v238, v167, v168\n" \
    "v_mfma_f32_16x16x32_bf16 v[208:211], v[44:47], v[172:175], v[208:211]\n" \
    "v_max3_u32 v238, v238, v169, v170\n" \
    "v_mfma_f32_16x16x32_bf16 v[212:215], v[64:67], v[168:171], v[212:215]\n" \
    "v_max3_u32 v238, v238, v171, v172\n" \
    "v_mfma_f32_16x16x32_bf16 v[212:215], v[68:71], v[172:175], v[212:215]\n" \
    "v_max3_u32 v238, v238, v173, v174\n" \
    "v_mfma_f32_16x16x32_bf16 v[216:219], v[88:91], v[168:171], v[216:219]\n" \
    "s_waitcnt vmcnt(0)\n" \
    "v_max3_u32 v238, v238, v175, v176\n" \
    "v_mfma_f32_16x16x32_bf16 v[216:219], v[92:95], v[172:175], v[216:219]\n" \
    "v_max_u32 v238, v238, v177\n" \
    "ds_write_b128 v145, v[204:207] offset:0\n" \
    "v_mfma_f32_16x16x32_bf16 v[220:223], v[112:115], v[168:171], v[220:223]\n" \
    "v_mfma_f32_16x16x32_bf16 v[220:223], v[116:119], v[172:175], v[220:223]\n" \
    "ds_write_b128 v145, v[208:211] offset:1024\n" \
    "v_mfma_f32_16x16x32_bf16 v[224:227], v[136:139], v[168:171], v[224:227]\n" \
    "v_mfma_f32_16x16x32_bf16 v[224:227], v[140:143], v[172:175], v[224:227]\n" \
    "ds_write_b128 v145, v[212:215] offset:2048\n" \
    "v_cmp_eq_u32 vcc, -1, v238\n" \
    "ds_write_b128 v145, v[216:219] offset:3072\n" \
    "s_nop 1\n" \
    "ds_write_b128 v145, v[220:223] offset:4096\n" \
    "s_nop 1\n" \
    "ds_write_b128 v145, v[224:227] offset:5120\n" \
    "s_cbranch_vccnz .Ldn_cNa_%=\n" \
    ".Ldn_xNa_%=:\n" \
    "v_lshlrev_b32 v240, 16, v176\n" \
    "v_and_b32 v241, 0xffff0000, v176\n" \
    "v_lshlrev_b32 v242, 16, v177\n" \
    "v_and_b32 v243, 0xffff0000, v177\n" \
    "s_mul_i32 s39, s33, 98304\n" \
    "s_add_u32 s39, s39, s17\n" \
    "s_add_u32 s39, s39, 6291456\n" \
    "buffer_load_dwordx4 v[178:181], v144, s[8:11], s39 offen offset:0 sc1\n" \
    "buffer_load_dwordx4 v[182:185], v144, s[8:11], s39 offen offset:1024 sc1\n" \
    "buffer_load_dwordx4 v[186:189], v144, s[8:11], s39 offen offset:2048 sc1\n" \
    "buffer_load_dwordx4 v[190:193], v144, s[8:11], s39 offen offset:3072 sc1\n" \
    "s_add_u32 s39, s39, 4096\n" \
    "buffer_load_dwordx4 v[194:197], v144, s[8:11], s39 offen offset:0 sc1\n" \
    "buffer_load_dwordx4 v[198:201], v144, s[8:11], s39 offen offset:1024 sc1\n" \
    "s_mul_i32 s39, s33, 40960\n" \
    "s_add_u32 s39, s39, %[x1]+2621440\n" \
    "buffer_load_dwordx2 v[202:203], v147, s[8:11], s39 offen sc1\n" \
    "s_add_u32 s48, s32, 3\n" \
    "s_min_u32 s48, s48, 64\n" \
    "s_and_b32 s49, s48, 1\n" \
    "s_xor_b32 s49, s49, 1\n" \
    "s_lshl_b32 s49, s49, 9\n" \
    "s_add_u32 s49, s49, s18\n" \
    "buffer_load_dwordx2 v[150:151], off, s[8:11], s49 sc1\n" \
    "s_waitcnt lgkmcnt(0)\n" \
    "s_barrier\n" \
    "s_barrier\n" \
    "s_add_u32 s32, s32, 1\n" \
    "s_cmp_lt_u32 s32, %[eup]+1\n" \
    "s_cbranch_scc1 .Ldn_lN_%=\n" \
    "s_waitcnt vmcnt(0)\n" \
    ".Ldn_mN_%=:\n" \
    "s_memrealtime s[50:51]\n" \
    "v_readfirstlane_b32 s36, v151\n" \
    "v_readfirstlane_b32 s35, v150\n" \
    "s_waitcnt lgkmcnt(0)\n" \
    "s_sub_u32 s35, s50, s35\n" \
    "s_cmp_ge_i32 s35, %[dxd]\n" \
    "s_addc_u32 s36, s36, 0\n" \
    "s_cmp_eq_u32 s32, 63\n" \
    "s_cselect_b32 s36, 0, s36\n" \
    "s_cmp_gt_i32 s36, s48\n" \
    "s_cbranch_scc0 .Ldn_ndN_%=\n" \
    "s_mul_i32 s39, s32, 98304\n" \
    "s_add_u32 s39, s39, s17\n" \
    "buffer_load_dwordx4 v[152:155], v144, s[8:11], s39 offen offset:0 sc1\n" \
    "buffer_load_dwordx4 v[156:159], v144, s[8:11], s39 offen offset:1024 sc1\n" \
    "buffer_load_dwordx4 v[160:163], v144, s[8:11], s39 offen offset:2048 sc1\n" \
    "buffer_load_dwordx4 v[164:167], v144, s[8:11], s39 offen offset:3072 sc1\n" \
    "s_add_u32 s39, s39, 4096\n" \
    "buffer_load_dwordx4 v[168:171], v144, s[8:11], s39 offen offset:0 sc1\n" \
    "buffer_load_dwordx4 v[172:175], v144, s[8:11], s39 offen offset:1024 sc1\n" \
    "s_mul_i32 s39, s32, 40960\n" \
    "s_add_u32 s39, s39, %[x1]+0\n" \
    "buffer_load_dwordx2 v[176:177], v147, s[8:11], s39 offen sc1\n" \
    "s_waitcnt vmcnt(7)\n" \
    "s_mov_b32 s37, 0\n" \
    ".Ldn_rNb_%=:\n" \
    "v_mfma_f32_16x16x32_bf16 v[204:207], v[0:3], v[178:181], 0\n" \
    "v_mfma_f32_16x16x32_bf16 v[204:207], v[4:7], v[182:185], v[204:207]\n" \
    "v_mfma_f32_16x16x32_bf16 v[204:207], v[8:11], v[186:189], v[204:207]\n" \
    "v_mfma_f32_16x16x32_bf16 v[204:207], v[12:15], v[190:193], v[204:207]\n" \
    "v_mfma_f32_16x16x32_bf16 v[204:207], v[16:19], v[194:197], v[204:207]\n" \
    "v_mfma_f32_16x16x32_bf16 v[204:207], v[20:23], v[198:201], v[204:207]\n" \
    "v_mfma_f32_16x16x32_bf16 v[208:211], v[24:27], v[178:181], 0\n" \
    "v_max3_u32 v238, v178, v179, v180\n" \
    "v_mfma_f32_16x16x32_bf16 v[208:211], v[28:31], v[182:185], v[208:211]\n" \
    "v_max3_u32 v238, v238, v181, v182\n" \
    "v_mfma_f32_16x16x32_bf16 v[208:211], v[32:35], v[186:189], v[208:211]\n" \
    "v_max3_u32 v238, v238, v183, v184\n" \
    "v_mfma_f32_16x16x32_bf16 v[208:211], v[36:39], v[190:193], v[208:211]\n" \
    "v_max3_u32 v238, v238, v185, v186\n" \
    "v_mfma_f32_16x16x32_bf16 v[208:211], v[40:43], v[194:197], v[208:211]\n" \
    "v_max3_u32 v238, v238, v187, v188\n" \
    "v_mfma_f32_16x16x32_bf16 v[208:211], v[44:47], v[198:201], v[208:211]\n" \
    "v_max3_u32 v238, v238, v189, v190\n" \
    "v_mfma_f32_16x16x32_bf16 v[212:215], v[48:51], v[178:181], 0\n" \
    "v_max3_u32 v238, v238, v191, v192\n" \
    "v_mfma_f32_16x16x32_bf16 v[212:215], v[52:55], v[182:185], v[212:215]\n" \
    "v_max3_u32 v238, v238, v193, v194\n" \
    "v_mfma_f32_16x16x32_bf16 v[212:215], v[56:59], v[186:189], v[212:215]\n" \
    "v_max3_u32 v238, v238, v195, v196\n" \
    "v_mfma_f32_16x16x32_bf16 v[212:215], v[60:63], v[190:193], v[212:215]\n" \
    "v_max3_u32 v238, v238, v197, v198\n" \
    "v_mfma_f32_16x16x32_bf16 v[212:215], v[64:67], v[194:197], v[212:215]\n" \
    "v_max3_u32 v238, v238, v199, v200\n" \
    "v_mfma_f32_16x16x32_bf16 v[212:215], v[68:71], v[198:201], v[212:215]\n" \
    "v_max3_u32 v238, v238, v201, v202\n" \
    "ds_write_b128 v145, v[204:207] offset:49152\n" \
    "v_mfma_f32_16x16x32_bf16 v[216:219], v[72:75], v[178:181], 0\n" \
    "v_max_u32 v238, v238, v203\n" \
    "v_mfma_f32_16x16x32_bf16 v[216:219], v[76:79], v[182:185], v[216:219]\n" \
    "v_mfma_f32_16x16x32_bf16 v[216:219], v[80:83], v[186:189], v[216:219]\n" \
    "v_mfma_f32_16x16x32_bf16 v[216:219], v[84:87], v[190:193], v[216:219]\n" \
    "v_mfma_f32_16x16x32_bf16 v[216:219], v[88:91], v[194:197], v[216:219]\n" \
    "v_mfma_f32_16x16x32_bf16 v[216:219], v[92:95], v[198:201], v[216:219]\n" \
    "ds_write_b128 v145, v[208:211] offset:50176\n" \
    "v_mfma_f32_16x16x32_bf16 v[220:223], v[96:99], v[178:181], 0\n" \
    "v_mfma_f32_16x16x32_bf16 v[220:223], v[100:103], v[182:185], v[220:223]\n" \
    "v_mfma_f32_16x16x32_bf16 v[220:223], v[104:107], v[186:189], v[220:223]\n" \
    "v_mfma_f32_16x16x32_bf16 v[220:223], v[108:111], v[190:193], v[220:223]\n" \
    "v_mfma_f32_16x16x32_bf16 v[220:223], v[112:115], v[194:197], v[220:223]\n" \
    "v_mfma_f32_16x16x32_bf16 v[220:223], v[116:119], v[198:201], v[220:223]\n" \
    "ds_write_b128 v145, v[212:215] offset:51200\n" \
    "v_mfma_f32_16x16x32_bf16 v[224:227], v[120:123], v[178:181], 0\n" \
    "v_mfma_f32_16x16x32_bf16 v[224:227], v[124:127], v[182:185], v[224:227]\n" \
    "v_mfma_f32_16x16x32_bf16 v[224:227], v[128:131], v[186:189], v[224:227]\n" \
    "v_mfma_f32_16x16x32_bf16 v[224:227], v[132:135], v[190:193], v[224:227]\n" \
    "v_mfma_f32_16x16x32_bf16 v[224:227], v[136:139], v[194:197], v[224:227]\n" \
    "v_mfma_f32_16x16x32_bf16 v[224:227], v[140:143], v[198:201], v[224:227]\n" \
    "ds_write_b128 v145, v[216:219] offset:52224\n" \
    "v_cmp_eq_u32 vcc, -1, v238\n" \
    "ds_write_b128 v145, v[220:223] offset:53248\n" \
    "s_nop 4\n" \
    "ds_write_b128 v145, v[224:227] offset:54272\n" \
    "s_cbranch_vccnz .Ldn_cNb_%=\n" \
    ".Ldn_xNb_%=:\n" \
    "s_branch .Ldn_jnN_%=\n" \
    ".Ldn_ndN_%=:\n" \
    "s_waitcnt vmcnt(0)\n" \
    "s_mov_b32 s37, 0\n" \
    ".Ldn_rNc_%=:\n" \
    "v_mfma_f32_16x16x32_bf16 v[204:207], v[0:3], v[178:181], 0\n" \
    "v_mfma_f32_16x16x32_bf16 v[204:207], v[4:7], v[182:185], v[204:207]\n" \
    "v_mfma_f32_16x16x32_bf16 v[204:207], v[8:11], v[186:189], v[204:207]\n" \
    "v_mfma_f32_16x16x32_bf16 v[204:207], v[12:15], v[190:193], v[204:207]\n" \
    "v_mfma_f32_16x16x32_bf16 v[204:207], v[16:19], v[194:197], v[204:207]\n" \
    "v_mfma_f32_16x16x32_bf16 v[204:207], v[20:23], v[198:201], v[204:207]\n" \
    "v_mfma_f32_16x16x32_bf16 v[208:211], v[24:27], v[178:181], 0\n" \
    "v_max3_u32 v238, v178, v179, v180\n" \
    "v_mfma_f32_16x16x32_bf16 v[208:211], v[28:31], v[182:185], v[208:211]\n" \
    "v_max3_u32 v238, v238, v181, v182\n" \
    "v_mfma_f32_16x16x32_bf16 v[208:211], v[32:35], v[186:189], v[208:211]\n" \
    "v_max3_u32 v238, v238, v183, v184\n" \
    "v_mfma_f32_16x16x32_bf16 v[208:211], v[36:39], v[190:193], v[208:211]\n" \
    "v_max3_u32 v238, v238, v185, v186\n" \
    "v_mfma_f32_16x16x32_bf16 v[208:211], v[40:43], v[194:197], v[208:211]\n" \
    "v_max3_u32 v238, v238, v187, v188\n" \
    "v_mfma_f32_16x16x32_bf16 v[208:211], v[44:47], v[198:201], v[208:211]\n" \
    "v_max3_u32 v238, v238, v189, v190\n" \
    "v_mfma_f32_16x16x32_bf16 v[212:215], v[48:51], v[178:181], 0\n" \
    "v_max3_u32 v238, v238, v191, v192\n" \
    "v_mfma_f32_16x16x32_bf16 v[212:215], v[52:55], v[182:185], v[212:215]\n" \
    "v_max3_u32 v238, v238, v193, v194\n" \
    "v_mfma_f32_16x16x32_bf16 v[212:215], v[56:59], v[186:189], v[212:215]\n" \
    "v_max3_u32 v238, v238, v195, v196\n" \
    "v_mfma_f32_16x16x32_bf16 v[212:215], v[60:63], v[190:193], v[212:215]\n" \
    "v_max3_u32 v238, v238, v197, v198\n" \
    "v_mfma_f32_16x16x32_bf16 v[212:215], v[64:67], v[194:197], v[212:215]\n" \
    "v_max3_u32 v238, v238, v199, v200\n" \
    "v_mfma_f32_16x16x32_bf16 v[212:215], v[68:71], v[198:201], v[212:215]\n" \
    "v_max3_u32 v238, v238, v201, v202\n" \
    "ds_write_b128 v145, v[204:207] offset:49152\n" \
    "v_mfma_f32_16x16x32_bf16 v[216:219], v[72:75], v[178:181], 0\n" \
    "v_max_u32 v238, v238, v203\n" \
    "v_mfma_f32_16x16x32_bf16 v[216:219], v[76:79], v[182:185], v[216:219]\n" \
    "v_mfma_f32_16x16x32_bf16 v[216:219], v[80:83], v[186:189], v[216:219]\n" \
    "v_mfma_f32_16x16x32_bf16 v[216:219], v[84:87], v[190:193], v[216:219]\n" \
    "v_mfma_f32_16x16x32_bf16 v[216:219], v[88:91], v[194:197], v[216:219]\n" \
    "v_mfma_f32_16x16x32_bf16 v[216:219], v[92:95], v[198:201], v[216:219]\n" \
    "ds_write_b128 v145, v[208:211] offset:50176\n" \
    "v_mfma_f32_16x16x32_bf16 v[220:223], v[96:99], v[178:181], 0\n" \
    "v_mfma_f32_16x16x32_bf16 v[220:223], v[100:103], v[182:185], v[220:223]\n" \
    "v_mfma_f32_16x16x32_bf16 v[220:223], v[104:107], v[186:189], v[220:223]\n" \
    "v_mfma_f32_16x16x32_bf16 v[220:223], v[108:111], v[190:193], v[220:223]\n" \
    "v_mfma_f32_16x16x32_bf16 v[220:223], v[112:115], v[194:197], v[220:223]\n" \
    "v_mfma_f32_16x16x32_bf16 v[220:223], v[116:119], v[198:201], v[220:223]\n" \
    "ds_write_b128 v145, v[212:215] offset:51200\n" \
    "v_mfma_f32_16x16x32_bf16 v[224:227], v[120:123], v[178:181], 0\n" \
    "v_mfma_f32_16x16x32_bf16 v[224:227], v[124:127], v[182:185], v[224:227]\n" \
    "v_mfma_f32_16x16x32_bf16 v[224:227], v[128:131], v[186:189], v[224:227]\n" \
    "v_mfma_f32_16x16x32_bf16 v[224:227], v[132:135], v[190:193], v[224:227]\n" \
    "v_mfma_f32_16x16x32_bf16 v[224:227], v[136:139], v[194:197], v[224:227]\n" \
    "v_mfma_f32_16x16x32_bf16 v[224:227], v[140:143], v[198:201], v[224:227]\n" \
    "ds_write_b128 v145, v[216:219] offset:52224\n" \
    "v_cmp_eq_u32 vcc, -1, v238\n" \
    "ds_write_b128 v145, v[220:223] offset:53248\n" \
    "s_nop 4\n" \
    "ds_write_b128 v145, v[224:227] offset:54272\n" \
    "s_cbranch_vccnz .Ldn_cNc_%=\n" \
    ".Ldn_xNc_%=:\n" \
    "s_mov_b32 s37, 0\n" \
    "s_waitcnt vmcnt(63)\n" \
    ".Ldn_Nep_%=:\n" \
    "v_readfirstlane_b32 s36, v151\n" \
    "s_nop 0\n" \
    "s_cmp_gt_i32 s36, s48\n" \
    "s_cbranch_scc1 .Ldn_Nex_%=\n" \
    "s_cmp_eq_u32 s36, s48\n" \
    "s_cbranch_scc1 .Ldn_New_%=\n" \
    "s_sleep 3\n" \
    "buffer_load_dwordx2 v[150:151], off, s[8:11], s49 sc1\n" \
    "s_waitcnt vmcnt(0)\n" \
    "s_add_u32 s37, s37, 1\n" \
    "s_cmp_lt_u32 s37, s38\n" \
    "s_cbranch_scc1 .Ldn_Nep_%=\n" \
    "s_mov_b32 s38, 0\n" \
    "s_branch .Ldn_Nex_%=\n" \
    ".Ldn_New_%=:\n" \
    "v_readfirstlane_b32 s36, v150\n" \
    "s_cmp_eq_u32 s32, 63\n" \
    "s_cselect_b32 s34, %[dlast], 0\n" \
    "s_add_u32 s36, s36, s34\n" \
    "s_add_u32 s36, s36, %[dxd]\n" \
    "s_branch .Ldn_Netb_%=\n" \
    ".Ldn_Neta_%=:\n" \
    "s_sleep 1\n" \
    ".Ldn_Netb_%=:\n" \
    "s_memrealtime vcc\n" \
    "s_waitcnt lgkmcnt(0)\n" \
    "s_sub_i32 vcc_lo, s36, vcc_lo\n" \
    "s_cmp_gt_i32 vcc_lo, 8\n" \
    "s_cbranch_scc1 .Ldn_Neta_%=\n" \
    "s_cmp_gt_i32 vcc_lo, 0\n" \
    "s_cbranch_scc1 .Ldn_Netb_%=\n" \
    ".Ldn_Nex_%=:\n" \
    "s_mul_i32 s39, s32, 98304\n" \
    "s_add_u32 s39, s39, s17\n" \
    "buffer_load_dwordx4 v[152:155], v144, s[8:11], s39 offen offset:0 sc1\n" \
    "buffer_load_dwordx4 v[156:159], v144, s[8:11], s39 offen offset:1024 sc1\n" \
    "buffer_load_dwordx4 v[160:163], v144, s[8:11], s39 offen offset:2048 sc1\n" \
    "buffer_load_dwordx4 v[164:167], v144, s[8:11], s39 offen offset:3072 sc1\n" \
    "s_add_u32 s39, s39, 4096\n" \
    "buffer_load_dwordx4 v[168:171], v144, s[8:11], s39 offen offset:0 sc1\n" \
    "buffer_load_dwordx4 v[172:175], v144, s[8:11], s39 offen offset:1024 sc1\n" \
    "s_mul_i32 s39, s32, 40960\n" \
    "s_add_u32 s39, s39, %[x1]+0\n" \
    "buffer_load_dwordx2 v[176:177], v147, s[8:11], s39 offen sc1\n" \
    ".Ldn_jnN_%=:\n" \
    "v_lshlrev_b32 v244, 16, v202\n" \
    "v_and_b32 v245, 0xffff0000, v202\n" \
    "v_lshlrev_b32 v246, 16, v203\n" \
    "v_and_b32 v247, 0xffff0000, v203\n" \
    "s_add_u32 s48, s32, 3\n" \
    "s_min_u32 s48, s48, 64\n" \
    "s_and_b32 s49, s48, 1\n" \
    "s_xor_b32 s49, s49, 1\n" \
    "s_lshl_b32 s49, s49, 9\n" \
    "s_add_u32 s49, s49, s18\n" \
    "buffer_load_dwordx2 v[150:151], off, s[8:11], s49 sc1\n" \
    "s_add_u32 s36, s33, 1\n" \
    "s_mov_b32 s37, 0\n" \
    ".Ldn_rNd_%=:\n" \
    "s_waitcnt vmcnt(7)\n" \
    "v_mfma_f32_16x16x32_bf16 v[204:207], v[0:3], v[152:155], 0\n" \
    "v_mfma_f32_16x16x32_bf16 v[208:211], v[24:27], v[152:155], 0\n" \
    "v_mfma_f32_16x16x32_bf16 v[212:215], v[48:51], v[152:155], 0\n" \
    "v_mfma_f32_16x16x32_bf16 v[216:219], v[72:75], v[152:155], 0\n" \
    "v_max3_u32 v238, v152, v153, v154\n" \
    "v_mfma_f32_16x16x32_bf16 v[220:223], v[96:99], v[152:155], 0\n" \
    "v_mfma_f32_16x16x32_bf16 v[224:227], v[120:123], v[152:155], 0\n" \
    "s_waitcnt vmcnt(6)\n" \
    "v_mfma_f32_16x16x32_bf16 v[204:207], v[4:7], v[156:159], v[204:207]\n" \
    "v_mfma_f32_16x16x32_bf16 v[208:211], v[28:31], v[156:159], v[208:211]\n" \
    "v_max3_u32 v238, v238, v155, v156\n" \
    "v_mfma_f32_16x16x32_bf16 v[212:215], v[52:55], v[156:159], v[212:215]\n" \
    "v_mfma_f32_16x16x32_bf16 v[216:219], v[76:79], v[156:159], v[216:219]\n" \
    "v_max3_u32 v238, v238, v157, v158\n" \
    "v_mfma_f32_16x16x32_bf16 v[220:223], v[100:103], v[156:159], v[220:223]\n" \
    "v_mfma_f32_16x16x32_bf16 v[224:227], v[124:127], v[156:159], v[224:227]\n" \
    "s_waitcnt vmcnt(5)\n" \
    "v_mfma_f32_16x16x32_bf16 v[204:207], v[8:11], v[160:163], v[204:207]\n" \
    "v_mfma_f32_16x16x32_bf16 v[208:211], v[32:35], v[160:163], v[208:211]\n" \
    "v_max3_u32 v238, v238, v159, v160\n" \
    "v_mfma_f32_16x16x32_bf16 v[212:215], v[56:59], v[160:163], v[212:215]\n" \
    "v_mfma_f32_16x16x32_bf16 v[216:219], v[80:83], v[160:163], v[216:219]\n" \
    "v_max3_u32 v238, v238, v161, v162\n" \
    "v_mfma_f32_16x16x32_bf16 v[220:223], v[104:107], v[160:163], v[220:223]\n" \
    "v_mfma_f32_16x16x32_bf16 v[224:227], v[128:131], v[160:163], v[224:227]\n" \
    "s_waitcnt vmcnt(4)\n" \
    "v_mfma_f32_16x16x32_bf16 v[204:207], v[12:15], v[164:167], v[204:207]\n" \
    "v_mfma_f32_16x16x32_bf16 v[208:211], v[36:39], v[164:167], v[208:211]\n" \
    "v_max3_u32 v238, v238, v163, v164\n" \
    "v_mfma_f32_16x16x32_bf16 v[212:215], v[60:63], v[164:167], v[212:215]\n" \
    "v_mfma_f32_16x16x32_bf16 v[216:219], v[84:87], v[164:167], v[216:219]\n" \
    "v_max3_u32 v238, v238, v165, v166\n" \
    "v_mfma_f32_16x16x32_bf16 v[220:223], v[108:111], v[164:167], v[220:223]\n" \
    "v_mfma_f32_16x16x32_bf16 v[224:227], v[132:135], v[164:167], v[224:227]\n" \
    "s_waitcnt vmcnt(3)\n" \
    "v_mfma_f32_16x16x32_bf16 v[204:207], v[16:19], v[168:171], v[204:207]\n" \
    "s_waitcnt vmcnt(2)\n" \
    "v_mfma_f32_16x16x32_bf16 v[204:207], v[20:23], v[172:175], v[204:207]\n" \
    "v_mfma_f32_16x16x32_bf16 v[208:211], v[40:43], v[168:171], v[208:211]\n" \
    "v_max3_u32 v238, v238, v167, v168\n" \
    "v_mfma_f32_16x16x32_bf16 v[208:211], v[44:47], v[172:175], v[208:211]\n" \
    "v_max3_u32 v238, v238, v169, v170\n" \
    "v_mfma_f32_16x16x32_bf16 v[212:215], v[64:67], v[168:171], v[212:215]\n" \
    "v_max3_u32 v238, v238, v171, v172\n" \
    "v_mfma_f32_16x16x32_bf16 v[212:215], v[68:71], v[172:175], v[212:215]\n" \
    "v_max3_u32 v238, v238, v173, v174\n" \
    "v_mfma_f32_16x16x32_bf16 v[216:219], v[88:91], v[168:171], v[216:219]\n" \
    "s_waitcnt vmcnt(1)\n" \
    "v_max3_u32 v238, v238, v175, v176\n" \
    "v_mfma_f32_16x16x32_bf16 v[216:219], v[92:95], v[172:175], v[216:219]\n" \
    "v_max_u32 v238, v238, v177\n" \
    "ds_write_b128 v145, v[204:207] offset:0\n" \
    "v_mfma_f32_16x16x32_bf16 v[220:223], v[112:115], v[168:171], v[220:223]\n" \
    "v_mfma_f32_16x16x32_bf16 v[220:223], v[116:119], v[172:175], v[220:223]\n" \
    "ds_write_b128 v145, v[208:211] offset:1024\n" \
    "v_mfma_f32_16x16x32_bf16 v[224:227], v[136:139], v[168:171], v[224:227]\n" \
    "v_mfma_f32_16x16x32_bf16 v[224:227], v[140:143], v[172:175], v[224:227]\n" \
    "ds_write_b128 v145, v[212:215] offset:2048\n" \
    "v_cmp_eq_u32 vcc, -1, v238\n" \
    "ds_write_b128 v145, v[216:219] offset:3072\n" \
    "s_nop 1\n" \
    "ds_write_b128 v145, v[220:223] offset:4096\n" \
    "s_nop 1\n" \
    "ds_write_b128 v145, v[224:227] offset:5120\n" \
    "s_cbranch_vccnz .Ldn_cNd_%=\n" \
    ".Ldn_xNd_%=:\n" \
    "v_lshlrev_b32 v240, 16, v176\n" \
    "v_and_b32 v241, 0xffff0000, v176\n" \
    "v_lshlrev_b32 v242, 16, v177\n" \
    "v_and_b32 v243, 0xffff0000, v177\n" \
    "s_mul_i32 s39, s36, 98304\n" \
    "s_add_u32 s39, s39, s17\n" \
    "s_add_u32 s39, s39, 6291456\n" \
    "buffer_load_dwordx4 v[178:181], v144, s[8:11], s39 offen offset:0 sc1\n" \
    "buffer_load_dwordx4 v[182:185], v144, s[8:11], s39 offen offset:1024 sc1\n" \
    "buffer_load_dwordx4 v[186:189], v144, s[8:11], s39 offen offset:2048 sc1\n" \
    "buffer_load_dwordx4 v[190:193], v144, s[8:11], s39 offen offset:3072 sc1\n" \
    "s_add_u32 s39, s39, 4096\n" \
    "buffer_load_dwordx4 v[194:197], v144, s[8:11], s39 offen offset:0 sc1\n" \
    "buffer_load_dwordx4 v[198:201], v144, s[8:11], s39 offen offset:1024 sc1\n" \
    "s_mul_i32 s39, s36, 40960\n" \
    "s_add_u32 s39, s39, %[x1]+2621440\n" \
    "buffer_load_dwordx2 v[202:203], v147, s[8:11], s39 offen sc1\n" \
    "s_waitcnt vmcnt(7) lgkmcnt(0)\n" \
    "s_barrier\n" \
    "s_barrier\n" \
    "s_add_u32 s33, s33, 1\n" \
    "s_add_u32 s32, s32, 1\n" \
    "s_cmp_lt_u32 s32, 64\n" \
    "s_cbranch_scc1 .Ldn_mN_%=\n" \
    "s_add_u32 s18, s18, 1024\n" \
    "s_waitcnt vmcnt(0)\n" \
    "s_add_u32 s48, s33, 2\n" \
    "s_min_u32 s48, s48, 64\n" \
    "s_and_b32 s49, s48, 1\n" \
    "s_xor_b32 s49, s49, 1\n" \
    "s_lshl_b32 s49, s49, 9\n" \
    "s_add_u32 s49, s49, s18\n" \
    "buffer_load_dwordx2 v[150:151], off, s[8:11], s49 sc1\n" \
    ".Ldn_nN_%=:\n" \
    "s_mov_b32 s37, 0\n" \
    "s_waitcnt vmcnt(0)\n" \
    ".Ldn_Nfp_%=:\n" \
    "v_readfirstlane_b32 s36, v151\n" \
    "s_nop 0\n" \
    "s_cmp_gt_i32 s36, s48\n" \
    "s_cbranch_scc1 .Ldn_Nfx_%=\n" \
    "s_cmp_eq_u32 s36, s48\n" \
    "s_cbranch_scc1 .Ldn_Nfw_%=\n" \
    "s_sleep 3\n" \
    "buffer_load_dwordx2 v[150:151], off, s[8:11], s49 sc1\n" \
    "s_waitcnt vmcnt(0)\n" \
    "s_add_u32 s37, s37, 1\n" \
    "s_cmp_lt_u32 s37, s38\n" \
    "s_cbranch_scc1 .Ldn_Nfp_%=\n" \
    "s_mov_b32 s38, 0\n" \
    "s_branch .Ldn_Nfx_%=\n" \
    ".Ldn_Nfw_%=:\n" \
    "v_readfirstlane_b32 s36, v150\n" \
    "s_cmp_eq_u32 s33, 63\n" \
    "s_cselect_b32 s34, %[dlast], 0\n" \
    "s_add_u32 s36, s36, s34\n" \
    "s_add_u32 s36, s36, %[dxd]\n" \
    "s_branch .Ldn_Nftb_%=\n" \
    ".Ldn_Nfta_%=:\n" \
    "s_sleep 1\n" \
    ".Ldn_Nftb_%=:\n" \
    "s_memrealtime vcc\n" \
    "s_waitcnt lgkmcnt(0)\n" \
    "s_sub_i32 vcc_lo, s36, vcc_lo\n" \
    "s_cmp_gt_i32 vcc_lo, 8\n" \
    "s_cbranch_scc1 .Ldn_Nfta_%=\n" \
    "s_cmp_gt_i32 vcc_lo, 0\n" \
    "s_cbranch_scc1 .Ldn_Nftb_%=\n" \
    ".Ldn_Nfx_%=:\n" \
    "s_mul_i32 s39, s33, 98304\n" \
    "s_add_u32 s39, s39, s17\n" \
    "s_add_u32 s39, s39, 6291456\n" \
    "buffer_load_dwordx4 v[178:181], v144, s[8:11], s39 offen offset:0 sc1\n" \
    "buffer_load_dwordx4 v[182:185], v144, s[8:11], s39 offen offset:1024 sc1\n" \
    "buffer_load_dwordx4 v[186:189], v144, s[8:11], s39 offen offset:2048 sc1\n" \
    "buffer_load_dwordx4 v[190:193], v144, s[8:11], s39 offen offset:3072 sc1\n" \
    "s_add_u32 s39, s39, 4096\n" \
    "buffer_load_dwordx4 v[194:197], v144, s[8:11], s39 offen offset:0 sc1\n" \
    "buffer_load_dwordx4 v[198:201], v144, s[8:11], s39 offen offset:1024 sc1\n" \
    "s_mul_i32 s39, s33, 40960\n" \
    "s_add_u32 s39, s39, %[x1]+2621440\n" \
    "buffer_load_dwordx2 v[202:203], v147, s[8:11], s39 offen sc1\n" \
    "s_mov_b32 s37, 0\n" \
    ".Ldn_rNf_%=:\n" \
    "s_waitcnt vmcnt(6)\n" \
    "v_mfma_f32_16x16x32_bf16 v[204:207], v[0:3], v[178:181], 0\n" \
    "v_mfma_f32_16x16x32_bf16 v[208:211], v[24:27], v[178:181], 0\n" \
    "v_mfma_f32_16x16x32_bf16 v[212:215], v[48:51], v[178:181], 0\n" \
    "v_mfma_f32_16x16x32_bf16 v[216:219], v[72:75], v[178:181], 0\n" \
    "v_max3_u32 v238, v178, v179, v180\n" \
    "v_mfma_f32_16x16x32_bf16 v[220:223], v[96:99], v[178:181], 0\n" \
    "v_mfma_f32_16x16x32_bf16 v[224:227], v[120:123], v[178:181], 0\n" \
    "s_waitcnt vmcnt(5)\n" \
    "v_mfma_f32_16x16x32_bf16 v[204:207], v[4:7], v[182:185], v[204:207]\n" \
    "v_mfma_f32_16x16x32_bf16 v[208:211], v[28:31], v[182:185], v[208:211]\n" \
    "v_max3_u32 v238, v238, v181, v182\n" \
    "v_mfma_f32_16x16x32_bf16 v[212:215], v[52:55], v[182:185], v[212:215]\n" \
    "v_mfma_f32_16x16x32_bf16 v[216:219], v[76:79], v[182:185], v[216:219]\n" \
    "v_max3_u32 v238, v238, v183, v184\n" \
    "v_mfma_f32_16x16x32_bf16 v[220:223], v[100:103], v[182:185], v[220:223]\n" \
    "v_mfma_f32_16x16x32_bf16 v[224:227], v[124:127], v[182:185], v[224:227]\n" \
    "s_waitcnt vmcnt(4)\n" \
    "v_mfma_f32_16x16x32_bf16 v[204:207], v[8:11], v[186:189], v[204:207]\n" \
    "v_mfma_f32_16x16x32_bf16 v[208:211], v[32:35], v[186:189], v[208:211]\n" \
    "v_max3_u32 v238, v238, v185, v186\n" \
    "v_mfma_f32_16x16x32_bf16 v[212:215], v[56:59], v[186:189], v[212:215]\n" \
    "v_mfma_f32_16x16x32_bf16 v[216:219], v[80:83], v[186:189], v[216:219]\n" \
    "v_max3_u32 v238, v238, v187, v188\n" \
    "v_mfma_f32_16x16x32_bf16 v[220:223], v[104:107], v[186:189], v[220:223]\n" \
    "v_mfma_f32_16x16x32_bf16 v[224:227], v[128:131], v[186:189], v[224:227]\n" \
    "s_waitcnt vmcnt(3)\n" \
    "v_mfma_f32_16x16x32_bf16 v[204:207], v[12:15], v[190:193], v[204:207]\n" \
    "v_mfma_f32_16x16x32_bf16 v[208:211], v[36:39], v[190:193], v[208:211]\n" \
    "v_max3_u32 v238, v238, v189, v190\n" \
    "v_mfma_f32_16x16x32_bf16 v[212:215], v[60:63], v[190:193], v[212:215]\n" \
    "v_mfma_f32_16x16x32_bf16 v[216:219], v[84:87], v[190:193], v[216:219]\n" \
    "v_max3_u32 v238, v238, v191, v192\n" \
    "v_mfma_f32_16x16x32_bf16 v[220:223], v[108:111], v[190:193], v[220:223]\n" \
    "v_mfma_f32_16x16x32_bf16 v[224:227], v[132:135], v[190:193], v[224:227]\n" \
    "s_waitcnt vmcnt(2)\n" \
    "v_mfma_f32_16x16x32_bf16 v[204:207], v[16:19], v[194:197], v[204:207]\n" \
    "s_waitcnt vmcnt(1)\n" \
    "v_mfma_f32_16x16x32_bf16 v[204:207], v[20:23], v[198:201], v[204:207]\n" \
    "v_mfma_f32_16x16x32_bf16 v[208:211], v[40:43], v[194:197], v[208:211]\n" \
    "v_max3_u32 v238, v238, v193, v194\n" \
    "v_mfma_f32_16x16x32_bf16 v[208:211], v[44:47], v[198:201], v[208:211]\n" \
    "v_max3_u32 v238, v238, v195, v196\n" \
    "v_mfma_f32_16x16x32_bf16 v[212:215], v[64:67], v[194:197], v[212:215]\n" \
    "v_max3_u32 v238, v238, v197, v198\n" \
    "v_mfma_f32_16x16x32_bf16 v[212:215], v[68:71], v[198:201], v[212:215]\n" \
    "v_max3_u32 v238, v238, v199, v200\n" \
    "v_mfma_f32_16x16x32_bf16 v[216:219], v[88:91], v[194:197], v[216:219]\n" \
    "s_waitcnt vmcnt(0)\n" \
    "v_max3_u32 v238, v238, v201, v202\n" \
    "v_mfma_f32_16x16x32_bf16 v[216:219], v[92:95], v[198:201], v[216:219]\n" \
    "v_max_u32 v238, v238, v203\n" \
    "ds_write_b128 v145, v[204:207] offset:49152\n" \
    "v_mfma_f32_16x16x32_bf16 v[220:223], v[112:115], v[194:197], v[220:223]\n" \
    "v_mfma_f32_16x16x32_bf16 v[220:223], v[116:119], v[198:201], v[220:223]\n" \
    "ds_write_b128 v145, v[208:211] offset:50176\n" \
    "v_mfma_f32_16x16x32_bf16 v[224:227], v[136:139], v[194:197], v[224:227]\n" \
    "v_mfma_f32_16x16x32_bf16 v[224:227], v[140:143], v[198:201], v[224:227]\n" \
    "ds_write_b128 v145, v[212:215] offset:51200\n" \
    "v_cmp_eq_u32 vcc, -1, v238\n" \
    "ds_write_b128 v145, v[216:219] offset:52224\n" \
    "s_nop 1\n" \
    "ds_write_b128 v145, v[220:223] offset:53248\n" \
    "s_nop 1\n" \
    "ds_write_b128 v145, v[224:227] offset:54272\n" \
    "s_cbranch_vccnz .Ldn_cNf_%=\n" \
    ".Ldn_xNf_%=:\n" \
    "v_lshlrev_b32 v244, 16, v202\n" \
    "v_and_b32 v245, 0xffff0000, v202\n" \
    "v_lshlrev_b32 v246, 16, v203\n" \
    "v_and_b32 v247, 0xffff0000, v203\n" \
    "s_add_u32 s48, s33, 3\n" \
    "s_min_u32 s48, s48, 64\n" \
    "s_and_b32 s49, s48, 1\n" \
    "s_xor_b32 s49, s49, 1\n" \
    "s_lshl_b32 s49, s49, 9\n" \
    "s_add_u32 s49, s49, s18\n" \
    "buffer_load_dwordx2 v[150:151], off, s[8:11], s49 sc1\n" \
    "s_waitcnt lgkmcnt(0)\n" \
    "s_barrier\n" \
    "s_barrier\n" \
    "s_add_u32 s33, s33, 1\n" \
    "s_cmp_lt_u32 s33, 64\n" \
    "s_cbranch_scc1 .Ldn_nN_%=\n" \
    "s_waitcnt vmcnt(0)\n" \
    "s_endpgm\n" \
    ".Ldn_cFa_%=:\n" \
    "s_add_u32 s37, s37, 1\n" \
    "s_cmp_lt_u32 s37, s38\n" \
    "s_cselect_b32 s38, s38, 0\n" \
    "s_cbranch_scc0 .Ldn_xFa_%=\n" \
    "s_sleep 1\n" \
    "s_mul_i32 s39, s32, 98304\n" \
    "s_add_u32 s39, s39, s17\n" \
    "buffer_load_dwordx4 v[152:155], v144, s[8:11], s39 offen offset:0 sc1\n" \
    "buffer_load_dwordx4 v[156:159], v144, s[8:11], s39 offen offset:1024 sc1\n" \
    "buffer_load_dwordx4 v[160:163], v144, s[8:11], s39 offen offset:2048 sc1\n" \
    "buffer_load_dwordx4 v[164:167], v144, s[8:11], s39 offen offset:3072 sc1\n" \
    "s_add_u32 s39, s39, 4096\n" \
    "buffer_load_dwordx4 v[168:171], v144, s[8:11], s39 offen offset:0 sc1\n" \
    "buffer_load_dwordx4 v[172:175], v144, s[8:11], s39 offen offset:1024 sc1\n" \
    "s_mul_i32 s39, s32, 40960\n" \
    "s_add_u32 s39, s39, %[x1]+0\n" \
    "buffer_load_dwordx2 v[176:177], v147, s[8:11], s39 offen sc1\n" \
    "s_waitcnt vmcnt(6)\n" \
    "v_max3_u32 v238, v152, v153, v154\n" \
    "s_waitcnt vmcnt(5)\n" \
    "v_max3_u32 v238, v238, v155, v156\n" \
    "v_max3_u32 v238, v238, v157, v158\n" \
    "s_waitcnt vmcnt(4)\n" \
    "v_max3_u32 v238, v238, v159, v160\n" \
    "v_max3_u32 v238, v238, v161, v162\n" \
    "s_waitcnt vmcnt(3)\n" \
    "v_max3_u32 v238, v238, v163, v164\n" \
    "v_max3_u32 v238, v238, v165, v166\n" \
    "s_waitcnt vmcnt(2)\n" \
    "v_max3_u32 v238, v238, v167, v168\n" \
    "v_max3_u32 v238, v238, v169, v170\n" \
    "s_waitcnt vmcnt(1)\n" \
    "v_max3_u32 v238, v238, v171, v172\n" \
    "v_max3_u32 v238, v238, v173, v174\n" \
    "s_waitcnt vmcnt(0)\n" \
    "v_max3_u32 v238, v238, v175, v176\n" \
    "v_max_u32 v238, v238, v177\n" \
    "v_cmp_eq_u32 vcc, -1, v238\n" \
    "s_nop 0\n" \
    "s_cbranch_vccnz .Ldn_cFa_%=\n" \
    "s_waitcnt vmcnt(0) lgkmcnt(0)\n" \
    "s_branch .Ldn_rFa_%=\n" \
    ".Ldn_cFb_%=:\n" \
    "s_add_u32 s37, s37, 1\n" \
    "s_cmp_lt_u32 s37, s38\n" \
    "s_cselect_b32 s38, s38, 0\n" \
    "s_cbranch_scc0 .Ldn_xFb_%=\n" \
    "s_sleep 1\n" \
    "s_mul_i32 s39, s33, 98304\n" \
    "s_add_u32 s39, s39, s17\n" \
    "s_add_u32 s39, s39, 6291456\n" \
    "buffer_load_dwordx4 v[178:181], v144, s[8:11], s39 offen offset:0 sc1\n" \
    "buffer_load_dwordx4 v[182:185], v144, s[8:11], s39 offen offset:1024 sc1\n" \
    "buffer_load_dwordx4 v[186:189], v144, s[8:11], s39 offen offset:2048 sc1\n" \
    "buffer_load_dwordx4 v[190:193], v144, s[8:11], s39 offen offset:3072 sc1\n" \
    "s_add_u32 s39, s39, 4096\n" \
    "buffer_load_dwordx4 v[194:197], v144, s[8:11], s39 offen offset:0 sc1\n" \
    "buffer_load_dwordx4 v[198:201], v144, s[8:11], s39 offen offset:1024 sc1\n" \
    "s_mul_i32 s39, s33, 40960\n" \
    "s_add_u32 s39, s39, %[x1]+2621440\n" \
    "buffer_load_dwordx2 v[202:203], v147, s[8:11], s39 offen sc1\n" \
    "s_waitcnt vmcnt(6)\n" \
    "v_max3_u32 v238, v178, v179, v180\n" \
    "s_waitcnt vmcnt(5)\n" \
    "v_max3_u32 v238, v238, v181, v182\n" \
    "v_max3_u32 v238, v238, v183, v184\n" \
    "s_waitcnt vmcnt(4)\n" \
    "v_max3_u32 v238, v238, v185, v186\n" \
    "v_max3_u32 v238, v238, v187, v188\n" \
    "s_waitcnt vmcnt(3)\n" \
    "v_max3_u32 v238, v238, v189, v190\n" \
    "v_max3_u32 v238, v238, v191, v192\n" \
    "s_waitcnt vmcnt(2)\n" \
    "v_max3_u32 v238, v238, v193, v194\n" \
    "v_max3_u32 v238, v238, v195, v196\n" \
    "s_waitcnt vmcnt(1)\n" \
    "v_max3_u32 v238, v238, v197, v198\n" \
    "v_max3_u32 v238, v238, v199, v200\n" \
    "s_waitcnt vmcnt(0)\n" \
    "v_max3_u32 v238, v238, v201, v202\n" \
    "v_max_u32 v238, v238, v203\n" \
    "v_cmp_eq_u32 vcc, -1, v238\n" \
    "s_nop 0\n" \
    "s_cbranch_vccnz .Ldn_cFb_%=\n" \
    "s_waitcnt vmcnt(0) lgkmcnt(0)\n" \
    "s_branch .Ldn_rFb_%=\n" \
    ".Ldn_cFc_%=:\n" \
    "s_add_u32 s37, s37, 1\n" \
    "s_cmp_lt_u32 s37, s38\n" \
    "s_cselect_b32 s38, s38, 0\n" \
    "s_cbranch_scc0 .Ldn_xFc_%=\n" \
    "s_sleep 1\n" \
    "s_mul_i32 s39, s33, 98304\n" \
    "s_add_u32 s39, s39, s17\n" \
    "s_add_u32 s39, s39, 6291456\n" \
    "buffer_load_dwordx4 v[178:181], v144, s[8:11], s39 offen offset:0 sc1\n" \
    "buffer_load_dwordx4 v[182:185], v144, s[8:11], s39 offen offset:1024 sc1\n" \
    "buffer_load_dwordx4 v[186:189], v144, s[8:11], s39 offen offset:2048 sc1\n" \
    "buffer_load_dwordx4 v[190:193], v144, s[8:11], s39 offen offset:3072 sc1\n" \
    "s_add_u32 s39, s39, 4096\n" \
    "buffer_load_dwordx4 v[194:197], v144, s[8:11], s39 offen offset:0 sc1\n" \
    "buffer_load_dwordx4 v[198:201], v144, s[8:11], s39 offen offset:1024 sc1\n" \
    "s_mul_i32 s39, s33, 40960\n" \
    "s_add_u32 s39, s39, %[x1]+2621440\n" \
    "buffer_load_dwordx2 v[202:203], v147, s[8:11], s39 offen sc1\n" \
    "s_waitcnt vmcnt(6)\n" \
    "v_max3_u32 v238, v178, v179, v180\n" \
    "s_waitcnt vmcnt(5)\n" \
    "v_max3_u32 v238, v238, v181, v182\n" \
    "v_max3_u32 v238, v238, v183, v184\n" \
    "s_waitcnt vmcnt(4)\n" \
    "v_max3_u32 v238, v238, v185, v186\n" \
    "v_max3_u32 v238, v238, v187, v188\n" \
    "s_waitcnt vmcnt(3)\n" \
    "v_max3_u32 v238, v238, v189, v190\n" \
    "v_max3_u32 v238, v238, v191, v192\n" \
    "s_waitcnt vmcnt(2)\n" \
    "v_max3_u32 v238, v238, v193, v194\n" \
    "v_max3_u32 v238, v238, v195, v196\n" \
    "s_waitcnt vmcnt(1)\n" \
    "v_max3_u32 v238, v238, v197, v198\n" \
    "v_max3_u32 v238, v238, v199, v200\n" \
    "s_waitcnt vmcnt(0)\n" \
    "v_max3_u32 v238, v238, v201, v202\n" \
    "v_max_u32 v238, v238, v203\n" \
    "v_cmp_eq_u32 vcc, -1, v238\n" \
    "s_nop 0\n" \
    "s_cbranch_vccnz .Ldn_cFc_%=\n" \
    "s_waitcnt vmcnt(0) lgkmcnt(0)\n" \
    "s_branch .Ldn_rFc_%=\n" \
    ".Ldn_cFd_%=:\n" \
    "s_add_u32 s37, s37, 1\n" \
    "s_cmp_lt_u32 s37, s38\n" \
    "s_cselect_b32 s38, s38, 0\n" \
    "s_cbranch_scc0 .Ldn_xFd_%=\n" \
    "s_sleep 1\n" \
    "s_mul_i32 s39, s32, 98304\n" \
    "s_add_u32 s39, s39, s17\n" \
    "buffer_load_dwordx4 v[152:155], v144, s[8:11], s39 offen offset:0 sc1\n" \
    "buffer_load_dwordx4 v[156:159], v144, s[8:11], s39 offen offset:1024 sc1\n" \
    "buffer_load_dwordx4 v[160:163], v144, s[8:11], s39 offen offset:2048 sc1\n" \
    "buffer_load_dwordx4 v[164:167], v144, s[8:11], s39 offen offset:3072 sc1\n" \
    "s_add_u32 s39, s39, 4096\n" \
    "buffer_load_dwordx4 v[168:171], v144, s[8:11], s39 offen offset:0 sc1\n" \
    "buffer_load_dwordx4 v[172:175], v144, s[8:11], s39 offen offset:1024 sc1\n" \
    "s_mul_i32 s39, s32, 40960\n" \
    "s_add_u32 s39, s39, %[x1]+0\n" \
    "buffer_load_dwordx2 v[176:177], v147, s[8:11], s39 offen sc1\n" \
    "s_waitcnt vmcnt(6)\n" \
    "v_max3_u32 v238, v152, v153, v154\n" \
    "s_waitcnt vmcnt(5)\n" \
    "v_max3_u32 v238, v238, v155, v156\n" \
    "v_max3_u32 v238, v238, v157, v158\n" \
    "s_waitcnt vmcnt(4)\n" \
    "v_max3_u32 v238, v238, v159, v160\n" \
    "v_max3_u32 v238, v238, v161, v162\n" \
    "s_waitcnt vmcnt(3)\n" \
    "v_max3_u32 v238, v238, v163, v164\n" \
    "v_max3_u32 v238, v238, v165, v166\n" \
    "s_waitcnt vmcnt(2)\n" \
    "v_max3_u32 v238, v238, v167, v168\n" \
    "v_max3_u32 v238, v238, v169, v170\n" \
    "s_waitcnt vmcnt(1)\n" \
    "v_max3_u32 v238, v238, v171, v172\n" \
    "v_max3_u32 v238, v238, v173, v174\n" \
    "s_waitcnt vmcnt(0)\n" \
    "v_max3_u32 v238, v238, v175, v176\n" \
    "v_max_u32 v238, v238, v177\n" \
    "v_cmp_eq_u32 vcc, -1, v238\n" \
    "s_nop 0\n" \
    "s_cbranch_vccnz .Ldn_cFd_%=\n" \
    "s_waitcnt vmcnt(0) lgkmcnt(0)\n" \
    "s_branch .Ldn_rFd_%=\n" \
    ".Ldn_cFf_%=:\n" \
    "s_add_u32 s37, s37, 1\n" \
    "s_cmp_lt_u32 s37, s38\n" \
    "s_cselect_b32 s38, s38, 0\n" \
    "s_cbranch_scc0 .Ldn_xFf_%=\n" \
    "s_sleep 1\n" \
    "s_mul_i32 s39, s33, 98304\n" \
    "s_add_u32 s39, s39, s17\n" \
    "s_add_u32 s39, s39, 6291456\n" \
    "buffer_load_dwordx4 v[178:181], v144, s[8:11], s39 offen offset:0 sc1\n" \
    "buffer_load_dwordx4 v[182:185], v144, s[8:11], s39 offen offset:1024 sc1\n" \
    "buffer_load_dwordx4 v[186:189], v144, s[8:11], s39 offen offset:2048 sc1\n" \
    "buffer_load_dwordx4 v[190:193], v144, s[8:11], s39 offen offset:3072 sc1\n" \
    "s_add_u32 s39, s39, 4096\n" \
    "buffer_load_dwordx4 v[194:197], v144, s[8:11], s39 offen offset:0 sc1\n" \
    "buffer_load_dwordx4 v[198:201], v144, s[8:11], s39 offen offset:1024 sc1\n" \
    "s_mul_i32 s39, s33, 40960\n" \
    "s_add_u32 s39, s39, %[x1]+2621440\n" \
    "buffer_load_dwordx2 v[202:203], v147, s[8:11], s39 offen sc1\n" \
    "s_waitcnt vmcnt(6)\n" \
    "v_max3_u32 v238, v178, v179, v180\n" \
    "s_waitcnt vmcnt(5)\n" \
    "v_max3_u32 v238, v238, v181, v182\n" \
    "v_max3_u32 v238, v238, v183, v184\n" \
    "s_waitcnt vmcnt(4)\n" \
    "v_max3_u32 v238, v238, v185, v186\n" \
    "v_max3_u32 v238, v238, v187, v188\n" \
    "s_waitcnt vmcnt(3)\n" \
    "v_max3_u32 v238, v238, v189, v190\n" \
    "v_max3_u32 v238, v238, v191, v192\n" \
    "s_waitcnt vmcnt(2)\n" \
    "v_max3_u32 v238, v238, v193, v194\n" \
    "v_max3_u32 v238, v238, v195, v196\n" \
    "s_waitcnt vmcnt(1)\n" \
    "v_max3_u32 v238, v238, v197, v198\n" \
    "v_max3_u32 v238, v238, v199, v200\n" \
    "s_waitcnt vmcnt(0)\n" \
    "v_max3_u32 v238, v238, v201, v202\n" \
    "v_max_u32 v238, v238, v203\n" \
    "v_cmp_eq_u32 vcc, -1, v238\n" \
    "s_nop 0\n" \
    "s_cbranch_vccnz .Ldn_cFf_%=\n" \
    "s_waitcnt vmcnt(0) lgkmcnt(0)\n" \
    "s_branch .Ldn_rFf_%=\n" \
    ".Ldn_cNa_%=:\n" \
    "s_add_u32 s37, s37, 1\n" \
    "s_cmp_lt_u32 s37, s38\n" \
    "s_cselect_b32 s38, s38, 0\n" \
    "s_cbranch_scc0 .Ldn_xNa_%=\n" \
    "s_sleep 1\n" \
    "s_mul_i32 s39, s32, 98304\n" \
    "s_add_u32 s39, s39, s17\n" \
    "buffer_load_dwordx4 v[152:155], v144, s[8:11], s39 offen offset:0 sc1\n" \
    "buffer_load_dwordx4 v[156:159], v144, s[8:11], s39 offen offset:1024 sc1\n" \
    "buffer_load_dwordx4 v[160:163], v144, s[8:11], s39 offen offset:2048 sc1\n" \
    "buffer_load_dwordx4 v[164:167], v144, s[8:11], s39 offen offset:3072 sc1\n" \
    "s_add_u32 s39, s39, 4096\n" \
    "buffer_load_dwordx4 v[168:171], v144, s[8:11], s39 offen offset:0 sc1\n" \
    "buffer_load_dwordx4 v[172:175], v144, s[8:11], s39 offen offset:1024 sc1\n" \
    "s_mul_i32 s39, s32, 40960\n" \
    "s_add_u32 s39, s39, %[x1]+0\n" \
    "buffer_load_dwordx2 v[176:177], v147, s[8:11], s39 offen sc1\n" \
    "s_waitcnt vmcnt(6)\n" \
    "v_max3_u32 v238, v152, v153, v154\n" \
    "s_waitcnt vmcnt(5)\n" \
    "v_max3_u32 v238, v238, v155, v156\n" \
    "v_max3_u32 v238, v238, v157, v158\n" \
    "s_waitcnt vmcnt(4)\n" \
    "v_max3_u32 v238, v238, v159, v160\n" \
    "v_max3_u32 v238, v238, v161, v162\n" \
    "s_waitcnt vmcnt(3)\n" \
    "v_max3_u32 v238, v238, v163, v164\n" \
    "v_max3_u32 v238, v238, v165, v166\n" \
    "s_waitcnt vmcnt(2)\n" \
    "v_max3_u32 v238, v238, v167, v168\n" \
    "v_max3_u32 v238, v238, v169, v170\n" \
    "s_waitcnt vmcnt(1)\n" \
    "v_max3_u32 v238, v238, v171, v172\n" \
    "v_max3_u32 v238, v238, v173, v174\n" \
    "s_waitcnt vmcnt(0)\n" \
    "v_max3_u32 v238, v238, v175, v176\n" \
    "v_max_u32 v238, v238, v177\n" \
    "v_cmp_eq_u32 vcc, -1, v238\n" \
    "s_nop 0\n" \
    "s_cbranch_vccnz .Ldn_cNa_%=\n" \
    "s_waitcnt vmcnt(0) lgkmcnt(0)\n" \
    "s_branch .Ldn_rNa_%=\n" \
    ".Ldn_cNb_%=:\n" \
    "s_add_u32 s37, s37, 1\n" \
    "s_cmp_lt_u32 s37, s38\n" \
    "s_cselect_b32 s38, s38, 0\n" \
    "s_cbranch_scc0 .Ldn_xNb_%=\n" \
    "s_sleep 1\n" \
    "s_mul_i32 s39, s33, 98304\n" \
    "s_add_u32 s39, s39, s17\n" \
    "s_add_u32 s39, s39, 6291456\n" \
    "buffer_load_dwordx4 v[178:181], v144, s[8:11], s39 offen offset:0 sc1\n" \
    "buffer_load_dwordx4 v[182:185], v144, s[8:11], s39 offen offset:1024 sc1\n" \
    "buffer_load_dwordx4 v[186:189], v144, s[8:11], s39 offen offset:2048 sc1\n" \
    "buffer_load_dwordx4 v[190:193], v144, s[8:11], s39 offen offset:3072 sc1\n" \
    "s_add_u32 s39, s39, 4096\n" \
    "buffer_load_dwordx4 v[194:197], v144, s[8:11], s39 offen offset:0 sc1\n" \
    "buffer_load_dwordx4 v[198:201], v144, s[8:11], s39 offen offset:1024 sc1\n" \
    "s_mul_i32 s39, s33, 40960\n" \
    "s_add_u32 s39, s39, %[x1]+2621440\n" \
    "buffer_load_dwordx2 v[202:203], v147, s[8:11], s39 offen sc1\n" \
    "s_waitcnt vmcnt(6)\n" \
    "v_max3_u32 v238, v178, v179, v180\n" \
    "s_waitcnt vmcnt(5)\n" \
    "v_max3_u32 v238, v238, v181, v182\n" \
    "v_max3_u32 v238, v238, v183, v184\n" \
    "s_waitcnt vmcnt(4)\n" \
    "v_max3_u32 v238, v238, v185, v186\n" \
    "v_max3_u32 v238, v238, v187, v188\n" \
    "s_waitcnt vmcnt(3)\n" \
    "v_max3_u32 v238, v238, v189, v190\n" \
    "v_max3_u32 v238, v238, v191, v192\n" \
    "s_waitcnt vmcnt(2)\n" \
    "v_max3_u32 v238, v238, v193, v194\n" \
    "v_max3_u32 v238, v238, v195, v196\n" \
    "s_waitcnt vmcnt(1)\n" \
    "v_max3_u32 v238, v238, v197, v198\n" \
    "v_max3_u32 v238, v238, v199, v200\n" \
    "s_waitcnt vmcnt(0)\n" \
    "v_max3_u32 v238, v238, v201, v202\n" \
    "v_max_u32 v238, v238, v203\n" \
    "v_cmp_eq_u32 vcc, -1, v238\n" \
    "s_nop 0\n" \
    "s_cbranch_vccnz .Ldn_cNb_%=\n" \
    "s_waitcnt vmcnt(0) lgkmcnt(0)\n" \
    "s_branch .Ldn_rNb_%=\n" \
    ".Ldn_cNc_%=:\n" \
    "s_add_u32 s37, s37, 1\n" \
    "s_cmp_lt_u32 s37, s38\n" \
    "s_cselect_b32 s38, s38, 0\n" \
    "s_cbranch_scc0 .Ldn_xNc_%=\n" \
    "s_sleep 1\n" \
    "s_mul_i32 s39, s33, 98304\n" \
    "s_add_u32 s39, s39, s17\n" \
    "s_add_u32 s39, s39, 6291456\n" \
    "buffer_load_dwordx4 v[178:181], v144, s[8:11], s39 offen offset:0 sc1\n" \
    "buffer_load_dwordx4 v[182:185], v144, s[8:11], s39 offen offset:1024 sc1\n" \
    "buffer_load_dwordx4 v[186:189], v144, s[8:11], s39 offen offset:2048 sc1\n" \
    "buffer_load_dwordx4 v[190:193], v144, s[8:11], s39 offen offset:3072 sc1\n" \
    "s_add_u32 s39, s39, 4096\n" \
    "buffer_load_dwordx4 v[194:197], v144, s[8:11], s39 offen offset:0 sc1\n" \
    "buffer_load_dwordx4 v[198:201], v144, s[8:11], s39 offen offset:1024 sc1\n" \
    "s_mul_i32 s39, s33, 40960\n" \
    "s_add_u32 s39, s39, %[x1]+2621440\n" \
    "buffer_load_dwordx2 v[202:203], v147, s[8:11], s39 offen sc1\n" \
    "s_waitcnt vmcnt(6)\n" \
    "v_max3_u32 v238, v178, v179, v180\n" \
    "s_waitcnt vmcnt(5)\n" \
    "v_max3_u32 v238, v238, v181, v182\n" \
    "v_max3_u32 v238, v238, v183, v184\n" \
    "s_waitcnt vmcnt(4)\n" \
    "v_max3_u32 v238, v238, v185, v186\n" \
    "v_max3_u32 v238, v238, v187, v188\n" \
    "s_waitcnt vmcnt(3)\n" \
    "v_max3_u32 v238, v238, v189, v190\n" \
    "v_max3_u32 v238, v238, v191, v192\n" \
    "s_waitcnt vmcnt(2)\n" \
    "v_max3_u32 v238, v238, v193, v194\n" \
    "v_max3_u32 v238, v238, v195, v196\n" \
    "s_waitcnt vmcnt(1)\n" \
    "v_max3_u32 v238, v238, v197, v198\n" \
    "v_max3_u32 v238, v238, v199, v200\n" \
    "s_waitcnt vmcnt(0)\n" \
    "v_max3_u32 v238, v238, v201, v202\n" \
    "v_max_u32 v238, v238, v203\n" \
    "v_cmp_eq_u32 vcc, -1, v238\n" \
    "s_nop 0\n" \
    "s_cbranch_vccnz .Ldn_cNc_%=\n" \
    "s_waitcnt vmcnt(0) lgkmcnt(0)\n" \
    "s_branch .Ldn_rNc_%=\n" \
    ".Ldn_cNd_%=:\n" \
    "s_add_u32 s37, s37, 1\n" \
    "s_cmp_lt_u32 s37, s38\n" \
    "s_cselect_b32 s38, s38, 0\n" \
    "s_cbranch_scc0 .Ldn_xNd_%=\n" \
    "s_sleep 1\n" \
    "s_mul_i32 s39, s32, 98304\n" \
    "s_add_u32 s39, s39, s17\n" \
    "buffer_load_dwordx4 v[152:155], v144, s[8:11], s39 offen offset:0 sc1\n" \
    "buffer_load_dwordx4 v[156:159], v144, s[8:11], s39 offen offset:1024 sc1\n" \
    "buffer_load_dwordx4 v[160:163], v144, s[8:11], s39 offen offset:2048 sc1\n" \
    "buffer_load_dwordx4 v[164:167], v144, s[8:11], s39 offen offset:3072 sc1\n" \
    "s_add_u32 s39, s39, 4096\n" \
    "buffer_load_dwordx4 v[168:171], v144, s[8:11], s39 offen offset:0 sc1\n" \
    "buffer_load_dwordx4 v[172:175], v144, s[8:11], s39 offen offset:1024 sc1\n" \
    "s_mul_i32 s39, s32, 40960\n" \
    "s_add_u32 s39, s39, %[x1]+0\n" \
    "buffer_load_dwordx2 v[176:177], v147, s[8:11], s39 offen sc1\n" \
    "s_waitcnt vmcnt(6)\n" \
    "v_max3_u32 v238, v152, v153, v154\n" \
    "s_waitcnt vmcnt(5)\n" \
    "v_max3_u32 v238, v238, v155, v156\n" \
    "v_max3_u32 v238, v238, v157, v158\n" \
    "s_waitcnt vmcnt(4)\n" \
    "v_max3_u32 v238, v238, v159, v160\n" \
    "v_max3_u32 v238, v238, v161, v162\n" \
    "s_waitcnt vmcnt(3)\n" \
    "v_max3_u32 v238, v238, v163, v164\n" \
    "v_max3_u32 v238, v238, v165, v166\n" \
    "s_waitcnt vmcnt(2)\n" \
    "v_max3_u32 v238, v238, v167, v168\n" \
    "v_max3_u32 v238, v238, v169, v170\n" \
    "s_waitcnt vmcnt(1)\n" \
    "v_max3_u32 v238, v238, v171, v172\n" \
    "v_max3_u32 v238, v238, v173, v174\n" \
    "s_waitcnt vmcnt(0)\n" \
    "v_max3_u32 v238, v238, v175, v176\n" \
    "v_max_u32 v238, v238, v177\n" \
    "v_cmp_eq_u32 vcc, -1, v238\n" \
    "s_nop 0\n" \
    "s_cbranch_vccnz .Ldn_cNd_%=\n" \
    "s_waitcnt vmcnt(0) lgkmcnt(0)\n" \
    "s_branch .Ldn_rNd_%=\n" \
    ".Ldn_cNf_%=:\n" \
    "s_add_u32 s37, s37, 1\n" \
    "s_cmp_lt_u32 s37, s38\n" \
    "s_cselect_b32 s38, s38, 0\n" \
    "s_cbranch_scc0 .Ldn_xNf_%=\n" \
    "s_sleep 1\n" \
    "s_mul_i32 s39, s33, 98304\n" \
    "s_add_u32 s39, s39, s17\n" \
    "s_add_u32 s39, s39, 6291456\n" \
    "buffer_load_dwordx4 v[178:181], v144, s[8:11], s39 offen offset:0 sc1\n" \
    "buffer_load_dwordx4 v[182:185], v144, s[8:11], s39 offen offset:1024 sc1\n" \
    "buffer_load_dwordx4 v[186:189], v144, s[8:11], s39 offen offset:2048 sc1\n" \
    "buffer_load_dwordx4 v[190:193], v144, s[8:11], s39 offen offset:3072 sc1\n" \
    "s_add_u32 s39, s39, 4096\n" \
    "buffer_load_dwordx4 v[194:197], v144, s[8:11], s39 offen offset:0 sc1\n" \
    "buffer_load_dwordx4 v[198:201], v144, s[8:11], s39 offen offset:1024 sc1\n" \
    "s_mul_i32 s39, s33, 40960\n" \
    "s_add_u32 s39, s39, %[x1]+2621440\n" \
    "buffer_load_dwordx2 v[202:203], v147, s[8:11], s39 offen sc1\n" \
    "s_waitcnt vmcnt(6)\n" \
    "v_max3_u32 v238, v178, v179, v180\n" \
    "s_waitcnt vmcnt(5)\n" \
    "v_max3_u32 v238, v238, v181, v182\n" \
    "v_max3_u32 v238, v238, v183, v184\n" \
    "s_waitcnt vmcnt(4)\n" \
    "v_max3_u32 v238, v238, v185, v186\n" \
    "v_max3_u32 v238, v238, v187, v188\n" \
    "s_waitcnt vmcnt(3)\n" \
    "v_max3_u32 v238, v238, v189, v190\n" \
    "v_max3_u32 v238, v238, v191, v192\n" \
    "s_waitcnt vmcnt(2)\n" \
    "v_max3_u32 v238, v238, v193, v194\n" \
    "v_max3_u32 v238, v238, v195, v196\n" \
    "s_waitcnt vmcnt(1)\n" \
    "v_max3_u32 v238, v238, v197, v198\n" \
    "v_max3_u32 v238, v238, v199, v200\n" \
    "s_waitcnt vmcnt(0)\n" \
    "v_max3_u32 v238, v238, v201, v202\n" \
    "v_max_u32 v238, v238, v203\n" \
    "v_cmp_eq_u32 vcc, -1, v238\n" \
    "s_nop 0\n" \
    "s_cbranch_vccnz .Ldn_cNf_%=\n" \
    "s_waitcnt vmcnt(0) lgkmcnt(0)\n" \
    "s_branch .Ldn_rNf_%=\n" \
    ""
#define LN_ASM \
    "s_mov_b32 s32, 0\n" \
    "s_mov_b32 s36, 0x40000\n" \
    "s_mov_b32 s44, 0xaaaaaaaa\n" \
    "s_mov_b32 s45, 0xaaaaaaaa\n" \
    "s_mov_b32 s46, 0xcccccccc\n" \
    "s_mov_b32 s47, 0xcccccccc\n" \
    "v_mov_b32 v12, 0x3f803f80\n" \
    "v_mov_b32 v64, 1\n" \
    "v_mov_b32 v13, 0x3f803f80\n" \
    "v_mov_b32 v65, 1\n" \
    "v_mov_b32 v14, 0x3f803f80\n" \
    "v_mov_b32 v66, 1\n" \
    "v_mov_b32 v15, 0x3f803f80\n" \
    "v_mov_b32 v67, 1\n" \
    "s_add_u32 s48, s32, 2\n" \
    "s_min_u32 s48, s48, 64\n" \
    "s_and_b32 s49, s48, 1\n" \
    "s_xor_b32 s49, s49, 1\n" \
    "s_lshl_b32 s49, s49, 9\n" \
    "s_add_u32 s49, s49, s20\n" \
    "buffer_load_dwordx2 v[8:9], off, s[8:11], s49 sc1\n" \
    ".Lln_l_%=:\n" \
    "s_mov_b32 s35, 0\n" \
    "s_waitcnt vmcnt(0)\n" \
    ".Lln_kp_%=:\n" \
    "v_readfirstlane_b32 s34, v9\n" \
    "s_nop 0\n" \
    "s_cmp_gt_i32 s34, s48\n" \
    "s_cbranch_scc1 .Lln_kx_%=\n" \
    "s_cmp_eq_u32 s34, s48\n" \
    "s_cbranch_scc1 .Lln_kw_%=\n" \
    "s_sleep 3\n" \
    "buffer_load_dwordx2 v[8:9], off, s[8:11], s49 sc1\n" \
    "s_waitcnt vmcnt(0)\n" \
    "s_add_u32 s35, s35, 1\n" \
    "s_cmp_lt_u32 s35, s36\n" \
    "s_cbranch_scc1 .Lln_kp_%=\n" \
    "s_mov_b32 s36, 0\n" \
    "s_branch .Lln_kx_%=\n" \
    ".Lln_kw_%=:\n" \
    "v_readfirstlane_b32 s34, v8\n" \
    "s_cmp_eq_u32 s32, 63\n" \
    "s_cselect_b32 s33, %[dlast], 0\n" \
    "s_add_u32 s34, s34, s33\n" \
    "s_add_u32 s34, s34, %[dxl]\n" \
    "s_branch .Lln_ktb_%=\n" \
    ".Lln_kta_%=:\n" \
    "s_sleep 1\n" \
    ".Lln_ktb_%=:\n" \
    "s_memrealtime vcc\n" \
    "s_waitcnt lgkmcnt(0)\n" \
    "s_sub_i32 vcc_lo, s34, vcc_lo\n" \
    "s_cmp_gt_i32 vcc_lo, 8\n" \
    "s_cbranch_scc1 .Lln_kta_%=\n" \
    "s_cmp_gt_i32 vcc_lo, 0\n" \
    "s_cbranch_scc1 .Lln_ktb_%=\n" \
    ".Lln_kx_%=:\n" \
    "s_mul_i32 s37, s32, 40960\n" \
    "s_add_u32 s37, s37, s17\n" \
    "buffer_load_dwordx4 v[16:19], v0, s[8:11], s37 offen sc1\n" \
    "s_add_u32 s37, s37, 8192\n" \
    "buffer_load_dwordx4 v[20:23], v0, s[8:11], s37 offen sc1\n" \
    "s_add_u32 s37, s37, 8192\n" \
    "buffer_load_dwordx4 v[24:27], v1, s[8:11], s37 offen sc1\n" \
    "s_mov_b32 s35, 0\n" \
    ".Lln_r_%=:\n" \
    "s_waitcnt vmcnt(2)\n" \
    "v_mfma_f32_16x16x32_bf16 v[32:35], v[16:19], v[16:19], 0\n" \
    "v_mfma_f32_16x16x32_bf16 v[28:31], v[12:15], v[16:19], 0\n" \
    "s_waitcnt vmcnt(1)\n" \
    "v_mfma_f32_16x16x32_bf16 v[32:35], v[20:23], v[20:23], v[32:35]\n" \
    "v_mfma_f32_16x16x32_bf16 v[28:31], v[12:15], v[20:23], v[28:31]\n" \
    "s_waitcnt vmcnt(0)\n" \
    "v_mfma_f32_16x16x32_bf16 v[32:35], v[24:27], v[24:27], v[32:35]\n" \
    "v_mfma_f32_16x16x32_bf16 v[28:31], v[12:15], v[24:27], v[28:31]\n" \
    "v_max3_u32 v10, v16, v17, v18\n" \
    "v_max3_u32 v10, v10, v19, v20\n" \
    "v_max3_u32 v10, v10, v21, v22\n" \
    "v_max3_u32 v10, v10, v23, v24\n" \
    "v_max3_u32 v10, v10, v25, v26\n" \
    "v_max_u32 v10, v10, v27\n" \
    "v_cmp_eq_u32 vcc, -1, v10\n" \
    "s_nop 0\n" \
    "s_cbranch_vccnz .Lln_c_%=\n" \
    ".Lln_x_%=:\n" \
    "v_cndmask_b32 v10, v32, v33, s[44:45]\n" \
    "v_cndmask_b32 v11, v34, v35, s[44:45]\n" \
    "v_cndmask_b32 v29, v10, v11, s[46:47]\n" \
    "ds_write_b64 v2, v[28:29]\n" \
    "v_lshlrev_b32 v40, 16, v16\n" \
    "v_and_b32 v41, 0xffff0000, v16\n" \
    "v_lshlrev_b32 v42, 16, v17\n" \
    "v_and_b32 v43, 0xffff0000, v17\n" \
    "v_lshlrev_b32 v44, 16, v18\n" \
    "v_and_b32 v45, 0xffff0000, v18\n" \
    "v_lshlrev_b32 v46, 16, v19\n" \
    "v_and_b32 v47, 0xffff0000, v19\n" \
    "v_lshlrev_b32 v48, 16, v20\n" \
    "v_and_b32 v49, 0xffff0000, v20\n" \
    "v_lshlrev_b32 v50, 16, v21\n" \
    "v_and_b32 v51, 0xffff0000, v21\n" \
    "v_lshlrev_b32 v52, 16, v22\n" \
    "v_and_b32 v53, 0xffff0000, v22\n" \
    "v_lshlrev_b32 v54, 16, v23\n" \
    "v_and_b32 v55, 0xffff0000, v23\n" \
    "v_lshlrev_b32 v56, 16, v24\n" \
    "v_and_b32 v57, 0xffff0000, v24\n" \
    "v_lshlrev_b32 v58, 16, v25\n" \
    "v_and_b32 v59, 0xffff0000, v25\n" \
    "v_lshlrev_b32 v60, 16, v26\n" \
    "v_and_b32 v61, 0xffff0000, v26\n" \
    "v_lshlrev_b32 v62, 16, v27\n" \
    "v_and_b32 v63, 0xffff0000, v27\n" \
    "s_mul_i32 s37, s32, 40960\n" \
    "s_add_u32 s37, s37, s18\n" \
    "s_waitcnt lgkmcnt(0)\n" \
    "s_barrier\n" \
    "ds_read2_b64 v[36:39], v3 offset1:16\n" \
    "v_xor_b32 v2, 1024, v2\n" \
    "v_xor_b32 v3, 1024, v3\n" \
    "s_waitcnt lgkmcnt(0)\n" \
    "v_add_f32 v36, v36, v38\n" \
    "v_add_f32 v37, v37, v39\n" \
    "s_nop 1\n" \
    "v_permlane16_swap_b32 v36, v37\n" \
    "v_add_f32 v38, v36, v37\n" \
    "v_add_f32 v36, v36, v37\n" \
    "s_nop 1\n" \
    "v_permlane32_swap_b32 v36, v38\n" \
    "v_add_f32 v37, v36, v38\n" \
    "v_add_f32 v36, v36, v38\n" \
    "s_nop 1\n" \
    "v_permlane16_swap_b32 v36, v37\n" \
    "v_mul_f32 v36, 0xbacccccd, v36\n" \
    "v_mul_f32 v37, 0x3acccccd, v37\n" \
    "v_fma_f32 v37, -v36, v36, v37\n" \
    "v_max_f32 v37, 0, v37\n" \
    "v_add_f32 v37, 0x3727c5ac, v37\n" \
    "v_rsq_f32 v38, v37\n" \
    "s_nop 0\n" \
    "v_mul_f32 v36, v36, v38\n" \
    "v_fma_f32 v40, v40, v38, v36\n" \
    "v_fma_f32 v41, v41, v38, v36\n" \
    "v_cvt_pk_bf16_f32 v16, v40, v41\n" \
    "v_fma_f32 v42, v42, v38, v36\n" \
    "v_fma_f32 v43, v43, v38, v36\n" \
    "v_cvt_pk_bf16_f32 v17, v42, v43\n" \
    "v_fma_f32 v44, v44, v38, v36\n" \
    "v_fma_f32 v45, v45, v38, v36\n" \
    "v_cvt_pk_bf16_f32 v18, v44, v45\n" \
    "v_fma_f32 v46, v46, v38, v36\n" \
    "v_fma_f32 v47, v47, v38, v36\n" \
    "v_cvt_pk_bf16_f32 v19, v46, v47\n" \
    "buffer_store_dwordx4 v[16:19], v0, s[8:11], s37 offen sc1\n" \
    "s_add_u32 s37, s37, 8192\n" \
    "v_fma_f32 v48, v48, v38, v36\n" \
    "v_fma_f32 v49, v49, v38, v36\n" \
    "v_cvt_pk_bf16_f32 v20, v48, v49\n" \
    "v_fma_f32 v50, v50, v38, v36\n" \
    "v_fma_f32 v51, v51, v38, v36\n" \
    "v_cvt_pk_bf16_f32 v21, v50, v51\n" \
    "v_fma_f32 v52, v52, v38, v36\n" \
    "v_fma_f32 v53, v53, v38, v36\n" \
    "v_cvt_pk_bf16_f32 v22, v52, v53\n" \
    "v_fma_f32 v54, v54, v38, v36\n" \
    "v_fma_f32 v55, v55, v38, v36\n" \
    "v_cvt_pk_bf16_f32 v23, v54, v55\n" \
    "buffer_store_dwordx4 v[20:23], v0, s[8:11], s37 offen sc1\n" \
    "s_add_u32 s37, s37, 8192\n" \
    "v_fma_f32 v56, v56, v38, v36\n" \
    "v_fma_f32 v57, v57, v38, v36\n" \
    "v_cvt_pk_bf16_f32 v24, v56, v57\n" \
    "v_fma_f32 v58, v58, v38, v36\n" \
    "v_fma_f32 v59, v59, v38, v36\n" \
    "v_cvt_pk_bf16_f32 v25, v58, v59\n" \
    "v_fma_f32 v60, v60, v38, v36\n" \
    "v_fma_f32 v61, v61, v38, v36\n" \
    "v_cvt_pk_bf16_f32 v26, v60, v61\n" \
    "v_fma_f32 v62, v62, v38, v36\n" \
    "v_fma_f32 v63, v63, v38, v36\n" \
    "v_cvt_pk_bf16_f32 v27, v62, v63\n" \
    "buffer_store_dwordx4 v[24:27], v1, s[8:11], s37 offen sc1\n" \
    "s_lshl_b32 s33, s32, 10\n" \
    "s_add_u32 s33, s33, s19\n" \
    "s_waitcnt vmcnt(0)\n" \
    "buffer_store_dwordx4 v[64:67], v4, s[8:11], s33 offen sc1\n" \
    "s_add_u32 s48, s32, 3\n" \
    "s_min_u32 s48, s48, 64\n" \
    "s_and_b32 s49, s48, 1\n" \
    "s_xor_b32 s49, s49, 1\n" \
    "s_lshl_b32 s49, s49, 9\n" \
    "s_add_u32 s49, s49, s20\n" \
    "buffer_load_dwordx2 v[8:9], off, s[8:11], s49 sc1\n" \
    "s_add_u32 s32, s32, 1\n" \
    "s_cmp_lt_u32 s32, 64\n" \
    "s_cbranch_scc1 .Lln_l_%=\n" \
    "s_waitcnt vmcnt(0)\n" \
    "s_endpgm\n" \
    ".Lln_c_%=:\n" \
    "s_add_u32 s35, s35, 1\n" \
    "s_cmp_lt_u32 s35, s36\n" \
    "s_cselect_b32 s36, s36, 0\n" \
    "s_cbranch_scc0 .Lln_x_%=\n" \
    "s_sleep 2\n" \
    "s_mul_i32 s37, s32, 40960\n" \
    "s_add_u32 s37, s37, s17\n" \
    "buffer_load_dwordx4 v[16:19], v0, s[8:11], s37 offen sc1\n" \
    "s_add_u32 s37, s37, 8192\n" \
    "buffer_load_dwordx4 v[20:23], v0, s[8:11], s37 offen sc1\n" \
    "s_add_u32 s37, s37, 8192\n" \
    "buffer_load_dwordx4 v[24:27], v1, s[8:11], s37 offen sc1\n" \
    "s_waitcnt vmcnt(0)\n" \
    "s_branch .Lln_r_%=\n" \
    ""
#define QK_WL 65536
template <int WL, int SL2>
__device__ __forceinline__ void qk_weights(const u16* wa, const u16* wb, char* smem, const int w, const int l, bf16x16 (&wt)[3][8]) {
    const rsrc_t rq_ = __builtin_amdgcn_make_buffer_rsrc((void*)(w < 4 ? wa : wb), 0, 639 * 12288 + 384, 0x00020000);
#define QK_SLOT(r_) ((r_) % 3 == 0 ? SL2 : SL2 ? ((r_) % 3 - 1) * 49152 : ((r_) % 3) * 49152)
#define QK_ISSUE(r_) { if ((r_) < 10) { _Pragma("unroll") for (int i_ = 0; i_ < 6; ++i_) { \
        const unsigned ix_ = (unsigned)(((w & 3) * 6 + i_) * 64 + l), rw_ = ix_ / 24, cc_ = (ix_ % 24) ^ ((rw_ & 3) << 1); \
        __builtin_amdgcn_raw_ptr_buffer_load_lds(rq_, (lds_ptr_t)(smem + QK_SLOT(r_) + (w * 6 + i_) * 1024), 16, (int)(rw_ * 12288 + cc_ * 16), (r_) * 64 * 12288, 0, AXQ); } } }
    const unsigned c01 = (unsigned)((w & 3) * 96 + (l & 3) * 8);
    bf16x8 hold[6];
    char* wl = smem + WL + w * 12288 + l * 16;
    QK_ISSUE(0) QK_ISSUE(1)
#pragma unroll
    for (int r = 0; r < 10; ++r) {
        if (r + 1 < 10) WAITVN(6) else WAITV0();
        RAWBAR()
        QK_ISSUE(r + 2)
        const char* sb = smem + QK_SLOT(r) + (w >> 2) * 24576;
        if (r == 9) {
#pragma unroll
            for (int i = 0; i < 6; ++i) *(bf16x8*)(wl + i * 1024) = hold[i];
        }
#pragma unroll
        for (int i = 0; i < 3; ++i) {
            const bf16x8 f0 = tr_frag<24, false>(sb, 0, c01 + i * 32, l), f1 = tr_frag<24, false>(sb, 32, c01 + i * 32, l);
            if (r < 8) wt[i][r & 7] = cat16(f0, f1);
            else if (r == 8) { hold[i] = f0; hold[3 + i] = f1; }
            else { *(bf16x8*)(wl + (6 + i) * 1024) = f0; *(bf16x8*)(wl + (9 + i) * 1024) = f1; }
        }
    }
    RAWBAR()
}
#define WLT(i) cat16(LDS(bf16x8, wl + (((i) / 3) * 6 + (i) % 3) * 1024), LDS(bf16x8, wl + (((i) / 3) * 6 + 3 + (i) % 3) * 1024))
template <typename S, typename F, typename M>
__device__ __forceinline__ void projm(const bf16x16 (&wt)[3][8], const unsigned mf, const unsigned wl, int ix, f32x4& a0, f32x4& a1, f32x4& a2, S sync, F side, M mid) {
    bf16x8 A[8], AX[2];
    bf16x16 W[6];
    MEMB();
    W[0] = WLT(0); W[1] = WLT(1);
    a0 = Z4; a1 = Z4; a2 = Z4;
    asm volatile("" : "+v"(a0), "+v"(a1), "+v"(a2));
    SB0();
    sync();
    A[0] = LDS(bf16x8, mf); A[1] = LDS(bf16x8, mf + 1024); AX[0] = LDS(bf16x8, mf + 8192); A[2] = LDS(bf16x8, mf + 2048);
    SB0();
    side();
    SB0();
    bf16x8 Wh[6][2];
#define WH(i, h_) Wh[i][h_] = LDS(bf16x8, wl + (((i) / 3) * 6 + (h_) * 3 + (i) % 3) * 1024);
#define PMS(d, x, y) { d = SMFMA(x, y, d, ix); asm volatile("" : "+v"(d)); SB0(); }
    static_for<0, 8>([&](auto ic) __attribute__((always_inline)) {
        constexpr int i = decltype(ic)::v;
        constexpr int q = i == 2 || i == 3 ? i : i == 5 || i == 6 ? i - 1 : 0;
        mid(ic);
        if constexpr (i == 7) { PMS(a0, A[i], wt[0][i]) PMS(a1, AX[1], cat16(Wh[4][0], Wh[4][1])) PMS(a1, A[i], wt[1][i]) PMS(a2, A[i], wt[2][i]) PMS(a2, AX[1], cat16(Wh[5][0], Wh[5][1])) }
        else {
        PMS(a0, A[i], wt[0][i])
        if constexpr (i + 3 < 8) A[i + 3] = LDS(bf16x8, mf + (i + 3) * 1024);
        if (i >= 5 && q) WH(q, 0)
        SB0();
        PMS(a1, A[i], wt[1][i])
        if (i == 1) AX[1] = LDS(bf16x8, mf + 9216);
        if (q) WH(q, i >= 5)
        SB0();
        PMS(a2, A[i], wt[2][i])
        if (i < 5 && q) WH(q, 1)
        SB0();
        if (i == 1) PMS(a0, AX[0], W[0])
        if (i == 2) PMS(a1, AX[0], W[1])
        if (i == 4) PMS(a2, AX[0], cat16(Wh[2][0], Wh[2][1]))
        if (i == 5) PMS(a0, AX[1], cat16(Wh[3][0], Wh[3][1]))
        }
    });
}
#define NOF [&]() __attribute__((always_inline)) {}
#define NOM [&](auto) __attribute__((always_inline)) {}
#define BAREBAR() { asm volatile("s_barrier" ::: "memory"); SB0(); }
__device__ __forceinline__ void wait_fine(unsigned t) {
    asm volatile("s_branch 2f\n1:\n\ts_sleep 1\n2:\n\ts_memrealtime vcc\n\ts_waitcnt lgkmcnt(0)\n\ts_sub_i32 vcc_lo, %0, vcc_lo\n\ts_cmp_gt_i32 vcc_lo, 8\n\ts_cbranch_scc1 1b\n\t"
                 "s_cmp_gt_i32 vcc_lo, 0\n\ts_cbranch_scc1 2b" :: "s"(t) : "memory", "scc", "vcc");
}
__device__ __forceinline__ void dma6(u32x4 rd, unsigned dst, unsigned voff, unsigned soff) {
    unsigned d2, s2;
    asm volatile("s_mov_b32 m0, %2\n\ts_add_u32 %0, %2, 0x1000\n\tbuffer_load_dwordx4 %3, %4, %5 offen sc1 lds\n\tbuffer_load_dwordx4 %3, %4, %5 offen offset:1024 sc1 lds\n\t"
                 "buffer_load_dwordx4 %3, %4, %5 offen offset:2048 sc1 lds\n\tbuffer_load_dwordx4 %3, %4, %5 offen offset:3072 sc1 lds\n\t"
                 "s_mov_b32 m0, %0\n\ts_add_u32 %1, %5, 0x1000\n\tbuffer_load_dwordx4 %3, %4, %1 offen sc1 lds\n\tbuffer_load_dwordx4 %3, %4, %1 offen offset:1024 sc1 lds"
                 : "=&s"(d2), "=&s"(s2) : "s"(dst), "v"(voff), "s"(rd), "s"(soff) : "memory", "scc");
}
__device__ __forceinline__ void st64(rsrc_t rs, unsigned voff, unsigned soff, u32x2 v) { __builtin_amdgcn_raw_buffer_store_b64(v, rs, (int)voff, (int)soff, AUX_SC1); }

#define QM_MF 0
#define QM_QM 10240
#define QM_KM 13312
#define QM_WL 16384
#define QM_QK 114688
#define KWIN 5
#define AWIN 4
#define BQP 2
template <int D> __device__ __forceinline__ int ev_id(int e) {
    const int d = e - D, id = (d >> 1) + ((d & 1) ? 64 : D);
    return (unsigned)d > (unsigned)(127 - 2 * D) ? e : id;
}
__device__ __forceinline__ unsigned fold1(f32x4 a) { return pack2(a[0] + a[2], a[1] + a[3]); }
typedef __attribute__((ext_vector_type(32))) unsigned u32x32;
typedef __attribute__((ext_vector_type(16))) unsigned u32x16;
typedef __attribute__((ext_vector_type(8))) unsigned u32x8;
__device__ __forceinline__ u32x32 cat4(bf16x16 a, bf16x16 b, bf16x16 c, bf16x16 d) {
    const u32x8 A = __builtin_bit_cast(u32x8, a), B = __builtin_bit_cast(u32x8, b), C = __builtin_bit_cast(u32x8, c), D = __builtin_bit_cast(u32x8, d);
    const u32x16 x = __builtin_shufflevector(A, B, 0, 1, 2, 3, 4, 5, 6, 7, 8, 9, 10, 11, 12, 13, 14, 15), y = __builtin_shufflevector(C, D, 0, 1, 2, 3, 4, 5, 6, 7, 8, 9, 10, 11, 12, 13, 14, 15);
    return __builtin_shufflevector(x, y, 0, 1, 2, 3, 4, 5, 6, 7, 8, 9, 10, 11, 12, 13, 14, 15, 16, 17, 18, 19, 20, 21, 22, 23, 24, 25, 26, 27, 28, 29, 30, 31);
}
#define WIN(t) "{v[0:31]}"(cat4(t[0], t[1], t[2], t[3])), "{v[32:63]}"(cat4(t[4], t[5], t[6], t[7])), "{v[64:95]}"(cat4(t[8], t[9], t[10], t[11])), \
               "{v[96:127]}"(cat4(t[12], t[13], t[14], t[15])), "{v[128:159]}"(cat4(t[16], t[17], t[18], t[19])), "{v[160:191]}"(cat4(t[20], t[21], t[22], t[23]))
#define ACLOB "memory", "vcc", "scc", "v255", "s99"
__device__ __forceinline__ void qkm_role(const Params& p, char* smem, const int w, const int h, const u32x2 nc) {
    bf16x16 wt[3][8];
    qk_weights<QM_WL, QM_QK>(p.Wq + h * 192, p.Wk + h * 192, smem, w, lane_id(), wt);
    SU(3)
    const rsrc_t rs = __builtin_amdgcn_make_buffer_rsrc((void*)wsp(p), 0, 0x7fffffff, 0x00020000);
    const unsigned ns = start_wait(p, smem, w, nc), hm = hot_page(ns, 2), hp = hm ^ RFX;
    RAWRD()
    const unsigned lo = lane_id() * 16;
    const unsigned dr = QM_QM + (w >> 2) * 3072 + (lo >> 8) * 4;
    unsigned po1, po2 = 0xC0000000u;
    const unsigned g8 = (lo >> 5) & 8;
    if (w == 0) { po1 = ((lo >> 1) & 0x100) + (lo & 0x60) * 2 + ((lo & 0x80) >> 2) + (lo & 0x10) + g8 + (h & 3) * 512; if (!(lo & 0x280)) po2 = (lo & 0x70) + g8 + 2048 + (h & 3) * 128; }
    else if (w == 1) { po1 = (lo & 0x2f0) + g8 + (h & 2) * 512 + (h & 1) * 256; if (lo < 512) po2 = (lo & 0xf0) + g8 + 2048 + (h & 3) * 256; }
    else if (w == 2) { po1 = lo + 3072 + (h & 3) * 1024; if (lo < 512) po2 = (lo & 0xf0) + g8 + 7168 + (h & 3) * 256; }
    else if (w == 4) po1 = lo < 512 ? lo + (h & 3) * 512 : lo < 640 ? lo + 1536 + (h & 3) * 128 : 0xC0000000u;
    else if (w == 5) { po1 = (lo & 256) ? 0xC0000000u : lo + (h & 2) * 512 + (h & 1) * 256; if (lo < 256) po2 = lo + 2048 + (h & 3) * 256; }
    else if (w == 6) { po1 = lo + 3072 + (h & 3) * 1024; if (lo < 256) po2 = lo + 7168 + (h & 3) * 256; }
    else { po1 = lo < 128 ? lo : 0xC0000000u; if (lo < 256) po2 = HB_TU - HB_TS + h * 128 + lo; }
    const bool pm = !(w & 3);
    const unsigned pbs = pm ? hp + HB_PM + (h >> 3) * 16384 + ((h >> 2) & 1) * 2560 : hp + HB_PT + (h >> 2) * 16384;
    const int psh = pm ? 16 : 17;
    if (w >= 4 && w < 7) {
        const u32x4 ps = poison4();
#pragma unroll
        for (int i = 0; i < 4; ++i) { const unsigned b_ = pbs + ((unsigned)((i & 1) + (i >> 1) * 4) << psh); st128(rs, po1, b_, ps); st128(rs, po2, b_, ps); }
    }
    const u32x8 lc = {lo, 0x4444 + ((lo >> 5) & 1) * 0xAAAA, dr + (w & 3) * 768 + (lo & 0xf0), dr + (w & 3) * 768 + (lo & 0xf0) + 512,
                      w < 4 ? troff(lo) : QM_WL + w * 12288 + lo, po1, po2, lo ? 0xC0000000u : h * 4u};
    const int mk = (w >> 1) & 1, p0 = h * 12 + (w & 1) * 6, sw = (p0 & 7) == 6;
    const u32x16 sc = {UNI(w), UNI(pbs), UNI(psh), UNI(hm + HB_MEM + ((w - 3) >> 2) * 16384 + ((w - 3) & 3) * 2048), UNI(OFF_QK + mk * 786432u + blk(p0 + sw * 2)),
                       UNI(lb + QM_QK + (w & 3) * 6144 + sw * 2048), UNI(hot_page(ns, 0) + HB_FK + h * 256u + mk * 96), UNI(hp + HB_TS + h * 128),
                       UNI(OFF_QK + mk * 786432u + blk(p0 + (sw ? 0 : 4))), UNI(sw ? -2048 : 4096), UNI(ns ^ (mk ? KSX : QSX)), UNI(mk ? HSEG : QSEG), UNI((mk ? GH : QH) / 2), 0u, 0u, 0u};
    const bf16x16* t = &wt[0][0];
    asm volatile("s_nop 4\n" QKM_ASM :: WIN(t), "{v[192:199]}"(lc), "{s[16:31]}"(sc), "{s[8:11]}"(rd), [lag] "n"(LAG), [dq] "n"(DQ), [dq3] "n"(DQ3), [dend] "n"(DEND), [r0] "n"(R0N) : ACLOB);
}

__device__ __forceinline__ void g_role(const Params& p, char* smem, const int w, const int j, const int a0, const int a1, const int b0, const u32x2 nc, unsigned ns) {
    const int l = lane_id(), m = j / 24, c0 = (j - m * 24) * 256;
    const rsrc_t rw = __builtin_amdgcn_make_buffer_rsrc((void*)((m == 0 ? p.Wq : m == 1 ? p.Wk : p.Wv) + c0), 0, 639 * 12288 + 512, 0x00020000);
    bf16x8 wt[2][20];
#define G_ISS(r_, ax_) { _Pragma("unroll") for (int i_ = 0; i_ < 4; ++i_) { \
        const unsigned ix_ = (unsigned)((w * 4 + i_) * 64 + l), rw_ = ix_ >> 5, cc_ = (ix_ & 31) ^ ((rw_ & 3) << 1); \
        __builtin_amdgcn_raw_ptr_buffer_load_lds(rw, (lds_ptr_t)(smem + ((r_) & 3) * 32768 + (w * 4 + i_) * 1024), 16, (int)(rw_ * 12288 + cc_ * 16), (r_) * 64 * 12288, 0, ax_); } }
#define G_ISSUE(r_) { if ((r_) < 10) { if (m == 2) G_ISS(r_, AXV) else G_ISS(r_, AXG) } }
    G_ISSUE(0) G_ISSUE(1) G_ISSUE(2)
#pragma unroll
    for (int r = 0; r < 10; ++r) {
        if (r < 8) WAITVN(8) else if (r == 8) WAITVN(4) else WAITV0();
        RAWBAR()
        G_ISSUE(r + 3)
#pragma unroll
        for (int kk = 0; kk < 2; ++kk)
#pragma unroll
            for (int i = 0; i < 2; ++i) wt[i][r * 2 + kk] = tr_frag<32, false>(smem + (r & 3) * 32768, kk * 32, (unsigned)(w * 64 + i * 32 + (l & 3) * 8), l);
    }
    RAWBAR()
    if (w == 0) *(unsigned*)(smem + 81920 + (l & 1) * 4) = 0u;
    SU(3)
    if (b0) RAWBAR() else ns = start_wait(p, smem, w, nc);
    RAWRD()
    const unsigned lo = lane_id() * 16;
    const int pc = (j - m * 24) * 8 + w, h = pc / 6, i = pc - h * 6;
    const unsigned fb = m == 2 ? FL_V + h * 512u + i * 64 : hot_page(ns, 0) + HB_FK + h * 256u + m * 96 + i * 16, fs = 16384;
    const u32x4 lc = {lo, lo < (m == 2 ? 64 : 16) ? lo : 0xC0000000u, FL_N + min(lo, 368u) * 4, j == 30 ? lo : 0xC0000000u};
    const u32x16 sc = {UNI(w), UNI(OFF_QK + (m & 1) * 786432u + blk(h * 12 + i)), UNI(OFF_QK + (m & 1) * 786432u + blk(h * 12 + 6 + i)), UNI(ns ^ (m == 1 ? KSX : m ? QSX ^ 0x2000 : QSX)),
                      UNI(fb + (b0 ? 2 * fs : 0)), UNI(fb + fs), UNI(OFF_XN + (w < 4 ? w * 4096 : 12288 + w * 1024)), UNI(lb + (w < 4 ? w * 4096 : 12288 + w * 1024)),
                       UNI(a0), UNI(a1), UNI(a1 + 128), UNI(b0), UNI(263500000 + (j * 8 + w) * 4), UNI(b0 ? 1000 : HS0(m)), UNI(HS1(m)), 0u};
    bf16x16 t[20];
#pragma unroll
    for (int k = 0; k < 20; ++k) t[k] = cat16(wt[k / 10][(k % 10) * 2], wt[k / 10][(k % 10) * 2 + 1]);
    asm volatile("s_nop 4\n" G_ASM :: "{v[0:31]}"(cat4(t[0], t[1], t[2], t[3])), "{v[32:63]}"(cat4(t[4], t[5], t[6], t[7])), "{v[64:95]}"(cat4(t[8], t[9], t[10], t[11])),
                 "{v[96:127]}"(cat4(t[12], t[13], t[14], t[15])), "{v[128:159]}"(cat4(t[16], t[17], t[18], t[19])), "{v[160:163]}"(lc), "{s[16:31]}"(sc), "{s[8:11]}"(rd), [r0] "n"(R0N)
                 : "memory", "vcc", "scc", "m0", "v164", "v165", "v166", "v167", "v168", "v169", "v170", "v171", "v172", "v173", "v174", "v175", "v176", "v177", "v178", "v179", "v180", "v181", "v182", "v183", "v184", "v185", "v186", "v187", "v188", "v189", "v190", "v191", "v192", "v193", "v194", "v195", "v196", "v197", "v198", "v199", "v200", "v201", "v202", "v203", "v204", "v205", "v206", "v207", "v208", "v209", "v210", "v211", "v212", "v213", "v214", "v215", "s32", "s33", "s34", "s35", "s36", "s37", "s38", "s39", "s40", "s41", "s42", "s43", "s44", "s45", "s46", "s47", "s48", "s49", "s50", "s51", "s69", "s70", "s71", "s72", "s73", "s74", "s75", "s76", "s77", "s78", "s79", "s80", "s81", "s82", "s83", "s84", "s85", "s86", "s87", "s88", "s89", "s90", "s91");
    RAWBAR()
}

#define UA_VM 10240
#define UA_Z 13312
#define VO_WL 65536
__device__ __forceinline__ void ua_role(const Params& p, char* smem, const int w, const int h, const u32x2 nc) {
    const rsrc_t rs = __builtin_amdgcn_make_buffer_rsrc((void*)wsp(p), 0, 0x7fffffff, 0x00020000);
    const rsrc_t ro_ = __builtin_amdgcn_make_buffer_rsrc((void*)p.Wo, 0, 6144 * 640 * 2, 0x00020000);
    const rsrc_t rv_ = __builtin_amdgcn_make_buffer_rsrc((void*)p.Wv, 0, 640 * 6144 * 2, 0x00020000);
#define VO_ISSUE(r_) { \
        if ((r_) >= UVF && (r_) < UVF + 6) { _Pragma("unroll") for (int i_ = 0; i_ < 5; ++i_) { \
            const unsigned ix_ = (unsigned)((w * 5 + i_) * 64 + l), rw_ = ix_ / 80, cc_ = (ix_ % 80) ^ ((rw_ & 3) << 1); \
            __builtin_amdgcn_raw_ptr_buffer_load_lds(ro_, (lds_ptr_t)(smem + ((r_) % 3) * 49152 + (w * 5 + i_) * 1024), 16, (int)(rw_ * 1280 + cc_ * 16), (h * 192 + ((r_) - UVF) * 32) * 1280, 0, AXUO); } } \
        else if ((r_) < 11) { _Pragma("unroll") for (int i_ = 0; i_ < 6; ++i_) { \
            const unsigned ix_ = (unsigned)((w * 6 + i_) * 64 + l), rw_ = ix_ / 24, cc_ = (ix_ % 24) ^ ((rw_ & 3) << 1); \
            __builtin_amdgcn_raw_ptr_buffer_load_lds(rv_, (lds_ptr_t)(smem + ((r_) % 3) * 49152 + (w * 6 + i_) * 1024), 16, (int)(rw_ * 12288 + cc_ * 16), ((r_) < UVF ? (r_) : (r_) - 6) * 128 * 12288 + h * 384, 0, AXUV); } } }
#define VO_STAGE(BODY_O, BODY_V) { \
        VO_ISSUE(0) VO_ISSUE(1) \
        _Pragma("unroll") for (int rr = 0; rr < 11; ++rr) { \
            if (rr + 1 >= UVF && rr + 1 < UVF + 6) WAITVN(5) else if (rr + 1 < 11) WAITVN(6) else WAITV0(); \
            RAWBAR() \
            VO_ISSUE(rr + 2) \
            const char* sb = smem + (rr % 3) * 49152; \
            if (rr >= UVF && rr < UVF + 6) { const int r = rr - UVF; BODY_O } else { const int r = rr < UVF ? rr : rr - 6; BODY_V } \
        } \
        RAWBAR() }
    const int wq = w ^ 4;
    if (w < 4) {
        const int l = lane_id(), tid = w * 64 + l;
        const unsigned lo_ = l * 16;
        bf16x16 wt[3][8];
        const unsigned c01 = (unsigned)(w * 96 + (l & 3) * 8);
        VO_STAGE({ (void)r; (void)sb; }, {
            _Pragma("unroll") for (int kk = 0; kk < 2; ++kk)
                _Pragma("unroll") for (int i = 0; i < 3; ++i) {
                    const bf16x8 f0 = (tr_frag<24, false>(sb, kk * 64, c01 + i * 32, l));
                    const bf16x8 f1 = (tr_frag<24, false>(sb, kk * 64 + 32, c01 + i * 32, l));
                    if (r < 4) wt[i][(r * 2 + kk) & 7] = cat16(f0, f1);
                    else { *(bf16x8*)(smem + VO_WL + wq * 12288 + (kk * 6 + i) * 1024 + lo_) = f0; *(bf16x8*)(smem + VO_WL + wq * 12288 + (kk * 6 + 3 + i) * 1024 + lo_) = f1; }
                }
        })
        *(unsigned*)(smem + UA_Z + tid * 4) = 0u;
        SU(3)
        const unsigned hm = hot_page(start_wait(p, smem, w, nc), 2);
        RAWRD()
        RAWBAR()
        const unsigned lo = lane_id() * 16;
        const unsigned dr = UA_VM + (lo & 0x10) * 2 + (lo >> 8) * 8 + ((lo >> 3) & 4);
        const u32x8 lc = {lo, 0x4444 + ((lo >> 5) & 1) * 0xAAAA, dr + w * 768 + (lo & 0xc0), dr + w * 768 + (lo & 0xc0) + 512,
                          VO_WL + wq * 12288 + lo, lo + w * 2048, lo + 8192 + (w >> 1) * 1024, (lo & 0x1f0) * 8};
        const u32x8 sc = {UNI(w), UNI(hm + HB_MEM + w * 2048), UNI(hm + HB_MEM + 16384 + (w >> 1) * 1024), UNI((hm ^ RFX) + HB_TU + h * 256 + w * 64), 0u, 0u, 0u, 0u};
        const bf16x16* t = &wt[0][0];
        asm volatile("s_nop 4\n" UAV_ASM :: WIN(t), "{v[192:199]}"(lc), "{s[16:23]}"(sc), "{s[8:11]}"(rd), [lag] "n"(LAG), [dv] "n"(DV), [dend] "n"(DEND) : ACLOB);
    } else {
        const int l = lane_id();
        bf16x16 wo[8][3];
        bf16x8 hold[12], ev[8];
        VO_STAGE({
            _Pragma("unroll") for (int i = 0; i < 10; ++i) {
                const int jt = wq * 10 + i;
                const int pp = l & 3;
                const unsigned cb = (unsigned)((jt >> 1) * 32 + (pp & 1) * 16 + 4 * (2 * (jt & 1) + (pp >> 1))) * 2;
                const bf16x8 f = (tr_frag<80, false>(sb, 0, cb, l));
                if (i >= 8) hold[(i - 8) * 6 + r] = f;
                else if (r & 1) wo[i & 7][r >> 1] = cat16(ev[i & 7], f);
                else ev[i & 7] = f;
            }
        }, { (void)r; (void)sb; })
#pragma unroll
        for (int i = 0; i < 12; ++i) *(bf16x8*)(smem + VO_WL + wq * 12288 + i * 1024 + lane_id() * 16) = hold[i];
        RAWRD()
        const unsigned ns = start_wait(p, smem, w, nc), hu0 = hot_page(ns, 0) ^ RFX, hu1 = hot_page(ns, 1) ^ RFX, hm = hot_page(ns, 2) ^ RFX;
        const unsigned lo = lane_id() * 16;
        const unsigned ul = (unsigned)(wq * 10 + (lo >> 8)) * 16384u + h * 256 + (lo & 0xf0);
        {
            const u32x4 ps = poison4();
#pragma unroll
            for (int i = 0; i < 4; ++i) { const unsigned b_ = ((i & 2) ? hu1 : hu0) + (i & 1) * 655360u; st128(rs, ul, b_, ps); st128(rs, ul, b_ + 65536, ps); st128(rs, ul, b_ + 98304, ps); }
        }
        WAITV0();
        RAWBAR()
        const u32x8 lc = {lo, 0x4444 + ((lo >> 5) & 1) * 0xAAAA, VO_WL + wq * 12288 + lo, UA_VM + (lo & 0x200) + (lo & 0x80) * 2 + ((lo & 0x100) >> 1) + ((lo & 0x70) >> 1),
                          (unsigned)(wq * 10 + ((lo >> 8) & 1)) * 16384u + h * 256 + (lo & 0xf0) + (lo >> 9) * 8, ul, (lo < 128 && w == 4) || (lo < 512 && h == 0 && w == 5) ? lo : 0xC0000000u, lo || w != 5 ? 0xC0000000u : h * 4u};
        const bool ck = h == 0 && w == 5;
        const u32x8 sc = {UNI(w), UNI(hu0), UNI(hu1), UNI(ck ? FL_T : hm + HB_TS + 4096 + h * 128), UNI(ck ? 9 : 14), 0u, 0u, 0u};
        const bf16x16* t = &wo[0][0];
        asm volatile("s_nop 4\n" UAO_ASM :: WIN(t), "{v[192:199]}"(lc), "{s[16:23]}"(sc), "{s[8:11]}"(rd), [lag] "n"(LAG), [brn] "n"(BRN) : ACLOB);
    }
}

__device__ __forceinline__ void ob_role(const Params& p, char* smem, const int w, const int h, const u32x2 nc) {
    const int l = lane_id();
    const rsrc_t rs = __builtin_amdgcn_make_buffer_rsrc((void*)wsp(p), 0, 0x7fffffff, 0x00020000);
    const rsrc_t ro_ = __builtin_amdgcn_make_buffer_rsrc((void*)p.Wo, 0, 6144 * 640 * 2, 0x00020000);
    bf16x8 wo[5][6];
#define OB_ISSUE(r_) { if ((r_) < 6) { _Pragma("unroll") for (int i_ = 0; i_ < 5; ++i_) { \
            const unsigned ix_ = (unsigned)((w * 5 + i_) * 64 + l), rw_ = ix_ / 80, cc_ = (ix_ % 80) ^ ((rw_ & 3) << 1); \
            __builtin_amdgcn_raw_ptr_buffer_load_lds(ro_, (lds_ptr_t)(smem + ((r_) % 3) * 49152 + (w * 5 + i_) * 1024), 16, (int)(rw_ * 1280 + cc_ * 16), (h * 192 + (r_) * 32) * 1280, 0, AXO); } } }
    OB_ISSUE(0) OB_ISSUE(1)
#pragma unroll
    for (int r = 0; r < 6; ++r) {
        if (r + 1 < 6) WAITVN(5) else WAITV0();
        RAWBAR()
        OB_ISSUE(r + 2)
        const char* sb = smem + (r % 3) * 49152;
#pragma unroll
        for (int i = 0; i < 5; ++i) {
            const int jt = w * 5 + i, pp = l & 3;
            const unsigned cb = (unsigned)((jt >> 1) * 32 + (pp & 1) * 16 + 4 * (2 * (jt & 1) + (pp >> 1))) * 2;
            wo[i][r] = tr_frag<80, false>(sb, 0, cb, l);
        }
    }
    RAWBAR()
    if (w == 0) *(unsigned*)(smem + 24576 + (l & 3) * 4) = 0u;
    SU(3)
    const unsigned ns = start_wait(p, smem, w, nc);
    RAWRD()
    const unsigned lo = lane_id() * 16;
    const u32x4 lc = {lo, lo < 64 ? lo : 0xC0000000u, FL_V + h * 512u + min(lo >> 7, 2u) * 16384 + min(lo & 112, 80u) * 4, h == 7 ? lo : 0xC0000000u};
    const u32x8 sc = {UNI(w), UNI(OFF_BT + (unsigned)(w * 5) * 65536u + (h >> 3) * 16384 + (h & 7) * 1024), UNI((hot_page(ns, 0) ^ RFX) + HB_FB + (w >> 2) * 16384 + (w & 3) * 2048 + h * 64), UNI(w > 4 ? OFF_QK + blk(h * 12 + (w - 5) * 4) : 0x80000000u), UNI(lb + (w > 4 ? (w - 5) * 4096 : 32768)),
                      UNI(ns ^ (h < BNH ? 0 : 0x2000)), UNI(ns ^ QSX ^ 0x2000), UNI(263500000 + (576 + h * 8 + w) * 4)};
    bf16x16 t[15];
#pragma unroll
    for (int k = 0; k < 15; ++k) t[k] = cat16(wo[k / 3][(k % 3) * 2], wo[k / 3][(k % 3) * 2 + 1]);
    asm volatile("s_nop 4\n" OB_ASM :: "{v[0:31]}"(cat4(t[0], t[1], t[2], t[3])), "{v[32:63]}"(cat4(t[4], t[5], t[6], t[7])), "{v[64:95]}"(cat4(t[8], t[9], t[10], t[11])),
                 "{v[96:103]}"(t[12]), "{v[104:111]}"(t[13]), "{v[112:119]}"(t[14]), "{v[120:123]}"(lc), "{s[16:23]}"(sc), "{s[8:11]}"(rd), [brn] "n"(BRN), [r0] "n"(R0N), [hs] "n"(VSEG), [hn] "n"(VH / 2) : ACLOB);
}

__device__ __forceinline__ void r_role(const Params& p, char* smem, const int w, const int c, const int r, const unsigned ns) {
    const rsrc_t rs = __builtin_amdgcn_make_buffer_rsrc((void*)wsp(p), 0, 0x7fffffff, 0x00020000);
    const unsigned hu = hot_page(ns, c) ^ RFX, hm = hot_page(ns, 2), hp = hm ^ RFX;
    RAWRD()
    const unsigned lo = lane_id() * 16;
    const unsigned mb = hm + HB_MEM + (unsigned)(c * 8 + (r >> 4)) * 16384u + (r & 15) * 512;
    if (w == 0) {
        const int l = lo >> 4;
        const unsigned* src = (const unsigned*)(p.mem0 + (((l >> 1) & 6) + (l & 1)) * 640 + r * 32 + ((l >> 4) & 1) * 8 + (l & 2));
        st128(rs, lo < 512 ? lo : 0xC0000000u, mb, (u32x4){nop(src[0]), nop(src[8]), nop(src[2]), nop(src[10])});
    }
    SU(3)
    const unsigned pn = PL == 4 || (PL == 9 && r >= 14) ? 43 : PL == 7 ? 52 : PL == 9 ? 19 : 13, pl = ((PL == 4 ? r / 3 : PL == 7 ? r >> 2 : PL == 9 && r >= 14 ? r - 14 : r) * 2 + (w >> 2)) * pn + (lo >> 4);
    const unsigned fw = (lo & 64) ? (2 * r + 1) / 5 : (2 * r) / 5;
    const unsigned xl = (unsigned)(w & 1) * 20480u + r * 1024 + lo;
    const u32x16 lc = {lo, mb + (lo & 0x1f0) + (lo >> 9) * 8,
                       lo < 128 ? (hot_page(ns, 0) ^ RFX) + HB_FB + c * 32768u + (fw >> 2) * 16384 + (fw & 3) * 2048 + w * 256 + (lo & 48) * 4 : 0xC0000000u,
                       lo < 128 ? hp + HB_TS + (unsigned)c * 32768u + w * 512 + (lo & 64) * 64 + (lo & 48) * 8 : 0xC0000000u,
                       (lo & 0x20) ? 0xC0000000u : ((lo >> 1) & 0x1e0) + (lo & 0x10) + 2048,
                       w * 1024 + (lo & 0x80) * 4 + (lo >> 8) * 64 + (lo & 0x20) + ((lo & 0x40) >> 3) + ((lo & 0x10) >> 2),
                       (w >> 1) == 1 ? xl : 0xC0000000u, 8192 + (w & 1) * 2048 + (lo >> 8) * 512 + (lo & 0xf0), 8192 + w * 4096 + lo,
                       w == 5 ? (lo < 512 ? mb + lo : 0xC0000000u) : w >= 6 ? xl + OFF_X1 + (c * 64 + PDIST) * 40960u : 0xC0000000u,
                       0x4444 + ((lo >> 5) & 1) * 0xAAAA, lo || w != 1 ? 0xC0000000u : r * 4u, r == 5 ? lo : 0xC0000000u,
                       pl < 512 && lo < pn * 16 ? hp + HB_PT + (c * 32 + (pl >> 6)) * 16384 + (pl & 63) * 128 : 0xC0000000u, lo < 128 ? lo >> 2 : 0xC0000000u, lo ? 0xC0000000u : r * 4u};
    const int dru = DRU - DRL * ((0x102ac >> (4 * (r % 5))) & 15);
    const u32x16 sc = {UNI(w), UNI(hp + HB_PM + (unsigned)(c * 16 + (w >> 1)) * 16384u + (w & 1) * 2560), UNI(hu + (unsigned)(2 * r) * 16384u + w * 1024),
                       UNI(OFF_BT + (unsigned)(c * BRN) * 2621440u + r * 131072u + (w >> 1) * 16384 + (w & 1) * 4096), UNI(hp + HB_PT + (unsigned)(c * 32 + w) * 16384u),
                       UNI(c * 64 * 40960), UNI(dru), UNI(w == 5 ? 3 : 63), UNI(w == 5 ? 32768 : 40960), UNI(c * 64), UNI(r * 8 + w), UNI(ns ^ (w < BNH / 4 ? 0 : 0x2000)), UNI(hp + HB_TS + c * 32768 + w * 512), 0u, 0u, 0u};
    asm volatile("s_nop 4\n" R_ASM :: "{v[0:15]}"(lc), "{s[16:31]}"(sc), "{s[8:11]}"(rd), [brn] "n"(BRN), [dr] "n"(DR), [dh] "n"(DH), [x0] "n"(OFF_X0), [x1] "n"(OFF_X1), [pdist] "n"(PDIST) : ACLOB);
}

#undef RB
#define RB 5
__device__ __forceinline__ void up_role(const Params& p, char* smem, const int w, const int j, const u32x2 nc) {
    const int l = lane_id(), tid = w * 64 + l;
    const rsrc_t rs = __builtin_amdgcn_make_buffer_rsrc((void*)wsp(p), 0, 0x7fffffff, 0x00020000);
    const unsigned lo_ = l * 16;
    const int fg = j >> 1, mt = j & 1;
    bf16x8 wt[2][20];
    {
        auto src = [&](int rnd) { return [&p, fg, rnd](unsigned r, unsigned c) { return (c < 16 ? p.wg : p.wu) + (size_t)(rnd * 64 + r) * 1536 + fg * 128 + (c & 15) * 8; }; };
        const unsigned cb = (unsigned)((w >> 1) * 32 + (l & 1) * 16 + 4 * (2 * (w & 1) + ((l & 3) >> 1))) * 2;
#pragma unroll
        for (int r = 0; r < 3; ++r) stage_issue<32, 64>(smem + r * 32768, w, l, src(r));
#pragma unroll
        for (int r = 0; r < 10; ++r) {
            if (r < 8) WAITVN(8) else if (r == 8) WAITVN(4) else WAITV0();
            RAWBAR()
            if (r + 3 < 10) stage_issue<32, 64>(smem + ((r + 3) & 3) * 32768, w, l, src(r + 3));
#pragma unroll
            for (int kk = 0; kk < 2; ++kk)
#pragma unroll
                for (int i = 0; i < 2; ++i) wt[i][r * 2 + kk] = tr_frag<32, false>(smem + (r & 3) * 32768, kk * 32, cb + i * 256, l);
        }
        RAWBAR()
    }
    f32x4 cs0 = Z4, cs1 = Z4;
#pragma unroll
    for (int ks = 0; ks < 20; ++ks) { cs0 = MFMA(wt[0][ks], ONES8, cs0); cs1 = MFMA(wt[1][ks], ONES8, cs1); }
    SU(3)
    start_wait(p, smem, w, nc);
    RAWRD()
    const unsigned lo = lane_id() * 16;
    const u32x8 lc = {lo, lo + (w & 3) * 1024, j == 5 ? lo : 0xC0000000u, (lo >> 8) == ((lo >> 6) & 3) ? 40960 + (w & 3) * 128 + ((lo >> 1) & 0x78) : 43008 + (lo >> 1), 40960 + ((lo & 0x300) >> 1) + ((lo >> 1) & 0x78),
                      OFF_HG + (unsigned)(mt * 48 + fg * 4 + (w >> 1)) * 1024u + (w & 1) * 512 + ((lo & 512) >> 1) + (lo & 0xf0) + ((lo >> 5) & 8), lo || w != 7 ? 0xC0000000u : j * 4u, 0u};
    const u32x4 sc = {UNI(w), UNI(OFF_X1 + mt * 20480 + (w & 3) * 1024), UNI(FL_T + 128), 0u};
    bf16x16 t[20];
#pragma unroll
    for (int k = 0; k < 20; ++k) t[k] = cat16(wt[k / 10][(k % 10) * 2], wt[k / 10][(k % 10) * 2 + 1]);
    asm volatile("s_nop 7\n" UP_ASM :: "{v[0:31]}"(cat4(t[0], t[1], t[2], t[3])), "{v[32:63]}"(cat4(t[4], t[5], t[6], t[7])), "{v[64:95]}"(cat4(t[8], t[9], t[10], t[11])),
                 "{v[96:127]}"(cat4(t[12], t[13], t[14], t[15])), "{v[128:159]}"(cat4(t[16], t[17], t[18], t[19])), "{v[160:163]}"(cs0), "{v[164:167]}"(cs1), "{v[168:175]}"(lc),
                 "{s[16:19]}"(sc), "{s[8:11]}"(rd), [dx] "n"(DX), [dlast] "n"(DLAST), [eup] "n"(EUP), [pdist] "n"(PDIST) : ACLOB);
}

#undef RB
#define RB 6
__device__ __forceinline__ void dn_role(const Params& p, char* smem, const int w, const int j, const u32x2 nc) {
    const int l = lane_id(), tid = w * 64 + l;
    const rsrc_t rs = __builtin_amdgcn_make_buffer_rsrc((void*)wsp(p), 0, 0x7fffffff, 0x00020000);
    const int cg = j >> 1, mt = j & 1, c0 = cg * 96;
    bf16x8 wt[6][6];
    {
        const rsrc_t rw = __builtin_amdgcn_make_buffer_rsrc((void*)p.wd, 0, 1536 * 640 * 2, 0x00020000);
#define D_ISSUE(r_) { _Pragma("unroll") for (int i_ = 0; i_ < 8; ++i_) { \
            const unsigned ix_ = (unsigned)((w * 8 + i_) * 64 + l), rw_ = ix_ >> 4, cc_ = (ix_ & 15) ^ ((rw_ & 3) << 1); \
            __builtin_amdgcn_raw_ptr_buffer_load_lds(rw, (lds_ptr_t)(smem + ((r_) & 1) * 65536 + (w * 8 + i_) * 1024), 16, \
                cc_ < 12 && c0 + cc_ * 8 < 640 ? (int)(((rw_ >> 5) * 192 + (rw_ & 31)) * 1280 + cc_ * 16) : (int)0xC0000000, (r_) * 32 * 1280 + c0 * 2, 0, GAUX); } }
        D_ISSUE(0)
#pragma unroll
        for (int r = 0; r < 6; ++r) {
            if (r + 1 < 6) { D_ISSUE(r + 1) WAITVN(8) } else WAITV0();
            RAWBAR()
#pragma unroll
            for (int i = 0; i < 6; ++i) {
                const int pp = l & 3;
                const unsigned cb = (unsigned)((i >> 1) * 32 + (pp & 1) * 16 + 4 * (2 * (i & 1) + (pp >> 1))) * 2;
                wt[i][r] = tr_frag<16, false>(smem + (r & 1) * 65536, w * 32, cb, l);
            }
            RAWBAR()
        }
    }
    SU(3)
    start_wait(p, smem, w, nc);
    RAWRD()
    const u32x4 ro = (u32x4){UNI((size_t)p.out), UNI(((size_t)p.out >> 32) & 0xffffu), 2048u * 640 * 2, 0x00020000u};
    const unsigned lo = lane_id() * 16;
    const int pc = cg * 3 + (w >> 1);
    const bool fv = w < 6 && pc < 20;
    const u32x8 lc = {lo, w * 6144 + lo, w * 1024 + lo, fv ? mt * 20480 + pc * 1024 + (w & 1) * 512 + ((lo & 512) >> 1) + (lo & 0xf0) + ((lo >> 5) & 8) : 0xC0000000u,
                      fv ? (unsigned)(mt * 16 + ((lo >> 4) & 15)) * 1280u + pc * 64 + ((lo >> 3) & 32) + (w & 1) * 16 + ((lo >> 6) & 8) : 0xC0000000u,
                      lo || w ? 0xC0000000u : j * 4u, j == 5 ? lo : 0xC0000000u, 0u};
    const u32x4 sc = {UNI(w), UNI(OFF_HG + (unsigned)(mt * 48 + w * 6) * 1024u), UNI(FL_T + 256), 0u};
    bf16x16 t[18];
#pragma unroll
    for (int k = 0; k < 18; ++k) t[k] = cat16(wt[k / 3][(k % 3) * 2], wt[k / 3][(k % 3) * 2 + 1]);
    asm volatile("s_nop 4\n" DN_ASM :: "{v[0:31]}"(cat4(t[0], t[1], t[2], t[3])), "{v[32:63]}"(cat4(t[4], t[5], t[6], t[7])), "{v[64:95]}"(cat4(t[8], t[9], t[10], t[11])),
                 "{v[96:127]}"(cat4(t[12], t[13], t[14], t[15])), "{v[128:135]}"(t[16]), "{v[136:143]}"(t[17]), "{v[144:151]}"(lc), "{s[16:19]}"(sc), "{s[8:11]}"(rd), "{s[12:15]}"(ro),
                 [dxd] "n"(DXD), [dlast] "n"(DLAST), [eup] "n"(EUP), [pdist] "n"(PDIST), [x1] "n"(OFF_X1), [x2] "n"(OFF_X2) : ACLOB);
}

__device__ __forceinline__ void ln_role(const Params& p, char* smem, const int w, const int mt) {
    RAWRD()
    const unsigned lo = lane_id() * 16;
    const u32x8 lc = {lo, w < 4 ? lo : 0xC0000000u, (lo >> 8) == ((lo >> 6) & 3) ? w * 128 + ((lo >> 1) & 0x78) : 2048 + (lo >> 1), (lo & 0x300) + ((lo >> 1) & 0x78),
                      lo < 64 ? lo : 0xC0000000u, lo || w ? 0xC0000000u : mt * 4u, 0u, 0u};
    const u32x8 sc = {UNI(w), UNI(OFF_X2 + mt * 20480 + w * 1024), UNI(OFF_XN + 64 * 40960 + mt * 20480 + w * 1024), UNI(FL_N + mt * 512 + w * 64), UNI(FL_T + 384), 0u, 0u, 0u};
    asm volatile("s_nop 4\n" LN_ASM :: "{v[0:7]}"(lc), "{s[16:23]}"(sc), "{s[8:11]}"(rd), [dxl] "n"(DXL), [dlast] "n"(DLAST) : ACLOB);
}

#define MEGA_LDS 163840
__device__ __forceinline__ int role_of(const int b) {
    const int x = b & 7, r = b >> 3, i = r * 4 + (x & 3);
    if (x >= 4) return i < 32 ? 2048 + i : i < 56 ? 1280 + i - 32 : i < 104 ? 768 + i - 32 : i < 118 ? 1536 + i - 104 : i < 120 ? 1792 + i - 118 : 768 + i - 48;
    if (x == 0) return r;
    return x == 1 ? 512 + r : r < 20 ? 256 + (x - 2) * 20 + r : 768 + (x - 2) * 12 + r - 20;
    const int j = r * 3 + x - 1;
    return j < 40 ? 256 + (j & 1) * 20 + (j >> 1) : j < 72 ? 512 + j - 40 : 768 + j - 72;
}
__device__ __forceinline__ int init_of(const int ra) {
    const int role = ra >> 8, a = ra & 255;
    return role == 1 ? a : role == 5 ? 40 + a : role == 6 ? 64 + a : role == 3 && a >= 72 ? 6 + a : -1;
}
__global__ __launch_bounds__(512, 2) void mega_kernel(Params p) {
    extern __shared__ __attribute__((aligned(16))) char smem[];
    const int b = blockIdx.x, w = __builtin_amdgcn_readfirstlane(threadIdx.x >> 6);
    const unsigned long long t64 = __builtin_amdgcn_s_memrealtime();
    const u32x2 nc = {(unsigned)t64, (unsigned)(t64 >> 32)};
    if (threadIdx.x == 0) __builtin_amdgcn_raw_buffer_store_b64(nc, __builtin_amdgcn_make_buffer_rsrc((void*)wsp(p), 0, 0x7fffffff, 0x00020000), 0, HS_N + b * 8, AUX_SC1);
    const int ra = role_of(b), role = ra >> 8, a = ra & 255, ik = init_of(ra);
    SU(2)
    unsigned ns = 0u;
    if (ik >= 0 || role == 7) {
        unsigned dv = 1u;
        if (ik >= 0) init_work(p, smem, w, ik);
        else if (a) { if (w == 0) dv = 2u | calibrate(p); }
        else if (w == 1) start_master(p, [](int q) { const int z = role_of(q); return z == 1793 ? 2 : init_of(z) >= 0 ? 1 : 0; });
        SU(0)
        if (role == 5 || role == 6) start_wait<false>(p, smem, w, nc); else ns = start_wait(p, smem, w, nc, dv);
        SU(1)
        if (role != 1 && role != 7) wait_until(nc[0] + (role == 3 ? GHD : GLD));
    }
    if (ik < 0 && role != 7) wait_until(nc[0] + (role == 0 ? SDQ : role == 2 ? SDU : role == 8 ? SDO : a < 24 ? SDA : a < 48 ? SDB : SDV));
    if (role == 3 || (role == 1 && a >= 20)) {
        const bool hp = ik >= 0;
        if (role == 1) wait_until(nc[0] + RHD);
        int j = role == 1 ? a + 4 : hp ? 143 - a : a;
#pragma unroll 1
        do {
            if (hp && j < 24) wait_flags(p, __builtin_amdgcn_make_buffer_rsrc((void*)wsp(p), 0, 0x7fffffff, 0x00020000), 0, FL_N + (QSEG + QH / 2 - 1 - R0N) * 1024u, true);
            int jn = j - 8;
            if (jn < 44 && jn >= 36) jn -= 20;
            if (!hp || role != 3 || jn < (QH ? 0 : 24)) jn = -1;
            g_role(p, smem, w, j, hp ? HS0(j / 24) : 0, hp ? HS1(j / 24) : 128, hp ? 128 : 0, nc, ns);
            j = jn;
        } while (j >= 0);
        if (role == 3) return;
    }
    if (role == 0) qkm_role(p, smem, w, a, nc);
    else if (role == 1) r_role(p, smem, w, a >= 20, a >= 20 ? a - 20 : a, ns);
    else if (role == 2) ua_role(p, smem, w, a, nc);
    else if (role == 8) ob_role(p, smem, w, a, nc);
    else if (role == 5) up_role(p, smem, w, a, nc);
    else if (role == 6) dn_role(p, smem, w, a, nc);
    else ln_role(p, smem, w, a);
}

extern "C" void kernel_launch(void* const* d_in, const int* in_sizes, int n_in,
                              void* d_out, int out_size, void* d_ws, size_t ws_size,
                              hipStream_t stream) {
    Params p;
    p.x = (const u16*)d_in[0]; p.mem0 = (const u16*)d_in[1];
    p.Wq = (const u16*)d_in[2]; p.Wk = (const u16*)d_in[3]; p.Wv = (const u16*)d_in[4]; p.Wo = (const u16*)d_in[5];
    p.wg = (const u16*)d_in[8]; p.wu = (const u16*)d_in[9]; p.wd = (const u16*)d_in[10];
    p.ws = (char*)d_ws; p.out = (u16*)d_out;
    (void)hipFuncSetAttribute((const void*)mega_kernel, hipFuncAttributeMaxDynamicSharedMemorySize, MEGA_LDS);
    mega_kernel<<<256, 512, MEGA_LDS, stream>>>(p);
}
```
